```python
import torch

def _softmax(x, axis=-1):
    return torch.softmax(x, dim=axis)
def _gelu(x):
    return torch.nn.functional.gelu(x, approximate="tanh")
def _silu(x):
    return torch.nn.functional.silu(x)
def _sigmoid(x):
    return torch.sigmoid(x)
def _layernorm(x, axis=-1, eps=1e-5):
    m = x.mean(dim=axis, keepdim=True)
    v = x.var(dim=axis, keepdim=True, unbiased=False)
    return (x - m) / torch.sqrt(v + eps)
def _scatter_add(tgt, idx, src, axis=0):
    return tgt.clone().index_add_(axis, idx, src)
def _segment_sum(src, seg, n):
    out = torch.zeros((n,) + src.shape[1:], dtype=src.dtype)
    return out.index_add_(0, seg, src)
def _pad(x, pw):
    flat = tuple(v for pair in reversed(pw) for v in pair)
    return torch.nn.functional.pad(x, flat)

kernel_name = 'interleave_layers__sliding_block_cached__moe_arch_mod__decode'

def setup_inputs():

    _w = (lambda a: (lambda t: t.to(torch.bfloat16) if t.is_floating_point() else t)(torch.as_tensor(a)))
    def _A_setup():
        B, Sq, D = 4, 1, 640
        Hq, Hkv, Sk, Dh = 32, 32, 4096, 192
        assert Hq % Hkv == 0, (Hq, Hkv)
        return {
            "x": _w((lambda *s: torch.randn(*s, dtype=torch.bfloat16) * 0.05)(B, Sq, D)),
            "k_cache": _w((lambda *s: torch.randn(*s, dtype=torch.bfloat16) * 1.6)(B, Hkv, Sk, Dh)),
            "v_cache": _w((lambda *s: torch.randn(*s, dtype=torch.bfloat16) * 1.6)(B, Hkv, Sk, Dh)),
            "wq": _w((lambda *s: torch.randn(*s, dtype=torch.bfloat16) * 1.6)(D, Hq * Dh) * (D ** -0.5)),
            "wo": _w((lambda *s: torch.randn(*s, dtype=torch.bfloat16) * 1.6)(Hq * Dh, D) * ((Hq * Dh) ** -0.5)),
        }
    _A_in = _A_setup()

    def _B_setup():
        B, S, D, F, C = 4, 1, 640, 1536, 1
        return {
            "x": _w((lambda *s: torch.randn(*s, dtype=torch.bfloat16) * 0.05)(B, S, D)),
            "w_r": _w((lambda *s: torch.randn(*s, dtype=torch.bfloat16) * 0.05)(D)),
            "w_gate": _w((lambda *s: torch.randn(*s, dtype=torch.bfloat16) * 0.05)(D, F)),
            "w_up": _w((lambda *s: torch.randn(*s, dtype=torch.bfloat16) * 0.05)(D, F)),
            "w_down": _w((lambda *s: torch.randn(*s, dtype=torch.bfloat16) * 0.05)(F, D)),
        }
    _B_in = _B_setup()

    return {
        "A_x": _A_in["x"],
        "A_k_cache": _A_in["k_cache"],
        "A_v_cache": _A_in["v_cache"],
        "A_wq": _A_in["wq"],
        "A_wo": _A_in["wo"],
        "B_x": _B_in["x"],
        "B_w_r": _B_in["w_r"],
        "B_w_gate": _B_in["w_gate"],
        "B_w_up": _B_in["w_up"],
        "B_w_down": _B_in["w_down"],
    }

def reference(A_x, A_k_cache, A_v_cache, A_wq, A_wo, B_x, B_w_r, B_w_gate, B_w_up, B_w_down):
    def _A_ref(x, k_cache, v_cache, wq, wo):
        B, Sq, D = x.shape
        Hq, Dh = 32, 192
        Hkv, Sk = k_cache.shape[1], k_cache.shape[2]
        W = min(4096, max(256, Sk // 4))
        rep = Hq // Hkv
        q = (lambda x, *d: x.permute(*d))((x @ wq).reshape(B, Sq, Hq, Dh), 0, 2, 1, 3)
        k = (lambda x, n, axis: torch.repeat_interleave(x, n, dim=axis))(k_cache[:, :, Sk - W:, :], rep, axis=1)
        v = (lambda x, n, axis: torch.repeat_interleave(x, n, dim=axis))(v_cache[:, :, Sk - W:, :], rep, axis=1)
        scores = torch.einsum("bhqd,bhkd->bhqk", q, k) * (Dh ** -0.5)
        p = _softmax(scores, axis=-1)
        o = torch.einsum("bhqk,bhkd->bhqd", p, v)
        return (lambda x, *d: x.permute(*d))(o, 0, 2, 1, 3).reshape(B, Sq, Hq * Dh) @ wo

    def _B_ref(x, w_r, w_gate, w_up, w_down):
        B, S, D = x.shape
        C = 1
        r = x @ w_r
        idx = torch.argsort(-r, axis=-1)[:, :C]
        idx_e = (lambda x, n, axis: torch.repeat_interleave(x, n, dim=axis))(idx.reshape(B, C, 1), D, axis=2)
        x_sel = (lambda x, i, axis: torch.take_along_dim(x, i, dim=axis))(x, idx_e, axis=1)
        g = _silu(x_sel @ w_gate)
        u = x_sel @ w_up
        y = (g * u) @ w_down
        r_sel = _sigmoid((lambda x, i, axis: torch.take_along_dim(x, i, dim=axis))(r, idx, axis=1))
        y = y * r_sel.reshape(B, C, 1)
        b_off = (torch.arange(B) * S).reshape(B, 1)
        flat_idx = (idx + b_off).reshape(B * C)
        zero = (x * 0.0).reshape(B * S, D)
        upd = _scatter_add(zero, flat_idx, y.reshape(B * C, D), axis=0)
        return x + upd.reshape(B, S, D)

    x = A_x
    x = x + _A_ref(_layernorm(x, axis=-1), A_k_cache, A_v_cache, A_wq, A_wo)
    x = x + _B_ref(_layernorm(x, axis=-1), B_w_r, B_w_gate, B_w_up, B_w_down)
    x = x + _A_ref(_layernorm(x, axis=-1), A_k_cache, A_v_cache, A_wq, A_wo)
    x = x + _B_ref(_layernorm(x, axis=-1), B_w_r, B_w_gate, B_w_up, B_w_down)
    return x
```

```cpp
#include <hip/hip_runtime.h>
#include <hip/hip_bf16.h>
#include <stdint.h>

typedef unsigned short u16;
typedef unsigned u32x4 __attribute__((ext_vector_type(4)));
typedef unsigned u32x2 __attribute__((ext_vector_type(2)));
typedef float f32x4 __attribute__((ext_vector_type(4)));
typedef short s16x4 __attribute__((ext_vector_type(4)));
typedef __attribute__((ext_vector_type(4))) __bf16 bf16x4;
typedef __attribute__((ext_vector_type(8))) __bf16 bf16x8;
#define LAS __attribute__((address_space(3)))

constexpr int DM = 640, HQ = 32, SK = 4096, DH = 192, WIN = 1024, FF = 1536;
constexpr int QD = HQ * DH;
constexpr float SCALE = 0.07216878364870322f, LOG2E = 1.44269504088896341f;
constexpr unsigned kSpinCap = 1u << 16;
#define GJ(g) (((g) >> 3) & 7)
#define GI(g) (((g) & 7) * 4 + ((g) >> 6))

#define D4A 168
#define D5A 107
#define D1 138
#define D2 192
#define D3 78
#define D4 118
#define D5B 91
#define PACE 61
#define PINIT 100
#define PMIN 50
constexpr unsigned X4A = D4A, X5A = X4A + D5A, X1 = X5A + D1, X2 = X1 + D2, X3 = X2 + D3, X4 = X3 + D4, X5B = X4 + D5B;
constexpr unsigned XCOST = 16, NUNIT = 441 + XCOST, PBURST = 30;
constexpr int GP = 0, GA = 2, GB = 4, GC = 6;
constexpr int QKT = 2;

constexpr unsigned WS_R = 0;
constexpr unsigned WS_B = 1024;
constexpr unsigned WS_MB = 4096;
constexpr unsigned WS_H1 = WS_MB + 256 * 128;
constexpr unsigned WS_OWN = WS_H1 + 32 * 4 * 7168;
constexpr unsigned WS_H35 = WS_OWN + 256 * 2048;
constexpr unsigned WS_H4 = WS_H35 + 160 * 8192;
constexpr unsigned WS_END = WS_H4 + 32 * 16384;
constexpr unsigned WS_DBG = 6680576;
static_assert(WS_END <= WS_DBG, "ws map");

constexpr int L_WQ = 0;
constexpr int L_WO = 33792;
constexpr int L_GU = L_WO + 30720;
constexpr int L_WD = L_GU + 16384;
constexpr int L_X0 = L_WD + 8192;
constexpr int L_WR = L_X0 + 5120;
constexpr int L_XS = L_WR + 2048;
constexpr int L_Q = L_XS + 1024;
constexpr int L_OP = L_Q + 512;
constexpr int L_M = L_OP + 8192;
constexpr int L_H = L_M + 64;
constexpr int L_ST = L_H + 384;
constexpr int L_CS = L_ST + 64;
constexpr int L_CS2 = L_CS + 1024;
constexpr int L_STASH = L_CS2 + 1536;
constexpr int L_CNT = L_STASH + 4096;
constexpr int L_P = L_CNT + 80;
constexpr int L_O2 = L_P + 72 * 192;
constexpr int L_DBG = L_O2 + 3328;
constexpr int L_END = L_DBG + 2048;
static_assert(L_END <= 163840 && L_END > 81920, "LDS");

typedef __attribute__((ext_vector_type(2))) __bf16 bf16x2;
typedef float f32x2 __attribute__((ext_vector_type(2)));
__device__ __forceinline__ unsigned pk2(float a, float b) { return __builtin_bit_cast(unsigned, __builtin_convertvector((f32x2){a, b}, bf16x2)); }
__device__ __forceinline__ float lo16(unsigned w) { return __uint_as_float(w << 16); }
__device__ __forceinline__ float hi16(unsigned w) { return __uint_as_float(w & 0xffff0000u); }
__device__ __forceinline__ float bfr(float f) { return lo16(pk2(f, f)); }
template <int CTRL> __device__ __forceinline__ float dpp(float x) {
    return __builtin_bit_cast(float, __builtin_amdgcn_mov_dpp(__builtin_bit_cast(int, x), CTRL, 0xf, 0xf, true));
}
template <int CTRL> __device__ __forceinline__ unsigned dppu(unsigned x) { return (unsigned)__builtin_amdgcn_mov_dpp((int)x, CTRL, 0xf, 0xf, true); }
__device__ __forceinline__ float row16_sum(float v) {
    v += dpp<0xB1>(v); v += dpp<0x4E>(v); v += dpp<0x141>(v); v += dpp<0x140>(v); return v;
}
__device__ __forceinline__ float fmax2(float a, float b) { return __builtin_elementwise_maximum(a, b); }
__device__ __forceinline__ float fmax3(float a, float b, float c) { return fmax2(fmax2(a, b), c); }
__device__ __forceinline__ float x16_add(float v) { auto s = __builtin_amdgcn_permlane16_swap(__float_as_uint(v), __float_as_uint(v), false, false); return __uint_as_float(s[0]) + __uint_as_float(s[1]); }
__device__ __forceinline__ float x32_add(float v) { auto s = __builtin_amdgcn_permlane32_swap(__float_as_uint(v), __float_as_uint(v), false, false); return __uint_as_float(s[0]) + __uint_as_float(s[1]); }
__device__ __forceinline__ float wave_sum(float v) { return x32_add(x16_add(row16_sum(v))); }
__device__ __forceinline__ float rs32(float a, float b) { auto s = __builtin_amdgcn_permlane32_swap(__float_as_uint(a), __float_as_uint(b), false, false); return __uint_as_float(s[0]) + __uint_as_float(s[1]); }
__device__ __forceinline__ f32x2 sw16(float a, float b) { auto s = __builtin_amdgcn_permlane16_swap(__float_as_uint(a), __float_as_uint(b), false, false); return (f32x2){__uint_as_float(s[0]), __uint_as_float(s[1])}; }

#define BAR() asm volatile("s_waitcnt lgkmcnt(0)\n\ts_barrier" ::: "memory")
#define VMW(n) asm volatile("s_waitcnt vmcnt(" #n ")" ::: "memory")

template <int OFF = 0> __device__ __forceinline__ u32x4 ld16_sc1(const void* sbase, unsigned voff) {
    u32x4 r; asm volatile("global_load_dwordx4 %0, %1, %2 offset:%3 sc1" : "=v"(r) : "v"(voff), "s"(sbase), "i"(OFF) : "memory"); return r;
}
__device__ __forceinline__ unsigned ld4_sc1(const void* sbase, unsigned voff) {
    unsigned r; asm volatile("global_load_dword %0, %1, %2 sc1" : "=v"(r) : "v"(voff), "s"(sbase) : "memory"); return r;
}
template <int OFF = 0> __device__ __forceinline__ void st16_sc1(void* sbase, unsigned voff, u32x4 v) {
    asm volatile("global_store_dwordx4 %0, %1, %2 offset:%3 sc1\n\ts_nop 1" :: "v"(voff), "v"(v), "s"(sbase), "i"(OFF) : "memory");
}
__device__ __forceinline__ void st4_sc1(void* sbase, unsigned voff, unsigned v) {
    asm volatile("global_store_dword %0, %1, %2 sc1" :: "v"(voff), "v"(v), "s"(sbase) : "memory");
}
__device__ __forceinline__ void st8(void* sbase, unsigned voff, u32x2 v) {
    asm volatile("global_store_dwordx2 %0, %1, %2" :: "v"(voff), "v"(v), "s"(sbase) : "memory");
}
__device__ __forceinline__ void st2(void* sbase, unsigned voff, unsigned v) {
    asm volatile("global_store_short %0, %1, %2" :: "v"(voff), "v"(v), "s"(sbase) : "memory");
}
template <int IMM> __device__ __forceinline__ u32x4 ld16_nt(const void* sbase, unsigned voff) {
    u32x4 r; asm volatile("global_load_dwordx4 %0, %1, %2 offset:%3 nt" : "=v"(r) : "v"(voff), "s"(sbase), "i"(IMM) : "memory"); return r;
}
__device__ __forceinline__ void dma16(const void* sbase, unsigned voff, unsigned lds_dst) {
    unsigned keep;
    asm volatile("s_mov_b32 %0, m0\n\ts_mov_b32 m0, %3\n\ts_nop 0\n\tglobal_load_lds_dwordx4 %1, %2 nt\n\ts_mov_b32 m0, %0"
                 : "=&s"(keep) : "v"(voff), "s"(sbase), "s"(lds_dst) : "memory");
}
__device__ __forceinline__ void dma16p(const void* sbase, unsigned voff, unsigned lds_dst) {
    unsigned keep;
    asm volatile("s_mov_b32 %0, m0\n\ts_mov_b32 m0, %3\n\ts_nop 0\n\tglobal_load_lds_dwordx4 %1, %2\n\ts_mov_b32 m0, %0"
                 : "=&s"(keep) : "v"(voff), "s"(sbase), "s"(lds_dst) : "memory");
}
__device__ __forceinline__ unsigned um4(u32x4 a) { return max(max(a[0], a[1]), max(a[2], a[3])); }
__device__ __forceinline__ bool none(unsigned t) { return __builtin_amdgcn_ballot_w64(t != 0u) == 0ull; }
__device__ __forceinline__ bool any_empty(unsigned mx) { return __builtin_amdgcn_ballot_w64(mx == ~0u) != 0ull; }
__device__ __forceinline__ unsigned long long any_nan(float a, float b) { return __builtin_amdgcn_ballot_w64(__builtin_isunordered(a, b)); }
__device__ __forceinline__ f32x4 asf(u32x4 a) { return __builtin_bit_cast(f32x4, a); }
__device__ __forceinline__ u32x4 asu(f32x4 a) { return __builtin_bit_cast(u32x4, a); }
#define LANES(m) __builtin_amdgcn_inverse_ballot_w64(m)

__device__ __forceinline__ bf16x4 tr64(unsigned lds_addr) { return __builtin_amdgcn_ds_read_tr16_b64_v4bf16((LAS bf16x4*)(size_t)lds_addr); }
__device__ __forceinline__ bf16x8 cat8(bf16x4 a, bf16x4 b) { return __builtin_shufflevector(a, b, 0, 1, 2, 3, 4, 5, 6, 7); }
__device__ __forceinline__ bf16x8 lds16(unsigned a) { return *(const LAS bf16x8*)(size_t)a; }
__device__ __forceinline__ bf16x4 lds8(unsigned a) { return *(const LAS bf16x4*)(size_t)a; }
__device__ __forceinline__ f32x4 mfma32(bf16x8 a, bf16x8 b, f32x4 c) { return __builtin_amdgcn_mfma_f32_16x16x32_bf16(a, b, c, 0, 0, 0); }
__device__ __forceinline__ f32x4 mfma32u(u32x4 a, u32x4 b, f32x4 c) { return mfma32(__builtin_bit_cast(bf16x8, a), __builtin_bit_cast(bf16x8, b), c); }
__device__ __forceinline__ f32x4 mfma16(bf16x4 a, bf16x4 b, f32x4 c) {
    return __builtin_amdgcn_mfma_f32_16x16x16bf16_1k(__builtin_bit_cast(s16x4, a), __builtin_bit_cast(s16x4, b), c, 0, 0, 0);
}

#define STAMP(i) do {} while (0)

__device__ __forceinline__ void wait_word(const void* sbase, unsigned off) {
    unsigned t, cnt = kSpinCap;
    asm volatile("1:\n\tglobal_load_dword %0, %2, %3 sc1\n\ts_sub_u32 %1, %1, 1\n\ts_waitcnt vmcnt(0)\n\tv_cmp_eq_u32 vcc, -1, %0\n\ts_cbranch_vccz 2f\n\ts_cbranch_scc0 1b\n2:"
                 : "=&v"(t), "+s"(cnt) : "v"(off), "s"(sbase) : "memory", "vcc", "scc");
}
template <int OFF = 0> __device__ __forceinline__ void st16m(void* sbase, unsigned voff, u32x4 v, unsigned long long mask) {
    asm volatile("s_mov_b64 exec, %4\n\tglobal_store_dwordx4 %0, %1, %2 offset:%3 sc1\n\ts_mov_b64 exec, -1\n\ts_nop 0" :: "v"(voff), "v"(v), "s"(sbase), "i"(OFF), "s"(mask) : "memory");
}
template <int OFF> __device__ __forceinline__ void st16m2(void* sbase, unsigned voff, u32x4 v, unsigned voff2, u32x4 v2, unsigned long long mask) {
    asm volatile("s_mov_b64 exec, %6\n\tglobal_store_dwordx4 %0, %1, %2 sc1\n\tglobal_store_dwordx4 %4, %5, %2 offset:%3 sc1\n\ts_mov_b64 exec, -1\n\ts_nop 0"
                 :: "v"(voff), "v"(v), "s"(sbase), "i"(OFF), "v"(voff2), "v"(v2), "s"(mask) : "memory");
}
#define PINS(a) asm volatile("" : "+s"(a))
#define RETRY(LOAD, EMPTY, COMPUTE, NAN_, X) do { LOAD; COMPUTE; if (__builtin_expect((NAN_) != 0ull, 0)) { \
    _Pragma("unroll 1") for (unsigned spins_ = 0; spins_ < kSpinCap; ++spins_) { slip(X); LOAD; if (!any_empty(EMPTY)) break; } COMPUTE; } } while (0)
__device__ __forceinline__ unsigned now() { return (unsigned)__builtin_amdgcn_s_memrealtime(); }
__device__ __forceinline__ void until(unsigned t) {
    asm volatile("1:\n\ts_memrealtime vcc\n\ts_waitcnt lgkmcnt(0)\n\ts_sub_u32 vcc_lo, %0, vcc_lo\n\ts_cmp_lt_u32 vcc_lo, 0x400\n\ts_cbranch_scc1 1b" :: "s"(t - 1u) : "vcc", "scc", "memory");
}
__device__ __forceinline__ unsigned lds_word(unsigned a) {
    unsigned t_; asm volatile("ds_read_b32 %0, %1\n\ts_waitcnt lgkmcnt(0)\n\ts_nop 0" : "=v"(t_) : "v"(a) : "memory");
    return __builtin_amdgcn_readfirstlane(t_);
}
__device__ __forceinline__ void lds_set(unsigned a, unsigned v) { asm volatile("ds_write_b32 %0, %1" :: "v"(a), "v"(v) : "memory"); }
__device__ __forceinline__ int swz(int r) { return r ^ (((r >> 3) & 1) << 2); }
__device__ __forceinline__ f32x2 unpk(unsigned w) { return (f32x2){lo16(w), hi16(w)}; }
__device__ __forceinline__ unsigned pk2v(f32x2 v) { return __builtin_bit_cast(unsigned, __builtin_convertvector(v, bf16x2)); }
__device__ __forceinline__ float frcp(float x) { return __builtin_amdgcn_rcpf(x); }
__device__ __forceinline__ float ex2(float x) { return __builtin_amdgcn_exp2f(x); }
__device__ __forceinline__ int lane_id() { int l; asm volatile("v_mbcnt_lo_u32_b32 %0, -1, 0\n\tv_mbcnt_hi_u32_b32 %0, -1, %0" : "=v"(l)); return l & 63; }
__device__ __forceinline__ f32x2 ln_stats(float ss, float s) {
    const float mean = s * (1.f / DM);
    const float var = fmax2(__builtin_fmaf(-mean, mean, ss * (1.f / DM)), 0.f);
    const unsigned mv = pk2(mean, var);
    return (f32x2){lo16(mv), frcp(bfr(__builtin_amdgcn_sqrtf(bfr(hi16(mv) + 1e-5f))))};
}
#define GATHER8(a, o, S) do { a[0] = ld16_sc1<-4 * (S)>(ws, o); a[1] = ld16_sc1<-3 * (S)>(ws, o); a[2] = ld16_sc1<-2 * (S)>(ws, o); a[3] = ld16_sc1<-(S)>(ws, o); \
    a[4] = ld16_sc1<0>(ws, o); a[5] = ld16_sc1<(S)>(ws, o); a[6] = ld16_sc1<2 * (S)>(ws, o); a[7] = ld16_sc1<3 * (S)>(ws, o); } while (0)
#define WAIT8(a, N) asm volatile("s_waitcnt vmcnt(%8)" : "+v"((a)[0]), "+v"((a)[1]), "+v"((a)[2]), "+v"((a)[3]), "+v"((a)[4]), "+v"((a)[5]), "+v"((a)[6]), "+v"((a)[7]) : "i"(N) : "memory")
#define UM32(a) max(max(max(um4(a[0]), um4(a[1])), max(um4(a[2]), um4(a[3]))), max(max(um4(a[4]), um4(a[5])), max(um4(a[6]), um4(a[7]))))
#define SUM8(a) (((((((asf(a[0]) + asf(a[1])) + asf(a[2])) + asf(a[3])) + asf(a[4])) + asf(a[5])) + asf(a[6])) + asf(a[7]))
#define WAIT1(x, N) asm volatile("s_waitcnt vmcnt(%1)" : "+v"(x) : "i"(N) : "memory")
#define LO2(x) __builtin_shufflevector(x, x, 0, 1)
#define HI2(x) __builtin_shufflevector(x, x, 2, 3)
#define ACC8(a, s) do { f32x2 l_, h_; \
    asm volatile("s_waitcnt vmcnt(5)\n\tv_pk_add_f32 %0, %2, %4\n\tv_pk_add_f32 %1, %3, %5\n\tv_pk_add_f32 %0, %0, %6\n\tv_pk_add_f32 %1, %1, %7\n\t" \
                 "s_waitcnt vmcnt(3)\n\tv_pk_add_f32 %0, %0, %8\n\tv_pk_add_f32 %1, %1, %9\n\tv_pk_add_f32 %0, %0, %10\n\tv_pk_add_f32 %1, %1, %11\n\t" \
                 "s_waitcnt vmcnt(1)\n\tv_pk_add_f32 %0, %0, %12\n\tv_pk_add_f32 %1, %1, %13\n\tv_pk_add_f32 %0, %0, %14\n\tv_pk_add_f32 %1, %1, %15\n\t" \
                 "s_waitcnt vmcnt(0)\n\tv_pk_add_f32 %0, %0, %16\n\tv_pk_add_f32 %1, %1, %17\n\ts_nop 0" \
                 : "=&v"(l_), "=&v"(h_) : "v"(LO2(a[0])), "v"(HI2(a[0])), "v"(LO2(a[1])), "v"(HI2(a[1])), "v"(LO2(a[2])), "v"(HI2(a[2])), "v"(LO2(a[3])), "v"(HI2(a[3])), \
                   "v"(LO2(a[4])), "v"(HI2(a[4])), "v"(LO2(a[5])), "v"(HI2(a[5])), "v"(LO2(a[6])), "v"(HI2(a[6])), "v"(LO2(a[7])), "v"(HI2(a[7])) : "memory"); \
    s = __builtin_shufflevector(l_, h_, 0, 1, 2, 3); } while (0)
#define RETRY8(a, o, S, s, MID, POST, NAN_, X) do { GATHER8(a, o, S); MID; ACC8(a, s); POST; if (__builtin_expect((NAN_) != 0ull, 0)) { \
    _Pragma("unroll 1") for (unsigned spins_ = 0; spins_ < kSpinCap; ++spins_) { slip(X); GATHER8(a, o, S); WAIT8(a, 0); if (!any_empty(UM32(a))) break; } s = SUM8(a); POST; } } while (0)
#define PIN1(a) asm volatile("" : "+v"(a))
#define PIN2(a, b) asm volatile("" : "+v"(a), "+v"(b))
#define PIN3(a, b, c) asm volatile("" : "+v"(a), "+v"(b), "+v"(c))
#define LANE_VARS() const int lane = lane_id(); \
    const int kq = lane >> 4, n = lane & 15, q4 = n >> 2, p4 = lane & 3; \
    const int r0s = 8 * kq + q4 + 4 * (kq & 1), r1s = 8 * kq + q4 + 4 * ((kq & 1) ^ 1);     \
    const int r2s = swz(4 * kq + q4);                                                       \
    (void)r0s; (void)r1s; (void)r2s; (void)p4
template <int V> struct IC { static constexpr int value = V; };
struct Frag80 { bf16x8 a0, a1; bf16x4 a2; };

__global__ void __launch_bounds__(512, 2)
mega_kernel(const u16* __restrict__ x0, const u16* __restrict__ kc, const u16* __restrict__ vc, const u16* __restrict__ wq,
            const u16* __restrict__ wo, const u16* __restrict__ wr, const u16* __restrict__ wg, const u16* __restrict__ wu,
            const u16* __restrict__ wd, u16* __restrict__ out, unsigned char* __restrict__ ws) {
    extern __shared__ __attribute__((aligned(16))) unsigned char smem[];
    const unsigned lb = (unsigned)(size_t)smem;
    const int wave = __builtin_amdgcn_readfirstlane(threadIdx.x >> 6);
    const int g = blockIdx.x, gj = GJ(g), gi = GI(g);
    const f32x4 z4 = {0.f, 0.f, 0.f, 0.f};
    u32x4 kr[24], vr[24];
    auto sched = [&](unsigned X, unsigned gap) {
        unsigned v; unsigned long long c;
        asm volatile("s_memrealtime %1\n\tds_read_b32 %0, %2\n\ts_waitcnt lgkmcnt(0)" : "=v"(v), "=&s"(c) : "v"(lb + L_CNT + 56) : "memory");
        const unsigned n = (unsigned)c + gap;
        unsigned t = __builtin_amdgcn_readfirstlane(v) + X;
        if ((int)(n - t) > 0) t = n;
        until(t);
    };
    auto slip = [&](unsigned X) {
        if (X) lds_set(lb + L_CNT + 56, now() - X);
    };
    float xe = 0.f;

    auto code_pf = [&](int slice, int lane) {
        unsigned long long pc0; unsigned len, back, t;
        asm volatile("s_getpc_b64 %0\n.Lmega_pf%=:\n\ts_mov_b32 %1, .Lmega_code_end-.Lmega_code_begin\n\ts_mov_b32 %2, .Lmega_pf%=-.Lmega_code_begin" : "=s"(pc0), "=s"(len), "=s"(back));
        unsigned off = (unsigned)(slice * 4096 + lane * 64); if (off > len) off = len;
        asm volatile("global_load_dword %0, %1, %2" : "=v"(t) : "v"(off & ~3u), "s"(pc0 - back) : "memory");
        return t;
    };
    asm volatile(".Lmega_code_begin:");
    auto fix_k1 = [&](int t, int s, int N) {
        asm volatile("s_waitcnt vmcnt(%2)" : "+v"(kr[(2 * t) * 3 + s]), "+v"(kr[(2 * t + 1) * 3 + s]) : "i"(N) : "memory");
        const u32x4 A = kr[(2 * t) * 3 + s], Bv = kr[(2 * t + 1) * 3 + s];
        u32x4 An, Bn;
#pragma unroll
        for (int j = 0; j < 4; ++j) {
            An[j] = (unsigned)__builtin_amdgcn_update_dpp((int)A[j], (int)Bv[j], 0x128, 0xf, 0xc, false);
            Bn[j] = (unsigned)__builtin_amdgcn_update_dpp((int)Bv[j], (int)A[j], 0x128, 0xf, 0x3, false);
        }
        kr[(2 * t) * 3 + s] = An; kr[(2 * t + 1) * 3 + s] = Bn;
        PIN2(kr[(2 * t) * 3 + s], kr[(2 * t + 1) * 3 + s]);
    };
    auto trans_v = [&](int u, int N) {
#pragma unroll
        for (int r2 = 0; r2 < 4; ++r2) {
            asm volatile("s_waitcnt vmcnt(%2)" : "+v"(vr[u * 8 + 2 * r2]), "+v"(vr[u * 8 + 2 * r2 + 1]) : "i"(N + 6 - 2 * r2) : "memory");
#pragma unroll
            for (int dw = 0; dw < 4; ++dw) {
                const unsigned a = vr[u * 8 + 2 * r2][dw], bb = vr[u * 8 + 2 * r2 + 1][dw];
                vr[u * 8 + 2 * r2][dw] = __builtin_amdgcn_perm(bb, a, 0x05040100u);
                vr[u * 8 + 2 * r2 + 1][dw] = __builtin_amdgcn_perm(bb, a, 0x07060302u);
            }
            PIN2(vr[u * 8 + 2 * r2], vr[u * 8 + 2 * r2 + 1]);
        }
#pragma unroll
        for (int hl = 0; hl < 2; ++hl)
#pragma unroll
            for (int i = 0; i < 4; ++i)
#pragma unroll
                for (int j = i + 1; j < 4; ++j)
                    asm volatile("v_swap_b32 %0, %1" : "+v"(vr[u * 8 + 2 * i + hl][j]), "+v"(vr[u * 8 + 2 * j + hl][i]));
        asm volatile("s_nop 1");
        __builtin_amdgcn_sched_barrier(0);
    };
    auto frag80 = [&](unsigned im, int rs, int r0s, int r1s, int r2s) -> Frag80 {
        return Frag80{cat8(tr64(im + r0s * rs), tr64(im + r1s * rs)), cat8(tr64(im + (r0s + 32) * rs), tr64(im + (r1s + 32) * rs)), tr64(im + (r2s + 64) * rs)};
    };
    auto mm80 = [&](const Frag80& a, const Frag80& b) -> f32x4 {
        return mfma32(a.a1, b.a1, mfma32(a.a0, b.a0, z4)) + mfma16(a.a2, b.a2, z4);
    };
    auto rows80 = [&](unsigned a16, unsigned a8) -> Frag80 { const Frag80 f{lds16(a16), lds16(a16 + 64), lds8(a8)}; __builtin_amdgcn_sched_barrier(0); return f; };
    auto empty35 = [&](int q0, int part, int lane) {
        st16m(ws, WS_H35 + (unsigned)(((gj * 20 + q0 + 4 * part + (lane >> 4)) * 32 + gi) * 256 + (lane & 15) * 16), (u32x4){~0u, ~0u, ~0u, ~0u}, part ? 0xffffull : ~0ull);
    };
    auto empty4 = [&](int part, int lane) {
        st16_sc1(ws, WS_H4 + (unsigned)(gi * 16384) + (unsigned)((part == 0 || lane < 32) ? (part * 2 + (lane >> 5)) * 4096 + gj * 512 + (lane & 31) * 16
                                                                                 : 12288 + ((lane - 32) >> 3) * 1024 + gj * 128 + (lane & 7) * 16), (u32x4){~0u, ~0u, ~0u, ~0u});
    };
    const unsigned tst = now();
    auto pace = [&](unsigned n, bool wait = true) {
        unsigned u, w; u32x2 bp;
        asm volatile("s_mov_b64 exec, 1\n\tds_add_rtn_u32 %0, %3, %4\n\tds_max_u32 %3, %5 offset:12\n\tds_add_rtn_u32 %2, %3, %6 offset:12\n\ts_mov_b64 exec, -1\n\t"
                     "ds_read_b64 %1, %3 offset:4\n\ts_waitcnt lgkmcnt(0)"
                     : "=&v"(u), "=&v"(bp), "=&v"(w) : "v"(lb + L_CNT + 60), "v"(n), "v"((now() - tst) << 6), "v"(n * PMIN * 4u) : "memory");
        unsigned tn = __builtin_amdgcn_readfirstlane(bp[0] + u * bp[1]);
        const unsigned tw = __builtin_amdgcn_readfirstlane(w) + ((tst - PBURST) << 6);
        if ((int)(tw - tn) > 0) tn = tw;
        if (wait) {
#pragma unroll 1
            for (int k = 0; k < 2048 && (int)((now() << 6) - tn) < 0; ++k) {}
        }
    };
    auto plan = [&](unsigned bd, unsigned t00) {
        int d = (int)(bd - t00); if (d > 400 || d < -400) d = 0;
        d += (int)dppu<0xB1>((unsigned)d); d += (int)dppu<0x4E>((unsigned)d); d += (int)dppu<0x141>((unsigned)d);
        const unsigned u = *reinterpret_cast<volatile LAS unsigned*>(lb + L_CNT + 60);
        const u32x2 bp = *reinterpret_cast<volatile LAS u32x2*>(lb + L_CNT + 64);
        const unsigned tu = bp[0] + u * bp[1], fin = ((t00 + PINIT) << 6) + NUNIT * PACE * 4 + (unsigned)(d * 8);
        unsigned p = (unsigned)((float)(int)(fin - tu) * frcp((float)(NUNIT - u)));
        p = min(max(p, PACE * 34u / 10u), PACE * 46u / 10u);
        *reinterpret_cast<volatile LAS u32x2*>(lb + L_CNT + 64) = (u32x2){tu - u * p, p};
    };
    auto load_wo = [&](int k, int lane) {
        const int c = k * 64 + lane, pp = c / 10, cc = c - 10 * pp, r = swz(pp);
        const unsigned f = (unsigned)((32 * ((r >> 1) & 3) + 8 * (r & 1) + (r >> 3)) * 1280 + cc * 16);
        const unsigned f2 = (unsigned)((128 + 32 * ((r >> 1) & 1) + 8 * (r & 1) + (r >> 2)) * 1280 + cc * 16), fd = (unsigned)(r * 1280 + cc * 16);
        const u16* wb = wo + (size_t)gi * DH * DM + gj * 80;
#pragma unroll
        for (int a2 = 0; a2 < 6; ++a2) { if (a2 % 4 == 0) pace(4); dma16(wb + (a2 < 4 ? 16 * (a2 >> 1) + 4 * (a2 & 1) : 16 * (a2 & 1)) * DM, a2 < 4 ? f : f2, lb + L_WO + (5 * a2 + k) * 1024); }
        const u16* db = wd + gi * 48 * DM + gj * 80;
        dma16(db, fd, lb + L_WD + k * 1024);
        if (k < 3) dma16(db + 32 * DM, (k < 2 || lane < 32) ? fd : 0u, lb + L_WD + (5 + k) * 1024);
    };
    auto send_flag = [&](int lane) {
        st4_sc1(ws, WS_MB + (unsigned)((gi * 8 + (lane & 7)) * 128 + gj * 4), 1u);
    };
    auto send_q0 = [&](int lane) {
#pragma unroll
        for (int k = 0; k < 4; ++k) {
            int c = k * 64 + lane; if (c > 239) c = 239;
            const int row = c / 48;
            const u32x4 w = *reinterpret_cast<const u32x4*>(smem + L_STASH + c * 16);
            st16_sc1(ws, (k < 3 ? WS_H1 + (unsigned)(((gi * 4 + row) * 8 + gj) * 896) : WS_OWN + (unsigned)((gi * 8 + gj) * 2048 + 1024)) + (unsigned)((c - 48 * row) * 16), w);
        }
    };
    struct StatAddr { unsigned a16, b16, a8, b8, dst; };
    auto stats_addr = [&](int lane, unsigned dbase, int dstride) -> StatAddr {
        const int kq = lane >> 4, q4 = (lane >> 2) & 3, p4 = lane & 3;
        const int x1 = LANES(0x4444444444444444ull) ? L_WR + gj * 160 : L_XS + q4 * 160, xa = LANES(0xaaaaaaaaaaaaaaaaull) ? L_XS + 640 : x1;
        StatAddr r{lb + (unsigned)(xa + kq * 16), lb + (unsigned)(L_XS + p4 * 160 + kq * 16), lb + (unsigned)(xa + 128 + kq * 8), lb + (unsigned)(L_XS + p4 * 160 + 128 + kq * 8), dbase + (unsigned)(kq * dstride)};
        PIN3(r.a16, r.b16, r.dst); PIN2(r.a8, r.b8);
        return r;
    };
    auto stats = [&](const StatAddr& r) {
        const bf16x8 a0 = lds16(r.a16), b0 = lds16(r.b16), a1 = lds16(r.a16 + 64), b1 = lds16(r.b16 + 64);
        const bf16x4 a2 = lds8(r.a8), b2 = lds8(r.b8);
        unsigned long long mk = 0x0008000400020001ull; PINS(mk);
        __builtin_amdgcn_sched_barrier(0);
        st16m(ws, r.dst, asu(mfma32(a1, b1, mfma32(a0, b0, z4)) + mfma16(a2, b2, z4)), mk);
    };
    unsigned qa, pw, ma, ea = 0u; float mwv;
    auto qk_addr = [&](bool L1) {
        LANE_VARS();
        qa = lb + L_Q + (unsigned)(kq * 16);
        pw = lb + L_P + (unsigned)((7 + 8 * wave) * 192 + ((kq >> 1) * 4 + (q4 ^ (kq & 2))) * 16 + ((kq & 1) * 4 + p4) * 2);
        ma = lb + L_M + (unsigned)(wave * 4);
        PIN3(qa, pw, ma);
        if (L1) {
            const int part = wave & 1;
            ea = wave < 6 ? WS_H4 + (unsigned)(gi * 16384) + (unsigned)((part == 0 || lane < 32) ? (part * 2 + (lane >> 5)) * 4096 + gj * 512 + (lane & 31) * 16 : 12288 + ((lane - 32) >> 3) * 1024 + gj * 128 + (lane & 7) * 16)
                          : WS_H35 + (unsigned)(((gj * 20 + 4 * part + (lane >> 4)) * 32 + gi) * 256 + (lane & 15) * 16);
            PIN1(ea);
            if (wave < 4) __builtin_amdgcn_s_setprio(2);
        }
    };
    auto att_qk = [&](auto LC) {
        constexpr int layer = decltype(LC)::value;
        if (layer == 0) qk_addr(false);
        if (layer != 0) {
            BAR();
            if (wave >= 4) st16m(ws, ea, (u32x4){~0u, ~0u, ~0u, ~0u}, wave == 7 ? 0xffffull : ~0ull);
        } else {
#pragma unroll 1
            for (unsigned spins = 0; spins < (kSpinCap << 4); ++spins) {
                if (lds_word(lb + L_CNT + 4) != 0u) break;
                __builtin_amdgcn_s_sleep(1);
            }
        }
        STAMP(3 + 32 * layer);
        f32x4 acc[4];
        u32x4 qf[6];
#pragma unroll
        for (int u = 0; u < 6; ++u) qf[u] = *(const LAS u32x4*)(size_t)(qa + u * 64);
        f32x2 mom = {0.f, 0.f};
        if (layer != 0) mom = *(const LAS f32x2*)(size_t)(qa + 384);
        __builtin_amdgcn_sched_barrier(0);
        float rq = 1.f, sc = SCALE;
        unsigned long long k1 = 0xffff0000ffff0000ull, k2 = 0xffffffff00000000ull;
        PINS(k1); PINS(k2); PINS(sc);
        float mn = mom[1]; if (layer != 0) rq = mom[0];
#pragma unroll
        for (int u = 0; u < 6; ++u) {
#pragma unroll
            for (int t = 0; t < 4; ++t) {
                acc[t] = mfma32u(qf[u], kr[(2 * t + (u & 1)) * 3 + (u >> 1)], u ? acc[t] : z4);
                if (layer != 0) {
                    __builtin_amdgcn_sched_barrier(0);
                    switch (4 * u + t) {
                    case 7: asm volatile("v_mul_f32 %0, 0x3acccccd, %0" : "+v"(mn)); break;
                    case 8: asm volatile("v_mul_f32 %0, 0x3acccccd, %0" : "+v"(rq)); break;
                    case 9: asm volatile("v_fma_f32 %0, -%1, %1, %0" : "+v"(rq) : "v"(mn)); break;
                    case 10: asm volatile("v_maximum3_f32 %0, %0, 0, 0" : "+v"(rq)); break;
                    case 11: case 14: case 17: asm volatile("v_cvt_pk_bf16_f32 %0, %0, %0" : "+v"(rq)); break;
                    case 12: case 15: case 18: asm volatile("v_lshlrev_b32 %0, 16, %0" : "+v"(rq)); break;
                    case 13: asm volatile("v_add_f32 %0, 0x3727c5ac, %0" : "+v"(rq)); break;
                    case 16: asm volatile("v_sqrt_f32 %0, %0" : "+v"(rq)); break;
                    case 19: asm volatile("v_rcp_f32 %0, %0" : "+v"(rq)); break;
                    default: break;
                    }
                    __builtin_amdgcn_sched_barrier(0);
                }
            }
        }
        const float m4 = fmax2(fmax3(acc[0][0], acc[1][0], acc[2][0]), acc[3][0]);
        const float s01 = LANES(k1) ? acc[1][0] : acc[0][0], s23 = LANES(k1) ? acc[3][0] : acc[2][0];
        const float sv = LANES(k2) ? s23 : s01;
        const unsigned w1 = layer ? pk2(sv * rq, m4 * rq) : pk2(sv, m4), w2 = pk2(lo16(w1) * sc, hi16(w1) * sc);
        float m = hi16(w2);
        asm volatile("s_nop 1\n\tv_max_f32_dpp %0, %0, %0 quad_perm:[1,0,3,2] row_mask:0xf bank_mask:0xf\n\ts_nop 1\n\tv_max_f32_dpp %0, %0, %0 quad_perm:[2,3,0,1] row_mask:0xf bank_mask:0xf\n\t"
                     "s_nop 1\n\tv_max_f32_dpp %0, %0, %0 row_ror:4 row_mask:0xf bank_mask:0xf\n\ts_nop 1\n\tv_max_f32_dpp %0, %0, %0 row_ror:8 row_mask:0xf bank_mask:0xf" : "+v"(m));
        asm volatile("" :: "v"(acc[0]), "v"(acc[1]), "v"(acc[2]), "v"(acc[3]));
        const float p = ex2((lo16(w2) - m) * LOG2E);
        *(LAS u16*)(size_t)pw = (u16)pk2(p, p);
        if (layer == 0) *(LAS float*)(size_t)ma = m;
        mwv = m;
        __builtin_amdgcn_sched_barrier(0);
    };
    unsigned pr, oa, oc;
    auto att_addr = [&]() {
        LANE_VARS();
        pr = lb + L_P + (unsigned)((7 + 8 * wave - (lane & 7)) * 192 + ((n >> 3) * 4 + (kq ^ ((n >> 3) * 2))) * 16);
        oa = lb + L_OP + (unsigned)(wave * 1024 + (lane & 7) * 64 + kq * 16);
        oc = lb + L_OP + (unsigned)(wave * 1024 + 512 + (kq >> 1) * 256 + (lane & 7) * 32 + (kq & 1) * 16);
        PIN3(pr, oa, oc);
    };
    auto comb_o = [&](int part, f32x4 m0, f32x4 m1, f32x4 mm, f32x4 o0, f32x4 o1, f32x4 o2, f32x4 o3, unsigned so, unsigned long long mk) __attribute__((always_inline)) {
        const float M = fmax3(fmax3(m0[0], m0[1], m0[2]), fmax3(m0[3], m1[0], m1[1]), fmax2(m1[2], m1[3])) * LOG2E;
        f32x4 f;
#pragma unroll
        for (int k = 0; k < 4; ++k) f[k] = ex2(__builtin_fmaf(mm[k], LOG2E, -M));
        const f32x4 o = f[3] * o3 + (f[2] * o2 + (f[1] * o1 + f[0] * o0));
        float u0 = rs32(o[0], o[2]), u1 = rs32(o[1], o[3]);
        if (part == 1) { u0 = x16_add(u0); u1 = x16_add(u1); }
        const unsigned w = min(pk2(u0, u1), 0xfffffffeu);
        const u32x4 w4 = {w, dppu<0x55>(w), dppu<0xAA>(w), dppu<0xFF>(w)};
        st16m(ws, so, w4, mk);
    };
    auto ml_addr = [&](int lane, unsigned& xa, unsigned& ra) {
        xa = lb + L_M + (unsigned)(((lane & 15) >> 1) * 4); ra = lb + L_P + (unsigned)((7 + 8 * ((lane & 15) >> 1)) * 192 + (lane & 1) * 64 + (lane >> 4) * 16);
    };
    auto comb_ml = [&](unsigned ma, unsigned xa, unsigned ra, unsigned so) __attribute__((always_inline)) {
        const u32x4 x4 = *(const LAS u32x4*)(size_t)ra;
        const f32x4 m0 = *(const LAS f32x4*)(size_t)ma, m1 = *(const LAS f32x4*)(size_t)(ma + 16);
        const float mw = *(const LAS float*)(size_t)xa;
        __builtin_amdgcn_sched_barrier(0);
        const f32x4 sp = mfma32u((u32x4){0x3f803f80u, 0x3f803f80u, 0x3f803f80u, 0x3f803f80u}, x4, z4);
        __builtin_amdgcn_sched_barrier(0);
        const float M = fmax3(fmax3(m0[0], m0[1], m0[2]), fmax3(m0[3], m1[0], m1[1]), fmax2(m1[2], m1[3])) * LOG2E;
        const float L = row16_sum(sp[0] * ex2(__builtin_fmaf(mw, LOG2E, -M)));
        st16m(ws, so, asu((f32x4){M, L, M, L}), 1ull);
    };
    auto att_pv = [&](auto LC) {
        constexpr int layer = decltype(LC)::value;
        u32x4 bm[8];
        f32x4 da = z4, db = z4, dc = z4;
        if (layer == 0) {
            trans_v(0, 16); trans_v(1, 8);
#pragma unroll
            for (int e = 0; e < 8; ++e) bm[e] = *(const LAS u32x4*)(size_t)(pr + e * 192);
#pragma unroll
            for (int e = 0; e < 8; ++e) { da = mfma32u(vr[e], bm[e], da); db = mfma32u(vr[8 + e], bm[e], db); }
#pragma unroll
            for (int r = 0; r < 4; ++r) da[r] += dpp<0x128>(db[r]);
            if (LANES(0x00ff00ff00ff00ffull)) *(LAS f32x4*)(size_t)oa = da;
            asm volatile("s_mov_b64 exec, 1\n\tds_add_u32 %0, %1\n\ts_mov_b64 exec, -1" :: "v"(lb + L_CNT + 24), "v"(1u) : "memory");
            __builtin_amdgcn_sched_barrier(0);
            trans_v(2, 0);
            STAMP(4);
#pragma unroll
            for (int e = 0; e < 8; ++e) dc = mfma32u(vr[16 + e], bm[e], dc);
            if (LANES(0xff00ff0000ff00ffull)) *(LAS f32x4*)(size_t)oc = dc;
        } else {
#pragma unroll
            for (int e = 0; e < 8; ++e) bm[e] = *(const LAS u32x4*)(size_t)(pr + e * 192);
            *(LAS float*)(size_t)ma = mwv;
            unsigned long long ka = 0x00ff00ff00ff00ffull, kc = 0xff00ff0000ff00ffull;
            PINS(ka); PINS(kc);
            __builtin_amdgcn_sched_barrier(0);
#pragma unroll
            for (int e = 0; e < 8; ++e) { db = mfma32u(vr[8 + e], bm[e], db); da = mfma32u(vr[e], bm[e], da); if (e < 4) dc = mfma32u(vr[16 + e], bm[e], dc); }
            __builtin_amdgcn_sched_barrier(0);
#pragma unroll
            for (int e = 4; e < 8; ++e) dc = mfma32u(vr[16 + e], bm[e], dc);
            __builtin_amdgcn_sched_barrier(0);
            float a0, a1, a2, a3;
            asm volatile("s_nop 3\n\tv_add_f32_dpp %0, %8, %4 row_ror:8 row_mask:0xf bank_mask:0xf\n\tv_add_f32_dpp %1, %9, %5 row_ror:8 row_mask:0xf bank_mask:0xf\n\t"
                         "v_add_f32_dpp %2, %10, %6 row_ror:8 row_mask:0xf bank_mask:0xf\n\tv_add_f32_dpp %3, %11, %7 row_ror:8 row_mask:0xf bank_mask:0xf"
                         : "=&v"(a0), "=&v"(a1), "=&v"(a2), "=&v"(a3) : "v"(da[0]), "v"(da[1]), "v"(da[2]), "v"(da[3]), "v"(db[0]), "v"(db[1]), "v"(db[2]), "v"(db[3]), "v"(dc));
            asm volatile("s_mov_b64 exec, %4\n\tds_write_b128 %0, %1\n\ts_mov_b64 exec, %5\n\tds_write_b128 %2, %3\n\ts_mov_b64 exec, -1"
                         :: "v"(oa), "v"((f32x4){a0, a1, a2, a3}), "v"(oc), "v"(dc), "s"(ka), "s"(kc) : "memory");
            if (wave >= 3) { VMW(0); BAR(); }
        }
        STAMP(20 + 32 * layer);
    };

    unsigned cma, cxa, cra, cso; int ord;
    {
        const int tid = threadIdx.x, lane = tid & 63;
        STAMP(0);
        const unsigned t00 = tst;
        const u32x4 ones = {~0u, ~0u, ~0u, ~0u};
        unsigned pf = 0u;
        const u16* kbase = kc + ((size_t)((gj >> 1) * HQ + gi) * SK + (SK - WIN) + (gj & 1) * 512) * DH;
        const u16* vbase = vc + ((size_t)((gj >> 1) * HQ + gi) * SK + (SK - WIN) + (gj & 1) * 512) * DH;
        if (wave == 7) {
            __builtin_amdgcn_s_setprio(3);
            unsigned bd = ld4_sc1(ws, WS_B + (unsigned)((lane & 7) * 4));
            if (g < 8) st4_sc1(ws, WS_B + (unsigned)(g * 4), t00);
            pf = code_pf((g >> 3) % 10, lane);
            if (gi + gj == 0) st4_sc1(ws, WS_R + (lane & 31) * 4, 0u);
            for (int i = 0; i < 5; ++i) dma16p(x0, (unsigned)(i * 1024 + lane * 16), lb + L_X0 + i * 1024);
            if (lane < 20) *reinterpret_cast<unsigned*>(smem + L_CNT + lane * 4) = lane == 16 ? (t00 + PINIT) << 6 : lane == 17 ? PACE * 4u : 0u;
            BAR();
            st4_sc1(ws, WS_MB + (unsigned)((gi * 8 + gj) * 128 + (lane & 31) * 4), 0u);
            st16m(ws, WS_OWN + (unsigned)((gi * 8 + gj) * 2048 + lane * 16), ones, ~0ull);
            if (lane < 10) *reinterpret_cast<u32x4*>(smem + L_XS + 640 + lane * 16) = (u32x4){0x3f803f80u, 0x3f803f80u, 0x3f803f80u, 0x3f803f80u};
            if (lane < 16) { *reinterpret_cast<u32x4*>(smem + L_CS + 768 + lane * 16) = (u32x4){0u, 0u, 0u, 0u}; }
            WAIT1(bd, 8);
            plan(bd, t00);
        } else {
#pragma unroll 1
            for (int i = wave; i < 35; i += 7) {
                if (i >= 33) dma16p(wr, (unsigned)((i == 33 || lane < 16) ? (i - 33) * 1024 + lane * 16 : 0), lb + L_WR + (i - 33) * 1024);
                else {
                    const int c = i * 64 + lane; int row = c / 26, cc = c - 26 * row; if (cc > 23) cc = 23; if (row > 79) row = 79;
                    dma16(wq, (unsigned)(((gj * 80 + swz(row)) * QD + gi * DH + cc * 8) * 2), lb + L_WQ + i * 1024);
                }
            }
            if (wave >= 4) {
                if (wave < 6) {
                    empty35(8 * (wave - 4), 0, lane);
                    st16m(ws, WS_H35 + (unsigned)(((gj * 20 + 8 * (wave - 4) + 4 + (lane >> 4)) * 32 + gi) * 256 + (lane & 15) * 16), ones, wave == 4 ? ~0ull : 0xffffffffffffull);
                } else { empty4(0, lane); empty4(1, lane); }
            }
            BAR();
            *reinterpret_cast<u32x4*>(smem + L_P + tid * 16) = (u32x4){0u, 0u, 0u, 0u};
            if (tid < 72 * 12 - 448) *reinterpret_cast<u32x4*>(smem + L_P + 7168 + tid * 16) = (u32x4){0u, 0u, 0u, 0u};
            if (wave >= 4) {
                asm volatile("s_waitcnt vmcnt(%0) lgkmcnt(0)" :: "i"(2) : "memory");
                if (lane == 0) __hip_atomic_fetch_add(reinterpret_cast<unsigned*>(smem + L_CNT), 1u, __ATOMIC_RELAXED, __HIP_MEMORY_SCOPE_WORKGROUP);
                VMW(0);
                if (lane == 0) __hip_atomic_fetch_add(reinterpret_cast<unsigned*>(smem + L_CNT + 32), 1u, __ATOMIC_RELAXED, __HIP_MEMORY_SCOPE_WORKGROUP);
                if (wave == 4) load_wo(2, lane);
                else {
                    {
#pragma unroll
                        for (int k = 0; k < 3; ++k) {
                            const int c = k * 64 + lane, row = c / 6, cc = c - 6 * row;
                            const unsigned o = (unsigned)((swz(row) * FF + cc * 8) * 2), d = lb + L_GU + (wave - 5) * 8192 + k * 1024;
                            const u16* wb = (wave == 5 ? wg : wu) + gj * 80 * FF + gi * 48;
                            pace(3);
                            dma16(wb, o, d); dma16(wb + 32 * FF, o, d + 3072);
                            if (k < 2) dma16(wb + 64 * FF, (k == 0 || lane < 32) ? o : 0u, d + 6144);
                        }
                    }
                }
            }
        }
        u32x4 a[8]; unsigned mb = 0u;
        const unsigned ho = WS_H1 + (unsigned)((gi * 4 + (gj >> 1)) * 7168 + 3584 + (lane < 48 ? lane : 0) * 16);
        const unsigned ko = (unsigned)((wave * 64 + (lane & 7)) * 384 + (((lane >> 3) & 1) * 4 + (lane >> 4)) * 16);
#pragma unroll
        for (int G = 0; G < 8; ++G) {
            const unsigned o = ko + G * 3072;
            pace(3);
            kr[G * 3 + 0] = ld16_nt<0>(kbase, o); kr[G * 3 + 1] = ld16_nt<128>(kbase, o); kr[G * 3 + 2] = ld16_nt<256>(kbase, o);
            if (G == 0 && wave < 4) {
                asm volatile("; COND_BEGIN");
                asm volatile("s_waitcnt vmcnt(%0) lgkmcnt(0)" :: "i"(3) : "memory");
                if (lane == 0) __hip_atomic_fetch_add(reinterpret_cast<unsigned*>(smem + L_CNT), 1u, __ATOMIC_RELAXED, __HIP_MEMORY_SCOPE_WORKGROUP);
                if (wave != 2) {
#pragma unroll 1
                    for (int k = wave; k < (wave == 3 ? 5 : wave + 1); ++k) load_wo(k, lane);
                }
                asm volatile("; COND_END");
            }
            if (wave == 7) {
                asm volatile("; COND_BEGIN");
                if (G == GP) {
                    WAIT1(pf, 2 + 3 * (GP + 1));
                    const unsigned char* p = smem + L_X0 + (lane >> 4) * 1280;
                    const float c0 = lo16(*reinterpret_cast<const u16*>(p));
                    const f32x2 c2 = {c0, c0};
                    f32x2 s2 = {0.f, 0.f}, q2 = s2;
#pragma unroll
                    for (int k = 0; k < 5; ++k) {
                        const u32x4 w = *reinterpret_cast<const u32x4*>(p + k * 256 + (lane & 15) * 16);
#pragma unroll
                        for (int e = 0; e < 4; ++e) { const f32x2 d = unpk(w[e]) - c2; s2 += d; q2 += d * d; }
                    }
                    const float s = row16_sum(s2[0] + s2[1]), qq = row16_sum(q2[0] + q2[1]);
                    const float ms = s * (1.f / DM);
                    const unsigned mv = pk2(c0 + ms, fmax2(qq - s * ms, 0.f) * (1.f / DM));
                    const float mb = lo16(mv), rden = frcp(bfr(__builtin_amdgcn_sqrtf(bfr(hi16(mv) + 1e-5f))));
                    const f32x2 mb2 = {mb, mb}, rd2 = {rden, rden};
                    if ((lane & 15) < 10) {
                        u32x4 w = *reinterpret_cast<const u32x4*>(p + gj * 160 + (lane & 15) * 16);
#pragma unroll
                        for (int e = 0; e < 4; ++e) w[e] = pk2v(unpk(pk2v(unpk(w[e]) - mb2)) * rd2);
                        *reinterpret_cast<u32x4*>(smem + L_XS + (lane >> 4) * 160 + (lane & 15) * 16) = w;
                    }
                    STAMP(30);
#pragma unroll 1
                    for (unsigned spins = 0; spins < (kSpinCap << 4); ++spins) {
                        if (lds_word(lb + L_CNT) >= 7u) break;
                        __builtin_amdgcn_s_sleep(1);
                    }
                    STAMP(26);
                    {
                        const int kq = lane >> 4, n = lane & 15, q4 = n >> 2, p4 = lane & 3;
                        const int r0s = 8 * kq + q4 + 4 * (kq & 1), r1s = 8 * kq + q4 + 4 * ((kq & 1) ^ 1), r2s = swz(4 * kq + q4);
                        const unsigned xb = lb + L_XS + (unsigned)((n < 4 ? n : 4) * 160);
                        const Frag80 b = rows80(xb + kq * 16, xb + 128 + kq * 8);
#pragma unroll 4
                        for (int T = 0; T < 12; ++T) {
                            const f32x4 c = mm80(frag80(lb + L_WQ + (unsigned)(32 * T + 8 * p4), 416, r0s, r1s, r2s), b);
                            if (n < 5) *reinterpret_cast<f32x4*>(smem + L_STASH + n * 768 + 64 * T + kq * 16) = c;
                        }
                    }
                    f32x2 r2 = {0.f, 0.f};
#pragma unroll
                    for (int k = 0; k < 5; ++k) r2 += unpk(*reinterpret_cast<const unsigned*>(smem + L_WR + k * 256 + lane * 4));
                    send_q0(lane);
                    const float sw = wave_sum(r2[0] + r2[1]);
                    if (lane == 0) *reinterpret_cast<float*>(smem + L_CNT + 12) = sw;
                    STAMP(2);
                    pace(XCOST, false);
                }
                if (G == GP) mb = ld4_sc1(ws, WS_B + (unsigned)((lane & 7) * 4));
                if (G == GA) { WAIT1(mb, 3 * (GA - GP));
#pragma unroll 1
                    for (unsigned spins = 0; spins < (kSpinCap << 4); ++spins) if (lds_word(lb + L_CNT + 32) >= 3u) break;
                    send_flag(lane);
                    plan(mb, t00);
                }
                if (G == GB) mb = ld4_sc1(ws, WS_MB + (unsigned)((gi * 8 + gj) * 128 + (lane & 7) * 4));
                if (G == GC) {
                    WAIT1(mb, 3 * (GC - GB));
#pragma unroll 1
                    for (unsigned spins = 0; spins < kSpinCap && !none(mb ^ 1u); ++spins) {
                        if ((spins & 7u) == 7u) send_flag(lane);
                        mb = ld4_sc1(ws, WS_MB + (unsigned)((gi * 8 + gj) * 128 + (lane & 7) * 4));
                        WAIT1(mb, 0);
                    }
                    if (gj == (gi & 7)) st4_sc1(ws, WS_R + (unsigned)(gi * 4), 1u);
                    GATHER8(a, ho, 896);
                }
                asm volatile("; COND_END");
            } else if (G == GC) { for (int k = 0; k < 8; ++k) asm volatile("" : "=v"(a[k])); }
        }
        {
            const unsigned v0 = (unsigned)((wave * 64 + 4 * (lane >> 4)) * 384 + (lane & 15) * 16);
#pragma unroll
            for (int t = 0; t < 4; ++t) {
                const unsigned o = v0 + t * 6144;
                pace(2);
                vr[t * 4 + 0] = ld16_nt<0>(vbase, o); vr[t * 4 + 1] = ld16_nt<384>(vbase, o);
                if (t == 0 && wave == 7) {
                    asm volatile("; COND_BEGIN");
                    WAIT8(a, 3 * (7 - GC) + 2);
                    const f32x4 sq = SUM8(a);
                    *reinterpret_cast<u32x2*>(smem + L_Q + lane * 8) = (u32x2){pk2(sq[0], sq[1]), pk2(sq[2], sq[3])};
                    asm volatile("s_waitcnt lgkmcnt(0)\n\tds_write_b32 %0, %1" :: "v"(lb + L_CNT + 4), "v"(1u) : "memory");
                    STAMP(27);
                    asm volatile("; COND_END");
                }
                if (t < 3) { fix_k1((4 * t) / 3, (4 * t) % 3, 20 - 6 * ((4 * t) / 3) - (4 * t) % 3 + 4 * t + 2); fix_k1((4 * t + 1) / 3, (4 * t + 1) % 3, 20 - 6 * ((4 * t + 1) / 3) - (4 * t + 1) % 3 + 4 * t + 2); }
                pace(2);
                vr[t * 4 + 2] = ld16_nt<768>(vbase, o); vr[t * 4 + 3] = ld16_nt<1152>(vbase, o);
                if (t < 3) { fix_k1((4 * t + 2) / 3, (4 * t + 2) % 3, 20 - 6 * ((4 * t + 2) / 3) - (4 * t + 2) % 3 + 4 * t + 4); fix_k1((4 * t + 3) / 3, (4 * t + 3) % 3, 20 - 6 * ((4 * t + 3) / 3) - (4 * t + 3) % 3 + 4 * t + 4); }
                if (t == QKT - 1 && wave == 7 && gi + gj == 0) { asm volatile("; COND_BEGIN"); mb = ld4_sc1(ws, WS_R + (unsigned)((lane & 31) * 4)); asm volatile("; COND_END"); }
                if (t == QKT) {
                    STAMP(1);
                    if (wave == 7 && gi + gj == 0) {
                        asm volatile("; COND_BEGIN");
                        WAIT1(mb, 4);
                        if (none(mb ^ 1u)) {
#pragma unroll
                            for (int k = 0; k < 4; ++k) st4_sc1(ws, WS_MB + (unsigned)((k * 64 + lane) * 128 + 32), 1u);
                            lds_set(lb + L_CNT + 36, 1u);
                        }
                        asm volatile("; COND_END");
                    }
                    __builtin_amdgcn_s_setprio(0);
                    att_qk(IC<0>{});
                }
            }
            const unsigned v1 = (unsigned)((wave * 64 + 32 * ((lane >> 3) & 1) + 4 * (lane >> 4)) * 384 + 256 + (lane & 7) * 16);
#pragma unroll
            for (int t = 4; t < 6; ++t) {
                const unsigned o = v1 + (t - 4) * 6144;
                pace(2);
                vr[t * 4 + 0] = ld16_nt<0>(vbase, o); vr[t * 4 + 1] = ld16_nt<384>(vbase, o);
                pace(2);
                vr[t * 4 + 2] = ld16_nt<768>(vbase, o); vr[t * 4 + 3] = ld16_nt<1152>(vbase, o);
            }
        }
        if (lane == 0) __hip_atomic_fetch_add(reinterpret_cast<unsigned*>(smem + L_CNT + 16), 1u, __ATOMIC_RELAXED, __HIP_MEMORY_SCOPE_WORKGROUP);
        STAMP(28);
        att_addr();
        cma = lb + L_M; cxa = lb + L_M + (unsigned)((lane >> 5) * 16); cra = lb + L_OP + (unsigned)(512 + (lane >> 5) * 4096 + (lane & 31) * 16);
        cso = WS_OWN + (unsigned)((gi * 8 + gj) * 2048 + 256 + (lane >> 5) * 64 + (lane & 15) * 4);
        PIN2(cma, cxa); PIN2(cra, cso);
        att_pv(IC<0>{});
    }
    {
        f32x4 m0, m1, mm, o0, o1, o2, o3; unsigned r;
        asm volatile("s_mov_b64 exec, 1\n\tds_add_rtn_u32 %7, %8, %9 offset:%12\n\ts_mov_b64 exec, -1\n\t"
                     "ds_read_b128 %0, %8\n\tds_read_b128 %1, %8 offset:16\n\tds_read_b128 %2, %10\n\tds_read_b128 %3, %11\n\tds_read_b128 %4, %11 offset:1024\n\t"
                     "ds_read_b128 %5, %11 offset:2048\n\tds_read_b128 %6, %11 offset:3072\n\ts_waitcnt lgkmcnt(0)"
                     : "=&v"(m0), "=&v"(m1), "=&v"(mm), "=&v"(o0), "=&v"(o1), "=&v"(o2), "=&v"(o3), "=&v"(r)
                     : "v"(cma), "v"(1u), "v"(cxa), "v"(cra), "i"(L_CNT + 28 - L_M) : "memory");
        ord = __builtin_amdgcn_readfirstlane(r);
        if (ord == 7) { STAMP(5); comb_o(1, m0, m1, mm, o0, o1, o2, o3, cso, 0x0000111100001111ull); STAMP(21); }
        const int lane = lane_id();
        auto wait_cnt = [&](int off, unsigned v) {
#pragma unroll 1
            for (unsigned spins = 0; spins < (kSpinCap << 4); ++spins) { if (lds_word(lb + L_CNT + off) >= v) break; __builtin_amdgcn_s_sleep(1); }
        };
        if (ord == 1) {
            wait_cnt(16, 8u);
            unsigned xa, ra; ml_addr(lane, xa, ra);
            comb_ml(cma, xa, ra, WS_OWN + (unsigned)((gi * 8 + gj) * 2048 + 384));
            STAMP(24);
        } else if (ord == 0) {
            if (gi + gj == 0 && !lds_word(lb + L_CNT + 36)) {
#pragma unroll 1
                for (unsigned spins = 0; spins < kSpinCap; ++spins) {
                    unsigned r = ld4_sc1(ws, WS_R + (unsigned)((lane & 31) * 4));
                    WAIT1(r, 0);
                    if (none(r ^ 1u)) break;
                    if ((spins & 7u) == 7u) send_flag(lane);
                }
#pragma unroll
                for (int k = 0; k < 4; ++k) st4_sc1(ws, WS_MB + (unsigned)((k * 64 + lane) * 128 + 32), 1u);
            }
#pragma unroll 1
            for (unsigned spins = 0; spins < kSpinCap; ++spins) {
                unsigned r = ld4_sc1(ws, WS_MB + (unsigned)((gi * 8 + gj) * 128 + 32));
                WAIT1(r, 0);
                if (__builtin_amdgcn_readfirstlane(r) == 1u) break;
                if ((spins & 7u) == 7u) { send_flag(lane); if (gj == (gi & 7)) st4_sc1(ws, WS_R + (unsigned)(gi * 4), 1u); }
                __builtin_amdgcn_s_sleep(16);
            }
            STAMP(29);
        } else if (ord == 2) {
#pragma unroll 1
            for (unsigned spins = 0; spins < (kSpinCap << 6); ++spins) if (lds_word(lb + L_CNT + 24) >= 8u) break;
            const unsigned ra = cra - 512;
            comb_o(0, *(const LAS f32x4*)(size_t)cma, *(const LAS f32x4*)(size_t)(cma + 16), *(const LAS f32x4*)(size_t)cxa, *(const LAS f32x4*)(size_t)ra, *(const LAS f32x4*)(size_t)(ra + 1024),
                   *(const LAS f32x4*)(size_t)(ra + 2048), *(const LAS f32x4*)(size_t)(ra + 3072), WS_OWN + (unsigned)((gi * 8 + gj) * 2048 + lane * 4), 0x1111111111111111ull);
            STAMP(22);
        }
    }

    auto chain = [&](auto LC) {
        constexpr int layer = decltype(LC)::value;
        const unsigned h4 = WS_H4 + (unsigned)(gi * 16384);
        if (wave < 3) {
            if (layer != 0) {
                const int lane = lane_id();
                const unsigned ob = WS_OWN + (unsigned)((gi * 8 + gj) * 2048 + layer * 512);
                unsigned ma = lb + L_M, xa = lb + L_M + (unsigned)((lane >> 5) * 16);
                unsigned ra = lb + L_OP + (unsigned)(wave * 512 + (lane >> 5) * 4096 + (lane & 31) * 16);
                if (wave == 2) ml_addr(lane, xa, ra);
                unsigned so = ob + (unsigned)(wave == 0 ? lane * 4 : wave == 1 ? 256 + (lane >> 5) * 64 + (lane & 15) * 4 : 384);
                unsigned long long mk = wave == 0 ? 0x1111111111111111ull : 0x0000111100001111ull;
                PIN2(ma, xa); PIN2(ra, so); PINS(mk);
                __builtin_amdgcn_s_setprio(0);
                if (wave == 2) {
                    BAR();
                    comb_ml(ma, xa, ra, so);
                } else {
                    BAR();
                    STAMP(5 + 32 * layer);
                    comb_o(wave, *(const LAS f32x4*)(size_t)ma, *(const LAS f32x4*)(size_t)(ma + 16), *(const LAS f32x4*)(size_t)xa, *(const LAS f32x4*)(size_t)ra,
                           *(const LAS f32x4*)(size_t)(ra + 1024), *(const LAS f32x4*)(size_t)(ra + 2048), *(const LAS f32x4*)(size_t)(ra + 3072), so, mk);
                }
                STAMP(21 + 32 * layer);
            }
            LANE_VARS();
            if (layer == 0) {
                if (wave == 0) {
#pragma unroll 1
                    for (unsigned spins = 1; spins < kSpinCap; ++spins) {
                        if (lds_word(lb + L_CNT + 52) != 0u) break;
                        __builtin_amdgcn_s_sleep(2);
                        if ((spins & 63u) == 0u) send_flag(lane);
                    }
                }
            }
            BAR();
            if (layer == 0) {
                __builtin_amdgcn_s_sleep(4);
                if (wave < 2) {
                    const unsigned o = WS_H1 + (unsigned)(((gi * 4 + 2 * wave) * 8 + gj) * 896 + (lane < 56 ? lane : 55) * 16);
                    st16_sc1(ws, o, (u32x4){~0u, ~0u, ~0u, ~0u}); st16_sc1(ws, o + 7168, (u32x4){~0u, ~0u, ~0u, ~0u});
                    VMW(0);
                }
            }
            {
                const unsigned im = lb + L_GU + (unsigned)(32 * wave + 8 * p4);
                Frag80 fg = frag80(im, 96, r0s, r1s, r2s), fu = frag80(im + 8192, 96, r0s, r1s, r2s);
                const unsigned xb = lb + L_XS + (unsigned)((n < 4 ? n : 4) * 160);
                unsigned b16 = xb + kq * 16, b8 = xb + 128 + kq * 8;
                unsigned sg = h4 + (unsigned)((n < 4 ? wave * 4096 + gj * 512 + n * 64 : 12288 + wave * 1024 + gj * 128) + kq * 16), su = sg + (n < 4 ? 256u : 64u);
                unsigned o = h4 + (unsigned)(wave * 4096 + lane * 16);
                unsigned oc = h4 + (unsigned)(12288 + wave * 1024 + ((lane >> 5) * 4 + q4) * 128 + (kq & 1) * 64 + p4 * 16);
                unsigned os = h4 + (unsigned)(15360 + (p4 + 4 * (kq & 1)) * 128 + q4 * 16);
                unsigned ha = lb + L_H + (unsigned)(q4 * 96 + wave * 32 + p4 * 8 + kq * 2);
                f32x2 cs = {0.f, 0.f};
                if (layer != 0) cs = *reinterpret_cast<const f32x2*>(smem + L_CS2 + wave * 512 + lane * 8);
                PIN3(fg.a0, fg.a1, fg.a2); PIN3(fu.a0, fu.a1, fu.a2); PIN3(b16, b8, sg); PIN3(su, o, os); PIN2(ha, cs);
                unsigned long long mk = layer ? 0x000f000f000f000full : 0x001f001f001f001full;
                unsigned ta = WS_MB + (unsigned)((lane >> 1) * 1024 + 64 + (lane & 1) * 16);
                if (layer == 0) PIN2(oc, ta);
                PINS(mk);
                BAR();
                STAMP(10 + 32 * layer);
                u32x4 tl = {0u, 0u, 0u, 0u};
                if (layer == 0) tl = ld16_sc1(ws, ta);
                const Frag80 b = rows80(b16, b8);
                const f32x4 g0 = mfma32(fg.a0, b.a0, z4), u0 = mfma32(fu.a0, b.a0, z4), g1 = mfma32(fg.a1, b.a1, g0), g2 = mfma16(fg.a2, b.a2, z4), u1 = mfma32(fu.a1, b.a1, u0), u2 = mfma16(fu.a2, b.a2, z4);
                __builtin_amdgcn_sched_barrier(0);
                const f32x4 cg = g1 + g2, cu = u1 + u2;
                st16m2<0>(ws, sg, asu(cg), su, asu(cu), mk);
                STAMP(11 + 32 * layer);
                if (layer == 0) {
                    const unsigned tn = now();
                    WAIT1(tl, 2);
                    unsigned d = min(min(min(tn - tl[0], tn - tl[1]), min(tn - tl[2], tn - tl[3])), 800u);
                    d = min(d, dppu<0xB1>(d)); d = min(d, dppu<0x4E>(d)); d = min(d, dppu<0x124>(d)); d = min(d, dppu<0x128>(d));
                    unsigned tr = tn - min(min((unsigned)__builtin_amdgcn_readlane(d, 0), (unsigned)__builtin_amdgcn_readlane(d, 16)), min((unsigned)__builtin_amdgcn_readlane(d, 32), (unsigned)__builtin_amdgcn_readlane(d, 48)));
                    lds_set(lb + L_CNT + 56, tr | 1u);
                    if ((int)(tn + 12 - X4A - tr) > 0) tr = tn + 12 - X4A;
                    until(tr + X4A);
                } else sched(X4, 12);
                STAMP(16 + 32 * layer);
                float hv; f32x2 st;
                u32x4 a0, a1, a2, a3, ac, as;
                auto load = [&]() __attribute__((always_inline)) {
                    as = ld16_sc1(ws, os);
                    if (layer == 0) ac = ld16_sc1(ws, oc);
                    a0 = ld16_sc1<0>(ws, o); a1 = ld16_sc1<1024>(ws, o); a2 = ld16_sc1<2048>(ws, o); a3 = ld16_sc1<3072>(ws, o);
                    if (layer == 0) asm volatile("s_waitcnt vmcnt(0)" : "+v"(a0), "+v"(a1), "+v"(a2), "+v"(a3), "+v"(ac), "+v"(as) :: "memory");
                    else { asm volatile("s_waitcnt vmcnt(0)" : "+v"(a0), "+v"(a1), "+v"(a2), "+v"(a3), "+v"(as) :: "memory"); ac = as; }
                };
                auto compute = [&]() __attribute__((always_inline)) {
                    float ss, sm;
                    asm volatile("v_add_f32_dpp %0, %2, %2 quad_perm:[1,0,3,2] row_mask:0xf bank_mask:0xf\n\tv_add_f32_dpp %1, %3, %3 quad_perm:[1,0,3,2] row_mask:0xf bank_mask:0xf\n\ts_nop 0\n\t"
                                 "v_add_f32_dpp %0, %0, %0 quad_perm:[2,3,0,1] row_mask:0xf bank_mask:0xf\n\tv_add_f32_dpp %1, %1, %1 quad_perm:[2,3,0,1] row_mask:0xf bank_mask:0xf\n\ts_nop 0"
                                 : "=&v"(ss), "=&v"(sm) : "v"(as[0]), "v"(as[1]));
                    ss = x16_add(ss); sm = x16_add(sm);
                    st = ln_stats(ss, sm);
                    const f32x4 t = (asf(a0) + asf(a1)) + (asf(a2) + asf(a3));
                    const f32x2 gu = sw16(rs32(t[0], t[2]), rs32(t[1], t[3]));
                    if (layer == 0) {
                        cs = sw16(rs32(asf(ac)[0], asf(ac)[2]), rs32(asf(ac)[1], asf(ac)[3]));
                        cs[0] += dpp<0x124>(cs[0]); cs[1] += dpp<0x124>(cs[1]); cs[0] += dpp<0x128>(cs[0]); cs[1] += dpp<0x128>(cs[1]);
                    }
                    const unsigned gw = pk2v((gu - st[0] * cs) * st[1]);
                    const float gv = lo16(gw);
                    hv = bfr(gv * frcp(1.f + ex2(gv * -LOG2E))) * hi16(gw);
                    *(LAS u16*)(size_t)ha = (u16)pk2(hv, hv);
                    __builtin_amdgcn_sched_barrier(0);
                };
                RETRY(load(), max(max(max(um4(a0), um4(a1)), max(um4(a2), um4(a3))), max(um4(ac), um4(as))), compute(), any_nan(hv, hv), layer ? X4 : X4A);
                BAR();
                STAMP(12 + 32 * layer);
                if (layer == 0) {
                    *reinterpret_cast<f32x2*>(smem + L_CS2 + wave * 512 + lane * 8) = cs;
                }
            }
            if (layer == 0) {
                if (wave == 1) {
                    const unsigned o = WS_OWN + (unsigned)(gi * 16384 + 1024 + 2048 + (lane < 48 ? lane : 0) * 16);
                    u32x4 a[8];
                    a[0] = ld16_sc1<-2048>(ws, o); a[1] = ld16_sc1<0>(ws, o); a[2] = ld16_sc1<2048>(ws, o);
                    a[3] = ld16_sc1<-2048>(ws, o + 6144); a[4] = ld16_sc1<0>(ws, o + 6144); a[5] = ld16_sc1<2048>(ws, o + 6144);
                    a[6] = ld16_sc1<-2048>(ws, o + 12288); a[7] = ld16_sc1<0>(ws, o + 12288);
                    WAIT8(a, 0);
                    if (lane < 48) *reinterpret_cast<f32x4*>(smem + L_CS + lane * 16) = SUM8(a);
                }
                Frag80 f[4];
#pragma unroll
                for (int i = 0; i < 4; ++i) { f[i] = frag80(lb + L_WQ + (unsigned)(128 * wave + 32 * i + 8 * p4), 416, r0s, r1s, r2s); PIN3(f[i].a0, f[i].a1, f[i].a2); }
                unsigned b16 = lb + L_XS + (unsigned)(p4 * 160 + kq * 16), b8 = lb + L_XS + (unsigned)(p4 * 160 + 128 + kq * 8);
                unsigned so = WS_H1 + (unsigned)(((gi * 4 + p4) * 8 + gj) * 896 + 256 * wave + q4 * 64 + kq * 16);
                unsigned long long mk = 0x000f000f000f000full;
                PIN3(b16, b8, so); PINS(mk);
                BAR();
                STAMP(33);
                const Frag80 b = rows80(b16, b8);
                f32x4 c[4], d[4];
#pragma unroll
                for (int i = 0; i < 4; ++i) c[i] = mfma32(f[i].a0, b.a0, z4);
#pragma unroll
                for (int i = 0; i < 4; ++i) { c[i] = mfma32(f[i].a1, b.a1, c[i]); d[i] = mfma16(f[i].a2, b.a2, z4); }
                __builtin_amdgcn_sched_barrier(0);
                f32x2 lo, hi;
                asm volatile("s_nop 1\n\ts_mov_b64 exec, %18\n\tv_pk_add_f32 %0, %2, %10\n\tv_pk_add_f32 %1, %3, %11\n\t"
                             "s_lshl_b64 exec, exec, 4\n\tv_pk_add_f32 %0, %4, %12\n\tv_pk_add_f32 %1, %5, %13\n\t"
                             "s_lshl_b64 exec, exec, 4\n\tv_pk_add_f32 %0, %6, %14\n\tv_pk_add_f32 %1, %7, %15\n\t"
                             "s_lshl_b64 exec, exec, 4\n\tv_pk_add_f32 %0, %8, %16\n\tv_pk_add_f32 %1, %9, %17\n\ts_mov_b64 exec, -1"
                             : "=&v"(lo), "=&v"(hi) : "v"(LO2(c[0])), "v"(HI2(c[0])), "v"(LO2(c[1])), "v"(HI2(c[1])), "v"(LO2(c[2])), "v"(HI2(c[2])), "v"(LO2(c[3])), "v"(HI2(c[3])),
                               "v"(LO2(d[0])), "v"(HI2(d[0])), "v"(LO2(d[1])), "v"(HI2(d[1])), "v"(LO2(d[2])), "v"(HI2(d[2])), "v"(LO2(d[3])), "v"(HI2(d[3])), "s"(mk) : "scc");
                st16_sc1(ws, so, asu(__builtin_shufflevector(lo, hi, 0, 1, 2, 3)));
                STAMP(34);
                qk_addr(true); att_addr();
            }
        } else {
            const int t = wave - 3;
            StatAddr sa;
            auto gather32 = [&](unsigned o, unsigned xs, unsigned X, auto mid, auto fin) __attribute__((always_inline)) {
                float v; u32x4 a[8]; f32x4 s; unsigned xw;
                RETRY8(a, o, 1024, s, mid(), { const f32x2 v2 = sw16(rs32(s[0], s[2]), rs32(s[1], s[3])); v = v2[0] + v2[1]; xw = pk2(fin(v), 0.f); *(LAS u16*)(size_t)xs = (u16)xw; __builtin_amdgcn_sched_barrier(0); },
                       any_nan(v, v), X);
                xe = lo16(xw);
            };
            __builtin_amdgcn_s_setprio(0);
            {
                LANE_VARS();
                if (layer == 0) xe = lo16(*reinterpret_cast<const u16*>(smem + L_X0 + q4 * 1280 + gj * 160 + t * 32 + p4 * 8 + kq * 2));
                STAMP(6 + 32 * layer);
                unsigned lo = WS_OWN + (unsigned)(gi * 16384 + layer * 512 + (lane >> 3) * 2048 + t * 128 + (t == 3 ? 0 : (lane & 7) * 16));
                unsigned lw = lb + L_O2 + (unsigned)((lane >> 3) * 416 + t * 128 + (t == 3 ? 0 : (lane & 7) * 16));
                PIN2(lo, lw);
                u32x4 ov = {~0u, ~0u, ~0u, ~0u};
                if (layer != 0 && t < 4) { sched(X2, 20); STAMP(49); ov = ld16_sc1(ws, lo); }
                const unsigned wa = lb + L_WO + (unsigned)(r0s * 160 + 32 * t + 8 * p4), wa1 = lb + L_WO + (unsigned)(r1s * 160 + 32 * t + 8 * p4);
                bf16x8 af[6];
                if (layer == 0) {
#pragma unroll 1
                    for (unsigned spins = 0; spins < (kSpinCap << 4); ++spins) { if (lds_word(lb + L_CNT + 16) >= 8u) break; __builtin_amdgcn_s_sleep(1); }
                }
#pragma unroll
                for (int s = 0; s < 6; ++s) af[s] = cat8(tr64(wa + s * 32 * 160), tr64(wa1 + s * 32 * 160));
                const unsigned h3 = WS_H35 + (unsigned)((gj * 20 + 10 * layer + t) * 8192);
                unsigned so = h3 + (unsigned)(gi * 256 + (n >> 1) * 64 + kq * 16), go = h3 + (unsigned)(4096 + lane * 16);
                unsigned xs = lb + L_XS + (unsigned)(q4 * 160 + t * 32 + p4 * 8 + kq * 2);
                unsigned pb = h3 + (unsigned)(((gi * 7 + t * 5 + 3) & 31) * 256);
                unsigned br = lb + L_O2 + (unsigned)((n & 7) * 416 + kq * 16), mr = lb + L_O2 + (unsigned)((n & 7) * 416 + 384);
                unsigned long long mk = 0x0055005500550055ull;
                PIN3(af[0], af[1], af[2]); PIN3(af[3], af[4], af[5]); PIN3(br, mr, so); PIN3(go, xs, pb); PINS(mk);
                if (t < 4) {
                    WAIT1(ov, 0);
                    if (layer != 0) *(LAS u32x4*)(size_t)lw = ov;
                    if (layer == 0 || __builtin_expect(any_empty(um4(ov)), 0)) {
#pragma unroll 1
                        for (unsigned spins = 0; spins < kSpinCap; ++spins) {
                            if (layer != 0) slip(X2);
                            ov = ld16_sc1(ws, lo);
                            WAIT1(ov, 0);
                            *(LAS u32x4*)(size_t)lw = ov;
                            if (!any_empty(um4(ov))) break;
                            if (layer == 0) __builtin_amdgcn_s_sleep(1);
                        }
                    }
                    if (layer == 0 && t == 2) lds_set(lb + L_CNT + 52, 1u);
                }
                BAR();
                bf16x8 bf[6];
                const f32x2 ml = *(const LAS f32x2*)(size_t)mr;
#pragma unroll
                for (int s = 0; s < 6; ++s) bf[s] = lds16(br + s * 64);
                __builtin_amdgcn_sched_barrier(0);
                f32x4 ca = z4;
#pragma unroll
                for (int s = 0; s < 6; ++s) ca = mfma32(af[s], bf[s], ca);
                const float e = ex2(ml[0] - fmax2(ml[0], dpp<0xB1>(ml[0]))), el = e * ml[1];
                const float f = e * frcp(el + dpp<0xB1>(el));
                float c0, c1, c2, c3;
                asm volatile("s_nop 7\n\t"
                             "v_mul_f32 %0, %4, %8\n\tv_mul_f32 %1, %5, %8\n\tv_mul_f32 %2, %6, %8\n\tv_mul_f32 %3, %7, %8\n\t"
                             "v_add_f32_dpp %0, %0, %0 quad_perm:[1,0,3,2] row_mask:0xf bank_mask:0xf\n\tv_add_f32_dpp %1, %1, %1 quad_perm:[1,0,3,2] row_mask:0xf bank_mask:0xf\n\t"
                             "v_add_f32_dpp %2, %2, %2 quad_perm:[1,0,3,2] row_mask:0xf bank_mask:0xf\n\tv_add_f32_dpp %3, %3, %3 quad_perm:[1,0,3,2] row_mask:0xf bank_mask:0xf"
                             : "=&v"(c0), "=&v"(c1), "=&v"(c2), "=&v"(c3) : "v"(ca[0]), "v"(ca[1]), "v"(ca[2]), "v"(ca[3]), "v"(f));
                st16m(ws, so, asu((f32x4){c0, c1, c2, c3}), mk);
                if (layer == 0 && wave == 7) st4_sc1(ws, WS_MB + (unsigned)(gi * 1024 + 64 + gj * 4), now());
                STAMP(7 + 32 * layer);
                if (layer != 0) sched(X3, 12); else wait_word(ws, pb);
                STAMP(18 + 32 * layer);
                gather32(go, xs, layer ? X3 : 0u, [&]() { sa = stats_addr(lane, h4 + (unsigned)(15360 + gj * 128), 16); }, [&](float s) { return xe + bfr(s); });
                STAMP(9 + 32 * layer);
            }
            BAR();
            if (wave == 3) stats(sa);
            STAMP(11 + 32 * layer);
            {
                LANE_VARS();
                const unsigned im = lb + L_WD + (unsigned)(32 * t + 8 * p4);
                bf16x8 wd0 = cat8(tr64(im + r0s * 160), tr64(im + r1s * 160)); bf16x4 wd1 = tr64(im + (32 + r2s) * 160);
                const unsigned h5 = WS_H35 + (unsigned)((gj * 20 + (layer ? 0 : 5) + t) * 8192);
                unsigned h16 = lb + L_H + (unsigned)(p4 * 96 + kq * 16), h8 = lb + L_H + (unsigned)(p4 * 96 + 64 + kq * 8);
                unsigned so = h5 + (unsigned)(gi * 256 + n * 64 + kq * 16);
                unsigned long long mk = 0x000f000f000f000full;
                PIN2(wd0, wd1); PIN3(h16, h8, so); PINS(mk);
                if (wave == 4) {
                    const unsigned os = h4 + (unsigned)(15360 + (p4 + 4 * (kq & 1)) * 128 + q4 * 16);
                    if (layer == 0) {
#pragma unroll 1
                        for (unsigned spins = 0; spins < (kSpinCap << 4); ++spins) if (lds_word(lb + L_CNT + 56)) break;
                    }
                    sched(layer ? X4 : X4A, 0);
                    u32x4 as;
#pragma unroll 1
                    for (unsigned spins = 0; spins < kSpinCap; ++spins) { as = ld16_sc1(ws, os); WAIT1(as, 0); if (!any_empty(um4(as))) break; }
                    float ss = __uint_as_float(as[0]), sm = __uint_as_float(as[1]), rd = __uint_as_float(as[2]);
                    ss += dpp<0xB1>(ss); sm += dpp<0xB1>(sm); rd += dpp<0xB1>(rd);
                    ss += dpp<0x4E>(ss); sm += dpp<0x4E>(sm); rd += dpp<0x4E>(rd);
                    ss = x16_add(ss); sm = x16_add(sm); rd = x16_add(rd);
                    if (LANES(0x1111ull)) *reinterpret_cast<f32x4*>(smem + L_ST + q4 * 16) = (f32x4){ss, sm, rd, 0.f};
                }
                BAR();
                STAMP(12 + 32 * layer);
                const bf16x8 hb0 = lds16(h16); const bf16x4 hb1 = lds8(h8);
                __builtin_amdgcn_sched_barrier(0);
                const f32x4 c = mfma32(wd0, hb0, z4) + mfma16(wd1, hb1, z4);
                st16m(ws, so, asu(c), mk);
                STAMP(13 + 33 * layer);
                if (layer == 0 || t == gi % 5) {
                    float rs, n1;
                    auto prep = [&]() __attribute__((always_inline)) {
                        const f32x4 sr = *reinterpret_cast<const f32x4*>(smem + L_ST + q4 * 16);
                        const float sw = *reinterpret_cast<const float*>(smem + L_CNT + 12);
                        const f32x2 st = ln_stats(sr[0], sr[1]);
                        const float r = bfr((sr[2] - st[0] * sw) * st[1]);
                        rs = bfr(frcp(1.f + ex2(r * -LOG2E)));
                        n1 = bfr(bfr(xe - st[0]) * st[1]);
                        PIN2(rs, n1);
                    };
                    if (layer == 0) {
                        unsigned go = h5 + (unsigned)(4096 + lane * 16), xs = lb + L_XS + (unsigned)(q4 * 160 + t * 32 + p4 * 8 + kq * 2);
                        PIN2(go, xs);
                        sched(X5A, 8);
                        STAMP(15);
                        gather32(go, xs, X5A, [&]() { prep(); sa = stats_addr(lane, WS_H1 + (unsigned)((gi * 32 + gj) * 896 + 768), 7168); }, [&](float y) { return xe + bfr(n1 + bfr(bfr(y) * rs)); });
                        STAMP(14);
                    } else if (gi < 20) {
                        const int b = gi / 5;
                        unsigned go = h5 + (unsigned)(2048 + (lane >> 2) * 256 + b * 64 + p4 * 16), oo = (unsigned)(q4 * 1280 + gj * 160 + t * 32 + p4 * 8 + kq * 2);
                        PIN2(go, oo);
                        sched(X5B, 8);
                        STAMP(47);
                        float y; u32x4 a0, a1;
                        RETRY({ a0 = ld16_sc1<-2048>(ws, go); a1 = ld16_sc1<2048>(ws, go); prep(); asm volatile("s_waitcnt vmcnt(0)" : "+v"(a0), "+v"(a1) :: "memory"); }, max(um4(a0), um4(a1)),
                              { const f32x4 s = asf(a0) + asf(a1); const f32x2 v2 = sw16(rs32(s[0], s[2]), rs32(s[1], s[3])); y = v2[0] + v2[1]; y += dpp<0x124>(y); y += dpp<0x128>(y); }, any_nan(y, y), 0u);
                        const unsigned xw = pk2(xe + bfr(n1 + bfr(bfr(y) * rs)), 0.f);
                        if (LANES(0x000f000f000f000full << (4 * b))) st2(out, oo, xw);
                        STAMP(45);
                    }
                }
            }
            if (layer == 0) {
                BAR();
                if (wave == 3) { stats(sa); STAMP(34); }
                if (wave != 7) { qk_addr(true); att_addr(); }
                else {
                    const int lane = lane_id();
                    unsigned o = WS_H1 + (unsigned)((gi * 4 + (gj >> 1)) * 7168 + 3584 + (lane < 48 ? lane : 48) * 16), qw;
                    f32x4 cs;
                    PIN1(o);
                    sched(X1, 26);
                    STAMP(51);
                    f32x4 s; u32x4 a[8];
                    RETRY8(a, o, 896, s, { qw = lb + L_Q + (unsigned)(lane * 8); cs = *reinterpret_cast<const f32x4*>(smem + L_CS + lane * 16); PIN2(qw, cs); qk_addr(true); att_addr(); }, { s -= bfr(__int_as_float(__builtin_amdgcn_readlane(__float_as_int(s[1]), 48)) * (1.f / DM)) * cs;
                                           u32x2 w; w[0] = pk2(s[0], s[1]); w[1] = pk2(s[2], s[3]);
                                           if (LANES(0xffff000000000000ull)) { w[0] = __float_as_uint(s[0]); w[1] = __float_as_uint(s[1]); }
                                           *(LAS u32x2*)(size_t)qw = w; __builtin_amdgcn_sched_barrier(0); }, any_nan(s[0] + s[2], s[1] + s[3]), X1);
                }
            }
        }
    };
    chain(IC<0>{});
    att_qk(IC<1>{});
    att_pv(IC<1>{});
    chain(IC<1>{});
    if (wave == 0) asm volatile("buffer_wbl2 sc0 sc1\n\ts_waitcnt vmcnt(0)" ::: "memory");
    while ((int)(now() - tst - 4500u) < 0) __builtin_amdgcn_s_sleep(1);
}

__device__ __attribute__((used, noinline)) void mega_code_end() { asm volatile(".Lmega_code_end:"); }

extern "C" void kernel_launch(void* const* d_in, const int* in_sizes, int n_in, void* d_out, int out_size, void* d_ws,
                              size_t ws_size, hipStream_t stream) {
    const u16* x0 = (const u16*)d_in[0];
    const u16* kc = (const u16*)d_in[1];
    const u16* vc = (const u16*)d_in[2];
    const u16* wq = (const u16*)d_in[3];
    const u16* wo = (const u16*)d_in[4];
    const u16* wr = (const u16*)d_in[6];
    const u16* wg = (const u16*)d_in[7];
    const u16* wu = (const u16*)d_in[8];
    const u16* wd = (const u16*)d_in[9];
    (void)hipFuncSetAttribute((const void*)mega_kernel, hipFuncAttributeMaxDynamicSharedMemorySize, L_END);
    mega_kernel<<<256, 512, L_END, stream>>>(x0, kc, vc, wq, wo, wr, wg, wu, wd, (u16*)d_out, (unsigned char*)d_ws);
}
```

```cpp
#define GMODE 0
#define PACE 58
#define PINIT 200
#define PMIN 50
#define XG2 46
#define WOC 2
#include <hip/hip_runtime.h>
#include <hip/hip_bf16.h>
#include <stdint.h>

typedef unsigned short u16;
typedef unsigned u32x4 __attribute__((ext_vector_type(4)));
typedef unsigned u32x2 __attribute__((ext_vector_type(2)));
typedef float f32x4 __attribute__((ext_vector_type(4)));
typedef short s16x4 __attribute__((ext_vector_type(4)));
typedef __attribute__((ext_vector_type(4))) __bf16 bf16x4;
typedef __attribute__((ext_vector_type(8))) __bf16 bf16x8;
#define LAS __attribute__((address_space(3)))

constexpr int DM = 640, HQ = 32, SK = 4096, DH = 192, WIN = 1024, FF = 1536;
constexpr int QD = HQ * DH;
constexpr float SCALE = 0.07216878364870322f, LOG2E = 1.44269504088896341f;
constexpr unsigned kSpinCap = 1u << 16;
#define GJ(g) (((g) >> 3) & 7)
#define GI(g) (((g) & 7) * 4 + ((g) >> 6))

#define D4A 168
#define D5A 107
#define D1 138
#define D2 192
#define D3 78
#define D4 118
#define D5B 91
constexpr unsigned X4A = D4A, X5A = X4A + D5A, X1 = X5A + D1, X2 = X1 + D2, X3 = X2 + D3, X4 = X3 + D4, X5B = X4 + D5B;
#define GUC 3
#define DG2 15
#define DGU 25
constexpr unsigned XCOST = 16, NUNIT = 384 + XCOST, PBURST = 30;
#define GSTEPS GP = 0, GC = 1, GD = 2, GL = 0, GE = 3, GF = 4
#define WOFF 0
#define FREEW(w) ((w) >= 6)
#define LEAD 12
#define DQ 40
#define DMB 30
constexpr int GSTEPS;
constexpr int QKT = 2;

constexpr unsigned WS_R = 0;
constexpr unsigned WS_B = 1024;
constexpr unsigned WS_MB = 4096;
constexpr unsigned WS_H1 = WS_MB + 256 * 128;
constexpr unsigned WS_OWN = WS_H1 + 32 * 4 * 7168;
constexpr unsigned WS_H35 = WS_OWN + 256 * 2048;
constexpr unsigned WS_H4 = WS_H35 + 160 * 8192;
constexpr unsigned WS_END = WS_H4 + 32 * 16384;
constexpr unsigned WS_DBG = 6680576;
static_assert(WS_END <= WS_DBG, "ws map");

constexpr int L_WQ = 0;
constexpr int L_WO = 33792;
constexpr int L_GU = L_WO + 30720;
constexpr int L_WD = L_GU + 16384;
constexpr int L_X0 = L_WD + 8192;
constexpr int L_WR = L_X0 + 5120;
constexpr int L_XS = L_WR + 2048;
constexpr int L_Q = L_XS + 1024;
constexpr int L_OP = L_Q + 512;
constexpr int L_M = L_OP + 8192;
constexpr int L_H = L_M + 64;
constexpr int L_ST = L_H + 384;
constexpr int L_CS = L_ST + 64;
constexpr int L_CS2 = L_CS + 1024;
constexpr int L_CNT = L_CS2 + 1536;
constexpr int L_P = L_CNT + 80;
constexpr int L_O2 = L_P + 72 * 192;
constexpr int L_DBG = L_O2 + 3328;
constexpr int L_END = L_DBG + 2048;
static_assert(L_END <= 163840 && L_END > 81920, "LDS");

typedef __attribute__((ext_vector_type(2))) __bf16 bf16x2;
typedef float f32x2 __attribute__((ext_vector_type(2)));
__device__ __forceinline__ unsigned pk2(float a, float b) { return __builtin_bit_cast(unsigned, __builtin_convertvector((f32x2){a, b}, bf16x2)); }
__device__ __forceinline__ float lo16(unsigned w) { return __uint_as_float(w << 16); }
__device__ __forceinline__ float hi16(unsigned w) { return __uint_as_float(w & 0xffff0000u); }
__device__ __forceinline__ float bfr(float f) { return lo16(pk2(f, f)); }
template <int CTRL> __device__ __forceinline__ float dpp(float x) {
    return __builtin_bit_cast(float, __builtin_amdgcn_mov_dpp(__builtin_bit_cast(int, x), CTRL, 0xf, 0xf, true));
}
template <int CTRL> __device__ __forceinline__ unsigned dppu(unsigned x) { return (unsigned)__builtin_amdgcn_mov_dpp((int)x, CTRL, 0xf, 0xf, true); }
__device__ __forceinline__ float row16_sum(float v) {
    v += dpp<0xB1>(v); v += dpp<0x4E>(v); v += dpp<0x141>(v); v += dpp<0x140>(v); return v;
}
__device__ __forceinline__ float fmax2(float a, float b) { return __builtin_elementwise_maximum(a, b); }
__device__ __forceinline__ float fmax3(float a, float b, float c) { return fmax2(fmax2(a, b), c); }
__device__ __forceinline__ float x16_add(float v) { auto s = __builtin_amdgcn_permlane16_swap(__float_as_uint(v), __float_as_uint(v), false, false); return __uint_as_float(s[0]) + __uint_as_float(s[1]); }
__device__ __forceinline__ float x32_add(float v) { auto s = __builtin_amdgcn_permlane32_swap(__float_as_uint(v), __float_as_uint(v), false, false); return __uint_as_float(s[0]) + __uint_as_float(s[1]); }
__device__ __forceinline__ float wave_sum(float v) { return x32_add(x16_add(row16_sum(v))); }
__device__ __forceinline__ float rs32(float a, float b) { auto s = __builtin_amdgcn_permlane32_swap(__float_as_uint(a), __float_as_uint(b), false, false); return __uint_as_float(s[0]) + __uint_as_float(s[1]); }
__device__ __forceinline__ f32x2 sw16(float a, float b) { auto s = __builtin_amdgcn_permlane16_swap(__float_as_uint(a), __float_as_uint(b), false, false); return (f32x2){__uint_as_float(s[0]), __uint_as_float(s[1])}; }

#define BAR() asm volatile("s_waitcnt lgkmcnt(0)\n\ts_barrier" ::: "memory")
#define VMW(n) asm volatile("s_waitcnt vmcnt(%0)" :: "i"(n) : "memory")

template <int OFF = 0> __device__ __forceinline__ u32x4 ld16_sc1(const void* sbase, unsigned voff) {
    u32x4 r; asm volatile("global_load_dwordx4 %0, %1, %2 offset:%3 sc1" : "=v"(r) : "v"(voff), "s"(sbase), "i"(OFF) : "memory"); return r;
}
__device__ __forceinline__ unsigned ld4_sc1(const void* sbase, unsigned voff) {
    unsigned r; asm volatile("global_load_dword %0, %1, %2 sc1" : "=v"(r) : "v"(voff), "s"(sbase) : "memory"); return r;
}
template <int OFF = 0> __device__ __forceinline__ void st16_sc1(void* sbase, unsigned voff, u32x4 v) {
    asm volatile("global_store_dwordx4 %0, %1, %2 offset:%3 sc1\n\ts_nop 1" :: "v"(voff), "v"(v), "s"(sbase), "i"(OFF) : "memory");
}
__device__ __forceinline__ void st4_sc1(void* sbase, unsigned voff, unsigned v) {
    asm volatile("global_store_dword %0, %1, %2 sc1" :: "v"(voff), "v"(v), "s"(sbase) : "memory");
}
__device__ __forceinline__ void st8(void* sbase, unsigned voff, u32x2 v) {
    asm volatile("global_store_dwordx2 %0, %1, %2" :: "v"(voff), "v"(v), "s"(sbase) : "memory");
}
__device__ __forceinline__ void st2(void* sbase, unsigned voff, unsigned v) {
    asm volatile("global_store_short %0, %1, %2" :: "v"(voff), "v"(v), "s"(sbase) : "memory");
}
template <int IMM> __device__ __forceinline__ u32x4 ld16_nt(const void* sbase, unsigned voff) {
    u32x4 r; asm volatile("global_load_dwordx4 %0, %1, %2 offset:%3 nt" : "=v"(r) : "v"(voff), "s"(sbase), "i"(IMM) : "memory"); return r;
}
__device__ __forceinline__ void dma16(const void* sbase, unsigned voff, unsigned lds_dst) {
    unsigned keep;
    asm volatile("s_mov_b32 %0, m0\n\ts_mov_b32 m0, %3\n\ts_nop 0\n\tglobal_load_lds_dwordx4 %1, %2 nt\n\ts_mov_b32 m0, %0"
                 : "=&s"(keep) : "v"(voff), "s"(sbase), "s"(lds_dst) : "memory");
}
__device__ __forceinline__ void dma16p(const void* sbase, unsigned voff, unsigned lds_dst) {
    unsigned keep;
    asm volatile("s_mov_b32 %0, m0\n\ts_mov_b32 m0, %3\n\ts_nop 0\n\tglobal_load_lds_dwordx4 %1, %2\n\ts_mov_b32 m0, %0"
                 : "=&s"(keep) : "v"(voff), "s"(sbase), "s"(lds_dst) : "memory");
}
__device__ __forceinline__ unsigned um4(u32x4 a) { return max(max(a[0], a[1]), max(a[2], a[3])); }
__device__ __forceinline__ bool none(unsigned t) { return __builtin_amdgcn_ballot_w64(t != 0u) == 0ull; }
__device__ __forceinline__ bool any_empty(unsigned mx) { return __builtin_amdgcn_ballot_w64(mx == ~0u) != 0ull; }
__device__ __forceinline__ unsigned long long any_nan(float a, float b) { return __builtin_amdgcn_ballot_w64(__builtin_isunordered(a, b)); }
__device__ __forceinline__ f32x4 asf(u32x4 a) { return __builtin_bit_cast(f32x4, a); }
__device__ __forceinline__ u32x4 asu(f32x4 a) { return __builtin_bit_cast(u32x4, a); }
#define LANES(m) __builtin_amdgcn_inverse_ballot_w64(m)

__device__ __forceinline__ bf16x4 tr64(unsigned lds_addr) { return __builtin_amdgcn_ds_read_tr16_b64_v4bf16((LAS bf16x4*)(size_t)lds_addr); }
__device__ __forceinline__ bf16x8 cat8(bf16x4 a, bf16x4 b) { return __builtin_shufflevector(a, b, 0, 1, 2, 3, 4, 5, 6, 7); }
__device__ __forceinline__ bf16x8 lds16(unsigned a) { return *(const LAS bf16x8*)(size_t)a; }
__device__ __forceinline__ bf16x4 lds8(unsigned a) { return *(const LAS bf16x4*)(size_t)a; }
__device__ __forceinline__ f32x4 mfma32(bf16x8 a, bf16x8 b, f32x4 c) { return __builtin_amdgcn_mfma_f32_16x16x32_bf16(a, b, c, 0, 0, 0); }
__device__ __forceinline__ f32x4 mfma32u(u32x4 a, u32x4 b, f32x4 c) { return mfma32(__builtin_bit_cast(bf16x8, a), __builtin_bit_cast(bf16x8, b), c); }
__device__ __forceinline__ f32x4 mfma16(bf16x4 a, bf16x4 b, f32x4 c) {
    return __builtin_amdgcn_mfma_f32_16x16x16bf16_1k(__builtin_bit_cast(s16x4, a), __builtin_bit_cast(s16x4, b), c, 0, 0, 0);
}

#define STAMP(i) do {} while (0)

__device__ __forceinline__ void wait_word(const void* sbase, unsigned off) {
    unsigned t, cnt = kSpinCap;
    asm volatile("1:\n\tglobal_load_dword %0, %2, %3 sc1\n\ts_sub_u32 %1, %1, 1\n\ts_waitcnt vmcnt(0)\n\tv_cmp_eq_u32 vcc, -1, %0\n\ts_cbranch_vccz 2f\n\ts_cbranch_scc0 1b\n2:"
                 : "=&v"(t), "+s"(cnt) : "v"(off), "s"(sbase) : "memory", "vcc", "scc");
}
template <int OFF = 0> __device__ __forceinline__ void st16m(void* sbase, unsigned voff, u32x4 v, unsigned long long mask) {
    asm volatile("s_mov_b64 exec, %4\n\tglobal_store_dwordx4 %0, %1, %2 offset:%3 sc1\n\ts_mov_b64 exec, -1\n\ts_nop 0" :: "v"(voff), "v"(v), "s"(sbase), "i"(OFF), "s"(mask) : "memory");
}
template <int OFF> __device__ __forceinline__ void st16m2(void* sbase, unsigned voff, u32x4 v, unsigned voff2, u32x4 v2, unsigned long long mask) {
    asm volatile("s_mov_b64 exec, %6\n\tglobal_store_dwordx4 %0, %1, %2 sc1\n\tglobal_store_dwordx4 %4, %5, %2 offset:%3 sc1\n\ts_mov_b64 exec, -1\n\ts_nop 0"
                 :: "v"(voff), "v"(v), "s"(sbase), "i"(OFF), "v"(voff2), "v"(v2), "s"(mask) : "memory");
}
#define PINS(a) asm volatile("" : "+s"(a))
#define RETRY(LOAD, EMPTY, COMPUTE, NAN_, X) do { LOAD; COMPUTE; if (__builtin_expect((NAN_) != 0ull, 0)) { \
    _Pragma("unroll 1") for (unsigned spins_ = 0; spins_ < kSpinCap; ++spins_) { slip(X); LOAD; if (!any_empty(EMPTY)) break; } COMPUTE; } } while (0)
__device__ __forceinline__ unsigned now() { return (unsigned)__builtin_amdgcn_s_memrealtime(); }
__device__ __forceinline__ void until(unsigned t) {
    asm volatile("1:\n\ts_memrealtime vcc\n\ts_waitcnt lgkmcnt(0)\n\ts_sub_u32 vcc_lo, %0, vcc_lo\n\ts_cmp_lt_u32 vcc_lo, 0x400\n\ts_cbranch_scc1 1b" :: "s"(t - 1u) : "vcc", "scc", "memory");
}
__device__ __forceinline__ unsigned lds_word(unsigned a) {
    unsigned t_; asm volatile("ds_read_b32 %0, %1\n\ts_waitcnt lgkmcnt(0)\n\ts_nop 0" : "=v"(t_) : "v"(a) : "memory");
    return __builtin_amdgcn_readfirstlane(t_);
}
__device__ __forceinline__ void lds_set(unsigned a, unsigned v) { asm volatile("ds_write_b32 %0, %1" :: "v"(a), "v"(v) : "memory"); }
__device__ __forceinline__ int swz(int r) { return r ^ (((r >> 3) & 1) << 2); }
__device__ __forceinline__ f32x2 unpk(unsigned w) { return (f32x2){lo16(w), hi16(w)}; }
__device__ __forceinline__ unsigned pk2v(f32x2 v) { return __builtin_bit_cast(unsigned, __builtin_convertvector(v, bf16x2)); }
__device__ __forceinline__ float frcp(float x) { return __builtin_amdgcn_rcpf(x); }
__device__ __forceinline__ float ex2(float x) { return __builtin_amdgcn_exp2f(x); }
__device__ __forceinline__ int lane_id() { int l; asm volatile("v_mbcnt_lo_u32_b32 %0, -1, 0\n\tv_mbcnt_hi_u32_b32 %0, -1, %0" : "=v"(l)); return l & 63; }
__device__ __forceinline__ f32x2 ln_stats(float ss, float s) {
    const float mean = s * (1.f / DM);
    const float var = fmax2(__builtin_fmaf(-mean, mean, ss * (1.f / DM)), 0.f);
    const unsigned mv = pk2(mean, var);
    return (f32x2){lo16(mv), frcp(bfr(__builtin_amdgcn_sqrtf(bfr(hi16(mv) + 1e-5f))))};
}
#define GATHER8(a, o, S) do { a[0] = ld16_sc1<-4 * (S)>(ws, o); a[1] = ld16_sc1<-3 * (S)>(ws, o); a[2] = ld16_sc1<-2 * (S)>(ws, o); a[3] = ld16_sc1<-(S)>(ws, o); \
    a[4] = ld16_sc1<0>(ws, o); a[5] = ld16_sc1<(S)>(ws, o); a[6] = ld16_sc1<2 * (S)>(ws, o); a[7] = ld16_sc1<3 * (S)>(ws, o); } while (0)
#define WAIT8(a, N) asm volatile("s_waitcnt vmcnt(%8)" : "+v"((a)[0]), "+v"((a)[1]), "+v"((a)[2]), "+v"((a)[3]), "+v"((a)[4]), "+v"((a)[5]), "+v"((a)[6]), "+v"((a)[7]) : "i"(N) : "memory")
#define UM32(a) max(max(max(um4(a[0]), um4(a[1])), max(um4(a[2]), um4(a[3]))), max(max(um4(a[4]), um4(a[5])), max(um4(a[6]), um4(a[7]))))
#define SUM8(a) (((((((asf(a[0]) + asf(a[1])) + asf(a[2])) + asf(a[3])) + asf(a[4])) + asf(a[5])) + asf(a[6])) + asf(a[7]))
#define WAIT1(x, N) asm volatile("s_waitcnt vmcnt(%1)" : "+v"(x) : "i"(N) : "memory")
#define LO2(x) __builtin_shufflevector(x, x, 0, 1)
#define HI2(x) __builtin_shufflevector(x, x, 2, 3)
#define ACC8(a, s) do { f32x2 l_, h_; \
    asm volatile("s_waitcnt vmcnt(5)\n\tv_pk_add_f32 %0, %2, %4\n\tv_pk_add_f32 %1, %3, %5\n\tv_pk_add_f32 %0, %0, %6\n\tv_pk_add_f32 %1, %1, %7\n\t" \
                 "s_waitcnt vmcnt(3)\n\tv_pk_add_f32 %0, %0, %8\n\tv_pk_add_f32 %1, %1, %9\n\tv_pk_add_f32 %0, %0, %10\n\tv_pk_add_f32 %1, %1, %11\n\t" \
                 "s_waitcnt vmcnt(1)\n\tv_pk_add_f32 %0, %0, %12\n\tv_pk_add_f32 %1, %1, %13\n\tv_pk_add_f32 %0, %0, %14\n\tv_pk_add_f32 %1, %1, %15\n\t" \
                 "s_waitcnt vmcnt(0)\n\tv_pk_add_f32 %0, %0, %16\n\tv_pk_add_f32 %1, %1, %17\n\ts_nop 0" \
                 : "=&v"(l_), "=&v"(h_) : "v"(LO2(a[0])), "v"(HI2(a[0])), "v"(LO2(a[1])), "v"(HI2(a[1])), "v"(LO2(a[2])), "v"(HI2(a[2])), "v"(LO2(a[3])), "v"(HI2(a[3])), \
                   "v"(LO2(a[4])), "v"(HI2(a[4])), "v"(LO2(a[5])), "v"(HI2(a[5])), "v"(LO2(a[6])), "v"(HI2(a[6])), "v"(LO2(a[7])), "v"(HI2(a[7])) : "memory"); \
    s = __builtin_shufflevector(l_, h_, 0, 1, 2, 3); } while (0)
#define RETRY8(a, o, S, s, MID, POST, NAN_, X) do { GATHER8(a, o, S); MID; ACC8(a, s); POST; if (__builtin_expect((NAN_) != 0ull, 0)) { \
    _Pragma("unroll 1") for (unsigned spins_ = 0; spins_ < kSpinCap; ++spins_) { slip(X); GATHER8(a, o, S); WAIT8(a, 0); if (!any_empty(UM32(a))) break; } s = SUM8(a); POST; } } while (0)
#define PIN1(a) asm volatile("" : "+v"(a))
#define PIN2(a, b) asm volatile("" : "+v"(a), "+v"(b))
#define PIN3(a, b, c) asm volatile("" : "+v"(a), "+v"(b), "+v"(c))
#define LANE_VARS() const int lane = lane_id(); \
    const int kq = lane >> 4, n = lane & 15, q4 = n >> 2, p4 = lane & 3; \
    const int r0s = 8 * kq + q4 + 4 * (kq & 1), r1s = 8 * kq + q4 + 4 * ((kq & 1) ^ 1);     \
    const int r2s = swz(4 * kq + q4);                                                       \
    (void)r0s; (void)r1s; (void)r2s; (void)p4
template <int V> struct IC { static constexpr int value = V; };
struct Frag80 { bf16x8 a0, a1; bf16x4 a2; };

__global__ void __launch_bounds__(512, 2)
mega_kernel(const u16* __restrict__ x0, const u16* __restrict__ kc, const u16* __restrict__ vc, const u16* __restrict__ wq,
            const u16* __restrict__ wo, const u16* __restrict__ wr, const u16* __restrict__ wg, const u16* __restrict__ wu,
            const u16* __restrict__ wd, u16* __restrict__ out, unsigned char* __restrict__ ws) {
    extern __shared__ __attribute__((aligned(16))) unsigned char smem[];
    const unsigned lb = (unsigned)(size_t)smem;
    const int wave = __builtin_amdgcn_readfirstlane(threadIdx.x >> 6);
    const int g = blockIdx.x, gj = GJ(g), gi = GI(g);
    const f32x4 z4 = {0.f, 0.f, 0.f, 0.f};
    u32x4 kr[24], vr[24];
    auto sched = [&](unsigned X, unsigned gap) {
        unsigned v; unsigned long long c;
        asm volatile("s_memrealtime %1\n\tds_read_b32 %0, %2\n\ts_waitcnt lgkmcnt(0)" : "=v"(v), "=&s"(c) : "v"(lb + L_CNT + 56) : "memory");
        const unsigned n = (unsigned)c + gap;
        unsigned t = __builtin_amdgcn_readfirstlane(v) + X;
        if ((int)(n - t) > 0) t = n;
        until(t);
    };
    auto slip = [&](unsigned X) {
        if (X) lds_set(lb + L_CNT + 56, now() - X);
    };
    float xe = 0.f;

    auto code_pf = [&](int slice, int lane) {
        unsigned long long pc0; unsigned len, back, t;
        asm volatile("s_getpc_b64 %0\n.Lmega_pf%=:\n\ts_mov_b32 %1, .Lmega_code_end-.Lmega_code_begin\n\ts_mov_b32 %2, .Lmega_pf%=-.Lmega_code_begin" : "=s"(pc0), "=s"(len), "=s"(back));
        unsigned off = (unsigned)(slice * 4096 + lane * 64); if (off > len) off = len;
        asm volatile("global_load_dword %0, %1, %2" : "=v"(t) : "v"(off & ~3u), "s"(pc0 - back) : "memory");
        return t;
    };
    asm volatile(".Lmega_code_begin:");
    auto fix_k1 = [&](int t, int s, int N) {
        asm volatile("s_waitcnt vmcnt(%2)" : "+v"(kr[(2 * t) * 3 + s]), "+v"(kr[(2 * t + 1) * 3 + s]) : "i"(N) : "memory");
        const u32x4 A = kr[(2 * t) * 3 + s], Bv = kr[(2 * t + 1) * 3 + s];
        u32x4 An, Bn;
#pragma unroll
        for (int j = 0; j < 4; ++j) {
            An[j] = (unsigned)__builtin_amdgcn_update_dpp((int)A[j], (int)Bv[j], 0x128, 0xf, 0xc, false);
            Bn[j] = (unsigned)__builtin_amdgcn_update_dpp((int)Bv[j], (int)A[j], 0x128, 0xf, 0x3, false);
        }
        kr[(2 * t) * 3 + s] = An; kr[(2 * t + 1) * 3 + s] = Bn;
        PIN2(kr[(2 * t) * 3 + s], kr[(2 * t + 1) * 3 + s]);
    };
    auto trans_v = [&](int u, int N) {
#pragma unroll
        for (int r2 = 0; r2 < 4; ++r2) {
            asm volatile("s_waitcnt vmcnt(%2)" : "+v"(vr[u * 8 + 2 * r2]), "+v"(vr[u * 8 + 2 * r2 + 1]) : "i"(N + 6 - 2 * r2) : "memory");
#pragma unroll
            for (int dw = 0; dw < 4; ++dw) {
                const unsigned a = vr[u * 8 + 2 * r2][dw], bb = vr[u * 8 + 2 * r2 + 1][dw];
                vr[u * 8 + 2 * r2][dw] = __builtin_amdgcn_perm(bb, a, 0x05040100u);
                vr[u * 8 + 2 * r2 + 1][dw] = __builtin_amdgcn_perm(bb, a, 0x07060302u);
            }
            PIN2(vr[u * 8 + 2 * r2], vr[u * 8 + 2 * r2 + 1]);
        }
#pragma unroll
        for (int hl = 0; hl < 2; ++hl)
#pragma unroll
            for (int i = 0; i < 4; ++i)
#pragma unroll
                for (int j = i + 1; j < 4; ++j)
                    asm volatile("v_swap_b32 %0, %1" : "+v"(vr[u * 8 + 2 * i + hl][j]), "+v"(vr[u * 8 + 2 * j + hl][i]));
        asm volatile("s_nop 1");
        __builtin_amdgcn_sched_barrier(0);
    };
    auto frag80 = [&](unsigned im, int rs, int r0s, int r1s, int r2s) -> Frag80 {
        return Frag80{cat8(tr64(im + r0s * rs), tr64(im + r1s * rs)), cat8(tr64(im + (r0s + 32) * rs), tr64(im + (r1s + 32) * rs)), tr64(im + (r2s + 64) * rs)};
    };
    auto mm80 = [&](const Frag80& a, const Frag80& b) -> f32x4 {
        return mfma32(a.a1, b.a1, mfma32(a.a0, b.a0, z4)) + mfma16(a.a2, b.a2, z4);
    };
    auto rows80 = [&](unsigned a16, unsigned a8) -> Frag80 { const Frag80 f{lds16(a16), lds16(a16 + 64), lds8(a8)}; __builtin_amdgcn_sched_barrier(0); return f; };
    auto empty35 = [&](int q0, int part, int lane) {
        st16m(ws, WS_H35 + (unsigned)(((gj * 20 + q0 + 4 * part + (lane >> 4)) * 32 + gi) * 256 + (lane & 15) * 16), (u32x4){~0u, ~0u, ~0u, ~0u}, part ? 0xffffull : ~0ull);
    };
    auto empty4 = [&](int part, int lane) {
        st16_sc1(ws, WS_H4 + (unsigned)(gi * 16384) + (unsigned)((part == 0 || lane < 32) ? (part * 2 + (lane >> 5)) * 4096 + gj * 512 + (lane & 31) * 16
                                                                                 : 12288 + ((lane - 32) >> 3) * 1024 + gj * 128 + (lane & 7) * 16), (u32x4){~0u, ~0u, ~0u, ~0u});
    };
    const unsigned tst = now();
    const int lead = FREEW(wave) ? LEAD * PACE * 4 : 0;
    auto pace = [&](unsigned n, bool wait = true) {
        unsigned u, w;
        asm volatile("s_mov_b64 exec, 1\n\tds_add_rtn_u32 %0, %2, %3\n\tds_max_u32 %2, %4 offset:12\n\tds_add_rtn_u32 %1, %2, %5 offset:12\n\ts_mov_b64 exec, -1\n\ts_waitcnt lgkmcnt(0)"
                     : "=&v"(u), "=&v"(w) : "v"(lb + L_CNT + 60), "v"(n), "v"((now() - tst) << 6), "v"(n * PMIN * 4u) : "memory");
        int tn = PINIT * 64 + __builtin_amdgcn_readfirstlane(u) * (PACE * 4);
        const int tw = __builtin_amdgcn_readfirstlane(w) - PBURST * 64;
        if (tw > tn) tn = tw;
        tn -= lead;
        if (wait) {
#pragma unroll 1
            for (int k = 0; k < 2048 && (int)((now() - tst) << 6) < tn; ++k) {}
        }
    };
    auto slot = [&](unsigned u) {
        const unsigned tn = tst + PINIT + u * PACE / 16;
#pragma unroll 1
        for (int k = 0; k < 8192 && (int)(now() - tn) < 0; ++k) __builtin_amdgcn_s_sleep(1);
    };
    auto load_wo = [&](int k, int lane) {
        const int c = k * 64 + lane, pp = c / 10, cc = c - 10 * pp, r = swz(pp);
        const unsigned f = (unsigned)((32 * ((r >> 1) & 3) + 8 * (r & 1) + (r >> 3)) * 1280 + cc * 16);
        const unsigned f2 = (unsigned)((128 + 32 * ((r >> 1) & 1) + 8 * (r & 1) + (r >> 2)) * 1280 + cc * 16);
        const u16* wb = wo + (size_t)gi * DH * DM + gj * 80;
#pragma unroll
        for (int a2 = 0; a2 < 6; ++a2) { if (a2 % 3 == 0) slot(NUNIT - WOFF + (2 * k + a2 / 3) * WOC); dma16(wb + (a2 < 4 ? 16 * (a2 >> 1) + 4 * (a2 & 1) : 16 * (a2 & 1)) * DM, a2 < 4 ? f : f2, lb + L_WO + (5 * a2 + k) * 1024); }
    };
    auto load_wd = [&](int lane) {
        const u16* db = wd + gi * 48 * DM + gj * 80;
#pragma unroll 1
        for (int k = 0; k < 5; ++k) {
            const int c = k * 64 + lane, pp = c / 10, cc = c - 10 * pp;
            const unsigned fd = (unsigned)(swz(pp) * 1280 + cc * 16);
            dma16(db, fd, lb + L_WD + k * 1024);
            if (k < 3) dma16(db + 32 * DM, (k < 2 || lane < 32) ? fd : 0u, lb + L_WD + (5 + k) * 1024);
        }
    };
    auto load_gu = [&](int m, int lane) {
#pragma unroll
        for (int k = 0; k < 3; ++k) {
            const int c = k * 64 + lane, row = c / 6, cc = c - 6 * row;
            const unsigned o = (unsigned)((swz(row) * FF + cc * 8) * 2), d = lb + L_GU + m * 8192 + k * 1024;
            const u16* wb = (m == 0 ? wg : wu) + gj * 80 * FF + gi * 48;
            dma16(wb, o, d); dma16(wb + 32 * FF, o, d + 3072);
            if (k < 2) dma16(wb + 64 * FF, (k == 0 || lane < 32) ? o : 0u, d + 6144);
        }
    };
    auto lds_add = [&](int off, unsigned v) { asm volatile("s_mov_b64 exec, 1\n\tds_add_u32 %0, %1\n\ts_mov_b64 exec, -1" :: "v"(lb + L_CNT + off), "v"(v) : "memory"); };
    auto send_flag = [&](int lane) {
        st4_sc1(ws, WS_MB + (unsigned)((gi * 8 + (lane & 7)) * 128 + gj * 4), 1u);
    };
    auto tiles4 = [&](const Frag80 (&f)[4], const Frag80& b, unsigned long long mk) __attribute__((always_inline)) -> u32x4 {
        f32x4 c[4], d[4];
#pragma unroll
        for (int i = 0; i < 4; ++i) c[i] = mfma32(f[i].a0, b.a0, z4);
#pragma unroll
        for (int i = 0; i < 4; ++i) { c[i] = mfma32(f[i].a1, b.a1, c[i]); d[i] = mfma16(f[i].a2, b.a2, z4); }
        __builtin_amdgcn_sched_barrier(0);
        f32x2 lo, hi;
        asm volatile("s_nop 1\n\ts_mov_b64 exec, %18\n\tv_pk_add_f32 %0, %2, %10\n\tv_pk_add_f32 %1, %3, %11\n\t"
                     "s_lshl_b64 exec, exec, 4\n\tv_pk_add_f32 %0, %4, %12\n\tv_pk_add_f32 %1, %5, %13\n\t"
                     "s_lshl_b64 exec, exec, 4\n\tv_pk_add_f32 %0, %6, %14\n\tv_pk_add_f32 %1, %7, %15\n\t"
                     "s_lshl_b64 exec, exec, 4\n\tv_pk_add_f32 %0, %8, %16\n\tv_pk_add_f32 %1, %9, %17\n\ts_mov_b64 exec, -1"
                     : "=&v"(lo), "=&v"(hi) : "v"(LO2(c[0])), "v"(HI2(c[0])), "v"(LO2(c[1])), "v"(HI2(c[1])), "v"(LO2(c[2])), "v"(HI2(c[2])), "v"(LO2(c[3])), "v"(HI2(c[3])),
                       "v"(LO2(d[0])), "v"(HI2(d[0])), "v"(LO2(d[1])), "v"(HI2(d[1])), "v"(LO2(d[2])), "v"(HI2(d[2])), "v"(LO2(d[3])), "v"(HI2(d[3])), "s"(mk) : "scc");
        return asu(__builtin_shufflevector(lo, hi, 0, 1, 2, 3));
    };
    auto empty1 = [&](int half, int lane) {
        const unsigned o = WS_H1 + (unsigned)(((gi * 4 + 2 * half) * 8 + gj) * 896 + (lane < 56 ? lane : 55) * 16);
        st16_sc1(ws, o, (u32x4){~0u, ~0u, ~0u, ~0u}); st16_sc1(ws, o + 7168, (u32x4){~0u, ~0u, ~0u, ~0u});
    };
    auto answer = [&](int lane) {
#pragma unroll
        for (int k = 0; k < 4; ++k) st4_sc1(ws, WS_MB + (unsigned)((k * 64 + lane) * 128 + 32), 1u);
    };
    struct StatAddr { unsigned a16, b16, a8, b8, dst; };
    auto stats_addr = [&](int lane, unsigned dbase, int dstride) -> StatAddr {
        const int kq = lane >> 4, q4 = (lane >> 2) & 3, p4 = lane & 3;
        const int x1 = LANES(0x4444444444444444ull) ? L_WR + gj * 160 : L_XS + q4 * 160, xa = LANES(0xaaaaaaaaaaaaaaaaull) ? L_XS + 640 : x1;
        StatAddr r{lb + (unsigned)(xa + kq * 16), lb + (unsigned)(L_XS + p4 * 160 + kq * 16), lb + (unsigned)(xa + 128 + kq * 8), lb + (unsigned)(L_XS + p4 * 160 + 128 + kq * 8), dbase + (unsigned)(kq * dstride)};
        PIN3(r.a16, r.b16, r.dst); PIN2(r.a8, r.b8);
        return r;
    };
    auto stats = [&](const StatAddr& r) {
        const bf16x8 a0 = lds16(r.a16), b0 = lds16(r.b16), a1 = lds16(r.a16 + 64), b1 = lds16(r.b16 + 64);
        const bf16x4 a2 = lds8(r.a8), b2 = lds8(r.b8);
        unsigned long long mk = 0x0008000400020001ull; PINS(mk);
        __builtin_amdgcn_sched_barrier(0);
        st16m(ws, r.dst, asu(mfma32(a1, b1, mfma32(a0, b0, z4)) + mfma16(a2, b2, z4)), mk);
    };
    unsigned qa, pw, ma, ea = 0u; float mwv;
    auto qk_addr = [&](bool L1) {
        LANE_VARS();
        qa = lb + L_Q + (unsigned)(kq * 16);
        pw = lb + L_P + (unsigned)((7 + 8 * wave) * 192 + ((kq >> 1) * 4 + (q4 ^ (kq & 2))) * 16 + ((kq & 1) * 4 + p4) * 2);
        ma = lb + L_M + (unsigned)(wave * 4);
        PIN3(qa, pw, ma);
        if (L1) {
            const int part = wave & 1;
            ea = wave < 6 ? WS_H4 + (unsigned)(gi * 16384) + (unsigned)((part == 0 || lane < 32) ? (part * 2 + (lane >> 5)) * 4096 + gj * 512 + (lane & 31) * 16 : 12288 + ((lane - 32) >> 3) * 1024 + gj * 128 + (lane & 7) * 16)
                          : WS_H35 + (unsigned)(((gj * 20 + 4 * part + (lane >> 4)) * 32 + gi) * 256 + (lane & 15) * 16);
            PIN1(ea);
            if (wave < 4) __builtin_amdgcn_s_setprio(2);
        }
    };
    auto att_qk = [&](auto LC) {
        constexpr int layer = decltype(LC)::value;
        if (layer == 0) qk_addr(false);
        if (layer != 0) {
            BAR();
            if (wave >= 4) st16m(ws, ea, (u32x4){~0u, ~0u, ~0u, ~0u}, wave == 7 ? 0xffffull : ~0ull);
        } else {
#pragma unroll 1
            for (unsigned spins = 0; spins < (kSpinCap << 4); ++spins) {
                if (lds_word(lb + L_CNT + 4) != 0u) break;
                __builtin_amdgcn_s_sleep(1);
            }
        }
        STAMP(3 + 32 * layer);
        f32x4 acc[4];
        u32x4 qf[6];
#pragma unroll
        for (int u = 0; u < 6; ++u) qf[u] = *(const LAS u32x4*)(size_t)(qa + u * 64);
        f32x2 mom = {0.f, 0.f};
        if (layer != 0) mom = *(const LAS f32x2*)(size_t)(qa + 384);
        __builtin_amdgcn_sched_barrier(0);
        float rq = 1.f, sc = SCALE;
        unsigned long long k1 = 0xffff0000ffff0000ull, k2 = 0xffffffff00000000ull;
        PINS(k1); PINS(k2); PINS(sc);
        float mn = mom[1]; if (layer != 0) rq = mom[0];
#pragma unroll
        for (int u = 0; u < 6; ++u) {
#pragma unroll
            for (int t = 0; t < 4; ++t) {
                acc[t] = mfma32u(qf[u], kr[(2 * t + (u & 1)) * 3 + (u >> 1)], u ? acc[t] : z4);
                if (layer != 0) {
                    __builtin_amdgcn_sched_barrier(0);
                    switch (4 * u + t) {
                    case 7: asm volatile("v_mul_f32 %0, 0x3acccccd, %0" : "+v"(mn)); break;
                    case 8: asm volatile("v_mul_f32 %0, 0x3acccccd, %0" : "+v"(rq)); break;
                    case 9: asm volatile("v_fma_f32 %0, -%1, %1, %0" : "+v"(rq) : "v"(mn)); break;
                    case 10: asm volatile("v_maximum3_f32 %0, %0, 0, 0" : "+v"(rq)); break;
                    case 11: case 14: case 17: asm volatile("v_cvt_pk_bf16_f32 %0, %0, %0" : "+v"(rq)); break;
                    case 12: case 15: case 18: asm volatile("v_lshlrev_b32 %0, 16, %0" : "+v"(rq)); break;
                    case 13: asm volatile("v_add_f32 %0, 0x3727c5ac, %0" : "+v"(rq)); break;
                    case 16: asm volatile("v_sqrt_f32 %0, %0" : "+v"(rq)); break;
                    case 19: asm volatile("v_rcp_f32 %0, %0" : "+v"(rq)); break;
                    default: break;
                    }
                    __builtin_amdgcn_sched_barrier(0);
                }
            }
        }
        const float m4 = fmax2(fmax3(acc[0][0], acc[1][0], acc[2][0]), acc[3][0]);
        const float s01 = LANES(k1) ? acc[1][0] : acc[0][0], s23 = LANES(k1) ? acc[3][0] : acc[2][0];
        const float sv = LANES(k2) ? s23 : s01;
        const unsigned w1 = layer ? pk2(sv * rq, m4 * rq) : pk2(sv, m4), w2 = pk2(lo16(w1) * sc, hi16(w1) * sc);
        float m = hi16(w2);
        asm volatile("s_nop 1\n\tv_max_f32_dpp %0, %0, %0 quad_perm:[1,0,3,2] row_mask:0xf bank_mask:0xf\n\ts_nop 1\n\tv_max_f32_dpp %0, %0, %0 quad_perm:[2,3,0,1] row_mask:0xf bank_mask:0xf\n\t"
                     "s_nop 1\n\tv_max_f32_dpp %0, %0, %0 row_ror:4 row_mask:0xf bank_mask:0xf\n\ts_nop 1\n\tv_max_f32_dpp %0, %0, %0 row_ror:8 row_mask:0xf bank_mask:0xf" : "+v"(m));
        asm volatile("" :: "v"(acc[0]), "v"(acc[1]), "v"(acc[2]), "v"(acc[3]));
        const float p = ex2((lo16(w2) - m) * LOG2E);
        *(LAS u16*)(size_t)pw = (u16)pk2(p, p);
        if (layer == 0) *(LAS float*)(size_t)ma = m;
        mwv = m;
        __builtin_amdgcn_sched_barrier(0);
    };
    unsigned pr, oa, oc;
    auto att_addr = [&]() {
        LANE_VARS();
        pr = lb + L_P + (unsigned)((7 + 8 * wave - (lane & 7)) * 192 + ((n >> 3) * 4 + (kq ^ ((n >> 3) * 2))) * 16);
        oa = lb + L_OP + (unsigned)(wave * 1024 + (lane & 7) * 64 + kq * 16);
        oc = lb + L_OP + (unsigned)(wave * 1024 + 512 + (kq >> 1) * 256 + (lane & 7) * 32 + (kq & 1) * 16);
        PIN3(pr, oa, oc);
    };
    auto comb_o = [&](int part, f32x4 m0, f32x4 m1, f32x4 mm, f32x4 o0, f32x4 o1, f32x4 o2, f32x4 o3, unsigned so, unsigned long long mk) __attribute__((always_inline)) {
        const float M = fmax3(fmax3(m0[0], m0[1], m0[2]), fmax3(m0[3], m1[0], m1[1]), fmax2(m1[2], m1[3])) * LOG2E;
        f32x4 f;
#pragma unroll
        for (int k = 0; k < 4; ++k) f[k] = ex2(__builtin_fmaf(mm[k], LOG2E, -M));
        const f32x4 o = f[3] * o3 + (f[2] * o2 + (f[1] * o1 + f[0] * o0));
        float u0 = rs32(o[0], o[2]), u1 = rs32(o[1], o[3]);
        if (part == 1) { u0 = x16_add(u0); u1 = x16_add(u1); }
        const unsigned w = min(pk2(u0, u1), 0xfffffffeu);
        const u32x4 w4 = {w, dppu<0x55>(w), dppu<0xAA>(w), dppu<0xFF>(w)};
        st16m(ws, so, w4, mk);
    };
    auto ml_addr = [&](int lane, unsigned& xa, unsigned& ra) {
        xa = lb + L_M + (unsigned)(((lane & 15) >> 1) * 4); ra = lb + L_P + (unsigned)((7 + 8 * ((lane & 15) >> 1)) * 192 + (lane & 1) * 64 + (lane >> 4) * 16);
    };
    auto comb_ml = [&](unsigned ma, unsigned xa, unsigned ra, unsigned so) __attribute__((always_inline)) {
        const u32x4 x4 = *(const LAS u32x4*)(size_t)ra;
        const f32x4 m0 = *(const LAS f32x4*)(size_t)ma, m1 = *(const LAS f32x4*)(size_t)(ma + 16);
        const float mw = *(const LAS float*)(size_t)xa;
        __builtin_amdgcn_sched_barrier(0);
        const f32x4 sp = mfma32u((u32x4){0x3f803f80u, 0x3f803f80u, 0x3f803f80u, 0x3f803f80u}, x4, z4);
        __builtin_amdgcn_sched_barrier(0);
        const float M = fmax3(fmax3(m0[0], m0[1], m0[2]), fmax3(m0[3], m1[0], m1[1]), fmax2(m1[2], m1[3])) * LOG2E;
        const float L = row16_sum(sp[0] * ex2(__builtin_fmaf(mw, LOG2E, -M)));
        st16m(ws, so, asu((f32x4){M, L, M, L}), 1ull);
    };
    auto att_pv = [&](auto LC) {
        constexpr int layer = decltype(LC)::value;
        u32x4 bm[8];
        f32x4 da = z4, db = z4, dc = z4;
        if (layer == 0) {
            trans_v(0, 16); trans_v(1, 8);
#pragma unroll
            for (int e = 0; e < 8; ++e) bm[e] = *(const LAS u32x4*)(size_t)(pr + e * 192);
#pragma unroll
            for (int e = 0; e < 8; ++e) { da = mfma32u(vr[e], bm[e], da); db = mfma32u(vr[8 + e], bm[e], db); }
#pragma unroll
            for (int r = 0; r < 4; ++r) da[r] += dpp<0x128>(db[r]);
            if (LANES(0x00ff00ff00ff00ffull)) *(LAS f32x4*)(size_t)oa = da;
            asm volatile("s_mov_b64 exec, 1\n\tds_add_u32 %0, %1\n\ts_mov_b64 exec, -1" :: "v"(lb + L_CNT + 24), "v"(1u) : "memory");
            __builtin_amdgcn_sched_barrier(0);
            trans_v(2, 0);
            STAMP(4);
#pragma unroll
            for (int e = 0; e < 8; ++e) dc = mfma32u(vr[16 + e], bm[e], dc);
            if (LANES(0xff00ff0000ff00ffull)) *(LAS f32x4*)(size_t)oc = dc;
        } else {
#pragma unroll
            for (int e = 0; e < 8; ++e) bm[e] = *(const LAS u32x4*)(size_t)(pr + e * 192);
            *(LAS float*)(size_t)ma = mwv;
            unsigned long long ka = 0x00ff00ff00ff00ffull, kc = 0xff00ff0000ff00ffull;
            PINS(ka); PINS(kc);
            __builtin_amdgcn_sched_barrier(0);
#pragma unroll
            for (int e = 0; e < 8; ++e) { db = mfma32u(vr[8 + e], bm[e], db); da = mfma32u(vr[e], bm[e], da); if (e < 4) dc = mfma32u(vr[16 + e], bm[e], dc); }
            __builtin_amdgcn_sched_barrier(0);
#pragma unroll
            for (int e = 4; e < 8; ++e) dc = mfma32u(vr[16 + e], bm[e], dc);
            __builtin_amdgcn_sched_barrier(0);
            float a0, a1, a2, a3;
            asm volatile("s_nop 3\n\tv_add_f32_dpp %0, %8, %4 row_ror:8 row_mask:0xf bank_mask:0xf\n\tv_add_f32_dpp %1, %9, %5 row_ror:8 row_mask:0xf bank_mask:0xf\n\t"
                         "v_add_f32_dpp %2, %10, %6 row_ror:8 row_mask:0xf bank_mask:0xf\n\tv_add_f32_dpp %3, %11, %7 row_ror:8 row_mask:0xf bank_mask:0xf"
                         : "=&v"(a0), "=&v"(a1), "=&v"(a2), "=&v"(a3) : "v"(da[0]), "v"(da[1]), "v"(da[2]), "v"(da[3]), "v"(db[0]), "v"(db[1]), "v"(db[2]), "v"(db[3]), "v"(dc));
            asm volatile("s_mov_b64 exec, %4\n\tds_write_b128 %0, %1\n\ts_mov_b64 exec, %5\n\tds_write_b128 %2, %3\n\ts_mov_b64 exec, -1"
                         :: "v"(oa), "v"((f32x4){a0, a1, a2, a3}), "v"(oc), "v"(dc), "s"(ka), "s"(kc) : "memory");
            if (wave >= 3) { VMW(0); BAR(); }
        }
        STAMP(20 + 32 * layer);
    };

    unsigned cma, cxa, cra, cso, nj = 0u; int ord;
    {
        const int tid = threadIdx.x, lane = tid & 63;
        STAMP(0);
        const u32x4 ones = {~0u, ~0u, ~0u, ~0u};
        unsigned pf = 0u, mb = 0u, tfl = 0u;
        const u16* kbase = kc + ((size_t)((gj >> 1) * HQ + gi) * SK + (SK - WIN) + (gj & 1) * 512) * DH;
        const u16* vbase = vc + ((size_t)((gj >> 1) * HQ + gi) * SK + (SK - WIN) + (gj & 1) * 512) * DH;
        if (wave == 7) {
            __builtin_amdgcn_s_setprio(3);
            for (int i = 0; i < 5; ++i) dma16p(x0, (unsigned)(i * 1024 + lane * 16), lb + L_X0 + i * 1024);
            pf = code_pf((g >> 3) % 10, lane);
            if (lane < 20) *reinterpret_cast<unsigned*>(smem + L_CNT + lane * 4) = 0u;
            BAR();
            if (lane < 10) *reinterpret_cast<u32x4*>(smem + L_XS + 640 + lane * 16) = (u32x4){0x3f803f80u, 0x3f803f80u, 0x3f803f80u, 0x3f803f80u};
            if (lane < 16) { *reinterpret_cast<u32x4*>(smem + L_CS + 768 + lane * 16) = (u32x4){0u, 0u, 0u, 0u}; }
        } else {
            if (wave >= 4) {
                if (wave < 6) {
                    empty1(wave - 4, lane);
                    empty35(8 * (wave - 4), 0, lane);
                    st16m(ws, WS_H35 + (unsigned)(((gj * 20 + 8 * (wave - 4) + 4 + (lane >> 4)) * 32 + gi) * 256 + (lane & 15) * 16), ones, wave == 4 ? ~0ull : 0xffffffffffffull);
                } else {
                    if (gi + gj == 0) st4_sc1(ws, WS_R + (lane & 31) * 4, 0u);
                    st4_sc1(ws, WS_MB + (unsigned)((gi * 8 + gj) * 128 + (lane & 31) * 4), 0u);
                    st16_sc1(ws, WS_OWN + (unsigned)((gi * 8 + gj) * 2048 + lane * 16), ones);
                    empty4(0, lane); empty4(1, lane);
                }
            }
#pragma unroll 1
            for (int i = wave; i < 35; i += 7) {
                if (i >= 33) dma16p(wr, (unsigned)((i == 33 || lane < 16) ? (i - 33) * 1024 + lane * 16 : 0), lb + L_WR + (i - 33) * 1024);
                else {
                    const int c = i * 64 + lane; int row = c / 26, cc = c - 26 * row; if (cc > 23) cc = 23; if (row > 79) row = 79;
                    dma16(wq, (unsigned)(((gj * 80 + swz(row)) * QD + gi * DH + cc * 8) * 2), lb + L_WQ + i * 1024);
                }
            }
            BAR();
            *reinterpret_cast<u32x4*>(smem + L_P + tid * 16) = (u32x4){0u, 0u, 0u, 0u};
            if (tid < 72 * 12 - 448) *reinterpret_cast<u32x4*>(smem + L_P + 7168 + tid * 16) = (u32x4){0u, 0u, 0u, 0u};
            if (wave >= 4) {
                VMW(5);
                lds_add(32, 1u);
                if (wave == 6) {
#pragma unroll 1
                    for (unsigned spins = 0; spins < (kSpinCap << 4); ++spins) if (lds_word(lb + L_CNT + 32) >= 3u) break;
                    send_flag(lane);
                    tfl = now();
                    STAMP(17);
                    asm volatile("s_waitcnt vmcnt(1) lgkmcnt(0)" ::: "memory");
                } else asm volatile("s_waitcnt vmcnt(0) lgkmcnt(0)" ::: "memory");
                lds_add(0, 1u);
                if (wave == 6) {
                    until(__builtin_amdgcn_readfirstlane(tfl) + DMB);
                    mb = ld4_sc1(ws, WS_MB + (unsigned)((gi * 8 + gj) * 128 + (lane & 7) * 4));
                }
            }
        }
        u32x4 a[8];
        const unsigned ho = WS_H1 + (unsigned)((gi * 4 + (gj >> 1)) * 7168 + 3584 + (lane < 48 ? lane : 0) * 16);
        const unsigned ko = (unsigned)((wave * 64 + (lane & 7)) * 384 + (((lane >> 3) & 1) * 4 + (lane >> 4)) * 16);
#pragma unroll
        for (int G = 0; G < 8; ++G) {
            const unsigned o = ko + G * 3072;
            pace(3);
            kr[G * 3 + 0] = ld16_nt<0>(kbase, o); kr[G * 3 + 1] = ld16_nt<128>(kbase, o); kr[G * 3 + 2] = ld16_nt<256>(kbase, o);
            if (wave < 7) {
                asm volatile("; COND_BEGIN");
                if (G == GL && wave < 4) {
                    asm volatile("s_waitcnt vmcnt(%0) lgkmcnt(0)" :: "i"(3 * GL + 3) : "memory");
                    lds_add(0, 1u);
                }
                if (wave == 6) {
                    if (G == 0) {
                        WAIT1(mb, 3);
#pragma unroll 1
                        for (unsigned spins = 0; spins < kSpinCap && !none(mb ^ 1u); ++spins) {
                            if ((spins & 7u) == 7u) send_flag(lane);
                            mb = ld4_sc1(ws, WS_MB + (unsigned)((gi * 8 + gj) * 128 + (lane & 7) * 4));
                            WAIT1(mb, 0);
                        }
                        lds_set(lb + L_CNT + 8, 1u);
                        if (gj == (gi & 7)) st4_sc1(ws, WS_R + (unsigned)(gi * 4), 1u);
                        STAMP(31);
                    }
                    if (gi + gj == 0) {
                        if (G == GE) mb = ld4_sc1(ws, WS_R + (unsigned)((lane & 31) * 4));
                        if (G == GF) {
                            WAIT1(mb, 3 * (GF - GE));
                            if (none(mb ^ 1u)) { answer(lane); lds_set(lb + L_CNT + 36, 1u); }
                        }
                    }
                }
                asm volatile("; COND_END");
            }
            if (wave == 7) {
                asm volatile("; COND_BEGIN");
                if (G == GP) {
                    VMW(3 * (GP + 1) + 1);
                    const unsigned char* p = smem + L_X0 + (lane >> 4) * 1280;
                    const float c0 = lo16(*reinterpret_cast<const u16*>(p));
                    const f32x2 c2 = {c0, c0};
                    f32x2 s2 = {0.f, 0.f}, q2 = s2;
#pragma unroll
                    for (int k = 0; k < 5; ++k) {
                        const u32x4 w = *reinterpret_cast<const u32x4*>(p + k * 256 + (lane & 15) * 16);
#pragma unroll
                        for (int e = 0; e < 4; ++e) { const f32x2 d = unpk(w[e]) - c2; s2 += d; q2 += d * d; }
                    }
                    const float s = row16_sum(s2[0] + s2[1]), qq = row16_sum(q2[0] + q2[1]);
                    const float ms = s * (1.f / DM);
                    const unsigned mv = pk2(c0 + ms, fmax2(qq - s * ms, 0.f) * (1.f / DM));
                    const float mb = lo16(mv), rden = frcp(bfr(__builtin_amdgcn_sqrtf(bfr(hi16(mv) + 1e-5f))));
                    const f32x2 mb2 = {mb, mb}, rd2 = {rden, rden};
                    if ((lane & 15) < 10) {
                        u32x4 w = *reinterpret_cast<const u32x4*>(p + gj * 160 + (lane & 15) * 16);
#pragma unroll
                        for (int e = 0; e < 4; ++e) w[e] = pk2v(unpk(pk2v(unpk(w[e]) - mb2)) * rd2);
                        *reinterpret_cast<u32x4*>(smem + L_XS + (lane >> 4) * 160 + (lane & 15) * 16) = w;
                    }
                    STAMP(30);
#pragma unroll 1
                    for (unsigned spins = 0; spins < (kSpinCap << 4); ++spins) {
                        if (lds_word(lb + L_CNT) >= 7u) break;
                        __builtin_amdgcn_s_sleep(1);
                    }
                    STAMP(26);
                    {
                        const int kq = lane >> 4, n = lane & 15, q4 = n >> 2, p4 = lane & 3;
                        const int r0s = 8 * kq + q4 + 4 * (kq & 1), r1s = 8 * kq + q4 + 4 * ((kq & 1) ^ 1), r2s = swz(4 * kq + q4);
#pragma unroll 1
                        for (int i6 = 0; i6 < 6; ++i6) {
                            const int blk = i6 < 3 ? i6 : i6 - 3;
                            const unsigned xb = lb + L_XS + (unsigned)((i6 < 3 ? p4 : 4) * 160);
                            Frag80 f[4];
#pragma unroll
                            for (int i = 0; i < 4; ++i) f[i] = frag80(lb + L_WQ + (unsigned)(128 * blk + 32 * i + 8 * p4), 416, r0s, r1s, r2s);
                            const Frag80 b = rows80(xb + kq * 16, xb + 128 + kq * 8);
                            const unsigned so = (i6 < 3 ? WS_H1 + (unsigned)(((gi * 4 + p4) * 8 + gj) * 896) : WS_OWN + (unsigned)((gi * 8 + gj) * 2048 + 1024)) + (unsigned)(256 * blk + q4 * 64 + kq * 16);
                            st16m(ws, so, tiles4(f, b, 0x000f000f000f000full), i6 < 3 ? ~0ull : 0x1111111111111111ull);
                            if (i6 == 2) { tfl = now(); STAMP(2); }
                        }
                    }
                    f32x2 r2 = {0.f, 0.f};
#pragma unroll
                    for (int k = 0; k < 5; ++k) r2 += unpk(*reinterpret_cast<const unsigned*>(smem + L_WR + k * 256 + lane * 4));
                    const float sw = wave_sum(r2[0] + r2[1]);
                    if (lane == 0) *reinterpret_cast<float*>(smem + L_CNT + 12) = sw;
                    WAIT1(pf, 3 * (GP + 1) + 6);
                    pace(XCOST, false);
                }
                if (G == GC) {
#pragma unroll 1
                    for (unsigned spins = 0; spins < (kSpinCap << 8); ++spins) { if (lds_word(lb + L_CNT + 8)) break; __builtin_amdgcn_s_sleep(1); }
                    until(__builtin_amdgcn_readfirstlane(tfl) + DQ);
                    GATHER8(a, ho, 896);
                }
                if (G == GD) {
                    WAIT8(a, 3 * (GD - GC));
#pragma unroll 1
                    for (unsigned spins = 0; spins < kSpinCap && any_empty(UM32(a)); ++spins) { slip(0u); GATHER8(a, ho, 896); WAIT8(a, 0); }
                    const f32x4 sq = SUM8(a);
                    *reinterpret_cast<u32x2*>(smem + L_Q + lane * 8) = (u32x2){pk2(sq[0], sq[1]), pk2(sq[2], sq[3])};
                    asm volatile("s_waitcnt lgkmcnt(0)\n\tds_write_b32 %0, %1" :: "v"(lb + L_CNT + 4), "v"(1u) : "memory");
                    STAMP(27);
                }
                asm volatile("; COND_END");
            } else if (G == GC) { for (int k = 0; k < 8; ++k) asm volatile("" : "=v"(a[k])); }
        }
        {
            const unsigned v0 = (unsigned)((wave * 64 + 4 * (lane >> 4)) * 384 + (lane & 15) * 16);
#pragma unroll
            for (int t = 0; t < 4; ++t) {
                const unsigned o = v0 + t * 6144;
                pace(2);
                vr[t * 4 + 0] = ld16_nt<0>(vbase, o); vr[t * 4 + 1] = ld16_nt<384>(vbase, o);
                if (t < 3) { fix_k1((4 * t) / 3, (4 * t) % 3, 20 - 6 * ((4 * t) / 3) - (4 * t) % 3 + 4 * t + 2); fix_k1((4 * t + 1) / 3, (4 * t + 1) % 3, 20 - 6 * ((4 * t + 1) / 3) - (4 * t + 1) % 3 + 4 * t + 2); }
                pace(2);
                vr[t * 4 + 2] = ld16_nt<768>(vbase, o); vr[t * 4 + 3] = ld16_nt<1152>(vbase, o);
                if (t < 3) { fix_k1((4 * t + 2) / 3, (4 * t + 2) % 3, 20 - 6 * ((4 * t + 2) / 3) - (4 * t + 2) % 3 + 4 * t + 4); fix_k1((4 * t + 3) / 3, (4 * t + 3) % 3, 20 - 6 * ((4 * t + 3) / 3) - (4 * t + 3) % 3 + 4 * t + 4); }
                if (t == QKT - 1 && wave == 6 && gi + gj == 0) { asm volatile("; COND_BEGIN"); mb = ld4_sc1(ws, WS_R + (unsigned)((lane & 31) * 4)); asm volatile("; COND_END"); }
                if (t == QKT) {
                    STAMP(1);
                    if (wave == 6 && gi + gj == 0) {
                        asm volatile("; COND_BEGIN");
                        WAIT1(mb, 4);
                        if (none(mb ^ 1u) && !lds_word(lb + L_CNT + 36)) { answer(lane); lds_set(lb + L_CNT + 36, 1u); }
                        asm volatile("; COND_END");
                    }
                    __builtin_amdgcn_s_setprio(0);
                    att_qk(IC<0>{});
                }
            }
            const unsigned v1 = (unsigned)((wave * 64 + 32 * ((lane >> 3) & 1) + 4 * (lane >> 4)) * 384 + 256 + (lane & 7) * 16);
#pragma unroll
            for (int t = 4; t < 6; ++t) {
                const unsigned o = v1 + (t - 4) * 6144;
                pace(2);
                vr[t * 4 + 0] = ld16_nt<0>(vbase, o); vr[t * 4 + 1] = ld16_nt<384>(vbase, o);
                pace(2);
                vr[t * 4 + 2] = ld16_nt<768>(vbase, o); vr[t * 4 + 3] = ld16_nt<1152>(vbase, o);
            }
        }
        if (lane == 0) __hip_atomic_fetch_add(reinterpret_cast<unsigned*>(smem + L_CNT + 16), 1u, __ATOMIC_RELAXED, __HIP_MEMORY_SCOPE_WORKGROUP);
        STAMP(28);
        att_addr();
        cma = lb + L_M; cxa = lb + L_M + (unsigned)((lane >> 5) * 16); cra = lb + L_OP + (unsigned)(512 + (lane >> 5) * 4096 + (lane & 31) * 16);
        cso = WS_OWN + (unsigned)((gi * 8 + gj) * 2048 + 256 + (lane >> 5) * 64 + (lane & 15) * 4);
        PIN2(cma, cxa); PIN2(cra, cso);
        att_pv(IC<0>{});
    }
    {
        f32x4 m0, m1, mm, o0, o1, o2, o3; unsigned r;
        asm volatile("s_mov_b64 exec, 1\n\tds_add_rtn_u32 %7, %8, %9 offset:%12\n\ts_mov_b64 exec, -1\n\t"
                     "ds_read_b128 %0, %8\n\tds_read_b128 %1, %8 offset:16\n\tds_read_b128 %2, %10\n\tds_read_b128 %3, %11\n\tds_read_b128 %4, %11 offset:1024\n\t"
                     "ds_read_b128 %5, %11 offset:2048\n\tds_read_b128 %6, %11 offset:3072\n\ts_waitcnt lgkmcnt(0)"
                     : "=&v"(m0), "=&v"(m1), "=&v"(mm), "=&v"(o0), "=&v"(o1), "=&v"(o2), "=&v"(o3), "=&v"(r)
                     : "v"(cma), "v"(1u), "v"(cxa), "v"(cra), "i"(L_CNT + 28 - L_M) : "memory");
        ord = __builtin_amdgcn_readfirstlane(r);
        if (ord == 7) { STAMP(5); comb_o(1, m0, m1, mm, o0, o1, o2, o3, cso, 0x0000111100001111ull); lds_set(lb + L_CNT + 48, now() | 1u); STAMP(21); }
        const int lane = lane_id();
        auto wait_cnt = [&](int off, unsigned v) {
#pragma unroll 1
            for (unsigned spins = 0; spins < (kSpinCap << 4); ++spins) { if (lds_word(lb + L_CNT + off) >= v) break; __builtin_amdgcn_s_sleep(1); }
        };
        if (ord == 1) {
            wait_cnt(16, 8u);
            unsigned xa, ra; ml_addr(lane, xa, ra);
            comb_ml(cma, xa, ra, WS_OWN + (unsigned)((gi * 8 + gj) * 2048 + 384));
            STAMP(24);
        } else if (ord == 0) {
            if (gi + gj == 0 && !lds_word(lb + L_CNT + 36)) {
#pragma unroll 1
                for (unsigned spins = 0; spins < kSpinCap; ++spins) {
                    unsigned r = ld4_sc1(ws, WS_R + (unsigned)((lane & 31) * 4));
                    WAIT1(r, 0);
                    if (none(r ^ 1u)) break;
                    if ((spins & 7u) == 7u) send_flag(lane);
                }
                answer(lane);
            }
#pragma unroll 1
            for (unsigned spins = 0; spins < kSpinCap; ++spins) {
                unsigned r = ld4_sc1(ws, WS_MB + (unsigned)((gi * 8 + gj) * 128 + 32));
                WAIT1(r, 0);
                if (__builtin_amdgcn_readfirstlane(r) == 1u) break;
                if ((spins & 7u) == 7u) { send_flag(lane); if (gj == (gi & 7)) st4_sc1(ws, WS_R + (unsigned)(gi * 4), 1u); }
                __builtin_amdgcn_s_sleep(16);
            }
            STAMP(29);
        } else if (ord == 2) {
#pragma unroll 1
            for (unsigned spins = 0; spins < (kSpinCap << 6); ++spins) if (lds_word(lb + L_CNT + 24) >= 8u) break;
            const unsigned ra = cra - 512;
            comb_o(0, *(const LAS f32x4*)(size_t)cma, *(const LAS f32x4*)(size_t)(cma + 16), *(const LAS f32x4*)(size_t)cxa, *(const LAS f32x4*)(size_t)ra, *(const LAS f32x4*)(size_t)(ra + 1024),
                   *(const LAS f32x4*)(size_t)(ra + 2048), *(const LAS f32x4*)(size_t)(ra + 3072), WS_OWN + (unsigned)((gi * 8 + gj) * 2048 + lane * 4), 0x1111111111111111ull);
            STAMP(22);
        }
#pragma unroll 1
        for (;;) {
            unsigned j;
            asm volatile("s_mov_b64 exec, 1\n\tds_add_rtn_u32 %0, %1, %2\n\ts_mov_b64 exec, -1\n\ts_waitcnt lgkmcnt(0)" : "=&v"(j) : "v"(lb + L_CNT + 40), "v"(1u) : "memory");
            j = __builtin_amdgcn_readfirstlane(j);
            if (j >= 5u) break;
            load_wo((int)j, lane); ++nj;
        }
        STAMP(23);
    }

    auto chain = [&](auto LC) {
        constexpr int layer = decltype(LC)::value;
        const unsigned h4 = WS_H4 + (unsigned)(gi * 16384);
        if (wave < 3) {
            if (layer != 0) {
                const int lane = lane_id();
                const unsigned ob = WS_OWN + (unsigned)((gi * 8 + gj) * 2048 + layer * 512);
                unsigned ma = lb + L_M, xa = lb + L_M + (unsigned)((lane >> 5) * 16);
                unsigned ra = lb + L_OP + (unsigned)(wave * 512 + (lane >> 5) * 4096 + (lane & 31) * 16);
                if (wave == 2) ml_addr(lane, xa, ra);
                unsigned so = ob + (unsigned)(wave == 0 ? lane * 4 : wave == 1 ? 256 + (lane >> 5) * 64 + (lane & 15) * 4 : 384);
                unsigned long long mk = wave == 0 ? 0x1111111111111111ull : 0x0000111100001111ull;
                PIN2(ma, xa); PIN2(ra, so); PINS(mk);
                __builtin_amdgcn_s_setprio(0);
                if (wave == 2) {
                    BAR();
                    comb_ml(ma, xa, ra, so);
                } else {
                    BAR();
                    STAMP(5 + 32 * layer);
                    comb_o(wave, *(const LAS f32x4*)(size_t)ma, *(const LAS f32x4*)(size_t)(ma + 16), *(const LAS f32x4*)(size_t)xa, *(const LAS f32x4*)(size_t)ra,
                           *(const LAS f32x4*)(size_t)(ra + 1024), *(const LAS f32x4*)(size_t)(ra + 2048), *(const LAS f32x4*)(size_t)(ra + 3072), so, mk);
                }
                STAMP(21 + 32 * layer);
            }
            LANE_VARS();
            if (layer == 0) {
                VMW(0);
                lds_add(44, nj);
                unsigned tf = 0u;
#pragma unroll 1
                for (unsigned spins = 0; spins < (kSpinCap << 4); ++spins) { tf = lds_word(lb + L_CNT + 48); if (tf) break; __builtin_amdgcn_s_sleep(1); }
                slot(NUNIT + XG2);
                until((int)(now() - tf) > 0 ? now() + DGU - DG2 : tf + DGU);
                if (wave < 2) load_gu(wave, lane); else load_wd(lane);
                STAMP(25);
                if (wave == 0) {
#pragma unroll 1
                    for (unsigned spins = 1; spins < kSpinCap; ++spins) {
                        if (lds_word(lb + L_CNT + 52) != 0u) break;
                        __builtin_amdgcn_s_sleep(2);
                        if ((spins & 63u) == 0u) send_flag(lane);
                    }
                }
            }
            BAR();
            if (layer == 0) {
                __builtin_amdgcn_s_sleep(4);
                if (wave < 2) {
                    empty1(wave, lane);
                    VMW(2);
                    lds_add(20, 1u);
                }
#pragma unroll 1
                for (unsigned spins = 0; spins < (kSpinCap << 4); ++spins) if (lds_word(lb + L_CNT + 20) >= 2u) break;
                STAMP(19);
            }
            {
                const unsigned im = lb + L_GU + (unsigned)(32 * wave + 8 * p4);
                Frag80 fg = frag80(im, 96, r0s, r1s, r2s), fu = frag80(im + 8192, 96, r0s, r1s, r2s);
                const unsigned xb = lb + L_XS + (unsigned)((n < 4 ? n : 4) * 160);
                unsigned b16 = xb + kq * 16, b8 = xb + 128 + kq * 8;
                unsigned sg = h4 + (unsigned)((n < 4 ? wave * 4096 + gj * 512 + n * 64 : 12288 + wave * 1024 + gj * 128) + kq * 16), su = sg + (n < 4 ? 256u : 64u);
                unsigned o = h4 + (unsigned)(wave * 4096 + lane * 16);
                unsigned oc = h4 + (unsigned)(12288 + wave * 1024 + ((lane >> 5) * 4 + q4) * 128 + (kq & 1) * 64 + p4 * 16);
                unsigned os = h4 + (unsigned)(15360 + (p4 + 4 * (kq & 1)) * 128 + q4 * 16);
                unsigned ha = lb + L_H + (unsigned)(q4 * 96 + wave * 32 + p4 * 8 + kq * 2);
                f32x2 cs = {0.f, 0.f};
                if (layer != 0) cs = *reinterpret_cast<const f32x2*>(smem + L_CS2 + wave * 512 + lane * 8);
                PIN3(fg.a0, fg.a1, fg.a2); PIN3(fu.a0, fu.a1, fu.a2); PIN3(b16, b8, sg); PIN3(su, o, os); PIN2(ha, cs);
                unsigned long long mk = layer ? 0x000f000f000f000full : 0x001f001f001f001full;
                unsigned ta = WS_MB + (unsigned)((lane >> 1) * 1024 + 64 + (lane & 1) * 16);
                if (layer == 0) PIN2(oc, ta);
                PINS(mk);
                if (layer == 0) VMW(0);
                BAR();
                STAMP(10 + 32 * layer);
                u32x4 tl = {0u, 0u, 0u, 0u};
                if (layer == 0) tl = ld16_sc1(ws, ta);
                const Frag80 b = rows80(b16, b8);
                const f32x4 g0 = mfma32(fg.a0, b.a0, z4), u0 = mfma32(fu.a0, b.a0, z4), g1 = mfma32(fg.a1, b.a1, g0), g2 = mfma16(fg.a2, b.a2, z4), u1 = mfma32(fu.a1, b.a1, u0), u2 = mfma16(fu.a2, b.a2, z4);
                __builtin_amdgcn_sched_barrier(0);
                const f32x4 cg = g1 + g2, cu = u1 + u2;
                st16m2<0>(ws, sg, asu(cg), su, asu(cu), mk);
                STAMP(11 + 32 * layer);
                if (layer == 0) {
                    const unsigned tn = now();
                    WAIT1(tl, 2);
                    unsigned d = min(min(min(tn - tl[0], tn - tl[1]), min(tn - tl[2], tn - tl[3])), 800u);
                    d = min(d, dppu<0xB1>(d)); d = min(d, dppu<0x4E>(d)); d = min(d, dppu<0x124>(d)); d = min(d, dppu<0x128>(d));
                    unsigned tr = tn - min(min((unsigned)__builtin_amdgcn_readlane(d, 0), (unsigned)__builtin_amdgcn_readlane(d, 16)), min((unsigned)__builtin_amdgcn_readlane(d, 32), (unsigned)__builtin_amdgcn_readlane(d, 48)));
                    lds_set(lb + L_CNT + 56, tr | 1u);
                    if ((int)(tn + 12 - X4A - tr) > 0) tr = tn + 12 - X4A;
                    until(tr + X4A);
                } else sched(X4, 12);
                STAMP(16 + 32 * layer);
                float hv; f32x2 st;
                u32x4 a0, a1, a2, a3, ac, as;
                auto load = [&]() __attribute__((always_inline)) {
                    as = ld16_sc1(ws, os);
                    if (layer == 0) ac = ld16_sc1(ws, oc);
                    a0 = ld16_sc1<0>(ws, o); a1 = ld16_sc1<1024>(ws, o); a2 = ld16_sc1<2048>(ws, o); a3 = ld16_sc1<3072>(ws, o);
                    if (layer == 0) asm volatile("s_waitcnt vmcnt(0)" : "+v"(a0), "+v"(a1), "+v"(a2), "+v"(a3), "+v"(ac), "+v"(as) :: "memory");
                    else { asm volatile("s_waitcnt vmcnt(0)" : "+v"(a0), "+v"(a1), "+v"(a2), "+v"(a3), "+v"(as) :: "memory"); ac = as; }
                };
                auto compute = [&]() __attribute__((always_inline)) {
                    float ss, sm;
                    asm volatile("v_add_f32_dpp %0, %2, %2 quad_perm:[1,0,3,2] row_mask:0xf bank_mask:0xf\n\tv_add_f32_dpp %1, %3, %3 quad_perm:[1,0,3,2] row_mask:0xf bank_mask:0xf\n\ts_nop 0\n\t"
                                 "v_add_f32_dpp %0, %0, %0 quad_perm:[2,3,0,1] row_mask:0xf bank_mask:0xf\n\tv_add_f32_dpp %1, %1, %1 quad_perm:[2,3,0,1] row_mask:0xf bank_mask:0xf\n\ts_nop 0"
                                 : "=&v"(ss), "=&v"(sm) : "v"(as[0]), "v"(as[1]));
                    ss = x16_add(ss); sm = x16_add(sm);
                    st = ln_stats(ss, sm);
                    const f32x4 t = (asf(a0) + asf(a1)) + (asf(a2) + asf(a3));
                    const f32x2 gu = sw16(rs32(t[0], t[2]), rs32(t[1], t[3]));
                    if (layer == 0) {
                        cs = sw16(rs32(asf(ac)[0], asf(ac)[2]), rs32(asf(ac)[1], asf(ac)[3]));
                        cs[0] += dpp<0x124>(cs[0]); cs[1] += dpp<0x124>(cs[1]); cs[0] += dpp<0x128>(cs[0]); cs[1] += dpp<0x128>(cs[1]);
                    }
                    const unsigned gw = pk2v((gu - st[0] * cs) * st[1]);
                    const float gv = lo16(gw);
                    hv = bfr(gv * frcp(1.f + ex2(gv * -LOG2E))) * hi16(gw);
                    *(LAS u16*)(size_t)ha = (u16)pk2(hv, hv);
                    __builtin_amdgcn_sched_barrier(0);
                };
                RETRY(load(), max(max(max(um4(a0), um4(a1)), max(um4(a2), um4(a3))), max(um4(ac), um4(as))), compute(), any_nan(hv, hv), layer ? X4 : X4A);
                BAR();
                STAMP(12 + 32 * layer);
                if (layer == 0) {
                    *reinterpret_cast<f32x2*>(smem + L_CS2 + wave * 512 + lane * 8) = cs;
                }
            }
            if (layer == 0) {
                if (wave == 1) {
                    const unsigned o = WS_OWN + (unsigned)(gi * 16384 + 1024 + 2048 + (lane < 48 ? lane : 0) * 16);
                    u32x4 a[8];
                    a[0] = ld16_sc1<-2048>(ws, o); a[1] = ld16_sc1<0>(ws, o); a[2] = ld16_sc1<2048>(ws, o);
                    a[3] = ld16_sc1<-2048>(ws, o + 6144); a[4] = ld16_sc1<0>(ws, o + 6144); a[5] = ld16_sc1<2048>(ws, o + 6144);
                    a[6] = ld16_sc1<-2048>(ws, o + 12288); a[7] = ld16_sc1<0>(ws, o + 12288);
                    WAIT8(a, 0);
                    if (lane < 48) *reinterpret_cast<f32x4*>(smem + L_CS + lane * 16) = SUM8(a);
                }
                Frag80 f[4];
#pragma unroll
                for (int i = 0; i < 4; ++i) { f[i] = frag80(lb + L_WQ + (unsigned)(128 * wave + 32 * i + 8 * p4), 416, r0s, r1s, r2s); PIN3(f[i].a0, f[i].a1, f[i].a2); }
                unsigned b16 = lb + L_XS + (unsigned)(p4 * 160 + kq * 16), b8 = lb + L_XS + (unsigned)(p4 * 160 + 128 + kq * 8);
                unsigned so = WS_H1 + (unsigned)(((gi * 4 + p4) * 8 + gj) * 896 + 256 * wave + q4 * 64 + kq * 16);
                unsigned long long mk = 0x000f000f000f000full;
                PIN3(b16, b8, so); PINS(mk);
                BAR();
                STAMP(33);
                const Frag80 b = rows80(b16, b8);
                st16_sc1(ws, so, tiles4(f, b, mk));
                STAMP(34);
                qk_addr(true); att_addr();
            }
        } else {
            const int t = wave - 3;
            StatAddr sa;
            auto gather32 = [&](unsigned o, unsigned xs, unsigned X, auto mid, auto fin) __attribute__((always_inline)) {
                float v; u32x4 a[8]; f32x4 s; unsigned xw;
                RETRY8(a, o, 1024, s, mid(), { const f32x2 v2 = sw16(rs32(s[0], s[2]), rs32(s[1], s[3])); v = v2[0] + v2[1]; xw = pk2(fin(v), 0.f); *(LAS u16*)(size_t)xs = (u16)xw; __builtin_amdgcn_sched_barrier(0); },
                       any_nan(v, v), X);
                xe = lo16(xw);
            };
            __builtin_amdgcn_s_setprio(0);
            {
                LANE_VARS();
                if (layer == 0) xe = lo16(*reinterpret_cast<const u16*>(smem + L_X0 + q4 * 1280 + gj * 160 + t * 32 + p4 * 8 + kq * 2));
                STAMP(6 + 32 * layer);
                unsigned lo = WS_OWN + (unsigned)(gi * 16384 + layer * 512 + (lane >> 3) * 2048 + t * 128 + (t == 3 ? 0 : (lane & 7) * 16));
                unsigned lw = lb + L_O2 + (unsigned)((lane >> 3) * 416 + t * 128 + (t == 3 ? 0 : (lane & 7) * 16));
                PIN2(lo, lw);
                u32x4 ov = {~0u, ~0u, ~0u, ~0u};
                if (layer != 0 && t < 4) { sched(X2, 20); STAMP(49); ov = ld16_sc1(ws, lo); }
                const unsigned wa = lb + L_WO + (unsigned)(r0s * 160 + 32 * t + 8 * p4), wa1 = lb + L_WO + (unsigned)(r1s * 160 + 32 * t + 8 * p4);
                bf16x8 af[6];
                if (layer == 0) {
                    if (t < 4) {
                        unsigned tf = 0u;
#pragma unroll 1
                        for (unsigned spins = 0; spins < (kSpinCap << 4); ++spins) { tf = lds_word(lb + L_CNT + 48); if (tf) break; }
                        slot(NUNIT + XG2);
                        until(tf + DG2);
                        ov = ld16_sc1(ws, lo);
                        if (nj) { WAIT1(ov, 1); lds_add(44, nj); }
                    } else { VMW(0); lds_add(44, nj); }
#pragma unroll 1
                    for (unsigned spins = 0; spins < (kSpinCap << 4); ++spins) if (lds_word(lb + L_CNT + 44) >= 5u) break;
                    STAMP(8);
                }
#pragma unroll
                for (int s = 0; s < 6; ++s) af[s] = cat8(tr64(wa + s * 32 * 160), tr64(wa1 + s * 32 * 160));
                const unsigned h3 = WS_H35 + (unsigned)((gj * 20 + 10 * layer + t) * 8192);
                unsigned so = h3 + (unsigned)(gi * 256 + (n >> 1) * 64 + kq * 16), go = h3 + (unsigned)(4096 + lane * 16);
                unsigned xs = lb + L_XS + (unsigned)(q4 * 160 + t * 32 + p4 * 8 + kq * 2);
                unsigned pb = h3 + (unsigned)(((gi * 7 + t * 5 + 3) & 31) * 256);
                unsigned br = lb + L_O2 + (unsigned)((n & 7) * 416 + kq * 16), mr = lb + L_O2 + (unsigned)((n & 7) * 416 + 384);
                unsigned long long mk = 0x0055005500550055ull;
                PIN3(af[0], af[1], af[2]); PIN3(af[3], af[4], af[5]); PIN3(br, mr, so); PIN3(go, xs, pb); PINS(mk);
                if (t < 4) {
                    WAIT1(ov, 0);
                    *(LAS u32x4*)(size_t)lw = ov;
                    if (layer == 0 ? any_empty(um4(ov)) : __builtin_expect(any_empty(um4(ov)), 0)) {
#pragma unroll 1
                        for (unsigned spins = 0; spins < kSpinCap; ++spins) {
                            if (layer != 0) slip(X2);
                            ov = ld16_sc1(ws, lo);
                            WAIT1(ov, 0);
                            *(LAS u32x4*)(size_t)lw = ov;
                            if (!any_empty(um4(ov))) break;
                            if (layer == 0) __builtin_amdgcn_s_sleep(1);
                        }
                    }
                    if (layer == 0 && t == 2) lds_set(lb + L_CNT + 52, 1u);
                }
                BAR();
                bf16x8 bf[6];
                const f32x2 ml = *(const LAS f32x2*)(size_t)mr;
#pragma unroll
                for (int s = 0; s < 6; ++s) bf[s] = lds16(br + s * 64);
                __builtin_amdgcn_sched_barrier(0);
                f32x4 ca = z4;
#pragma unroll
                for (int s = 0; s < 6; ++s) ca = mfma32(af[s], bf[s], ca);
                const float e = ex2(ml[0] - fmax2(ml[0], dpp<0xB1>(ml[0]))), el = e * ml[1];
                const float f = e * frcp(el + dpp<0xB1>(el));
                float c0, c1, c2, c3;
                asm volatile("s_nop 7\n\t"
                             "v_mul_f32 %0, %4, %8\n\tv_mul_f32 %1, %5, %8\n\tv_mul_f32 %2, %6, %8\n\tv_mul_f32 %3, %7, %8\n\t"
                             "v_add_f32_dpp %0, %0, %0 quad_perm:[1,0,3,2] row_mask:0xf bank_mask:0xf\n\tv_add_f32_dpp %1, %1, %1 quad_perm:[1,0,3,2] row_mask:0xf bank_mask:0xf\n\t"
                             "v_add_f32_dpp %2, %2, %2 quad_perm:[1,0,3,2] row_mask:0xf bank_mask:0xf\n\tv_add_f32_dpp %3, %3, %3 quad_perm:[1,0,3,2] row_mask:0xf bank_mask:0xf"
                             : "=&v"(c0), "=&v"(c1), "=&v"(c2), "=&v"(c3) : "v"(ca[0]), "v"(ca[1]), "v"(ca[2]), "v"(ca[3]), "v"(f));
                st16m(ws, so, asu((f32x4){c0, c1, c2, c3}), mk);
                if (layer == 0 && wave == 7) st4_sc1(ws, WS_MB + (unsigned)(gi * 1024 + 64 + gj * 4), now());
                STAMP(7 + 32 * layer);
                if (layer != 0) sched(X3, 12); else wait_word(ws, pb);
                STAMP(18 + 32 * layer);
                gather32(go, xs, layer ? X3 : 0u, [&]() { sa = stats_addr(lane, h4 + (unsigned)(15360 + gj * 128), 16); }, [&](float s) { return xe + bfr(s); });
                STAMP(9 + 32 * layer);
            }
            BAR();
            if (wave == 3) stats(sa);
            STAMP(11 + 32 * layer);
            {
                LANE_VARS();
                const unsigned im = lb + L_WD + (unsigned)(32 * t + 8 * p4);
                bf16x8 wd0 = cat8(tr64(im + r0s * 160), tr64(im + r1s * 160)); bf16x4 wd1 = tr64(im + (32 + r2s) * 160);
                const unsigned h5 = WS_H35 + (unsigned)((gj * 20 + (layer ? 0 : 5) + t) * 8192);
                unsigned h16 = lb + L_H + (unsigned)(p4 * 96 + kq * 16), h8 = lb + L_H + (unsigned)(p4 * 96 + 64 + kq * 8);
                unsigned so = h5 + (unsigned)(gi * 256 + n * 64 + kq * 16);
                unsigned long long mk = 0x000f000f000f000full;
                PIN2(wd0, wd1); PIN3(h16, h8, so); PINS(mk);
                if (wave == 4) {
                    const unsigned os = h4 + (unsigned)(15360 + (p4 + 4 * (kq & 1)) * 128 + q4 * 16);
                    if (layer == 0) {
#pragma unroll 1
                        for (unsigned spins = 0; spins < (kSpinCap << 4); ++spins) if (lds_word(lb + L_CNT + 56)) break;
                    }
                    sched(layer ? X4 : X4A, 0);
                    u32x4 as;
#pragma unroll 1
                    for (unsigned spins = 0; spins < kSpinCap; ++spins) { as = ld16_sc1(ws, os); WAIT1(as, 0); if (!any_empty(um4(as))) break; }
                    float ss = __uint_as_float(as[0]), sm = __uint_as_float(as[1]), rd = __uint_as_float(as[2]);
                    ss += dpp<0xB1>(ss); sm += dpp<0xB1>(sm); rd += dpp<0xB1>(rd);
                    ss += dpp<0x4E>(ss); sm += dpp<0x4E>(sm); rd += dpp<0x4E>(rd);
                    ss = x16_add(ss); sm = x16_add(sm); rd = x16_add(rd);
                    if (LANES(0x1111ull)) *reinterpret_cast<f32x4*>(smem + L_ST + q4 * 16) = (f32x4){ss, sm, rd, 0.f};
                }
                BAR();
                STAMP(12 + 32 * layer);
                const bf16x8 hb0 = lds16(h16); const bf16x4 hb1 = lds8(h8);
                __builtin_amdgcn_sched_barrier(0);
                const f32x4 c = mfma32(wd0, hb0, z4) + mfma16(wd1, hb1, z4);
                st16m(ws, so, asu(c), mk);
                STAMP(13 + 33 * layer);
                if (layer == 0 || t == gi % 5) {
                    float rs, n1;
                    auto prep = [&]() __attribute__((always_inline)) {
                        const f32x4 sr = *reinterpret_cast<const f32x4*>(smem + L_ST + q4 * 16);
                        const float sw = *reinterpret_cast<const float*>(smem + L_CNT + 12);
                        const f32x2 st = ln_stats(sr[0], sr[1]);
                        const float r = bfr((sr[2] - st[0] * sw) * st[1]);
                        rs = bfr(frcp(1.f + ex2(r * -LOG2E)));
                        n1 = bfr(bfr(xe - st[0]) * st[1]);
                        PIN2(rs, n1);
                    };
                    if (layer == 0) {
                        unsigned go = h5 + (unsigned)(4096 + lane * 16), xs = lb + L_XS + (unsigned)(q4 * 160 + t * 32 + p4 * 8 + kq * 2);
                        PIN2(go, xs);
                        sched(X5A, 8);
                        STAMP(15);
                        gather32(go, xs, X5A, [&]() { prep(); sa = stats_addr(lane, WS_H1 + (unsigned)((gi * 32 + gj) * 896 + 768), 7168); }, [&](float y) { return xe + bfr(n1 + bfr(bfr(y) * rs)); });
                        STAMP(14);
                    } else if (gi < 20) {
                        const int b = gi / 5;
                        unsigned go = h5 + (unsigned)(2048 + (lane >> 2) * 256 + b * 64 + p4 * 16), oo = (unsigned)(q4 * 1280 + gj * 160 + t * 32 + p4 * 8 + kq * 2);
                        PIN2(go, oo);
                        sched(X5B, 8);
                        STAMP(47);
                        float y; u32x4 a0, a1;
                        RETRY({ a0 = ld16_sc1<-2048>(ws, go); a1 = ld16_sc1<2048>(ws, go); prep(); asm volatile("s_waitcnt vmcnt(0)" : "+v"(a0), "+v"(a1) :: "memory"); }, max(um4(a0), um4(a1)),
                              { const f32x4 s = asf(a0) + asf(a1); const f32x2 v2 = sw16(rs32(s[0], s[2]), rs32(s[1], s[3])); y = v2[0] + v2[1]; y += dpp<0x124>(y); y += dpp<0x128>(y); }, any_nan(y, y), 0u);
                        const unsigned xw = pk2(xe + bfr(n1 + bfr(bfr(y) * rs)), 0.f);
                        if (LANES(0x000f000f000f000full << (4 * b))) st2(out, oo, xw);
                        STAMP(45);
                    }
                }
            }
            if (layer == 0) {
                BAR();
                if (wave == 3) { stats(sa); STAMP(34); }
                if (wave != 7) { qk_addr(true); att_addr(); }
                else {
                    const int lane = lane_id();
                    unsigned o = WS_H1 + (unsigned)((gi * 4 + (gj >> 1)) * 7168 + 3584 + (lane < 48 ? lane : 48) * 16), qw;
                    f32x4 cs;
                    PIN1(o);
                    sched(X1, 26);
                    STAMP(51);
                    f32x4 s; u32x4 a[8];
                    RETRY8(a, o, 896, s, { qw = lb + L_Q + (unsigned)(lane * 8); cs = *reinterpret_cast<const f32x4*>(smem + L_CS + lane * 16); PIN2(qw, cs); qk_addr(true); att_addr(); }, { s -= bfr(__int_as_float(__builtin_amdgcn_readlane(__float_as_int(s[1]), 48)) * (1.f / DM)) * cs;
                                           u32x2 w; w[0] = pk2(s[0], s[1]); w[1] = pk2(s[2], s[3]);
                                           if (LANES(0xffff000000000000ull)) { w[0] = __float_as_uint(s[0]); w[1] = __float_as_uint(s[1]); }
                                           *(LAS u32x2*)(size_t)qw = w; __builtin_amdgcn_sched_barrier(0); }, any_nan(s[0] + s[2], s[1] + s[3]), X1);
                }
            }
        }
    };
    chain(IC<0>{});
    att_qk(IC<1>{});
    att_pv(IC<1>{});
    chain(IC<1>{});
}

__device__ __attribute__((used, noinline)) void mega_code_end() { asm volatile(".Lmega_code_end:"); }

extern "C" void kernel_launch(void* const* d_in, const int* in_sizes, int n_in, void* d_out, int out_size, void* d_ws,
                              size_t ws_size, hipStream_t stream) {
    const u16* x0 = (const u16*)d_in[0];
    const u16* kc = (const u16*)d_in[1];
    const u16* vc = (const u16*)d_in[2];
    const u16* wq = (const u16*)d_in[3];
    const u16* wo = (const u16*)d_in[4];
    const u16* wr = (const u16*)d_in[6];
    const u16* wg = (const u16*)d_in[7];
    const u16* wu = (const u16*)d_in[8];
    const u16* wd = (const u16*)d_in[9];
    (void)hipFuncSetAttribute((const void*)mega_kernel, hipFuncAttributeMaxDynamicSharedMemorySize, L_END);
    mega_kernel<<<256, 512, L_END, stream>>>(x0, kc, vc, wq, wo, wr, wg, wu, wd, (u16*)d_out, (unsigned char*)d_ws);
}
```

```cpp
#include <hip/hip_runtime.h>
#include <hip/hip_bf16.h>
#include <stdint.h>

typedef unsigned short u16;
typedef unsigned u32x4 __attribute__((ext_vector_type(4)));
typedef unsigned u32x2 __attribute__((ext_vector_type(2)));
typedef float f32x4 __attribute__((ext_vector_type(4)));
typedef short s16x4 __attribute__((ext_vector_type(4)));
typedef __attribute__((ext_vector_type(4))) __bf16 bf16x4;
typedef __attribute__((ext_vector_type(8))) __bf16 bf16x8;
#define LAS __attribute__((address_space(3)))

constexpr int DM = 640, HQ = 32, SK = 4096, DH = 192, WIN = 1024, FF = 1536;
constexpr int QD = HQ * DH;
constexpr float SCALE = 0.07216878364870322f, LOG2E = 1.44269504088896341f;
constexpr unsigned kSpinCap = 1u << 16;
#define GJ(g) (((g) >> 3) & 7)
#define GI(g) (((g) & 7) * 4 + ((g) >> 6))

#define D4A 148
#define D5A 104
#define D1 112
#define D2 162
#define D3 76
#define D4 96
#define D5B 88
#define PACE 60
#define PINIT 200
#define PMIN 50
constexpr unsigned X4A = D4A, X5A = X4A + D5A, X1 = X5A + D1, X2 = X1 + D2, X3 = X2 + D3, X4 = X3 + D4, X5B = X4 + D5B;
#define WOC 2
#define GUC 3
#define DG2 15
#define DGU 25
#define EARLY 7
#define XCOST 16
constexpr unsigned NUNIT = 384 + XCOST + 9 * ((EARLY & 1) + ((EARLY >> 1) & 1) + ((EARLY >> 2) & 1)), PBURST = 30;
#define GSTEPS GP = 0, GC = 3, GD = 5, GL = 0, GE = 2, GF = 4
#define C3 (PINIT + NUNIT * PACE / 16 + E3)
#define DWD 20
#define ADOPT 1
#define TOFF 15
#define FX 0
#define PRIO 0
#define GOPT 0
#define GB4 0
#define GST 0
#define POLL3 1
#define M3 20
#define E3 330
#define D4S 125
#define XG2 0
#define WOFF 0
#define FREEW(w) ((w) >= 6)
#define LEAD 12
#define DQ 40
#define DMB 30
constexpr int GSTEPS;
constexpr int QKT = 2;

constexpr unsigned WS_R = 0;
constexpr unsigned WS_B = 1024;
constexpr unsigned WS_MB = 4096;
constexpr unsigned WS_H1 = WS_MB + 256 * 128;
constexpr unsigned WS_OWN = WS_H1 + 32 * 4 * 7168;
constexpr unsigned WS_H35 = WS_OWN + 256 * 2048;
constexpr unsigned WS_H4 = WS_H35 + 160 * 4096;
constexpr unsigned WS_END = WS_H4 + 64 * 16384;
constexpr unsigned WS_DBG = 6680576;
static_assert(WS_END <= WS_DBG, "ws map");

constexpr int L_WQ = 0;
constexpr int L_WO = 33792;
constexpr int L_GU = L_WO + 30720;
constexpr int L_WD = L_GU + 16384;
constexpr int L_X0 = L_WD + 8192;
constexpr int L_WR = L_X0 + 5120;
constexpr int L_XS = L_WR + 2048;
constexpr int L_Q = L_XS + 1024;
constexpr int L_OP = L_Q + 512;
constexpr int L_M = L_OP + 8192;
constexpr int L_H = L_M + 64;
constexpr int L_ST = L_H + 384;
constexpr int L_CS = L_ST + 64;
constexpr int L_CS2 = L_CS + 1024;
constexpr int L_CNT = L_CS2 + 1536;
constexpr int L_P = L_CNT + 80;
constexpr int L_O2 = L_P + 72 * 192;
constexpr int L_DBG = L_O2 + 3328;
constexpr int L_END = L_DBG + 2048;
static_assert(L_END <= 163840 && L_END > 81920, "LDS");

typedef __attribute__((ext_vector_type(2))) __bf16 bf16x2;
typedef float f32x2 __attribute__((ext_vector_type(2)));
__device__ __forceinline__ unsigned pk2(float a, float b) { return __builtin_bit_cast(unsigned, __builtin_convertvector((f32x2){a, b}, bf16x2)); }
__device__ __forceinline__ float lo16(unsigned w) { return __uint_as_float(w << 16); }
__device__ __forceinline__ float hi16(unsigned w) { return __uint_as_float(w & 0xffff0000u); }
__device__ __forceinline__ float bfr(float f) { return __uint_as_float(pk2(0.f, f)); }
template <int CTRL> __device__ __forceinline__ float dpp(float x) {
    return __builtin_bit_cast(float, __builtin_amdgcn_mov_dpp(__builtin_bit_cast(int, x), CTRL, 0xf, 0xf, true));
}
template <int CTRL> __device__ __forceinline__ unsigned dppu(unsigned x) { return (unsigned)__builtin_amdgcn_mov_dpp((int)x, CTRL, 0xf, 0xf, true); }
__device__ __forceinline__ float row16_sum(float v) {
    v += dpp<0xB1>(v); v += dpp<0x4E>(v); v += dpp<0x141>(v); v += dpp<0x140>(v); return v;
}
__device__ __forceinline__ float fmax2(float a, float b) { return __builtin_elementwise_maximum(a, b); }
__device__ __forceinline__ float fmax3(float a, float b, float c) { return fmax2(fmax2(a, b), c); }
__device__ __forceinline__ float x16_add(float v) { auto s = __builtin_amdgcn_permlane16_swap(__float_as_uint(v), __float_as_uint(v), false, false); return __uint_as_float(s[0]) + __uint_as_float(s[1]); }
__device__ __forceinline__ float x32_add(float v) { auto s = __builtin_amdgcn_permlane32_swap(__float_as_uint(v), __float_as_uint(v), false, false); return __uint_as_float(s[0]) + __uint_as_float(s[1]); }
__device__ __forceinline__ float wave_sum(float v) { return x32_add(x16_add(row16_sum(v))); }
__device__ __forceinline__ float rs32(float a, float b) { auto s = __builtin_amdgcn_permlane32_swap(__float_as_uint(a), __float_as_uint(b), false, false); return __uint_as_float(s[0]) + __uint_as_float(s[1]); }
__device__ __forceinline__ f32x2 sw16(float a, float b) { auto s = __builtin_amdgcn_permlane16_swap(__float_as_uint(a), __float_as_uint(b), false, false); return (f32x2){__uint_as_float(s[0]), __uint_as_float(s[1])}; }

#define BAR() asm volatile("s_waitcnt lgkmcnt(0)\n\ts_barrier" ::: "memory")
#define VMW(n) asm volatile("s_waitcnt vmcnt(%0)" :: "i"(n) : "memory")

template <int OFF = 0> __device__ __forceinline__ u32x4 ld16_sc1(const void* sbase, unsigned voff) {
    u32x4 r; asm volatile("global_load_dwordx4 %0, %1, %2 offset:%3 sc1" : "=v"(r) : "v"(voff), "s"(sbase), "i"(OFF) : "memory"); return r;
}
__device__ __forceinline__ unsigned ld4_sc1(const void* sbase, unsigned voff) {
    unsigned r; asm volatile("global_load_dword %0, %1, %2 sc1" : "=v"(r) : "v"(voff), "s"(sbase) : "memory"); return r;
}
template <int OFF = 0> __device__ __forceinline__ void st16_sc1(void* sbase, unsigned voff, u32x4 v) {
    asm volatile("global_store_dwordx4 %0, %1, %2 offset:%3 sc1\n\ts_nop 1" :: "v"(voff), "v"(v), "s"(sbase), "i"(OFF) : "memory");
}
__device__ __forceinline__ void st4_sc1(void* sbase, unsigned voff, unsigned v) {
    asm volatile("global_store_dword %0, %1, %2 sc1" :: "v"(voff), "v"(v), "s"(sbase) : "memory");
}
__device__ __forceinline__ void st8(void* sbase, unsigned voff, u32x2 v) {
    asm volatile("global_store_dwordx2 %0, %1, %2" :: "v"(voff), "v"(v), "s"(sbase) : "memory");
}
__device__ __forceinline__ void st2(void* sbase, unsigned voff, unsigned v) {
    asm volatile("global_store_short %0, %1, %2" :: "v"(voff), "v"(v), "s"(sbase) : "memory");
}
constexpr int RK = 64, RV = 160;
template <int R, int IMM> __device__ __forceinline__ void ld16_nt(const void* sbase, unsigned voff) {
    asm volatile("global_load_dwordx4 v[%c3:%c4], %0, %1 offset:%2 nt" :: "v"(voff), "s"(sbase), "i"(IMM), "i"(R), "i"(R + 3) : "memory");
}
template <int R, int IMM> __device__ __forceinline__ void ld8_nt(const void* sbase, unsigned voff) {
    asm volatile("global_load_dwordx2 v[%c3:%c4], %0, %1 offset:%2 nt" :: "v"(voff), "s"(sbase), "i"(IMM), "i"(R), "i"(R + 1) : "memory");
}
template <int R> __device__ __forceinline__ void smf(f32x4& d, u32x4 a, unsigned idx) {
    asm volatile("v_smfmac_f32_16x16x64_bf16 %0, %1, v[%c3:%c4], %2" : "+v"(d) : "v"(a), "v"(idx), "i"(R), "i"(R + 7));
}
template <int A, int B> __device__ __forceinline__ void zip_in() {
    unsigned t;
    asm volatile("v_perm_b32 %0, v%c2, v%c1, %3\n\tv_perm_b32 v%c2, v%c2, v%c1, %4\n\tv_mov_b32 v%c1, %0" : "=&v"(t) : "i"(A), "i"(B), "s"(0x05040100u), "s"(0x07060302u));
}
template <int A, int B> __device__ __forceinline__ void zip_out(unsigned& lo, unsigned& hi) {
    asm volatile("v_perm_b32 %0, v%c3, v%c2, %4\n\tv_perm_b32 %1, v%c3, v%c2, %5" : "=&v"(lo), "=&v"(hi) : "i"(A), "i"(B), "s"(0x05040100u), "s"(0x07060302u));
}
template <int A, int B, int C, int D> __device__ __forceinline__ void zip_x(unsigned lo, unsigned hi) {
    asm volatile("v_perm_b32 v%c4, v%c3, v%c2, %6\n\tv_perm_b32 v%c5, v%c3, v%c2, %7\n\tv_mov_b32 v%c2, %0\n\tv_mov_b32 v%c3, %1"
                 :: "v"(lo), "v"(hi), "i"(A), "i"(B), "i"(C), "i"(D), "s"(0x05040100u), "s"(0x07060302u));
}
__device__ __forceinline__ void dma16(const void* sbase, unsigned voff, unsigned lds_dst) {
    unsigned keep;
    asm volatile("s_mov_b32 %0, m0\n\ts_mov_b32 m0, %3\n\ts_nop 0\n\tglobal_load_lds_dwordx4 %1, %2 nt\n\ts_mov_b32 m0, %0"
                 : "=&s"(keep) : "v"(voff), "s"(sbase), "s"(lds_dst) : "memory");
}
__device__ __forceinline__ void dma16p(const void* sbase, unsigned voff, unsigned lds_dst) {
    unsigned keep;
    asm volatile("s_mov_b32 %0, m0\n\ts_mov_b32 m0, %3\n\ts_nop 0\n\tglobal_load_lds_dwordx4 %1, %2\n\ts_mov_b32 m0, %0"
                 : "=&s"(keep) : "v"(voff), "s"(sbase), "s"(lds_dst) : "memory");
}
__device__ __forceinline__ unsigned um4(u32x4 a) { return max(max(a[0], a[1]), max(a[2], a[3])); }
__device__ __forceinline__ bool none(unsigned t) { return __builtin_amdgcn_ballot_w64(t != 0u) == 0ull; }
__device__ __forceinline__ bool any_empty(unsigned mx) { return __builtin_amdgcn_ballot_w64(mx == ~0u) != 0ull; }
__device__ __forceinline__ unsigned long long any_nan(float a, float b) { return __builtin_amdgcn_ballot_w64(__builtin_isunordered(a, b)); }
__device__ __forceinline__ f32x4 asf(u32x4 a) { return __builtin_bit_cast(f32x4, a); }
__device__ __forceinline__ u32x4 asu(f32x4 a) { return __builtin_bit_cast(u32x4, a); }
#define LANES(m) __builtin_amdgcn_inverse_ballot_w64(m)

__device__ __forceinline__ bf16x4 tr64(unsigned lds_addr) { return __builtin_amdgcn_ds_read_tr16_b64_v4bf16((LAS bf16x4*)(size_t)lds_addr); }
__device__ __forceinline__ bf16x8 cat8(bf16x4 a, bf16x4 b) { return __builtin_shufflevector(a, b, 0, 1, 2, 3, 4, 5, 6, 7); }
__device__ __forceinline__ bf16x8 lds16(unsigned a) { return *(const LAS bf16x8*)(size_t)a; }
__device__ __forceinline__ bf16x4 lds8(unsigned a) { return *(const LAS bf16x4*)(size_t)a; }
__device__ __forceinline__ f32x4 mfma32(bf16x8 a, bf16x8 b, f32x4 c) { return __builtin_amdgcn_mfma_f32_16x16x32_bf16(a, b, c, 0, 0, 0); }
__device__ __forceinline__ f32x4 mfma32u(u32x4 a, u32x4 b, f32x4 c) { return mfma32(__builtin_bit_cast(bf16x8, a), __builtin_bit_cast(bf16x8, b), c); }
__device__ __forceinline__ f32x4 mfma16(bf16x4 a, bf16x4 b, f32x4 c) {
    return __builtin_amdgcn_mfma_f32_16x16x16bf16_1k(__builtin_bit_cast(s16x4, a), __builtin_bit_cast(s16x4, b), c, 0, 0, 0);
}

#define STAMP(i) do {} while (0)

__device__ __forceinline__ void wait_word(const void* sbase, unsigned off) {
    unsigned t, cnt = kSpinCap;
    asm volatile("1:\n\tglobal_load_dword %0, %2, %3 sc1\n\ts_sub_u32 %1, %1, 1\n\ts_waitcnt vmcnt(0)\n\tv_cmp_eq_u32 vcc, -1, %0\n\ts_cbranch_vccz 2f\n\ts_cbranch_scc0 1b\n2:"
                 : "=&v"(t), "+s"(cnt) : "v"(off), "s"(sbase) : "memory", "vcc", "scc");
}
template <int OFF = 0> __device__ __forceinline__ void st16m(void* sbase, unsigned voff, u32x4 v, unsigned long long mask) {
    asm volatile("s_mov_b64 exec, %4\n\tglobal_store_dwordx4 %0, %1, %2 offset:%3 sc1\n\ts_mov_b64 exec, -1\n\ts_nop 0" :: "v"(voff), "v"(v), "s"(sbase), "i"(OFF), "s"(mask) : "memory");
}
__device__ __forceinline__ void st8m(void* sbase, unsigned voff, u32x2 v, unsigned long long mask) {
    asm volatile("s_mov_b64 exec, %3\n\tglobal_store_dwordx2 %0, %1, %2 sc1\n\ts_mov_b64 exec, -1" :: "v"(voff), "v"(v), "s"(sbase), "s"(mask) : "memory");
}
template <int OFF> __device__ __forceinline__ void st16m2(void* sbase, unsigned voff, u32x4 v, unsigned voff2, u32x4 v2, unsigned long long mask) {
    asm volatile("s_mov_b64 exec, %6\n\tglobal_store_dwordx4 %0, %1, %2 sc1\n\tglobal_store_dwordx4 %4, %5, %2 offset:%3 sc1\n\ts_mov_b64 exec, -1\n\ts_nop 0"
                 :: "v"(voff), "v"(v), "s"(sbase), "i"(OFF), "v"(voff2), "v"(v2), "s"(mask) : "memory");
}
#define PINS(a) asm volatile("" : "+s"(a))
#define RETRY(LOAD, EMPTY, COMPUTE, NAN_, X) do { LOAD; COMPUTE; if (__builtin_expect((NAN_) != 0ull, 0)) { \
    _Pragma("unroll 1") for (unsigned spins_ = 0; spins_ < kSpinCap; ++spins_) { slip(X); LOAD; if (!any_empty(EMPTY)) break; } COMPUTE; } } while (0)
__device__ __forceinline__ unsigned now() { return (unsigned)__builtin_amdgcn_s_memrealtime(); }
__device__ __forceinline__ void until(unsigned t) {
    asm volatile("1:\n\ts_memrealtime vcc\n\ts_waitcnt lgkmcnt(0)\n\ts_sub_u32 vcc_lo, %0, vcc_lo\n\ts_cmp_lt_u32 vcc_lo, 0x400\n\ts_cbranch_scc1 1b" :: "s"(t - 1u) : "vcc", "scc", "memory");
}
__device__ __forceinline__ unsigned lds_word(unsigned a) {
    unsigned t_; asm volatile("ds_read_b32 %0, %1\n\ts_waitcnt lgkmcnt(0)\n\ts_nop 0" : "=v"(t_) : "v"(a) : "memory");
    return __builtin_amdgcn_readfirstlane(t_);
}
__device__ __forceinline__ void lds_set(unsigned a, unsigned v) { asm volatile("ds_write_b32 %0, %1" :: "v"(a), "v"(v) : "memory"); }
__device__ __forceinline__ int swz(int r) { return r ^ (((r >> 3) & 1) << 2); }
__device__ __forceinline__ f32x2 unpk(unsigned w) { return (f32x2){lo16(w), hi16(w)}; }
__device__ __forceinline__ unsigned pk2v(f32x2 v) { return __builtin_bit_cast(unsigned, __builtin_convertvector(v, bf16x2)); }
__device__ __forceinline__ float frcp(float x) { return __builtin_amdgcn_rcpf(x); }
__device__ __forceinline__ float ex2(float x) { return __builtin_amdgcn_exp2f(x); }
__device__ __forceinline__ int lane_id() { int l; asm volatile("v_mbcnt_lo_u32_b32 %0, -1, 0\n\tv_mbcnt_hi_u32_b32 %0, -1, %0" : "=v"(l)); return l & 63; }
__device__ __forceinline__ f32x2 ln_stats(float ss, float s) {
    const float mean = s * (1.f / DM);
    const float var = fmax2(__builtin_fmaf(-mean, mean, ss * (1.f / DM)), 0.f);
    const unsigned mv = pk2(mean, var);
    return (f32x2){lo16(mv), frcp(bfr(__builtin_amdgcn_sqrtf(bfr(hi16(mv) + 1e-5f))))};
}
#define GATHER8(a, o, S) do { a[0] = ld16_sc1<-4 * (S)>(ws, o); a[1] = ld16_sc1<-3 * (S)>(ws, o); a[2] = ld16_sc1<-2 * (S)>(ws, o); a[3] = ld16_sc1<-(S)>(ws, o); \
    a[4] = ld16_sc1<0>(ws, o); a[5] = ld16_sc1<(S)>(ws, o); a[6] = ld16_sc1<2 * (S)>(ws, o); a[7] = ld16_sc1<3 * (S)>(ws, o); } while (0)
template <int OFF> __device__ __forceinline__ void again16(u32x4& r, const void* sbase, unsigned voff) {
    asm volatile("v_cmp_eq_u32 vcc, -1, %1\n\ts_mov_b64 exec, vcc\n\tglobal_load_dwordx4 %0, %2, %3 offset:%4 sc1\n\ts_mov_b64 exec, -1" : "+v"(r) : "v"(um4(r)), "v"(voff), "s"(sbase), "i"(OFF) : "memory", "vcc");
}
#define AGAIN8(a, o, S) do { again16<-4 * (S)>(a[0], ws, o); again16<-3 * (S)>(a[1], ws, o); again16<-2 * (S)>(a[2], ws, o); again16<-(S)>(a[3], ws, o); \
    again16<0>(a[4], ws, o); again16<(S)>(a[5], ws, o); again16<2 * (S)>(a[6], ws, o); again16<3 * (S)>(a[7], ws, o); } while (0)
#define WAIT8(a, N) asm volatile("s_waitcnt vmcnt(%8)" : "+v"((a)[0]), "+v"((a)[1]), "+v"((a)[2]), "+v"((a)[3]), "+v"((a)[4]), "+v"((a)[5]), "+v"((a)[6]), "+v"((a)[7]) : "i"(N) : "memory")
#define UM32(a) max(max(max(um4(a[0]), um4(a[1])), max(um4(a[2]), um4(a[3]))), max(max(um4(a[4]), um4(a[5])), max(um4(a[6]), um4(a[7]))))
#define SUM8(a) (((((((asf(a[0]) + asf(a[1])) + asf(a[2])) + asf(a[3])) + asf(a[4])) + asf(a[5])) + asf(a[6])) + asf(a[7]))
#define WAIT1(x, N) asm volatile("s_waitcnt vmcnt(%1)" : "+v"(x) : "i"(N) : "memory")
#define LO2(x) __builtin_shufflevector(x, x, 0, 1)
#define HI2(x) __builtin_shufflevector(x, x, 2, 3)
#define ACC8(a, s) do { f32x2 l_, h_; \
    asm volatile("s_waitcnt vmcnt(5)\n\tv_pk_add_f32 %0, %2, %4\n\tv_pk_add_f32 %1, %3, %5\n\tv_pk_add_f32 %0, %0, %6\n\tv_pk_add_f32 %1, %1, %7\n\t" \
                 "s_waitcnt vmcnt(3)\n\tv_pk_add_f32 %0, %0, %8\n\tv_pk_add_f32 %1, %1, %9\n\tv_pk_add_f32 %0, %0, %10\n\tv_pk_add_f32 %1, %1, %11\n\t" \
                 "s_waitcnt vmcnt(1)\n\tv_pk_add_f32 %0, %0, %12\n\tv_pk_add_f32 %1, %1, %13\n\tv_pk_add_f32 %0, %0, %14\n\tv_pk_add_f32 %1, %1, %15\n\t" \
                 "s_waitcnt vmcnt(0)\n\tv_pk_add_f32 %0, %0, %16\n\tv_pk_add_f32 %1, %1, %17\n\ts_nop 0" \
                 : "=&v"(l_), "=&v"(h_) : "v"(LO2(a[0])), "v"(HI2(a[0])), "v"(LO2(a[1])), "v"(HI2(a[1])), "v"(LO2(a[2])), "v"(HI2(a[2])), "v"(LO2(a[3])), "v"(HI2(a[3])), \
                   "v"(LO2(a[4])), "v"(HI2(a[4])), "v"(LO2(a[5])), "v"(HI2(a[5])), "v"(LO2(a[6])), "v"(HI2(a[6])), "v"(LO2(a[7])), "v"(HI2(a[7])) : "memory"); \
    s = __builtin_shufflevector(l_, h_, 0, 1, 2, 3); } while (0)
#define RETRY8(a, o, S, s, MID, POST, NAN_, X) do { GATHER8(a, o, S); MID; ACC8(a, s); POST; if (__builtin_expect((NAN_) != 0ull, 0)) { \
    _Pragma("unroll 1") for (unsigned spins_ = 0; spins_ < kSpinCap; ++spins_) { slip(X); AGAIN8(a, o, S); WAIT8(a, 0); if (!any_empty(UM32(a))) break; } s = SUM8(a); POST; } } while (0)
#define PIN1(a) asm volatile("" : "+v"(a))
#define PIN2(a, b) asm volatile("" : "+v"(a), "+v"(b))
#define PIN3(a, b, c) asm volatile("" : "+v"(a), "+v"(b), "+v"(c))
#define LANE_VARS() const int lane = lane_id(); \
    const int kq = lane >> 4, n = lane & 15, q4 = n >> 2, p4 = lane & 3; \
    const int r0s = 8 * kq + q4 + 4 * (kq & 1), r1s = 8 * kq + q4 + 4 * ((kq & 1) ^ 1);     \
    const int r2s = swz(4 * kq + q4);                                                       \
    (void)r0s; (void)r1s; (void)r2s; (void)p4
template <int V> struct IC { static constexpr int value = V; };
struct Frag80 { bf16x8 a0, a1; bf16x4 a2; };

__global__ void __launch_bounds__(512, 2) __attribute__((amdgpu_num_vgpr(32)))
mega_kernel(const u16* __restrict__ x0, const u16* __restrict__ kc, const u16* __restrict__ vc, const u16* __restrict__ wq,
            const u16* __restrict__ wo, const u16* __restrict__ wr, const u16* __restrict__ wg, const u16* __restrict__ wu,
            const u16* __restrict__ wd, u16* __restrict__ out, unsigned char* __restrict__ ws) {
    extern __shared__ __attribute__((aligned(16))) unsigned char smem[];
    const unsigned lb = (unsigned)(size_t)smem;
    const int wave = __builtin_amdgcn_readfirstlane(threadIdx.x >> 6);
    const int g = blockIdx.x, gj = GJ(g), gi = GI(g);
    const f32x4 z4 = {0.f, 0.f, 0.f, 0.f};
    asm volatile("" ::: "v255");
    auto sched = [&](unsigned X, unsigned gap) {
        unsigned v; unsigned long long c;
        asm volatile("s_memrealtime %1\n\tds_read_b32 %0, %2\n\ts_waitcnt lgkmcnt(0)" : "=v"(v), "=&s"(c) : "v"(lb + L_CNT + 56) : "memory");
        const unsigned n = (unsigned)c + gap;
        unsigned t = __builtin_amdgcn_readfirstlane(v) + X;
        if ((int)(n - t) > 0) t = n;
        until(t);
    };
    auto slip = [&](unsigned X) {
        if (X) lds_set(lb + L_CNT + 56, now() - X);
    };
    float xe = 0.f;

    auto code_pf = [&](int slice, int lane) {
        unsigned long long pc0; unsigned len, back, t;
        asm volatile("s_getpc_b64 %0\n.Lmega_pf%=:\n\ts_mov_b32 %1, .Lmega_code_end-.Lmega_code_begin\n\ts_mov_b32 %2, .Lmega_pf%=-.Lmega_code_begin" : "=s"(pc0), "=s"(len), "=s"(back));
        unsigned off = (unsigned)(slice * 4096 + lane * 64); if (off > len) off = len;
        asm volatile("global_load_dword %0, %1, %2" : "=v"(t) : "v"(off & ~3u), "s"(pc0 - back) : "memory");
        return t;
    };
    asm volatile(".Lmega_code_begin:");
    auto fix_k1 = [&](auto IC_, auto NC) {
        constexpr int A = RK + 8 * decltype(IC_)::value, B = A + 4;
        unsigned t0, t1, t2, t3;
        asm volatile("s_waitcnt vmcnt(%12)\n\tv_mov_b32 %0, v%c4\n\tv_mov_b32 %1, v%c5\n\tv_mov_b32 %2, v%c6\n\tv_mov_b32 %3, v%c7\n\t"
                     "v_mov_b32_dpp v%c4, v%c8 row_ror:8 row_mask:0xf bank_mask:0xc\n\tv_mov_b32_dpp v%c5, v%c9 row_ror:8 row_mask:0xf bank_mask:0xc\n\t"
                     "v_mov_b32_dpp v%c6, v%c10 row_ror:8 row_mask:0xf bank_mask:0xc\n\tv_mov_b32_dpp v%c7, v%c11 row_ror:8 row_mask:0xf bank_mask:0xc\n\t"
                     "v_mov_b32_dpp v%c8, %0 row_ror:8 row_mask:0xf bank_mask:0x3\n\tv_mov_b32_dpp v%c9, %1 row_ror:8 row_mask:0xf bank_mask:0x3\n\t"
                     "v_mov_b32_dpp v%c10, %2 row_ror:8 row_mask:0xf bank_mask:0x3\n\tv_mov_b32_dpp v%c11, %3 row_ror:8 row_mask:0xf bank_mask:0x3"
                     : "=&v"(t0), "=&v"(t1), "=&v"(t2), "=&v"(t3)
                     : "i"(A), "i"(A + 1), "i"(A + 2), "i"(A + 3), "i"(B), "i"(B + 1), "i"(B + 2), "i"(B + 3), "i"(decltype(NC)::value) : "memory");
    };
    auto trans_v = [&](auto UC, auto NC) {
        constexpr int R = RV + 4 * decltype(UC)::value, N = decltype(NC)::value;
#define CELL(r, c) R + 16 * (r) + (c), R + 16 * (r) + 8 + (c)
        unsigned a0, a1, b0, b1, c0, c1;
        VMW(N + 6); zip_in<CELL(0, 0)>(); zip_out<CELL(0, 1)>(a0, a1); zip_out<CELL(0, 2)>(b0, b1); zip_out<CELL(0, 3)>(c0, c1);
        VMW(N + 4); zip_x<CELL(1, 0), CELL(0, 1)>(a0, a1); zip_in<CELL(1, 1)>(); zip_out<CELL(1, 2)>(a0, a1);
        VMW(N + 2); zip_x<CELL(2, 0), CELL(0, 2)>(b0, b1); zip_x<CELL(2, 1), CELL(1, 2)>(a0, a1); zip_in<CELL(2, 2)>(); zip_out<CELL(1, 3)>(a0, a1); zip_out<CELL(2, 3)>(b0, b1);
        VMW(N); zip_x<CELL(3, 0), CELL(0, 3)>(c0, c1); zip_x<CELL(3, 1), CELL(1, 3)>(a0, a1); zip_x<CELL(3, 2), CELL(2, 3)>(b0, b1); zip_in<CELL(3, 3)>();
#undef CELL
    };
    auto trans_w = [&](auto UC, auto NC) {
        constexpr int R = RV + 64 + 2 * decltype(UC)::value, N = decltype(NC)::value;
        unsigned a0, a1;
        VMW(N + 2); zip_in<R, R + 8>(); zip_out<R + 1, R + 9>(a0, a1);
        VMW(N); zip_x<R + 16, R + 24, R + 1, R + 9>(a0, a1); zip_in<R + 17, R + 25>();
    };
    auto frag80 = [&](unsigned im, int rs, int r0s, int r1s, int r2s) -> Frag80 {
        return Frag80{cat8(tr64(im + r0s * rs), tr64(im + r1s * rs)), cat8(tr64(im + (r0s + 32) * rs), tr64(im + (r1s + 32) * rs)), tr64(im + (r2s + 64) * rs)};
    };
    auto mm80 = [&](const Frag80& a, const Frag80& b) -> f32x4 {
        return mfma32(a.a1, b.a1, mfma32(a.a0, b.a0, z4)) + mfma16(a.a2, b.a2, z4);
    };
    auto rows80 = [&](unsigned a16, unsigned a8) -> Frag80 { const Frag80 f{lds16(a16), lds16(a16 + 64), lds8(a8)}; __builtin_amdgcn_sched_barrier(0); return f; };
    auto empty35 = [&](int layer, int part, int lane) {
        const unsigned t = WS_H35 + (unsigned)(((gj * 20 + 10 * layer + 8 * part + (lane >> 3)) * 32 + gi) * 128 + (lane & 7) * 16);
        st16m(ws, (part == 0 || lane < 16) ? t : WS_OWN + (unsigned)((gi * 8 + gj) * 2048 + layer * 512 + (lane - 16) * 16), (u32x4){~0u, ~0u, ~0u, ~0u}, part ? 0xffffffffffffull : ~0ull);
    };
    auto empty4 = [&](int layer, int part, int lane) {
        st16m(ws, WS_H4 + (unsigned)((layer * 32 + gi) * 16384) + (unsigned)((part == 0 || lane < 32) ? (part * 2 + (lane >> 5)) * 4096 + gj * 512 + (lane & 31) * 16
                                                                                           : 12288 + ((lane - 32) >> 3) * 1024 + gj * 128 + (lane & 7) * 16), (u32x4){~0u, ~0u, ~0u, ~0u},
              layer && part ? 0xff000000ffffffffull : ~0ull);
    };
    const unsigned tst = now();
    const int lead = FREEW(wave) ? LEAD * PACE * 4 : 0;
    auto pace = [&](unsigned n, bool wait = true) {
        unsigned u, w, t0;
        asm volatile("s_mov_b64 exec, 1\n\tds_add_rtn_u32 %0, %3, %4\n\tds_max_u32 %3, %5 offset:12\n\tds_add_rtn_u32 %1, %3, %6 offset:12\n\tds_read_b32 %2, %3 offset:4\n\ts_mov_b64 exec, -1\n\ts_waitcnt lgkmcnt(0)"
                     : "=&v"(u), "=&v"(w), "=&v"(t0) : "v"(lb + L_CNT + 60), "v"(n), "v"((now() - tst) << 6), "v"(n * PMIN * 4u) : "memory");
        int tn = (int)(__builtin_amdgcn_readfirstlane(t0) - tst + PINIT) * 64 + __builtin_amdgcn_readfirstlane(u) * (PACE * 4);
        const int tw = __builtin_amdgcn_readfirstlane(w) - PBURST * 64;
        if (tw > tn) tn = tw;
        tn -= lead;
        if (wait) {
#pragma unroll 1
            for (int k = 0; k < 2048 && (int)((now() - tst) << 6) < tn; ++k) {}
        }
    };
    auto slot = [&](unsigned u) {
        const unsigned tn = lds_word(lb + L_CNT + 64) + PINIT + u * PACE / 16;
#pragma unroll 1
        for (int k = 0; k < 8192 && (int)(now() - tn) < 0; ++k) __builtin_amdgcn_s_sleep(1);
    };
    auto load_wo = [&](int k, int lane) {
        const int c = k * 64 + lane, pp = c / 10, cc = c - 10 * pp;
        const unsigned f = (unsigned)(swz(pp) * 1280 + cc * 16);
        const u16* wb = wo + (size_t)gi * DH * DM + gj * 80;
#pragma unroll
        for (int a2 = 0; a2 < 6; ++a2) { if (a2 % 3 == 0) slot(NUNIT - WOFF + (2 * k + a2 / 3) * WOC); dma16(wb + 32 * a2 * DM, f, lb + L_WO + (5 * a2 + k) * 1024); }
    };
    auto load_wd = [&](int lane) {
        const u16* db = wd + gi * 48 * DM + gj * 80;
#pragma unroll 1
        for (int k = 0; k < 5; ++k) {
            const int c = k * 64 + lane, pp = c / 10, cc = c - 10 * pp;
            const unsigned fd = (unsigned)(swz(pp) * 1280 + cc * 16);
            if (!(k & 1)) pace(3);
            dma16(db, fd, lb + L_WD + k * 1024);
            if (k < 3) dma16(db + 32 * DM, (k < 2 || lane < 32) ? fd : 0u, lb + L_WD + (5 + k) * 1024);
        }
    };
    auto load_gu = [&](int m, int lane) {
#pragma unroll
        for (int k = 0; k < 3; ++k) {
            const int c = k * 64 + lane, row = c / 6, cc = c - 6 * row;
            const unsigned o = (unsigned)((swz(row) * FF + cc * 8) * 2), d = lb + L_GU + m * 8192 + k * 1024;
            const u16* wb = (m == 0 ? wg : wu) + gj * 80 * FF + gi * 48;
            pace(3);
            dma16(wb, o, d); dma16(wb + 32 * FF, o, d + 3072);
            if (k < 2) dma16(wb + 64 * FF, (k == 0 || lane < 32) ? o : 0u, d + 6144);
        }
    };
    auto lds_add = [&](int off, unsigned v) { asm volatile("s_mov_b64 exec, 1\n\tds_add_u32 %0, %1\n\ts_mov_b64 exec, -1" :: "v"(lb + L_CNT + off), "v"(v) : "memory"); };
    auto send_flag = [&](int lane) {
        st4_sc1(ws, WS_MB + (unsigned)((gi * 8 + (lane & 7)) * 128 + gj * 4), 1u);
    };
    auto tiles4 = [&](const Frag80 (&f)[4], const Frag80& b, unsigned long long mk) __attribute__((always_inline)) -> u32x4 {
        f32x4 c[4], d[4];
#pragma unroll
        for (int i = 0; i < 4; ++i) c[i] = mfma32(f[i].a0, b.a0, z4);
#pragma unroll
        for (int i = 0; i < 4; ++i) { c[i] = mfma32(f[i].a1, b.a1, c[i]); d[i] = mfma16(f[i].a2, b.a2, z4); }
        __builtin_amdgcn_sched_barrier(0);
        f32x2 lo, hi;
        asm volatile("s_nop 1\n\ts_mov_b64 exec, %18\n\tv_pk_add_f32 %0, %2, %10\n\tv_pk_add_f32 %1, %3, %11\n\t"
                     "s_lshl_b64 exec, exec, 4\n\tv_pk_add_f32 %0, %4, %12\n\tv_pk_add_f32 %1, %5, %13\n\t"
                     "s_lshl_b64 exec, exec, 4\n\tv_pk_add_f32 %0, %6, %14\n\tv_pk_add_f32 %1, %7, %15\n\t"
                     "s_lshl_b64 exec, exec, 4\n\tv_pk_add_f32 %0, %8, %16\n\tv_pk_add_f32 %1, %9, %17\n\ts_mov_b64 exec, -1"
                     : "=&v"(lo), "=&v"(hi) : "v"(LO2(c[0])), "v"(HI2(c[0])), "v"(LO2(c[1])), "v"(HI2(c[1])), "v"(LO2(c[2])), "v"(HI2(c[2])), "v"(LO2(c[3])), "v"(HI2(c[3])),
                       "v"(LO2(d[0])), "v"(HI2(d[0])), "v"(LO2(d[1])), "v"(HI2(d[1])), "v"(LO2(d[2])), "v"(HI2(d[2])), "v"(LO2(d[3])), "v"(HI2(d[3])), "s"(mk) : "scc");
        return asu(__builtin_shufflevector(lo, hi, 0, 1, 2, 3));
    };
    auto empty1 = [&](int half, int lane) {
        const unsigned o = WS_H1 + (unsigned)(((gi * 4 + 2 * half) * 8 + gj) * 896 + (lane < 56 ? lane : 55) * 16);
        st16_sc1(ws, o, (u32x4){~0u, ~0u, ~0u, ~0u}); st16_sc1(ws, o + 7168, (u32x4){~0u, ~0u, ~0u, ~0u});
    };
    auto answer = [&](int lane) {
        st4_sc1(ws, WS_MB + (unsigned)(((lane & 31) * 8 + (lane & 7)) * 128 + 32), (tst + TOFF) | 1u);
    };
    auto adopt = [&](unsigned t) {
        if (ADOPT && t - tst + 256u < 512u) lds_set(lb + L_CNT + 64, t);
        lds_set(lb + L_CNT + 52, 1u);
    };
    struct StatAddr { unsigned a16, b16, a8, b8, dst; };
    auto stats_addr = [&](int lane, unsigned dbase, int dstride) -> StatAddr {
        const int kq = lane >> 4, q4 = (lane >> 2) & 3, p4 = lane & 3;
        const int x1 = LANES(0x4444444444444444ull) ? L_WR + gj * 160 : L_XS + q4 * 160, xa = LANES(0xaaaaaaaaaaaaaaaaull) ? L_XS + 640 : x1;
        StatAddr r{lb + (unsigned)(xa + kq * 16), lb + (unsigned)(L_XS + p4 * 160 + kq * 16), lb + (unsigned)(xa + 128 + kq * 8), lb + (unsigned)(L_XS + p4 * 160 + 128 + kq * 8), dbase + (unsigned)(kq * dstride)};
        PIN3(r.a16, r.b16, r.dst); PIN2(r.a8, r.b8);
        return r;
    };
    auto stats = [&](const StatAddr& r) {
        const bf16x8 a0 = lds16(r.a16), b0 = lds16(r.b16), a1 = lds16(r.a16 + 64), b1 = lds16(r.b16 + 64);
        const bf16x4 a2 = lds8(r.a8), b2 = lds8(r.b8);
        unsigned long long mk = 0x0008000400020001ull; PINS(mk);
        __builtin_amdgcn_sched_barrier(0);
        st16m(ws, r.dst, asu(mfma32(a1, b1, mfma32(a0, b0, z4)) + mfma16(a2, b2, z4)), mk);
    };
    unsigned qa, pw, ma, idx, qm;
    auto qk_addr = [&](bool L1) {
        LANE_VARS();
        qa = lb + L_Q + (unsigned)(kq * 32 + (lane & 2) * 8);
        pw = lb + L_P + (unsigned)((7 + 8 * wave) * 192 + (kq >> 1) * 64 + (n >> 3) * 32 + (lane & 2) * 8 + (q4 & 1) * 8 + (kq & 1) * 4 + (lane & 1) * 2);
        ma = lb + L_M + (unsigned)(wave * 4);
        idx = (lane & 2) ? 0xeeeeu : 0x4444u;
        qm = lb + L_Q;
        PIN3(qa, pw, ma); PIN2(idx, qm);
        if (L1 && wave < 4) __builtin_amdgcn_s_setprio(2);
    };
    auto att_qk0 = [&]() {
        qk_addr(false);
#pragma unroll 1
        for (unsigned spins = 0; spins < (kSpinCap << 4); ++spins) {
            if (lds_word(lb + L_CNT + 4) != 0u) break;
            __builtin_amdgcn_s_sleep(1);
        }
        STAMP(3);
        const u32x4 q0 = *(const LAS u32x4*)(size_t)qa, q1 = *(const LAS u32x4*)(size_t)(qa + 128), q2 = *(const LAS u32x4*)(size_t)(qa + 256);
        f32x4 c0 = z4, c1 = z4, c2 = z4, c3 = z4;
#define SMF(c, q, r) "v_smfmac_f32_16x16x64_bf16 %" #c ", %" #q ", v[" #r "], %7\n\t"
        asm volatile("s_nop 1\n\t" SMF(0, 4, 64:71) SMF(1, 4, 88:95) SMF(2, 4, 112:119) SMF(3, 4, 136:143) SMF(0, 5, 72:79) SMF(1, 5, 96:103) SMF(2, 5, 120:127) SMF(3, 5, 144:151)
                     SMF(0, 6, 80:87) SMF(1, 6, 104:111) SMF(2, 6, 128:135) SMF(3, 6, 152:159) "s_nop 7"
                     : "+v"(c0), "+v"(c1), "+v"(c2), "+v"(c3) : "v"(q0), "v"(q1), "v"(q2), "v"(idx));
#undef SMF
        const float s0 = c0[0] + c0[2], s1 = c1[0] + c1[2], s2 = c2[0] + c2[2], s3 = c3[0] + c3[2];
        float m = fmax2(fmax3(s0, s1, s2), s3);
        m = fmax2(m, dpp<0xB1>(m)); m = fmax2(m, dpp<0x4E>(m)); m = fmax2(m, dpp<0x124>(m)); m = fmax2(m, dpp<0x128>(m));
        const float s01 = LANES(0xffff0000ffff0000ull) ? s1 : s0, s23 = LANES(0xffff0000ffff0000ull) ? s3 : s2;
        const float sv = LANES(0xffffffff00000000ull) ? s23 : s01;
        m = bfr(bfr(m) * SCALE);
        const float p = ex2((bfr(bfr(sv) * SCALE) - m) * LOG2E);
        *(LAS u16*)(size_t)pw = (u16)pk2(p, p);
        *(LAS float*)(size_t)ma = m;
        __builtin_amdgcn_sched_barrier(0);
    };
    unsigned pr, oa, oc, cma, cxa, cra, cso;
    auto att_addr = [&]() {
        LANE_VARS();
        pr = lb + L_P + (unsigned)((7 + 8 * wave - 2 * q4 - (lane & 1)) * 192 + kq * 32 + (lane & 2) * 8);
        oa = lb + L_OP + (unsigned)(wave * 1024 + (lane & 7) * 64 + kq * 16 + (n >> 3) * 8);
        oc = lb + L_OP + (unsigned)(wave * 1024 + 512 + (kq >> 1) * 256 + (lane & 7) * 32 + (kq & 1) * 16 + (n >> 3) * 8);
        PIN3(pr, oa, oc);
    };
    auto comb_out = [&](float u0, float u1, unsigned so, unsigned long long mk) __attribute__((always_inline)) {
        const unsigned w = min(pk2(u0, u1), 0xfffffffeu);
        const u32x4 w4 = {w, dppu<0x55>(w), dppu<0xAA>(w), dppu<0xFF>(w)};
        st16m(ws, so, w4, mk);
    };
    auto max8 = [&](f32x4 m0, f32x4 m1) __attribute__((always_inline)) { return fmax3(fmax3(m0[0], m0[1], m0[2]), fmax3(m0[3], m1[0], m1[1]), fmax2(m1[2], m1[3])) * LOG2E; };
    auto comb_o0 = [&](f32x4 m0, f32x4 m1, f32x4 mm, f32x4 o0, f32x4 o1, f32x4 o2, f32x4 o3, unsigned so, unsigned long long mk) __attribute__((always_inline)) {
        const float M = max8(m0, m1);
        f32x4 f;
#pragma unroll
        for (int k = 0; k < 4; ++k) f[k] = ex2(__builtin_fmaf(mm[k], LOG2E, -M));
        const f32x4 o = f[3] * o3 + (f[2] * o2 + (f[1] * o1 + f[0] * o0));
        comb_out(rs32(o[0], o[2]), rs32(o[1], o[3]), so, mk);
    };
    auto comb_o1 = [&](f32x4 m0, f32x4 m1, f32x2 mm, f32x4 o0, f32x4 o1, unsigned so, unsigned long long mk) __attribute__((always_inline)) {
        const float M = max8(m0, m1);
        const f32x4 o = ex2(__builtin_fmaf(mm[1], LOG2E, -M)) * o1 + ex2(__builtin_fmaf(mm[0], LOG2E, -M)) * o0;
        comb_out(x16_add(rs32(o[0], o[2])), x16_add(rs32(o[1], o[3])), so, mk);
    };
    auto ml_addr = [&](int lane, unsigned& xa, unsigned& ra) {
        xa = lb + L_M + (unsigned)(((lane & 15) >> 1) * 4); ra = lb + L_P + (unsigned)((7 + 8 * ((lane & 15) >> 1)) * 192 + (lane & 1) * 64 + (lane >> 4) * 16);
    };
    auto comb_ml = [&](unsigned ma, unsigned xa, unsigned ra, unsigned so) __attribute__((always_inline)) {
        const u32x4 x4 = *(const LAS u32x4*)(size_t)ra;
        const f32x4 m0 = *(const LAS f32x4*)(size_t)ma, m1 = *(const LAS f32x4*)(size_t)(ma + 16);
        const float mw = *(const LAS float*)(size_t)xa;
        __builtin_amdgcn_sched_barrier(0);
        const f32x4 sp = mfma32u((u32x4){0x3f803f80u, 0x3f803f80u, 0x3f803f80u, 0x3f803f80u}, x4, z4);
        __builtin_amdgcn_sched_barrier(0);
        const float M = max8(m0, m1);
        const float L = row16_sum(sp[0] * ex2(__builtin_fmaf(mw, LOG2E, -M)));
        st16m(ws, so, asu((f32x4){M, L, M, L}), 1ull);
    };
    auto att_pv0 = [&]() {
        u32x4 bm[8];
        trans_v(IC<0>{}, IC<24>{}); trans_v(IC<1>{}, IC<16>{});
#pragma unroll
        for (int e = 0; e < 8; ++e) bm[e] = *(const LAS u32x4*)(size_t)(pr + e * 192);
        __builtin_amdgcn_sched_barrier(0);
        f32x4 da = z4, dc = z4;
        PIN2(da, dc);
        asm volatile("s_nop 1");
        __builtin_amdgcn_sched_barrier(0);
        f32x2 r;
        smf<RV>(da, bm[0], idx); smf<RV + 8>(da, bm[1], idx); smf<RV + 16>(da, bm[2], idx); smf<RV + 24>(da, bm[3], idx);
        smf<RV + 32>(da, bm[4], idx); smf<RV + 40>(da, bm[5], idx); smf<RV + 48>(da, bm[6], idx); smf<RV + 56>(da, bm[7], idx);
        asm volatile("s_nop 7\n\tv_pk_add_f32 %0, %1, %2\n\tds_write_b64 %3, %0\n\ts_mov_b64 exec, 1\n\tds_add_u32 %4, %5\n\ts_mov_b64 exec, -1"
                     : "=&v"(r) : "v"(LO2(da)), "v"(HI2(da)), "v"(oa), "v"(lb + L_CNT + 24), "v"(1u) : "memory");
        trans_w(IC<0>{}, IC<12>{}); trans_w(IC<1>{}, IC<8>{}); trans_w(IC<2>{}, IC<4>{}); trans_w(IC<3>{}, IC<0>{});
        STAMP(4);
        asm volatile("s_nop 1\n\tv_smfmac_f32_16x16x64_bf16 %0, %1, v[224:231], %5\n\tv_smfmac_f32_16x16x64_bf16 %0, %2, v[232:239], %5\n\t"
                     "v_smfmac_f32_16x16x64_bf16 %0, %3, v[240:247], %5\n\tv_smfmac_f32_16x16x64_bf16 %0, %4, v[248:255], %5\n\ts_nop 6"
                     : "+v"(dc) : "v"(bm[0]), "v"(bm[1]), "v"(bm[2]), "v"(bm[3]), "v"(idx));
        asm volatile("s_nop 0\n\tv_pk_add_f32 %0, %1, %2\n\tds_write_b64 %3, %0" : "=&v"(r) : "v"(LO2(dc)), "v"(HI2(dc)), "v"(oc) : "memory");
        STAMP(20);
    };
    unsigned long long ck1 = 0xffff0000ffff0000ull, ck2 = 0xffffffff00000000ull, csc = 0x3d93cd3a3d93cd3aull;
    auto att1 = [&]() {
        constexpr int layer = 1;
        {
#define SMF(c, q, r) "v_smfmac_f32_16x16x64_bf16 v[" #c "], v[" #q "], v[" #r "], %0\n\t"
#define MAXD(x) "v_max_f32_dpp v41, v41, v41 " x " row_mask:0xf bank_mask:0xf\n\t"
            asm volatile("s_waitcnt lgkmcnt(0)\n\ts_barrier\n\t"
                         "ds_read_b128 v[0:3], %1\n\tds_read2_b32 v[12:13], %2 offset0:96 offset1:100\n\tds_read_b128 v[4:7], %1 offset:128\n\tds_read_b128 v[8:11], %1 offset:256\n\t"
                         "v_mov_b64 v[16:17], 0\n\tv_mov_b64 v[18:19], 0\n\tv_mov_b64 v[20:21], 0\n\tv_mov_b64 v[22:23], 0\n\tv_mov_b64 v[24:25], 0\n\tv_mov_b64 v[26:27], 0\n\t"
                         "v_mov_b64 v[28:29], 0\n\tv_mov_b64 v[30:31], 0\n\tv_mov_b64 v[32:33], 0\n\tv_mov_b64 v[34:35], 0\n\tv_mov_b64 v[36:37], 0\n\tv_mov_b64 v[38:39], 0\n\t"
                         "s_waitcnt lgkmcnt(3)\n\t" SMF(16:19, 0:3, 64:71) SMF(20:23, 0:3, 88:95) "s_waitcnt lgkmcnt(1)\n\tv_mul_f32 v13, 0x3acccccd, v13\n\t"
                         SMF(24:27, 0:3, 112:119) "v_mul_f32 v12, 0x3acccccd, v12\n\t" SMF(28:31, 0:3, 136:143) "v_fma_f32 v12, -v13, v13, v12\n\t"
                         SMF(16:19, 4:7, 72:79) "v_maximum3_f32 v12, v12, 0, 0\n\t" SMF(20:23, 4:7, 96:103) "v_cvt_pk_bf16_f32 v12, 0, v12\n\t"
                         SMF(24:27, 4:7, 120:127) "v_add_f32 v12, 0x3727c5ac, v12\n\t" SMF(28:31, 4:7, 144:151) "v_cvt_pk_bf16_f32 v12, 0, v12\n\ts_waitcnt lgkmcnt(0)\n\t"
                         SMF(16:19, 8:11, 80:87) "v_sqrt_f32 v12, v12\n\t" SMF(20:23, 8:11, 104:111) "v_cvt_pk_bf16_f32 v12, 0, v12\n\t"
                         SMF(24:27, 8:11, 128:135) "v_rcp_f32 v12, v12\n\t" SMF(28:31, 8:11, 152:159)
                         "s_nop 2\n\tv_add_f32 v42, v16, v18\n\ts_nop 0\n\tv_add_f32 v43, v20, v22\n\tv_max_f32 v41, v42, v43\n\tv_add_f32 v44, v24, v26\n\tv_add_f32 v45, v28, v30\n\t"
                         "v_max3_f32 v41, v41, v44, v45\n\tv_cndmask_b32 v42, v42, v43, %8\n\tv_cndmask_b32 v44, v44, v45, %8\n\t"
                         MAXD("quad_perm:[1,0,3,2]") "v_cndmask_b32 v40, v42, v44, %9\n\ts_nop 0\n\t" MAXD("quad_perm:[2,3,0,1]") "s_nop 1\n\t" MAXD("row_ror:4") "s_nop 1\n\t" MAXD("row_ror:8")
                         "v_pk_mul_f32 v[40:41], v[12:13], v[40:41] op_sel_hi:[0,1]\n\tv_cvt_pk_bf16_f32 v40, 0, v40\n\tv_cvt_pk_bf16_f32 v41, 0, v41\n\t"
                         "v_pk_mul_f32 v[40:41], %10, v[40:41]\n\tv_cvt_pk_bf16_f32 v40, 0, v40\n\tv_cvt_pk_bf16_f32 v41, 0, v41\n\t"
                         "v_sub_f32 v40, v40, v41\n\tv_mul_f32 v40, 0x3fb8aa3b, v40\n\tv_exp_f32 v40, v40\n\ts_nop 0\n\tv_cvt_pk_bf16_f32 v40, v40, v40\n\tds_write_b16 %3, v40\n\t"
                         "ds_read_b128 v[0:3], %4\n\tds_read_b128 v[4:7], %4 offset:192\n\tds_read_b128 v[8:11], %4 offset:384\n\tds_read_b128 v[12:15], %4 offset:576\n\t"
                         "ds_read_b128 v[16:19], %4 offset:768\n\tds_read_b128 v[20:23], %4 offset:960\n\tds_read_b128 v[24:27], %4 offset:1152\n\tds_read_b128 v[28:31], %4 offset:1344\n\t"
                         "ds_write_b32 %5, v41\n\t"
                         "s_waitcnt lgkmcnt(8)\n\t" SMF(32:35, 0:3, 160:167) SMF(36:39, 0:3, 224:231) "s_waitcnt lgkmcnt(7)\n\t" SMF(32:35, 4:7, 168:175) SMF(36:39, 4:7, 232:239)
                         "s_waitcnt lgkmcnt(6)\n\t" SMF(32:35, 8:11, 176:183) SMF(36:39, 8:11, 240:247) "s_waitcnt lgkmcnt(4)\n\t" SMF(32:35, 12:15, 184:191) SMF(36:39, 12:15, 248:255)
                         SMF(32:35, 16:19, 192:199) "s_waitcnt lgkmcnt(1)\n\t" SMF(32:35, 20:23, 200:207) SMF(32:35, 24:27, 208:215) SMF(32:35, 28:31, 216:223)
                         "s_nop 2\n\tv_pk_add_f32 v[36:37], v[36:37], v[38:39]\n\tds_write_b64 %7, v[36:37]\n\ts_nop 2\n\tv_pk_add_f32 v[32:33], v[32:33], v[34:35]\n\tds_write_b64 %6, v[32:33]"
                         :: "v"(idx), "v"(qa), "v"(qm), "v"(pw), "v"(pr), "v"(ma), "v"(oa), "v"(oc), "s"(ck1), "s"(ck2), "s"(csc)
                         : "memory", "v0", "v1", "v2", "v3", "v4", "v5", "v6", "v7", "v8", "v9", "v10", "v11", "v12", "v13", "v14", "v15", "v16", "v17", "v18", "v19", "v20", "v21", "v22", "v23",
                           "v24", "v25", "v26", "v27", "v28", "v29", "v30", "v31", "v32", "v33", "v34", "v35", "v36", "v37", "v38", "v39", "v40", "v41", "v42", "v43", "v44", "v45");
#undef SMF
#undef MAXD
            STAMP(20 + 32 * layer);
            if (__builtin_expect(wave >= 3, 1)) BAR();
            else {
                __builtin_amdgcn_s_setprio(0);
                if (wave == 0) {
                    unsigned long long mk = 0x1111111111111111ull; PINS(mk);
                    BAR();
                    STAMP(5 + 32 * layer);
                    comb_o0(*(const LAS f32x4*)(size_t)cma, *(const LAS f32x4*)(size_t)(cma + 16), *(const LAS f32x4*)(size_t)cxa, *(const LAS f32x4*)(size_t)cra,
                            *(const LAS f32x4*)(size_t)(cra + 1024), *(const LAS f32x4*)(size_t)(cra + 2048), *(const LAS f32x4*)(size_t)(cra + 3072), cso, mk);
                } else if (wave == 1) {
                    unsigned long long mk = 0x0000111100001111ull; PINS(mk);
                    BAR();
                    comb_o1(*(const LAS f32x4*)(size_t)cma, *(const LAS f32x4*)(size_t)(cma + 16), *(const LAS f32x2*)(size_t)cxa, *(const LAS f32x4*)(size_t)cra, *(const LAS f32x4*)(size_t)(cra + 1024), cso, mk);
                } else {
                    BAR();
                    comb_ml(cma, cxa, cra, cso);
                }
                STAMP(21 + 32 * layer);
            }
        }
    };

    unsigned nj = 0u; int ord;
    {
        const int tid = threadIdx.x, lane = tid & 63;
        STAMP(0);
        unsigned pf = 0u, mb = 0u, tfl = 0u;
        const u16* kbase = kc + ((size_t)((gj >> 1) * HQ + gi) * SK + (SK - WIN) + (gj & 1) * 512) * DH;
        const u16* vbase = vc + ((size_t)((gj >> 1) * HQ + gi) * SK + (SK - WIN) + (gj & 1) * 512) * DH;
        if (wave == 7) {
            __builtin_amdgcn_s_setprio(3);
            for (int i = 0; i < 5; ++i) dma16p(x0, (unsigned)(i * 1024 + lane * 16), lb + L_X0 + i * 1024);
            pf = code_pf((g >> 3) % 10, lane);
            if (lane < 20) *reinterpret_cast<unsigned*>(smem + L_CNT + lane * 4) = lane == 16 ? tst : 0u;
            BAR();
            if (lane < 10) *reinterpret_cast<u32x4*>(smem + L_XS + 640 + lane * 16) = (u32x4){0x3f803f80u, 0x3f803f80u, 0x3f803f80u, 0x3f803f80u};
            if (lane < 16) { *reinterpret_cast<u32x4*>(smem + L_CS + 768 + lane * 16) = (u32x4){0u, 0u, 0u, 0u}; }
        } else {
            if (wave >= 4) {
                if (wave < 6) {
                    empty1(wave - 4, lane);
                    empty35(0, wave - 4, lane);
                } else {
                    __builtin_amdgcn_s_setprio(3);
                    if (gi + gj == 0) st4_sc1(ws, WS_R + (lane & 31) * 4, 0u);
                    st4_sc1(ws, WS_MB + (unsigned)((gi * 8 + gj) * 128 + (lane & 31) * 4), 0u);
                    empty4(0, 0, lane); empty4(0, 1, lane);
                }
            }
#pragma unroll 1
            for (int i = wave; i < 35; i += 7) {
                if (i >= 33) dma16p(wr, (unsigned)((i == 33 || lane < 16) ? (i - 33) * 1024 + lane * 16 : 0), lb + L_WR + (i - 33) * 1024);
                else {
                    const int c = i * 64 + lane; int row = c / 26, cc = c - 26 * row; if (cc > 23) cc = 23; if (row > 79) row = 79;
                    dma16(wq, (unsigned)(((gj * 80 + swz(row)) * QD + gi * DH + cc * 8) * 2), lb + L_WQ + i * 1024);
                }
            }
            BAR();
            *reinterpret_cast<u32x4*>(smem + L_P + tid * 16) = (u32x4){0u, 0u, 0u, 0u};
            if (tid < 72 * 12 - 448) *reinterpret_cast<u32x4*>(smem + L_P + 7168 + tid * 16) = (u32x4){0u, 0u, 0u, 0u};
            if (wave >= 4) {
                VMW(5);
                lds_add(32, 1u);
                if (wave == 6) {
#pragma unroll 1
                    for (unsigned spins = 0; spins < (kSpinCap << 4); ++spins) if (lds_word(lb + L_CNT + 32) >= 3u) break;
                    send_flag(lane);
                    tfl = now();
                    STAMP(17);
                    asm volatile("s_waitcnt vmcnt(1) lgkmcnt(0)" ::: "memory");
                } else asm volatile("s_waitcnt vmcnt(0) lgkmcnt(0)" ::: "memory");
                lds_add(0, 1u);
                if (wave == 6) {
                    until(__builtin_amdgcn_readfirstlane(tfl) + DMB);
#pragma unroll 1
                    for (unsigned spins = 0; spins < kSpinCap; ++spins) {
                        unsigned r = ld4_sc1(ws, WS_MB + (unsigned)((gi * 8 + gj) * 128 + (lane & 7) * 4));
                        WAIT1(r, 0);
                        if (none(r ^ 1u)) break;
                        if ((spins & 7u) == 7u) send_flag(lane);
                    }
                    lds_set(lb + L_CNT + 8, 1u);
                    if (gj == (gi & 7)) st4_sc1(ws, WS_R + (unsigned)(gi * 4), 1u);
                    STAMP(31);
                }
            }
        }
        u32x4 a[8];
        unsigned ko = (unsigned)((wave * 64 + (lane & 7)) * 384 + (((lane >> 3) & 1) * 4 + (lane >> 4)) * 16);
        PIN1(ko);
        auto k_group = [&](auto GC_) __attribute__((always_inline)) {
            constexpr int G = decltype(GC_)::value, R = RK + 24 * (G >> 1) + 4 * (G & 1);
            const unsigned o = ko + G * 3072;
            if (PRIO && G == 0) __builtin_amdgcn_s_setprio(3);
            if (PRIO && G == 4 && wave < 6) __builtin_amdgcn_s_setprio(2);
            pace(3);
            ld16_nt<R, 0>(kbase, o); ld16_nt<R + 8, 128>(kbase, o); ld16_nt<R + 16, 256>(kbase, o);
            const int lane = lane_id();
            const unsigned ho = WS_H1 + (unsigned)((gi * 4 + (gj >> 1)) * 7168 + 3584 + (lane < 48 ? lane : 0) * 16);
            if (wave < 7) {
                asm volatile("; COND_BEGIN");
                if (G == GL && wave < 4) {
                    asm volatile("s_waitcnt vmcnt(%0) lgkmcnt(0)" :: "i"(3 * GL + 3) : "memory");
                    lds_add(0, 1u);
                    if (wave < 2 && (EARLY >> wave & 1)) load_gu(wave, lane);
                    if (wave == 2 && (EARLY & 4)) load_wd(lane);
                }
                if (wave == 6) {
                    if (gi + gj == 0) {
                        if (G == GE) mb = ld4_sc1(ws, WS_R + (unsigned)((lane & 31) * 4));
                        if (G == GF) {
                            WAIT1(mb, 3 * (GF - GE));
                            if (none(mb ^ 1u)) { answer(lane); lds_set(lb + L_CNT + 36, 1u); }
                        }
                    }
                    if (G == 7) mb = ld4_sc1(ws, WS_MB + (unsigned)((gi * 8 + (gi & 7)) * 128 + 32));
                }
                asm volatile("; COND_END");
            }
            if (wave == 7) {
                asm volatile("; COND_BEGIN");
                if (G == GP) {
                    VMW(3 * (GP + 1) + 1);
                    const unsigned char* p = smem + L_X0 + (lane >> 4) * 1280;
                    const float c0 = lo16(*reinterpret_cast<const u16*>(p));
                    const f32x2 c2 = {c0, c0};
                    f32x2 s2 = {0.f, 0.f}, q2 = s2;
#pragma unroll
                    for (int k = 0; k < 5; ++k) {
                        const u32x4 w = *reinterpret_cast<const u32x4*>(p + k * 256 + (lane & 15) * 16);
#pragma unroll
                        for (int e = 0; e < 4; ++e) { const f32x2 d = unpk(w[e]) - c2; s2 += d; q2 += d * d; }
                    }
                    const float s = row16_sum(s2[0] + s2[1]), qq = row16_sum(q2[0] + q2[1]);
                    const float ms = s * (1.f / DM);
                    const unsigned mv = pk2(c0 + ms, fmax2(qq - s * ms, 0.f) * (1.f / DM));
                    const float mb = lo16(mv), rden = frcp(bfr(__builtin_amdgcn_sqrtf(bfr(hi16(mv) + 1e-5f))));
                    const f32x2 mb2 = {mb, mb}, rd2 = {rden, rden};
                    if ((lane & 15) < 10) {
                        u32x4 w = *reinterpret_cast<const u32x4*>(p + gj * 160 + (lane & 15) * 16);
#pragma unroll
                        for (int e = 0; e < 4; ++e) w[e] = pk2v(unpk(pk2v(unpk(w[e]) - mb2)) * rd2);
                        *reinterpret_cast<u32x4*>(smem + L_XS + (lane >> 4) * 160 + (lane & 15) * 16) = w;
                    }
                    STAMP(30);
#pragma unroll 1
                    for (unsigned spins = 0; spins < (kSpinCap << 4); ++spins) {
                        if (lds_word(lb + L_CNT) >= 7u) break;
                        __builtin_amdgcn_s_sleep(1);
                    }
                    STAMP(26);
                    {
#pragma unroll 1
                        for (int i6 = 0; i6 < 6; ++i6) {
                            LANE_VARS();
                            const int blk = i6 < 3 ? i6 : i6 - 3;
                            const unsigned xb = lb + L_XS + (unsigned)((i6 < 3 ? p4 : 4) * 160);
                            Frag80 f[4];
#pragma unroll
                            for (int i = 0; i < 4; ++i) f[i] = frag80(lb + L_WQ + (unsigned)(128 * blk + 32 * i + 8 * p4), 416, r0s, r1s, r2s);
                            const Frag80 b = rows80(xb + kq * 16, xb + 128 + kq * 8);
                            const unsigned so = (i6 < 3 ? WS_H1 + (unsigned)(((gi * 4 + p4) * 8 + gj) * 896) : WS_OWN + (unsigned)((gi * 8 + gj) * 2048 + 1024)) + (unsigned)(256 * blk + q4 * 64 + kq * 16);
                            st16m(ws, so, tiles4(f, b, 0x000f000f000f000full), i6 < 3 ? ~0ull : 0x1111111111111111ull);
                            if (i6 == 2) { tfl = now(); STAMP(2); }
                        }
                    }
                    f32x2 r2 = {0.f, 0.f};
#pragma unroll
                    for (int k = 0; k < 5; ++k) r2 += unpk(*reinterpret_cast<const unsigned*>(smem + L_WR + k * 256 + lane_id() * 4));
                    const float sw = wave_sum(r2[0] + r2[1]);
                    *reinterpret_cast<float*>(smem + L_CNT + 12) = sw;
                    WAIT1(pf, 3 * (GP + 1) + 6);
                    pace(XCOST, false);
                }
                if (G == GC) {
#pragma unroll 1
                    for (unsigned spins = 0; spins < (kSpinCap << 8); ++spins) { if (lds_word(lb + L_CNT + 8)) break; __builtin_amdgcn_s_sleep(1); }
                    until(__builtin_amdgcn_readfirstlane(tfl) + DQ);
                    GATHER8(a, ho, 896);
                }
                if (G == GD) {
                    WAIT8(a, 3 * (GD - GC));
#pragma unroll 1
                    for (unsigned spins = 0; spins < kSpinCap && any_empty(UM32(a)); ++spins) { slip(0u); AGAIN8(a, ho, 896); WAIT8(a, 0); }
                    const f32x4 sq = SUM8(a);
                    unsigned* qp = reinterpret_cast<unsigned*>(smem + L_Q + (lane >> 2) * 32 + (lane & 3) * 4);
                    qp[0] = pk2(sq[0], sq[1]); qp[4] = pk2(sq[2], sq[3]);
                    asm volatile("s_waitcnt lgkmcnt(0)\n\tds_write_b32 %0, %1" :: "v"(lb + L_CNT + 4), "v"(1u) : "memory");
                    STAMP(27);
                }
                asm volatile("; COND_END");
            } else if (G == GC) { for (int k = 0; k < 8; ++k) asm volatile("" : "=v"(a[k])); }
        };
        k_group(IC<0>{}); k_group(IC<1>{}); k_group(IC<2>{}); k_group(IC<3>{}); k_group(IC<4>{}); k_group(IC<5>{}); k_group(IC<6>{}); k_group(IC<7>{});
        {
            const int lane = lane_id();
            const unsigned v0 = (unsigned)((wave * 64 + 4 * (lane >> 4)) * 384 + (lane & 15) * 16);
            const unsigned v1 = v0 + 256 - (unsigned)((lane & 15) * 8);
            auto v_group = [&](auto HC) __attribute__((always_inline)) {
                constexpr int h = decltype(HC)::value, R = h < 8 ? RV + 16 * (h & 3) + 4 * (h >> 2) : RV + 64 + 2 * (h - 8), NV = h < 8 ? 2 * h + 2 : 4 * h - 12;
                const unsigned o = (h < 8 ? v0 + (h >> 1) * 6144 : v1 + (h - 8) * 6144);
                if (PRIO && h == 0) __builtin_amdgcn_s_setprio(1);
                if (PRIO && h == 6) __builtin_amdgcn_s_setprio(0);
                pace(2);
                if (h < 8) { ld16_nt<R, (h & 1) * 768>(vbase, o); ld16_nt<R + 8, (h & 1) * 768 + 384>(vbase, o); }
                else { ld8_nt<R, 0>(vbase, o); ld8_nt<R + 8, 384>(vbase, o); ld8_nt<R + 16, 768>(vbase, o); ld8_nt<R + 24, 1152>(vbase, o); }
                if (h == 0 && wave == 6) {
                    asm volatile("; COND_BEGIN");
                    WAIT1(mb, 2);
                    const unsigned r = __builtin_amdgcn_readfirstlane(mb);
                    if (r) { adopt(r); STAMP(29); }
                    asm volatile("; COND_END");
                }
                if constexpr (h >= FX && h < FX + 6) {
                    constexpr int i = 2 * (h - FX);
                    fix_k1(IC<i>{}, IC<20 - 6 * (i / 3) - i % 3 + NV>{});
                    fix_k1(IC<i + 1>{}, IC<20 - 6 * ((i + 1) / 3) - (i + 1) % 3 + NV>{});
                }
                if (h == 1 && wave == 6 && gi + gj == 0) { asm volatile("; COND_BEGIN"); mb = ld4_sc1(ws, WS_R + (unsigned)((lane & 31) * 4)); asm volatile("; COND_END"); }
                if (h == 3 && wave == 6 && gi + gj == 0) {
                    asm volatile("; COND_BEGIN");
                    WAIT1(mb, 4);
                    if (none(mb ^ 1u) && !lds_word(lb + L_CNT + 36)) { answer(lane); lds_set(lb + L_CNT + 36, 1u); }
                    asm volatile("; COND_END");
                }
                if (h == FX + 5) {
                    STAMP(1);
                    if (!PRIO) __builtin_amdgcn_s_setprio(0);
                    att_qk0();
                }
            };
            v_group(IC<0>{}); v_group(IC<1>{}); v_group(IC<2>{}); v_group(IC<3>{}); v_group(IC<4>{}); v_group(IC<5>{});
            v_group(IC<6>{}); v_group(IC<7>{}); v_group(IC<8>{}); v_group(IC<9>{}); v_group(IC<10>{}); v_group(IC<11>{});
        }
    }
    {
        lds_add(16, 1u);
        STAMP(28);
        att_addr();
        const int lane = lane_id();
        cma = lb + L_M; cxa = lb + L_M + (unsigned)((lane >> 4) * 8); cra = lb + L_OP + (unsigned)(512 + (lane >> 4) * 2048 + (lane & 15) * 16);
        cso = WS_OWN + (unsigned)((gi * 8 + gj) * 2048 + 256 + (lane >> 5) * 64 + (lane & 15) * 4);
        PIN2(cma, cxa); PIN2(cra, cso);
        att_pv0();
    }
    {
        f32x4 m0, m1, o0, o1; f32x2 mm; unsigned r;
        asm volatile("s_mov_b64 exec, 1\n\tds_add_rtn_u32 %5, %6, %7 offset:%10\n\ts_mov_b64 exec, -1\n\t"
                     "ds_read_b128 %0, %6\n\tds_read_b128 %1, %6 offset:16\n\tds_read_b64 %2, %8\n\tds_read_b128 %3, %9\n\tds_read_b128 %4, %9 offset:1024\n\ts_waitcnt lgkmcnt(0)"
                     : "=&v"(m0), "=&v"(m1), "=&v"(mm), "=&v"(o0), "=&v"(o1), "=&v"(r)
                     : "v"(cma), "v"(1u), "v"(cxa), "v"(cra), "i"(L_CNT + 28 - L_M) : "memory");
        ord = __builtin_amdgcn_readfirstlane(r);
        if (ord == 7) { STAMP(5); comb_o1(m0, m1, mm, o0, o1, cso, 0x0000111100001111ull); lds_set(lb + L_CNT + 72, ~7u); lds_set(lb + L_CNT + 48, now() | 1u); STAMP(21); }
        const int lane = lane_id();
        auto wait_cnt = [&](int off, unsigned v) {
#pragma unroll 1
            for (unsigned spins = 0; spins < (kSpinCap << 4); ++spins) { if (lds_word(lb + L_CNT + off) >= v) break; __builtin_amdgcn_s_sleep(1); }
        };
        if (ord == 1) {
            wait_cnt(16, 8u);
            unsigned xa, ra; ml_addr(lane, xa, ra);
            comb_ml(cma, xa, ra, WS_OWN + (unsigned)((gi * 8 + gj) * 2048 + 384));
            STAMP(24);
        } else if (ord == 0 && !lds_word(lb + L_CNT + 52)) {
            if (gi + gj == 0 && !lds_word(lb + L_CNT + 36)) {
#pragma unroll 1
                for (unsigned spins = 0; spins < kSpinCap; ++spins) {
                    unsigned r = ld4_sc1(ws, WS_R + (unsigned)((lane & 31) * 4));
                    WAIT1(r, 0);
                    if (none(r ^ 1u)) break;
                    if ((spins & 7u) == 7u) send_flag(lane);
                }
                answer(lane);
            }
#pragma unroll 1
            for (unsigned spins = 0; spins < kSpinCap; ++spins) {
                unsigned r = ld4_sc1(ws, WS_MB + (unsigned)((gi * 8 + (gi & 7)) * 128 + 32));
                WAIT1(r, 0);
                r = __builtin_amdgcn_readfirstlane(r);
                if (r) { adopt(r); break; }
                if ((spins & 7u) == 7u) { send_flag(lane); if (gj == (gi & 7)) st4_sc1(ws, WS_R + (unsigned)(gi * 4), 1u); }
                __builtin_amdgcn_s_sleep(16);
            }
            STAMP(29);
        } else if (ord == 2) {
#pragma unroll 1
            for (unsigned spins = 0; spins < (kSpinCap << 6); ++spins) if (lds_word(lb + L_CNT + 24) >= 8u) break;
            const unsigned ra = lb + L_OP + (unsigned)((lane >> 5) * 4096 + (lane & 31) * 16);
            comb_o0(*(const LAS f32x4*)(size_t)cma, *(const LAS f32x4*)(size_t)(cma + 16), *(const LAS f32x4*)(size_t)(cma + (lane >> 5) * 16), *(const LAS f32x4*)(size_t)ra, *(const LAS f32x4*)(size_t)(ra + 1024),
                    *(const LAS f32x4*)(size_t)(ra + 2048), *(const LAS f32x4*)(size_t)(ra + 3072), WS_OWN + (unsigned)((gi * 8 + gj) * 2048 + lane * 4), 0x1111111111111111ull);
            STAMP(22);
        }
#pragma unroll 1
        for (;;) {
            unsigned j;
            asm volatile("s_mov_b64 exec, 1\n\tds_add_rtn_u32 %0, %1, %2\n\ts_mov_b64 exec, -1\n\ts_waitcnt lgkmcnt(0)" : "=&v"(j) : "v"(lb + L_CNT + 40), "v"(1u) : "memory");
            j = __builtin_amdgcn_readfirstlane(j);
            if (j >= 5u) break;
            load_wo((int)j, lane); ++nj;
        }
        STAMP(23);
    }

    auto chain = [&](auto LC) {
        constexpr int layer = decltype(LC)::value;
        const unsigned h4 = WS_H4 + (unsigned)((layer * 32 + gi) * 16384);
        if (wave < 3) {
            LANE_VARS();
            if (layer == 0) {
                VMW(0);
                lds_add(44, nj);
                const int c = wave * 64 + lane, row = c / 6, cc = c - 6 * row;
                unsigned o = (unsigned)((swz(row) * FF + cc * 8) * 2), o2 = (wave == 0 || lane < 32) ? o : 0u, d = lb + L_GU + wave * 1024;
                const u16* wb0 = wg + gj * 80 * FF + gi * 48;
                const u16* wb1 = wu + gj * 80 * FF + gi * 48;
                const u16* db = wd + gi * 48 * DM + gj * 80;
                const int pp = c / 10, c2 = c + 192, p2 = c2 / 10;
                unsigned fd = (unsigned)(swz(pp) * 1280 + (c - 10 * pp) * 16), f2 = (unsigned)(swz(p2) * 1280 + (c2 - 10 * p2) * 16), f3 = (wave < 2 || lane < 32) ? fd : 0u;
                PIN2(o, o2); PIN3(fd, f2, f3); PINS(wb0); PINS(wb1); PINS(d); PINS(db);
                auto late = [&](int m) {
                    if (m & 1) { dma16(wb0, o, d); dma16(wb0 + 32 * FF, o, d + 3072); if (wave < 2) dma16(wb0 + 64 * FF, o2, d + 6144); }
                    if (m & 2) { dma16(wb1, o, d + 8192); dma16(wb1 + 32 * FF, o, d + 8192 + 3072); if (wave < 2) dma16(wb1 + 64 * FF, o2, d + 8192 + 6144); }
                    if (m & 4) { dma16(db, fd, d + L_WD - L_GU); dma16(db + 32 * DM, f3, d + L_WD - L_GU + 5120); if (wave < 2) dma16(db, f2, d + L_WD - L_GU + 3072); }
                };
                if (GOPT) {
#pragma unroll 1
                    for (unsigned spins = 0; spins < (kSpinCap << 4); ++spins) if (lds_word(lb + L_CNT + 72) == ~3u) break;
                    late(GOPT);
                    STAMP(25);
                }
                BAR();
                late(GB4);
#pragma unroll 1
                for (unsigned spins = 0; spins < (kSpinCap << 4); ++spins) if (lds_word(lb + L_CNT + 76) >= 5u) break;
                late(GST);
                if (wave < 2) { empty1(wave, lane); empty35(1, wave, lane); VMW(3); } else { empty4(1, 0, lane); empty4(1, 1, lane); VMW(2); }
                if ((EARLY & 3) != 3) {
                    lds_add(20, 1u);
#pragma unroll 1
                    for (unsigned spins = 0; spins < (kSpinCap << 4); ++spins) if (lds_word(lb + L_CNT + 20) >= 3u) break;
                }
                STAMP(19);
            } else BAR();
            {
                const unsigned im = lb + L_GU + (unsigned)(32 * wave + 8 * p4);
                Frag80 fg = frag80(im, 96, r0s, r1s, r2s), fu = frag80(im + 8192, 96, r0s, r1s, r2s);
                const unsigned xb = lb + L_XS + (unsigned)((n < 4 ? n : 4) * 160);
                unsigned b16 = xb + kq * 16, b8 = xb + 128 + kq * 8;
                unsigned sg = h4 + (unsigned)((n < 4 ? wave * 4096 + gj * 512 + n * 64 : 12288 + wave * 1024 + gj * 128) + kq * 16), su = sg + (n < 4 ? 256u : 64u);
                unsigned o = h4 + (unsigned)(wave * 4096 + lane * 16);
                unsigned oc = h4 + (unsigned)(12288 + wave * 1024 + ((lane >> 5) * 4 + q4) * 128 + (kq & 1) * 64 + p4 * 16);
                unsigned os = h4 + (unsigned)(15360 + (p4 + 4 * (kq & 1)) * 128 + q4 * 16);
                unsigned ha = lb + L_H + (unsigned)(q4 * 96 + wave * 32 + p4 * 8 + kq * 2);
                f32x2 cs = {0.f, 0.f};
                if (layer != 0) cs = *reinterpret_cast<const f32x2*>(smem + L_CS2 + wave * 512 + lane * 8);
                PIN3(fg.a0, fg.a1, fg.a2); PIN3(fu.a0, fu.a1, fu.a2); PIN3(b16, b8, sg); PIN3(su, o, os); PIN2(ha, cs);
                unsigned long long mk = layer ? 0x000f000f000f000full : 0x001f001f001f001full;
                unsigned ta = WS_MB + (unsigned)((lane >> 1) * 1024 + 64 + (lane & 1) * 16);
                if (layer == 0) PIN2(oc, ta);
                PINS(mk);
                unsigned t3 = 0u;
                if (layer == 0) {
                    const u16* db = wd + gi * 48 * DM + gj * 80;
                    const int c = wave * 64 + lane, pp = c / 10, c2 = c + 192, p2 = c2 / 10;
                    const unsigned fd = (unsigned)(swz(pp) * 1280 + (c - 10 * pp) * 16), f2 = (unsigned)(swz(p2) * 1280 + (c2 - 10 * p2) * 16);
                    t3 = lds_word(lb + L_CNT + 64) + C3;
                    VMW(0);
                    if (!((EARLY | GOPT | GB4 | GST) & 4)) {
                        until(t3 + DWD);
                        dma16(db, fd, lb + L_WD + wave * 1024);
                        dma16(db + 32 * DM, (wave < 2 || lane < 32) ? fd : 0u, lb + L_WD + (5 + wave) * 1024);
                        if (wave < 2) dma16(db, f2, lb + L_WD + (3 + wave) * 1024);
                    }
                }
                BAR();
                STAMP(10 + 32 * layer);
                u32x4 tl = {0u, 0u, 0u, 0u};
                if (layer == 0) tl = ld16_sc1(ws, ta);
                const Frag80 b = rows80(b16, b8);
                const f32x4 g0 = mfma32(fg.a0, b.a0, z4), u0 = mfma32(fu.a0, b.a0, z4), g1 = mfma32(fg.a1, b.a1, g0), g2 = mfma16(fg.a2, b.a2, z4), u1 = mfma32(fu.a1, b.a1, u0), u2 = mfma16(fu.a2, b.a2, z4);
                __builtin_amdgcn_sched_barrier(0);
                const f32x4 cg = g1 + g2, cu = u1 + u2;
                st16m2<0>(ws, sg, asu(cg), su, asu(cu), mk);
                STAMP(11 + 32 * layer);
                if (layer == 0) {
                    const unsigned tn = now();
                    WAIT1(tl, 2);
                    unsigned d = min(min(min(tn - tl[0], tn - tl[1]), min(tn - tl[2], tn - tl[3])), 800u);
                    d = min(d, dppu<0xB1>(d)); d = min(d, dppu<0x4E>(d)); d = min(d, dppu<0x124>(d)); d = min(d, dppu<0x128>(d));
                    unsigned tr = tn - min(min((unsigned)__builtin_amdgcn_readlane(d, 0), (unsigned)__builtin_amdgcn_readlane(d, 16)), min((unsigned)__builtin_amdgcn_readlane(d, 32), (unsigned)__builtin_amdgcn_readlane(d, 48)));
                    if (!POLL3 && (int)(t3 - tr) >= M3) tr = t3 + D4S - D4A;
                    lds_set(lb + L_CNT + 56, tr | 1u);
                    lds_add(68, 1u);
                    if ((int)(tn + 12 - X4A - tr) > 0) tr = tn + 12 - X4A;
                    until(tr + X4A);
                } else sched(X4, 12);
                STAMP(16 + 32 * layer);
                float hv; f32x2 st;
                u32x4 a0, a1, a2, a3, ac, as;
                auto load = [&]() __attribute__((always_inline)) {
                    as = ld16_sc1(ws, os);
                    if (layer == 0) ac = ld16_sc1(ws, oc);
                    a0 = ld16_sc1<0>(ws, o); a1 = ld16_sc1<1024>(ws, o); a2 = ld16_sc1<2048>(ws, o); a3 = ld16_sc1<3072>(ws, o);
                    if (layer == 0) asm volatile("s_waitcnt vmcnt(0)" : "+v"(a0), "+v"(a1), "+v"(a2), "+v"(a3), "+v"(ac), "+v"(as) :: "memory");
                    else { asm volatile("s_waitcnt vmcnt(0)" : "+v"(a0), "+v"(a1), "+v"(a2), "+v"(a3), "+v"(as) :: "memory"); ac = as; }
                };
                auto compute = [&]() __attribute__((always_inline)) {
                    float ss, sm;
                    asm volatile("v_add_f32_dpp %0, %2, %2 quad_perm:[1,0,3,2] row_mask:0xf bank_mask:0xf\n\tv_add_f32_dpp %1, %3, %3 quad_perm:[1,0,3,2] row_mask:0xf bank_mask:0xf\n\ts_nop 0\n\t"
                                 "v_add_f32_dpp %0, %0, %0 quad_perm:[2,3,0,1] row_mask:0xf bank_mask:0xf\n\tv_add_f32_dpp %1, %1, %1 quad_perm:[2,3,0,1] row_mask:0xf bank_mask:0xf\n\ts_nop 0"
                                 : "=&v"(ss), "=&v"(sm) : "v"(as[0]), "v"(as[1]));
                    ss = x16_add(ss); sm = x16_add(sm);
                    st = ln_stats(ss, sm);
                    const f32x4 t = (asf(a0) + asf(a1)) + (asf(a2) + asf(a3));
                    const f32x2 gu = sw16(rs32(t[0], t[2]), rs32(t[1], t[3]));
                    if (layer == 0) {
                        cs = sw16(rs32(asf(ac)[0], asf(ac)[2]), rs32(asf(ac)[1], asf(ac)[3]));
                        cs[0] += dpp<0x124>(cs[0]); cs[1] += dpp<0x124>(cs[1]); cs[0] += dpp<0x128>(cs[0]); cs[1] += dpp<0x128>(cs[1]);
                    }
                    const unsigned gw = pk2v((gu - st[0] * cs) * st[1]);
                    const float gv = lo16(gw);
                    hv = bfr(gv * frcp(1.f + ex2(gv * -LOG2E))) * hi16(gw);
                    *(LAS u16*)(size_t)ha = (u16)pk2(hv, hv);
                    __builtin_amdgcn_sched_barrier(0);
                };
                RETRY(load(), max(max(max(um4(a0), um4(a1)), max(um4(a2), um4(a3))), max(um4(ac), um4(as))), compute(), any_nan(hv, hv), layer ? X4 : X4A);
                BAR();
                STAMP(12 + 32 * layer);
                if (layer == 0) {
                    *reinterpret_cast<f32x2*>(smem + L_CS2 + wave * 512 + lane * 8) = cs;
                }
            }
            if (layer == 0) {
                if (wave == 1) {
                    const unsigned o = WS_OWN + (unsigned)(gi * 16384 + 1024 + 2048 + (lane < 48 ? lane : 0) * 16);
                    u32x4 a[8];
                    a[0] = ld16_sc1<-2048>(ws, o); a[1] = ld16_sc1<0>(ws, o); a[2] = ld16_sc1<2048>(ws, o);
                    a[3] = ld16_sc1<-2048>(ws, o + 6144); a[4] = ld16_sc1<0>(ws, o + 6144); a[5] = ld16_sc1<2048>(ws, o + 6144);
                    a[6] = ld16_sc1<-2048>(ws, o + 12288); a[7] = ld16_sc1<0>(ws, o + 12288);
                    WAIT8(a, 0);
                    if (lane < 48) *reinterpret_cast<f32x4*>(smem + L_CS + lane * 16) = SUM8(a);
                }
                Frag80 f[4];
#pragma unroll
                for (int i = 0; i < 4; ++i) { f[i] = frag80(lb + L_WQ + (unsigned)(128 * wave + 32 * i + 8 * p4), 416, r0s, r1s, r2s); PIN3(f[i].a0, f[i].a1, f[i].a2); }
                unsigned b16 = lb + L_XS + (unsigned)(p4 * 160 + kq * 16), b8 = lb + L_XS + (unsigned)(p4 * 160 + 128 + kq * 8);
                unsigned so = WS_H1 + (unsigned)(((gi * 4 + p4) * 8 + gj) * 896 + 256 * wave + q4 * 64 + kq * 16);
                unsigned long long mk = 0x000f000f000f000full;
                PIN3(b16, b8, so); PINS(mk);
                BAR();
                STAMP(33);
                const Frag80 b = rows80(b16, b8);
                st16_sc1(ws, so, tiles4(f, b, mk));
                STAMP(34);
                qk_addr(true); att_addr();
                cma = lb + L_M; cxa = lb + L_M + (unsigned)(wave == 0 ? (lane >> 5) * 16 : (lane >> 4) * 8);
                cra = lb + L_OP + (unsigned)(wave == 0 ? (lane >> 5) * 4096 + (lane & 31) * 16 : 512 + (lane >> 4) * 2048 + (lane & 15) * 16);
                if (wave == 2) ml_addr(lane, cxa, cra);
                cso = WS_OWN + (unsigned)((gi * 8 + gj) * 2048 + 512) + (unsigned)(wave == 0 ? lane * 4 : wave == 1 ? 256 + (lane >> 5) * 64 + (lane & 15) * 4 : 384);
                PIN2(cma, cxa); PIN2(cra, cso);
            }
        } else {
            const int t = wave - 3;
            StatAddr sa;
            auto gather32 = [&](unsigned o, int lane, unsigned X, auto mid, auto fin) __attribute__((always_inline)) {
                u32x4 a0 = ld16_sc1<-2048>(ws, o), a1 = ld16_sc1<-1024>(ws, o), a2 = ld16_sc1<0>(ws, o), a3 = ld16_sc1<1024>(ws, o), sel;
                const unsigned one = (lane & 1) ? 0x3f800000u : 0x3f80u;
#pragma unroll
                for (int k = 0; k < 4; ++k) sel[k] = ((lane >> 1) & 3) == k ? one : 0u;
                unsigned long long hi = 0xff00ff00ff00ff00ull;
                PIN1(sel); PINS(hi);
                mid();
                float v;
                auto sum = [&]() __attribute__((always_inline)) {
                    f32x4 d; float w;
                    asm volatile("s_waitcnt vmcnt(3)\n\tv_mfma_f32_16x16x32_bf16 %0, %1, %5, 0\n\ts_waitcnt vmcnt(2)\n\tv_mfma_f32_16x16x32_bf16 %0, %2, %5, %0\n\t"
                                 "s_waitcnt vmcnt(1)\n\tv_mfma_f32_16x16x32_bf16 %0, %3, %5, %0\n\ts_waitcnt vmcnt(0)\n\tv_mfma_f32_16x16x32_bf16 %0, %4, %5, %0"
                                 : "=&v"(d), "+v"(a0), "+v"(a1), "+v"(a2), "+v"(a3) : "v"(sel) : "memory");
                    asm volatile("s_nop 7\n\tv_cndmask_b32 %0, %2, %3, %6\n\tv_cndmask_b32 %1, %4, %5, %6\n\ts_nop 1\n\tv_permlane32_swap_b32 %0, %1\n\tv_add_f32 %0, %0, %1"
                                 : "=&v"(v), "=&v"(w) : "v"(d[0]), "v"(d[1]), "v"(d[2]), "v"(d[3]), "s"(hi));
                    fin(v);
                    __builtin_amdgcn_sched_barrier(0);
                };
                sum();
                if (__builtin_expect(any_nan(v, v) != 0ull, 0)) {
#pragma unroll 1
                    for (unsigned spins = 0; spins < kSpinCap; ++spins) {
                        slip(X);
                        again16<-2048>(a0, ws, o); again16<-1024>(a1, ws, o); again16<0>(a2, ws, o); again16<1024>(a3, ws, o);
                        asm volatile("s_waitcnt vmcnt(0)" : "+v"(a0), "+v"(a1), "+v"(a2), "+v"(a3) :: "memory");
                        if (!any_empty(max(max(um4(a0), um4(a1)), max(um4(a2), um4(a3))))) break;
                    }
                    sum();
                }
            };
            auto pack4 = [&](float c0, float c1, float c2, float c3) __attribute__((always_inline)) { const unsigned w0 = min(pk2(c0, c1), 0xfffffffeu), w1 = min(pk2(c2, c3), 0xfffffffeu); return (u32x2){w0, w1}; };
            unsigned xw;
            __builtin_amdgcn_s_setprio(0);
            {
                LANE_VARS();
                if (layer == 0) xe = lo16(*reinterpret_cast<const u16*>(smem + L_X0 + kq * 1280 + gj * 160 + t * 32 + n * 2));
                STAMP(6 + 32 * layer);
                unsigned lo = WS_OWN + (unsigned)(gi * 16384 + layer * 512 + (lane >> 3) * 2048 + t * 128 + (t == 3 ? 0 : (lane & 7) * 16));
                unsigned lw = lb + L_O2 + (unsigned)((lane >> 3) * 416 + t * 128 + (t == 3 ? 0 : (lane & 7) * 16));
                PIN2(lo, lw);
                u32x4 ov = {~0u, ~0u, ~0u, ~0u};
                if (layer != 0 && t < 4) { sched(X2, 20); STAMP(49); ov = ld16_sc1(ws, lo); }
                const unsigned wa = lb + L_WO + (unsigned)(r0s * 160 + 32 * t + 8 * p4), wa1 = lb + L_WO + (unsigned)(r1s * 160 + 32 * t + 8 * p4);
                bf16x8 af[6];
                if (layer == 0) {
                    if (t < 4) {
                        unsigned tf = 0u;
#pragma unroll 1
                        for (unsigned spins = 0; spins < (kSpinCap << 4); ++spins) { tf = lds_word(lb + L_CNT + 48); if (tf) break; }
                        slot(NUNIT + XG2);
                        until(tf + DG2);
                        ov = ld16_sc1(ws, lo);
                        if (GOPT) lds_add(72, 1u);
                        if (nj) { WAIT1(ov, 1); lds_add(44, nj); }
                    } else { VMW(0); lds_add(44, nj); }
#pragma unroll 1
                    for (unsigned spins = 0; spins < (kSpinCap << 4); ++spins) if (lds_word(lb + L_CNT + 44) >= 5u) break;
                    STAMP(8);
                }
#pragma unroll
                for (int s = 0; s < 6; ++s) af[s] = cat8(tr64(wa + s * 32 * 160), tr64(wa1 + s * 32 * 160));
                const unsigned h3 = WS_H35 + (unsigned)((gj * 20 + 10 * layer + t) * 4096);
                unsigned so = h3 + (unsigned)(gi * 128 + (lane & 2) * 32 + (lane & 4) * 8 + kq * 8), go = h3 + (unsigned)(2048 + lane * 16);
                unsigned xs = lb + L_XS + (unsigned)(kq * 160 + t * 32 + n * 2);
                unsigned t3 = layer ? 0u : lds_word(lb + L_CNT + 64) + C3;
                unsigned br = lb + L_O2 + (unsigned)((n & 7) * 416 + kq * 16), mr = lb + L_O2 + (unsigned)((n & 7) * 416 + 384);
                unsigned long long mk = 0x0055005500550055ull;
                PIN3(af[0], af[1], af[2]); PIN3(af[3], af[4], af[5]); PIN3(br, mr, so); PIN2(go, xs); PINS(mk); PINS(t3);
                if (t < 4) {
                    WAIT1(ov, 0);
                    *(LAS u32x4*)(size_t)lw = ov;
                    if (layer == 0 ? any_empty(um4(ov)) : __builtin_expect(any_empty(um4(ov)), 0)) {
#pragma unroll 1
                        for (unsigned spins = 0; spins < kSpinCap; ++spins) {
                            if (layer != 0) slip(X2);
                            ov = ld16_sc1(ws, lo);
                            WAIT1(ov, 0);
                            *(LAS u32x4*)(size_t)lw = ov;
                            if (!any_empty(um4(ov))) break;
                            if (layer == 0) { if ((spins & 15u) == 15u) send_flag(lane); __builtin_amdgcn_s_sleep(1); }
                        }
                    }
                }
                BAR();
                bf16x8 bf[6];
                const f32x2 ml = *(const LAS f32x2*)(size_t)mr;
#pragma unroll
                for (int s = 0; s < 6; ++s) bf[s] = lds16(br + s * 64);
                __builtin_amdgcn_sched_barrier(0);
                f32x4 ca = z4;
#pragma unroll
                for (int s = 0; s < 6; ++s) ca = mfma32(af[s], bf[s], ca);
                const float e = ex2(ml[0] - fmax2(ml[0], dpp<0xB1>(ml[0]))), el = e * ml[1];
                const float f = e * frcp(el + dpp<0xB1>(el));
                float c0, c1, c2, c3;
                asm volatile("s_nop 7\n\t"
                             "v_mul_f32 %0, %4, %8\n\tv_mul_f32 %1, %5, %8\n\tv_mul_f32 %2, %6, %8\n\tv_mul_f32 %3, %7, %8\n\t"
                             "v_add_f32_dpp %0, %0, %0 quad_perm:[1,0,3,2] row_mask:0xf bank_mask:0xf\n\tv_add_f32_dpp %1, %1, %1 quad_perm:[1,0,3,2] row_mask:0xf bank_mask:0xf\n\t"
                             "v_add_f32_dpp %2, %2, %2 quad_perm:[1,0,3,2] row_mask:0xf bank_mask:0xf\n\tv_add_f32_dpp %3, %3, %3 quad_perm:[1,0,3,2] row_mask:0xf bank_mask:0xf"
                             : "=&v"(c0), "=&v"(c1), "=&v"(c2), "=&v"(c3) : "v"(ca[0]), "v"(ca[1]), "v"(ca[2]), "v"(ca[3]), "v"(f));
                st8m(ws, so, pack4(c0, c1, c2, c3), mk);
                if (layer == 0 && wave == 7) st4_sc1(ws, WS_MB + (unsigned)(gi * 1024 + 64 + gj * 4), now());
                if (layer == 0) lds_add(76, 1u);
                STAMP(7 + 32 * layer);
                if (layer != 0) sched(X3, 12);
                else {
                    if (POLL3) wait_word(ws, h3 + (unsigned)(((gi * 7 + t * 5 + 3) & 31) * 128));
                    else {
                        const unsigned n2 = now() + 20;
                        until((int)(n2 - t3) > 0 ? n2 : t3);
                    }
                }
                STAMP(18 + 32 * layer);
                gather32(go, lane, layer ? X3 : 0u, [&]() { sa = stats_addr(lane, h4 + (unsigned)(15360 + gj * 128), 16); }, [&](float s) { xw = pk2(xe + bfr(s), 0.f); *(LAS u16*)(size_t)xs = (u16)xw; });
                xe = lo16(xw);
                STAMP(9 + 32 * layer);
            }
            BAR();
            if (wave == 3) stats(sa);
            STAMP(11 + 32 * layer);
            {
                LANE_VARS();
                const unsigned im = lb + L_WD + (unsigned)(32 * t + 8 * p4);
                if (layer == 0 && !((EARLY | GOPT | GB4 | GST) & 4)) {
#pragma unroll 1
                    for (unsigned spins = 0; spins < (kSpinCap << 4); ++spins) { if (lds_word(lb + L_CNT + 68) >= 3u) break; __builtin_amdgcn_s_sleep(1); }
                }
                bf16x8 wd0 = cat8(tr64(im + r0s * 160), tr64(im + r1s * 160)); bf16x4 wd1 = tr64(im + (32 + r2s) * 160);
                const unsigned h5 = WS_H35 + (unsigned)((gj * 20 + 10 * layer + 5 + t) * 4096);
                unsigned h16 = lb + L_H + (unsigned)(p4 * 96 + kq * 16), h8 = lb + L_H + (unsigned)(p4 * 96 + 64 + kq * 8);
                unsigned so = h5 + (unsigned)(gi * 128 + (lane & 1) * 64 + (lane & 2) * 16 + kq * 8);
                unsigned long long mk = 0x000f000f000f000full;
                PIN2(wd0, wd1); PIN3(h16, h8, so); PINS(mk);
                if (wave == 4) {
                    const unsigned os = h4 + (unsigned)(15360 + (p4 + 4 * (kq & 1)) * 128 + q4 * 16);
                    if (layer == 0) {
#pragma unroll 1
                        for (unsigned spins = 0; spins < (kSpinCap << 4); ++spins) if (lds_word(lb + L_CNT + 56)) break;
                    }
                    sched(layer ? X4 : X4A, 0);
                    u32x4 as;
#pragma unroll 1
                    for (unsigned spins = 0; spins < kSpinCap; ++spins) { as = ld16_sc1(ws, os); WAIT1(as, 0); if (!any_empty(um4(as))) break; }
                    float ss = __uint_as_float(as[0]), sm = __uint_as_float(as[1]), rd = __uint_as_float(as[2]);
                    ss += dpp<0xB1>(ss); sm += dpp<0xB1>(sm); rd += dpp<0xB1>(rd);
                    ss += dpp<0x4E>(ss); sm += dpp<0x4E>(sm); rd += dpp<0x4E>(rd);
                    ss = x16_add(ss); sm = x16_add(sm); rd = x16_add(rd);
                    if (LANES(0x1111ull)) *reinterpret_cast<f32x4*>(smem + L_ST + q4 * 16) = (f32x4){ss, sm, rd, 0.f};
                }
                BAR();
                STAMP(12 + 32 * layer);
                const bf16x8 hb0 = lds16(h16); const bf16x4 hb1 = lds8(h8);
                __builtin_amdgcn_sched_barrier(0);
                const f32x4 c = mfma32(wd0, hb0, z4) + mfma16(wd1, hb1, z4);
                st8m(ws, so, pack4(c[0], c[1], c[2], c[3]), mk);
                STAMP(13 + 33 * layer);
                if (layer == 0 || gi == 4 * t + (gj & 3)) {
                    float rs, n1;
                    auto prep = [&]() __attribute__((always_inline)) {
                        const f32x4 sr = *reinterpret_cast<const f32x4*>(smem + L_ST + kq * 16);
                        const float sw = *reinterpret_cast<const float*>(smem + L_CNT + 12);
                        const f32x2 st = ln_stats(sr[0], sr[1]);
                        const float r = bfr((sr[2] - st[0] * sw) * st[1]);
                        rs = bfr(frcp(1.f + ex2(r * -LOG2E)));
                        n1 = bfr(bfr(xe - st[0]) * st[1]);
                        PIN2(rs, n1);
                    };
                    if (layer == 0) {
                        unsigned go = h5 + (unsigned)(2048 + lane * 16), xs = lb + L_XS + (unsigned)(kq * 160 + t * 32 + n * 2);
                        PIN2(go, xs);
                        sched(X5A, 8);
                        STAMP(15);
                        gather32(go, lane, X5A, [&]() { prep(); sa = stats_addr(lane, WS_H1 + (unsigned)((gi * 32 + gj) * 896 + 768), 7168); },
                                 [&](float y) { xw = pk2(xe + bfr(n1 + bfr(bfr(y) * rs)), 0.f); *(LAS u16*)(size_t)xs = (u16)xw; });
                        xe = lo16(xw);
                        STAMP(14);
                    } else {
                        unsigned go = h5 + (unsigned)(2048 + lane * 16), oo = (unsigned)(kq * 1280 + gj * 160 + t * 32 + n * 2);
                        PIN2(go, oo);
                        sched(X5B, 8);
                        STAMP(47);
                        gather32(go, lane, X5B, prep, [&](float y) { xw = pk2(xe + bfr(n1 + bfr(bfr(y) * rs)), 0.f); });
                        st2(out, oo, xw);
                        STAMP(45);
                    }
                }
            }
            if (layer == 0) {
                BAR();
                if (wave == 3) { stats(sa); STAMP(34); }
                asm volatile("" : "=v"(cma), "=v"(cxa), "=v"(cra), "=v"(cso));
                if (wave != 7) { qk_addr(true); att_addr(); }
                else {
                    const int lane = lane_id();
                    unsigned o = WS_H1 + (unsigned)((gi * 4 + (gj >> 1)) * 7168 + 3584 + (lane < 48 ? lane : 48) * 16), qw;
                    f32x4 cs;
                    PIN1(o);
                    sched(X1, 26);
                    STAMP(51);
                    f32x4 s; u32x4 a[8];
                    RETRY8(a, o, 896, s, { qw = lb + L_Q + (unsigned)((lane >> 2) * 32 + (lane & 3) * 4); cs = *reinterpret_cast<const f32x4*>(smem + L_CS + lane * 16); PIN2(qw, cs); qk_addr(true); att_addr(); }, { s -= bfr(__int_as_float(__builtin_amdgcn_readlane(__float_as_int(s[1]), 48)) * (1.f / DM)) * cs;
                                           u32x2 w; w[0] = pk2(s[0], s[1]); w[1] = pk2(s[2], s[3]);
                                           if (LANES(0xffff000000000000ull)) { w[0] = __float_as_uint(s[0]); w[1] = __float_as_uint(s[1]); }
                                           *(LAS unsigned*)(size_t)qw = w[0]; *(LAS unsigned*)(size_t)(qw + 16) = w[1]; __builtin_amdgcn_sched_barrier(0); }, any_nan(s[0] + s[2], s[1] + s[3]), X1);
                }
            }
        }
    };
    PINS(ck1); PINS(ck2); PINS(csc);
    chain(IC<0>{});
    STAMP(35);
    att1();
    chain(IC<1>{});
}

__device__ __attribute__((used, noinline)) void mega_code_end() { asm volatile(".Lmega_code_end:"); }

extern "C" void kernel_launch(void* const* d_in, const int* in_sizes, int n_in, void* d_out, int out_size, void* d_ws,
                              size_t ws_size, hipStream_t stream) {
    const u16* x0 = (const u16*)d_in[0];
    const u16* kc = (const u16*)d_in[1];
    const u16* vc = (const u16*)d_in[2];
    const u16* wq = (const u16*)d_in[3];
    const u16* wo = (const u16*)d_in[4];
    const u16* wr = (const u16*)d_in[6];
    const u16* wg = (const u16*)d_in[7];
    const u16* wu = (const u16*)d_in[8];
    const u16* wd = (const u16*)d_in[9];
    (void)hipFuncSetAttribute((const void*)mega_kernel, hipFuncAttributeMaxDynamicSharedMemorySize, L_END);
    mega_kernel<<<256, 512, L_END, stream>>>(x0, kc, vc, wq, wo, wr, wg, wu, wd, (u16*)d_out, (unsigned char*)d_ws);
}
```

```cpp
#include <hip/hip_runtime.h>
#include <hip/hip_bf16.h>
#include <stdint.h>

typedef unsigned short u16;
typedef unsigned u32x4 __attribute__((ext_vector_type(4)));
typedef unsigned u32x2 __attribute__((ext_vector_type(2)));
typedef float f32x4 __attribute__((ext_vector_type(4)));
typedef short s16x4 __attribute__((ext_vector_type(4)));
typedef __attribute__((ext_vector_type(4))) __bf16 bf16x4;
typedef __attribute__((ext_vector_type(8))) __bf16 bf16x8;
#define LAS __attribute__((address_space(3)))

constexpr int DM = 640, HQ = 32, SK = 4096, DH = 192, WIN = 1024, FF = 1536;
constexpr int QD = HQ * DH;
constexpr float SCALE = 0.07216878364870322f, LOG2E = 1.44269504088896341f;
constexpr unsigned kSpinCap = 1u << 16;
#define GJ(g) (((g) >> 3) & 7)
#define GI(g) (((g) & 7) * 4 + ((g) >> 6))

#define D4A 148
#define D5A 104
#define D1 112
#define D2 162
#define D3 76
#define D4 96
#define D5B 92
#define PACE 62
#define PINIT 100
#define PMIN 50
constexpr unsigned X4A = D4A, X5A = X4A + D5A, X1 = X5A + D1, X2 = X1 + D2, X3 = X2 + D3, X4 = X3 + D4, X5B = X4 + D5B;
#define WOC 2
#define GUC 3
#define DG2 15
#define DGU 25
#define EARLY 7
#define WEARLY 1
#define WEC 3
#define XCOST 16
constexpr unsigned NUNIT = 384 + XCOST + 9 * ((EARLY & 1) + ((EARLY >> 1) & 1) + ((EARLY >> 2) & 1)) + 2 * WEC * WEARLY, PBURST = 30;
#define GSTEPS GP = 0, GC = 3, GD = 5, GL = 0, GE = 2, GF = 4
#define C3 (PINIT + NUNIT * PACE / 16 + E3)
#define DWD 20
#define ADOPT 1
#define TOFF 15
#define FX 0
#define PRIO 0
#define GOPT 0
#define GB4 0
#define GST 0
#define POLL3 1
#define M3 20
#define E3 330
#define D4S 125
#define XG2 0
#define WOFF 6
#define FREEW(w) ((w) >= 6)
#define LEAD 12
#define DQ 40
#define DMB 30
constexpr int GSTEPS;
constexpr int QKT = 2;

constexpr unsigned WS_R = 0;
constexpr unsigned WS_B = 1024;
constexpr unsigned WS_MB = 4096;
constexpr unsigned WS_H1 = WS_MB + 256 * 128;
constexpr unsigned WS_OWN = WS_H1 + 32 * 4 * 7168;
constexpr unsigned WS_H35 = WS_OWN + 256 * 2048;
constexpr unsigned WS_H4 = WS_H35 + 160 * 4096;
constexpr unsigned WS_END = WS_H4 + 64 * 16384;
constexpr unsigned WS_DBG = 6680576;
static_assert(WS_END <= WS_DBG, "ws map");

constexpr int L_WQ = 0;
constexpr int L_WO = 33792;
constexpr int L_GU = L_WO + 30720;
constexpr int L_WD = L_GU + 16384;
constexpr int L_X0 = L_WD + 8192;
constexpr int L_WR = L_X0 + 5120;
constexpr int L_XS = L_WR + 2048;
constexpr int L_Q = L_XS + 1024;
constexpr int L_OP = L_Q + 512;
constexpr int L_M = L_OP + 8192;
constexpr int L_H = L_M + 64;
constexpr int L_ST = L_H + 384;
constexpr int L_CS = L_ST + 64;
constexpr int L_CS2 = L_CS + 1024;
constexpr int L_CNT = L_CS2 + 1536;
constexpr int L_P = L_CNT + 80;
constexpr int L_O2 = L_P + 72 * 192;
constexpr int L_DBG = L_O2 + 3328;
constexpr int L_END = L_DBG + 2048;
static_assert(L_END <= 163840 && L_END > 81920, "LDS");

typedef __attribute__((ext_vector_type(2))) __bf16 bf16x2;
typedef float f32x2 __attribute__((ext_vector_type(2)));
__device__ __forceinline__ unsigned pk2(float a, float b) { return __builtin_bit_cast(unsigned, __builtin_convertvector((f32x2){a, b}, bf16x2)); }
__device__ __forceinline__ float lo16(unsigned w) { return __uint_as_float(w << 16); }
__device__ __forceinline__ float hi16(unsigned w) { return __uint_as_float(w & 0xffff0000u); }
__device__ __forceinline__ float bfr(float f) { return __uint_as_float(pk2(0.f, f)); }
template <int CTRL> __device__ __forceinline__ float dpp(float x) {
    return __builtin_bit_cast(float, __builtin_amdgcn_mov_dpp(__builtin_bit_cast(int, x), CTRL, 0xf, 0xf, true));
}
template <int CTRL> __device__ __forceinline__ unsigned dppu(unsigned x) { return (unsigned)__builtin_amdgcn_mov_dpp((int)x, CTRL, 0xf, 0xf, true); }
__device__ __forceinline__ float row16_sum(float v) {
    v += dpp<0xB1>(v); v += dpp<0x4E>(v); v += dpp<0x141>(v); v += dpp<0x140>(v); return v;
}
__device__ __forceinline__ float fmax2(float a, float b) { return __builtin_elementwise_maximum(a, b); }
__device__ __forceinline__ float fmax3(float a, float b, float c) { return fmax2(fmax2(a, b), c); }
__device__ __forceinline__ float x16_add(float v) { auto s = __builtin_amdgcn_permlane16_swap(__float_as_uint(v), __float_as_uint(v), false, false); return __uint_as_float(s[0]) + __uint_as_float(s[1]); }
__device__ __forceinline__ float x32_add(float v) { auto s = __builtin_amdgcn_permlane32_swap(__float_as_uint(v), __float_as_uint(v), false, false); return __uint_as_float(s[0]) + __uint_as_float(s[1]); }
__device__ __forceinline__ float wave_sum(float v) { return x32_add(x16_add(row16_sum(v))); }
__device__ __forceinline__ float rs32(float a, float b) { auto s = __builtin_amdgcn_permlane32_swap(__float_as_uint(a), __float_as_uint(b), false, false); return __uint_as_float(s[0]) + __uint_as_float(s[1]); }
__device__ __forceinline__ f32x2 sw16(float a, float b) { auto s = __builtin_amdgcn_permlane16_swap(__float_as_uint(a), __float_as_uint(b), false, false); return (f32x2){__uint_as_float(s[0]), __uint_as_float(s[1])}; }

#define BAR() asm volatile("s_waitcnt lgkmcnt(0)\n\ts_barrier" ::: "memory")
#define VMW(n) asm volatile("s_waitcnt vmcnt(%0)" :: "i"(n) : "memory")

template <int OFF = 0> __device__ __forceinline__ u32x4 ld16_sc1(const void* sbase, unsigned voff) {
    u32x4 r; asm volatile("global_load_dwordx4 %0, %1, %2 offset:%3 sc1" : "=v"(r) : "v"(voff), "s"(sbase), "i"(OFF) : "memory"); return r;
}
__device__ __forceinline__ unsigned ld4_sc1(const void* sbase, unsigned voff) {
    unsigned r; asm volatile("global_load_dword %0, %1, %2 sc1" : "=v"(r) : "v"(voff), "s"(sbase) : "memory"); return r;
}
template <int OFF = 0> __device__ __forceinline__ void st16_sc1(void* sbase, unsigned voff, u32x4 v) {
    asm volatile("global_store_dwordx4 %0, %1, %2 offset:%3 sc1\n\ts_nop 1" :: "v"(voff), "v"(v), "s"(sbase), "i"(OFF) : "memory");
}
__device__ __forceinline__ void st4_sc1(void* sbase, unsigned voff, unsigned v) {
    asm volatile("global_store_dword %0, %1, %2 sc1" :: "v"(voff), "v"(v), "s"(sbase) : "memory");
}
__device__ __forceinline__ void st8(void* sbase, unsigned voff, u32x2 v) {
    asm volatile("global_store_dwordx2 %0, %1, %2" :: "v"(voff), "v"(v), "s"(sbase) : "memory");
}
__device__ __forceinline__ void st2(void* sbase, unsigned voff, unsigned v) {
    asm volatile("global_store_short %0, %1, %2" :: "v"(voff), "v"(v), "s"(sbase) : "memory");
}
constexpr int RK = 64, RV = 160;
template <int R, int IMM> __device__ __forceinline__ void ld16_nt(const void* sbase, unsigned voff) {
    asm volatile("global_load_dwordx4 v[%c3:%c4], %0, %1 offset:%2 nt" :: "v"(voff), "s"(sbase), "i"(IMM), "i"(R), "i"(R + 3) : "memory");
}
template <int R, int IMM> __device__ __forceinline__ void ld8_nt(const void* sbase, unsigned voff) {
    asm volatile("global_load_dwordx2 v[%c3:%c4], %0, %1 offset:%2 nt" :: "v"(voff), "s"(sbase), "i"(IMM), "i"(R), "i"(R + 1) : "memory");
}
template <int R> __device__ __forceinline__ void smf(f32x4& d, u32x4 a, unsigned idx) {
    asm volatile("v_smfmac_f32_16x16x64_bf16 %0, %1, v[%c3:%c4], %2" : "+v"(d) : "v"(a), "v"(idx), "i"(R), "i"(R + 7));
}
template <int A, int B> __device__ __forceinline__ void zip_in() {
    unsigned t;
    asm volatile("v_perm_b32 %0, v%c2, v%c1, %3\n\tv_perm_b32 v%c2, v%c2, v%c1, %4\n\tv_mov_b32 v%c1, %0" : "=&v"(t) : "i"(A), "i"(B), "s"(0x05040100u), "s"(0x07060302u));
}
template <int A, int B> __device__ __forceinline__ void zip_out(unsigned& lo, unsigned& hi) {
    asm volatile("v_perm_b32 %0, v%c3, v%c2, %4\n\tv_perm_b32 %1, v%c3, v%c2, %5" : "=&v"(lo), "=&v"(hi) : "i"(A), "i"(B), "s"(0x05040100u), "s"(0x07060302u));
}
template <int A, int B, int C, int D> __device__ __forceinline__ void zip_x(unsigned lo, unsigned hi) {
    asm volatile("v_perm_b32 v%c4, v%c3, v%c2, %6\n\tv_perm_b32 v%c5, v%c3, v%c2, %7\n\tv_mov_b32 v%c2, %0\n\tv_mov_b32 v%c3, %1"
                 :: "v"(lo), "v"(hi), "i"(A), "i"(B), "i"(C), "i"(D), "s"(0x05040100u), "s"(0x07060302u));
}
__device__ __forceinline__ void dma16(const void* sbase, unsigned voff, unsigned lds_dst) {
    unsigned keep;
    asm volatile("s_mov_b32 %0, m0\n\ts_mov_b32 m0, %3\n\ts_nop 0\n\tglobal_load_lds_dwordx4 %1, %2 nt\n\ts_mov_b32 m0, %0"
                 : "=&s"(keep) : "v"(voff), "s"(sbase), "s"(lds_dst) : "memory");
}
__device__ __forceinline__ void dma16p(const void* sbase, unsigned voff, unsigned lds_dst) {
    unsigned keep;
    asm volatile("s_mov_b32 %0, m0\n\ts_mov_b32 m0, %3\n\ts_nop 0\n\tglobal_load_lds_dwordx4 %1, %2\n\ts_mov_b32 m0, %0"
                 : "=&s"(keep) : "v"(voff), "s"(sbase), "s"(lds_dst) : "memory");
}
__device__ __forceinline__ unsigned um4(u32x4 a) { return max(max(a[0], a[1]), max(a[2], a[3])); }
__device__ __forceinline__ bool none(unsigned t) { return __builtin_amdgcn_ballot_w64(t != 0u) == 0ull; }
__device__ __forceinline__ bool any_empty(unsigned mx) { return __builtin_amdgcn_ballot_w64(mx == ~0u) != 0ull; }
__device__ __forceinline__ unsigned long long any_nan(float a, float b) { return __builtin_amdgcn_ballot_w64(__builtin_isunordered(a, b)); }
__device__ __forceinline__ f32x4 asf(u32x4 a) { return __builtin_bit_cast(f32x4, a); }
__device__ __forceinline__ u32x4 asu(f32x4 a) { return __builtin_bit_cast(u32x4, a); }
#define LANES(m) __builtin_amdgcn_inverse_ballot_w64(m)

__device__ __forceinline__ bf16x4 tr64(unsigned lds_addr) { return __builtin_amdgcn_ds_read_tr16_b64_v4bf16((LAS bf16x4*)(size_t)lds_addr); }
__device__ __forceinline__ bf16x8 cat8(bf16x4 a, bf16x4 b) { return __builtin_shufflevector(a, b, 0, 1, 2, 3, 4, 5, 6, 7); }
__device__ __forceinline__ bf16x8 lds16(unsigned a) { return *(const LAS bf16x8*)(size_t)a; }
__device__ __forceinline__ bf16x4 lds8(unsigned a) { return *(const LAS bf16x4*)(size_t)a; }
__device__ __forceinline__ f32x4 mfma32(bf16x8 a, bf16x8 b, f32x4 c) { return __builtin_amdgcn_mfma_f32_16x16x32_bf16(a, b, c, 0, 0, 0); }
__device__ __forceinline__ f32x4 mfma32u(u32x4 a, u32x4 b, f32x4 c) { return mfma32(__builtin_bit_cast(bf16x8, a), __builtin_bit_cast(bf16x8, b), c); }
__device__ __forceinline__ f32x4 mfma16(bf16x4 a, bf16x4 b, f32x4 c) {
    return __builtin_amdgcn_mfma_f32_16x16x16bf16_1k(__builtin_bit_cast(s16x4, a), __builtin_bit_cast(s16x4, b), c, 0, 0, 0);
}

#define STAMP(i) do {} while (0)

__device__ __forceinline__ void wait_word(const void* sbase, unsigned off) {
    unsigned t, cnt = kSpinCap;
    asm volatile("1:\n\tglobal_load_dword %0, %2, %3 sc1\n\ts_sub_u32 %1, %1, 1\n\ts_waitcnt vmcnt(0)\n\tv_cmp_eq_u32 vcc, -1, %0\n\ts_cbranch_vccz 2f\n\ts_cbranch_scc0 1b\n2:"
                 : "=&v"(t), "+s"(cnt) : "v"(off), "s"(sbase) : "memory", "vcc", "scc");
}
template <int OFF = 0> __device__ __forceinline__ void st16m(void* sbase, unsigned voff, u32x4 v, unsigned long long mask) {
    asm volatile("s_mov_b64 exec, %4\n\tglobal_store_dwordx4 %0, %1, %2 offset:%3 sc1\n\ts_mov_b64 exec, -1\n\ts_nop 0" :: "v"(voff), "v"(v), "s"(sbase), "i"(OFF), "s"(mask) : "memory");
}
__device__ __forceinline__ void st8m(void* sbase, unsigned voff, u32x2 v, unsigned long long mask) {
    asm volatile("s_mov_b64 exec, %3\n\tglobal_store_dwordx2 %0, %1, %2 sc1\n\ts_mov_b64 exec, -1" :: "v"(voff), "v"(v), "s"(sbase), "s"(mask) : "memory");
}
template <int OFF> __device__ __forceinline__ void st16m2(void* sbase, unsigned voff, u32x4 v, unsigned voff2, u32x4 v2, unsigned long long mask) {
    asm volatile("s_mov_b64 exec, %6\n\tglobal_store_dwordx4 %0, %1, %2 sc1\n\tglobal_store_dwordx4 %4, %5, %2 offset:%3 sc1\n\ts_mov_b64 exec, -1\n\ts_nop 0"
                 :: "v"(voff), "v"(v), "s"(sbase), "i"(OFF), "v"(voff2), "v"(v2), "s"(mask) : "memory");
}
#define PINS(a) asm volatile("" : "+s"(a))
#define RETRY(LOAD, EMPTY, COMPUTE, NAN_, X) do { LOAD; COMPUTE; if (__builtin_expect((NAN_) != 0ull, 0)) { \
    _Pragma("unroll 1") for (unsigned spins_ = 0; spins_ < kSpinCap; ++spins_) { slip(X); LOAD; if (!any_empty(EMPTY)) break; } COMPUTE; } } while (0)
__device__ __forceinline__ unsigned now() { return (unsigned)__builtin_amdgcn_s_memrealtime(); }
__device__ __forceinline__ void until(unsigned t) {
    asm volatile("1:\n\ts_memrealtime vcc\n\ts_waitcnt lgkmcnt(0)\n\ts_sub_u32 vcc_lo, %0, vcc_lo\n\ts_cmp_lt_u32 vcc_lo, 0x400\n\ts_cbranch_scc1 1b" :: "s"(t - 1u) : "vcc", "scc", "memory");
}
__device__ __forceinline__ unsigned lds_word(unsigned a) {
    unsigned t_; asm volatile("ds_read_b32 %0, %1\n\ts_waitcnt lgkmcnt(0)\n\ts_nop 0" : "=v"(t_) : "v"(a) : "memory");
    return __builtin_amdgcn_readfirstlane(t_);
}
__device__ __forceinline__ void lds_set(unsigned a, unsigned v) { asm volatile("ds_write_b32 %0, %1" :: "v"(a), "v"(v) : "memory"); }
__device__ __forceinline__ int swz(int r) { return r ^ (((r >> 3) & 1) << 2); }
__device__ __forceinline__ f32x2 unpk(unsigned w) { return (f32x2){lo16(w), hi16(w)}; }
__device__ __forceinline__ unsigned pk2v(f32x2 v) { return __builtin_bit_cast(unsigned, __builtin_convertvector(v, bf16x2)); }
__device__ __forceinline__ float frcp(float x) { return __builtin_amdgcn_rcpf(x); }
__device__ __forceinline__ float ex2(float x) { return __builtin_amdgcn_exp2f(x); }
__device__ __forceinline__ int lane_id() { int l; asm volatile("v_mbcnt_lo_u32_b32 %0, -1, 0\n\tv_mbcnt_hi_u32_b32 %0, -1, %0" : "=v"(l)); return l & 63; }
__device__ __forceinline__ f32x2 ln_stats(float ss, float s) {
    const float mean = s * (1.f / DM);
    const float var = fmax2(__builtin_fmaf(-mean, mean, ss * (1.f / DM)), 0.f);
    const unsigned mv = pk2(mean, var);
    return (f32x2){lo16(mv), frcp(bfr(__builtin_amdgcn_sqrtf(bfr(hi16(mv) + 1e-5f))))};
}
#define GATHER8(a, o, S) do { a[0] = ld16_sc1<-4 * (S)>(ws, o); a[1] = ld16_sc1<-3 * (S)>(ws, o); a[2] = ld16_sc1<-2 * (S)>(ws, o); a[3] = ld16_sc1<-(S)>(ws, o); \
    a[4] = ld16_sc1<0>(ws, o); a[5] = ld16_sc1<(S)>(ws, o); a[6] = ld16_sc1<2 * (S)>(ws, o); a[7] = ld16_sc1<3 * (S)>(ws, o); } while (0)
template <int OFF> __device__ __forceinline__ void again16(u32x4& r, const void* sbase, unsigned voff) {
    asm volatile("v_cmp_eq_u32 vcc, -1, %1\n\ts_mov_b64 exec, vcc\n\tglobal_load_dwordx4 %0, %2, %3 offset:%4 sc1\n\ts_mov_b64 exec, -1" : "+v"(r) : "v"(um4(r)), "v"(voff), "s"(sbase), "i"(OFF) : "memory", "vcc");
}
#define AGAIN8(a, o, S) do { again16<-4 * (S)>(a[0], ws, o); again16<-3 * (S)>(a[1], ws, o); again16<-2 * (S)>(a[2], ws, o); again16<-(S)>(a[3], ws, o); \
    again16<0>(a[4], ws, o); again16<(S)>(a[5], ws, o); again16<2 * (S)>(a[6], ws, o); again16<3 * (S)>(a[7], ws, o); } while (0)
#define WAIT8(a, N) asm volatile("s_waitcnt vmcnt(%8)" : "+v"((a)[0]), "+v"((a)[1]), "+v"((a)[2]), "+v"((a)[3]), "+v"((a)[4]), "+v"((a)[5]), "+v"((a)[6]), "+v"((a)[7]) : "i"(N) : "memory")
#define UM32(a) max(max(max(um4(a[0]), um4(a[1])), max(um4(a[2]), um4(a[3]))), max(max(um4(a[4]), um4(a[5])), max(um4(a[6]), um4(a[7]))))
#define SUM8(a) (((((((asf(a[0]) + asf(a[1])) + asf(a[2])) + asf(a[3])) + asf(a[4])) + asf(a[5])) + asf(a[6])) + asf(a[7]))
#define WAIT1(x, N) asm volatile("s_waitcnt vmcnt(%1)" : "+v"(x) : "i"(N) : "memory")
#define LO2(x) __builtin_shufflevector(x, x, 0, 1)
#define HI2(x) __builtin_shufflevector(x, x, 2, 3)
#define ACC8(a, s) do { f32x2 l_, h_; \
    asm volatile("s_waitcnt vmcnt(5)\n\tv_pk_add_f32 %0, %2, %4\n\tv_pk_add_f32 %1, %3, %5\n\tv_pk_add_f32 %0, %0, %6\n\tv_pk_add_f32 %1, %1, %7\n\t" \
                 "s_waitcnt vmcnt(3)\n\tv_pk_add_f32 %0, %0, %8\n\tv_pk_add_f32 %1, %1, %9\n\tv_pk_add_f32 %0, %0, %10\n\tv_pk_add_f32 %1, %1, %11\n\t" \
                 "s_waitcnt vmcnt(1)\n\tv_pk_add_f32 %0, %0, %12\n\tv_pk_add_f32 %1, %1, %13\n\tv_pk_add_f32 %0, %0, %14\n\tv_pk_add_f32 %1, %1, %15\n\t" \
                 "s_waitcnt vmcnt(0)\n\tv_pk_add_f32 %0, %0, %16\n\tv_pk_add_f32 %1, %1, %17\n\ts_nop 0" \
                 : "=&v"(l_), "=&v"(h_) : "v"(LO2(a[0])), "v"(HI2(a[0])), "v"(LO2(a[1])), "v"(HI2(a[1])), "v"(LO2(a[2])), "v"(HI2(a[2])), "v"(LO2(a[3])), "v"(HI2(a[3])), \
                   "v"(LO2(a[4])), "v"(HI2(a[4])), "v"(LO2(a[5])), "v"(HI2(a[5])), "v"(LO2(a[6])), "v"(HI2(a[6])), "v"(LO2(a[7])), "v"(HI2(a[7])) : "memory"); \
    s = __builtin_shufflevector(l_, h_, 0, 1, 2, 3); } while (0)
#define RETRY8(a, o, S, s, MID, POST, NAN_, X) do { GATHER8(a, o, S); MID; ACC8(a, s); POST; if (__builtin_expect((NAN_) != 0ull, 0)) { \
    _Pragma("unroll 1") for (unsigned spins_ = 0; spins_ < kSpinCap; ++spins_) { slip(X); AGAIN8(a, o, S); WAIT8(a, 0); if (!any_empty(UM32(a))) break; } s = SUM8(a); POST; } } while (0)
#define PIN1(a) asm volatile("" : "+v"(a))
#define PIN2(a, b) asm volatile("" : "+v"(a), "+v"(b))
#define PIN3(a, b, c) asm volatile("" : "+v"(a), "+v"(b), "+v"(c))
#define LANE_VARS() const int lane = lane_id(); \
    const int kq = lane >> 4, n = lane & 15, q4 = n >> 2, p4 = lane & 3; \
    const int r0s = 8 * kq + q4 + 4 * (kq & 1), r1s = 8 * kq + q4 + 4 * ((kq & 1) ^ 1);     \
    const int r2s = swz(4 * kq + q4);                                                       \
    (void)r0s; (void)r1s; (void)r2s; (void)p4
template <int V> struct IC { static constexpr int value = V; };
struct Frag80 { bf16x8 a0, a1; bf16x4 a2; };

__global__ void __launch_bounds__(512, 2) __attribute__((amdgpu_num_vgpr(32)))
mega_kernel(const u16* __restrict__ x0, const u16* __restrict__ kc, const u16* __restrict__ vc, const u16* __restrict__ wq,
            const u16* __restrict__ wo, const u16* __restrict__ wr, const u16* __restrict__ wg, const u16* __restrict__ wu,
            const u16* __restrict__ wd, u16* __restrict__ out, unsigned char* __restrict__ ws) {
    extern __shared__ __attribute__((aligned(16))) unsigned char smem[];
    const unsigned lb = (unsigned)(size_t)smem;
    const int wave = __builtin_amdgcn_readfirstlane(threadIdx.x >> 6);
    const int g = blockIdx.x, gj = GJ(g), gi = GI(g);
    const f32x4 z4 = {0.f, 0.f, 0.f, 0.f};
    asm volatile("" ::: "v255");
    auto sched = [&](unsigned X, unsigned gap) {
        unsigned v; unsigned long long c;
        asm volatile("s_memrealtime %1\n\tds_read_b32 %0, %2\n\ts_waitcnt lgkmcnt(0)" : "=v"(v), "=&s"(c) : "v"(lb + L_CNT + 56) : "memory");
        const unsigned n = (unsigned)c + gap;
        unsigned t = __builtin_amdgcn_readfirstlane(v) + X;
        if ((int)(n - t) > 0) t = n;
        until(t);
    };
    auto slip = [&](unsigned X) {
        if (X) lds_set(lb + L_CNT + 56, now() - X);
    };
    float xe = 0.f;

    auto code_pf = [&](int slice, int lane) {
        unsigned long long pc0; unsigned len, back, t;
        asm volatile("s_getpc_b64 %0\n.Lmega_pf%=:\n\ts_mov_b32 %1, .Lmega_code_end-.Lmega_code_begin\n\ts_mov_b32 %2, .Lmega_pf%=-.Lmega_code_begin" : "=s"(pc0), "=s"(len), "=s"(back));
        unsigned off = (unsigned)(slice * 4096 + lane * 64); if (off > len) off = len;
        asm volatile("global_load_dword %0, %1, %2" : "=v"(t) : "v"(off & ~3u), "s"(pc0 - back) : "memory");
        return t;
    };
    asm volatile(".Lmega_code_begin:");
    auto fix_k1 = [&](auto IC_, auto NC) {
        constexpr int A = RK + 8 * decltype(IC_)::value, B = A + 4;
        unsigned t0, t1, t2, t3;
        asm volatile("s_waitcnt vmcnt(%12)\n\tv_mov_b32 %0, v%c4\n\tv_mov_b32 %1, v%c5\n\tv_mov_b32 %2, v%c6\n\tv_mov_b32 %3, v%c7\n\t"
                     "v_mov_b32_dpp v%c4, v%c8 row_ror:8 row_mask:0xf bank_mask:0xc\n\tv_mov_b32_dpp v%c5, v%c9 row_ror:8 row_mask:0xf bank_mask:0xc\n\t"
                     "v_mov_b32_dpp v%c6, v%c10 row_ror:8 row_mask:0xf bank_mask:0xc\n\tv_mov_b32_dpp v%c7, v%c11 row_ror:8 row_mask:0xf bank_mask:0xc\n\t"
                     "v_mov_b32_dpp v%c8, %0 row_ror:8 row_mask:0xf bank_mask:0x3\n\tv_mov_b32_dpp v%c9, %1 row_ror:8 row_mask:0xf bank_mask:0x3\n\t"
                     "v_mov_b32_dpp v%c10, %2 row_ror:8 row_mask:0xf bank_mask:0x3\n\tv_mov_b32_dpp v%c11, %3 row_ror:8 row_mask:0xf bank_mask:0x3"
                     : "=&v"(t0), "=&v"(t1), "=&v"(t2), "=&v"(t3)
                     : "i"(A), "i"(A + 1), "i"(A + 2), "i"(A + 3), "i"(B), "i"(B + 1), "i"(B + 2), "i"(B + 3), "i"(decltype(NC)::value) : "memory");
    };
    auto trans_v = [&](auto UC, auto NC) {
        constexpr int R = RV + 4 * decltype(UC)::value, N = decltype(NC)::value;
#define CELL(r, c) R + 16 * (r) + (c), R + 16 * (r) + 8 + (c)
        unsigned a0, a1, b0, b1, c0, c1;
        VMW(N + 6); zip_in<CELL(0, 0)>(); zip_out<CELL(0, 1)>(a0, a1); zip_out<CELL(0, 2)>(b0, b1); zip_out<CELL(0, 3)>(c0, c1);
        VMW(N + 4); zip_x<CELL(1, 0), CELL(0, 1)>(a0, a1); zip_in<CELL(1, 1)>(); zip_out<CELL(1, 2)>(a0, a1);
        VMW(N + 2); zip_x<CELL(2, 0), CELL(0, 2)>(b0, b1); zip_x<CELL(2, 1), CELL(1, 2)>(a0, a1); zip_in<CELL(2, 2)>(); zip_out<CELL(1, 3)>(a0, a1); zip_out<CELL(2, 3)>(b0, b1);
        VMW(N); zip_x<CELL(3, 0), CELL(0, 3)>(c0, c1); zip_x<CELL(3, 1), CELL(1, 3)>(a0, a1); zip_x<CELL(3, 2), CELL(2, 3)>(b0, b1); zip_in<CELL(3, 3)>();
#undef CELL
    };
    auto trans_w = [&](auto UC, auto NC) {
        constexpr int R = RV + 64 + 2 * decltype(UC)::value, N = decltype(NC)::value;
        unsigned a0, a1;
        VMW(N + 2); zip_in<R, R + 8>(); zip_out<R + 1, R + 9>(a0, a1);
        VMW(N); zip_x<R + 16, R + 24, R + 1, R + 9>(a0, a1); zip_in<R + 17, R + 25>();
    };
    auto frag80 = [&](unsigned im, int rs, int r0s, int r1s, int r2s) -> Frag80 {
        return Frag80{cat8(tr64(im + r0s * rs), tr64(im + r1s * rs)), cat8(tr64(im + (r0s + 32) * rs), tr64(im + (r1s + 32) * rs)), tr64(im + (r2s + 64) * rs)};
    };
    auto mm80 = [&](const Frag80& a, const Frag80& b) -> f32x4 {
        return mfma32(a.a1, b.a1, mfma32(a.a0, b.a0, z4)) + mfma16(a.a2, b.a2, z4);
    };
    auto rows80 = [&](unsigned a16, unsigned a8) -> Frag80 { const Frag80 f{lds16(a16), lds16(a16 + 64), lds8(a8)}; __builtin_amdgcn_sched_barrier(0); return f; };
    auto empty35 = [&](int layer, int part, int lane) {
        const unsigned t = WS_H35 + (unsigned)(((gj * 20 + 10 * layer + 8 * part + (lane >> 3)) * 32 + gi) * 128 + (lane & 7) * 16);
        st16m(ws, (part == 0 || lane < 16) ? t : WS_OWN + (unsigned)((gi * 8 + gj) * 2048 + layer * 512 + (lane - 16) * 16), (u32x4){~0u, ~0u, ~0u, ~0u}, part ? 0xffffffffffffull : ~0ull);
    };
    auto empty4 = [&](int layer, int part, int lane) {
        st16m(ws, WS_H4 + (unsigned)((layer * 32 + gi) * 16384) + (unsigned)((part == 0 || lane < 32) ? (part * 2 + (lane >> 5)) * 4096 + gj * 512 + (lane & 31) * 16
                                                                                           : 12288 + ((lane - 32) >> 3) * 1024 + gj * 128 + (lane & 7) * 16), (u32x4){~0u, ~0u, ~0u, ~0u},
              layer && part ? 0xff000000ffffffffull : ~0ull);
    };
    const unsigned tst = now();
    const int lead = FREEW(wave) ? LEAD * PACE * 4 : 0;
    auto pace = [&](unsigned n, bool wait = true) {
        unsigned u, w, t0;
        asm volatile("s_mov_b64 exec, 1\n\tds_add_rtn_u32 %0, %3, %4\n\tds_max_u32 %3, %5 offset:12\n\tds_add_rtn_u32 %1, %3, %6 offset:12\n\tds_read_b32 %2, %3 offset:4\n\ts_mov_b64 exec, -1\n\ts_waitcnt lgkmcnt(0)"
                     : "=&v"(u), "=&v"(w), "=&v"(t0) : "v"(lb + L_CNT + 60), "v"(n), "v"((now() - tst) << 6), "v"(n * PMIN * 4u) : "memory");
        int tn = (int)(__builtin_amdgcn_readfirstlane(t0) - tst + PINIT) * 64 + __builtin_amdgcn_readfirstlane(u) * (PACE * 4);
        const int tw = __builtin_amdgcn_readfirstlane(w) - PBURST * 64;
        if (tw > tn) tn = tw;
        tn -= lead;
        if (wait) {
#pragma unroll 1
            for (int k = 0; k < 2048 && (int)((now() - tst) << 6) < tn; ++k) {}
        }
    };
    auto slot = [&](unsigned u) {
        const unsigned tn = lds_word(lb + L_CNT + 64) + PINIT + u * PACE / 16;
#pragma unroll 1
        for (int k = 0; k < 8192 && (int)(now() - tn) < 0; ++k) __builtin_amdgcn_s_sleep(1);
    };
    auto load_wo = [&](int k, int lane, bool early) {
        const int c = k * 64 + lane, pp = c / 10, cc = c - 10 * pp;
        const unsigned f = (unsigned)(swz(pp) * 1280 + cc * 16);
        const u16* wb = wo + (size_t)gi * DH * DM + gj * 80;
#pragma unroll
        for (int a2 = 0; a2 < 6; ++a2) { if (a2 % 3 == 0) { if (early) pace(WEC); else slot(NUNIT - WOFF + (2 * k + a2 / 3) * WOC); } dma16(wb + 32 * a2 * DM, f, lb + L_WO + (5 * a2 + k) * 1024); }
    };
    auto load_wd = [&](int lane) {
        const u16* db = wd + gi * 48 * DM + gj * 80;
#pragma unroll 1
        for (int k = 0; k < 5; ++k) {
            const int c = k * 64 + lane, pp = c / 10, cc = c - 10 * pp;
            const unsigned fd = (unsigned)(swz(pp) * 1280 + cc * 16);
            if (!(k & 1)) pace(3);
            dma16(db, fd, lb + L_WD + k * 1024);
            if (k < 3) dma16(db + 32 * DM, (k < 2 || lane < 32) ? fd : 0u, lb + L_WD + (5 + k) * 1024);
        }
    };
    auto load_gu = [&](int m, int lane) {
#pragma unroll
        for (int k = 0; k < 3; ++k) {
            const int c = k * 64 + lane, row = c / 6, cc = c - 6 * row;
            const unsigned o = (unsigned)((swz(row) * FF + cc * 8) * 2), d = lb + L_GU + m * 8192 + k * 1024;
            const u16* wb = (m == 0 ? wg : wu) + gj * 80 * FF + gi * 48;
            pace(3);
            dma16(wb, o, d); dma16(wb + 32 * FF, o, d + 3072);
            if (k < 2) dma16(wb + 64 * FF, (k == 0 || lane < 32) ? o : 0u, d + 6144);
        }
    };
    auto lds_add = [&](int off, unsigned v) { asm volatile("s_mov_b64 exec, 1\n\tds_add_u32 %0, %1\n\ts_mov_b64 exec, -1" :: "v"(lb + L_CNT + off), "v"(v) : "memory"); };
    auto send_flag = [&](int lane) {
        st4_sc1(ws, WS_MB + (unsigned)((gi * 8 + (lane & 7)) * 128 + gj * 4), 1u);
    };
    auto tiles4 = [&](const Frag80 (&f)[4], const Frag80& b, unsigned long long mk) __attribute__((always_inline)) -> u32x4 {
        f32x4 c[4], d[4];
#pragma unroll
        for (int i = 0; i < 4; ++i) c[i] = mfma32(f[i].a0, b.a0, z4);
#pragma unroll
        for (int i = 0; i < 4; ++i) { c[i] = mfma32(f[i].a1, b.a1, c[i]); d[i] = mfma16(f[i].a2, b.a2, z4); }
        __builtin_amdgcn_sched_barrier(0);
        f32x2 lo, hi;
        asm volatile("s_nop 1\n\ts_mov_b64 exec, %18\n\tv_pk_add_f32 %0, %2, %10\n\tv_pk_add_f32 %1, %3, %11\n\t"
                     "s_lshl_b64 exec, exec, 4\n\tv_pk_add_f32 %0, %4, %12\n\tv_pk_add_f32 %1, %5, %13\n\t"
                     "s_lshl_b64 exec, exec, 4\n\tv_pk_add_f32 %0, %6, %14\n\tv_pk_add_f32 %1, %7, %15\n\t"
                     "s_lshl_b64 exec, exec, 4\n\tv_pk_add_f32 %0, %8, %16\n\tv_pk_add_f32 %1, %9, %17\n\ts_mov_b64 exec, -1"
                     : "=&v"(lo), "=&v"(hi) : "v"(LO2(c[0])), "v"(HI2(c[0])), "v"(LO2(c[1])), "v"(HI2(c[1])), "v"(LO2(c[2])), "v"(HI2(c[2])), "v"(LO2(c[3])), "v"(HI2(c[3])),
                       "v"(LO2(d[0])), "v"(HI2(d[0])), "v"(LO2(d[1])), "v"(HI2(d[1])), "v"(LO2(d[2])), "v"(HI2(d[2])), "v"(LO2(d[3])), "v"(HI2(d[3])), "s"(mk) : "scc");
        return asu(__builtin_shufflevector(lo, hi, 0, 1, 2, 3));
    };
    auto empty1 = [&](int half, int lane) {
        const unsigned o = WS_H1 + (unsigned)(((gi * 4 + 2 * half) * 8 + gj) * 896 + (lane < 56 ? lane : 55) * 16);
        st16_sc1(ws, o, (u32x4){~0u, ~0u, ~0u, ~0u}); st16_sc1(ws, o + 7168, (u32x4){~0u, ~0u, ~0u, ~0u});
    };
    auto answer = [&](int lane) {
        st4_sc1(ws, WS_MB + (unsigned)(((lane & 31) * 8 + (lane & 7)) * 128 + 32), (tst + TOFF) | 1u);
    };
    auto adopt = [&](unsigned t) {
        if (ADOPT && t - tst + 256u < 512u) lds_set(lb + L_CNT + 64, t);
        lds_set(lb + L_CNT + 52, 1u);
    };
    struct StatAddr { unsigned a16, b16, a8, b8, dst; };
    auto stats_addr = [&](int lane, unsigned dbase, int dstride) -> StatAddr {
        const int kq = lane >> 4, q4 = (lane >> 2) & 3, p4 = lane & 3;
        const int x1 = LANES(0x4444444444444444ull) ? L_WR + gj * 160 : L_XS + q4 * 160, xa = LANES(0xaaaaaaaaaaaaaaaaull) ? L_XS + 640 : x1;
        StatAddr r{lb + (unsigned)(xa + kq * 16), lb + (unsigned)(L_XS + p4 * 160 + kq * 16), lb + (unsigned)(xa + 128 + kq * 8), lb + (unsigned)(L_XS + p4 * 160 + 128 + kq * 8), dbase + (unsigned)(kq * dstride)};
        PIN3(r.a16, r.b16, r.dst); PIN2(r.a8, r.b8);
        return r;
    };
    auto stats = [&](const StatAddr& r) {
        const bf16x8 a0 = lds16(r.a16), b0 = lds16(r.b16), a1 = lds16(r.a16 + 64), b1 = lds16(r.b16 + 64);
        const bf16x4 a2 = lds8(r.a8), b2 = lds8(r.b8);
        unsigned long long mk = 0x0008000400020001ull; PINS(mk);
        __builtin_amdgcn_sched_barrier(0);
        st16m(ws, r.dst, asu(mfma32(a1, b1, mfma32(a0, b0, z4)) + mfma16(a2, b2, z4)), mk);
    };
    unsigned qa, pw, ma, idx, qm;
    auto qk_addr = [&](bool L1) {
        LANE_VARS();
        qa = lb + L_Q + (unsigned)(kq * 32 + (lane & 2) * 8);
        pw = lb + L_P + (unsigned)((7 + 8 * wave) * 192 + (kq >> 1) * 64 + (n >> 3) * 32 + (lane & 2) * 8 + (q4 & 1) * 8 + (kq & 1) * 4 + (lane & 1) * 2);
        ma = lb + L_M + (unsigned)(wave * 4);
        idx = (lane & 2) ? 0xeeeeu : 0x4444u;
        qm = lb + L_Q;
        PIN3(qa, pw, ma); PIN2(idx, qm);
        if (L1 && wave < 4) __builtin_amdgcn_s_setprio(2);
    };
    auto att_qk0 = [&]() {
        qk_addr(false);
#pragma unroll 1
        for (unsigned spins = 0; spins < (kSpinCap << 4); ++spins) {
            if (lds_word(lb + L_CNT + 4) != 0u) break;
            __builtin_amdgcn_s_sleep(1);
        }
        STAMP(3);
        const u32x4 q0 = *(const LAS u32x4*)(size_t)qa, q1 = *(const LAS u32x4*)(size_t)(qa + 128), q2 = *(const LAS u32x4*)(size_t)(qa + 256);
        f32x4 c0 = z4, c1 = z4, c2 = z4, c3 = z4;
#define SMF(c, q, r) "v_smfmac_f32_16x16x64_bf16 %" #c ", %" #q ", v[" #r "], %7\n\t"
        asm volatile("s_nop 1\n\t" SMF(0, 4, 64:71) SMF(1, 4, 88:95) SMF(2, 4, 112:119) SMF(3, 4, 136:143) SMF(0, 5, 72:79) SMF(1, 5, 96:103) SMF(2, 5, 120:127) SMF(3, 5, 144:151)
                     SMF(0, 6, 80:87) SMF(1, 6, 104:111) SMF(2, 6, 128:135) SMF(3, 6, 152:159) "s_nop 7"
                     : "+v"(c0), "+v"(c1), "+v"(c2), "+v"(c3) : "v"(q0), "v"(q1), "v"(q2), "v"(idx));
#undef SMF
        const float s0 = c0[0] + c0[2], s1 = c1[0] + c1[2], s2 = c2[0] + c2[2], s3 = c3[0] + c3[2];
        float m = fmax2(fmax3(s0, s1, s2), s3);
        m = fmax2(m, dpp<0xB1>(m)); m = fmax2(m, dpp<0x4E>(m)); m = fmax2(m, dpp<0x124>(m)); m = fmax2(m, dpp<0x128>(m));
        const float s01 = LANES(0xffff0000ffff0000ull) ? s1 : s0, s23 = LANES(0xffff0000ffff0000ull) ? s3 : s2;
        const float sv = LANES(0xffffffff00000000ull) ? s23 : s01;
        m = bfr(bfr(m) * SCALE);
        const float p = ex2((bfr(bfr(sv) * SCALE) - m) * LOG2E);
        *(LAS u16*)(size_t)pw = (u16)pk2(p, p);
        *(LAS float*)(size_t)ma = m;
        __builtin_amdgcn_sched_barrier(0);
    };
    unsigned pr, oa, oc, cma, cxa, cra, cso;
    auto att_addr = [&]() {
        LANE_VARS();
        pr = lb + L_P + (unsigned)((7 + 8 * wave - 2 * q4 - (lane & 1)) * 192 + kq * 32 + (lane & 2) * 8);
        oa = lb + L_OP + (unsigned)(wave * 1024 + (lane & 7) * 64 + kq * 16 + (n >> 3) * 8);
        oc = lb + L_OP + (unsigned)(wave * 1024 + 512 + (kq >> 1) * 256 + (lane & 7) * 32 + (kq & 1) * 16 + (n >> 3) * 8);
        PIN3(pr, oa, oc);
    };
    auto comb_out = [&](float u0, float u1, unsigned so, unsigned long long mk) __attribute__((always_inline)) {
        const unsigned w = min(pk2(u0, u1), 0xfffffffeu);
        const u32x4 w4 = {w, dppu<0x55>(w), dppu<0xAA>(w), dppu<0xFF>(w)};
        st16m(ws, so, w4, mk);
    };
    auto max8 = [&](f32x4 m0, f32x4 m1) __attribute__((always_inline)) { return fmax3(fmax3(m0[0], m0[1], m0[2]), fmax3(m0[3], m1[0], m1[1]), fmax2(m1[2], m1[3])) * LOG2E; };
    auto comb_o0 = [&](f32x4 m0, f32x4 m1, f32x4 mm, f32x4 o0, f32x4 o1, f32x4 o2, f32x4 o3, unsigned so, unsigned long long mk) __attribute__((always_inline)) {
        const float M = max8(m0, m1);
        f32x4 f;
#pragma unroll
        for (int k = 0; k < 4; ++k) f[k] = ex2(__builtin_fmaf(mm[k], LOG2E, -M));
        const f32x4 o = f[3] * o3 + (f[2] * o2 + (f[1] * o1 + f[0] * o0));
        comb_out(rs32(o[0], o[2]), rs32(o[1], o[3]), so, mk);
    };
    auto comb_o1 = [&](f32x4 m0, f32x4 m1, f32x2 mm, f32x4 o0, f32x4 o1, unsigned so, unsigned long long mk) __attribute__((always_inline)) {
        const float M = max8(m0, m1);
        const f32x4 o = ex2(__builtin_fmaf(mm[1], LOG2E, -M)) * o1 + ex2(__builtin_fmaf(mm[0], LOG2E, -M)) * o0;
        comb_out(x16_add(rs32(o[0], o[2])), x16_add(rs32(o[1], o[3])), so, mk);
    };
    auto ml_addr = [&](int lane, unsigned& xa, unsigned& ra) {
        xa = lb + L_M + (unsigned)(((lane & 15) >> 1) * 4); ra = lb + L_P + (unsigned)((7 + 8 * ((lane & 15) >> 1)) * 192 + (lane & 1) * 64 + (lane >> 4) * 16);
    };
    auto comb_ml = [&](unsigned ma, unsigned xa, unsigned ra, unsigned so) __attribute__((always_inline)) {
        const u32x4 x4 = *(const LAS u32x4*)(size_t)ra;
        const f32x4 m0 = *(const LAS f32x4*)(size_t)ma, m1 = *(const LAS f32x4*)(size_t)(ma + 16);
        const float mw = *(const LAS float*)(size_t)xa;
        __builtin_amdgcn_sched_barrier(0);
        const f32x4 sp = mfma32u((u32x4){0x3f803f80u, 0x3f803f80u, 0x3f803f80u, 0x3f803f80u}, x4, z4);
        __builtin_amdgcn_sched_barrier(0);
        const float M = max8(m0, m1);
        const float L = row16_sum(sp[0] * ex2(__builtin_fmaf(mw, LOG2E, -M)));
        st16m(ws, so, asu((f32x4){M, L, M, L}), 1ull);
    };
    auto att_pv0 = [&]() {
        u32x4 bm[8];
        trans_v(IC<0>{}, IC<24>{}); trans_v(IC<1>{}, IC<16>{});
#pragma unroll
        for (int e = 0; e < 8; ++e) bm[e] = *(const LAS u32x4*)(size_t)(pr + e * 192);
        __builtin_amdgcn_sched_barrier(0);
        f32x4 da = z4, dc = z4;
        PIN2(da, dc);
        asm volatile("s_nop 1");
        __builtin_amdgcn_sched_barrier(0);
        f32x2 r;
        smf<RV>(da, bm[0], idx); smf<RV + 8>(da, bm[1], idx); smf<RV + 16>(da, bm[2], idx); smf<RV + 24>(da, bm[3], idx);
        smf<RV + 32>(da, bm[4], idx); smf<RV + 40>(da, bm[5], idx); smf<RV + 48>(da, bm[6], idx); smf<RV + 56>(da, bm[7], idx);
        asm volatile("s_nop 7\n\tv_pk_add_f32 %0, %1, %2\n\tds_write_b64 %3, %0\n\ts_mov_b64 exec, 1\n\tds_add_u32 %4, %5\n\ts_mov_b64 exec, -1"
                     : "=&v"(r) : "v"(LO2(da)), "v"(HI2(da)), "v"(oa), "v"(lb + L_CNT + 24), "v"(1u) : "memory");
        trans_w(IC<0>{}, IC<12>{}); trans_w(IC<1>{}, IC<8>{}); trans_w(IC<2>{}, IC<4>{}); trans_w(IC<3>{}, IC<0>{});
        STAMP(4);
        asm volatile("s_nop 1\n\tv_smfmac_f32_16x16x64_bf16 %0, %1, v[224:231], %5\n\tv_smfmac_f32_16x16x64_bf16 %0, %2, v[232:239], %5\n\t"
                     "v_smfmac_f32_16x16x64_bf16 %0, %3, v[240:247], %5\n\tv_smfmac_f32_16x16x64_bf16 %0, %4, v[248:255], %5\n\ts_nop 6"
                     : "+v"(dc) : "v"(bm[0]), "v"(bm[1]), "v"(bm[2]), "v"(bm[3]), "v"(idx));
        asm volatile("s_nop 0\n\tv_pk_add_f32 %0, %1, %2\n\tds_write_b64 %3, %0" : "=&v"(r) : "v"(LO2(dc)), "v"(HI2(dc)), "v"(oc) : "memory");
        STAMP(20);
    };
    unsigned long long ck1 = 0xffff0000ffff0000ull, ck2 = 0xffffffff00000000ull, csc = 0x3d93cd3a3d93cd3aull;
    auto att1 = [&]() {
        constexpr int layer = 1;
        {
#define SMF(c, q, r) "v_smfmac_f32_16x16x64_bf16 v[" #c "], v[" #q "], v[" #r "], %0\n\t"
#define MAXD(x) "v_max_f32_dpp v41, v41, v41 " x " row_mask:0xf bank_mask:0xf\n\t"
            asm volatile("s_waitcnt lgkmcnt(0)\n\ts_barrier\n\t"
                         "ds_read_b128 v[0:3], %1\n\tds_read2_b32 v[12:13], %2 offset0:96 offset1:100\n\tds_read_b128 v[4:7], %1 offset:128\n\tds_read_b128 v[8:11], %1 offset:256\n\t"
                         "v_mov_b64 v[16:17], 0\n\tv_mov_b64 v[18:19], 0\n\tv_mov_b64 v[20:21], 0\n\tv_mov_b64 v[22:23], 0\n\tv_mov_b64 v[24:25], 0\n\tv_mov_b64 v[26:27], 0\n\t"
                         "v_mov_b64 v[28:29], 0\n\tv_mov_b64 v[30:31], 0\n\tv_mov_b64 v[32:33], 0\n\tv_mov_b64 v[34:35], 0\n\tv_mov_b64 v[36:37], 0\n\tv_mov_b64 v[38:39], 0\n\t"
                         "s_waitcnt lgkmcnt(3)\n\t" SMF(16:19, 0:3, 64:71) SMF(20:23, 0:3, 88:95) "s_waitcnt lgkmcnt(1)\n\tv_mul_f32 v13, 0x3acccccd, v13\n\t"
                         SMF(24:27, 0:3, 112:119) "v_mul_f32 v12, 0x3acccccd, v12\n\t" SMF(28:31, 0:3, 136:143) "v_fma_f32 v12, -v13, v13, v12\n\t"
                         SMF(16:19, 4:7, 72:79) "v_maximum3_f32 v12, v12, 0, 0\n\t" SMF(20:23, 4:7, 96:103) "v_cvt_pk_bf16_f32 v12, 0, v12\n\t"
                         SMF(24:27, 4:7, 120:127) "v_add_f32 v12, 0x3727c5ac, v12\n\t" SMF(28:31, 4:7, 144:151) "v_cvt_pk_bf16_f32 v12, 0, v12\n\ts_waitcnt lgkmcnt(0)\n\t"
                         SMF(16:19, 8:11, 80:87) "v_sqrt_f32 v12, v12\n\t" SMF(20:23, 8:11, 104:111) "v_cvt_pk_bf16_f32 v12, 0, v12\n\t"
                         SMF(24:27, 8:11, 128:135) "v_rcp_f32 v12, v12\n\t" SMF(28:31, 8:11, 152:159)
                         "s_nop 2\n\tv_add_f32 v42, v16, v18\n\ts_nop 0\n\tv_add_f32 v43, v20, v22\n\tv_max_f32 v41, v42, v43\n\tv_add_f32 v44, v24, v26\n\tv_add_f32 v45, v28, v30\n\t"
                         "v_max3_f32 v41, v41, v44, v45\n\tv_cndmask_b32 v42, v42, v43, %8\n\tv_cndmask_b32 v44, v44, v45, %8\n\t"
                         MAXD("quad_perm:[1,0,3,2]") "v_cndmask_b32 v40, v42, v44, %9\n\ts_nop 0\n\t" MAXD("quad_perm:[2,3,0,1]") "s_nop 1\n\t" MAXD("row_ror:4") "s_nop 1\n\t" MAXD("row_ror:8")
                         "v_pk_mul_f32 v[40:41], v[12:13], v[40:41] op_sel_hi:[0,1]\n\tv_cvt_pk_bf16_f32 v40, 0, v40\n\tv_cvt_pk_bf16_f32 v41, 0, v41\n\t"
                         "v_pk_mul_f32 v[40:41], %10, v[40:41]\n\tv_cvt_pk_bf16_f32 v40, 0, v40\n\tv_cvt_pk_bf16_f32 v41, 0, v41\n\t"
                         "v_sub_f32 v40, v40, v41\n\tv_mul_f32 v40, 0x3fb8aa3b, v40\n\tv_exp_f32 v40, v40\n\ts_nop 0\n\tv_cvt_pk_bf16_f32 v40, v40, v40\n\tds_write_b16 %3, v40\n\t"
                         "ds_read_b128 v[0:3], %4\n\tds_read_b128 v[4:7], %4 offset:192\n\tds_read_b128 v[8:11], %4 offset:384\n\tds_read_b128 v[12:15], %4 offset:576\n\t"
                         "ds_read_b128 v[16:19], %4 offset:768\n\tds_read_b128 v[20:23], %4 offset:960\n\tds_read_b128 v[24:27], %4 offset:1152\n\tds_read_b128 v[28:31], %4 offset:1344\n\t"
                         "ds_write_b32 %5, v41\n\t"
                         "s_waitcnt lgkmcnt(8)\n\t" SMF(32:35, 0:3, 160:167) SMF(36:39, 0:3, 224:231) "s_waitcnt lgkmcnt(7)\n\t" SMF(32:35, 4:7, 168:175) SMF(36:39, 4:7, 232:239)
                         "s_waitcnt lgkmcnt(6)\n\t" SMF(32:35, 8:11, 176:183) SMF(36:39, 8:11, 240:247) "s_waitcnt lgkmcnt(4)\n\t" SMF(32:35, 12:15, 184:191) SMF(36:39, 12:15, 248:255)
                         SMF(32:35, 16:19, 192:199) "s_waitcnt lgkmcnt(1)\n\t" SMF(32:35, 20:23, 200:207) SMF(32:35, 24:27, 208:215) SMF(32:35, 28:31, 216:223)
                         "s_nop 2\n\tv_pk_add_f32 v[36:37], v[36:37], v[38:39]\n\tds_write_b64 %7, v[36:37]\n\ts_nop 2\n\tv_pk_add_f32 v[32:33], v[32:33], v[34:35]\n\tds_write_b64 %6, v[32:33]"
                         :: "v"(idx), "v"(qa), "v"(qm), "v"(pw), "v"(pr), "v"(ma), "v"(oa), "v"(oc), "s"(ck1), "s"(ck2), "s"(csc)
                         : "memory", "v0", "v1", "v2", "v3", "v4", "v5", "v6", "v7", "v8", "v9", "v10", "v11", "v12", "v13", "v14", "v15", "v16", "v17", "v18", "v19", "v20", "v21", "v22", "v23",
                           "v24", "v25", "v26", "v27", "v28", "v29", "v30", "v31", "v32", "v33", "v34", "v35", "v36", "v37", "v38", "v39", "v40", "v41", "v42", "v43", "v44", "v45");
#undef SMF
#undef MAXD
            STAMP(20 + 32 * layer);
            if (__builtin_expect(wave >= 3, 1)) BAR();
            else {
                __builtin_amdgcn_s_setprio(0);
                if (wave == 0) {
                    unsigned long long mk = 0x1111111111111111ull; PINS(mk);
                    BAR();
                    STAMP(5 + 32 * layer);
                    comb_o0(*(const LAS f32x4*)(size_t)cma, *(const LAS f32x4*)(size_t)(cma + 16), *(const LAS f32x4*)(size_t)cxa, *(const LAS f32x4*)(size_t)cra,
                            *(const LAS f32x4*)(size_t)(cra + 1024), *(const LAS f32x4*)(size_t)(cra + 2048), *(const LAS f32x4*)(size_t)(cra + 3072), cso, mk);
                } else if (wave == 1) {
                    unsigned long long mk = 0x0000111100001111ull; PINS(mk);
                    BAR();
                    comb_o1(*(const LAS f32x4*)(size_t)cma, *(const LAS f32x4*)(size_t)(cma + 16), *(const LAS f32x2*)(size_t)cxa, *(const LAS f32x4*)(size_t)cra, *(const LAS f32x4*)(size_t)(cra + 1024), cso, mk);
                } else {
                    BAR();
                    comb_ml(cma, cxa, cra, cso);
                }
                STAMP(21 + 32 * layer);
            }
        }
    };

    unsigned nj = wave == 3 ? WEARLY : 0u; int ord;
    {
        const int tid = threadIdx.x, lane = tid & 63;
        STAMP(0);
        unsigned pf = 0u, mb = 0u, tfl = 0u;
        const u16* kbase = kc + ((size_t)((gj >> 1) * HQ + gi) * SK + (SK - WIN) + (gj & 1) * 512) * DH;
        const u16* vbase = vc + ((size_t)((gj >> 1) * HQ + gi) * SK + (SK - WIN) + (gj & 1) * 512) * DH;
        if (wave == 7) {
            __builtin_amdgcn_s_setprio(3);
            for (int i = 0; i < 5; ++i) dma16p(x0, (unsigned)(i * 1024 + lane * 16), lb + L_X0 + i * 1024);
            pf = code_pf((g >> 3) % 10, lane);
            if (lane < 20) *reinterpret_cast<unsigned*>(smem + L_CNT + lane * 4) = lane == 16 ? tst : 0u;
            BAR();
            if (lane < 10) *reinterpret_cast<u32x4*>(smem + L_XS + 640 + lane * 16) = (u32x4){0x3f803f80u, 0x3f803f80u, 0x3f803f80u, 0x3f803f80u};
            if (lane < 16) { *reinterpret_cast<u32x4*>(smem + L_CS + 768 + lane * 16) = (u32x4){0u, 0u, 0u, 0u}; }
        } else {
            if (wave >= 4) {
                if (wave < 6) {
                    empty1(wave - 4, lane);
                    empty35(0, wave - 4, lane);
                } else {
                    __builtin_amdgcn_s_setprio(3);
                    if (gi + gj == 0) st4_sc1(ws, WS_R + (lane & 31) * 4, 0u);
                    st4_sc1(ws, WS_MB + (unsigned)((gi * 8 + gj) * 128 + (lane & 31) * 4), 0u);
                    empty4(0, 0, lane); empty4(0, 1, lane);
                }
            }
#pragma unroll 1
            for (int i = wave; i < 35; i += 7) {
                if (i >= 33) dma16p(wr, (unsigned)((i == 33 || lane < 16) ? (i - 33) * 1024 + lane * 16 : 0), lb + L_WR + (i - 33) * 1024);
                else {
                    const int c = i * 64 + lane; int row = c / 26, cc = c - 26 * row; if (cc > 23) cc = 23; if (row > 79) row = 79;
                    dma16(wq, (unsigned)(((gj * 80 + swz(row)) * QD + gi * DH + cc * 8) * 2), lb + L_WQ + i * 1024);
                }
            }
            BAR();
            *reinterpret_cast<u32x4*>(smem + L_P + tid * 16) = (u32x4){0u, 0u, 0u, 0u};
            if (tid < 72 * 12 - 448) *reinterpret_cast<u32x4*>(smem + L_P + 7168 + tid * 16) = (u32x4){0u, 0u, 0u, 0u};
            if (wave >= 4) {
                VMW(5);
                lds_add(32, 1u);
                if (wave == 6) {
#pragma unroll 1
                    for (unsigned spins = 0; spins < (kSpinCap << 4); ++spins) if (lds_word(lb + L_CNT + 32) >= 3u) break;
                    send_flag(lane);
                    tfl = now();
                    STAMP(17);
                    asm volatile("s_waitcnt vmcnt(1) lgkmcnt(0)" ::: "memory");
                } else asm volatile("s_waitcnt vmcnt(0) lgkmcnt(0)" ::: "memory");
                lds_add(0, 1u);
                if (wave == 6) {
                    until(__builtin_amdgcn_readfirstlane(tfl) + DMB);
#pragma unroll 1
                    for (unsigned spins = 0; spins < kSpinCap; ++spins) {
                        unsigned r = ld4_sc1(ws, WS_MB + (unsigned)((gi * 8 + gj) * 128 + (lane & 7) * 4));
                        WAIT1(r, 0);
                        if (none(r ^ 1u)) break;
                        if ((spins & 7u) == 7u) send_flag(lane);
                    }
                    lds_set(lb + L_CNT + 8, 1u);
                    if (gj == (gi & 7)) st4_sc1(ws, WS_R + (unsigned)(gi * 4), 1u);
                    STAMP(31);
                }
            }
        }
        u32x4 a[8];
        unsigned ko = (unsigned)((wave * 64 + (lane & 7)) * 384 + (((lane >> 3) & 1) * 4 + (lane >> 4)) * 16);
        PIN1(ko);
        auto k_group = [&](auto GC_) __attribute__((always_inline)) {
            constexpr int G = decltype(GC_)::value, R = RK + 24 * (G >> 1) + 4 * (G & 1);
            const unsigned o = ko + G * 3072;
            if (PRIO && G == 0) __builtin_amdgcn_s_setprio(3);
            if (PRIO && G == 4 && wave < 6) __builtin_amdgcn_s_setprio(2);
            pace(3);
            ld16_nt<R, 0>(kbase, o); ld16_nt<R + 8, 128>(kbase, o); ld16_nt<R + 16, 256>(kbase, o);
            const int lane = lane_id();
            const unsigned ho = WS_H1 + (unsigned)((gi * 4 + (gj >> 1)) * 7168 + 3584 + (lane < 48 ? lane : 0) * 16);
            if (wave < 7) {
                asm volatile("; COND_BEGIN");
                if (G == GL && wave < 4) {
                    asm volatile("s_waitcnt vmcnt(%0) lgkmcnt(0)" :: "i"(3 * GL + 3) : "memory");
                    lds_add(0, 1u);
                    if (wave < 2 && (EARLY >> wave & 1)) load_gu(wave, lane);
                    if (wave == 2 && (EARLY & 4)) load_wd(lane);
                    if (wave == 3) {
#pragma unroll 1
                        for (int k = 5 - WEARLY; k < 5; ++k) load_wo(k, lane, true);
                    }
                }
                if (wave == 6) {
                    if (gi + gj == 0) {
                        if (G == GE) mb = ld4_sc1(ws, WS_R + (unsigned)((lane & 31) * 4));
                        if (G == GF) {
                            WAIT1(mb, 3 * (GF - GE));
                            if (none(mb ^ 1u)) { answer(lane); lds_set(lb + L_CNT + 36, 1u); }
                        }
                    }
                    if (G == 7) mb = ld4_sc1(ws, WS_MB + (unsigned)((gi * 8 + (gi & 7)) * 128 + 32));
                }
                asm volatile("; COND_END");
            }
            if (wave == 7) {
                asm volatile("; COND_BEGIN");
                if (G == GP) {
                    VMW(3 * (GP + 1) + 1);
                    const unsigned char* p = smem + L_X0 + (lane >> 4) * 1280;
                    const float c0 = lo16(*reinterpret_cast<const u16*>(p));
                    const f32x2 c2 = {c0, c0};
                    f32x2 s2 = {0.f, 0.f}, q2 = s2;
#pragma unroll
                    for (int k = 0; k < 5; ++k) {
                        const u32x4 w = *reinterpret_cast<const u32x4*>(p + k * 256 + (lane & 15) * 16);
#pragma unroll
                        for (int e = 0; e < 4; ++e) { const f32x2 d = unpk(w[e]) - c2; s2 += d; q2 += d * d; }
                    }
                    const float s = row16_sum(s2[0] + s2[1]), qq = row16_sum(q2[0] + q2[1]);
                    const float ms = s * (1.f / DM);
                    const unsigned mv = pk2(c0 + ms, fmax2(qq - s * ms, 0.f) * (1.f / DM));
                    const float mb = lo16(mv), rden = frcp(bfr(__builtin_amdgcn_sqrtf(bfr(hi16(mv) + 1e-5f))));
                    const f32x2 mb2 = {mb, mb}, rd2 = {rden, rden};
                    if ((lane & 15) < 10) {
                        u32x4 w = *reinterpret_cast<const u32x4*>(p + gj * 160 + (lane & 15) * 16);
#pragma unroll
                        for (int e = 0; e < 4; ++e) w[e] = pk2v(unpk(pk2v(unpk(w[e]) - mb2)) * rd2);
                        *reinterpret_cast<u32x4*>(smem + L_XS + (lane >> 4) * 160 + (lane & 15) * 16) = w;
                    }
                    STAMP(30);
#pragma unroll 1
                    for (unsigned spins = 0; spins < (kSpinCap << 4); ++spins) {
                        if (lds_word(lb + L_CNT) >= 7u) break;
                        __builtin_amdgcn_s_sleep(1);
                    }
                    STAMP(26);
                    {
#pragma unroll 1
                        for (int i6 = 0; i6 < 6; ++i6) {
                            LANE_VARS();
                            const int blk = i6 < 3 ? i6 : i6 - 3;
                            const unsigned xb = lb + L_XS + (unsigned)((i6 < 3 ? p4 : 4) * 160);
                            Frag80 f[4];
#pragma unroll
                            for (int i = 0; i < 4; ++i) f[i] = frag80(lb + L_WQ + (unsigned)(128 * blk + 32 * i + 8 * p4), 416, r0s, r1s, r2s);
                            const Frag80 b = rows80(xb + kq * 16, xb + 128 + kq * 8);
                            const unsigned so = (i6 < 3 ? WS_H1 + (unsigned)(((gi * 4 + p4) * 8 + gj) * 896) : WS_OWN + (unsigned)((gi * 8 + gj) * 2048 + 1024)) + (unsigned)(256 * blk + q4 * 64 + kq * 16);
                            st16m(ws, so, tiles4(f, b, 0x000f000f000f000full), i6 < 3 ? ~0ull : 0x1111111111111111ull);
                            if (i6 == 2) { tfl = now(); STAMP(2); }
                        }
                    }
                    f32x2 r2 = {0.f, 0.f};
#pragma unroll
                    for (int k = 0; k < 5; ++k) r2 += unpk(*reinterpret_cast<const unsigned*>(smem + L_WR + k * 256 + lane_id() * 4));
                    const float sw = wave_sum(r2[0] + r2[1]);
                    *reinterpret_cast<float*>(smem + L_CNT + 12) = sw;
                    WAIT1(pf, 3 * (GP + 1) + 6);
                    pace(XCOST, false);
                }
                if (G == GC) {
#pragma unroll 1
                    for (unsigned spins = 0; spins < (kSpinCap << 8); ++spins) { if (lds_word(lb + L_CNT + 8)) break; __builtin_amdgcn_s_sleep(1); }
                    until(__builtin_amdgcn_readfirstlane(tfl) + DQ);
                    GATHER8(a, ho, 896);
                }
                if (G == GD) {
                    WAIT8(a, 3 * (GD - GC));
#pragma unroll 1
                    for (unsigned spins = 0; spins < kSpinCap && any_empty(UM32(a)); ++spins) { slip(0u); AGAIN8(a, ho, 896); WAIT8(a, 0); }
                    const f32x4 sq = SUM8(a);
                    unsigned* qp = reinterpret_cast<unsigned*>(smem + L_Q + (lane >> 2) * 32 + (lane & 3) * 4);
                    qp[0] = pk2(sq[0], sq[1]); qp[4] = pk2(sq[2], sq[3]);
                    asm volatile("s_waitcnt lgkmcnt(0)\n\tds_write_b32 %0, %1" :: "v"(lb + L_CNT + 4), "v"(1u) : "memory");
                    STAMP(27);
                }
                asm volatile("; COND_END");
            } else if (G == GC) { for (int k = 0; k < 8; ++k) asm volatile("" : "=v"(a[k])); }
        };
        k_group(IC<0>{}); k_group(IC<1>{}); k_group(IC<2>{}); k_group(IC<3>{}); k_group(IC<4>{}); k_group(IC<5>{}); k_group(IC<6>{}); k_group(IC<7>{});
        {
            const int lane = lane_id();
            const unsigned v0 = (unsigned)((wave * 64 + 4 * (lane >> 4)) * 384 + (lane & 15) * 16);
            const unsigned v1 = v0 + 256 - (unsigned)((lane & 15) * 8);
            auto v_group = [&](auto HC) __attribute__((always_inline)) {
                constexpr int h = decltype(HC)::value, R = h < 8 ? RV + 16 * (h & 3) + 4 * (h >> 2) : RV + 64 + 2 * (h - 8), NV = h < 8 ? 2 * h + 2 : 4 * h - 12;
                const unsigned o = (h < 8 ? v0 + (h >> 1) * 6144 : v1 + (h - 8) * 6144);
                if (PRIO && h == 0) __builtin_amdgcn_s_setprio(1);
                if (PRIO && h == 6) __builtin_amdgcn_s_setprio(0);
                pace(2);
                if (h < 8) { ld16_nt<R, (h & 1) * 768>(vbase, o); ld16_nt<R + 8, (h & 1) * 768 + 384>(vbase, o); }
                else { ld8_nt<R, 0>(vbase, o); ld8_nt<R + 8, 384>(vbase, o); ld8_nt<R + 16, 768>(vbase, o); ld8_nt<R + 24, 1152>(vbase, o); }
                if (h == 0 && wave == 6) {
                    asm volatile("; COND_BEGIN");
                    WAIT1(mb, 2);
                    const unsigned r = __builtin_amdgcn_readfirstlane(mb);
                    if (r) { adopt(r); STAMP(29); }
                    asm volatile("; COND_END");
                }
                if constexpr (h >= FX && h < FX + 6) {
                    constexpr int i = 2 * (h - FX);
                    fix_k1(IC<i>{}, IC<20 - 6 * (i / 3) - i % 3 + NV>{});
                    fix_k1(IC<i + 1>{}, IC<20 - 6 * ((i + 1) / 3) - (i + 1) % 3 + NV>{});
                }
                if (h == 1 && wave == 6 && gi + gj == 0) { asm volatile("; COND_BEGIN"); mb = ld4_sc1(ws, WS_R + (unsigned)((lane & 31) * 4)); asm volatile("; COND_END"); }
                if (h == 3 && wave == 6 && gi + gj == 0) {
                    asm volatile("; COND_BEGIN");
                    WAIT1(mb, 4);
                    if (none(mb ^ 1u) && !lds_word(lb + L_CNT + 36)) { answer(lane); lds_set(lb + L_CNT + 36, 1u); }
                    asm volatile("; COND_END");
                }
                if (h == FX + 5) {
                    STAMP(1);
                    if (!PRIO) __builtin_amdgcn_s_setprio(0);
                    att_qk0();
                }
            };
            v_group(IC<0>{}); v_group(IC<1>{}); v_group(IC<2>{}); v_group(IC<3>{}); v_group(IC<4>{}); v_group(IC<5>{});
            v_group(IC<6>{}); v_group(IC<7>{}); v_group(IC<8>{}); v_group(IC<9>{}); v_group(IC<10>{}); v_group(IC<11>{});
        }
    }
    {
        lds_add(16, 1u);
        STAMP(28);
        att_addr();
        const int lane = lane_id();
        cma = lb + L_M; cxa = lb + L_M + (unsigned)((lane >> 4) * 8); cra = lb + L_OP + (unsigned)(512 + (lane >> 4) * 2048 + (lane & 15) * 16);
        cso = WS_OWN + (unsigned)((gi * 8 + gj) * 2048 + 256 + (lane >> 5) * 64 + (lane & 15) * 4);
        PIN2(cma, cxa); PIN2(cra, cso);
        att_pv0();
    }
    {
        f32x4 m0, m1, o0, o1; f32x2 mm; unsigned r;
        asm volatile("s_mov_b64 exec, 1\n\tds_add_rtn_u32 %5, %6, %7 offset:%10\n\ts_mov_b64 exec, -1\n\t"
                     "ds_read_b128 %0, %6\n\tds_read_b128 %1, %6 offset:16\n\tds_read_b64 %2, %8\n\tds_read_b128 %3, %9\n\tds_read_b128 %4, %9 offset:1024\n\ts_waitcnt lgkmcnt(0)"
                     : "=&v"(m0), "=&v"(m1), "=&v"(mm), "=&v"(o0), "=&v"(o1), "=&v"(r)
                     : "v"(cma), "v"(1u), "v"(cxa), "v"(cra), "i"(L_CNT + 28 - L_M) : "memory");
        ord = __builtin_amdgcn_readfirstlane(r);
        if (ord == 7) { STAMP(5); comb_o1(m0, m1, mm, o0, o1, cso, 0x0000111100001111ull); lds_set(lb + L_CNT + 72, ~7u); lds_set(lb + L_CNT + 48, now() | 1u); STAMP(21); }
        const int lane = lane_id();
        auto wait_cnt = [&](int off, unsigned v) {
#pragma unroll 1
            for (unsigned spins = 0; spins < (kSpinCap << 4); ++spins) { if (lds_word(lb + L_CNT + off) >= v) break; __builtin_amdgcn_s_sleep(1); }
        };
        if (ord == 1) {
            wait_cnt(16, 8u);
            unsigned xa, ra; ml_addr(lane, xa, ra);
            comb_ml(cma, xa, ra, WS_OWN + (unsigned)((gi * 8 + gj) * 2048 + 384));
            STAMP(24);
        } else if (ord == 0 && !lds_word(lb + L_CNT + 52)) {
            if (gi + gj == 0 && !lds_word(lb + L_CNT + 36)) {
#pragma unroll 1
                for (unsigned spins = 0; spins < kSpinCap; ++spins) {
                    unsigned r = ld4_sc1(ws, WS_R + (unsigned)((lane & 31) * 4));
                    WAIT1(r, 0);
                    if (none(r ^ 1u)) break;
                    if ((spins & 7u) == 7u) send_flag(lane);
                }
                answer(lane);
            }
#pragma unroll 1
            for (unsigned spins = 0; spins < kSpinCap; ++spins) {
                unsigned r = ld4_sc1(ws, WS_MB + (unsigned)((gi * 8 + (gi & 7)) * 128 + 32));
                WAIT1(r, 0);
                r = __builtin_amdgcn_readfirstlane(r);
                if (r) { adopt(r); break; }
                if ((spins & 7u) == 7u) { send_flag(lane); if (gj == (gi & 7)) st4_sc1(ws, WS_R + (unsigned)(gi * 4), 1u); }
                __builtin_amdgcn_s_sleep(16);
            }
            STAMP(29);
        } else if (ord == 2) {
#pragma unroll 1
            for (unsigned spins = 0; spins < (kSpinCap << 6); ++spins) if (lds_word(lb + L_CNT + 24) >= 8u) break;
            const unsigned ra = lb + L_OP + (unsigned)((lane >> 5) * 4096 + (lane & 31) * 16);
            comb_o0(*(const LAS f32x4*)(size_t)cma, *(const LAS f32x4*)(size_t)(cma + 16), *(const LAS f32x4*)(size_t)(cma + (lane >> 5) * 16), *(const LAS f32x4*)(size_t)ra, *(const LAS f32x4*)(size_t)(ra + 1024),
                    *(const LAS f32x4*)(size_t)(ra + 2048), *(const LAS f32x4*)(size_t)(ra + 3072), WS_OWN + (unsigned)((gi * 8 + gj) * 2048 + lane * 4), 0x1111111111111111ull);
            STAMP(22);
        }
#pragma unroll 1
        for (;;) {
            unsigned j;
            asm volatile("s_mov_b64 exec, 1\n\tds_add_rtn_u32 %0, %1, %2\n\ts_mov_b64 exec, -1\n\ts_waitcnt lgkmcnt(0)" : "=&v"(j) : "v"(lb + L_CNT + 40), "v"(1u) : "memory");
            j = __builtin_amdgcn_readfirstlane(j);
            if (j >= 5u - WEARLY) break;
            load_wo((int)j, lane, false); ++nj;
        }
        STAMP(23);
    }

    auto chain = [&](auto LC) {
        constexpr int layer = decltype(LC)::value;
        const unsigned h4 = WS_H4 + (unsigned)((layer * 32 + gi) * 16384);
        if (wave < 3) {
            LANE_VARS();
            if (layer == 0) {
                VMW(0);
                lds_add(44, nj);
                const int c = wave * 64 + lane, row = c / 6, cc = c - 6 * row;
                unsigned o = (unsigned)((swz(row) * FF + cc * 8) * 2), o2 = (wave == 0 || lane < 32) ? o : 0u, d = lb + L_GU + wave * 1024;
                const u16* wb0 = wg + gj * 80 * FF + gi * 48;
                const u16* wb1 = wu + gj * 80 * FF + gi * 48;
                const u16* db = wd + gi * 48 * DM + gj * 80;
                const int pp = c / 10, c2 = c + 192, p2 = c2 / 10;
                unsigned fd = (unsigned)(swz(pp) * 1280 + (c - 10 * pp) * 16), f2 = (unsigned)(swz(p2) * 1280 + (c2 - 10 * p2) * 16), f3 = (wave < 2 || lane < 32) ? fd : 0u;
                PIN2(o, o2); PIN3(fd, f2, f3); PINS(wb0); PINS(wb1); PINS(d); PINS(db);
                auto late = [&](int m) {
                    if (m & 1) { dma16(wb0, o, d); dma16(wb0 + 32 * FF, o, d + 3072); if (wave < 2) dma16(wb0 + 64 * FF, o2, d + 6144); }
                    if (m & 2) { dma16(wb1, o, d + 8192); dma16(wb1 + 32 * FF, o, d + 8192 + 3072); if (wave < 2) dma16(wb1 + 64 * FF, o2, d + 8192 + 6144); }
                    if (m & 4) { dma16(db, fd, d + L_WD - L_GU); dma16(db + 32 * DM, f3, d + L_WD - L_GU + 5120); if (wave < 2) dma16(db, f2, d + L_WD - L_GU + 3072); }
                };
                if (GOPT) {
#pragma unroll 1
                    for (unsigned spins = 0; spins < (kSpinCap << 4); ++spins) if (lds_word(lb + L_CNT + 72) == ~3u) break;
                    late(GOPT);
                    STAMP(25);
                }
                BAR();
                late(GB4);
#pragma unroll 1
                for (unsigned spins = 0; spins < (kSpinCap << 4); ++spins) if (lds_word(lb + L_CNT + 76) >= 5u) break;
                late(GST);
                if (wave < 2) { empty1(wave, lane); empty35(1, wave, lane); VMW(3); } else { empty4(1, 0, lane); empty4(1, 1, lane); VMW(2); }
                if ((EARLY & 3) != 3) {
                    lds_add(20, 1u);
#pragma unroll 1
                    for (unsigned spins = 0; spins < (kSpinCap << 4); ++spins) if (lds_word(lb + L_CNT + 20) >= 3u) break;
                }
                STAMP(19);
            } else BAR();
            {
                const unsigned im = lb + L_GU + (unsigned)(32 * wave + 8 * p4);
                Frag80 fg = frag80(im, 96, r0s, r1s, r2s), fu = frag80(im + 8192, 96, r0s, r1s, r2s);
                const unsigned xb = lb + L_XS + (unsigned)((n < 4 ? n : 4) * 160);
                unsigned b16 = xb + kq * 16, b8 = xb + 128 + kq * 8;
                unsigned sg = h4 + (unsigned)((n < 4 ? wave * 4096 + gj * 512 + n * 64 : 12288 + wave * 1024 + gj * 128) + kq * 16), su = sg + (n < 4 ? 256u : 64u);
                unsigned o = h4 + (unsigned)(wave * 4096 + lane * 16);
                unsigned oc = h4 + (unsigned)(12288 + wave * 1024 + ((lane >> 5) * 4 + q4) * 128 + (kq & 1) * 64 + p4 * 16);
                unsigned os = h4 + (unsigned)(15360 + (p4 + 4 * (kq & 1)) * 128 + q4 * 16);
                unsigned ha = lb + L_H + (unsigned)(q4 * 96 + wave * 32 + p4 * 8 + kq * 2);
                f32x2 cs = {0.f, 0.f};
                if (layer != 0) cs = *reinterpret_cast<const f32x2*>(smem + L_CS2 + wave * 512 + lane * 8);
                PIN3(fg.a0, fg.a1, fg.a2); PIN3(fu.a0, fu.a1, fu.a2); PIN3(b16, b8, sg); PIN3(su, o, os); PIN2(ha, cs);
                unsigned long long mk = layer ? 0x000f000f000f000full : 0x001f001f001f001full;
                unsigned ta = WS_MB + (unsigned)((lane >> 1) * 1024 + 64 + (lane & 1) * 16);
                if (layer == 0) PIN2(oc, ta);
                PINS(mk);
                unsigned t3 = 0u;
                if (layer == 0) {
                    const u16* db = wd + gi * 48 * DM + gj * 80;
                    const int c = wave * 64 + lane, pp = c / 10, c2 = c + 192, p2 = c2 / 10;
                    const unsigned fd = (unsigned)(swz(pp) * 1280 + (c - 10 * pp) * 16), f2 = (unsigned)(swz(p2) * 1280 + (c2 - 10 * p2) * 16);
                    t3 = lds_word(lb + L_CNT + 64) + C3;
                    VMW(0);
                    if (!((EARLY | GOPT | GB4 | GST) & 4)) {
                        until(t3 + DWD);
                        dma16(db, fd, lb + L_WD + wave * 1024);
                        dma16(db + 32 * DM, (wave < 2 || lane < 32) ? fd : 0u, lb + L_WD + (5 + wave) * 1024);
                        if (wave < 2) dma16(db, f2, lb + L_WD + (3 + wave) * 1024);
                    }
                }
                BAR();
                STAMP(10 + 32 * layer);
                u32x4 tl = {0u, 0u, 0u, 0u};
                if (layer == 0) tl = ld16_sc1(ws, ta);
                const Frag80 b = rows80(b16, b8);
                const f32x4 g0 = mfma32(fg.a0, b.a0, z4), u0 = mfma32(fu.a0, b.a0, z4), g1 = mfma32(fg.a1, b.a1, g0), g2 = mfma16(fg.a2, b.a2, z4), u1 = mfma32(fu.a1, b.a1, u0), u2 = mfma16(fu.a2, b.a2, z4);
                __builtin_amdgcn_sched_barrier(0);
                const f32x4 cg = g1 + g2, cu = u1 + u2;
                st16m2<0>(ws, sg, asu(cg), su, asu(cu), mk);
                STAMP(11 + 32 * layer);
                if (layer == 0) {
                    const unsigned tn = now();
                    WAIT1(tl, 2);
                    unsigned d = min(min(min(tn - tl[0], tn - tl[1]), min(tn - tl[2], tn - tl[3])), 800u);
                    d = min(d, dppu<0xB1>(d)); d = min(d, dppu<0x4E>(d)); d = min(d, dppu<0x124>(d)); d = min(d, dppu<0x128>(d));
                    unsigned tr = tn - min(min((unsigned)__builtin_amdgcn_readlane(d, 0), (unsigned)__builtin_amdgcn_readlane(d, 16)), min((unsigned)__builtin_amdgcn_readlane(d, 32), (unsigned)__builtin_amdgcn_readlane(d, 48)));
                    if (!POLL3 && (int)(t3 - tr) >= M3) tr = t3 + D4S - D4A;
                    lds_set(lb + L_CNT + 56, tr | 1u);
                    lds_add(68, 1u);
                    if ((int)(tn + 12 - X4A - tr) > 0) tr = tn + 12 - X4A;
                    until(tr + X4A);
                } else sched(X4, 12);
                STAMP(16 + 32 * layer);
                float hv; f32x2 st;
                u32x4 a0, a1, a2, a3, ac, as;
                auto load = [&]() __attribute__((always_inline)) {
                    as = ld16_sc1(ws, os);
                    if (layer == 0) ac = ld16_sc1(ws, oc);
                    a0 = ld16_sc1<0>(ws, o); a1 = ld16_sc1<1024>(ws, o); a2 = ld16_sc1<2048>(ws, o); a3 = ld16_sc1<3072>(ws, o);
                    if (layer == 0) asm volatile("s_waitcnt vmcnt(0)" : "+v"(a0), "+v"(a1), "+v"(a2), "+v"(a3), "+v"(ac), "+v"(as) :: "memory");
                    else { asm volatile("s_waitcnt vmcnt(0)" : "+v"(a0), "+v"(a1), "+v"(a2), "+v"(a3), "+v"(as) :: "memory"); ac = as; }
                };
                auto compute = [&]() __attribute__((always_inline)) {
                    float ss, sm;
                    asm volatile("v_add_f32_dpp %0, %2, %2 quad_perm:[1,0,3,2] row_mask:0xf bank_mask:0xf\n\tv_add_f32_dpp %1, %3, %3 quad_perm:[1,0,3,2] row_mask:0xf bank_mask:0xf\n\ts_nop 0\n\t"
                                 "v_add_f32_dpp %0, %0, %0 quad_perm:[2,3,0,1] row_mask:0xf bank_mask:0xf\n\tv_add_f32_dpp %1, %1, %1 quad_perm:[2,3,0,1] row_mask:0xf bank_mask:0xf\n\ts_nop 0"
                                 : "=&v"(ss), "=&v"(sm) : "v"(as[0]), "v"(as[1]));
                    ss = x16_add(ss); sm = x16_add(sm);
                    st = ln_stats(ss, sm);
                    const f32x4 t = (asf(a0) + asf(a1)) + (asf(a2) + asf(a3));
                    const f32x2 gu = sw16(rs32(t[0], t[2]), rs32(t[1], t[3]));
                    if (layer == 0) {
                        cs = sw16(rs32(asf(ac)[0], asf(ac)[2]), rs32(asf(ac)[1], asf(ac)[3]));
                        cs[0] += dpp<0x124>(cs[0]); cs[1] += dpp<0x124>(cs[1]); cs[0] += dpp<0x128>(cs[0]); cs[1] += dpp<0x128>(cs[1]);
                    }
                    const unsigned gw = pk2v((gu - st[0] * cs) * st[1]);
                    const float gv = lo16(gw);
                    hv = bfr(gv * frcp(1.f + ex2(gv * -LOG2E))) * hi16(gw);
                    *(LAS u16*)(size_t)ha = (u16)pk2(hv, hv);
                    __builtin_amdgcn_sched_barrier(0);
                };
                RETRY(load(), max(max(max(um4(a0), um4(a1)), max(um4(a2), um4(a3))), max(um4(ac), um4(as))), compute(), any_nan(hv, hv), layer ? X4 : X4A);
                BAR();
                STAMP(12 + 32 * layer);
                if (layer == 0) {
                    *reinterpret_cast<f32x2*>(smem + L_CS2 + wave * 512 + lane * 8) = cs;
                }
            }
            if (layer == 0) {
                if (wave == 1) {
                    const unsigned o = WS_OWN + (unsigned)(gi * 16384 + 1024 + 2048 + (lane < 48 ? lane : 0) * 16);
                    u32x4 a[8];
                    a[0] = ld16_sc1<-2048>(ws, o); a[1] = ld16_sc1<0>(ws, o); a[2] = ld16_sc1<2048>(ws, o);
                    a[3] = ld16_sc1<-2048>(ws, o + 6144); a[4] = ld16_sc1<0>(ws, o + 6144); a[5] = ld16_sc1<2048>(ws, o + 6144);
                    a[6] = ld16_sc1<-2048>(ws, o + 12288); a[7] = ld16_sc1<0>(ws, o + 12288);
                    WAIT8(a, 0);
                    if (lane < 48) *reinterpret_cast<f32x4*>(smem + L_CS + lane * 16) = SUM8(a);
                }
                Frag80 f[4];
#pragma unroll
                for (int i = 0; i < 4; ++i) { f[i] = frag80(lb + L_WQ + (unsigned)(128 * wave + 32 * i + 8 * p4), 416, r0s, r1s, r2s); PIN3(f[i].a0, f[i].a1, f[i].a2); }
                unsigned b16 = lb + L_XS + (unsigned)(p4 * 160 + kq * 16), b8 = lb + L_XS + (unsigned)(p4 * 160 + 128 + kq * 8);
                unsigned so = WS_H1 + (unsigned)(((gi * 4 + p4) * 8 + gj) * 896 + 256 * wave + q4 * 64 + kq * 16);
                unsigned long long mk = 0x000f000f000f000full;
                PIN3(b16, b8, so); PINS(mk);
                BAR();
                STAMP(33);
                const Frag80 b = rows80(b16, b8);
                st16_sc1(ws, so, tiles4(f, b, mk));
                STAMP(34);
                qk_addr(true); att_addr();
                cma = lb + L_M; cxa = lb + L_M + (unsigned)(wave == 0 ? (lane >> 5) * 16 : (lane >> 4) * 8);
                cra = lb + L_OP + (unsigned)(wave == 0 ? (lane >> 5) * 4096 + (lane & 31) * 16 : 512 + (lane >> 4) * 2048 + (lane & 15) * 16);
                if (wave == 2) ml_addr(lane, cxa, cra);
                cso = WS_OWN + (unsigned)((gi * 8 + gj) * 2048 + 512) + (unsigned)(wave == 0 ? lane * 4 : wave == 1 ? 256 + (lane >> 5) * 64 + (lane & 15) * 4 : 384);
                PIN2(cma, cxa); PIN2(cra, cso);
            }
        } else {
            const int t = wave - 3;
            StatAddr sa;
            auto gather32 = [&](unsigned o, int lane, unsigned X, auto mid, auto fin) __attribute__((always_inline)) {
                u32x4 a0 = ld16_sc1<-2048>(ws, o), a1 = ld16_sc1<-1024>(ws, o), a2 = ld16_sc1<0>(ws, o), a3 = ld16_sc1<1024>(ws, o), sel;
                const unsigned one = (lane & 1) ? 0x3f800000u : 0x3f80u;
#pragma unroll
                for (int k = 0; k < 4; ++k) sel[k] = ((lane >> 1) & 3) == k ? one : 0u;
                unsigned long long hi = 0xff00ff00ff00ff00ull;
                PIN1(sel); PINS(hi);
                mid();
                float v;
                auto sum = [&]() __attribute__((always_inline)) {
                    f32x4 d; float w;
                    asm volatile("s_waitcnt vmcnt(3)\n\tv_mfma_f32_16x16x32_bf16 %0, %1, %5, 0\n\ts_waitcnt vmcnt(2)\n\tv_mfma_f32_16x16x32_bf16 %0, %2, %5, %0\n\t"
                                 "s_waitcnt vmcnt(1)\n\tv_mfma_f32_16x16x32_bf16 %0, %3, %5, %0\n\ts_waitcnt vmcnt(0)\n\tv_mfma_f32_16x16x32_bf16 %0, %4, %5, %0"
                                 : "=&v"(d), "+v"(a0), "+v"(a1), "+v"(a2), "+v"(a3) : "v"(sel) : "memory");
                    asm volatile("s_nop 7\n\tv_cndmask_b32 %0, %2, %3, %6\n\tv_cndmask_b32 %1, %4, %5, %6\n\ts_nop 1\n\tv_permlane32_swap_b32 %0, %1\n\tv_add_f32 %0, %0, %1"
                                 : "=&v"(v), "=&v"(w) : "v"(d[0]), "v"(d[1]), "v"(d[2]), "v"(d[3]), "s"(hi));
                    fin(v);
                    __builtin_amdgcn_sched_barrier(0);
                };
                sum();
                if (__builtin_expect(any_nan(v, v) != 0ull, 0)) {
#pragma unroll 1
                    for (unsigned spins = 0; spins < kSpinCap; ++spins) {
                        slip(X);
                        again16<-2048>(a0, ws, o); again16<-1024>(a1, ws, o); again16<0>(a2, ws, o); again16<1024>(a3, ws, o);
                        asm volatile("s_waitcnt vmcnt(0)" : "+v"(a0), "+v"(a1), "+v"(a2), "+v"(a3) :: "memory");
                        if (!any_empty(max(max(um4(a0), um4(a1)), max(um4(a2), um4(a3))))) break;
                    }
                    sum();
                }
            };
            auto pack4 = [&](float c0, float c1, float c2, float c3) __attribute__((always_inline)) { const unsigned w0 = min(pk2(c0, c1), 0xfffffffeu), w1 = min(pk2(c2, c3), 0xfffffffeu); return (u32x2){w0, w1}; };
            unsigned xw;
            __builtin_amdgcn_s_setprio(0);
            {
                LANE_VARS();
                if (layer == 0) xe = lo16(*reinterpret_cast<const u16*>(smem + L_X0 + kq * 1280 + gj * 160 + t * 32 + n * 2));
                STAMP(6 + 32 * layer);
                unsigned lo = WS_OWN + (unsigned)(gi * 16384 + layer * 512 + (lane >> 3) * 2048 + t * 128 + (t == 3 ? 0 : (lane & 7) * 16));
                unsigned lw = lb + L_O2 + (unsigned)((lane >> 3) * 416 + t * 128 + (t == 3 ? 0 : (lane & 7) * 16));
                PIN2(lo, lw);
                u32x4 ov = {~0u, ~0u, ~0u, ~0u};
                if (layer != 0 && t < 4) { sched(X2, 20); STAMP(49); ov = ld16_sc1(ws, lo); }
                const unsigned wa = lb + L_WO + (unsigned)(r0s * 160 + 32 * t + 8 * p4), wa1 = lb + L_WO + (unsigned)(r1s * 160 + 32 * t + 8 * p4);
                bf16x8 af[6];
                if (layer == 0) {
                    if (t < 4) {
                        unsigned tf = 0u;
#pragma unroll 1
                        for (unsigned spins = 0; spins < (kSpinCap << 4); ++spins) { tf = lds_word(lb + L_CNT + 48); if (tf) break; }
                        slot(NUNIT + XG2);
                        until(tf + DG2);
                        ov = ld16_sc1(ws, lo);
                        if (GOPT) lds_add(72, 1u);
                        if (nj) { WAIT1(ov, 1); lds_add(44, nj); }
                    } else { VMW(0); lds_add(44, nj); }
#pragma unroll 1
                    for (unsigned spins = 0; spins < (kSpinCap << 4); ++spins) if (lds_word(lb + L_CNT + 44) >= 5u) break;
                    STAMP(8);
                }
#pragma unroll
                for (int s = 0; s < 6; ++s) af[s] = cat8(tr64(wa + s * 32 * 160), tr64(wa1 + s * 32 * 160));
                const unsigned h3 = WS_H35 + (unsigned)((gj * 20 + 10 * layer + t) * 4096);
                unsigned so = h3 + (unsigned)(gi * 128 + (lane & 2) * 32 + (lane & 4) * 8 + kq * 8), go = h3 + (unsigned)(2048 + lane * 16);
                unsigned xs = lb + L_XS + (unsigned)(kq * 160 + t * 32 + n * 2);
                unsigned t3 = layer ? 0u : lds_word(lb + L_CNT + 64) + C3;
                unsigned br = lb + L_O2 + (unsigned)((n & 7) * 416 + kq * 16), mr = lb + L_O2 + (unsigned)((n & 7) * 416 + 384);
                unsigned long long mk = 0x0055005500550055ull;
                PIN3(af[0], af[1], af[2]); PIN3(af[3], af[4], af[5]); PIN3(br, mr, so); PIN2(go, xs); PINS(mk); PINS(t3);
                if (t < 4) {
                    WAIT1(ov, 0);
                    *(LAS u32x4*)(size_t)lw = ov;
                    if (layer == 0 ? any_empty(um4(ov)) : __builtin_expect(any_empty(um4(ov)), 0)) {
#pragma unroll 1
                        for (unsigned spins = 0; spins < kSpinCap; ++spins) {
                            if (layer != 0) slip(X2);
                            ov = ld16_sc1(ws, lo);
                            WAIT1(ov, 0);
                            *(LAS u32x4*)(size_t)lw = ov;
                            if (!any_empty(um4(ov))) break;
                            if (layer == 0) { if ((spins & 15u) == 15u) send_flag(lane); __builtin_amdgcn_s_sleep(1); }
                        }
                    }
                }
                BAR();
                bf16x8 bf[6];
                const f32x2 ml = *(const LAS f32x2*)(size_t)mr;
#pragma unroll
                for (int s = 0; s < 6; ++s) bf[s] = lds16(br + s * 64);
                __builtin_amdgcn_sched_barrier(0);
                f32x4 ca = z4;
#pragma unroll
                for (int s = 0; s < 6; ++s) ca = mfma32(af[s], bf[s], ca);
                const float e = ex2(ml[0] - fmax2(ml[0], dpp<0xB1>(ml[0]))), el = e * ml[1];
                const float f = e * frcp(el + dpp<0xB1>(el));
                float c0, c1, c2, c3;
                asm volatile("s_nop 7\n\t"
                             "v_mul_f32 %0, %4, %8\n\tv_mul_f32 %1, %5, %8\n\tv_mul_f32 %2, %6, %8\n\tv_mul_f32 %3, %7, %8\n\t"
                             "v_add_f32_dpp %0, %0, %0 quad_perm:[1,0,3,2] row_mask:0xf bank_mask:0xf\n\tv_add_f32_dpp %1, %1, %1 quad_perm:[1,0,3,2] row_mask:0xf bank_mask:0xf\n\t"
                             "v_add_f32_dpp %2, %2, %2 quad_perm:[1,0,3,2] row_mask:0xf bank_mask:0xf\n\tv_add_f32_dpp %3, %3, %3 quad_perm:[1,0,3,2] row_mask:0xf bank_mask:0xf"
                             : "=&v"(c0), "=&v"(c1), "=&v"(c2), "=&v"(c3) : "v"(ca[0]), "v"(ca[1]), "v"(ca[2]), "v"(ca[3]), "v"(f));
                st8m(ws, so, pack4(c0, c1, c2, c3), mk);
                if (layer == 0 && wave == 7) st4_sc1(ws, WS_MB + (unsigned)(gi * 1024 + 64 + gj * 4), now());
                if (layer == 0) lds_add(76, 1u);
                STAMP(7 + 32 * layer);
                if (layer != 0) sched(X3, 12);
                else {
                    if (POLL3) wait_word(ws, h3 + (unsigned)(((gi * 7 + t * 5 + 3) & 31) * 128));
                    else {
                        const unsigned n2 = now() + 20;
                        until((int)(n2 - t3) > 0 ? n2 : t3);
                    }
                }
                STAMP(18 + 32 * layer);
                gather32(go, lane, layer ? X3 : 0u, [&]() { sa = stats_addr(lane, h4 + (unsigned)(15360 + gj * 128), 16); }, [&](float s) { xw = pk2(xe + bfr(s), 0.f); *(LAS u16*)(size_t)xs = (u16)xw; });
                xe = lo16(xw);
                STAMP(9 + 32 * layer);
            }
            BAR();
            if (wave == 3) stats(sa);
            STAMP(11 + 32 * layer);
            {
                LANE_VARS();
                const unsigned im = lb + L_WD + (unsigned)(32 * t + 8 * p4);
                if (layer == 0 && !((EARLY | GOPT | GB4 | GST) & 4)) {
#pragma unroll 1
                    for (unsigned spins = 0; spins < (kSpinCap << 4); ++spins) { if (lds_word(lb + L_CNT + 68) >= 3u) break; __builtin_amdgcn_s_sleep(1); }
                }
                bf16x8 wd0 = cat8(tr64(im + r0s * 160), tr64(im + r1s * 160)); bf16x4 wd1 = tr64(im + (32 + r2s) * 160);
                const unsigned h5 = WS_H35 + (unsigned)((gj * 20 + 10 * layer + 5 + t) * 4096);
                unsigned h16 = lb + L_H + (unsigned)(p4 * 96 + kq * 16), h8 = lb + L_H + (unsigned)(p4 * 96 + 64 + kq * 8);
                unsigned so = h5 + (unsigned)(gi * 128 + (lane & 1) * 64 + (lane & 2) * 16 + kq * 8);
                unsigned long long mk = 0x000f000f000f000full;
                PIN2(wd0, wd1); PIN3(h16, h8, so); PINS(mk);
                if (wave == 4) {
                    const unsigned os = h4 + (unsigned)(15360 + (p4 + 4 * (kq & 1)) * 128 + q4 * 16);
                    if (layer == 0) {
#pragma unroll 1
                        for (unsigned spins = 0; spins < (kSpinCap << 4); ++spins) if (lds_word(lb + L_CNT + 56)) break;
                    }
                    sched(layer ? X4 : X4A, 0);
                    u32x4 as;
#pragma unroll 1
                    for (unsigned spins = 0; spins < kSpinCap; ++spins) { as = ld16_sc1(ws, os); WAIT1(as, 0); if (!any_empty(um4(as))) break; }
                    float ss = __uint_as_float(as[0]), sm = __uint_as_float(as[1]), rd = __uint_as_float(as[2]);
                    ss += dpp<0xB1>(ss); sm += dpp<0xB1>(sm); rd += dpp<0xB1>(rd);
                    ss += dpp<0x4E>(ss); sm += dpp<0x4E>(sm); rd += dpp<0x4E>(rd);
                    ss = x16_add(ss); sm = x16_add(sm); rd = x16_add(rd);
                    if (LANES(0x1111ull)) *reinterpret_cast<f32x4*>(smem + L_ST + q4 * 16) = (f32x4){ss, sm, rd, 0.f};
                }
                BAR();
                STAMP(12 + 32 * layer);
                const bf16x8 hb0 = lds16(h16); const bf16x4 hb1 = lds8(h8);
                __builtin_amdgcn_sched_barrier(0);
                const f32x4 c = mfma32(wd0, hb0, z4) + mfma16(wd1, hb1, z4);
                st8m(ws, so, pack4(c[0], c[1], c[2], c[3]), mk);
                STAMP(13 + 33 * layer);
                if (layer == 0 || gi == 4 * t + (gj & 3)) {
                    float rs, n1;
                    auto prep = [&]() __attribute__((always_inline)) {
                        const f32x4 sr = *reinterpret_cast<const f32x4*>(smem + L_ST + kq * 16);
                        const float sw = *reinterpret_cast<const float*>(smem + L_CNT + 12);
                        const f32x2 st = ln_stats(sr[0], sr[1]);
                        const float r = bfr((sr[2] - st[0] * sw) * st[1]);
                        rs = bfr(frcp(1.f + ex2(r * -LOG2E)));
                        n1 = bfr(bfr(xe - st[0]) * st[1]);
                        PIN2(rs, n1);
                    };
                    if (layer == 0) {
                        unsigned go = h5 + (unsigned)(2048 + lane * 16), xs = lb + L_XS + (unsigned)(kq * 160 + t * 32 + n * 2);
                        PIN2(go, xs);
                        sched(X5A, 8);
                        STAMP(15);
                        gather32(go, lane, X5A, [&]() { prep(); sa = stats_addr(lane, WS_H1 + (unsigned)((gi * 32 + gj) * 896 + 768), 7168); },
                                 [&](float y) { xw = pk2(xe + bfr(n1 + bfr(bfr(y) * rs)), 0.f); *(LAS u16*)(size_t)xs = (u16)xw; });
                        xe = lo16(xw);
                        STAMP(14);
                    } else {
                        unsigned go = h5 + (unsigned)(2048 + lane * 16), oo = (unsigned)(kq * 1280 + gj * 160 + t * 32 + n * 2);
                        PIN2(go, oo);
                        sched(X5B, 8);
                        STAMP(47);
                        gather32(go, lane, X5B, prep, [&](float y) { xw = pk2(xe + bfr(n1 + bfr(bfr(y) * rs)), 0.f); });
                        { const unsigned w = xw | (dppu<0xF5>(xw) << 16);
                          const u32x2 w2 = {w, dppu<0xAA>(w)};
                          asm volatile("s_mov_b64 exec, %3\n\tglobal_store_dwordx2 %0, %1, %2 sc0 sc1\n\ts_mov_b64 exec, -1" :: "v"(oo), "v"(w2), "s"(out), "s"(0x1111111111111111ull) : "memory"); }
                        STAMP(45);
                    }
                }
            }
            if (layer == 0) {
                BAR();
                if (wave == 3) { stats(sa); STAMP(34); }
                asm volatile("" : "=v"(cma), "=v"(cxa), "=v"(cra), "=v"(cso));
                if (wave != 7) { qk_addr(true); att_addr(); }
                else {
                    const int lane = lane_id();
                    unsigned o = WS_H1 + (unsigned)((gi * 4 + (gj >> 1)) * 7168 + 3584 + (lane < 48 ? lane : 48) * 16), qw;
                    f32x4 cs;
                    PIN1(o);
                    sched(X1, 26);
                    STAMP(51);
                    f32x4 s; u32x4 a[8];
                    RETRY8(a, o, 896, s, { qw = lb + L_Q + (unsigned)((lane >> 2) * 32 + (lane & 3) * 4); cs = *reinterpret_cast<const f32x4*>(smem + L_CS + lane * 16); PIN2(qw, cs); qk_addr(true); att_addr(); }, { s -= bfr(__int_as_float(__builtin_amdgcn_readlane(__float_as_int(s[1]), 48)) * (1.f / DM)) * cs;
                                           u32x2 w; w[0] = pk2(s[0], s[1]); w[1] = pk2(s[2], s[3]);
                                           if (LANES(0xffff000000000000ull)) { w[0] = __float_as_uint(s[0]); w[1] = __float_as_uint(s[1]); }
                                           *(LAS unsigned*)(size_t)qw = w[0]; *(LAS unsigned*)(size_t)(qw + 16) = w[1]; __builtin_amdgcn_sched_barrier(0); }, any_nan(s[0] + s[2], s[1] + s[3]), X1);
                }
            }
        }
    };
    PINS(ck1); PINS(ck2); PINS(csc);
    chain(IC<0>{});
    STAMP(35);
    att1();
    chain(IC<1>{});
}

__device__ __attribute__((used, noinline)) void mega_code_end() { asm volatile(".Lmega_code_end:"); }

extern "C" void kernel_launch(void* const* d_in, const int* in_sizes, int n_in, void* d_out, int out_size, void* d_ws,
                              size_t ws_size, hipStream_t stream) {
    const u16* x0 = (const u16*)d_in[0];
    const u16* kc = (const u16*)d_in[1];
    const u16* vc = (const u16*)d_in[2];
    const u16* wq = (const u16*)d_in[3];
    const u16* wo = (const u16*)d_in[4];
    const u16* wr = (const u16*)d_in[6];
    const u16* wg = (const u16*)d_in[7];
    const u16* wu = (const u16*)d_in[8];
    const u16* wd = (const u16*)d_in[9];
    (void)hipFuncSetAttribute((const void*)mega_kernel, hipFuncAttributeMaxDynamicSharedMemorySize, L_END);
    mega_kernel<<<256, 512, L_END, stream>>>(x0, kc, vc, wq, wo, wr, wg, wu, wd, (u16*)d_out, (unsigned char*)d_ws);
}
```

```cpp
#define D4A 168
#define D5A 119
#define D1 127
#define D2 177
#define D3 91
#define D4 111
#define D5B 107
#include <hip/hip_runtime.h>
#include <hip/hip_bf16.h>
#include <stdint.h>

typedef unsigned short u16;
typedef unsigned u32x4 __attribute__((ext_vector_type(4)));
typedef unsigned u32x2 __attribute__((ext_vector_type(2)));
typedef float f32x4 __attribute__((ext_vector_type(4)));
typedef short s16x4 __attribute__((ext_vector_type(4)));
typedef __attribute__((ext_vector_type(4))) __bf16 bf16x4;
typedef __attribute__((ext_vector_type(8))) __bf16 bf16x8;
#define LAS __attribute__((address_space(3)))

constexpr int DM = 640, HQ = 32, SK = 4096, DH = 192, WIN = 1024, FF = 1536;
constexpr int QD = HQ * DH;
constexpr float SCALE = 0.07216878364870322f, LOG2E = 1.44269504088896341f;
constexpr unsigned kSpinCap = 1u << 16;
#define GJ(g) (((g) >> 3) & 7)
#define GI(g) (((g) & 7) * 4 + ((g) >> 6))

#define PACE 62
#define PINIT 100
#define PMIN 50
constexpr unsigned X4A = D4A, X5A = X4A + D5A, X1 = X5A + D1, X2 = X1 + D2, X3 = X2 + D3, X4 = X3 + D4, X5B = X4 + D5B;
#define WOC 2
#define GUC 3
#define DG2 15
#define DGU 25
#define EARLY 7
#define WEARLY 1
#define WEC 3
#define XCOST 16
constexpr unsigned NUNIT = 384 + XCOST + 9 * ((EARLY & 1) + ((EARLY >> 1) & 1) + ((EARLY >> 2) & 1)) + 2 * WEC * WEARLY, PBURST = 30;
#define GSTEPS GP = 0, GC = 3, GD = 5, GL = 0, GE = 2, GF = 4
#define C3 (PINIT + NUNIT * PACE / 16 + E3)
#define DWD 20
#define ADOPT 1
#define TOFF 15
#define FX 0
#define PRIO 0
#define GOPT 0
#define GB4 0
#define GST 0
#define POLL3 1
#define M3 20
#define E3 330
#define D4S 125
#define XG2 0
#define WOFF 6
#define FREEW(w) ((w) >= 6)
#define LEAD 12
#define DQ 40
#define DMB 30
constexpr int GSTEPS;
constexpr int QKT = 2;

constexpr unsigned WS_R = 0;
constexpr unsigned WS_B = 1024;
constexpr unsigned WS_MB = 4096;
constexpr unsigned WS_H1 = WS_MB + 256 * 128;
constexpr unsigned WS_OWN = WS_H1 + 32 * 4 * 7168;
constexpr unsigned WS_H35 = WS_OWN + 256 * 2048;
constexpr unsigned WS_H4 = WS_H35 + 160 * 4096;
constexpr unsigned WS_END = WS_H4 + 64 * 16384;
constexpr unsigned WS_DBG = 6680576;
static_assert(WS_END <= WS_DBG, "ws map");

constexpr int L_WQ = 0;
constexpr int L_WO = 33792;
constexpr int L_GU = L_WO + 30720;
constexpr int L_WD = L_GU + 16384;
constexpr int L_X0 = L_WD + 8192;
constexpr int L_WR = L_X0 + 5120;
constexpr int L_XS = L_WR + 2048;
constexpr int L_Q = L_XS + 1024;
constexpr int L_OP = L_Q + 512;
constexpr int L_M = L_OP + 8192;
constexpr int L_H = L_M + 64;
constexpr int L_ST = L_H + 384;
constexpr int L_CS = L_ST + 64;
constexpr int L_CS2 = L_CS + 1024;
constexpr int L_CNT = L_CS2 + 1536;
constexpr int L_P = L_CNT + 80;
constexpr int L_O2 = L_P + 72 * 192;
constexpr int L_DBG = L_O2 + 3328;
constexpr int L_END = L_DBG + 2048;
static_assert(L_END <= 163840 && L_END > 81920, "LDS");

typedef __attribute__((ext_vector_type(2))) __bf16 bf16x2;
typedef float f32x2 __attribute__((ext_vector_type(2)));
__device__ __forceinline__ unsigned pk2(float a, float b) { return __builtin_bit_cast(unsigned, __builtin_convertvector((f32x2){a, b}, bf16x2)); }
__device__ __forceinline__ float lo16(unsigned w) { return __uint_as_float(w << 16); }
__device__ __forceinline__ float hi16(unsigned w) { return __uint_as_float(w & 0xffff0000u); }
__device__ __forceinline__ float bfr(float f) { return __uint_as_float(pk2(0.f, f)); }
template <int CTRL> __device__ __forceinline__ float dpp(float x) {
    return __builtin_bit_cast(float, __builtin_amdgcn_mov_dpp(__builtin_bit_cast(int, x), CTRL, 0xf, 0xf, true));
}
template <int CTRL> __device__ __forceinline__ unsigned dppu(unsigned x) { return (unsigned)__builtin_amdgcn_mov_dpp((int)x, CTRL, 0xf, 0xf, true); }
__device__ __forceinline__ float row16_sum(float v) {
    v += dpp<0xB1>(v); v += dpp<0x4E>(v); v += dpp<0x141>(v); v += dpp<0x140>(v); return v;
}
__device__ __forceinline__ float fmax2(float a, float b) { return __builtin_elementwise_maximum(a, b); }
__device__ __forceinline__ float fmax3(float a, float b, float c) { return fmax2(fmax2(a, b), c); }
__device__ __forceinline__ float x16_add(float v) { auto s = __builtin_amdgcn_permlane16_swap(__float_as_uint(v), __float_as_uint(v), false, false); return __uint_as_float(s[0]) + __uint_as_float(s[1]); }
__device__ __forceinline__ float x32_add(float v) { auto s = __builtin_amdgcn_permlane32_swap(__float_as_uint(v), __float_as_uint(v), false, false); return __uint_as_float(s[0]) + __uint_as_float(s[1]); }
__device__ __forceinline__ float wave_sum(float v) { return x32_add(x16_add(row16_sum(v))); }
__device__ __forceinline__ float rs32(float a, float b) { auto s = __builtin_amdgcn_permlane32_swap(__float_as_uint(a), __float_as_uint(b), false, false); return __uint_as_float(s[0]) + __uint_as_float(s[1]); }
__device__ __forceinline__ f32x2 sw16(float a, float b) { auto s = __builtin_amdgcn_permlane16_swap(__float_as_uint(a), __float_as_uint(b), false, false); return (f32x2){__uint_as_float(s[0]), __uint_as_float(s[1])}; }

#define BAR() asm volatile("s_waitcnt lgkmcnt(0)\n\ts_barrier" ::: "memory")
#define VMW(n) asm volatile("s_waitcnt vmcnt(%0)" :: "i"(n) : "memory")

template <int OFF = 0> __device__ __forceinline__ u32x4 ld16_sc1(const void* sbase, unsigned voff) {
    u32x4 r; asm volatile("global_load_dwordx4 %0, %1, %2 offset:%3 sc1" : "=v"(r) : "v"(voff), "s"(sbase), "i"(OFF) : "memory"); return r;
}
__device__ __forceinline__ unsigned ld4_sc1(const void* sbase, unsigned voff) {
    unsigned r; asm volatile("global_load_dword %0, %1, %2 sc1" : "=v"(r) : "v"(voff), "s"(sbase) : "memory"); return r;
}
template <int OFF = 0> __device__ __forceinline__ void st16_sc1(void* sbase, unsigned voff, u32x4 v) {
    asm volatile("global_store_dwordx4 %0, %1, %2 offset:%3 sc1\n\ts_nop 1" :: "v"(voff), "v"(v), "s"(sbase), "i"(OFF) : "memory");
}
__device__ __forceinline__ void st4_sc1(void* sbase, unsigned voff, unsigned v) {
    asm volatile("global_store_dword %0, %1, %2 sc1" :: "v"(voff), "v"(v), "s"(sbase) : "memory");
}
__device__ __forceinline__ void st8(void* sbase, unsigned voff, u32x2 v) {
    asm volatile("global_store_dwordx2 %0, %1, %2" :: "v"(voff), "v"(v), "s"(sbase) : "memory");
}
__device__ __forceinline__ void st2(void* sbase, unsigned voff, unsigned v) {
    asm volatile("global_store_short %0, %1, %2" :: "v"(voff), "v"(v), "s"(sbase) : "memory");
}
constexpr int RK = 64, RV = 160;
template <int R, int IMM> __device__ __forceinline__ void ld16_nt(const void* sbase, unsigned voff) {
    asm volatile("global_load_dwordx4 v[%c3:%c4], %0, %1 offset:%2 nt" :: "v"(voff), "s"(sbase), "i"(IMM), "i"(R), "i"(R + 3) : "memory");
}
template <int R, int IMM> __device__ __forceinline__ void ld8_nt(const void* sbase, unsigned voff) {
    asm volatile("global_load_dwordx2 v[%c3:%c4], %0, %1 offset:%2 nt" :: "v"(voff), "s"(sbase), "i"(IMM), "i"(R), "i"(R + 1) : "memory");
}
template <int R> __device__ __forceinline__ void smf(f32x4& d, u32x4 a, unsigned idx) {
    asm volatile("v_smfmac_f32_16x16x64_bf16 %0, %1, v[%c3:%c4], %2" : "+v"(d) : "v"(a), "v"(idx), "i"(R), "i"(R + 7));
}
template <int A, int B> __device__ __forceinline__ void zip_in() {
    unsigned t;
    asm volatile("v_perm_b32 %0, v%c2, v%c1, %3\n\tv_perm_b32 v%c2, v%c2, v%c1, %4\n\tv_mov_b32 v%c1, %0" : "=&v"(t) : "i"(A), "i"(B), "s"(0x05040100u), "s"(0x07060302u));
}
template <int A, int B> __device__ __forceinline__ void zip_out(unsigned& lo, unsigned& hi) {
    asm volatile("v_perm_b32 %0, v%c3, v%c2, %4\n\tv_perm_b32 %1, v%c3, v%c2, %5" : "=&v"(lo), "=&v"(hi) : "i"(A), "i"(B), "s"(0x05040100u), "s"(0x07060302u));
}
template <int A, int B, int C, int D> __device__ __forceinline__ void zip_x(unsigned lo, unsigned hi) {
    asm volatile("v_perm_b32 v%c4, v%c3, v%c2, %6\n\tv_perm_b32 v%c5, v%c3, v%c2, %7\n\tv_mov_b32 v%c2, %0\n\tv_mov_b32 v%c3, %1"
                 :: "v"(lo), "v"(hi), "i"(A), "i"(B), "i"(C), "i"(D), "s"(0x05040100u), "s"(0x07060302u));
}
__device__ __forceinline__ void dma16(const void* sbase, unsigned voff, unsigned lds_dst) {
    unsigned keep;
    asm volatile("s_mov_b32 %0, m0\n\ts_mov_b32 m0, %3\n\ts_nop 0\n\tglobal_load_lds_dwordx4 %1, %2 nt\n\ts_mov_b32 m0, %0"
                 : "=&s"(keep) : "v"(voff), "s"(sbase), "s"(lds_dst) : "memory");
}
__device__ __forceinline__ void dma16p(const void* sbase, unsigned voff, unsigned lds_dst) {
    unsigned keep;
    asm volatile("s_mov_b32 %0, m0\n\ts_mov_b32 m0, %3\n\ts_nop 0\n\tglobal_load_lds_dwordx4 %1, %2\n\ts_mov_b32 m0, %0"
                 : "=&s"(keep) : "v"(voff), "s"(sbase), "s"(lds_dst) : "memory");
}
__device__ __forceinline__ unsigned um4(u32x4 a) { return max(max(a[0], a[1]), max(a[2], a[3])); }
__device__ __forceinline__ bool none(unsigned t) { return __builtin_amdgcn_ballot_w64(t != 0u) == 0ull; }
__device__ __forceinline__ bool any_empty(unsigned mx) { return __builtin_amdgcn_ballot_w64(mx == ~0u) != 0ull; }
__device__ __forceinline__ unsigned long long any_nan(float a, float b) { return __builtin_amdgcn_ballot_w64(__builtin_isunordered(a, b)); }
__device__ __forceinline__ f32x4 asf(u32x4 a) { return __builtin_bit_cast(f32x4, a); }
__device__ __forceinline__ u32x4 asu(f32x4 a) { return __builtin_bit_cast(u32x4, a); }
#define LANES(m) __builtin_amdgcn_inverse_ballot_w64(m)

__device__ __forceinline__ bf16x4 tr64(unsigned lds_addr) { return __builtin_amdgcn_ds_read_tr16_b64_v4bf16((LAS bf16x4*)(size_t)lds_addr); }
__device__ __forceinline__ bf16x8 cat8(bf16x4 a, bf16x4 b) { return __builtin_shufflevector(a, b, 0, 1, 2, 3, 4, 5, 6, 7); }
__device__ __forceinline__ bf16x8 lds16(unsigned a) { return *(const LAS bf16x8*)(size_t)a; }
__device__ __forceinline__ bf16x4 lds8(unsigned a) { return *(const LAS bf16x4*)(size_t)a; }
__device__ __forceinline__ f32x4 mfma32(bf16x8 a, bf16x8 b, f32x4 c) { return __builtin_amdgcn_mfma_f32_16x16x32_bf16(a, b, c, 0, 0, 0); }
__device__ __forceinline__ f32x4 mfma32u(u32x4 a, u32x4 b, f32x4 c) { return mfma32(__builtin_bit_cast(bf16x8, a), __builtin_bit_cast(bf16x8, b), c); }
__device__ __forceinline__ f32x4 mfma16(bf16x4 a, bf16x4 b, f32x4 c) {
    return __builtin_amdgcn_mfma_f32_16x16x16bf16_1k(__builtin_bit_cast(s16x4, a), __builtin_bit_cast(s16x4, b), c, 0, 0, 0);
}

#define STAMP(i) do {} while (0)

__device__ __forceinline__ void wait_word(const void* sbase, unsigned off) {
    unsigned t, cnt = kSpinCap;
    asm volatile("1:\n\tglobal_load_dword %0, %2, %3 sc1\n\ts_sub_u32 %1, %1, 1\n\ts_waitcnt vmcnt(0)\n\tv_cmp_eq_u32 vcc, -1, %0\n\ts_cbranch_vccz 2f\n\ts_cbranch_scc0 1b\n2:"
                 : "=&v"(t), "+s"(cnt) : "v"(off), "s"(sbase) : "memory", "vcc", "scc");
}
template <int OFF = 0> __device__ __forceinline__ void st16m(void* sbase, unsigned voff, u32x4 v, unsigned long long mask) {
    asm volatile("s_mov_b64 exec, %4\n\tglobal_store_dwordx4 %0, %1, %2 offset:%3 sc1\n\ts_mov_b64 exec, -1\n\ts_nop 0" :: "v"(voff), "v"(v), "s"(sbase), "i"(OFF), "s"(mask) : "memory");
}
__device__ __forceinline__ void st8m(void* sbase, unsigned voff, u32x2 v, unsigned long long mask) {
    asm volatile("s_mov_b64 exec, %3\n\tglobal_store_dwordx2 %0, %1, %2 sc1\n\ts_mov_b64 exec, -1" :: "v"(voff), "v"(v), "s"(sbase), "s"(mask) : "memory");
}
template <int OFF> __device__ __forceinline__ void st16m2(void* sbase, unsigned voff, u32x4 v, unsigned voff2, u32x4 v2, unsigned long long mask) {
    asm volatile("s_mov_b64 exec, %6\n\tglobal_store_dwordx4 %0, %1, %2 sc1\n\tglobal_store_dwordx4 %4, %5, %2 offset:%3 sc1\n\ts_mov_b64 exec, -1\n\ts_nop 0"
                 :: "v"(voff), "v"(v), "s"(sbase), "i"(OFF), "v"(voff2), "v"(v2), "s"(mask) : "memory");
}
#define PINS(a) asm volatile("" : "+s"(a))
#define RETRY(LOAD, EMPTY, COMPUTE, NAN_, X) do { LOAD; COMPUTE; if (__builtin_expect((NAN_) != 0ull, 0)) { \
    _Pragma("unroll 1") for (unsigned spins_ = 0; spins_ < kSpinCap; ++spins_) { slip(X); LOAD; if (!any_empty(EMPTY)) break; } COMPUTE; } } while (0)
__device__ __forceinline__ unsigned now() { return (unsigned)__builtin_amdgcn_s_memrealtime(); }
__device__ __forceinline__ void until(unsigned t) {
    asm volatile("1:\n\ts_memrealtime vcc\n\ts_waitcnt lgkmcnt(0)\n\ts_sub_u32 vcc_lo, %0, vcc_lo\n\ts_cmp_lt_u32 vcc_lo, 0x400\n\ts_cbranch_scc1 1b" :: "s"(t - 1u) : "vcc", "scc", "memory");
}
__device__ __forceinline__ unsigned lds_word(unsigned a) {
    unsigned t_; asm volatile("ds_read_b32 %0, %1\n\ts_waitcnt lgkmcnt(0)\n\ts_nop 0" : "=v"(t_) : "v"(a) : "memory");
    return __builtin_amdgcn_readfirstlane(t_);
}
__device__ __forceinline__ void lds_set(unsigned a, unsigned v) { asm volatile("ds_write_b32 %0, %1" :: "v"(a), "v"(v) : "memory"); }
__device__ __forceinline__ int swz(int r) { return r ^ (((r >> 3) & 1) << 2); }
__device__ __forceinline__ f32x2 unpk(unsigned w) { return (f32x2){lo16(w), hi16(w)}; }
__device__ __forceinline__ unsigned pk2v(f32x2 v) { return __builtin_bit_cast(unsigned, __builtin_convertvector(v, bf16x2)); }
__device__ __forceinline__ float frcp(float x) { return __builtin_amdgcn_rcpf(x); }
__device__ __forceinline__ float ex2(float x) { return __builtin_amdgcn_exp2f(x); }
__device__ __forceinline__ int lane_id() { int l; asm volatile("v_mbcnt_lo_u32_b32 %0, -1, 0\n\tv_mbcnt_hi_u32_b32 %0, -1, %0" : "=v"(l)); return l & 63; }
__device__ __forceinline__ f32x2 ln_stats(float ss, float s) {
    const float mean = s * (1.f / DM);
    const float var = fmax2(__builtin_fmaf(-mean, mean, ss * (1.f / DM)), 0.f);
    const unsigned mv = pk2(mean, var);
    return (f32x2){lo16(mv), frcp(bfr(__builtin_amdgcn_sqrtf(bfr(hi16(mv) + 1e-5f))))};
}
#define GATHER8(a, o, S) do { a[0] = ld16_sc1<-4 * (S)>(ws, o); a[1] = ld16_sc1<-3 * (S)>(ws, o); a[2] = ld16_sc1<-2 * (S)>(ws, o); a[3] = ld16_sc1<-(S)>(ws, o); \
    a[4] = ld16_sc1<0>(ws, o); a[5] = ld16_sc1<(S)>(ws, o); a[6] = ld16_sc1<2 * (S)>(ws, o); a[7] = ld16_sc1<3 * (S)>(ws, o); } while (0)
template <int OFF> __device__ __forceinline__ void again16(u32x4& r, const void* sbase, unsigned voff) {
    asm volatile("v_cmp_eq_u32 vcc, -1, %1\n\ts_mov_b64 exec, vcc\n\tglobal_load_dwordx4 %0, %2, %3 offset:%4 sc1\n\ts_mov_b64 exec, -1" : "+v"(r) : "v"(um4(r)), "v"(voff), "s"(sbase), "i"(OFF) : "memory", "vcc");
}
#define AGAIN8(a, o, S) do { again16<-4 * (S)>(a[0], ws, o); again16<-3 * (S)>(a[1], ws, o); again16<-2 * (S)>(a[2], ws, o); again16<-(S)>(a[3], ws, o); \
    again16<0>(a[4], ws, o); again16<(S)>(a[5], ws, o); again16<2 * (S)>(a[6], ws, o); again16<3 * (S)>(a[7], ws, o); } while (0)
#define WAIT8(a, N) asm volatile("s_waitcnt vmcnt(%8)" : "+v"((a)[0]), "+v"((a)[1]), "+v"((a)[2]), "+v"((a)[3]), "+v"((a)[4]), "+v"((a)[5]), "+v"((a)[6]), "+v"((a)[7]) : "i"(N) : "memory")
#define UM32(a) max(max(max(um4(a[0]), um4(a[1])), max(um4(a[2]), um4(a[3]))), max(max(um4(a[4]), um4(a[5])), max(um4(a[6]), um4(a[7]))))
#define SUM8(a) (((((((asf(a[0]) + asf(a[1])) + asf(a[2])) + asf(a[3])) + asf(a[4])) + asf(a[5])) + asf(a[6])) + asf(a[7]))
#define WAIT1(x, N) asm volatile("s_waitcnt vmcnt(%1)" : "+v"(x) : "i"(N) : "memory")
#define LO2(x) __builtin_shufflevector(x, x, 0, 1)
#define HI2(x) __builtin_shufflevector(x, x, 2, 3)
#define ACC8(a, s) do { f32x2 l_, h_; \
    asm volatile("s_waitcnt vmcnt(5)\n\tv_pk_add_f32 %0, %2, %4\n\tv_pk_add_f32 %1, %3, %5\n\tv_pk_add_f32 %0, %0, %6\n\tv_pk_add_f32 %1, %1, %7\n\t" \
                 "s_waitcnt vmcnt(3)\n\tv_pk_add_f32 %0, %0, %8\n\tv_pk_add_f32 %1, %1, %9\n\tv_pk_add_f32 %0, %0, %10\n\tv_pk_add_f32 %1, %1, %11\n\t" \
                 "s_waitcnt vmcnt(1)\n\tv_pk_add_f32 %0, %0, %12\n\tv_pk_add_f32 %1, %1, %13\n\tv_pk_add_f32 %0, %0, %14\n\tv_pk_add_f32 %1, %1, %15\n\t" \
                 "s_waitcnt vmcnt(0)\n\tv_pk_add_f32 %0, %0, %16\n\tv_pk_add_f32 %1, %1, %17\n\ts_nop 0" \
                 : "=&v"(l_), "=&v"(h_) : "v"(LO2(a[0])), "v"(HI2(a[0])), "v"(LO2(a[1])), "v"(HI2(a[1])), "v"(LO2(a[2])), "v"(HI2(a[2])), "v"(LO2(a[3])), "v"(HI2(a[3])), \
                   "v"(LO2(a[4])), "v"(HI2(a[4])), "v"(LO2(a[5])), "v"(HI2(a[5])), "v"(LO2(a[6])), "v"(HI2(a[6])), "v"(LO2(a[7])), "v"(HI2(a[7])) : "memory"); \
    s = __builtin_shufflevector(l_, h_, 0, 1, 2, 3); } while (0)
#define RETRY8(a, o, S, s, MID, POST, NAN_, X) do { GATHER8(a, o, S); MID; ACC8(a, s); POST; if (__builtin_expect((NAN_) != 0ull, 0)) { \
    _Pragma("unroll 1") for (unsigned spins_ = 0; spins_ < kSpinCap; ++spins_) { slip(X); AGAIN8(a, o, S); WAIT8(a, 0); if (!any_empty(UM32(a))) break; } s = SUM8(a); POST; } } while (0)
#define PIN1(a) asm volatile("" : "+v"(a))
#define PIN2(a, b) asm volatile("" : "+v"(a), "+v"(b))
#define PIN3(a, b, c) asm volatile("" : "+v"(a), "+v"(b), "+v"(c))
#define LANE_VARS() const int lane = lane_id(); \
    const int kq = lane >> 4, n = lane & 15, q4 = n >> 2, p4 = lane & 3; \
    const int r0s = 8 * kq + q4 + 4 * (kq & 1), r1s = 8 * kq + q4 + 4 * ((kq & 1) ^ 1);     \
    const int r2s = swz(4 * kq + q4);                                                       \
    (void)r0s; (void)r1s; (void)r2s; (void)p4
template <int V> struct IC { static constexpr int value = V; };
struct Frag80 { bf16x8 a0, a1; bf16x4 a2; };

__global__ void __launch_bounds__(512, 2) __attribute__((amdgpu_num_vgpr(32)))
mega_kernel(const u16* __restrict__ x0, const u16* __restrict__ kc, const u16* __restrict__ vc, const u16* __restrict__ wq,
            const u16* __restrict__ wo, const u16* __restrict__ wr, const u16* __restrict__ wg, const u16* __restrict__ wu,
            const u16* __restrict__ wd, u16* __restrict__ out, unsigned char* __restrict__ ws) {
    extern __shared__ __attribute__((aligned(16))) unsigned char smem[];
    const unsigned lb = (unsigned)(size_t)smem;
    const int wave = __builtin_amdgcn_readfirstlane(threadIdx.x >> 6);
    const int g = blockIdx.x, gj = GJ(g), gi = GI(g);
    const f32x4 z4 = {0.f, 0.f, 0.f, 0.f};
    asm volatile("" ::: "v255");
    auto sched = [&](unsigned X, unsigned gap) {
        unsigned v; unsigned long long c;
        asm volatile("s_memrealtime %1\n\tds_read_b32 %0, %2\n\ts_waitcnt lgkmcnt(0)" : "=v"(v), "=&s"(c) : "v"(lb + L_CNT + 56) : "memory");
        const unsigned n = (unsigned)c + gap;
        unsigned t = __builtin_amdgcn_readfirstlane(v) + X;
        if ((int)(n - t) > 0) t = n;
        until(t);
    };
    auto slip = [&](unsigned X) {
        if (X) lds_set(lb + L_CNT + 56, now() - X);
    };
    float xe = 0.f;

    auto code_pf = [&](int slice, int lane) {
        unsigned long long pc0; unsigned len, back, t;
        asm volatile("s_getpc_b64 %0\n.Lmega_pf%=:\n\ts_mov_b32 %1, .Lmega_code_end-.Lmega_code_begin\n\ts_mov_b32 %2, .Lmega_pf%=-.Lmega_code_begin" : "=s"(pc0), "=s"(len), "=s"(back));
        unsigned off = (unsigned)(slice * 4096 + lane * 64); if (off > len) off = len;
        asm volatile("global_load_dword %0, %1, %2" : "=v"(t) : "v"(off & ~3u), "s"(pc0 - back) : "memory");
        return t;
    };
    asm volatile(".Lmega_code_begin:");
    auto fix_k1 = [&](auto IC_, auto NC) {
        constexpr int A = RK + 8 * decltype(IC_)::value, B = A + 4;
        unsigned t0, t1, t2, t3;
        asm volatile("s_waitcnt vmcnt(%12)\n\tv_mov_b32 %0, v%c4\n\tv_mov_b32 %1, v%c5\n\tv_mov_b32 %2, v%c6\n\tv_mov_b32 %3, v%c7\n\t"
                     "v_mov_b32_dpp v%c4, v%c8 row_ror:8 row_mask:0xf bank_mask:0xc\n\tv_mov_b32_dpp v%c5, v%c9 row_ror:8 row_mask:0xf bank_mask:0xc\n\t"
                     "v_mov_b32_dpp v%c6, v%c10 row_ror:8 row_mask:0xf bank_mask:0xc\n\tv_mov_b32_dpp v%c7, v%c11 row_ror:8 row_mask:0xf bank_mask:0xc\n\t"
                     "v_mov_b32_dpp v%c8, %0 row_ror:8 row_mask:0xf bank_mask:0x3\n\tv_mov_b32_dpp v%c9, %1 row_ror:8 row_mask:0xf bank_mask:0x3\n\t"
                     "v_mov_b32_dpp v%c10, %2 row_ror:8 row_mask:0xf bank_mask:0x3\n\tv_mov_b32_dpp v%c11, %3 row_ror:8 row_mask:0xf bank_mask:0x3"
                     : "=&v"(t0), "=&v"(t1), "=&v"(t2), "=&v"(t3)
                     : "i"(A), "i"(A + 1), "i"(A + 2), "i"(A + 3), "i"(B), "i"(B + 1), "i"(B + 2), "i"(B + 3), "i"(decltype(NC)::value) : "memory");
    };
    auto trans_v = [&](auto UC, auto NC) {
        constexpr int R = RV + 4 * decltype(UC)::value, N = decltype(NC)::value;
#define CELL(r, c) R + 16 * (r) + (c), R + 16 * (r) + 8 + (c)
        unsigned a0, a1, b0, b1, c0, c1;
        VMW(N + 6); zip_in<CELL(0, 0)>(); zip_out<CELL(0, 1)>(a0, a1); zip_out<CELL(0, 2)>(b0, b1); zip_out<CELL(0, 3)>(c0, c1);
        VMW(N + 4); zip_x<CELL(1, 0), CELL(0, 1)>(a0, a1); zip_in<CELL(1, 1)>(); zip_out<CELL(1, 2)>(a0, a1);
        VMW(N + 2); zip_x<CELL(2, 0), CELL(0, 2)>(b0, b1); zip_x<CELL(2, 1), CELL(1, 2)>(a0, a1); zip_in<CELL(2, 2)>(); zip_out<CELL(1, 3)>(a0, a1); zip_out<CELL(2, 3)>(b0, b1);
        VMW(N); zip_x<CELL(3, 0), CELL(0, 3)>(c0, c1); zip_x<CELL(3, 1), CELL(1, 3)>(a0, a1); zip_x<CELL(3, 2), CELL(2, 3)>(b0, b1); zip_in<CELL(3, 3)>();
#undef CELL
    };
    auto trans_w = [&](auto UC, auto NC) {
        constexpr int R = RV + 64 + 2 * decltype(UC)::value, N = decltype(NC)::value;
        unsigned a0, a1;
        VMW(N + 2); zip_in<R, R + 8>(); zip_out<R + 1, R + 9>(a0, a1);
        VMW(N); zip_x<R + 16, R + 24, R + 1, R + 9>(a0, a1); zip_in<R + 17, R + 25>();
    };
    auto frag80 = [&](unsigned im, int rs, int r0s, int r1s, int r2s) -> Frag80 {
        return Frag80{cat8(tr64(im + r0s * rs), tr64(im + r1s * rs)), cat8(tr64(im + (r0s + 32) * rs), tr64(im + (r1s + 32) * rs)), tr64(im + (r2s + 64) * rs)};
    };
    auto mm80 = [&](const Frag80& a, const Frag80& b) -> f32x4 {
        return mfma32(a.a1, b.a1, mfma32(a.a0, b.a0, z4)) + mfma16(a.a2, b.a2, z4);
    };
    auto rows80 = [&](unsigned a16, unsigned a8) -> Frag80 { const Frag80 f{lds16(a16), lds16(a16 + 64), lds8(a8)}; __builtin_amdgcn_sched_barrier(0); return f; };
    auto empty35 = [&](int layer, int part, int lane) {
        const unsigned t = WS_H35 + (unsigned)(((gj * 20 + 10 * layer + 8 * part + (lane >> 3)) * 32 + gi) * 128 + (lane & 7) * 16);
        st16m(ws, (part == 0 || lane < 16) ? t : WS_OWN + (unsigned)((gi * 8 + gj) * 2048 + layer * 512 + (lane - 16) * 16), (u32x4){~0u, ~0u, ~0u, ~0u}, part ? 0xffffffffffffull : ~0ull);
    };
    auto empty4 = [&](int layer, int part, int lane) {
        st16m(ws, WS_H4 + (unsigned)((layer * 32 + gi) * 16384) + (unsigned)((part == 0 || lane < 32) ? (part * 2 + (lane >> 5)) * 4096 + gj * 512 + (lane & 31) * 16
                                                                                           : 12288 + ((lane - 32) >> 3) * 1024 + gj * 128 + (lane & 7) * 16), (u32x4){~0u, ~0u, ~0u, ~0u},
              layer && part ? 0xff000000ffffffffull : ~0ull);
    };
    const unsigned tst = now();
    const int lead = FREEW(wave) ? LEAD * PACE * 4 : 0;
    auto pace = [&](unsigned n, bool wait = true) {
        unsigned u, w, t0;
        asm volatile("s_mov_b64 exec, 1\n\tds_add_rtn_u32 %0, %3, %4\n\tds_max_u32 %3, %5 offset:12\n\tds_add_rtn_u32 %1, %3, %6 offset:12\n\tds_read_b32 %2, %3 offset:4\n\ts_mov_b64 exec, -1\n\ts_waitcnt lgkmcnt(0)"
                     : "=&v"(u), "=&v"(w), "=&v"(t0) : "v"(lb + L_CNT + 60), "v"(n), "v"((now() - tst) << 6), "v"(n * PMIN * 4u) : "memory");
        int tn = (int)(__builtin_amdgcn_readfirstlane(t0) - tst + PINIT) * 64 + __builtin_amdgcn_readfirstlane(u) * (PACE * 4);
        const int tw = __builtin_amdgcn_readfirstlane(w) - PBURST * 64;
        if (tw > tn) tn = tw;
        tn -= lead;
        if (wait) {
#pragma unroll 1
            for (int k = 0; k < 2048 && (int)((now() - tst) << 6) < tn; ++k) {}
        }
    };
    auto slot = [&](unsigned u) {
        const unsigned tn = lds_word(lb + L_CNT + 64) + PINIT + u * PACE / 16;
#pragma unroll 1
        for (int k = 0; k < 8192 && (int)(now() - tn) < 0; ++k) __builtin_amdgcn_s_sleep(1);
    };
    auto load_wo = [&](int k, int lane, bool early) {
        const int c = k * 64 + lane, pp = c / 10, cc = c - 10 * pp;
        const unsigned f = (unsigned)(swz(pp) * 1280 + cc * 16);
        const u16* wb = wo + (size_t)gi * DH * DM + gj * 80;
#pragma unroll
        for (int a2 = 0; a2 < 6; ++a2) { if (a2 % 3 == 0) { if (early) pace(WEC); else slot(NUNIT - WOFF + (2 * k + a2 / 3) * WOC); } dma16(wb + 32 * a2 * DM, f, lb + L_WO + (5 * a2 + k) * 1024); }
    };
    auto load_wd = [&](int lane) {
        const u16* db = wd + gi * 48 * DM + gj * 80;
#pragma unroll 1
        for (int k = 0; k < 5; ++k) {
            const int c = k * 64 + lane, pp = c / 10, cc = c - 10 * pp;
            const unsigned fd = (unsigned)(swz(pp) * 1280 + cc * 16);
            if (!(k & 1)) pace(3);
            dma16(db, fd, lb + L_WD + k * 1024);
            if (k < 3) dma16(db + 32 * DM, (k < 2 || lane < 32) ? fd : 0u, lb + L_WD + (5 + k) * 1024);
        }
    };
    auto load_gu = [&](int m, int lane) {
#pragma unroll
        for (int k = 0; k < 3; ++k) {
            const int c = k * 64 + lane, row = c / 6, cc = c - 6 * row;
            const unsigned o = (unsigned)((swz(row) * FF + cc * 8) * 2), d = lb + L_GU + m * 8192 + k * 1024;
            const u16* wb = (m == 0 ? wg : wu) + gj * 80 * FF + gi * 48;
            pace(3);
            dma16(wb, o, d); dma16(wb + 32 * FF, o, d + 3072);
            if (k < 2) dma16(wb + 64 * FF, (k == 0 || lane < 32) ? o : 0u, d + 6144);
        }
    };
    auto lds_add = [&](int off, unsigned v) { asm volatile("s_mov_b64 exec, 1\n\tds_add_u32 %0, %1\n\ts_mov_b64 exec, -1" :: "v"(lb + L_CNT + off), "v"(v) : "memory"); };
    auto send_flag = [&](int lane) {
        st4_sc1(ws, WS_MB + (unsigned)((gi * 8 + (lane & 7)) * 128 + gj * 4), 1u);
    };
    auto tiles4 = [&](const Frag80 (&f)[4], const Frag80& b, unsigned long long mk) __attribute__((always_inline)) -> u32x4 {
        f32x4 c[4], d[4];
#pragma unroll
        for (int i = 0; i < 4; ++i) c[i] = mfma32(f[i].a0, b.a0, z4);
#pragma unroll
        for (int i = 0; i < 4; ++i) { c[i] = mfma32(f[i].a1, b.a1, c[i]); d[i] = mfma16(f[i].a2, b.a2, z4); }
        __builtin_amdgcn_sched_barrier(0);
        f32x2 lo, hi;
        asm volatile("s_nop 1\n\ts_mov_b64 exec, %18\n\tv_pk_add_f32 %0, %2, %10\n\tv_pk_add_f32 %1, %3, %11\n\t"
                     "s_lshl_b64 exec, exec, 4\n\tv_pk_add_f32 %0, %4, %12\n\tv_pk_add_f32 %1, %5, %13\n\t"
                     "s_lshl_b64 exec, exec, 4\n\tv_pk_add_f32 %0, %6, %14\n\tv_pk_add_f32 %1, %7, %15\n\t"
                     "s_lshl_b64 exec, exec, 4\n\tv_pk_add_f32 %0, %8, %16\n\tv_pk_add_f32 %1, %9, %17\n\ts_mov_b64 exec, -1"
                     : "=&v"(lo), "=&v"(hi) : "v"(LO2(c[0])), "v"(HI2(c[0])), "v"(LO2(c[1])), "v"(HI2(c[1])), "v"(LO2(c[2])), "v"(HI2(c[2])), "v"(LO2(c[3])), "v"(HI2(c[3])),
                       "v"(LO2(d[0])), "v"(HI2(d[0])), "v"(LO2(d[1])), "v"(HI2(d[1])), "v"(LO2(d[2])), "v"(HI2(d[2])), "v"(LO2(d[3])), "v"(HI2(d[3])), "s"(mk) : "scc");
        return asu(__builtin_shufflevector(lo, hi, 0, 1, 2, 3));
    };
    auto empty1 = [&](int half, int lane) {
        const unsigned o = WS_H1 + (unsigned)(((gi * 4 + 2 * half) * 8 + gj) * 896 + (lane < 56 ? lane : 55) * 16);
        st16_sc1(ws, o, (u32x4){~0u, ~0u, ~0u, ~0u}); st16_sc1(ws, o + 7168, (u32x4){~0u, ~0u, ~0u, ~0u});
    };
    auto answer = [&](int lane) {
        st4_sc1(ws, WS_MB + (unsigned)(((lane & 31) * 8 + (lane & 7)) * 128 + 32), (tst + TOFF) | 1u);
    };
    auto adopt = [&](unsigned t) {
        if (ADOPT && t - tst + 256u < 512u) lds_set(lb + L_CNT + 64, t);
        lds_set(lb + L_CNT + 52, 1u);
    };
    struct StatAddr { unsigned a16, b16, a8, b8, dst; };
    auto stats_addr = [&](int lane, unsigned dbase, int dstride) -> StatAddr {
        const int kq = lane >> 4, q4 = (lane >> 2) & 3, p4 = lane & 3;
        const int x1 = LANES(0x4444444444444444ull) ? L_WR + gj * 160 : L_XS + q4 * 160, xa = LANES(0xaaaaaaaaaaaaaaaaull) ? L_XS + 640 : x1;
        StatAddr r{lb + (unsigned)(xa + kq * 16), lb + (unsigned)(L_XS + p4 * 160 + kq * 16), lb + (unsigned)(xa + 128 + kq * 8), lb + (unsigned)(L_XS + p4 * 160 + 128 + kq * 8), dbase + (unsigned)(kq * dstride)};
        PIN3(r.a16, r.b16, r.dst); PIN2(r.a8, r.b8);
        return r;
    };
    auto stats = [&](const StatAddr& r) {
        const bf16x8 a0 = lds16(r.a16), b0 = lds16(r.b16), a1 = lds16(r.a16 + 64), b1 = lds16(r.b16 + 64);
        const bf16x4 a2 = lds8(r.a8), b2 = lds8(r.b8);
        unsigned long long mk = 0x0008000400020001ull; PINS(mk);
        __builtin_amdgcn_sched_barrier(0);
        st16m(ws, r.dst, asu(mfma32(a1, b1, mfma32(a0, b0, z4)) + mfma16(a2, b2, z4)), mk);
    };
    unsigned qa, pw, ma, idx, qm;
    auto qk_addr = [&](bool L1) {
        LANE_VARS();
        qa = lb + L_Q + (unsigned)(kq * 32 + (lane & 2) * 8);
        pw = lb + L_P + (unsigned)((7 + 8 * wave) * 192 + (kq >> 1) * 64 + (n >> 3) * 32 + (lane & 2) * 8 + (q4 & 1) * 8 + (kq & 1) * 4 + (lane & 1) * 2);
        ma = lb + L_M + (unsigned)(wave * 4);
        idx = (lane & 2) ? 0xeeeeu : 0x4444u;
        qm = lb + L_Q;
        PIN3(qa, pw, ma); PIN2(idx, qm);
        if (L1 && wave < 4) __builtin_amdgcn_s_setprio(2);
    };
    auto att_qk0 = [&]() {
        qk_addr(false);
#pragma unroll 1
        for (unsigned spins = 0; spins < (kSpinCap << 4); ++spins) {
            if (lds_word(lb + L_CNT + 4) != 0u) break;
            __builtin_amdgcn_s_sleep(1);
        }
        STAMP(3);
        const u32x4 q0 = *(const LAS u32x4*)(size_t)qa, q1 = *(const LAS u32x4*)(size_t)(qa + 128), q2 = *(const LAS u32x4*)(size_t)(qa + 256);
        f32x4 c0 = z4, c1 = z4, c2 = z4, c3 = z4;
#define SMF(c, q, r) "v_smfmac_f32_16x16x64_bf16 %" #c ", %" #q ", v[" #r "], %7\n\t"
        asm volatile("s_nop 1\n\t" SMF(0, 4, 64:71) SMF(1, 4, 88:95) SMF(2, 4, 112:119) SMF(3, 4, 136:143) SMF(0, 5, 72:79) SMF(1, 5, 96:103) SMF(2, 5, 120:127) SMF(3, 5, 144:151)
                     SMF(0, 6, 80:87) SMF(1, 6, 104:111) SMF(2, 6, 128:135) SMF(3, 6, 152:159) "s_nop 7"
                     : "+v"(c0), "+v"(c1), "+v"(c2), "+v"(c3) : "v"(q0), "v"(q1), "v"(q2), "v"(idx));
#undef SMF
        const float s0 = c0[0] + c0[2], s1 = c1[0] + c1[2], s2 = c2[0] + c2[2], s3 = c3[0] + c3[2];
        float m = fmax2(fmax3(s0, s1, s2), s3);
        m = fmax2(m, dpp<0xB1>(m)); m = fmax2(m, dpp<0x4E>(m)); m = fmax2(m, dpp<0x124>(m)); m = fmax2(m, dpp<0x128>(m));
        const float s01 = LANES(0xffff0000ffff0000ull) ? s1 : s0, s23 = LANES(0xffff0000ffff0000ull) ? s3 : s2;
        const float sv = LANES(0xffffffff00000000ull) ? s23 : s01;
        m = bfr(bfr(m) * SCALE);
        const float p = ex2((bfr(bfr(sv) * SCALE) - m) * LOG2E);
        *(LAS u16*)(size_t)pw = (u16)pk2(p, p);
        *(LAS float*)(size_t)ma = m;
        __builtin_amdgcn_sched_barrier(0);
    };
    unsigned pr, oa, oc, cma, cxa, cra, cso;
    auto att_addr = [&]() {
        LANE_VARS();
        pr = lb + L_P + (unsigned)((7 + 8 * wave - 2 * q4 - (lane & 1)) * 192 + kq * 32 + (lane & 2) * 8);
        oa = lb + L_OP + (unsigned)(wave * 1024 + (lane & 7) * 64 + kq * 16 + (n >> 3) * 8);
        oc = lb + L_OP + (unsigned)(wave * 1024 + 512 + (kq >> 1) * 256 + (lane & 7) * 32 + (kq & 1) * 16 + (n >> 3) * 8);
        PIN3(pr, oa, oc);
    };
    auto comb_out = [&](float u0, float u1, unsigned so, unsigned long long mk) __attribute__((always_inline)) {
        const unsigned w = min(pk2(u0, u1), 0xfffffffeu);
        const u32x4 w4 = {w, dppu<0x55>(w), dppu<0xAA>(w), dppu<0xFF>(w)};
        st16m(ws, so, w4, mk);
    };
    auto max8 = [&](f32x4 m0, f32x4 m1) __attribute__((always_inline)) { return fmax3(fmax3(m0[0], m0[1], m0[2]), fmax3(m0[3], m1[0], m1[1]), fmax2(m1[2], m1[3])) * LOG2E; };
    auto comb_o0 = [&](f32x4 m0, f32x4 m1, f32x4 mm, f32x4 o0, f32x4 o1, f32x4 o2, f32x4 o3, unsigned so, unsigned long long mk) __attribute__((always_inline)) {
        const float M = max8(m0, m1);
        f32x4 f;
#pragma unroll
        for (int k = 0; k < 4; ++k) f[k] = ex2(__builtin_fmaf(mm[k], LOG2E, -M));
        const f32x4 o = f[3] * o3 + (f[2] * o2 + (f[1] * o1 + f[0] * o0));
        comb_out(rs32(o[0], o[2]), rs32(o[1], o[3]), so, mk);
    };
    auto comb_o1 = [&](f32x4 m0, f32x4 m1, f32x2 mm, f32x4 o0, f32x4 o1, unsigned so, unsigned long long mk) __attribute__((always_inline)) {
        const float M = max8(m0, m1);
        const f32x4 o = ex2(__builtin_fmaf(mm[1], LOG2E, -M)) * o1 + ex2(__builtin_fmaf(mm[0], LOG2E, -M)) * o0;
        comb_out(x16_add(rs32(o[0], o[2])), x16_add(rs32(o[1], o[3])), so, mk);
    };
    auto ml_addr = [&](int lane, unsigned& xa, unsigned& ra) {
        xa = lb + L_M + (unsigned)(((lane & 15) >> 1) * 4); ra = lb + L_P + (unsigned)((7 + 8 * ((lane & 15) >> 1)) * 192 + (lane & 1) * 64 + (lane >> 4) * 16);
    };
    auto comb_ml = [&](unsigned ma, unsigned xa, unsigned ra, unsigned so) __attribute__((always_inline)) {
        const u32x4 x4 = *(const LAS u32x4*)(size_t)ra;
        const f32x4 m0 = *(const LAS f32x4*)(size_t)ma, m1 = *(const LAS f32x4*)(size_t)(ma + 16);
        const float mw = *(const LAS float*)(size_t)xa;
        __builtin_amdgcn_sched_barrier(0);
        const f32x4 sp = mfma32u((u32x4){0x3f803f80u, 0x3f803f80u, 0x3f803f80u, 0x3f803f80u}, x4, z4);
        __builtin_amdgcn_sched_barrier(0);
        const float M = max8(m0, m1);
        const float L = row16_sum(sp[0] * ex2(__builtin_fmaf(mw, LOG2E, -M)));
        st16m(ws, so, asu((f32x4){M, L, M, L}), 1ull);
    };
    auto att_pv0 = [&]() {
        u32x4 bm[8];
        trans_v(IC<0>{}, IC<24>{}); trans_v(IC<1>{}, IC<16>{});
#pragma unroll
        for (int e = 0; e < 8; ++e) bm[e] = *(const LAS u32x4*)(size_t)(pr + e * 192);
        __builtin_amdgcn_sched_barrier(0);
        f32x4 da = z4, dc = z4;
        PIN2(da, dc);
        asm volatile("s_nop 1");
        __builtin_amdgcn_sched_barrier(0);
        f32x2 r;
        smf<RV>(da, bm[0], idx); smf<RV + 8>(da, bm[1], idx); smf<RV + 16>(da, bm[2], idx); smf<RV + 24>(da, bm[3], idx);
        smf<RV + 32>(da, bm[4], idx); smf<RV + 40>(da, bm[5], idx); smf<RV + 48>(da, bm[6], idx); smf<RV + 56>(da, bm[7], idx);
        asm volatile("s_nop 7\n\tv_pk_add_f32 %0, %1, %2\n\tds_write_b64 %3, %0\n\ts_mov_b64 exec, 1\n\tds_add_u32 %4, %5\n\ts_mov_b64 exec, -1"
                     : "=&v"(r) : "v"(LO2(da)), "v"(HI2(da)), "v"(oa), "v"(lb + L_CNT + 24), "v"(1u) : "memory");
        trans_w(IC<0>{}, IC<12>{}); trans_w(IC<1>{}, IC<8>{}); trans_w(IC<2>{}, IC<4>{}); trans_w(IC<3>{}, IC<0>{});
        STAMP(4);
        asm volatile("s_nop 1\n\tv_smfmac_f32_16x16x64_bf16 %0, %1, v[224:231], %5\n\tv_smfmac_f32_16x16x64_bf16 %0, %2, v[232:239], %5\n\t"
                     "v_smfmac_f32_16x16x64_bf16 %0, %3, v[240:247], %5\n\tv_smfmac_f32_16x16x64_bf16 %0, %4, v[248:255], %5\n\ts_nop 6"
                     : "+v"(dc) : "v"(bm[0]), "v"(bm[1]), "v"(bm[2]), "v"(bm[3]), "v"(idx));
        asm volatile("s_nop 0\n\tv_pk_add_f32 %0, %1, %2\n\tds_write_b64 %3, %0" : "=&v"(r) : "v"(LO2(dc)), "v"(HI2(dc)), "v"(oc) : "memory");
        STAMP(20);
    };
    unsigned long long ck1 = 0xffff0000ffff0000ull, ck2 = 0xffffffff00000000ull, csc = 0x3d93cd3a3d93cd3aull;
    auto att1 = [&]() {
        constexpr int layer = 1;
        {
#define SMF(c, q, r) "v_smfmac_f32_16x16x64_bf16 v[" #c "], v[" #q "], v[" #r "], %0\n\t"
#define MAXD(x) "v_max_f32_dpp v41, v41, v41 " x " row_mask:0xf bank_mask:0xf\n\t"
            asm volatile("s_waitcnt lgkmcnt(0)\n\ts_barrier\n\t"
                         "ds_read_b128 v[0:3], %1\n\tds_read2_b32 v[12:13], %2 offset0:96 offset1:100\n\tds_read_b128 v[4:7], %1 offset:128\n\tds_read_b128 v[8:11], %1 offset:256\n\t"
                         "v_mov_b64 v[16:17], 0\n\tv_mov_b64 v[18:19], 0\n\tv_mov_b64 v[20:21], 0\n\tv_mov_b64 v[22:23], 0\n\tv_mov_b64 v[24:25], 0\n\tv_mov_b64 v[26:27], 0\n\t"
                         "v_mov_b64 v[28:29], 0\n\tv_mov_b64 v[30:31], 0\n\tv_mov_b64 v[32:33], 0\n\tv_mov_b64 v[34:35], 0\n\tv_mov_b64 v[36:37], 0\n\tv_mov_b64 v[38:39], 0\n\t"
                         "s_waitcnt lgkmcnt(3)\n\t" SMF(16:19, 0:3, 64:71) SMF(20:23, 0:3, 88:95) "s_waitcnt lgkmcnt(1)\n\tv_mul_f32 v13, 0x3acccccd, v13\n\t"
                         SMF(24:27, 0:3, 112:119) "v_mul_f32 v12, 0x3acccccd, v12\n\t" SMF(28:31, 0:3, 136:143) "v_fma_f32 v12, -v13, v13, v12\n\t"
                         SMF(16:19, 4:7, 72:79) "v_maximum3_f32 v12, v12, 0, 0\n\t" SMF(20:23, 4:7, 96:103) "v_cvt_pk_bf16_f32 v12, 0, v12\n\t"
                         SMF(24:27, 4:7, 120:127) "v_add_f32 v12, 0x3727c5ac, v12\n\t" SMF(28:31, 4:7, 144:151) "v_cvt_pk_bf16_f32 v12, 0, v12\n\ts_waitcnt lgkmcnt(0)\n\t"
                         SMF(16:19, 8:11, 80:87) "v_sqrt_f32 v12, v12\n\t" SMF(20:23, 8:11, 104:111) "v_cvt_pk_bf16_f32 v12, 0, v12\n\t"
                         SMF(24:27, 8:11, 128:135) "v_rcp_f32 v12, v12\n\t" SMF(28:31, 8:11, 152:159)
                         "s_nop 2\n\tv_add_f32 v42, v16, v18\n\ts_nop 0\n\tv_add_f32 v43, v20, v22\n\tv_max_f32 v41, v42, v43\n\tv_add_f32 v44, v24, v26\n\tv_add_f32 v45, v28, v30\n\t"
                         "v_max3_f32 v41, v41, v44, v45\n\tv_cndmask_b32 v42, v42, v43, %8\n\tv_cndmask_b32 v44, v44, v45, %8\n\t"
                         MAXD("quad_perm:[1,0,3,2]") "v_cndmask_b32 v40, v42, v44, %9\n\ts_nop 0\n\t" MAXD("quad_perm:[2,3,0,1]") "s_nop 1\n\t" MAXD("row_ror:4") "s_nop 1\n\t" MAXD("row_ror:8")
                         "v_pk_mul_f32 v[40:41], v[12:13], v[40:41] op_sel_hi:[0,1]\n\tv_cvt_pk_bf16_f32 v40, 0, v40\n\tv_cvt_pk_bf16_f32 v41, 0, v41\n\t"
                         "v_pk_mul_f32 v[40:41], %10, v[40:41]\n\tv_cvt_pk_bf16_f32 v40, 0, v40\n\tv_cvt_pk_bf16_f32 v41, 0, v41\n\t"
                         "v_sub_f32 v40, v40, v41\n\tv_mul_f32 v40, 0x3fb8aa3b, v40\n\tv_exp_f32 v40, v40\n\ts_nop 0\n\tv_cvt_pk_bf16_f32 v40, v40, v40\n\tds_write_b16 %3, v40\n\t"
                         "ds_read_b128 v[0:3], %4\n\tds_read_b128 v[4:7], %4 offset:192\n\tds_read_b128 v[8:11], %4 offset:384\n\tds_read_b128 v[12:15], %4 offset:576\n\t"
                         "ds_read_b128 v[16:19], %4 offset:768\n\tds_read_b128 v[20:23], %4 offset:960\n\tds_read_b128 v[24:27], %4 offset:1152\n\tds_read_b128 v[28:31], %4 offset:1344\n\t"
                         "ds_write_b32 %5, v41\n\t"
                         "s_waitcnt lgkmcnt(8)\n\t" SMF(32:35, 0:3, 160:167) SMF(36:39, 0:3, 224:231) "s_waitcnt lgkmcnt(7)\n\t" SMF(32:35, 4:7, 168:175) SMF(36:39, 4:7, 232:239)
                         "s_waitcnt lgkmcnt(6)\n\t" SMF(32:35, 8:11, 176:183) SMF(36:39, 8:11, 240:247) "s_waitcnt lgkmcnt(4)\n\t" SMF(32:35, 12:15, 184:191) SMF(36:39, 12:15, 248:255)
                         SMF(32:35, 16:19, 192:199) "s_waitcnt lgkmcnt(1)\n\t" SMF(32:35, 20:23, 200:207) SMF(32:35, 24:27, 208:215) SMF(32:35, 28:31, 216:223)
                         "s_nop 2\n\tv_pk_add_f32 v[36:37], v[36:37], v[38:39]\n\tds_write_b64 %7, v[36:37]\n\ts_nop 2\n\tv_pk_add_f32 v[32:33], v[32:33], v[34:35]\n\tds_write_b64 %6, v[32:33]"
                         :: "v"(idx), "v"(qa), "v"(qm), "v"(pw), "v"(pr), "v"(ma), "v"(oa), "v"(oc), "s"(ck1), "s"(ck2), "s"(csc)
                         : "memory", "v0", "v1", "v2", "v3", "v4", "v5", "v6", "v7", "v8", "v9", "v10", "v11", "v12", "v13", "v14", "v15", "v16", "v17", "v18", "v19", "v20", "v21", "v22", "v23",
                           "v24", "v25", "v26", "v27", "v28", "v29", "v30", "v31", "v32", "v33", "v34", "v35", "v36", "v37", "v38", "v39", "v40", "v41", "v42", "v43", "v44", "v45");
#undef SMF
#undef MAXD
            STAMP(20 + 32 * layer);
            if (__builtin_expect(wave >= 3, 1)) BAR();
            else {
                __builtin_amdgcn_s_setprio(0);
                if (wave == 0) {
                    unsigned long long mk = 0x1111111111111111ull; PINS(mk);
                    BAR();
                    STAMP(5 + 32 * layer);
                    comb_o0(*(const LAS f32x4*)(size_t)cma, *(const LAS f32x4*)(size_t)(cma + 16), *(const LAS f32x4*)(size_t)cxa, *(const LAS f32x4*)(size_t)cra,
                            *(const LAS f32x4*)(size_t)(cra + 1024), *(const LAS f32x4*)(size_t)(cra + 2048), *(const LAS f32x4*)(size_t)(cra + 3072), cso, mk);
                } else if (wave == 1) {
                    unsigned long long mk = 0x0000111100001111ull; PINS(mk);
                    BAR();
                    comb_o1(*(const LAS f32x4*)(size_t)cma, *(const LAS f32x4*)(size_t)(cma + 16), *(const LAS f32x2*)(size_t)cxa, *(const LAS f32x4*)(size_t)cra, *(const LAS f32x4*)(size_t)(cra + 1024), cso, mk);
                } else {
                    BAR();
                    comb_ml(cma, cxa, cra, cso);
                }
                STAMP(21 + 32 * layer);
            }
        }
    };

    unsigned nj = wave == 3 ? WEARLY : 0u; int ord;
    {
        const int tid = threadIdx.x, lane = tid & 63;
        STAMP(0);
        unsigned pf = 0u, mb = 0u, tfl = 0u;
        const u16* kbase = kc + ((size_t)((gj >> 1) * HQ + gi) * SK + (SK - WIN) + (gj & 1) * 512) * DH;
        const u16* vbase = vc + ((size_t)((gj >> 1) * HQ + gi) * SK + (SK - WIN) + (gj & 1) * 512) * DH;
        if (wave == 7) {
            __builtin_amdgcn_s_setprio(3);
            for (int i = 0; i < 5; ++i) dma16p(x0, (unsigned)(i * 1024 + lane * 16), lb + L_X0 + i * 1024);
            pf = code_pf((g >> 3) % 10, lane);
            if (lane < 20) *reinterpret_cast<unsigned*>(smem + L_CNT + lane * 4) = lane == 16 ? tst : 0u;
            BAR();
            if (lane < 10) *reinterpret_cast<u32x4*>(smem + L_XS + 640 + lane * 16) = (u32x4){0x3f803f80u, 0x3f803f80u, 0x3f803f80u, 0x3f803f80u};
            if (lane < 16) { *reinterpret_cast<u32x4*>(smem + L_CS + 768 + lane * 16) = (u32x4){0u, 0u, 0u, 0u}; }
        } else {
            if (wave >= 4) {
                if (wave < 6) {
                    empty1(wave - 4, lane);
                    empty35(0, wave - 4, lane);
                } else {
                    __builtin_amdgcn_s_setprio(3);
                    if (gi + gj == 0) st4_sc1(ws, WS_R + (lane & 31) * 4, 0u);
                    st4_sc1(ws, WS_MB + (unsigned)((gi * 8 + gj) * 128 + (lane & 31) * 4), 0u);
                    empty4(0, 0, lane); empty4(0, 1, lane);
                }
            }
#pragma unroll 1
            for (int i = wave; i < 35; i += 7) {
                if (i >= 33) dma16p(wr, (unsigned)((i == 33 || lane < 16) ? (i - 33) * 1024 + lane * 16 : 0), lb + L_WR + (i - 33) * 1024);
                else {
                    const int c = i * 64 + lane; int row = c / 26, cc = c - 26 * row; if (cc > 23) cc = 23; if (row > 79) row = 79;
                    dma16(wq, (unsigned)(((gj * 80 + swz(row)) * QD + gi * DH + cc * 8) * 2), lb + L_WQ + i * 1024);
                }
            }
            BAR();
            *reinterpret_cast<u32x4*>(smem + L_P + tid * 16) = (u32x4){0u, 0u, 0u, 0u};
            if (tid < 72 * 12 - 448) *reinterpret_cast<u32x4*>(smem + L_P + 7168 + tid * 16) = (u32x4){0u, 0u, 0u, 0u};
            if (wave >= 4) {
                VMW(5);
                lds_add(32, 1u);
                if (wave == 6) {
#pragma unroll 1
                    for (unsigned spins = 0; spins < (kSpinCap << 4); ++spins) if (lds_word(lb + L_CNT + 32) >= 3u) break;
                    send_flag(lane);
                    tfl = now();
                    STAMP(17);
                    asm volatile("s_waitcnt vmcnt(1) lgkmcnt(0)" ::: "memory");
                } else asm volatile("s_waitcnt vmcnt(0) lgkmcnt(0)" ::: "memory");
                lds_add(0, 1u);
                if (wave == 6) {
                    until(__builtin_amdgcn_readfirstlane(tfl) + DMB);
#pragma unroll 1
                    for (unsigned spins = 0; spins < kSpinCap; ++spins) {
                        unsigned r = ld4_sc1(ws, WS_MB + (unsigned)((gi * 8 + gj) * 128 + (lane & 7) * 4));
                        WAIT1(r, 0);
                        if (none(r ^ 1u)) break;
                        if ((spins & 7u) == 7u) send_flag(lane);
                    }
                    lds_set(lb + L_CNT + 8, 1u);
                    if (gj == (gi & 7)) st4_sc1(ws, WS_R + (unsigned)(gi * 4), 1u);
                    STAMP(31);
                }
            }
        }
        u32x4 a[8];
        unsigned ko = (unsigned)((wave * 64 + (lane & 7)) * 384 + (((lane >> 3) & 1) * 4 + (lane >> 4)) * 16);
        PIN1(ko);
        auto k_group = [&](auto GC_) __attribute__((always_inline)) {
            constexpr int G = decltype(GC_)::value, R = RK + 24 * (G >> 1) + 4 * (G & 1);
            const unsigned o = ko + G * 3072;
            if (PRIO && G == 0) __builtin_amdgcn_s_setprio(3);
            if (PRIO && G == 4 && wave < 6) __builtin_amdgcn_s_setprio(2);
            pace(3);
            ld16_nt<R, 0>(kbase, o); ld16_nt<R + 8, 128>(kbase, o); ld16_nt<R + 16, 256>(kbase, o);
            const int lane = lane_id();
            const unsigned ho = WS_H1 + (unsigned)((gi * 4 + (gj >> 1)) * 7168 + 3584 + (lane < 48 ? lane : 0) * 16);
            if (wave < 7) {
                asm volatile("; COND_BEGIN");
                if (G == GL && wave < 4) {
                    asm volatile("s_waitcnt vmcnt(%0) lgkmcnt(0)" :: "i"(3 * GL + 3) : "memory");
                    lds_add(0, 1u);
                    if (wave < 2 && (EARLY >> wave & 1)) load_gu(wave, lane);
                    if (wave == 2 && (EARLY & 4)) load_wd(lane);
                    if (wave == 3) {
#pragma unroll 1
                        for (int k = 5 - WEARLY; k < 5; ++k) load_wo(k, lane, true);
                    }
                }
                if (wave == 6) {
                    if (gi + gj == 0) {
                        if (G == GE) mb = ld4_sc1(ws, WS_R + (unsigned)((lane & 31) * 4));
                        if (G == GF) {
                            WAIT1(mb, 3 * (GF - GE));
                            if (none(mb ^ 1u)) { answer(lane); lds_set(lb + L_CNT + 36, 1u); }
                        }
                    }
                    if (G == 7) mb = ld4_sc1(ws, WS_MB + (unsigned)((gi * 8 + (gi & 7)) * 128 + 32));
                }
                asm volatile("; COND_END");
            }
            if (wave == 7) {
                asm volatile("; COND_BEGIN");
                if (G == GP) {
                    VMW(3 * (GP + 1) + 1);
                    const unsigned char* p = smem + L_X0 + (lane >> 4) * 1280;
                    const float c0 = lo16(*reinterpret_cast<const u16*>(p));
                    const f32x2 c2 = {c0, c0};
                    f32x2 s2 = {0.f, 0.f}, q2 = s2;
#pragma unroll
                    for (int k = 0; k < 5; ++k) {
                        const u32x4 w = *reinterpret_cast<const u32x4*>(p + k * 256 + (lane & 15) * 16);
#pragma unroll
                        for (int e = 0; e < 4; ++e) { const f32x2 d = unpk(w[e]) - c2; s2 += d; q2 += d * d; }
                    }
                    const float s = row16_sum(s2[0] + s2[1]), qq = row16_sum(q2[0] + q2[1]);
                    const float ms = s * (1.f / DM);
                    const unsigned mv = pk2(c0 + ms, fmax2(qq - s * ms, 0.f) * (1.f / DM));
                    const float mb = lo16(mv), rden = frcp(bfr(__builtin_amdgcn_sqrtf(bfr(hi16(mv) + 1e-5f))));
                    const f32x2 mb2 = {mb, mb}, rd2 = {rden, rden};
                    if ((lane & 15) < 10) {
                        u32x4 w = *reinterpret_cast<const u32x4*>(p + gj * 160 + (lane & 15) * 16);
#pragma unroll
                        for (int e = 0; e < 4; ++e) w[e] = pk2v(unpk(pk2v(unpk(w[e]) - mb2)) * rd2);
                        *reinterpret_cast<u32x4*>(smem + L_XS + (lane >> 4) * 160 + (lane & 15) * 16) = w;
                    }
                    STAMP(30);
#pragma unroll 1
                    for (unsigned spins = 0; spins < (kSpinCap << 4); ++spins) {
                        if (lds_word(lb + L_CNT) >= 7u) break;
                        __builtin_amdgcn_s_sleep(1);
                    }
                    STAMP(26);
                    {
#pragma unroll 1
                        for (int i6 = 0; i6 < 6; ++i6) {
                            LANE_VARS();
                            const int blk = i6 < 3 ? i6 : i6 - 3;
                            const unsigned xb = lb + L_XS + (unsigned)((i6 < 3 ? p4 : 4) * 160);
                            Frag80 f[4];
#pragma unroll
                            for (int i = 0; i < 4; ++i) f[i] = frag80(lb + L_WQ + (unsigned)(128 * blk + 32 * i + 8 * p4), 416, r0s, r1s, r2s);
                            const Frag80 b = rows80(xb + kq * 16, xb + 128 + kq * 8);
                            const unsigned so = (i6 < 3 ? WS_H1 + (unsigned)(((gi * 4 + p4) * 8 + gj) * 896) : WS_OWN + (unsigned)((gi * 8 + gj) * 2048 + 1024)) + (unsigned)(256 * blk + q4 * 64 + kq * 16);
                            st16m(ws, so, tiles4(f, b, 0x000f000f000f000full), i6 < 3 ? ~0ull : 0x1111111111111111ull);
                            if (i6 == 2) { tfl = now(); STAMP(2); }
                        }
                    }
                    f32x2 r2 = {0.f, 0.f};
#pragma unroll
                    for (int k = 0; k < 5; ++k) r2 += unpk(*reinterpret_cast<const unsigned*>(smem + L_WR + k * 256 + lane_id() * 4));
                    const float sw = wave_sum(r2[0] + r2[1]);
                    *reinterpret_cast<float*>(smem + L_CNT + 12) = sw;
                    WAIT1(pf, 3 * (GP + 1) + 6);
                    pace(XCOST, false);
                }
                if (G == GC) {
#pragma unroll 1
                    for (unsigned spins = 0; spins < (kSpinCap << 8); ++spins) { if (lds_word(lb + L_CNT + 8)) break; __builtin_amdgcn_s_sleep(1); }
                    until(__builtin_amdgcn_readfirstlane(tfl) + DQ);
                    GATHER8(a, ho, 896);
                }
                if (G == GD) {
                    WAIT8(a, 3 * (GD - GC));
#pragma unroll 1
                    for (unsigned spins = 0; spins < kSpinCap && any_empty(UM32(a)); ++spins) { slip(0u); AGAIN8(a, ho, 896); WAIT8(a, 0); }
                    const f32x4 sq = SUM8(a);
                    unsigned* qp = reinterpret_cast<unsigned*>(smem + L_Q + (lane >> 2) * 32 + (lane & 3) * 4);
                    qp[0] = pk2(sq[0], sq[1]); qp[4] = pk2(sq[2], sq[3]);
                    asm volatile("s_waitcnt lgkmcnt(0)\n\tds_write_b32 %0, %1" :: "v"(lb + L_CNT + 4), "v"(1u) : "memory");
                    STAMP(27);
                }
                asm volatile("; COND_END");
            } else if (G == GC) { for (int k = 0; k < 8; ++k) asm volatile("" : "=v"(a[k])); }
        };
        k_group(IC<0>{}); k_group(IC<1>{}); k_group(IC<2>{}); k_group(IC<3>{}); k_group(IC<4>{}); k_group(IC<5>{}); k_group(IC<6>{}); k_group(IC<7>{});
        {
            const int lane = lane_id();
            const unsigned v0 = (unsigned)((wave * 64 + 4 * (lane >> 4)) * 384 + (lane & 15) * 16);
            const unsigned v1 = v0 + 256 - (unsigned)((lane & 15) * 8);
            auto v_group = [&](auto HC) __attribute__((always_inline)) {
                constexpr int h = decltype(HC)::value, R = h < 8 ? RV + 16 * (h & 3) + 4 * (h >> 2) : RV + 64 + 2 * (h - 8), NV = h < 8 ? 2 * h + 2 : 4 * h - 12;
                const unsigned o = (h < 8 ? v0 + (h >> 1) * 6144 : v1 + (h - 8) * 6144);
                if (PRIO && h == 0) __builtin_amdgcn_s_setprio(1);
                if (PRIO && h == 6) __builtin_amdgcn_s_setprio(0);
                pace(2);
                if (h < 8) { ld16_nt<R, (h & 1) * 768>(vbase, o); ld16_nt<R + 8, (h & 1) * 768 + 384>(vbase, o); }
                else { ld8_nt<R, 0>(vbase, o); ld8_nt<R + 8, 384>(vbase, o); ld8_nt<R + 16, 768>(vbase, o); ld8_nt<R + 24, 1152>(vbase, o); }
                if (h == 0 && wave == 6) {
                    asm volatile("; COND_BEGIN");
                    WAIT1(mb, 2);
                    const unsigned r = __builtin_amdgcn_readfirstlane(mb);
                    if (r) { adopt(r); STAMP(29); }
                    asm volatile("; COND_END");
                }
                if constexpr (h >= FX && h < FX + 6) {
                    constexpr int i = 2 * (h - FX);
                    fix_k1(IC<i>{}, IC<20 - 6 * (i / 3) - i % 3 + NV>{});
                    fix_k1(IC<i + 1>{}, IC<20 - 6 * ((i + 1) / 3) - (i + 1) % 3 + NV>{});
                }
                if (h == 1 && wave == 6 && gi + gj == 0) { asm volatile("; COND_BEGIN"); mb = ld4_sc1(ws, WS_R + (unsigned)((lane & 31) * 4)); asm volatile("; COND_END"); }
                if (h == 3 && wave == 6 && gi + gj == 0) {
                    asm volatile("; COND_BEGIN");
                    WAIT1(mb, 4);
                    if (none(mb ^ 1u) && !lds_word(lb + L_CNT + 36)) { answer(lane); lds_set(lb + L_CNT + 36, 1u); }
                    asm volatile("; COND_END");
                }
                if (h == FX + 5) {
                    STAMP(1);
                    if (!PRIO) __builtin_amdgcn_s_setprio(0);
                    att_qk0();
                }
            };
            v_group(IC<0>{}); v_group(IC<1>{}); v_group(IC<2>{}); v_group(IC<3>{}); v_group(IC<4>{}); v_group(IC<5>{});
            v_group(IC<6>{}); v_group(IC<7>{}); v_group(IC<8>{}); v_group(IC<9>{}); v_group(IC<10>{}); v_group(IC<11>{});
        }
    }
    {
        lds_add(16, 1u);
        STAMP(28);
        att_addr();
        const int lane = lane_id();
        cma = lb + L_M; cxa = lb + L_M + (unsigned)((lane >> 4) * 8); cra = lb + L_OP + (unsigned)(512 + (lane >> 4) * 2048 + (lane & 15) * 16);
        cso = WS_OWN + (unsigned)((gi * 8 + gj) * 2048 + 256 + (lane >> 5) * 64 + (lane & 15) * 4);
        PIN2(cma, cxa); PIN2(cra, cso);
        att_pv0();
    }
    {
        f32x4 m0, m1, o0, o1; f32x2 mm; unsigned r;
        asm volatile("s_mov_b64 exec, 1\n\tds_add_rtn_u32 %5, %6, %7 offset:%10\n\ts_mov_b64 exec, -1\n\t"
                     "ds_read_b128 %0, %6\n\tds_read_b128 %1, %6 offset:16\n\tds_read_b64 %2, %8\n\tds_read_b128 %3, %9\n\tds_read_b128 %4, %9 offset:1024\n\ts_waitcnt lgkmcnt(0)"
                     : "=&v"(m0), "=&v"(m1), "=&v"(mm), "=&v"(o0), "=&v"(o1), "=&v"(r)
                     : "v"(cma), "v"(1u), "v"(cxa), "v"(cra), "i"(L_CNT + 28 - L_M) : "memory");
        ord = __builtin_amdgcn_readfirstlane(r);
        if (ord == 7) { STAMP(5); comb_o1(m0, m1, mm, o0, o1, cso, 0x0000111100001111ull); lds_set(lb + L_CNT + 72, ~7u); lds_set(lb + L_CNT + 48, now() | 1u); STAMP(21); }
        const int lane = lane_id();
        auto wait_cnt = [&](int off, unsigned v) {
#pragma unroll 1
            for (unsigned spins = 0; spins < (kSpinCap << 4); ++spins) { if (lds_word(lb + L_CNT + off) >= v) break; __builtin_amdgcn_s_sleep(1); }
        };
        if (ord == 1) {
            wait_cnt(16, 8u);
            unsigned xa, ra; ml_addr(lane, xa, ra);
            comb_ml(cma, xa, ra, WS_OWN + (unsigned)((gi * 8 + gj) * 2048 + 384));
            STAMP(24);
        } else if (ord == 0 && !lds_word(lb + L_CNT + 52)) {
            if (gi + gj == 0 && !lds_word(lb + L_CNT + 36)) {
#pragma unroll 1
                for (unsigned spins = 0; spins < kSpinCap; ++spins) {
                    unsigned r = ld4_sc1(ws, WS_R + (unsigned)((lane & 31) * 4));
                    WAIT1(r, 0);
                    if (none(r ^ 1u)) break;
                    if ((spins & 7u) == 7u) send_flag(lane);
                }
                answer(lane);
            }
#pragma unroll 1
            for (unsigned spins = 0; spins < kSpinCap; ++spins) {
                unsigned r = ld4_sc1(ws, WS_MB + (unsigned)((gi * 8 + (gi & 7)) * 128 + 32));
                WAIT1(r, 0);
                r = __builtin_amdgcn_readfirstlane(r);
                if (r) { adopt(r); break; }
                if ((spins & 7u) == 7u) { send_flag(lane); if (gj == (gi & 7)) st4_sc1(ws, WS_R + (unsigned)(gi * 4), 1u); }
                __builtin_amdgcn_s_sleep(16);
            }
            STAMP(29);
        } else if (ord == 2) {
#pragma unroll 1
            for (unsigned spins = 0; spins < (kSpinCap << 6); ++spins) if (lds_word(lb + L_CNT + 24) >= 8u) break;
            const unsigned ra = lb + L_OP + (unsigned)((lane >> 5) * 4096 + (lane & 31) * 16);
            comb_o0(*(const LAS f32x4*)(size_t)cma, *(const LAS f32x4*)(size_t)(cma + 16), *(const LAS f32x4*)(size_t)(cma + (lane >> 5) * 16), *(const LAS f32x4*)(size_t)ra, *(const LAS f32x4*)(size_t)(ra + 1024),
                    *(const LAS f32x4*)(size_t)(ra + 2048), *(const LAS f32x4*)(size_t)(ra + 3072), WS_OWN + (unsigned)((gi * 8 + gj) * 2048 + lane * 4), 0x1111111111111111ull);
            STAMP(22);
        }
#pragma unroll 1
        for (;;) {
            unsigned j;
            asm volatile("s_mov_b64 exec, 1\n\tds_add_rtn_u32 %0, %1, %2\n\ts_mov_b64 exec, -1\n\ts_waitcnt lgkmcnt(0)" : "=&v"(j) : "v"(lb + L_CNT + 40), "v"(1u) : "memory");
            j = __builtin_amdgcn_readfirstlane(j);
            if (j >= 5u - WEARLY) break;
            load_wo((int)j, lane, false); ++nj;
        }
        STAMP(23);
    }

    auto chain = [&](auto LC) {
        constexpr int layer = decltype(LC)::value;
        const unsigned h4 = WS_H4 + (unsigned)((layer * 32 + gi) * 16384);
        if (wave < 3) {
            LANE_VARS();
            if (layer == 0) {
                VMW(0);
                lds_add(44, nj);
                const int c = wave * 64 + lane, row = c / 6, cc = c - 6 * row;
                unsigned o = (unsigned)((swz(row) * FF + cc * 8) * 2), o2 = (wave == 0 || lane < 32) ? o : 0u, d = lb + L_GU + wave * 1024;
                const u16* wb0 = wg + gj * 80 * FF + gi * 48;
                const u16* wb1 = wu + gj * 80 * FF + gi * 48;
                const u16* db = wd + gi * 48 * DM + gj * 80;
                const int pp = c / 10, c2 = c + 192, p2 = c2 / 10;
                unsigned fd = (unsigned)(swz(pp) * 1280 + (c - 10 * pp) * 16), f2 = (unsigned)(swz(p2) * 1280 + (c2 - 10 * p2) * 16), f3 = (wave < 2 || lane < 32) ? fd : 0u;
                PIN2(o, o2); PIN3(fd, f2, f3); PINS(wb0); PINS(wb1); PINS(d); PINS(db);
                auto late = [&](int m) {
                    if (m & 1) { dma16(wb0, o, d); dma16(wb0 + 32 * FF, o, d + 3072); if (wave < 2) dma16(wb0 + 64 * FF, o2, d + 6144); }
                    if (m & 2) { dma16(wb1, o, d + 8192); dma16(wb1 + 32 * FF, o, d + 8192 + 3072); if (wave < 2) dma16(wb1 + 64 * FF, o2, d + 8192 + 6144); }
                    if (m & 4) { dma16(db, fd, d + L_WD - L_GU); dma16(db + 32 * DM, f3, d + L_WD - L_GU + 5120); if (wave < 2) dma16(db, f2, d + L_WD - L_GU + 3072); }
                };
                if (GOPT) {
#pragma unroll 1
                    for (unsigned spins = 0; spins < (kSpinCap << 4); ++spins) if (lds_word(lb + L_CNT + 72) == ~3u) break;
                    late(GOPT);
                    STAMP(25);
                }
                BAR();
                late(GB4);
#pragma unroll 1
                for (unsigned spins = 0; spins < (kSpinCap << 4); ++spins) if (lds_word(lb + L_CNT + 76) >= 5u) break;
                late(GST);
                if (wave < 2) { empty1(wave, lane); empty35(1, wave, lane); VMW(3); } else { empty4(1, 0, lane); empty4(1, 1, lane); VMW(2); }
                if ((EARLY & 3) != 3) {
                    lds_add(20, 1u);
#pragma unroll 1
                    for (unsigned spins = 0; spins < (kSpinCap << 4); ++spins) if (lds_word(lb + L_CNT + 20) >= 3u) break;
                }
                STAMP(19);
            } else BAR();
            {
                const unsigned im = lb + L_GU + (unsigned)(32 * wave + 8 * p4);
                Frag80 fg = frag80(im, 96, r0s, r1s, r2s), fu = frag80(im + 8192, 96, r0s, r1s, r2s);
                const unsigned xb = lb + L_XS + (unsigned)((n < 4 ? n : 4) * 160);
                unsigned b16 = xb + kq * 16, b8 = xb + 128 + kq * 8;
                unsigned sg = h4 + (unsigned)((n < 4 ? wave * 4096 + gj * 512 + n * 64 : 12288 + wave * 1024 + gj * 128) + kq * 16), su = sg + (n < 4 ? 256u : 64u);
                unsigned o = h4 + (unsigned)(wave * 4096 + lane * 16);
                unsigned oc = h4 + (unsigned)(12288 + wave * 1024 + ((lane >> 5) * 4 + q4) * 128 + (kq & 1) * 64 + p4 * 16);
                unsigned os = h4 + (unsigned)(15360 + (p4 + 4 * (kq & 1)) * 128 + q4 * 16);
                unsigned ha = lb + L_H + (unsigned)(q4 * 96 + wave * 32 + p4 * 8 + kq * 2);
                f32x2 cs = {0.f, 0.f};
                if (layer != 0) cs = *reinterpret_cast<const f32x2*>(smem + L_CS2 + wave * 512 + lane * 8);
                PIN3(fg.a0, fg.a1, fg.a2); PIN3(fu.a0, fu.a1, fu.a2); PIN3(b16, b8, sg); PIN3(su, o, os); PIN2(ha, cs);
                unsigned long long mk = layer ? 0x000f000f000f000full : 0x001f001f001f001full;
                unsigned ta = WS_MB + (unsigned)((lane >> 1) * 1024 + 64 + (lane & 1) * 16);
                if (layer == 0) PIN2(oc, ta);
                PINS(mk);
                unsigned t3 = 0u;
                if (layer == 0) {
                    const u16* db = wd + gi * 48 * DM + gj * 80;
                    const int c = wave * 64 + lane, pp = c / 10, c2 = c + 192, p2 = c2 / 10;
                    const unsigned fd = (unsigned)(swz(pp) * 1280 + (c - 10 * pp) * 16), f2 = (unsigned)(swz(p2) * 1280 + (c2 - 10 * p2) * 16);
                    t3 = lds_word(lb + L_CNT + 64) + C3;
                    VMW(0);
                    if (!((EARLY | GOPT | GB4 | GST) & 4)) {
                        until(t3 + DWD);
                        dma16(db, fd, lb + L_WD + wave * 1024);
                        dma16(db + 32 * DM, (wave < 2 || lane < 32) ? fd : 0u, lb + L_WD + (5 + wave) * 1024);
                        if (wave < 2) dma16(db, f2, lb + L_WD + (3 + wave) * 1024);
                    }
                }
                BAR();
                STAMP(10 + 32 * layer);
                u32x4 tl = {0u, 0u, 0u, 0u};
                if (layer == 0) tl = ld16_sc1(ws, ta);
                const Frag80 b = rows80(b16, b8);
                const f32x4 g0 = mfma32(fg.a0, b.a0, z4), u0 = mfma32(fu.a0, b.a0, z4), g1 = mfma32(fg.a1, b.a1, g0), g2 = mfma16(fg.a2, b.a2, z4), u1 = mfma32(fu.a1, b.a1, u0), u2 = mfma16(fu.a2, b.a2, z4);
                __builtin_amdgcn_sched_barrier(0);
                const f32x4 cg = g1 + g2, cu = u1 + u2;
                st16m2<0>(ws, sg, asu(cg), su, asu(cu), mk);
                STAMP(11 + 32 * layer);
                if (layer == 0) {
                    const unsigned tn = now();
                    WAIT1(tl, 2);
                    unsigned d = min(min(min(tn - tl[0], tn - tl[1]), min(tn - tl[2], tn - tl[3])), 800u);
                    d = min(d, dppu<0xB1>(d)); d = min(d, dppu<0x4E>(d)); d = min(d, dppu<0x124>(d)); d = min(d, dppu<0x128>(d));
                    unsigned tr = tn - min(min((unsigned)__builtin_amdgcn_readlane(d, 0), (unsigned)__builtin_amdgcn_readlane(d, 16)), min((unsigned)__builtin_amdgcn_readlane(d, 32), (unsigned)__builtin_amdgcn_readlane(d, 48)));
                    if (!POLL3 && (int)(t3 - tr) >= M3) tr = t3 + D4S - D4A;
                    lds_set(lb + L_CNT + 56, tr | 1u);
                    lds_add(68, 1u);
                    if ((int)(tn + 12 - X4A - tr) > 0) tr = tn + 12 - X4A;
                    until(tr + X4A);
                } else sched(X4, 12);
                STAMP(16 + 32 * layer);
                float hv; f32x2 st;
                u32x4 a0, a1, a2, a3, ac, as;
                auto load = [&]() __attribute__((always_inline)) {
                    as = ld16_sc1(ws, os);
                    if (layer == 0) ac = ld16_sc1(ws, oc);
                    a0 = ld16_sc1<0>(ws, o); a1 = ld16_sc1<1024>(ws, o); a2 = ld16_sc1<2048>(ws, o); a3 = ld16_sc1<3072>(ws, o);
                    if (layer == 0) asm volatile("s_waitcnt vmcnt(0)" : "+v"(a0), "+v"(a1), "+v"(a2), "+v"(a3), "+v"(ac), "+v"(as) :: "memory");
                    else { asm volatile("s_waitcnt vmcnt(0)" : "+v"(a0), "+v"(a1), "+v"(a2), "+v"(a3), "+v"(as) :: "memory"); ac = as; }
                };
                auto compute = [&]() __attribute__((always_inline)) {
                    float ss, sm;
                    asm volatile("v_add_f32_dpp %0, %2, %2 quad_perm:[1,0,3,2] row_mask:0xf bank_mask:0xf\n\tv_add_f32_dpp %1, %3, %3 quad_perm:[1,0,3,2] row_mask:0xf bank_mask:0xf\n\ts_nop 0\n\t"
                                 "v_add_f32_dpp %0, %0, %0 quad_perm:[2,3,0,1] row_mask:0xf bank_mask:0xf\n\tv_add_f32_dpp %1, %1, %1 quad_perm:[2,3,0,1] row_mask:0xf bank_mask:0xf\n\ts_nop 0"
                                 : "=&v"(ss), "=&v"(sm) : "v"(as[0]), "v"(as[1]));
                    ss = x16_add(ss); sm = x16_add(sm);
                    st = ln_stats(ss, sm);
                    const f32x4 t = (asf(a0) + asf(a1)) + (asf(a2) + asf(a3));
                    const f32x2 gu = sw16(rs32(t[0], t[2]), rs32(t[1], t[3]));
                    if (layer == 0) {
                        cs = sw16(rs32(asf(ac)[0], asf(ac)[2]), rs32(asf(ac)[1], asf(ac)[3]));
                        cs[0] += dpp<0x124>(cs[0]); cs[1] += dpp<0x124>(cs[1]); cs[0] += dpp<0x128>(cs[0]); cs[1] += dpp<0x128>(cs[1]);
                    }
                    const unsigned gw = pk2v((gu - st[0] * cs) * st[1]);
                    const float gv = lo16(gw);
                    hv = bfr(gv * frcp(1.f + ex2(gv * -LOG2E))) * hi16(gw);
                    *(LAS u16*)(size_t)ha = (u16)pk2(hv, hv);
                    __builtin_amdgcn_sched_barrier(0);
                };
                RETRY(load(), max(max(max(um4(a0), um4(a1)), max(um4(a2), um4(a3))), max(um4(ac), um4(as))), compute(), any_nan(hv, hv), layer ? X4 : X4A);
                BAR();
                STAMP(12 + 32 * layer);
                if (layer == 0) {
                    *reinterpret_cast<f32x2*>(smem + L_CS2 + wave * 512 + lane * 8) = cs;
                }
            }
            if (layer == 0) {
                if (wave == 1) {
                    const unsigned o = WS_OWN + (unsigned)(gi * 16384 + 1024 + 2048 + (lane < 48 ? lane : 0) * 16);
                    u32x4 a[8];
                    a[0] = ld16_sc1<-2048>(ws, o); a[1] = ld16_sc1<0>(ws, o); a[2] = ld16_sc1<2048>(ws, o);
                    a[3] = ld16_sc1<-2048>(ws, o + 6144); a[4] = ld16_sc1<0>(ws, o + 6144); a[5] = ld16_sc1<2048>(ws, o + 6144);
                    a[6] = ld16_sc1<-2048>(ws, o + 12288); a[7] = ld16_sc1<0>(ws, o + 12288);
                    WAIT8(a, 0);
                    if (lane < 48) *reinterpret_cast<f32x4*>(smem + L_CS + lane * 16) = SUM8(a);
                }
                Frag80 f[4];
#pragma unroll
                for (int i = 0; i < 4; ++i) { f[i] = frag80(lb + L_WQ + (unsigned)(128 * wave + 32 * i + 8 * p4), 416, r0s, r1s, r2s); PIN3(f[i].a0, f[i].a1, f[i].a2); }
                unsigned b16 = lb + L_XS + (unsigned)(p4 * 160 + kq * 16), b8 = lb + L_XS + (unsigned)(p4 * 160 + 128 + kq * 8);
                unsigned so = WS_H1 + (unsigned)(((gi * 4 + p4) * 8 + gj) * 896 + 256 * wave + q4 * 64 + kq * 16);
                unsigned long long mk = 0x000f000f000f000full;
                PIN3(b16, b8, so); PINS(mk);
                BAR();
                STAMP(33);
                const Frag80 b = rows80(b16, b8);
                st16_sc1(ws, so, tiles4(f, b, mk));
                STAMP(34);
                qk_addr(true); att_addr();
                cma = lb + L_M; cxa = lb + L_M + (unsigned)(wave == 0 ? (lane >> 5) * 16 : (lane >> 4) * 8);
                cra = lb + L_OP + (unsigned)(wave == 0 ? (lane >> 5) * 4096 + (lane & 31) * 16 : 512 + (lane >> 4) * 2048 + (lane & 15) * 16);
                if (wave == 2) ml_addr(lane, cxa, cra);
                cso = WS_OWN + (unsigned)((gi * 8 + gj) * 2048 + 512) + (unsigned)(wave == 0 ? lane * 4 : wave == 1 ? 256 + (lane >> 5) * 64 + (lane & 15) * 4 : 384);
                PIN2(cma, cxa); PIN2(cra, cso);
            }
        } else {
            const int t = wave - 3;
            StatAddr sa;
            auto gather32 = [&](unsigned o, int lane, unsigned X, auto mid, auto fin) __attribute__((always_inline)) {
                u32x4 a0 = ld16_sc1<-2048>(ws, o), a1 = ld16_sc1<-1024>(ws, o), a2 = ld16_sc1<0>(ws, o), a3 = ld16_sc1<1024>(ws, o), sel;
                const unsigned one = (lane & 1) ? 0x3f800000u : 0x3f80u;
#pragma unroll
                for (int k = 0; k < 4; ++k) sel[k] = ((lane >> 1) & 3) == k ? one : 0u;
                unsigned long long hi = 0xff00ff00ff00ff00ull;
                PIN1(sel); PINS(hi);
                mid();
                float v;
                auto sum = [&]() __attribute__((always_inline)) {
                    f32x4 d; float w;
                    asm volatile("s_waitcnt vmcnt(3)\n\tv_mfma_f32_16x16x32_bf16 %0, %1, %5, 0\n\ts_waitcnt vmcnt(2)\n\tv_mfma_f32_16x16x32_bf16 %0, %2, %5, %0\n\t"
                                 "s_waitcnt vmcnt(1)\n\tv_mfma_f32_16x16x32_bf16 %0, %3, %5, %0\n\ts_waitcnt vmcnt(0)\n\tv_mfma_f32_16x16x32_bf16 %0, %4, %5, %0"
                                 : "=&v"(d), "+v"(a0), "+v"(a1), "+v"(a2), "+v"(a3) : "v"(sel) : "memory");
                    asm volatile("s_nop 7\n\tv_cndmask_b32 %0, %2, %3, %6\n\tv_cndmask_b32 %1, %4, %5, %6\n\ts_nop 1\n\tv_permlane32_swap_b32 %0, %1\n\tv_add_f32 %0, %0, %1"
                                 : "=&v"(v), "=&v"(w) : "v"(d[0]), "v"(d[1]), "v"(d[2]), "v"(d[3]), "s"(hi));
                    fin(v);
                    __builtin_amdgcn_sched_barrier(0);
                };
                sum();
                if (__builtin_expect(any_nan(v, v) != 0ull, 0)) {
#pragma unroll 1
                    for (unsigned spins = 0; spins < kSpinCap; ++spins) {
                        slip(X);
                        again16<-2048>(a0, ws, o); again16<-1024>(a1, ws, o); again16<0>(a2, ws, o); again16<1024>(a3, ws, o);
                        asm volatile("s_waitcnt vmcnt(0)" : "+v"(a0), "+v"(a1), "+v"(a2), "+v"(a3) :: "memory");
                        if (!any_empty(max(max(um4(a0), um4(a1)), max(um4(a2), um4(a3))))) break;
                    }
                    sum();
                }
            };
            auto pack4 = [&](float c0, float c1, float c2, float c3) __attribute__((always_inline)) { const unsigned w0 = min(pk2(c0, c1), 0xfffffffeu), w1 = min(pk2(c2, c3), 0xfffffffeu); return (u32x2){w0, w1}; };
            unsigned xw;
            __builtin_amdgcn_s_setprio(0);
            {
                LANE_VARS();
                if (layer == 0) xe = lo16(*reinterpret_cast<const u16*>(smem + L_X0 + kq * 1280 + gj * 160 + t * 32 + n * 2));
                STAMP(6 + 32 * layer);
                unsigned lo = WS_OWN + (unsigned)(gi * 16384 + layer * 512 + (lane >> 3) * 2048 + t * 128 + (t == 3 ? 0 : (lane & 7) * 16));
                unsigned lw = lb + L_O2 + (unsigned)((lane >> 3) * 416 + t * 128 + (t == 3 ? 0 : (lane & 7) * 16));
                PIN2(lo, lw);
                u32x4 ov = {~0u, ~0u, ~0u, ~0u};
                if (layer != 0 && t < 4) { sched(X2, 20); STAMP(49); ov = ld16_sc1(ws, lo); }
                const unsigned wa = lb + L_WO + (unsigned)(r0s * 160 + 32 * t + 8 * p4), wa1 = lb + L_WO + (unsigned)(r1s * 160 + 32 * t + 8 * p4);
                bf16x8 af[6];
                if (layer == 0) {
                    if (t < 4) {
                        unsigned tf = 0u;
#pragma unroll 1
                        for (unsigned spins = 0; spins < (kSpinCap << 4); ++spins) { tf = lds_word(lb + L_CNT + 48); if (tf) break; }
                        slot(NUNIT + XG2);
                        until(tf + DG2);
                        ov = ld16_sc1(ws, lo);
                        if (GOPT) lds_add(72, 1u);
                        if (nj) { WAIT1(ov, 1); lds_add(44, nj); }
                    } else { VMW(0); lds_add(44, nj); }
#pragma unroll 1
                    for (unsigned spins = 0; spins < (kSpinCap << 4); ++spins) if (lds_word(lb + L_CNT + 44) >= 5u) break;
                    STAMP(8);
                }
#pragma unroll
                for (int s = 0; s < 6; ++s) af[s] = cat8(tr64(wa + s * 32 * 160), tr64(wa1 + s * 32 * 160));
                const unsigned h3 = WS_H35 + (unsigned)((gj * 20 + 10 * layer + t) * 4096);
                unsigned so = h3 + (unsigned)(gi * 128 + (lane & 2) * 32 + (lane & 4) * 8 + kq * 8), go = h3 + (unsigned)(2048 + lane * 16);
                unsigned xs = lb + L_XS + (unsigned)(kq * 160 + t * 32 + n * 2);
                unsigned t3 = layer ? 0u : lds_word(lb + L_CNT + 64) + C3;
                unsigned br = lb + L_O2 + (unsigned)((n & 7) * 416 + kq * 16), mr = lb + L_O2 + (unsigned)((n & 7) * 416 + 384);
                unsigned long long mk = 0x0055005500550055ull;
                PIN3(af[0], af[1], af[2]); PIN3(af[3], af[4], af[5]); PIN3(br, mr, so); PIN2(go, xs); PINS(mk); PINS(t3);
                if (t < 4) {
                    WAIT1(ov, 0);
                    *(LAS u32x4*)(size_t)lw = ov;
                    if (layer == 0 ? any_empty(um4(ov)) : __builtin_expect(any_empty(um4(ov)), 0)) {
#pragma unroll 1
                        for (unsigned spins = 0; spins < kSpinCap; ++spins) {
                            if (layer != 0) slip(X2);
                            ov = ld16_sc1(ws, lo);
                            WAIT1(ov, 0);
                            *(LAS u32x4*)(size_t)lw = ov;
                            if (!any_empty(um4(ov))) break;
                            if (layer == 0) { if ((spins & 15u) == 15u) send_flag(lane); __builtin_amdgcn_s_sleep(1); }
                        }
                    }
                }
                BAR();
                bf16x8 bf[6];
                const f32x2 ml = *(const LAS f32x2*)(size_t)mr;
#pragma unroll
                for (int s = 0; s < 6; ++s) bf[s] = lds16(br + s * 64);
                __builtin_amdgcn_sched_barrier(0);
                f32x4 ca = z4;
#pragma unroll
                for (int s = 0; s < 6; ++s) ca = mfma32(af[s], bf[s], ca);
                const float e = ex2(ml[0] - fmax2(ml[0], dpp<0xB1>(ml[0]))), el = e * ml[1];
                const float f = e * frcp(el + dpp<0xB1>(el));
                float c0, c1, c2, c3;
                asm volatile("s_nop 7\n\t"
                             "v_mul_f32 %0, %4, %8\n\tv_mul_f32 %1, %5, %8\n\tv_mul_f32 %2, %6, %8\n\tv_mul_f32 %3, %7, %8\n\t"
                             "v_add_f32_dpp %0, %0, %0 quad_perm:[1,0,3,2] row_mask:0xf bank_mask:0xf\n\tv_add_f32_dpp %1, %1, %1 quad_perm:[1,0,3,2] row_mask:0xf bank_mask:0xf\n\t"
                             "v_add_f32_dpp %2, %2, %2 quad_perm:[1,0,3,2] row_mask:0xf bank_mask:0xf\n\tv_add_f32_dpp %3, %3, %3 quad_perm:[1,0,3,2] row_mask:0xf bank_mask:0xf"
                             : "=&v"(c0), "=&v"(c1), "=&v"(c2), "=&v"(c3) : "v"(ca[0]), "v"(ca[1]), "v"(ca[2]), "v"(ca[3]), "v"(f));
                st8m(ws, so, pack4(c0, c1, c2, c3), mk);
                if (layer == 0 && wave == 7) st4_sc1(ws, WS_MB + (unsigned)(gi * 1024 + 64 + gj * 4), now());
                if (layer == 0) lds_add(76, 1u);
                STAMP(7 + 32 * layer);
                if (layer != 0) sched(X3, 12);
                else {
                    if (POLL3) wait_word(ws, h3 + (unsigned)(((gi * 7 + t * 5 + 3) & 31) * 128));
                    else {
                        const unsigned n2 = now() + 20;
                        until((int)(n2 - t3) > 0 ? n2 : t3);
                    }
                }
                STAMP(18 + 32 * layer);
                gather32(go, lane, layer ? X3 : 0u, [&]() { sa = stats_addr(lane, h4 + (unsigned)(15360 + gj * 128), 16); }, [&](float s) { xw = pk2(xe + bfr(s), 0.f); *(LAS u16*)(size_t)xs = (u16)xw; });
                xe = lo16(xw);
                STAMP(9 + 32 * layer);
            }
            BAR();
            if (wave == 3) stats(sa);
            STAMP(11 + 32 * layer);
            {
                LANE_VARS();
                const unsigned im = lb + L_WD + (unsigned)(32 * t + 8 * p4);
                if (layer == 0 && !((EARLY | GOPT | GB4 | GST) & 4)) {
#pragma unroll 1
                    for (unsigned spins = 0; spins < (kSpinCap << 4); ++spins) { if (lds_word(lb + L_CNT + 68) >= 3u) break; __builtin_amdgcn_s_sleep(1); }
                }
                bf16x8 wd0 = cat8(tr64(im + r0s * 160), tr64(im + r1s * 160)); bf16x4 wd1 = tr64(im + (32 + r2s) * 160);
                const unsigned h5 = WS_H35 + (unsigned)((gj * 20 + 10 * layer + 5 + t) * 4096);
                unsigned h16 = lb + L_H + (unsigned)(p4 * 96 + kq * 16), h8 = lb + L_H + (unsigned)(p4 * 96 + 64 + kq * 8);
                unsigned so = h5 + (unsigned)(gi * 128 + (lane & 1) * 64 + (lane & 2) * 16 + kq * 8);
                unsigned long long mk = 0x000f000f000f000full;
                PIN2(wd0, wd1); PIN3(h16, h8, so); PINS(mk);
                if (wave == 4) {
                    const unsigned os = h4 + (unsigned)(15360 + (p4 + 4 * (kq & 1)) * 128 + q4 * 16);
                    if (layer == 0) {
#pragma unroll 1
                        for (unsigned spins = 0; spins < (kSpinCap << 4); ++spins) if (lds_word(lb + L_CNT + 56)) break;
                    }
                    sched(layer ? X4 : X4A, 0);
                    u32x4 as;
#pragma unroll 1
                    for (unsigned spins = 0; spins < kSpinCap; ++spins) { as = ld16_sc1(ws, os); WAIT1(as, 0); if (!any_empty(um4(as))) break; }
                    float ss = __uint_as_float(as[0]), sm = __uint_as_float(as[1]), rd = __uint_as_float(as[2]);
                    ss += dpp<0xB1>(ss); sm += dpp<0xB1>(sm); rd += dpp<0xB1>(rd);
                    ss += dpp<0x4E>(ss); sm += dpp<0x4E>(sm); rd += dpp<0x4E>(rd);
                    ss = x16_add(ss); sm = x16_add(sm); rd = x16_add(rd);
                    if (LANES(0x1111ull)) *reinterpret_cast<f32x4*>(smem + L_ST + q4 * 16) = (f32x4){ss, sm, rd, 0.f};
                }
                BAR();
                STAMP(12 + 32 * layer);
                const bf16x8 hb0 = lds16(h16); const bf16x4 hb1 = lds8(h8);
                __builtin_amdgcn_sched_barrier(0);
                const f32x4 c = mfma32(wd0, hb0, z4) + mfma16(wd1, hb1, z4);
                st8m(ws, so, pack4(c[0], c[1], c[2], c[3]), mk);
                STAMP(13 + 33 * layer);
                if (layer == 0 || gi == 4 * t + (gj & 3)) {
                    float rs, n1;
                    auto prep = [&]() __attribute__((always_inline)) {
                        const f32x4 sr = *reinterpret_cast<const f32x4*>(smem + L_ST + kq * 16);
                        const float sw = *reinterpret_cast<const float*>(smem + L_CNT + 12);
                        const f32x2 st = ln_stats(sr[0], sr[1]);
                        const float r = bfr((sr[2] - st[0] * sw) * st[1]);
                        rs = bfr(frcp(1.f + ex2(r * -LOG2E)));
                        n1 = bfr(bfr(xe - st[0]) * st[1]);
                        PIN2(rs, n1);
                    };
                    if (layer == 0) {
                        unsigned go = h5 + (unsigned)(2048 + lane * 16), xs = lb + L_XS + (unsigned)(kq * 160 + t * 32 + n * 2);
                        PIN2(go, xs);
                        sched(X5A, 8);
                        STAMP(15);
                        gather32(go, lane, X5A, [&]() { prep(); sa = stats_addr(lane, WS_H1 + (unsigned)((gi * 32 + gj) * 896 + 768), 7168); },
                                 [&](float y) { xw = pk2(xe + bfr(n1 + bfr(bfr(y) * rs)), 0.f); *(LAS u16*)(size_t)xs = (u16)xw; });
                        xe = lo16(xw);
                        STAMP(14);
                    } else {
                        unsigned go = h5 + (unsigned)(2048 + lane * 16), oo = (unsigned)(kq * 1280 + gj * 160 + t * 32 + n * 2);
                        PIN2(go, oo);
                        sched(X5B, 8);
                        STAMP(47);
                        gather32(go, lane, X5B, prep, [&](float y) { xw = pk2(xe + bfr(n1 + bfr(bfr(y) * rs)), 0.f); });
                        { const unsigned w = xw | (dppu<0xF5>(xw) << 16);
                          const u32x2 w2 = {w, dppu<0xAA>(w)};
                          asm volatile("s_mov_b64 exec, %3\n\tglobal_store_dwordx2 %0, %1, %2 sc0 sc1\n\ts_mov_b64 exec, -1" :: "v"(oo), "v"(w2), "s"(out), "s"(0x1111111111111111ull) : "memory"); }
                        STAMP(45);
                    }
                }
            }
            if (layer == 0) {
                BAR();
                if (wave == 3) { stats(sa); STAMP(34); }
                asm volatile("" : "=v"(cma), "=v"(cxa), "=v"(cra), "=v"(cso));
                if (wave != 7) { qk_addr(true); att_addr(); }
                else {
                    const int lane = lane_id();
                    unsigned o = WS_H1 + (unsigned)((gi * 4 + (gj >> 1)) * 7168 + 3584 + (lane < 48 ? lane : 48) * 16), qw;
                    f32x4 cs;
                    PIN1(o);
                    sched(X1, 26);
                    STAMP(51);
                    f32x4 s; u32x4 a[8];
                    RETRY8(a, o, 896, s, { qw = lb + L_Q + (unsigned)((lane >> 2) * 32 + (lane & 3) * 4); cs = *reinterpret_cast<const f32x4*>(smem + L_CS + lane * 16); PIN2(qw, cs); qk_addr(true); att_addr(); }, { s -= bfr(__int_as_float(__builtin_amdgcn_readlane(__float_as_int(s[1]), 48)) * (1.f / DM)) * cs;
                                           u32x2 w; w[0] = pk2(s[0], s[1]); w[1] = pk2(s[2], s[3]);
                                           if (LANES(0xffff000000000000ull)) { w[0] = __float_as_uint(s[0]); w[1] = __float_as_uint(s[1]); }
                                           *(LAS unsigned*)(size_t)qw = w[0]; *(LAS unsigned*)(size_t)(qw + 16) = w[1]; __builtin_amdgcn_sched_barrier(0); }, any_nan(s[0] + s[2], s[1] + s[3]), X1);
                }
            }
        }
    };
    PINS(ck1); PINS(ck2); PINS(csc);
    chain(IC<0>{});
    STAMP(35);
    att1();
    chain(IC<1>{});
}

__device__ __attribute__((used, noinline)) void mega_code_end() { asm volatile(".Lmega_code_end:"); }

extern "C" void kernel_launch(void* const* d_in, const int* in_sizes, int n_in, void* d_out, int out_size, void* d_ws,
                              size_t ws_size, hipStream_t stream) {
    const u16* x0 = (const u16*)d_in[0];
    const u16* kc = (const u16*)d_in[1];
    const u16* vc = (const u16*)d_in[2];
    const u16* wq = (const u16*)d_in[3];
    const u16* wo = (const u16*)d_in[4];
    const u16* wr = (const u16*)d_in[6];
    const u16* wg = (const u16*)d_in[7];
    const u16* wu = (const u16*)d_in[8];
    const u16* wd = (const u16*)d_in[9];
    (void)hipFuncSetAttribute((const void*)mega_kernel, hipFuncAttributeMaxDynamicSharedMemorySize, L_END);
    mega_kernel<<<256, 512, L_END, stream>>>(x0, kc, vc, wq, wo, wr, wg, wu, wd, (u16*)d_out, (unsigned char*)d_ws);
}
```

```cpp
#include <hip/hip_runtime.h>
#include <hip/hip_bf16.h>
#include <stdint.h>

typedef unsigned short u16;
typedef unsigned u32x4 __attribute__((ext_vector_type(4)));
typedef unsigned u32x2 __attribute__((ext_vector_type(2)));
typedef float f32x4 __attribute__((ext_vector_type(4)));
typedef short s16x4 __attribute__((ext_vector_type(4)));
typedef __attribute__((ext_vector_type(4))) __bf16 bf16x4;
typedef __attribute__((ext_vector_type(8))) __bf16 bf16x8;
#define LAS __attribute__((address_space(3)))

constexpr int DM = 640, HQ = 32, SK = 4096, DH = 192, WIN = 1024, FF = 1536;
constexpr int QD = HQ * DH;
constexpr float SCALE = 0.07216878364870322f, LOG2E = 1.44269504088896341f;
constexpr unsigned kSpinCap = 1u << 16;
#define GJ(g) (((g) >> 3) & 7)
#define GI(g) (((g) & 7) * 4 + ((g) >> 6))

#define D4A 148
#define D5A 104
#define D1 112
#define D2 162
#define D3 76
#define D4 96
#define D5B 92
#define PACE 62
#define PINIT 100
#define PMIN 50
constexpr unsigned X4A = D4A, X5A = X4A + D5A, X1 = X5A + D1, X2 = X1 + D2, X3 = X2 + D3, X4 = X3 + D4, X5B = X4 + D5B;
#define WOC 2
#define GUC 3
#define DG2 15
#define DGU 25
#define EARLY 7
#define WEARLY 1
#define WEC 3
#define XCOST 16
constexpr unsigned NUNIT = 384 + XCOST + 9 * ((EARLY & 1) + ((EARLY >> 1) & 1) + ((EARLY >> 2) & 1)) + 2 * WEC * WEARLY, PBURST = 30;
#define GSTEPS GP = 0, GC = 3, GD = 5, GL = 0, GE = 2, GF = 4
#define C3 (PINIT + NUNIT * PACE / 16 + E3)
#define DWD 20
#define ADOPT 1
#define TOFF 15
#define FX 0
#define PRIO 0
#define GOPT 0
#define GB4 0
#define GST 0
#define POLL3 1
#define M3 20
#define E3 330
#define D4S 125
#define XG2 0
#define WOFF 6
#define FREEW(w) ((w) >= 6)
#define LEAD 12
#define DQ 40
#define DMB 30
constexpr int GSTEPS;
constexpr int QKT = 2;

constexpr unsigned WS_R = 0;
constexpr unsigned WS_B = 1024;
constexpr unsigned WS_MB = 4096;
constexpr unsigned WS_H1 = WS_MB + 256 * 128;
constexpr unsigned WS_OWN = WS_H1 + 32 * 4 * 7168;
constexpr unsigned WS_H35 = WS_OWN + 256 * 2048;
constexpr unsigned WS_H4 = WS_H35 + 160 * 4096;
constexpr unsigned WS_END = WS_H4 + 64 * 16384;
constexpr unsigned WS_DBG = 6680576;
static_assert(WS_END <= WS_DBG, "ws map");

constexpr int L_WQ = 0;
constexpr int L_WO = 33792;
constexpr int L_GU = L_WO + 30720;
constexpr int L_WD = L_GU + 16384;
constexpr int L_X0 = L_WD + 8192;
constexpr int L_WR = L_X0 + 5120;
constexpr int L_XS = L_WR + 2048;
constexpr int L_Q = L_XS + 1024;
constexpr int L_OP = L_Q + 512;
constexpr int L_M = L_OP + 8192;
constexpr int L_H = L_M + 64;
constexpr int L_ST = L_H + 384;
constexpr int L_CS = L_ST + 64;
constexpr int L_CS2 = L_CS + 1024;
constexpr int L_CNT = L_CS2 + 1536;
constexpr int L_P = L_CNT + 80;
constexpr int L_O2 = L_P + 72 * 192;
constexpr int L_DBG = L_O2 + 3328;
constexpr int L_END = L_DBG + 2048;
static_assert(L_END <= 163840 && L_END > 81920, "LDS");

typedef __attribute__((ext_vector_type(2))) __bf16 bf16x2;
typedef float f32x2 __attribute__((ext_vector_type(2)));
__device__ __forceinline__ unsigned pk2(float a, float b) { return __builtin_bit_cast(unsigned, __builtin_convertvector((f32x2){a, b}, bf16x2)); }
__device__ __forceinline__ float lo16(unsigned w) { return __uint_as_float(w << 16); }
__device__ __forceinline__ float hi16(unsigned w) { return __uint_as_float(w & 0xffff0000u); }
__device__ __forceinline__ float bfr(float f) { return __uint_as_float(pk2(0.f, f)); }
template <int CTRL> __device__ __forceinline__ float dpp(float x) {
    return __builtin_bit_cast(float, __builtin_amdgcn_mov_dpp(__builtin_bit_cast(int, x), CTRL, 0xf, 0xf, true));
}
template <int CTRL> __device__ __forceinline__ unsigned dppu(unsigned x) { return (unsigned)__builtin_amdgcn_mov_dpp((int)x, CTRL, 0xf, 0xf, true); }
__device__ __forceinline__ float row16_sum(float v) {
    v += dpp<0xB1>(v); v += dpp<0x4E>(v); v += dpp<0x141>(v); v += dpp<0x140>(v); return v;
}
__device__ __forceinline__ float fmax2(float a, float b) { return __builtin_elementwise_maximum(a, b); }
__device__ __forceinline__ float fmax3(float a, float b, float c) { return fmax2(fmax2(a, b), c); }
__device__ __forceinline__ float x16_add(float v) { auto s = __builtin_amdgcn_permlane16_swap(__float_as_uint(v), __float_as_uint(v), false, false); return __uint_as_float(s[0]) + __uint_as_float(s[1]); }
__device__ __forceinline__ float x32_add(float v) { auto s = __builtin_amdgcn_permlane32_swap(__float_as_uint(v), __float_as_uint(v), false, false); return __uint_as_float(s[0]) + __uint_as_float(s[1]); }
__device__ __forceinline__ float wave_sum(float v) { return x32_add(x16_add(row16_sum(v))); }
__device__ __forceinline__ float rs32(float a, float b) { auto s = __builtin_amdgcn_permlane32_swap(__float_as_uint(a), __float_as_uint(b), false, false); return __uint_as_float(s[0]) + __uint_as_float(s[1]); }
__device__ __forceinline__ f32x2 sw16(float a, float b) { auto s = __builtin_amdgcn_permlane16_swap(__float_as_uint(a), __float_as_uint(b), false, false); return (f32x2){__uint_as_float(s[0]), __uint_as_float(s[1])}; }

#define BAR() asm volatile("s_waitcnt lgkmcnt(0)\n\ts_barrier" ::: "memory")
#define VMW(n) asm volatile("s_waitcnt vmcnt(%0)" :: "i"(n) : "memory")

template <int OFF = 0> __device__ __forceinline__ u32x4 ld16_sc1(const void* sbase, unsigned voff) {
    u32x4 r; asm volatile("global_load_dwordx4 %0, %1, %2 offset:%3 sc1" : "=v"(r) : "v"(voff), "s"(sbase), "i"(OFF) : "memory"); return r;
}
__device__ __forceinline__ unsigned ld4_sc1(const void* sbase, unsigned voff) {
    unsigned r; asm volatile("global_load_dword %0, %1, %2 sc1" : "=v"(r) : "v"(voff), "s"(sbase) : "memory"); return r;
}
template <int OFF = 0> __device__ __forceinline__ void st16_sc1(void* sbase, unsigned voff, u32x4 v) {
    asm volatile("global_store_dwordx4 %0, %1, %2 offset:%3 sc1\n\ts_nop 1" :: "v"(voff), "v"(v), "s"(sbase), "i"(OFF) : "memory");
}
__device__ __forceinline__ void st4_sc1(void* sbase, unsigned voff, unsigned v) {
    asm volatile("global_store_dword %0, %1, %2 sc1" :: "v"(voff), "v"(v), "s"(sbase) : "memory");
}
__device__ __forceinline__ void st8(void* sbase, unsigned voff, u32x2 v) {
    asm volatile("global_store_dwordx2 %0, %1, %2" :: "v"(voff), "v"(v), "s"(sbase) : "memory");
}
__device__ __forceinline__ void st2(void* sbase, unsigned voff, unsigned v) {
    asm volatile("global_store_short %0, %1, %2" :: "v"(voff), "v"(v), "s"(sbase) : "memory");
}
constexpr int RK = 64, RV = 160;
template <int R, int IMM> __device__ __forceinline__ void ld16_nt(const void* sbase, unsigned voff) {
    if (false) asm volatile("global_load_dwordx4 v[%c3:%c4], %0, %1 offset:%2" :: "v"(voff), "s"(sbase), "i"(IMM), "i"(R), "i"(R + 3) : "memory");
    else asm volatile("global_load_dwordx4 v[%c3:%c4], %0, %1 offset:%2 nt" :: "v"(voff), "s"(sbase), "i"(IMM), "i"(R), "i"(R + 3) : "memory");
}
template <int R, int IMM> __device__ __forceinline__ void ld8_nt(const void* sbase, unsigned voff) {
    if (false) asm volatile("global_load_dwordx2 v[%c3:%c4], %0, %1 offset:%2" :: "v"(voff), "s"(sbase), "i"(IMM), "i"(R), "i"(R + 1) : "memory");
    else asm volatile("global_load_dwordx2 v[%c3:%c4], %0, %1 offset:%2 nt" :: "v"(voff), "s"(sbase), "i"(IMM), "i"(R), "i"(R + 1) : "memory");
}
template <int R> __device__ __forceinline__ void smf(f32x4& d, u32x4 a, unsigned idx) {
    asm volatile("v_smfmac_f32_16x16x64_bf16 %0, %1, v[%c3:%c4], %2" : "+v"(d) : "v"(a), "v"(idx), "i"(R), "i"(R + 7));
}
template <int A, int B> __device__ __forceinline__ void zip_in() {
    unsigned t;
    asm volatile("v_perm_b32 %0, v%c2, v%c1, %3\n\tv_perm_b32 v%c2, v%c2, v%c1, %4\n\tv_mov_b32 v%c1, %0" : "=&v"(t) : "i"(A), "i"(B), "s"(0x05040100u), "s"(0x07060302u));
}
template <int A, int B> __device__ __forceinline__ void zip_out(unsigned& lo, unsigned& hi) {
    asm volatile("v_perm_b32 %0, v%c3, v%c2, %4\n\tv_perm_b32 %1, v%c3, v%c2, %5" : "=&v"(lo), "=&v"(hi) : "i"(A), "i"(B), "s"(0x05040100u), "s"(0x07060302u));
}
template <int A, int B, int C, int D> __device__ __forceinline__ void zip_x(unsigned lo, unsigned hi) {
    asm volatile("v_perm_b32 v%c4, v%c3, v%c2, %6\n\tv_perm_b32 v%c5, v%c3, v%c2, %7\n\tv_mov_b32 v%c2, %0\n\tv_mov_b32 v%c3, %1"
                 :: "v"(lo), "v"(hi), "i"(A), "i"(B), "i"(C), "i"(D), "s"(0x05040100u), "s"(0x07060302u));
}
__device__ __forceinline__ void dma16(const void* sbase, unsigned voff, unsigned lds_dst) {
    unsigned keep;
    asm volatile("s_mov_b32 %0, m0\n\ts_mov_b32 m0, %3\n\ts_nop 0\n\tglobal_load_lds_dwordx4 %1, %2\n\ts_mov_b32 m0, %0"
                 : "=&s"(keep) : "v"(voff), "s"(sbase), "s"(lds_dst) : "memory");
}
__device__ __forceinline__ void dma16p(const void* sbase, unsigned voff, unsigned lds_dst) {
    unsigned keep;
    asm volatile("s_mov_b32 %0, m0\n\ts_mov_b32 m0, %3\n\ts_nop 0\n\tglobal_load_lds_dwordx4 %1, %2\n\ts_mov_b32 m0, %0"
                 : "=&s"(keep) : "v"(voff), "s"(sbase), "s"(lds_dst) : "memory");
}
__device__ __forceinline__ unsigned um4(u32x4 a) { return max(max(a[0], a[1]), max(a[2], a[3])); }
__device__ __forceinline__ bool none(unsigned t) { return __builtin_amdgcn_ballot_w64(t != 0u) == 0ull; }
__device__ __forceinline__ bool any_empty(unsigned mx) { return __builtin_amdgcn_ballot_w64(mx == ~0u) != 0ull; }
__device__ __forceinline__ unsigned long long any_nan(float a, float b) { return __builtin_amdgcn_ballot_w64(__builtin_isunordered(a, b)); }
__device__ __forceinline__ f32x4 asf(u32x4 a) { return __builtin_bit_cast(f32x4, a); }
__device__ __forceinline__ u32x4 asu(f32x4 a) { return __builtin_bit_cast(u32x4, a); }
#define LANES(m) __builtin_amdgcn_inverse_ballot_w64(m)

__device__ __forceinline__ bf16x4 tr64(unsigned lds_addr) { return __builtin_amdgcn_ds_read_tr16_b64_v4bf16((LAS bf16x4*)(size_t)lds_addr); }
__device__ __forceinline__ bf16x8 cat8(bf16x4 a, bf16x4 b) { return __builtin_shufflevector(a, b, 0, 1, 2, 3, 4, 5, 6, 7); }
__device__ __forceinline__ bf16x8 lds16(unsigned a) { return *(const LAS bf16x8*)(size_t)a; }
__device__ __forceinline__ bf16x4 lds8(unsigned a) { return *(const LAS bf16x4*)(size_t)a; }
__device__ __forceinline__ f32x4 mfma32(bf16x8 a, bf16x8 b, f32x4 c) { return __builtin_amdgcn_mfma_f32_16x16x32_bf16(a, b, c, 0, 0, 0); }
__device__ __forceinline__ f32x4 mfma32u(u32x4 a, u32x4 b, f32x4 c) { return mfma32(__builtin_bit_cast(bf16x8, a), __builtin_bit_cast(bf16x8, b), c); }
__device__ __forceinline__ f32x4 mfma16(bf16x4 a, bf16x4 b, f32x4 c) {
    return __builtin_amdgcn_mfma_f32_16x16x16bf16_1k(__builtin_bit_cast(s16x4, a), __builtin_bit_cast(s16x4, b), c, 0, 0, 0);
}

#define STAMP(i) do {} while (0)

__device__ __forceinline__ void wait_word(const void* sbase, unsigned off) {
    unsigned t, cnt = kSpinCap;
    asm volatile("1:\n\tglobal_load_dword %0, %2, %3 sc1\n\ts_sub_u32 %1, %1, 1\n\ts_waitcnt vmcnt(0)\n\tv_cmp_eq_u32 vcc, -1, %0\n\ts_cbranch_vccz 2f\n\ts_cbranch_scc0 1b\n2:"
                 : "=&v"(t), "+s"(cnt) : "v"(off), "s"(sbase) : "memory", "vcc", "scc");
}
template <int OFF = 0> __device__ __forceinline__ void st16m(void* sbase, unsigned voff, u32x4 v, unsigned long long mask) {
    asm volatile("s_mov_b64 exec, %4\n\tglobal_store_dwordx4 %0, %1, %2 offset:%3 sc1\n\ts_mov_b64 exec, -1\n\ts_nop 0" :: "v"(voff), "v"(v), "s"(sbase), "i"(OFF), "s"(mask) : "memory");
}
__device__ __forceinline__ void st8m(void* sbase, unsigned voff, u32x2 v, unsigned long long mask) {
    asm volatile("s_mov_b64 exec, %3\n\tglobal_store_dwordx2 %0, %1, %2 sc1\n\ts_mov_b64 exec, -1" :: "v"(voff), "v"(v), "s"(sbase), "s"(mask) : "memory");
}
template <int OFF> __device__ __forceinline__ void st16m2(void* sbase, unsigned voff, u32x4 v, unsigned voff2, u32x4 v2, unsigned long long mask) {
    asm volatile("s_mov_b64 exec, %6\n\tglobal_store_dwordx4 %0, %1, %2 sc1\n\tglobal_store_dwordx4 %4, %5, %2 offset:%3 sc1\n\ts_mov_b64 exec, -1\n\ts_nop 0"
                 :: "v"(voff), "v"(v), "s"(sbase), "i"(OFF), "v"(voff2), "v"(v2), "s"(mask) : "memory");
}
#define PINS(a) asm volatile("" : "+s"(a))
#define RETRY(LOAD, EMPTY, COMPUTE, NAN_, X) do { LOAD; COMPUTE; if (__builtin_expect((NAN_) != 0ull, 0)) { \
    _Pragma("unroll 1") for (unsigned spins_ = 0; spins_ < kSpinCap; ++spins_) { slip(X); LOAD; if (!any_empty(EMPTY)) break; } COMPUTE; } } while (0)
__device__ __forceinline__ unsigned now() { return (unsigned)__builtin_amdgcn_s_memrealtime(); }
__device__ __forceinline__ void until(unsigned t) {
    asm volatile("1:\n\ts_memrealtime vcc\n\ts_waitcnt lgkmcnt(0)\n\ts_sub_u32 vcc_lo, %0, vcc_lo\n\ts_cmp_lt_u32 vcc_lo, 0x400\n\ts_cbranch_scc1 1b" :: "s"(t - 1u) : "vcc", "scc", "memory");
}
__device__ __forceinline__ unsigned lds_word(unsigned a) {
    unsigned t_; asm volatile("ds_read_b32 %0, %1\n\ts_waitcnt lgkmcnt(0)\n\ts_nop 0" : "=v"(t_) : "v"(a) : "memory");
    return __builtin_amdgcn_readfirstlane(t_);
}
__device__ __forceinline__ void lds_set(unsigned a, unsigned v) { asm volatile("ds_write_b32 %0, %1" :: "v"(a), "v"(v) : "memory"); }
__device__ __forceinline__ int swz(int r) { return r ^ (((r >> 3) & 1) << 2); }
__device__ __forceinline__ f32x2 unpk(unsigned w) { return (f32x2){lo16(w), hi16(w)}; }
__device__ __forceinline__ unsigned pk2v(f32x2 v) { return __builtin_bit_cast(unsigned, __builtin_convertvector(v, bf16x2)); }
__device__ __forceinline__ float frcp(float x) { return __builtin_amdgcn_rcpf(x); }
__device__ __forceinline__ float ex2(float x) { return __builtin_amdgcn_exp2f(x); }
__device__ __forceinline__ int lane_id() { int l; asm volatile("v_mbcnt_lo_u32_b32 %0, -1, 0\n\tv_mbcnt_hi_u32_b32 %0, -1, %0" : "=v"(l)); return l & 63; }
__device__ __forceinline__ f32x2 ln_stats(float ss, float s) {
    const float mean = s * (1.f / DM);
    const float var = fmax2(__builtin_fmaf(-mean, mean, ss * (1.f / DM)), 0.f);
    const unsigned mv = pk2(mean, var);
    return (f32x2){lo16(mv), frcp(bfr(__builtin_amdgcn_sqrtf(bfr(hi16(mv) + 1e-5f))))};
}
#define GATHER8(a, o, S) do { a[0] = ld16_sc1<-4 * (S)>(ws, o); a[1] = ld16_sc1<-3 * (S)>(ws, o); a[2] = ld16_sc1<-2 * (S)>(ws, o); a[3] = ld16_sc1<-(S)>(ws, o); \
    a[4] = ld16_sc1<0>(ws, o); a[5] = ld16_sc1<(S)>(ws, o); a[6] = ld16_sc1<2 * (S)>(ws, o); a[7] = ld16_sc1<3 * (S)>(ws, o); } while (0)
template <int OFF> __device__ __forceinline__ void again16(u32x4& r, const void* sbase, unsigned voff) {
    asm volatile("v_cmp_eq_u32 vcc, -1, %1\n\ts_mov_b64 exec, vcc\n\tglobal_load_dwordx4 %0, %2, %3 offset:%4 sc1\n\ts_mov_b64 exec, -1" : "+v"(r) : "v"(um4(r)), "v"(voff), "s"(sbase), "i"(OFF) : "memory", "vcc");
}
#define AGAIN8(a, o, S) do { again16<-4 * (S)>(a[0], ws, o); again16<-3 * (S)>(a[1], ws, o); again16<-2 * (S)>(a[2], ws, o); again16<-(S)>(a[3], ws, o); \
    again16<0>(a[4], ws, o); again16<(S)>(a[5], ws, o); again16<2 * (S)>(a[6], ws, o); again16<3 * (S)>(a[7], ws, o); } while (0)
#define WAIT8(a, N) asm volatile("s_waitcnt vmcnt(%8)" : "+v"((a)[0]), "+v"((a)[1]), "+v"((a)[2]), "+v"((a)[3]), "+v"((a)[4]), "+v"((a)[5]), "+v"((a)[6]), "+v"((a)[7]) : "i"(N) : "memory")
#define UM32(a) max(max(max(um4(a[0]), um4(a[1])), max(um4(a[2]), um4(a[3]))), max(max(um4(a[4]), um4(a[5])), max(um4(a[6]), um4(a[7]))))
#define SUM8(a) (((((((asf(a[0]) + asf(a[1])) + asf(a[2])) + asf(a[3])) + asf(a[4])) + asf(a[5])) + asf(a[6])) + asf(a[7]))
#define WAIT1(x, N) asm volatile("s_waitcnt vmcnt(%1)" : "+v"(x) : "i"(N) : "memory")
#define LO2(x) __builtin_shufflevector(x, x, 0, 1)
#define HI2(x) __builtin_shufflevector(x, x, 2, 3)
#define ACC8(a, s) do { f32x2 l_, h_; \
    asm volatile("s_waitcnt vmcnt(5)\n\tv_pk_add_f32 %0, %2, %4\n\tv_pk_add_f32 %1, %3, %5\n\tv_pk_add_f32 %0, %0, %6\n\tv_pk_add_f32 %1, %1, %7\n\t" \
                 "s_waitcnt vmcnt(3)\n\tv_pk_add_f32 %0, %0, %8\n\tv_pk_add_f32 %1, %1, %9\n\tv_pk_add_f32 %0, %0, %10\n\tv_pk_add_f32 %1, %1, %11\n\t" \
                 "s_waitcnt vmcnt(1)\n\tv_pk_add_f32 %0, %0, %12\n\tv_pk_add_f32 %1, %1, %13\n\tv_pk_add_f32 %0, %0, %14\n\tv_pk_add_f32 %1, %1, %15\n\t" \
                 "s_waitcnt vmcnt(0)\n\tv_pk_add_f32 %0, %0, %16\n\tv_pk_add_f32 %1, %1, %17\n\ts_nop 0" \
                 : "=&v"(l_), "=&v"(h_) : "v"(LO2(a[0])), "v"(HI2(a[0])), "v"(LO2(a[1])), "v"(HI2(a[1])), "v"(LO2(a[2])), "v"(HI2(a[2])), "v"(LO2(a[3])), "v"(HI2(a[3])), \
                   "v"(LO2(a[4])), "v"(HI2(a[4])), "v"(LO2(a[5])), "v"(HI2(a[5])), "v"(LO2(a[6])), "v"(HI2(a[6])), "v"(LO2(a[7])), "v"(HI2(a[7])) : "memory"); \
    s = __builtin_shufflevector(l_, h_, 0, 1, 2, 3); } while (0)
#define RETRY8(a, o, S, s, MID, POST, NAN_, X) do { GATHER8(a, o, S); MID; ACC8(a, s); POST; if (__builtin_expect((NAN_) != 0ull, 0)) { \
    _Pragma("unroll 1") for (unsigned spins_ = 0; spins_ < kSpinCap; ++spins_) { slip(X); AGAIN8(a, o, S); WAIT8(a, 0); if (!any_empty(UM32(a))) break; } s = SUM8(a); POST; } } while (0)
#define PIN1(a) asm volatile("" : "+v"(a))
#define PIN2(a, b) asm volatile("" : "+v"(a), "+v"(b))
#define PIN3(a, b, c) asm volatile("" : "+v"(a), "+v"(b), "+v"(c))
#define LANE_VARS() const int lane = lane_id(); \
    const int kq = lane >> 4, n = lane & 15, q4 = n >> 2, p4 = lane & 3; \
    const int r0s = 8 * kq + q4 + 4 * (kq & 1), r1s = 8 * kq + q4 + 4 * ((kq & 1) ^ 1);     \
    const int r2s = swz(4 * kq + q4);                                                       \
    (void)r0s; (void)r1s; (void)r2s; (void)p4
template <int V> struct IC { static constexpr int value = V; };
struct Frag80 { bf16x8 a0, a1; bf16x4 a2; };

__global__ void __launch_bounds__(512, 2) __attribute__((amdgpu_num_vgpr(32)))
mega_kernel(const u16* __restrict__ x0, const u16* __restrict__ kc, const u16* __restrict__ vc, const u16* __restrict__ wq,
            const u16* __restrict__ wo, const u16* __restrict__ wr, const u16* __restrict__ wg, const u16* __restrict__ wu,
            const u16* __restrict__ wd, u16* __restrict__ out, unsigned char* __restrict__ ws) {
    extern __shared__ __attribute__((aligned(16))) unsigned char smem[];
    const unsigned lb = (unsigned)(size_t)smem;
    const int wave = __builtin_amdgcn_readfirstlane(threadIdx.x >> 6);
    const int g = blockIdx.x, gj = GJ(g), gi = GI(g);
    const f32x4 z4 = {0.f, 0.f, 0.f, 0.f};
    asm volatile("" ::: "v255");
    auto sched = [&](unsigned X, unsigned gap) {
        unsigned v; unsigned long long c;
        asm volatile("s_memrealtime %1\n\tds_read_b32 %0, %2\n\ts_waitcnt lgkmcnt(0)" : "=v"(v), "=&s"(c) : "v"(lb + L_CNT + 56) : "memory");
        const unsigned n = (unsigned)c + gap;
        unsigned t = __builtin_amdgcn_readfirstlane(v) + X;
        if ((int)(n - t) > 0) t = n;
        until(t);
    };
    auto slip = [&](unsigned X) {
        if (X) lds_set(lb + L_CNT + 56, now() - X);
    };
    float xe = 0.f;

    auto code_pf = [&](int slice, int lane) {
        unsigned long long pc0; unsigned len, back, t;
        asm volatile("s_getpc_b64 %0\n.Lmega_pf%=:\n\ts_mov_b32 %1, .Lmega_code_end-.Lmega_code_begin\n\ts_mov_b32 %2, .Lmega_pf%=-.Lmega_code_begin" : "=s"(pc0), "=s"(len), "=s"(back));
        unsigned off = (unsigned)(slice * 4096 + lane * 64); if (off > len) off = len;
        asm volatile("global_load_dword %0, %1, %2" : "=v"(t) : "v"(off & ~3u), "s"(pc0 - back) : "memory");
        return t;
    };
    asm volatile(".Lmega_code_begin:");
    auto fix_k1 = [&](auto IC_, auto NC) {
        constexpr int A = RK + 8 * decltype(IC_)::value, B = A + 4;
        unsigned t0, t1, t2, t3;
        asm volatile("s_waitcnt vmcnt(%12)\n\tv_mov_b32 %0, v%c4\n\tv_mov_b32 %1, v%c5\n\tv_mov_b32 %2, v%c6\n\tv_mov_b32 %3, v%c7\n\t"
                     "v_mov_b32_dpp v%c4, v%c8 row_ror:8 row_mask:0xf bank_mask:0xc\n\tv_mov_b32_dpp v%c5, v%c9 row_ror:8 row_mask:0xf bank_mask:0xc\n\t"
                     "v_mov_b32_dpp v%c6, v%c10 row_ror:8 row_mask:0xf bank_mask:0xc\n\tv_mov_b32_dpp v%c7, v%c11 row_ror:8 row_mask:0xf bank_mask:0xc\n\t"
                     "v_mov_b32_dpp v%c8, %0 row_ror:8 row_mask:0xf bank_mask:0x3\n\tv_mov_b32_dpp v%c9, %1 row_ror:8 row_mask:0xf bank_mask:0x3\n\t"
                     "v_mov_b32_dpp v%c10, %2 row_ror:8 row_mask:0xf bank_mask:0x3\n\tv_mov_b32_dpp v%c11, %3 row_ror:8 row_mask:0xf bank_mask:0x3"
                     : "=&v"(t0), "=&v"(t1), "=&v"(t2), "=&v"(t3)
                     : "i"(A), "i"(A + 1), "i"(A + 2), "i"(A + 3), "i"(B), "i"(B + 1), "i"(B + 2), "i"(B + 3), "i"(decltype(NC)::value) : "memory");
    };
    auto trans_v = [&](auto UC, auto NC) {
        constexpr int R = RV + 4 * decltype(UC)::value, N = decltype(NC)::value;
#define CELL(r, c) R + 16 * (r) + (c), R + 16 * (r) + 8 + (c)
        unsigned a0, a1, b0, b1, c0, c1;
        VMW(N + 6); zip_in<CELL(0, 0)>(); zip_out<CELL(0, 1)>(a0, a1); zip_out<CELL(0, 2)>(b0, b1); zip_out<CELL(0, 3)>(c0, c1);
        VMW(N + 4); zip_x<CELL(1, 0), CELL(0, 1)>(a0, a1); zip_in<CELL(1, 1)>(); zip_out<CELL(1, 2)>(a0, a1);
        VMW(N + 2); zip_x<CELL(2, 0), CELL(0, 2)>(b0, b1); zip_x<CELL(2, 1), CELL(1, 2)>(a0, a1); zip_in<CELL(2, 2)>(); zip_out<CELL(1, 3)>(a0, a1); zip_out<CELL(2, 3)>(b0, b1);
        VMW(N); zip_x<CELL(3, 0), CELL(0, 3)>(c0, c1); zip_x<CELL(3, 1), CELL(1, 3)>(a0, a1); zip_x<CELL(3, 2), CELL(2, 3)>(b0, b1); zip_in<CELL(3, 3)>();
#undef CELL
    };
    auto trans_w = [&](auto UC, auto NC) {
        constexpr int R = RV + 64 + 2 * decltype(UC)::value, N = decltype(NC)::value;
        unsigned a0, a1;
        VMW(N + 2); zip_in<R, R + 8>(); zip_out<R + 1, R + 9>(a0, a1);
        VMW(N); zip_x<R + 16, R + 24, R + 1, R + 9>(a0, a1); zip_in<R + 17, R + 25>();
    };
    auto frag80 = [&](unsigned im, int rs, int r0s, int r1s, int r2s) -> Frag80 {
        return Frag80{cat8(tr64(im + r0s * rs), tr64(im + r1s * rs)), cat8(tr64(im + (r0s + 32) * rs), tr64(im + (r1s + 32) * rs)), tr64(im + (r2s + 64) * rs)};
    };
    auto mm80 = [&](const Frag80& a, const Frag80& b) -> f32x4 {
        return mfma32(a.a1, b.a1, mfma32(a.a0, b.a0, z4)) + mfma16(a.a2, b.a2, z4);
    };
    auto rows80 = [&](unsigned a16, unsigned a8) -> Frag80 { const Frag80 f{lds16(a16), lds16(a16 + 64), lds8(a8)}; __builtin_amdgcn_sched_barrier(0); return f; };
    auto empty35 = [&](int layer, int part, int lane) {
        const unsigned t = WS_H35 + (unsigned)(((gj * 20 + 10 * layer + 8 * part + (lane >> 3)) * 32 + gi) * 128 + (lane & 7) * 16);
        st16m(ws, (part == 0 || lane < 16) ? t : WS_OWN + (unsigned)((gi * 8 + gj) * 2048 + layer * 512 + (lane - 16) * 16), (u32x4){~0u, ~0u, ~0u, ~0u}, part ? 0xffffffffffffull : ~0ull);
    };
    auto empty4 = [&](int layer, int part, int lane) {
        st16m(ws, WS_H4 + (unsigned)((layer * 32 + gi) * 16384) + (unsigned)((part == 0 || lane < 32) ? (part * 2 + (lane >> 5)) * 4096 + gj * 512 + (lane & 31) * 16
                                                                                           : 12288 + ((lane - 32) >> 3) * 1024 + gj * 128 + (lane & 7) * 16), (u32x4){~0u, ~0u, ~0u, ~0u},
              layer && part ? 0xff000000ffffffffull : ~0ull);
    };
    const unsigned tst = now();
    const int lead = FREEW(wave) ? LEAD * PACE * 4 : 0;
    auto pace = [&](unsigned n, bool wait = true) {
        unsigned u, w, t0;
        asm volatile("s_mov_b64 exec, 1\n\tds_add_rtn_u32 %0, %3, %4\n\tds_max_u32 %3, %5 offset:12\n\tds_add_rtn_u32 %1, %3, %6 offset:12\n\tds_read_b32 %2, %3 offset:4\n\ts_mov_b64 exec, -1\n\ts_waitcnt lgkmcnt(0)"
                     : "=&v"(u), "=&v"(w), "=&v"(t0) : "v"(lb + L_CNT + 60), "v"(n), "v"((now() - tst) << 6), "v"(n * PMIN * 4u) : "memory");
        int tn = (int)(__builtin_amdgcn_readfirstlane(t0) - tst + PINIT) * 64 + __builtin_amdgcn_readfirstlane(u) * (PACE * 4);
        const int tw = __builtin_amdgcn_readfirstlane(w) - PBURST * 64;
        if (tw > tn) tn = tw;
        tn -= lead;
        if (wait) {
#pragma unroll 1
            for (int k = 0; k < 2048 && (int)((now() - tst) << 6) < tn; ++k) {}
        }
    };
    auto slot = [&](unsigned u) {
        const unsigned tn = lds_word(lb + L_CNT + 64) + PINIT + u * PACE / 16;
#pragma unroll 1
        for (int k = 0; k < 8192 && (int)(now() - tn) < 0; ++k) __builtin_amdgcn_s_sleep(1);
    };
    auto load_wo = [&](int k, int lane, bool early) {
        const int c = k * 64 + lane, pp = c / 10, cc = c - 10 * pp;
        const unsigned f = (unsigned)(swz(pp) * 1280 + cc * 16);
        const u16* wb = wo + (size_t)gi * DH * DM + gj * 80;
#pragma unroll
        for (int a2 = 0; a2 < 6; ++a2) { if (a2 % 3 == 0) { if (early) pace(WEC); else slot(NUNIT - WOFF + (2 * k + a2 / 3) * WOC); } dma16(wb + 32 * a2 * DM, f, lb + L_WO + (5 * a2 + k) * 1024); }
    };
    auto load_wd = [&](int lane) {
        const u16* db = wd + gi * 48 * DM + gj * 80;
#pragma unroll 1
        for (int k = 0; k < 5; ++k) {
            const int c = k * 64 + lane, pp = c / 10, cc = c - 10 * pp;
            const unsigned fd = (unsigned)(swz(pp) * 1280 + cc * 16);
            if (!(k & 1)) pace(3);
            dma16(db, fd, lb + L_WD + k * 1024);
            if (k < 3) dma16(db + 32 * DM, (k < 2 || lane < 32) ? fd : 0u, lb + L_WD + (5 + k) * 1024);
        }
    };
    auto load_gu = [&](int m, int lane) {
#pragma unroll
        for (int k = 0; k < 3; ++k) {
            const int c = k * 64 + lane, row = c / 6, cc = c - 6 * row;
            const unsigned o = (unsigned)((swz(row) * FF + cc * 8) * 2), d = lb + L_GU + m * 8192 + k * 1024;
            const u16* wb = (m == 0 ? wg : wu) + gj * 80 * FF + gi * 48;
            pace(3);
            dma16(wb, o, d); dma16(wb + 32 * FF, o, d + 3072);
            if (k < 2) dma16(wb + 64 * FF, (k == 0 || lane < 32) ? o : 0u, d + 6144);
        }
    };
    auto lds_add = [&](int off, unsigned v) { asm volatile("s_mov_b64 exec, 1\n\tds_add_u32 %0, %1\n\ts_mov_b64 exec, -1" :: "v"(lb + L_CNT + off), "v"(v) : "memory"); };
    auto send_flag = [&](int lane) {
        st4_sc1(ws, WS_MB + (unsigned)((gi * 8 + (lane & 7)) * 128 + gj * 4), 1u);
    };
    auto tiles4 = [&](const Frag80 (&f)[4], const Frag80& b, unsigned long long mk) __attribute__((always_inline)) -> u32x4 {
        f32x4 c[4], d[4];
#pragma unroll
        for (int i = 0; i < 4; ++i) c[i] = mfma32(f[i].a0, b.a0, z4);
#pragma unroll
        for (int i = 0; i < 4; ++i) { c[i] = mfma32(f[i].a1, b.a1, c[i]); d[i] = mfma16(f[i].a2, b.a2, z4); }
        __builtin_amdgcn_sched_barrier(0);
        f32x2 lo, hi;
        asm volatile("s_nop 1\n\ts_mov_b64 exec, %18\n\tv_pk_add_f32 %0, %2, %10\n\tv_pk_add_f32 %1, %3, %11\n\t"
                     "s_lshl_b64 exec, exec, 4\n\tv_pk_add_f32 %0, %4, %12\n\tv_pk_add_f32 %1, %5, %13\n\t"
                     "s_lshl_b64 exec, exec, 4\n\tv_pk_add_f32 %0, %6, %14\n\tv_pk_add_f32 %1, %7, %15\n\t"
                     "s_lshl_b64 exec, exec, 4\n\tv_pk_add_f32 %0, %8, %16\n\tv_pk_add_f32 %1, %9, %17\n\ts_mov_b64 exec, -1"
                     : "=&v"(lo), "=&v"(hi) : "v"(LO2(c[0])), "v"(HI2(c[0])), "v"(LO2(c[1])), "v"(HI2(c[1])), "v"(LO2(c[2])), "v"(HI2(c[2])), "v"(LO2(c[3])), "v"(HI2(c[3])),
                       "v"(LO2(d[0])), "v"(HI2(d[0])), "v"(LO2(d[1])), "v"(HI2(d[1])), "v"(LO2(d[2])), "v"(HI2(d[2])), "v"(LO2(d[3])), "v"(HI2(d[3])), "s"(mk) : "scc");
        return asu(__builtin_shufflevector(lo, hi, 0, 1, 2, 3));
    };
    auto empty1 = [&](int half, int lane) {
        const unsigned o = WS_H1 + (unsigned)(((gi * 4 + 2 * half) * 8 + gj) * 896 + (lane < 56 ? lane : 55) * 16);
        st16_sc1(ws, o, (u32x4){~0u, ~0u, ~0u, ~0u}); st16_sc1(ws, o + 7168, (u32x4){~0u, ~0u, ~0u, ~0u});
    };
    auto answer = [&](int lane) {
        st4_sc1(ws, WS_MB + (unsigned)(((lane & 31) * 8 + (lane & 7)) * 128 + 32), (tst + TOFF) | 1u);
    };
    auto adopt = [&](unsigned t) {
        if (ADOPT && t - tst + 256u < 512u) lds_set(lb + L_CNT + 64, t);
        lds_set(lb + L_CNT + 52, 1u);
    };
    struct StatAddr { unsigned a16, b16, a8, b8, dst; };
    auto stats_addr = [&](int lane, unsigned dbase, int dstride) -> StatAddr {
        const int kq = lane >> 4, q4 = (lane >> 2) & 3, p4 = lane & 3;
        const int x1 = LANES(0x4444444444444444ull) ? L_WR + gj * 160 : L_XS + q4 * 160, xa = LANES(0xaaaaaaaaaaaaaaaaull) ? L_XS + 640 : x1;
        StatAddr r{lb + (unsigned)(xa + kq * 16), lb + (unsigned)(L_XS + p4 * 160 + kq * 16), lb + (unsigned)(xa + 128 + kq * 8), lb + (unsigned)(L_XS + p4 * 160 + 128 + kq * 8), dbase + (unsigned)(kq * dstride)};
        PIN3(r.a16, r.b16, r.dst); PIN2(r.a8, r.b8);
        return r;
    };
    auto stats = [&](const StatAddr& r) {
        const bf16x8 a0 = lds16(r.a16), b0 = lds16(r.b16), a1 = lds16(r.a16 + 64), b1 = lds16(r.b16 + 64);
        const bf16x4 a2 = lds8(r.a8), b2 = lds8(r.b8);
        unsigned long long mk = 0x0008000400020001ull; PINS(mk);
        __builtin_amdgcn_sched_barrier(0);
        st16m(ws, r.dst, asu(mfma32(a1, b1, mfma32(a0, b0, z4)) + mfma16(a2, b2, z4)), mk);
    };
    unsigned qa, pw, ma, idx, qm;
    auto qk_addr = [&](bool L1) {
        LANE_VARS();
        qa = lb + L_Q + (unsigned)(kq * 32 + (lane & 2) * 8);
        pw = lb + L_P + (unsigned)((7 + 8 * wave) * 192 + (kq >> 1) * 64 + (n >> 3) * 32 + (lane & 2) * 8 + (q4 & 1) * 8 + (kq & 1) * 4 + (lane & 1) * 2);
        ma = lb + L_M + (unsigned)(wave * 4);
        idx = (lane & 2) ? 0xeeeeu : 0x4444u;
        qm = lb + L_Q;
        PIN3(qa, pw, ma); PIN2(idx, qm);
        if (L1 && wave < 4) __builtin_amdgcn_s_setprio(2);
    };
    auto att_qk0 = [&]() {
        qk_addr(false);
#pragma unroll 1
        for (unsigned spins = 0; spins < (kSpinCap << 4); ++spins) {
            if (lds_word(lb + L_CNT + 4) != 0u) break;
            __builtin_amdgcn_s_sleep(1);
        }
        STAMP(3);
        const u32x4 q0 = *(const LAS u32x4*)(size_t)qa, q1 = *(const LAS u32x4*)(size_t)(qa + 128), q2 = *(const LAS u32x4*)(size_t)(qa + 256);
        f32x4 c0 = z4, c1 = z4, c2 = z4, c3 = z4;
#define SMF(c, q, r) "v_smfmac_f32_16x16x64_bf16 %" #c ", %" #q ", v[" #r "], %7\n\t"
        asm volatile("s_nop 1\n\t" SMF(0, 4, 64:71) SMF(1, 4, 88:95) SMF(2, 4, 112:119) SMF(3, 4, 136:143) SMF(0, 5, 72:79) SMF(1, 5, 96:103) SMF(2, 5, 120:127) SMF(3, 5, 144:151)
                     SMF(0, 6, 80:87) SMF(1, 6, 104:111) SMF(2, 6, 128:135) SMF(3, 6, 152:159) "s_nop 7"
                     : "+v"(c0), "+v"(c1), "+v"(c2), "+v"(c3) : "v"(q0), "v"(q1), "v"(q2), "v"(idx));
#undef SMF
        const float s0 = c0[0] + c0[2], s1 = c1[0] + c1[2], s2 = c2[0] + c2[2], s3 = c3[0] + c3[2];
        float m = fmax2(fmax3(s0, s1, s2), s3);
        m = fmax2(m, dpp<0xB1>(m)); m = fmax2(m, dpp<0x4E>(m)); m = fmax2(m, dpp<0x124>(m)); m = fmax2(m, dpp<0x128>(m));
        const float s01 = LANES(0xffff0000ffff0000ull) ? s1 : s0, s23 = LANES(0xffff0000ffff0000ull) ? s3 : s2;
        const float sv = LANES(0xffffffff00000000ull) ? s23 : s01;
        m = bfr(bfr(m) * SCALE);
        const float p = ex2((bfr(bfr(sv) * SCALE) - m) * LOG2E);
        *(LAS u16*)(size_t)pw = (u16)pk2(p, p);
        *(LAS float*)(size_t)ma = m;
        __builtin_amdgcn_sched_barrier(0);
    };
    unsigned pr, oa, oc, cma, cxa, cra, cso;
    auto att_addr = [&]() {
        LANE_VARS();
        pr = lb + L_P + (unsigned)((7 + 8 * wave - 2 * q4 - (lane & 1)) * 192 + kq * 32 + (lane & 2) * 8);
        oa = lb + L_OP + (unsigned)(wave * 1024 + (lane & 7) * 64 + kq * 16 + (n >> 3) * 8);
        oc = lb + L_OP + (unsigned)(wave * 1024 + 512 + (kq >> 1) * 256 + (lane & 7) * 32 + (kq & 1) * 16 + (n >> 3) * 8);
        PIN3(pr, oa, oc);
    };
    auto comb_out = [&](float u0, float u1, unsigned so, unsigned long long mk) __attribute__((always_inline)) {
        const unsigned w = min(pk2(u0, u1), 0xfffffffeu);
        const u32x4 w4 = {w, dppu<0x55>(w), dppu<0xAA>(w), dppu<0xFF>(w)};
        st16m(ws, so, w4, mk);
    };
    auto max8 = [&](f32x4 m0, f32x4 m1) __attribute__((always_inline)) { return fmax3(fmax3(m0[0], m0[1], m0[2]), fmax3(m0[3], m1[0], m1[1]), fmax2(m1[2], m1[3])) * LOG2E; };
    auto comb_o0 = [&](f32x4 m0, f32x4 m1, f32x4 mm, f32x4 o0, f32x4 o1, f32x4 o2, f32x4 o3, unsigned so, unsigned long long mk) __attribute__((always_inline)) {
        const float M = max8(m0, m1);
        f32x4 f;
#pragma unroll
        for (int k = 0; k < 4; ++k) f[k] = ex2(__builtin_fmaf(mm[k], LOG2E, -M));
        const f32x4 o = f[3] * o3 + (f[2] * o2 + (f[1] * o1 + f[0] * o0));
        comb_out(rs32(o[0], o[2]), rs32(o[1], o[3]), so, mk);
    };
    auto comb_o1 = [&](f32x4 m0, f32x4 m1, f32x2 mm, f32x4 o0, f32x4 o1, unsigned so, unsigned long long mk) __attribute__((always_inline)) {
        const float M = max8(m0, m1);
        const f32x4 o = ex2(__builtin_fmaf(mm[1], LOG2E, -M)) * o1 + ex2(__builtin_fmaf(mm[0], LOG2E, -M)) * o0;
        comb_out(x16_add(rs32(o[0], o[2])), x16_add(rs32(o[1], o[3])), so, mk);
    };
    auto ml_addr = [&](int lane, unsigned& xa, unsigned& ra) {
        xa = lb + L_M + (unsigned)(((lane & 15) >> 1) * 4); ra = lb + L_P + (unsigned)((7 + 8 * ((lane & 15) >> 1)) * 192 + (lane & 1) * 64 + (lane >> 4) * 16);
    };
    auto comb_ml = [&](unsigned ma, unsigned xa, unsigned ra, unsigned so) __attribute__((always_inline)) {
        const u32x4 x4 = *(const LAS u32x4*)(size_t)ra;
        const f32x4 m0 = *(const LAS f32x4*)(size_t)ma, m1 = *(const LAS f32x4*)(size_t)(ma + 16);
        const float mw = *(const LAS float*)(size_t)xa;
        __builtin_amdgcn_sched_barrier(0);
        const f32x4 sp = mfma32u((u32x4){0x3f803f80u, 0x3f803f80u, 0x3f803f80u, 0x3f803f80u}, x4, z4);
        __builtin_amdgcn_sched_barrier(0);
        const float M = max8(m0, m1);
        const float L = row16_sum(sp[0] * ex2(__builtin_fmaf(mw, LOG2E, -M)));
        st16m(ws, so, asu((f32x4){M, L, M, L}), 1ull);
    };
    auto att_pv0 = [&]() {
        u32x4 bm[8];
        trans_v(IC<0>{}, IC<24>{}); trans_v(IC<1>{}, IC<16>{});
#pragma unroll
        for (int e = 0; e < 8; ++e) bm[e] = *(const LAS u32x4*)(size_t)(pr + e * 192);
        __builtin_amdgcn_sched_barrier(0);
        f32x4 da = z4, dc = z4;
        PIN2(da, dc);
        asm volatile("s_nop 1");
        __builtin_amdgcn_sched_barrier(0);
        f32x2 r;
        smf<RV>(da, bm[0], idx); smf<RV + 8>(da, bm[1], idx); smf<RV + 16>(da, bm[2], idx); smf<RV + 24>(da, bm[3], idx);
        smf<RV + 32>(da, bm[4], idx); smf<RV + 40>(da, bm[5], idx); smf<RV + 48>(da, bm[6], idx); smf<RV + 56>(da, bm[7], idx);
        asm volatile("s_nop 7\n\tv_pk_add_f32 %0, %1, %2\n\tds_write_b64 %3, %0\n\ts_mov_b64 exec, 1\n\tds_add_u32 %4, %5\n\ts_mov_b64 exec, -1"
                     : "=&v"(r) : "v"(LO2(da)), "v"(HI2(da)), "v"(oa), "v"(lb + L_CNT + 24), "v"(1u) : "memory");
        trans_w(IC<0>{}, IC<12>{}); trans_w(IC<1>{}, IC<8>{}); trans_w(IC<2>{}, IC<4>{}); trans_w(IC<3>{}, IC<0>{});
        STAMP(4);
        asm volatile("s_nop 1\n\tv_smfmac_f32_16x16x64_bf16 %0, %1, v[224:231], %5\n\tv_smfmac_f32_16x16x64_bf16 %0, %2, v[232:239], %5\n\t"
                     "v_smfmac_f32_16x16x64_bf16 %0, %3, v[240:247], %5\n\tv_smfmac_f32_16x16x64_bf16 %0, %4, v[248:255], %5\n\ts_nop 6"
                     : "+v"(dc) : "v"(bm[0]), "v"(bm[1]), "v"(bm[2]), "v"(bm[3]), "v"(idx));
        asm volatile("s_nop 0\n\tv_pk_add_f32 %0, %1, %2\n\tds_write_b64 %3, %0" : "=&v"(r) : "v"(LO2(dc)), "v"(HI2(dc)), "v"(oc) : "memory");
        STAMP(20);
    };
    unsigned long long ck1 = 0xffff0000ffff0000ull, ck2 = 0xffffffff00000000ull, csc = 0x3d93cd3a3d93cd3aull;
    auto att1 = [&]() {
        constexpr int layer = 1;
        {
#define SMF(c, q, r) "v_smfmac_f32_16x16x64_bf16 v[" #c "], v[" #q "], v[" #r "], %0\n\t"
#define MAXD(x) "v_max_f32_dpp v41, v41, v41 " x " row_mask:0xf bank_mask:0xf\n\t"
            asm volatile("s_waitcnt lgkmcnt(0)\n\ts_barrier\n\t"
                         "ds_read_b128 v[0:3], %1\n\tds_read2_b32 v[12:13], %2 offset0:96 offset1:100\n\tds_read_b128 v[4:7], %1 offset:128\n\tds_read_b128 v[8:11], %1 offset:256\n\t"
                         "v_mov_b64 v[16:17], 0\n\tv_mov_b64 v[18:19], 0\n\tv_mov_b64 v[20:21], 0\n\tv_mov_b64 v[22:23], 0\n\tv_mov_b64 v[24:25], 0\n\tv_mov_b64 v[26:27], 0\n\t"
                         "v_mov_b64 v[28:29], 0\n\tv_mov_b64 v[30:31], 0\n\tv_mov_b64 v[32:33], 0\n\tv_mov_b64 v[34:35], 0\n\tv_mov_b64 v[36:37], 0\n\tv_mov_b64 v[38:39], 0\n\t"
                         "s_waitcnt lgkmcnt(3)\n\t" SMF(16:19, 0:3, 64:71) SMF(20:23, 0:3, 88:95) "s_waitcnt lgkmcnt(1)\n\tv_mul_f32 v13, 0x3acccccd, v13\n\t"
                         SMF(24:27, 0:3, 112:119) "v_mul_f32 v12, 0x3acccccd, v12\n\t" SMF(28:31, 0:3, 136:143) "v_fma_f32 v12, -v13, v13, v12\n\t"
                         SMF(16:19, 4:7, 72:79) "v_maximum3_f32 v12, v12, 0, 0\n\t" SMF(20:23, 4:7, 96:103) "v_cvt_pk_bf16_f32 v12, 0, v12\n\t"
                         SMF(24:27, 4:7, 120:127) "v_add_f32 v12, 0x3727c5ac, v12\n\t" SMF(28:31, 4:7, 144:151) "v_cvt_pk_bf16_f32 v12, 0, v12\n\ts_waitcnt lgkmcnt(0)\n\t"
                         SMF(16:19, 8:11, 80:87) "v_sqrt_f32 v12, v12\n\t" SMF(20:23, 8:11, 104:111) "v_cvt_pk_bf16_f32 v12, 0, v12\n\t"
                         SMF(24:27, 8:11, 128:135) "v_rcp_f32 v12, v12\n\t" SMF(28:31, 8:11, 152:159)
                         "s_nop 2\n\tv_add_f32 v42, v16, v18\n\ts_nop 0\n\tv_add_f32 v43, v20, v22\n\tv_max_f32 v41, v42, v43\n\tv_add_f32 v44, v24, v26\n\tv_add_f32 v45, v28, v30\n\t"
                         "v_max3_f32 v41, v41, v44, v45\n\tv_cndmask_b32 v42, v42, v43, %8\n\tv_cndmask_b32 v44, v44, v45, %8\n\t"
                         MAXD("quad_perm:[1,0,3,2]") "v_cndmask_b32 v40, v42, v44, %9\n\ts_nop 0\n\t" MAXD("quad_perm:[2,3,0,1]") "s_nop 1\n\t" MAXD("row_ror:4") "s_nop 1\n\t" MAXD("row_ror:8")
                         "v_pk_mul_f32 v[40:41], v[12:13], v[40:41] op_sel_hi:[0,1]\n\tv_cvt_pk_bf16_f32 v40, 0, v40\n\tv_cvt_pk_bf16_f32 v41, 0, v41\n\t"
                         "v_pk_mul_f32 v[40:41], %10, v[40:41]\n\tv_cvt_pk_bf16_f32 v40, 0, v40\n\tv_cvt_pk_bf16_f32 v41, 0, v41\n\t"
                         "v_sub_f32 v40, v40, v41\n\tv_mul_f32 v40, 0x3fb8aa3b, v40\n\tv_exp_f32 v40, v40\n\ts_nop 0\n\tv_cvt_pk_bf16_f32 v40, v40, v40\n\tds_write_b16 %3, v40\n\t"
                         "ds_read_b128 v[0:3], %4\n\tds_read_b128 v[4:7], %4 offset:192\n\tds_read_b128 v[8:11], %4 offset:384\n\tds_read_b128 v[12:15], %4 offset:576\n\t"
                         "ds_read_b128 v[16:19], %4 offset:768\n\tds_read_b128 v[20:23], %4 offset:960\n\tds_read_b128 v[24:27], %4 offset:1152\n\tds_read_b128 v[28:31], %4 offset:1344\n\t"
                         "ds_write_b32 %5, v41\n\t"
                         "s_waitcnt lgkmcnt(8)\n\t" SMF(32:35, 0:3, 160:167) SMF(36:39, 0:3, 224:231) "s_waitcnt lgkmcnt(7)\n\t" SMF(32:35, 4:7, 168:175) SMF(36:39, 4:7, 232:239)
                         "s_waitcnt lgkmcnt(6)\n\t" SMF(32:35, 8:11, 176:183) SMF(36:39, 8:11, 240:247) "s_waitcnt lgkmcnt(4)\n\t" SMF(32:35, 12:15, 184:191) SMF(36:39, 12:15, 248:255)
                         SMF(32:35, 16:19, 192:199) "s_waitcnt lgkmcnt(1)\n\t" SMF(32:35, 20:23, 200:207) SMF(32:35, 24:27, 208:215) SMF(32:35, 28:31, 216:223)
                         "s_nop 2\n\tv_pk_add_f32 v[36:37], v[36:37], v[38:39]\n\tds_write_b64 %7, v[36:37]\n\ts_nop 2\n\tv_pk_add_f32 v[32:33], v[32:33], v[34:35]\n\tds_write_b64 %6, v[32:33]"
                         :: "v"(idx), "v"(qa), "v"(qm), "v"(pw), "v"(pr), "v"(ma), "v"(oa), "v"(oc), "s"(ck1), "s"(ck2), "s"(csc)
                         : "memory", "v0", "v1", "v2", "v3", "v4", "v5", "v6", "v7", "v8", "v9", "v10", "v11", "v12", "v13", "v14", "v15", "v16", "v17", "v18", "v19", "v20", "v21", "v22", "v23",
                           "v24", "v25", "v26", "v27", "v28", "v29", "v30", "v31", "v32", "v33", "v34", "v35", "v36", "v37", "v38", "v39", "v40", "v41", "v42", "v43", "v44", "v45");
#undef SMF
#undef MAXD
            STAMP(20 + 32 * layer);
            if (__builtin_expect(wave >= 3, 1)) BAR();
            else {
                __builtin_amdgcn_s_setprio(0);
                if (wave == 0) {
                    unsigned long long mk = 0x1111111111111111ull; PINS(mk);
                    BAR();
                    STAMP(5 + 32 * layer);
                    comb_o0(*(const LAS f32x4*)(size_t)cma, *(const LAS f32x4*)(size_t)(cma + 16), *(const LAS f32x4*)(size_t)cxa, *(const LAS f32x4*)(size_t)cra,
                            *(const LAS f32x4*)(size_t)(cra + 1024), *(const LAS f32x4*)(size_t)(cra + 2048), *(const LAS f32x4*)(size_t)(cra + 3072), cso, mk);
                } else if (wave == 1) {
                    unsigned long long mk = 0x0000111100001111ull; PINS(mk);
                    BAR();
                    comb_o1(*(const LAS f32x4*)(size_t)cma, *(const LAS f32x4*)(size_t)(cma + 16), *(const LAS f32x2*)(size_t)cxa, *(const LAS f32x4*)(size_t)cra, *(const LAS f32x4*)(size_t)(cra + 1024), cso, mk);
                } else {
                    BAR();
                    comb_ml(cma, cxa, cra, cso);
                }
                STAMP(21 + 32 * layer);
            }
        }
    };

    unsigned nj = wave == 3 ? WEARLY : 0u; int ord;
    {
        const int tid = threadIdx.x, lane = tid & 63;
        STAMP(0);
        unsigned pf = 0u, mb = 0u, tfl = 0u;
        const u16* kbase = kc + ((size_t)((gj >> 1) * HQ + gi) * SK + (SK - WIN) + (gj & 1) * 512) * DH;
        const u16* vbase = vc + ((size_t)((gj >> 1) * HQ + gi) * SK + (SK - WIN) + (gj & 1) * 512) * DH;
        if (wave == 7) {
            __builtin_amdgcn_s_setprio(3);
            for (int i = 0; i < 5; ++i) dma16p(x0, (unsigned)(i * 1024 + lane * 16), lb + L_X0 + i * 1024);
            pf = code_pf((g >> 3) % 10, lane);
            if (lane < 20) *reinterpret_cast<unsigned*>(smem + L_CNT + lane * 4) = lane == 16 ? tst : 0u;
            BAR();
            if (lane < 10) *reinterpret_cast<u32x4*>(smem + L_XS + 640 + lane * 16) = (u32x4){0x3f803f80u, 0x3f803f80u, 0x3f803f80u, 0x3f803f80u};
            if (lane < 16) { *reinterpret_cast<u32x4*>(smem + L_CS + 768 + lane * 16) = (u32x4){0u, 0u, 0u, 0u}; }
        } else {
            if (wave >= 4) {
                if (wave < 6) {
                    empty1(wave - 4, lane);
                    empty35(0, wave - 4, lane);
                } else {
                    __builtin_amdgcn_s_setprio(3);
                    if (gi + gj == 0) st4_sc1(ws, WS_R + (lane & 31) * 4, 0u);
                    st4_sc1(ws, WS_MB + (unsigned)((gi * 8 + gj) * 128 + (lane & 31) * 4), 0u);
                    empty4(0, 0, lane); empty4(0, 1, lane);
                }
            }
#pragma unroll 1
            for (int i = wave; i < 35; i += 7) {
                if (i >= 33) dma16p(wr, (unsigned)((i == 33 || lane < 16) ? (i - 33) * 1024 + lane * 16 : 0), lb + L_WR + (i - 33) * 1024);
                else {
                    const int c = i * 64 + lane; int row = c / 26, cc = c - 26 * row; if (cc > 23) cc = 23; if (row > 79) row = 79;
                    dma16(wq, (unsigned)(((gj * 80 + swz(row)) * QD + gi * DH + cc * 8) * 2), lb + L_WQ + i * 1024);
                }
            }
            BAR();
            *reinterpret_cast<u32x4*>(smem + L_P + tid * 16) = (u32x4){0u, 0u, 0u, 0u};
            if (tid < 72 * 12 - 448) *reinterpret_cast<u32x4*>(smem + L_P + 7168 + tid * 16) = (u32x4){0u, 0u, 0u, 0u};
            if (wave >= 4) {
                VMW(5);
                lds_add(32, 1u);
                if (wave == 6) {
#pragma unroll 1
                    for (unsigned spins = 0; spins < (kSpinCap << 4); ++spins) if (lds_word(lb + L_CNT + 32) >= 3u) break;
                    send_flag(lane);
                    tfl = now();
                    STAMP(17);
                    asm volatile("s_waitcnt vmcnt(1) lgkmcnt(0)" ::: "memory");
                } else asm volatile("s_waitcnt vmcnt(0) lgkmcnt(0)" ::: "memory");
                lds_add(0, 1u);
                if (wave == 6) {
                    until(__builtin_amdgcn_readfirstlane(tfl) + DMB);
#pragma unroll 1
                    for (unsigned spins = 0; spins < kSpinCap; ++spins) {
                        unsigned r = ld4_sc1(ws, WS_MB + (unsigned)((gi * 8 + gj) * 128 + (lane & 7) * 4));
                        WAIT1(r, 0);
                        if (none(r ^ 1u)) break;
                        if ((spins & 7u) == 7u) send_flag(lane);
                    }
                    lds_set(lb + L_CNT + 8, 1u);
                    if (gj == (gi & 7)) st4_sc1(ws, WS_R + (unsigned)(gi * 4), 1u);
                    STAMP(31);
                }
            }
        }
        u32x4 a[8];
        unsigned ko = (unsigned)((wave * 64 + (lane & 7)) * 384 + (((lane >> 3) & 1) * 4 + (lane >> 4)) * 16);
        PIN1(ko);
        auto k_group = [&](auto GC_) __attribute__((always_inline)) {
            constexpr int G = decltype(GC_)::value, R = RK + 24 * (G >> 1) + 4 * (G & 1);
            const unsigned o = ko + G * 3072;
            if (PRIO && G == 0) __builtin_amdgcn_s_setprio(3);
            if (PRIO && G == 4 && wave < 6) __builtin_amdgcn_s_setprio(2);
            pace(3);
            ld16_nt<R, 0>(kbase, o); ld16_nt<R + 8, 128>(kbase, o); ld16_nt<R + 16, 256>(kbase, o);
            const int lane = lane_id();
            const unsigned ho = WS_H1 + (unsigned)((gi * 4 + (gj >> 1)) * 7168 + 3584 + (lane < 48 ? lane : 0) * 16);
            if (wave < 7) {
                asm volatile("; COND_BEGIN");
                if (G == GL && wave < 4) {
                    asm volatile("s_waitcnt vmcnt(%0) lgkmcnt(0)" :: "i"(3 * GL + 3) : "memory");
                    lds_add(0, 1u);
                    if (wave < 2 && (EARLY >> wave & 1)) load_gu(wave, lane);
                    if (wave == 2 && (EARLY & 4)) load_wd(lane);
                    if (wave == 3) {
#pragma unroll 1
                        for (int k = 5 - WEARLY; k < 5; ++k) load_wo(k, lane, true);
                    }
                }
                if (wave == 6) {
                    if (gi + gj == 0) {
                        if (G == GE) mb = ld4_sc1(ws, WS_R + (unsigned)((lane & 31) * 4));
                        if (G == GF) {
                            WAIT1(mb, 3 * (GF - GE));
                            if (none(mb ^ 1u)) { answer(lane); lds_set(lb + L_CNT + 36, 1u); }
                        }
                    }
                    if (G == 7) mb = ld4_sc1(ws, WS_MB + (unsigned)((gi * 8 + (gi & 7)) * 128 + 32));
                }
                asm volatile("; COND_END");
            }
            if (wave == 7) {
                asm volatile("; COND_BEGIN");
                if (G == GP) {
                    VMW(3 * (GP + 1) + 1);
                    const unsigned char* p = smem + L_X0 + (lane >> 4) * 1280;
                    const float c0 = lo16(*reinterpret_cast<const u16*>(p));
                    const f32x2 c2 = {c0, c0};
                    f32x2 s2 = {0.f, 0.f}, q2 = s2;
#pragma unroll
                    for (int k = 0; k < 5; ++k) {
                        const u32x4 w = *reinterpret_cast<const u32x4*>(p + k * 256 + (lane & 15) * 16);
#pragma unroll
                        for (int e = 0; e < 4; ++e) { const f32x2 d = unpk(w[e]) - c2; s2 += d; q2 += d * d; }
                    }
                    const float s = row16_sum(s2[0] + s2[1]), qq = row16_sum(q2[0] + q2[1]);
                    const float ms = s * (1.f / DM);
                    const unsigned mv = pk2(c0 + ms, fmax2(qq - s * ms, 0.f) * (1.f / DM));
                    const float mb = lo16(mv), rden = frcp(bfr(__builtin_amdgcn_sqrtf(bfr(hi16(mv) + 1e-5f))));
                    const f32x2 mb2 = {mb, mb}, rd2 = {rden, rden};
                    if ((lane & 15) < 10) {
                        u32x4 w = *reinterpret_cast<const u32x4*>(p + gj * 160 + (lane & 15) * 16);
#pragma unroll
                        for (int e = 0; e < 4; ++e) w[e] = pk2v(unpk(pk2v(unpk(w[e]) - mb2)) * rd2);
                        *reinterpret_cast<u32x4*>(smem + L_XS + (lane >> 4) * 160 + (lane & 15) * 16) = w;
                    }
                    STAMP(30);
#pragma unroll 1
                    for (unsigned spins = 0; spins < (kSpinCap << 4); ++spins) {
                        if (lds_word(lb + L_CNT) >= 7u) break;
                        __builtin_amdgcn_s_sleep(1);
                    }
                    STAMP(26);
                    {
#pragma unroll 1
                        for (int i6 = 0; i6 < 6; ++i6) {
                            LANE_VARS();
                            const int blk = i6 < 3 ? i6 : i6 - 3;
                            const unsigned xb = lb + L_XS + (unsigned)((i6 < 3 ? p4 : 4) * 160);
                            Frag80 f[4];
#pragma unroll
                            for (int i = 0; i < 4; ++i) f[i] = frag80(lb + L_WQ + (unsigned)(128 * blk + 32 * i + 8 * p4), 416, r0s, r1s, r2s);
                            const Frag80 b = rows80(xb + kq * 16, xb + 128 + kq * 8);
                            const unsigned so = (i6 < 3 ? WS_H1 + (unsigned)(((gi * 4 + p4) * 8 + gj) * 896) : WS_OWN + (unsigned)((gi * 8 + gj) * 2048 + 1024)) + (unsigned)(256 * blk + q4 * 64 + kq * 16);
                            st16m(ws, so, tiles4(f, b, 0x000f000f000f000full), i6 < 3 ? ~0ull : 0x1111111111111111ull);
                            if (i6 == 2) { tfl = now(); STAMP(2); }
                        }
                    }
                    f32x2 r2 = {0.f, 0.f};
#pragma unroll
                    for (int k = 0; k < 5; ++k) r2 += unpk(*reinterpret_cast<const unsigned*>(smem + L_WR + k * 256 + lane_id() * 4));
                    const float sw = wave_sum(r2[0] + r2[1]);
                    *reinterpret_cast<float*>(smem + L_CNT + 12) = sw;
                    WAIT1(pf, 3 * (GP + 1) + 6);
                    pace(XCOST, false);
                }
                if (G == GC) {
#pragma unroll 1
                    for (unsigned spins = 0; spins < (kSpinCap << 8); ++spins) { if (lds_word(lb + L_CNT + 8)) break; __builtin_amdgcn_s_sleep(1); }
                    until(__builtin_amdgcn_readfirstlane(tfl) + DQ);
                    GATHER8(a, ho, 896);
                }
                if (G == GD) {
                    WAIT8(a, 3 * (GD - GC));
#pragma unroll 1
                    for (unsigned spins = 0; spins < kSpinCap && any_empty(UM32(a)); ++spins) { slip(0u); AGAIN8(a, ho, 896); WAIT8(a, 0); }
                    const f32x4 sq = SUM8(a);
                    unsigned* qp = reinterpret_cast<unsigned*>(smem + L_Q + (lane >> 2) * 32 + (lane & 3) * 4);
                    qp[0] = pk2(sq[0], sq[1]); qp[4] = pk2(sq[2], sq[3]);
                    asm volatile("s_waitcnt lgkmcnt(0)\n\tds_write_b32 %0, %1" :: "v"(lb + L_CNT + 4), "v"(1u) : "memory");
                    STAMP(27);
                }
                asm volatile("; COND_END");
            } else if (G == GC) { for (int k = 0; k < 8; ++k) asm volatile("" : "=v"(a[k])); }
        };
        k_group(IC<0>{}); k_group(IC<1>{}); k_group(IC<2>{}); k_group(IC<3>{}); k_group(IC<4>{}); k_group(IC<5>{}); k_group(IC<6>{}); k_group(IC<7>{});
        {
            const int lane = lane_id();
            const unsigned v0 = (unsigned)((wave * 64 + 4 * (lane >> 4)) * 384 + (lane & 15) * 16);
            const unsigned v1 = v0 + 256 - (unsigned)((lane & 15) * 8);
            auto v_group = [&](auto HC) __attribute__((always_inline)) {
                constexpr int h = decltype(HC)::value, R = h < 8 ? RV + 16 * (h & 3) + 4 * (h >> 2) : RV + 64 + 2 * (h - 8), NV = h < 8 ? 2 * h + 2 : 4 * h - 12;
                const unsigned o = (h < 8 ? v0 + (h >> 1) * 6144 : v1 + (h - 8) * 6144);
                if (PRIO && h == 0) __builtin_amdgcn_s_setprio(1);
                if (PRIO && h == 6) __builtin_amdgcn_s_setprio(0);
                pace(2);
                if (h < 8) { ld16_nt<R, (h & 1) * 768>(vbase, o); ld16_nt<R + 8, (h & 1) * 768 + 384>(vbase, o); }
                else { ld8_nt<R, 0>(vbase, o); ld8_nt<R + 8, 384>(vbase, o); ld8_nt<R + 16, 768>(vbase, o); ld8_nt<R + 24, 1152>(vbase, o); }
                if (h == 0 && wave == 6) {
                    asm volatile("; COND_BEGIN");
                    WAIT1(mb, 2);
                    const unsigned r = __builtin_amdgcn_readfirstlane(mb);
                    if (r) { adopt(r); STAMP(29); }
                    asm volatile("; COND_END");
                }
                if constexpr (h >= FX && h < FX + 6) {
                    constexpr int i = 2 * (h - FX);
                    fix_k1(IC<i>{}, IC<20 - 6 * (i / 3) - i % 3 + NV>{});
                    fix_k1(IC<i + 1>{}, IC<20 - 6 * ((i + 1) / 3) - (i + 1) % 3 + NV>{});
                }
                if (h == 1 && wave == 6 && gi + gj == 0) { asm volatile("; COND_BEGIN"); mb = ld4_sc1(ws, WS_R + (unsigned)((lane & 31) * 4)); asm volatile("; COND_END"); }
                if (h == 3 && wave == 6 && gi + gj == 0) {
                    asm volatile("; COND_BEGIN");
                    WAIT1(mb, 4);
                    if (none(mb ^ 1u) && !lds_word(lb + L_CNT + 36)) { answer(lane); lds_set(lb + L_CNT + 36, 1u); }
                    asm volatile("; COND_END");
                }
                if (h == FX + 5) {
                    STAMP(1);
                    if (!PRIO) __builtin_amdgcn_s_setprio(0);
                    att_qk0();
                }
            };
            v_group(IC<0>{}); v_group(IC<1>{}); v_group(IC<2>{}); v_group(IC<3>{}); v_group(IC<4>{}); v_group(IC<5>{});
            v_group(IC<6>{}); v_group(IC<7>{}); v_group(IC<8>{}); v_group(IC<9>{}); v_group(IC<10>{}); v_group(IC<11>{});
        }
    }
    {
        lds_add(16, 1u);
        STAMP(28);
        att_addr();
        const int lane = lane_id();
        cma = lb + L_M; cxa = lb + L_M + (unsigned)((lane >> 4) * 8); cra = lb + L_OP + (unsigned)(512 + (lane >> 4) * 2048 + (lane & 15) * 16);
        cso = WS_OWN + (unsigned)((gi * 8 + gj) * 2048 + 256 + (lane >> 5) * 64 + (lane & 15) * 4);
        PIN2(cma, cxa); PIN2(cra, cso);
        att_pv0();
    }
    {
        f32x4 m0, m1, o0, o1; f32x2 mm; unsigned r;
        asm volatile("s_mov_b64 exec, 1\n\tds_add_rtn_u32 %5, %6, %7 offset:%10\n\ts_mov_b64 exec, -1\n\t"
                     "ds_read_b128 %0, %6\n\tds_read_b128 %1, %6 offset:16\n\tds_read_b64 %2, %8\n\tds_read_b128 %3, %9\n\tds_read_b128 %4, %9 offset:1024\n\ts_waitcnt lgkmcnt(0)"
                     : "=&v"(m0), "=&v"(m1), "=&v"(mm), "=&v"(o0), "=&v"(o1), "=&v"(r)
                     : "v"(cma), "v"(1u), "v"(cxa), "v"(cra), "i"(L_CNT + 28 - L_M) : "memory");
        ord = __builtin_amdgcn_readfirstlane(r);
        if (ord == 7) { STAMP(5); comb_o1(m0, m1, mm, o0, o1, cso, 0x0000111100001111ull); lds_set(lb + L_CNT + 72, ~7u); lds_set(lb + L_CNT + 48, now() | 1u); STAMP(21); }
        const int lane = lane_id();
        auto wait_cnt = [&](int off, unsigned v) {
#pragma unroll 1
            for (unsigned spins = 0; spins < (kSpinCap << 4); ++spins) { if (lds_word(lb + L_CNT + off) >= v) break; __builtin_amdgcn_s_sleep(1); }
        };
        if (ord == 1) {
            wait_cnt(16, 8u);
            unsigned xa, ra; ml_addr(lane, xa, ra);
            comb_ml(cma, xa, ra, WS_OWN + (unsigned)((gi * 8 + gj) * 2048 + 384));
            STAMP(24);
        } else if (ord == 0 && !lds_word(lb + L_CNT + 52)) {
            if (gi + gj == 0 && !lds_word(lb + L_CNT + 36)) {
#pragma unroll 1
                for (unsigned spins = 0; spins < kSpinCap; ++spins) {
                    unsigned r = ld4_sc1(ws, WS_R + (unsigned)((lane & 31) * 4));
                    WAIT1(r, 0);
                    if (none(r ^ 1u)) break;
                    if ((spins & 7u) == 7u) send_flag(lane);
                }
                answer(lane);
            }
#pragma unroll 1
            for (unsigned spins = 0; spins < kSpinCap; ++spins) {
                unsigned r = ld4_sc1(ws, WS_MB + (unsigned)((gi * 8 + (gi & 7)) * 128 + 32));
                WAIT1(r, 0);
                r = __builtin_amdgcn_readfirstlane(r);
                if (r) { adopt(r); break; }
                if ((spins & 7u) == 7u) { send_flag(lane); if (gj == (gi & 7)) st4_sc1(ws, WS_R + (unsigned)(gi * 4), 1u); }
                __builtin_amdgcn_s_sleep(16);
            }
            STAMP(29);
        } else if (ord == 2) {
#pragma unroll 1
            for (unsigned spins = 0; spins < (kSpinCap << 6); ++spins) if (lds_word(lb + L_CNT + 24) >= 8u) break;
            const unsigned ra = lb + L_OP + (unsigned)((lane >> 5) * 4096 + (lane & 31) * 16);
            comb_o0(*(const LAS f32x4*)(size_t)cma, *(const LAS f32x4*)(size_t)(cma + 16), *(const LAS f32x4*)(size_t)(cma + (lane >> 5) * 16), *(const LAS f32x4*)(size_t)ra, *(const LAS f32x4*)(size_t)(ra + 1024),
                    *(const LAS f32x4*)(size_t)(ra + 2048), *(const LAS f32x4*)(size_t)(ra + 3072), WS_OWN + (unsigned)((gi * 8 + gj) * 2048 + lane * 4), 0x1111111111111111ull);
            STAMP(22);
        }
#pragma unroll 1
        for (;;) {
            unsigned j;
            asm volatile("s_mov_b64 exec, 1\n\tds_add_rtn_u32 %0, %1, %2\n\ts_mov_b64 exec, -1\n\ts_waitcnt lgkmcnt(0)" : "=&v"(j) : "v"(lb + L_CNT + 40), "v"(1u) : "memory");
            j = __builtin_amdgcn_readfirstlane(j);
            if (j >= 5u - WEARLY) break;
            load_wo((int)j, lane, false); ++nj;
        }
        STAMP(23);
    }

    auto chain = [&](auto LC) {
        constexpr int layer = decltype(LC)::value;
        const unsigned h4 = WS_H4 + (unsigned)((layer * 32 + gi) * 16384);
        if (wave < 3) {
            LANE_VARS();
            if (layer == 0) {
                VMW(0);
                lds_add(44, nj);
                const int c = wave * 64 + lane, row = c / 6, cc = c - 6 * row;
                unsigned o = (unsigned)((swz(row) * FF + cc * 8) * 2), o2 = (wave == 0 || lane < 32) ? o : 0u, d = lb + L_GU + wave * 1024;
                const u16* wb0 = wg + gj * 80 * FF + gi * 48;
                const u16* wb1 = wu + gj * 80 * FF + gi * 48;
                const u16* db = wd + gi * 48 * DM + gj * 80;
                const int pp = c / 10, c2 = c + 192, p2 = c2 / 10;
                unsigned fd = (unsigned)(swz(pp) * 1280 + (c - 10 * pp) * 16), f2 = (unsigned)(swz(p2) * 1280 + (c2 - 10 * p2) * 16), f3 = (wave < 2 || lane < 32) ? fd : 0u;
                PIN2(o, o2); PIN3(fd, f2, f3); PINS(wb0); PINS(wb1); PINS(d); PINS(db);
                auto late = [&](int m) {
                    if (m & 1) { dma16(wb0, o, d); dma16(wb0 + 32 * FF, o, d + 3072); if (wave < 2) dma16(wb0 + 64 * FF, o2, d + 6144); }
                    if (m & 2) { dma16(wb1, o, d + 8192); dma16(wb1 + 32 * FF, o, d + 8192 + 3072); if (wave < 2) dma16(wb1 + 64 * FF, o2, d + 8192 + 6144); }
                    if (m & 4) { dma16(db, fd, d + L_WD - L_GU); dma16(db + 32 * DM, f3, d + L_WD - L_GU + 5120); if (wave < 2) dma16(db, f2, d + L_WD - L_GU + 3072); }
                };
                if (GOPT) {
#pragma unroll 1
                    for (unsigned spins = 0; spins < (kSpinCap << 4); ++spins) if (lds_word(lb + L_CNT + 72) == ~3u) break;
                    late(GOPT);
                    STAMP(25);
                }
                BAR();
                late(GB4);
#pragma unroll 1
                for (unsigned spins = 0; spins < (kSpinCap << 4); ++spins) if (lds_word(lb + L_CNT + 76) >= 5u) break;
                late(GST);
                if (wave < 2) { empty1(wave, lane); empty35(1, wave, lane); VMW(3); } else { empty4(1, 0, lane); empty4(1, 1, lane); VMW(2); }
                if ((EARLY & 3) != 3) {
                    lds_add(20, 1u);
#pragma unroll 1
                    for (unsigned spins = 0; spins < (kSpinCap << 4); ++spins) if (lds_word(lb + L_CNT + 20) >= 3u) break;
                }
                STAMP(19);
            } else BAR();
            {
                const unsigned im = lb + L_GU + (unsigned)(32 * wave + 8 * p4);
                Frag80 fg = frag80(im, 96, r0s, r1s, r2s), fu = frag80(im + 8192, 96, r0s, r1s, r2s);
                const unsigned xb = lb + L_XS + (unsigned)((n < 4 ? n : 4) * 160);
                unsigned b16 = xb + kq * 16, b8 = xb + 128 + kq * 8;
                unsigned sg = h4 + (unsigned)((n < 4 ? wave * 4096 + gj * 512 + n * 64 : 12288 + wave * 1024 + gj * 128) + kq * 16), su = sg + (n < 4 ? 256u : 64u);
                unsigned o = h4 + (unsigned)(wave * 4096 + lane * 16);
                unsigned oc = h4 + (unsigned)(12288 + wave * 1024 + ((lane >> 5) * 4 + q4) * 128 + (kq & 1) * 64 + p4 * 16);
                unsigned os = h4 + (unsigned)(15360 + (p4 + 4 * (kq & 1)) * 128 + q4 * 16);
                unsigned ha = lb + L_H + (unsigned)(q4 * 96 + wave * 32 + p4 * 8 + kq * 2);
                f32x2 cs = {0.f, 0.f};
                if (layer != 0) cs = *reinterpret_cast<const f32x2*>(smem + L_CS2 + wave * 512 + lane * 8);
                PIN3(fg.a0, fg.a1, fg.a2); PIN3(fu.a0, fu.a1, fu.a2); PIN3(b16, b8, sg); PIN3(su, o, os); PIN2(ha, cs);
                unsigned long long mk = layer ? 0x000f000f000f000full : 0x001f001f001f001full;
                unsigned ta = WS_MB + (unsigned)((lane >> 1) * 1024 + 64 + (lane & 1) * 16);
                if (layer == 0) PIN2(oc, ta);
                PINS(mk);
                unsigned t3 = 0u;
                if (layer == 0) {
                    const u16* db = wd + gi * 48 * DM + gj * 80;
                    const int c = wave * 64 + lane, pp = c / 10, c2 = c + 192, p2 = c2 / 10;
                    const unsigned fd = (unsigned)(swz(pp) * 1280 + (c - 10 * pp) * 16), f2 = (unsigned)(swz(p2) * 1280 + (c2 - 10 * p2) * 16);
                    t3 = lds_word(lb + L_CNT + 64) + C3;
                    VMW(0);
                    if (!((EARLY | GOPT | GB4 | GST) & 4)) {
                        until(t3 + DWD);
                        dma16(db, fd, lb + L_WD + wave * 1024);
                        dma16(db + 32 * DM, (wave < 2 || lane < 32) ? fd : 0u, lb + L_WD + (5 + wave) * 1024);
                        if (wave < 2) dma16(db, f2, lb + L_WD + (3 + wave) * 1024);
                    }
                }
                BAR();
                STAMP(10 + 32 * layer);
                u32x4 tl = {0u, 0u, 0u, 0u};
                if (layer == 0) tl = ld16_sc1(ws, ta);
                const Frag80 b = rows80(b16, b8);
                const f32x4 g0 = mfma32(fg.a0, b.a0, z4), u0 = mfma32(fu.a0, b.a0, z4), g1 = mfma32(fg.a1, b.a1, g0), g2 = mfma16(fg.a2, b.a2, z4), u1 = mfma32(fu.a1, b.a1, u0), u2 = mfma16(fu.a2, b.a2, z4);
                __builtin_amdgcn_sched_barrier(0);
                const f32x4 cg = g1 + g2, cu = u1 + u2;
                st16m2<0>(ws, sg, asu(cg), su, asu(cu), mk);
                STAMP(11 + 32 * layer);
                if (layer == 0) {
                    const unsigned tn = now();
                    WAIT1(tl, 2);
                    unsigned d = min(min(min(tn - tl[0], tn - tl[1]), min(tn - tl[2], tn - tl[3])), 800u);
                    d = min(d, dppu<0xB1>(d)); d = min(d, dppu<0x4E>(d)); d = min(d, dppu<0x124>(d)); d = min(d, dppu<0x128>(d));
                    unsigned tr = tn - min(min((unsigned)__builtin_amdgcn_readlane(d, 0), (unsigned)__builtin_amdgcn_readlane(d, 16)), min((unsigned)__builtin_amdgcn_readlane(d, 32), (unsigned)__builtin_amdgcn_readlane(d, 48)));
                    if (!POLL3 && (int)(t3 - tr) >= M3) tr = t3 + D4S - D4A;
                    lds_set(lb + L_CNT + 56, tr | 1u);
                    lds_add(68, 1u);
                    if ((int)(tn + 12 - X4A - tr) > 0) tr = tn + 12 - X4A;
                    until(tr + X4A);
                } else sched(X4, 12);
                STAMP(16 + 32 * layer);
                float hv; f32x2 st;
                u32x4 a0, a1, a2, a3, ac, as;
                auto load = [&]() __attribute__((always_inline)) {
                    as = ld16_sc1(ws, os);
                    if (layer == 0) ac = ld16_sc1(ws, oc);
                    a0 = ld16_sc1<0>(ws, o); a1 = ld16_sc1<1024>(ws, o); a2 = ld16_sc1<2048>(ws, o); a3 = ld16_sc1<3072>(ws, o);
                    if (layer == 0) asm volatile("s_waitcnt vmcnt(0)" : "+v"(a0), "+v"(a1), "+v"(a2), "+v"(a3), "+v"(ac), "+v"(as) :: "memory");
                    else { asm volatile("s_waitcnt vmcnt(0)" : "+v"(a0), "+v"(a1), "+v"(a2), "+v"(a3), "+v"(as) :: "memory"); ac = as; }
                };
                auto compute = [&]() __attribute__((always_inline)) {
                    float ss, sm;
                    asm volatile("v_add_f32_dpp %0, %2, %2 quad_perm:[1,0,3,2] row_mask:0xf bank_mask:0xf\n\tv_add_f32_dpp %1, %3, %3 quad_perm:[1,0,3,2] row_mask:0xf bank_mask:0xf\n\ts_nop 0\n\t"
                                 "v_add_f32_dpp %0, %0, %0 quad_perm:[2,3,0,1] row_mask:0xf bank_mask:0xf\n\tv_add_f32_dpp %1, %1, %1 quad_perm:[2,3,0,1] row_mask:0xf bank_mask:0xf\n\ts_nop 0"
                                 : "=&v"(ss), "=&v"(sm) : "v"(as[0]), "v"(as[1]));
                    ss = x16_add(ss); sm = x16_add(sm);
                    st = ln_stats(ss, sm);
                    const f32x4 t = (asf(a0) + asf(a1)) + (asf(a2) + asf(a3));
                    const f32x2 gu = sw16(rs32(t[0], t[2]), rs32(t[1], t[3]));
                    if (layer == 0) {
                        cs = sw16(rs32(asf(ac)[0], asf(ac)[2]), rs32(asf(ac)[1], asf(ac)[3]));
                        cs[0] += dpp<0x124>(cs[0]); cs[1] += dpp<0x124>(cs[1]); cs[0] += dpp<0x128>(cs[0]); cs[1] += dpp<0x128>(cs[1]);
                    }
                    const unsigned gw = pk2v((gu - st[0] * cs) * st[1]);
                    const float gv = lo16(gw);
                    hv = bfr(gv * frcp(1.f + ex2(gv * -LOG2E))) * hi16(gw);
                    *(LAS u16*)(size_t)ha = (u16)pk2(hv, hv);
                    __builtin_amdgcn_sched_barrier(0);
                };
                RETRY(load(), max(max(max(um4(a0), um4(a1)), max(um4(a2), um4(a3))), max(um4(ac), um4(as))), compute(), any_nan(hv, hv), layer ? X4 : X4A);
                BAR();
                STAMP(12 + 32 * layer);
                if (layer == 0) {
                    *reinterpret_cast<f32x2*>(smem + L_CS2 + wave * 512 + lane * 8) = cs;
                }
            }
            if (layer == 0) {
                if (wave == 1) {
                    const unsigned o = WS_OWN + (unsigned)(gi * 16384 + 1024 + 2048 + (lane < 48 ? lane : 0) * 16);
                    u32x4 a[8];
                    a[0] = ld16_sc1<-2048>(ws, o); a[1] = ld16_sc1<0>(ws, o); a[2] = ld16_sc1<2048>(ws, o);
                    a[3] = ld16_sc1<-2048>(ws, o + 6144); a[4] = ld16_sc1<0>(ws, o + 6144); a[5] = ld16_sc1<2048>(ws, o + 6144);
                    a[6] = ld16_sc1<-2048>(ws, o + 12288); a[7] = ld16_sc1<0>(ws, o + 12288);
                    WAIT8(a, 0);
                    if (lane < 48) *reinterpret_cast<f32x4*>(smem + L_CS + lane * 16) = SUM8(a);
                }
                Frag80 f[4];
#pragma unroll
                for (int i = 0; i < 4; ++i) { f[i] = frag80(lb + L_WQ + (unsigned)(128 * wave + 32 * i + 8 * p4), 416, r0s, r1s, r2s); PIN3(f[i].a0, f[i].a1, f[i].a2); }
                unsigned b16 = lb + L_XS + (unsigned)(p4 * 160 + kq * 16), b8 = lb + L_XS + (unsigned)(p4 * 160 + 128 + kq * 8);
                unsigned so = WS_H1 + (unsigned)(((gi * 4 + p4) * 8 + gj) * 896 + 256 * wave + q4 * 64 + kq * 16);
                unsigned long long mk = 0x000f000f000f000full;
                PIN3(b16, b8, so); PINS(mk);
                BAR();
                STAMP(33);
                const Frag80 b = rows80(b16, b8);
                st16_sc1(ws, so, tiles4(f, b, mk));
                STAMP(34);
                qk_addr(true); att_addr();
                cma = lb + L_M; cxa = lb + L_M + (unsigned)(wave == 0 ? (lane >> 5) * 16 : (lane >> 4) * 8);
                cra = lb + L_OP + (unsigned)(wave == 0 ? (lane >> 5) * 4096 + (lane & 31) * 16 : 512 + (lane >> 4) * 2048 + (lane & 15) * 16);
                if (wave == 2) ml_addr(lane, cxa, cra);
                cso = WS_OWN + (unsigned)((gi * 8 + gj) * 2048 + 512) + (unsigned)(wave == 0 ? lane * 4 : wave == 1 ? 256 + (lane >> 5) * 64 + (lane & 15) * 4 : 384);
                PIN2(cma, cxa); PIN2(cra, cso);
            }
        } else {
            const int t = wave - 3;
            StatAddr sa;
            auto gather32 = [&](unsigned o, int lane, unsigned X, auto mid, auto fin) __attribute__((always_inline)) {
                u32x4 a0 = ld16_sc1<-2048>(ws, o), a1 = ld16_sc1<-1024>(ws, o), a2 = ld16_sc1<0>(ws, o), a3 = ld16_sc1<1024>(ws, o), sel;
                const unsigned one = (lane & 1) ? 0x3f800000u : 0x3f80u;
#pragma unroll
                for (int k = 0; k < 4; ++k) sel[k] = ((lane >> 1) & 3) == k ? one : 0u;
                unsigned long long hi = 0xff00ff00ff00ff00ull;
                PIN1(sel); PINS(hi);
                mid();
                float v;
                auto sum = [&]() __attribute__((always_inline)) {
                    f32x4 d; float w;
                    asm volatile("s_waitcnt vmcnt(3)\n\tv_mfma_f32_16x16x32_bf16 %0, %1, %5, 0\n\ts_waitcnt vmcnt(2)\n\tv_mfma_f32_16x16x32_bf16 %0, %2, %5, %0\n\t"
                                 "s_waitcnt vmcnt(1)\n\tv_mfma_f32_16x16x32_bf16 %0, %3, %5, %0\n\ts_waitcnt vmcnt(0)\n\tv_mfma_f32_16x16x32_bf16 %0, %4, %5, %0"
                                 : "=&v"(d), "+v"(a0), "+v"(a1), "+v"(a2), "+v"(a3) : "v"(sel) : "memory");
                    asm volatile("s_nop 7\n\tv_cndmask_b32 %0, %2, %3, %6\n\tv_cndmask_b32 %1, %4, %5, %6\n\ts_nop 1\n\tv_permlane32_swap_b32 %0, %1\n\tv_add_f32 %0, %0, %1"
                                 : "=&v"(v), "=&v"(w) : "v"(d[0]), "v"(d[1]), "v"(d[2]), "v"(d[3]), "s"(hi));
                    fin(v);
                    __builtin_amdgcn_sched_barrier(0);
                };
                sum();
                if (__builtin_expect(any_nan(v, v) != 0ull, 0)) {
#pragma unroll 1
                    for (unsigned spins = 0; spins < kSpinCap; ++spins) {
                        slip(X);
                        again16<-2048>(a0, ws, o); again16<-1024>(a1, ws, o); again16<0>(a2, ws, o); again16<1024>(a3, ws, o);
                        asm volatile("s_waitcnt vmcnt(0)" : "+v"(a0), "+v"(a1), "+v"(a2), "+v"(a3) :: "memory");
                        if (!any_empty(max(max(um4(a0), um4(a1)), max(um4(a2), um4(a3))))) break;
                    }
                    sum();
                }
            };
            auto pack4 = [&](float c0, float c1, float c2, float c3) __attribute__((always_inline)) { const unsigned w0 = min(pk2(c0, c1), 0xfffffffeu), w1 = min(pk2(c2, c3), 0xfffffffeu); return (u32x2){w0, w1}; };
            unsigned xw;
            __builtin_amdgcn_s_setprio(0);
            {
                LANE_VARS();
                if (layer == 0) xe = lo16(*reinterpret_cast<const u16*>(smem + L_X0 + kq * 1280 + gj * 160 + t * 32 + n * 2));
                STAMP(6 + 32 * layer);
                unsigned lo = WS_OWN + (unsigned)(gi * 16384 + layer * 512 + (lane >> 3) * 2048 + t * 128 + (t == 3 ? 0 : (lane & 7) * 16));
                unsigned lw = lb + L_O2 + (unsigned)((lane >> 3) * 416 + t * 128 + (t == 3 ? 0 : (lane & 7) * 16));
                PIN2(lo, lw);
                u32x4 ov = {~0u, ~0u, ~0u, ~0u};
                if (layer != 0 && t < 4) { sched(X2, 20); STAMP(49); ov = ld16_sc1(ws, lo); }
                const unsigned wa = lb + L_WO + (unsigned)(r0s * 160 + 32 * t + 8 * p4), wa1 = lb + L_WO + (unsigned)(r1s * 160 + 32 * t + 8 * p4);
                bf16x8 af[6];
                if (layer == 0) {
                    if (t < 4) {
                        unsigned tf = 0u;
#pragma unroll 1
                        for (unsigned spins = 0; spins < (kSpinCap << 4); ++spins) { tf = lds_word(lb + L_CNT + 48); if (tf) break; }
                        slot(NUNIT + XG2);
                        until(tf + DG2);
                        ov = ld16_sc1(ws, lo);
                        if (GOPT) lds_add(72, 1u);
                        if (nj) { WAIT1(ov, 1); lds_add(44, nj); }
                    } else { VMW(0); lds_add(44, nj); }
#pragma unroll 1
                    for (unsigned spins = 0; spins < (kSpinCap << 4); ++spins) if (lds_word(lb + L_CNT + 44) >= 5u) break;
                    STAMP(8);
                }
#pragma unroll
                for (int s = 0; s < 6; ++s) af[s] = cat8(tr64(wa + s * 32 * 160), tr64(wa1 + s * 32 * 160));
                const unsigned h3 = WS_H35 + (unsigned)((gj * 20 + 10 * layer + t) * 4096);
                unsigned so = h3 + (unsigned)(gi * 128 + (lane & 2) * 32 + (lane & 4) * 8 + kq * 8), go = h3 + (unsigned)(2048 + lane * 16);
                unsigned xs = lb + L_XS + (unsigned)(kq * 160 + t * 32 + n * 2);
                unsigned t3 = layer ? 0u : lds_word(lb + L_CNT + 64) + C3;
                unsigned br = lb + L_O2 + (unsigned)((n & 7) * 416 + kq * 16), mr = lb + L_O2 + (unsigned)((n & 7) * 416 + 384);
                unsigned long long mk = 0x0055005500550055ull;
                PIN3(af[0], af[1], af[2]); PIN3(af[3], af[4], af[5]); PIN3(br, mr, so); PIN2(go, xs); PINS(mk); PINS(t3);
                if (t < 4) {
                    WAIT1(ov, 0);
                    *(LAS u32x4*)(size_t)lw = ov;
                    if (layer == 0 ? any_empty(um4(ov)) : __builtin_expect(any_empty(um4(ov)), 0)) {
#pragma unroll 1
                        for (unsigned spins = 0; spins < kSpinCap; ++spins) {
                            if (layer != 0) slip(X2);
                            ov = ld16_sc1(ws, lo);
                            WAIT1(ov, 0);
                            *(LAS u32x4*)(size_t)lw = ov;
                            if (!any_empty(um4(ov))) break;
                            if (layer == 0) { if ((spins & 15u) == 15u) send_flag(lane); __builtin_amdgcn_s_sleep(1); }
                        }
                    }
                }
                BAR();
                bf16x8 bf[6];
                const f32x2 ml = *(const LAS f32x2*)(size_t)mr;
#pragma unroll
                for (int s = 0; s < 6; ++s) bf[s] = lds16(br + s * 64);
                __builtin_amdgcn_sched_barrier(0);
                f32x4 ca = z4;
#pragma unroll
                for (int s = 0; s < 6; ++s) ca = mfma32(af[s], bf[s], ca);
                const float e = ex2(ml[0] - fmax2(ml[0], dpp<0xB1>(ml[0]))), el = e * ml[1];
                const float f = e * frcp(el + dpp<0xB1>(el));
                float c0, c1, c2, c3;
                asm volatile("s_nop 7\n\t"
                             "v_mul_f32 %0, %4, %8\n\tv_mul_f32 %1, %5, %8\n\tv_mul_f32 %2, %6, %8\n\tv_mul_f32 %3, %7, %8\n\t"
                             "v_add_f32_dpp %0, %0, %0 quad_perm:[1,0,3,2] row_mask:0xf bank_mask:0xf\n\tv_add_f32_dpp %1, %1, %1 quad_perm:[1,0,3,2] row_mask:0xf bank_mask:0xf\n\t"
                             "v_add_f32_dpp %2, %2, %2 quad_perm:[1,0,3,2] row_mask:0xf bank_mask:0xf\n\tv_add_f32_dpp %3, %3, %3 quad_perm:[1,0,3,2] row_mask:0xf bank_mask:0xf"
                             : "=&v"(c0), "=&v"(c1), "=&v"(c2), "=&v"(c3) : "v"(ca[0]), "v"(ca[1]), "v"(ca[2]), "v"(ca[3]), "v"(f));
                st8m(ws, so, pack4(c0, c1, c2, c3), mk);
                if (layer == 0 && wave == 7) st4_sc1(ws, WS_MB + (unsigned)(gi * 1024 + 64 + gj * 4), now());
                if (layer == 0) lds_add(76, 1u);
                STAMP(7 + 32 * layer);
                if (layer != 0) sched(X3, 12);
                else {
                    if (POLL3) wait_word(ws, h3 + (unsigned)(((gi * 7 + t * 5 + 3) & 31) * 128));
                    else {
                        const unsigned n2 = now() + 20;
                        until((int)(n2 - t3) > 0 ? n2 : t3);
                    }
                }
                STAMP(18 + 32 * layer);
                gather32(go, lane, layer ? X3 : 0u, [&]() { sa = stats_addr(lane, h4 + (unsigned)(15360 + gj * 128), 16); }, [&](float s) { xw = pk2(xe + bfr(s), 0.f); *(LAS u16*)(size_t)xs = (u16)xw; });
                xe = lo16(xw);
                STAMP(9 + 32 * layer);
            }
            BAR();
            if (wave == 3) stats(sa);
            STAMP(11 + 32 * layer);
            {
                LANE_VARS();
                const unsigned im = lb + L_WD + (unsigned)(32 * t + 8 * p4);
                if (layer == 0 && !((EARLY | GOPT | GB4 | GST) & 4)) {
#pragma unroll 1
                    for (unsigned spins = 0; spins < (kSpinCap << 4); ++spins) { if (lds_word(lb + L_CNT + 68) >= 3u) break; __builtin_amdgcn_s_sleep(1); }
                }
                bf16x8 wd0 = cat8(tr64(im + r0s * 160), tr64(im + r1s * 160)); bf16x4 wd1 = tr64(im + (32 + r2s) * 160);
                const unsigned h5 = WS_H35 + (unsigned)((gj * 20 + 10 * layer + 5 + t) * 4096);
                unsigned h16 = lb + L_H + (unsigned)(p4 * 96 + kq * 16), h8 = lb + L_H + (unsigned)(p4 * 96 + 64 + kq * 8);
                unsigned so = h5 + (unsigned)(gi * 128 + (lane & 1) * 64 + (lane & 2) * 16 + kq * 8);
                unsigned long long mk = 0x000f000f000f000full;
                PIN2(wd0, wd1); PIN3(h16, h8, so); PINS(mk);
                if (wave == 4) {
                    const unsigned os = h4 + (unsigned)(15360 + (p4 + 4 * (kq & 1)) * 128 + q4 * 16);
                    if (layer == 0) {
#pragma unroll 1
                        for (unsigned spins = 0; spins < (kSpinCap << 4); ++spins) if (lds_word(lb + L_CNT + 56)) break;
                    }
                    sched(layer ? X4 : X4A, 0);
                    u32x4 as;
#pragma unroll 1
                    for (unsigned spins = 0; spins < kSpinCap; ++spins) { as = ld16_sc1(ws, os); WAIT1(as, 0); if (!any_empty(um4(as))) break; }
                    float ss = __uint_as_float(as[0]), sm = __uint_as_float(as[1]), rd = __uint_as_float(as[2]);
                    ss += dpp<0xB1>(ss); sm += dpp<0xB1>(sm); rd += dpp<0xB1>(rd);
                    ss += dpp<0x4E>(ss); sm += dpp<0x4E>(sm); rd += dpp<0x4E>(rd);
                    ss = x16_add(ss); sm = x16_add(sm); rd = x16_add(rd);
                    if (LANES(0x1111ull)) *reinterpret_cast<f32x4*>(smem + L_ST + q4 * 16) = (f32x4){ss, sm, rd, 0.f};
                }
                BAR();
                STAMP(12 + 32 * layer);
                const bf16x8 hb0 = lds16(h16); const bf16x4 hb1 = lds8(h8);
                __builtin_amdgcn_sched_barrier(0);
                const f32x4 c = mfma32(wd0, hb0, z4) + mfma16(wd1, hb1, z4);
                st8m(ws, so, pack4(c[0], c[1], c[2], c[3]), mk);
                STAMP(13 + 33 * layer);
                if (layer == 0 || gi == 4 * t + (gj & 3)) {
                    float rs, n1;
                    auto prep = [&]() __attribute__((always_inline)) {
                        const f32x4 sr = *reinterpret_cast<const f32x4*>(smem + L_ST + kq * 16);
                        const float sw = *reinterpret_cast<const float*>(smem + L_CNT + 12);
                        const f32x2 st = ln_stats(sr[0], sr[1]);
                        const float r = bfr((sr[2] - st[0] * sw) * st[1]);
                        rs = bfr(frcp(1.f + ex2(r * -LOG2E)));
                        n1 = bfr(bfr(xe - st[0]) * st[1]);
                        PIN2(rs, n1);
                    };
                    if (layer == 0) {
                        unsigned go = h5 + (unsigned)(2048 + lane * 16), xs = lb + L_XS + (unsigned)(kq * 160 + t * 32 + n * 2);
                        PIN2(go, xs);
                        sched(X5A, 8);
                        STAMP(15);
                        gather32(go, lane, X5A, [&]() { prep(); sa = stats_addr(lane, WS_H1 + (unsigned)((gi * 32 + gj) * 896 + 768), 7168); },
                                 [&](float y) { xw = pk2(xe + bfr(n1 + bfr(bfr(y) * rs)), 0.f); *(LAS u16*)(size_t)xs = (u16)xw; });
                        xe = lo16(xw);
                        STAMP(14);
                    } else {
                        unsigned go = h5 + (unsigned)(2048 + lane * 16), oo = (unsigned)(kq * 1280 + gj * 160 + t * 32 + n * 2);
                        PIN2(go, oo);
                        sched(X5B, 8);
                        STAMP(47);
                        gather32(go, lane, X5B, prep, [&](float y) { xw = pk2(xe + bfr(n1 + bfr(bfr(y) * rs)), 0.f); });
                        { const unsigned w = xw | (dppu<0xF5>(xw) << 16);
                          const u32x2 w2 = {w, dppu<0xAA>(w)};
                          asm volatile("s_mov_b64 exec, %3\n\tglobal_store_dwordx2 %0, %1, %2 sc0 sc1\n\ts_mov_b64 exec, -1" :: "v"(oo), "v"(w2), "s"(out), "s"(0x1111111111111111ull) : "memory"); }
                        STAMP(45);
                    }
                }
            }
            if (layer == 0) {
                BAR();
                if (wave == 3) { stats(sa); STAMP(34); }
                asm volatile("" : "=v"(cma), "=v"(cxa), "=v"(cra), "=v"(cso));
                if (wave != 7) { qk_addr(true); att_addr(); }
                else {
                    const int lane = lane_id();
                    unsigned o = WS_H1 + (unsigned)((gi * 4 + (gj >> 1)) * 7168 + 3584 + (lane < 48 ? lane : 48) * 16), qw;
                    f32x4 cs;
                    PIN1(o);
                    sched(X1, 26);
                    STAMP(51);
                    f32x4 s; u32x4 a[8];
                    RETRY8(a, o, 896, s, { qw = lb + L_Q + (unsigned)((lane >> 2) * 32 + (lane & 3) * 4); cs = *reinterpret_cast<const f32x4*>(smem + L_CS + lane * 16); PIN2(qw, cs); qk_addr(true); att_addr(); }, { s -= bfr(__int_as_float(__builtin_amdgcn_readlane(__float_as_int(s[1]), 48)) * (1.f / DM)) * cs;
                                           u32x2 w; w[0] = pk2(s[0], s[1]); w[1] = pk2(s[2], s[3]);
                                           if (LANES(0xffff000000000000ull)) { w[0] = __float_as_uint(s[0]); w[1] = __float_as_uint(s[1]); }
                                           *(LAS unsigned*)(size_t)qw = w[0]; *(LAS unsigned*)(size_t)(qw + 16) = w[1]; __builtin_amdgcn_sched_barrier(0); }, any_nan(s[0] + s[2], s[1] + s[3]), X1);
                }
            }
        }
    };
    PINS(ck1); PINS(ck2); PINS(csc);
    chain(IC<0>{});
    STAMP(35);
    att1();
    chain(IC<1>{});
}

__device__ __attribute__((used, noinline)) void mega_code_end() { asm volatile(".Lmega_code_end:"); }

extern "C" void kernel_launch(void* const* d_in, const int* in_sizes, int n_in, void* d_out, int out_size, void* d_ws,
                              size_t ws_size, hipStream_t stream) {
    const u16* x0 = (const u16*)d_in[0];
    const u16* kc = (const u16*)d_in[1];
    const u16* vc = (const u16*)d_in[2];
    const u16* wq = (const u16*)d_in[3];
    const u16* wo = (const u16*)d_in[4];
    const u16* wr = (const u16*)d_in[6];
    const u16* wg = (const u16*)d_in[7];
    const u16* wu = (const u16*)d_in[8];
    const u16* wd = (const u16*)d_in[9];
    (void)hipFuncSetAttribute((const void*)mega_kernel, hipFuncAttributeMaxDynamicSharedMemorySize, L_END);
    mega_kernel<<<256, 512, L_END, stream>>>(x0, kc, vc, wq, wo, wr, wg, wu, wd, (u16*)d_out, (unsigned char*)d_ws);
}
```

```cpp
#define XCOST 8
#define PACE 61
#include <hip/hip_runtime.h>
#include <hip/hip_bf16.h>
#include <stdint.h>

typedef unsigned short u16;
typedef unsigned u32x4 __attribute__((ext_vector_type(4)));
typedef unsigned u32x2 __attribute__((ext_vector_type(2)));
typedef float f32x4 __attribute__((ext_vector_type(4)));
typedef short s16x4 __attribute__((ext_vector_type(4)));
typedef __attribute__((ext_vector_type(4))) __bf16 bf16x4;
typedef __attribute__((ext_vector_type(8))) __bf16 bf16x8;
#define LAS __attribute__((address_space(3)))

constexpr int DM = 640, HQ = 32, SK = 4096, DH = 192, WIN = 1024, FF = 1536;
constexpr int QD = HQ * DH;
constexpr float SCALE = 0.07216878364870322f, LOG2E = 1.44269504088896341f;
constexpr unsigned kSpinCap = 1u << 16;
#define GJ(g) (((g) >> 3) & 7)
#define GI(g) (((g) & 7) * 4 + ((g) >> 6))

#define D4A 128
#define D5A 94
#define D1 94
#define D2 147
#define D3 74
#define D4 80
#define D5B 84
#define PINIT 100
#define PMIN 50
constexpr unsigned X4A = D4A, X5A = X4A + D5A, X1 = X5A + D1, X2 = X1 + D2, X3 = X2 + D3, X4 = X3 + D4, X5B = X4 + D5B;
#define WOC 2
#define WOFF 6
#define WEARLY 1
#define DG2 15
#define LEAD 12
#define TOFF 20
#define UG 360
#define SLOW 0
#define ADOPT 1
#define ALT 1
#define PROBE 1
constexpr unsigned NUNIT = 384 + XCOST + 27 + 6 * WEARLY, PBURST = 30;
#define GSTEPS GA = 1, GB = 6, GC = 7, HD = 1
#define EMODE 0
constexpr int GP = 0, GSTEPS;

constexpr unsigned WS_H1 = 0;
constexpr unsigned WS_H4 = 2u << 20;
constexpr unsigned H4C = 20480, H4T = H4C + 3072;
constexpr unsigned WS_H35 = 4u << 20;
constexpr unsigned WS_OWN = WS_H35 + 80 * 16384;
constexpr unsigned WS_CS = WS_OWN + 32 * 16384;
constexpr unsigned WS_MB = WS_CS + 256 * 768;
constexpr unsigned WS_R = WS_MB + 256 * 128;
constexpr unsigned WS_END = WS_R + 128;
static_assert(WS_END <= (6u << 20), "ws map");
__device__ __forceinline__ unsigned tile35(int j, int q) { return WS_H35 + (unsigned)((j * 10 + (q >> 1)) * 16384 + (q & 1) * 4096); }
__device__ __forceinline__ unsigned h4s(int s) { return (unsigned)((s >> 1) * 16384 + (s & 1) * 4096); }

constexpr int L_WQ = 0;
constexpr int L_WO = 33792;
constexpr int L_GU = L_WO + 30720;
constexpr int L_WD = L_GU + 16384;
constexpr int L_X0 = L_WD + 8192;
constexpr int L_WR = L_X0 + 5120;
constexpr int L_XS = L_WR + 2048;
constexpr int L_Q = L_XS + 1024;
constexpr int L_OP = L_Q + 512;
constexpr int L_M = L_OP + 8192;
constexpr int L_H = L_M + 64;
constexpr int L_ST = L_H + 384;
constexpr int L_CS = L_ST + 64;
constexpr int L_CS2 = L_CS + 1024;
constexpr int L_CNT = L_CS2 + 1536;
constexpr int L_P = L_CNT + 96;
constexpr int L_O2 = L_P + 72 * 192;
constexpr int L_DBG = L_O2 + 3328;
constexpr int L_END = L_DBG + 2048;
static_assert(L_END <= 163840 && L_END > 81920, "LDS");

typedef __attribute__((ext_vector_type(2))) __bf16 bf16x2;
typedef float f32x2 __attribute__((ext_vector_type(2)));
__device__ __forceinline__ unsigned pk2(float a, float b) { return __builtin_bit_cast(unsigned, __builtin_convertvector((f32x2){a, b}, bf16x2)); }
__device__ __forceinline__ float lo16(unsigned w) { return __uint_as_float(w << 16); }
__device__ __forceinline__ float hi16(unsigned w) { return __uint_as_float(w & 0xffff0000u); }
__device__ __forceinline__ float bfr(float f) { return __uint_as_float(pk2(0.f, f)); }
template <int CTRL> __device__ __forceinline__ float dpp(float x) {
    return __builtin_bit_cast(float, __builtin_amdgcn_mov_dpp(__builtin_bit_cast(int, x), CTRL, 0xf, 0xf, true));
}
template <int CTRL> __device__ __forceinline__ unsigned dppu(unsigned x) { return (unsigned)__builtin_amdgcn_mov_dpp((int)x, CTRL, 0xf, 0xf, true); }
__device__ __forceinline__ float row16_sum(float v) {
    v += dpp<0xB1>(v); v += dpp<0x4E>(v); v += dpp<0x141>(v); v += dpp<0x140>(v); return v;
}
__device__ __forceinline__ float fmax2(float a, float b) { return __builtin_elementwise_maximum(a, b); }
__device__ __forceinline__ float fmax3(float a, float b, float c) { return fmax2(fmax2(a, b), c); }
__device__ __forceinline__ float x16_add(float v) { auto s = __builtin_amdgcn_permlane16_swap(__float_as_uint(v), __float_as_uint(v), false, false); return __uint_as_float(s[0]) + __uint_as_float(s[1]); }
__device__ __forceinline__ float x32_add(float v) { auto s = __builtin_amdgcn_permlane32_swap(__float_as_uint(v), __float_as_uint(v), false, false); return __uint_as_float(s[0]) + __uint_as_float(s[1]); }
__device__ __forceinline__ float wave_sum(float v) { return x32_add(x16_add(row16_sum(v))); }
__device__ __forceinline__ float rs32(float a, float b) { auto s = __builtin_amdgcn_permlane32_swap(__float_as_uint(a), __float_as_uint(b), false, false); return __uint_as_float(s[0]) + __uint_as_float(s[1]); }
__device__ __forceinline__ f32x2 sw16(float a, float b) { auto s = __builtin_amdgcn_permlane16_swap(__float_as_uint(a), __float_as_uint(b), false, false); return (f32x2){__uint_as_float(s[0]), __uint_as_float(s[1])}; }

#define BAR() asm volatile("s_waitcnt lgkmcnt(0)\n\ts_barrier" ::: "memory")
#define VMW(n) asm volatile("s_waitcnt vmcnt(%0)" :: "i"(n) : "memory")

template <int OFF = 0> __device__ __forceinline__ u32x4 ld16_sc1(const void* sbase, unsigned voff) {
    u32x4 r; asm volatile("global_load_dwordx4 %0, %1, %2 offset:%3 sc1" : "=v"(r) : "v"(voff), "s"(sbase), "i"(OFF) : "memory"); return r;
}
__device__ __forceinline__ unsigned ld4_sc1(const void* sbase, unsigned voff) {
    unsigned r; asm volatile("global_load_dword %0, %1, %2 sc1" : "=v"(r) : "v"(voff), "s"(sbase) : "memory"); return r;
}
template <int OFF = 0> __device__ __forceinline__ void st16_sc1(void* sbase, unsigned voff, u32x4 v) {
    asm volatile("global_store_dwordx4 %0, %1, %2 offset:%3 sc1\n\ts_nop 1" :: "v"(voff), "v"(v), "s"(sbase), "i"(OFF) : "memory");
}
__device__ __forceinline__ void st4_sc1(void* sbase, unsigned voff, unsigned v) {
    asm volatile("global_store_dword %0, %1, %2 sc1" :: "v"(voff), "v"(v), "s"(sbase) : "memory");
}
__device__ __forceinline__ void st8(void* sbase, unsigned voff, u32x2 v) {
    asm volatile("global_store_dwordx2 %0, %1, %2" :: "v"(voff), "v"(v), "s"(sbase) : "memory");
}
__device__ __forceinline__ void st2(void* sbase, unsigned voff, unsigned v) {
    asm volatile("global_store_short %0, %1, %2" :: "v"(voff), "v"(v), "s"(sbase) : "memory");
}
constexpr int RK = 64, RV = 160;
template <int R, int IMM> __device__ __forceinline__ void ld16_nt(const void* sbase, unsigned voff) {
    asm volatile("global_load_dwordx4 v[%c3:%c4], %0, %1 offset:%2 nt" :: "v"(voff), "s"(sbase), "i"(IMM), "i"(R), "i"(R + 3) : "memory");
}
template <int R, int IMM> __device__ __forceinline__ void ld8_nt(const void* sbase, unsigned voff) {
    asm volatile("global_load_dwordx2 v[%c3:%c4], %0, %1 offset:%2 nt" :: "v"(voff), "s"(sbase), "i"(IMM), "i"(R), "i"(R + 1) : "memory");
}
template <int R> __device__ __forceinline__ void smf(f32x4& d, u32x4 a, unsigned idx) {
    asm volatile("v_smfmac_f32_16x16x64_bf16 %0, %1, v[%c3:%c4], %2" : "+v"(d) : "v"(a), "v"(idx), "i"(R), "i"(R + 7));
}
template <int A, int B> __device__ __forceinline__ void zip_in() {
    unsigned t;
    asm volatile("v_perm_b32 %0, v%c2, v%c1, %3\n\tv_perm_b32 v%c2, v%c2, v%c1, %4\n\tv_mov_b32 v%c1, %0" : "=&v"(t) : "i"(A), "i"(B), "s"(0x05040100u), "s"(0x07060302u));
}
template <int A, int B> __device__ __forceinline__ void zip_out(unsigned& lo, unsigned& hi) {
    asm volatile("v_perm_b32 %0, v%c3, v%c2, %4\n\tv_perm_b32 %1, v%c3, v%c2, %5" : "=&v"(lo), "=&v"(hi) : "i"(A), "i"(B), "s"(0x05040100u), "s"(0x07060302u));
}
template <int A, int B, int C, int D> __device__ __forceinline__ void zip_x(unsigned lo, unsigned hi) {
    asm volatile("v_perm_b32 v%c4, v%c3, v%c2, %6\n\tv_perm_b32 v%c5, v%c3, v%c2, %7\n\tv_mov_b32 v%c2, %0\n\tv_mov_b32 v%c3, %1"
                 :: "v"(lo), "v"(hi), "i"(A), "i"(B), "i"(C), "i"(D), "s"(0x05040100u), "s"(0x07060302u));
}
__device__ __forceinline__ void dma16(const void* sbase, unsigned voff, unsigned lds_dst) {
    unsigned keep;
    asm volatile("s_mov_b32 %0, m0\n\ts_mov_b32 m0, %3\n\ts_nop 0\n\tglobal_load_lds_dwordx4 %1, %2 nt\n\ts_mov_b32 m0, %0"
                 : "=&s"(keep) : "v"(voff), "s"(sbase), "s"(lds_dst) : "memory");
}
__device__ __forceinline__ void dma16p(const void* sbase, unsigned voff, unsigned lds_dst) {
    unsigned keep;
    asm volatile("s_mov_b32 %0, m0\n\ts_mov_b32 m0, %3\n\ts_nop 0\n\tglobal_load_lds_dwordx4 %1, %2\n\ts_mov_b32 m0, %0"
                 : "=&s"(keep) : "v"(voff), "s"(sbase), "s"(lds_dst) : "memory");
}
__device__ __forceinline__ unsigned um4(u32x4 a) { return max(max(a[0], a[1]), max(a[2], a[3])); }
__device__ __forceinline__ bool all_set(unsigned t) { return __builtin_amdgcn_ballot_w64(t == 0u) == 0ull; }
__device__ __forceinline__ bool any_empty(unsigned mx) { return __builtin_amdgcn_ballot_w64(mx == ~0u) != 0ull; }
__device__ __forceinline__ unsigned long long any_nan(float a, float b) { return __builtin_amdgcn_ballot_w64(__builtin_isunordered(a, b)); }
__device__ __forceinline__ f32x4 asf(u32x4 a) { return __builtin_bit_cast(f32x4, a); }
__device__ __forceinline__ u32x4 asu(f32x4 a) { return __builtin_bit_cast(u32x4, a); }
#define LANES(m) __builtin_amdgcn_inverse_ballot_w64(m)

__device__ __forceinline__ bf16x4 tr64(unsigned lds_addr) { return __builtin_amdgcn_ds_read_tr16_b64_v4bf16((LAS bf16x4*)(size_t)lds_addr); }
__device__ __forceinline__ bf16x8 cat8(bf16x4 a, bf16x4 b) { return __builtin_shufflevector(a, b, 0, 1, 2, 3, 4, 5, 6, 7); }
__device__ __forceinline__ bf16x8 lds16(unsigned a) { return *(const LAS bf16x8*)(size_t)a; }
__device__ __forceinline__ bf16x4 lds8(unsigned a) { return *(const LAS bf16x4*)(size_t)a; }
__device__ __forceinline__ f32x4 mfma32(bf16x8 a, bf16x8 b, f32x4 c) { return __builtin_amdgcn_mfma_f32_16x16x32_bf16(a, b, c, 0, 0, 0); }
__device__ __forceinline__ f32x4 mfma32u(u32x4 a, u32x4 b, f32x4 c) { return mfma32(__builtin_bit_cast(bf16x8, a), __builtin_bit_cast(bf16x8, b), c); }
__device__ __forceinline__ f32x4 mfma16(bf16x4 a, bf16x4 b, f32x4 c) {
    return __builtin_amdgcn_mfma_f32_16x16x16bf16_1k(__builtin_bit_cast(s16x4, a), __builtin_bit_cast(s16x4, b), c, 0, 0, 0);
}

#define STAMP(i) do {} while (0)

__device__ __forceinline__ void wait_word(const void* sbase, unsigned off) {
    unsigned t, cnt = kSpinCap;
    asm volatile("1:\n\tglobal_load_dword %0, %2, %3 sc1\n\ts_sub_u32 %1, %1, 1\n\ts_waitcnt vmcnt(0)\n\tv_cmp_eq_u32 vcc, -1, %0\n\ts_cbranch_vccz 2f\n\ts_cbranch_scc0 1b\n2:"
                 : "=&v"(t), "+s"(cnt) : "v"(off), "s"(sbase) : "memory", "vcc", "scc");
}
template <int OFF = 0> __device__ __forceinline__ void st16m(void* sbase, unsigned voff, u32x4 v, unsigned long long mask) {
    asm volatile("s_mov_b64 exec, %4\n\tglobal_store_dwordx4 %0, %1, %2 offset:%3 sc1\n\ts_mov_b64 exec, -1\n\ts_nop 0" :: "v"(voff), "v"(v), "s"(sbase), "i"(OFF), "s"(mask) : "memory");
}
__device__ __forceinline__ void st8m(void* sbase, unsigned voff, u32x2 v, unsigned long long mask) {
    asm volatile("s_mov_b64 exec, %3\n\tglobal_store_dwordx2 %0, %1, %2 sc1\n\ts_mov_b64 exec, -1" :: "v"(voff), "v"(v), "s"(sbase), "s"(mask) : "memory");
}
template <int OFF> __device__ __forceinline__ void st16m2(void* sbase, unsigned voff, u32x4 v, unsigned voff2, u32x4 v2, unsigned long long mask) {
    asm volatile("s_mov_b64 exec, %6\n\tglobal_store_dwordx4 %0, %1, %2 sc1\n\tglobal_store_dwordx4 %4, %5, %2 offset:%3 sc1\n\ts_mov_b64 exec, -1\n\ts_nop 0"
                 :: "v"(voff), "v"(v), "s"(sbase), "i"(OFF), "v"(voff2), "v"(v2), "s"(mask) : "memory");
}
#define PINS(a) asm volatile("" : "+s"(a))
#define RETRY(LOAD, EMPTY, COMPUTE, NAN_, X) do { LOAD; COMPUTE; if (__builtin_expect((NAN_) != 0ull, 0)) { \
    _Pragma("unroll 1") for (unsigned spins_ = 0; spins_ < kSpinCap; ++spins_) { slip(X); LOAD; if (!any_empty(EMPTY)) break; } COMPUTE; } } while (0)
__device__ __forceinline__ unsigned now() { return (unsigned)__builtin_amdgcn_s_memrealtime(); }
__device__ __forceinline__ void until(unsigned t) {
    asm volatile("1:\n\ts_memrealtime vcc\n\ts_waitcnt lgkmcnt(0)\n\ts_sub_u32 vcc_lo, %0, vcc_lo\n\ts_cmp_lt_u32 vcc_lo, 0x400\n\ts_cbranch_scc1 1b" :: "s"(t - 1u) : "vcc", "scc", "memory");
}
__device__ __forceinline__ unsigned lds_word(unsigned a) {
    unsigned t_; asm volatile("ds_read_b32 %0, %1\n\ts_waitcnt lgkmcnt(0)\n\ts_nop 0" : "=v"(t_) : "v"(a) : "memory");
    return __builtin_amdgcn_readfirstlane(t_);
}
__device__ __forceinline__ void lds_set(unsigned a, unsigned v) { asm volatile("ds_write_b32 %0, %1" :: "v"(a), "v"(v) : "memory"); }
__device__ __forceinline__ int swz(int r) { return r ^ (((r >> 3) & 1) << 2); }
__device__ __forceinline__ f32x2 unpk(unsigned w) { return (f32x2){lo16(w), hi16(w)}; }
__device__ __forceinline__ unsigned pk2v(f32x2 v) { return __builtin_bit_cast(unsigned, __builtin_convertvector(v, bf16x2)); }
__device__ __forceinline__ float frcp(float x) { return __builtin_amdgcn_rcpf(x); }
__device__ __forceinline__ float ex2(float x) { return __builtin_amdgcn_exp2f(x); }
__device__ __forceinline__ int lane_id() { int l; asm volatile("v_mbcnt_lo_u32_b32 %0, -1, 0\n\tv_mbcnt_hi_u32_b32 %0, -1, %0" : "=v"(l)); return l & 63; }
__device__ __forceinline__ f32x2 ln_stats(float ss, float s) {
    const float mean = s * (1.f / DM);
    const float var = fmax2(__builtin_fmaf(-mean, mean, ss * (1.f / DM)), 0.f);
    const unsigned mv = pk2(mean, var);
    return (f32x2){lo16(mv), frcp(bfr(__builtin_amdgcn_sqrtf(bfr(hi16(mv) + 1e-5f))))};
}
#define GATHER8(a, o, S) do { a[0] = ld16_sc1<-4 * (S)>(ws, o); a[1] = ld16_sc1<-3 * (S)>(ws, o); a[2] = ld16_sc1<-2 * (S)>(ws, o); a[3] = ld16_sc1<-(S)>(ws, o); \
    a[4] = ld16_sc1<0>(ws, o); a[5] = ld16_sc1<(S)>(ws, o); a[6] = ld16_sc1<2 * (S)>(ws, o); a[7] = ld16_sc1<3 * (S)>(ws, o); } while (0)
template <int OFF> __device__ __forceinline__ void again16(u32x4& r, const void* sbase, unsigned voff) {
    asm volatile("v_cmp_eq_u32 vcc, -1, %1\n\ts_mov_b64 exec, vcc\n\tglobal_load_dwordx4 %0, %2, %3 offset:%4 sc1\n\ts_mov_b64 exec, -1" : "+v"(r) : "v"(um4(r)), "v"(voff), "s"(sbase), "i"(OFF) : "memory", "vcc");
}
#define AGAIN8(a, o, S) do { again16<-4 * (S)>(a[0], ws, o); again16<-3 * (S)>(a[1], ws, o); again16<-2 * (S)>(a[2], ws, o); again16<-(S)>(a[3], ws, o); \
    again16<0>(a[4], ws, o); again16<(S)>(a[5], ws, o); again16<2 * (S)>(a[6], ws, o); again16<3 * (S)>(a[7], ws, o); } while (0)
#define WAIT8(a, N) asm volatile("s_waitcnt vmcnt(%8)" : "+v"((a)[0]), "+v"((a)[1]), "+v"((a)[2]), "+v"((a)[3]), "+v"((a)[4]), "+v"((a)[5]), "+v"((a)[6]), "+v"((a)[7]) : "i"(N) : "memory")
#define UM32(a) max(max(max(um4(a[0]), um4(a[1])), max(um4(a[2]), um4(a[3]))), max(max(um4(a[4]), um4(a[5])), max(um4(a[6]), um4(a[7]))))
#define SUM8(a) (((((((asf(a[0]) + asf(a[1])) + asf(a[2])) + asf(a[3])) + asf(a[4])) + asf(a[5])) + asf(a[6])) + asf(a[7]))
#define WAIT1(x, N) asm volatile("s_waitcnt vmcnt(%1)" : "+v"(x) : "i"(N) : "memory")
#define LO2(x) __builtin_shufflevector(x, x, 0, 1)
#define HI2(x) __builtin_shufflevector(x, x, 2, 3)
#define ACC8(a, s) do { f32x2 l_, h_; \
    asm volatile("s_waitcnt vmcnt(5)\n\tv_pk_add_f32 %0, %2, %4\n\tv_pk_add_f32 %1, %3, %5\n\tv_pk_add_f32 %0, %0, %6\n\tv_pk_add_f32 %1, %1, %7\n\t" \
                 "s_waitcnt vmcnt(3)\n\tv_pk_add_f32 %0, %0, %8\n\tv_pk_add_f32 %1, %1, %9\n\tv_pk_add_f32 %0, %0, %10\n\tv_pk_add_f32 %1, %1, %11\n\t" \
                 "s_waitcnt vmcnt(1)\n\tv_pk_add_f32 %0, %0, %12\n\tv_pk_add_f32 %1, %1, %13\n\tv_pk_add_f32 %0, %0, %14\n\tv_pk_add_f32 %1, %1, %15\n\t" \
                 "s_waitcnt vmcnt(0)\n\tv_pk_add_f32 %0, %0, %16\n\tv_pk_add_f32 %1, %1, %17\n\ts_nop 0" \
                 : "=&v"(l_), "=&v"(h_) : "v"(LO2(a[0])), "v"(HI2(a[0])), "v"(LO2(a[1])), "v"(HI2(a[1])), "v"(LO2(a[2])), "v"(HI2(a[2])), "v"(LO2(a[3])), "v"(HI2(a[3])), \
                   "v"(LO2(a[4])), "v"(HI2(a[4])), "v"(LO2(a[5])), "v"(HI2(a[5])), "v"(LO2(a[6])), "v"(HI2(a[6])), "v"(LO2(a[7])), "v"(HI2(a[7])) : "memory"); \
    s = __builtin_shufflevector(l_, h_, 0, 1, 2, 3); } while (0)
#define RETRY8(a, o, S, s, MID, POST, NAN_, X) do { GATHER8(a, o, S); MID; ACC8(a, s); POST; if (__builtin_expect((NAN_) != 0ull, 0)) { \
    _Pragma("unroll 1") for (unsigned spins_ = 0; spins_ < kSpinCap; ++spins_) { slip(X); AGAIN8(a, o, S); WAIT8(a, 0); if (!any_empty(UM32(a))) break; } s = SUM8(a); POST; } } while (0)
#define PIN1(a) asm volatile("" : "+v"(a))
#define PIN2(a, b) asm volatile("" : "+v"(a), "+v"(b))
#define PIN3(a, b, c) asm volatile("" : "+v"(a), "+v"(b), "+v"(c))
#define LANE_VARS() const int lane = lane_id(); \
    const int kq = lane >> 4, n = lane & 15, q4 = n >> 2, p4 = lane & 3; \
    const int r0s = 8 * kq + q4 + 4 * (kq & 1), r1s = 8 * kq + q4 + 4 * ((kq & 1) ^ 1);     \
    const int r2s = swz(4 * kq + q4);                                                       \
    (void)r0s; (void)r1s; (void)r2s; (void)p4
template <int V> struct IC { static constexpr int value = V; };
struct Frag80 { bf16x8 a0, a1; bf16x4 a2; };

__global__ void __launch_bounds__(512, 2) __attribute__((amdgpu_num_vgpr(32)))
mega_kernel(const u16* __restrict__ x0, const u16* __restrict__ kc, const u16* __restrict__ vc, const u16* __restrict__ wq,
            const u16* __restrict__ wo, const u16* __restrict__ wr, const u16* __restrict__ wg, const u16* __restrict__ wu,
            const u16* __restrict__ wd, u16* __restrict__ out, unsigned char* __restrict__ ws_in
            ) {
    extern __shared__ __attribute__((aligned(16))) unsigned char smem[];
    const unsigned lb = (unsigned)(size_t)smem;
    unsigned char* const ws = (unsigned char*)(((uintptr_t)ws_in + (32u << 20) - 1) & ~(uintptr_t)((16u << 20) - 1));
    const int wave = __builtin_amdgcn_readfirstlane(threadIdx.x >> 6);
    const int g = blockIdx.x, gj = GJ(g), gi = GI(g);
    const f32x4 z4 = {0.f, 0.f, 0.f, 0.f};
    asm volatile("" ::: "v255");
    auto sched = [&](unsigned X, unsigned gap) {
        unsigned v; unsigned long long c;
        asm volatile("s_memrealtime %1\n\tds_read_b32 %0, %2\n\ts_waitcnt lgkmcnt(0)" : "=v"(v), "=&s"(c) : "v"(lb + L_CNT + 56) : "memory");
        const unsigned n = (unsigned)c + gap;
        unsigned t = __builtin_amdgcn_readfirstlane(v) + X;
        if ((int)(n - t) > 0) t = n;
        until(t);
    };
    auto slip = [&](unsigned X) {
        if (X) lds_set(lb + L_CNT + 56, now() - X);
    };
    float xe = 0.f;

    auto code_pf = [&](int slice, int lane) {
        unsigned long long pc0; unsigned len, back, t;
        asm volatile("s_getpc_b64 %0\n.Lmega_pf%=:\n\ts_mov_b32 %1, .Lmega_code_end-.Lmega_code_begin\n\ts_mov_b32 %2, .Lmega_pf%=-.Lmega_code_begin" : "=s"(pc0), "=s"(len), "=s"(back));
        unsigned off = (unsigned)(slice * 4096 + lane * 64); if (off > len) off = len;
        asm volatile("global_load_dword %0, %1, %2" : "=v"(t) : "v"(off & ~3u), "s"(pc0 - back) : "memory");
        return t;
    };
    asm volatile(".Lmega_code_begin:");
    auto fix_k1 = [&](auto IC_, auto NC) {
        constexpr int A = RK + 8 * decltype(IC_)::value, B = A + 4;
        unsigned t0, t1, t2, t3;
        asm volatile("s_waitcnt vmcnt(%12)\n\tv_mov_b32 %0, v%c4\n\tv_mov_b32 %1, v%c5\n\tv_mov_b32 %2, v%c6\n\tv_mov_b32 %3, v%c7\n\t"
                     "v_mov_b32_dpp v%c4, v%c8 row_ror:8 row_mask:0xf bank_mask:0xc\n\tv_mov_b32_dpp v%c5, v%c9 row_ror:8 row_mask:0xf bank_mask:0xc\n\t"
                     "v_mov_b32_dpp v%c6, v%c10 row_ror:8 row_mask:0xf bank_mask:0xc\n\tv_mov_b32_dpp v%c7, v%c11 row_ror:8 row_mask:0xf bank_mask:0xc\n\t"
                     "v_mov_b32_dpp v%c8, %0 row_ror:8 row_mask:0xf bank_mask:0x3\n\tv_mov_b32_dpp v%c9, %1 row_ror:8 row_mask:0xf bank_mask:0x3\n\t"
                     "v_mov_b32_dpp v%c10, %2 row_ror:8 row_mask:0xf bank_mask:0x3\n\tv_mov_b32_dpp v%c11, %3 row_ror:8 row_mask:0xf bank_mask:0x3"
                     : "=&v"(t0), "=&v"(t1), "=&v"(t2), "=&v"(t3)
                     : "i"(A), "i"(A + 1), "i"(A + 2), "i"(A + 3), "i"(B), "i"(B + 1), "i"(B + 2), "i"(B + 3), "i"(decltype(NC)::value) : "memory");
    };
    auto trans_v = [&](auto UC, auto NC) {
        constexpr int R = RV + 4 * decltype(UC)::value, N = decltype(NC)::value;
#define CELL(r, c) R + 16 * (r) + (c), R + 16 * (r) + 8 + (c)
        unsigned a0, a1, b0, b1, c0, c1;
        VMW(N + 6); zip_in<CELL(0, 0)>(); zip_out<CELL(0, 1)>(a0, a1); zip_out<CELL(0, 2)>(b0, b1); zip_out<CELL(0, 3)>(c0, c1);
        VMW(N + 4); zip_x<CELL(1, 0), CELL(0, 1)>(a0, a1); zip_in<CELL(1, 1)>(); zip_out<CELL(1, 2)>(a0, a1);
        VMW(N + 2); zip_x<CELL(2, 0), CELL(0, 2)>(b0, b1); zip_x<CELL(2, 1), CELL(1, 2)>(a0, a1); zip_in<CELL(2, 2)>(); zip_out<CELL(1, 3)>(a0, a1); zip_out<CELL(2, 3)>(b0, b1);
        VMW(N); zip_x<CELL(3, 0), CELL(0, 3)>(c0, c1); zip_x<CELL(3, 1), CELL(1, 3)>(a0, a1); zip_x<CELL(3, 2), CELL(2, 3)>(b0, b1); zip_in<CELL(3, 3)>();
#undef CELL
    };
    auto trans_w = [&](auto UC, auto NC) {
        constexpr int R = RV + 64 + 2 * decltype(UC)::value, N = decltype(NC)::value;
        unsigned a0, a1;
        VMW(N + 2); zip_in<R, R + 8>(); zip_out<R + 1, R + 9>(a0, a1);
        VMW(N); zip_x<R + 16, R + 24, R + 1, R + 9>(a0, a1); zip_in<R + 17, R + 25>();
    };
    auto frag80 = [&](unsigned im, int rs, int r0s, int r1s, int r2s) -> Frag80 {
        return Frag80{cat8(tr64(im + r0s * rs), tr64(im + r1s * rs)), cat8(tr64(im + (r0s + 32) * rs), tr64(im + (r1s + 32) * rs)), tr64(im + (r2s + 64) * rs)};
    };
    auto mm80 = [&](const Frag80& a, const Frag80& b) -> f32x4 {
        return mfma32(a.a1, b.a1, mfma32(a.a0, b.a0, z4)) + mfma16(a.a2, b.a2, z4);
    };
    auto rows80 = [&](unsigned a16, unsigned a8) -> Frag80 { const Frag80 f{lds16(a16), lds16(a16 + 64), lds8(a8)}; __builtin_amdgcn_sched_barrier(0); return f; };
    auto empty35 = [&](int layer, unsigned S, int lane) {
        st16_sc1(ws, tile35(gj, 10 * layer + (lane >> 3)) + S + (unsigned)(gi * 128 + (lane & 7) * 16), (u32x4){~0u, ~0u, ~0u, ~0u});
        st16m(ws, S + (lane < 16 ? tile35(gj, 10 * layer + 8 + (lane >> 3)) + (unsigned)(gi * 128 + (lane & 7) * 16) : WS_OWN + (unsigned)(gi * 16384 + layer * 4096 + gj * 512 + (lane - 16) * 16)),
              (u32x4){~0u, ~0u, ~0u, ~0u}, 0xffffffffffffull);
    };
    auto empty5 = [&](int q0, unsigned S, int lane) {
        st16m(ws, tile35(gj, q0 + (lane >> 3)) + S + (unsigned)(gi * 128 + (lane & 7) * 16), (u32x4){~0u, ~0u, ~0u, ~0u}, 0xffffffffffull);
    };
    auto empty4 = [&](int layer, unsigned S, int lane) {
        const unsigned b = WS_H4 + (unsigned)((layer * 32 + gi) * 32768) + (S ^ 8192u);
        st16_sc1(ws, b + (unsigned)((lane >> 5) * 4096 + gj * 512 + (lane & 31) * 16), (u32x4){~0u, ~0u, ~0u, ~0u});
        st16m(ws, b + (unsigned)(lane < 32 ? 16384 + gj * 512 + lane * 16 : H4C + ((lane - 32) >> 3) * 1024 + gj * 128 + (lane & 7) * 16), (u32x4){~0u, ~0u, ~0u, ~0u},
              layer ? 0xff000000ffffffffull : ~0ull);
    };
    const unsigned tst = now();
    int lead = wave == 7 ? LEAD * PACE * 4 : 0;
    auto pace = [&](unsigned n, bool wait = true) {
        unsigned u, w, t0;
        asm volatile("s_mov_b64 exec, 1\n\tds_add_rtn_u32 %0, %3, %4\n\tds_max_u32 %3, %5 offset:12\n\tds_add_rtn_u32 %1, %3, %6 offset:12\n\tds_read_b32 %2, %3 offset:4\n\ts_mov_b64 exec, -1\n\ts_waitcnt lgkmcnt(0)"
                     : "=&v"(u), "=&v"(w), "=&v"(t0) : "v"(lb + L_CNT + 60), "v"(n), "v"((now() - tst) << 6), "v"(n * PMIN * 4u) : "memory");
        int tn = (int)(__builtin_amdgcn_readfirstlane(t0) - tst + PINIT) * 64 + __builtin_amdgcn_readfirstlane(u) * (PACE * 4);
        tn += max((int)__builtin_amdgcn_readfirstlane(u) - UG, 0) * (SLOW * 4);
        const int tw = __builtin_amdgcn_readfirstlane(w) - PBURST * 64;
        if (tw > tn) tn = tw;
        tn -= lead;
        if (wait) {
#pragma unroll 1
            for (int k = 0; k < 2048 && (int)((now() - tst) << 6) < tn; ++k) {}
        }
    };
    auto slot = [&](unsigned u) {
        const unsigned tn = lds_word(lb + L_CNT + 64) + PINIT + (u * PACE + (NUNIT - UG) * SLOW) / 16;
#pragma unroll 1
        for (int k = 0; k < 8192 && (int)(now() - tn) < 0; ++k) __builtin_amdgcn_s_sleep(1);
    };
    auto load_wo = [&](int k, int lane, bool early) {
        const int c = k * 64 + lane, pp = c / 10, cc = c - 10 * pp;
        const unsigned f = (unsigned)(swz(pp) * 1280 + cc * 16);
        const u16* wb = wo + (size_t)gi * DH * DM + gj * 80;
#pragma unroll
        for (int a2 = 0; a2 < 6; ++a2) { if (a2 % 3 == 0) { if (early) pace(3); else slot(NUNIT - WOFF + (2 * k + a2 / 3) * WOC); } dma16(wb + 32 * a2 * DM, f, lb + L_WO + (5 * a2 + k) * 1024); }
    };
    auto load_wd = [&](int lane) {
        const u16* db = wd + gi * 48 * DM + gj * 80;
#pragma unroll 1
        for (int k = 0; k < 5; ++k) {
            const int c = k * 64 + lane, pp = c / 10, cc = c - 10 * pp;
            const unsigned fd = (unsigned)(swz(pp) * 1280 + cc * 16);
            if (!(k & 1)) pace(3);
            dma16(db, fd, lb + L_WD + k * 1024);
            if (k < 3) dma16(db + 32 * DM, (k < 2 || lane < 32) ? fd : 0u, lb + L_WD + (5 + k) * 1024);
        }
    };
    auto load_gu = [&](int m, int lane) {
#pragma unroll
        for (int k = 0; k < 3; ++k) {
            const int c = k * 64 + lane, row = c / 6, cc = c - 6 * row;
            const unsigned o = (unsigned)((swz(row) * FF + cc * 8) * 2), d = lb + L_GU + m * 8192 + k * 1024;
            const u16* wb = (m == 0 ? wg : wu) + gj * 80 * FF + gi * 48;
            pace(3);
            dma16(wb, o, d); dma16(wb + 32 * FF, o, d + 3072);
            if (k < 2) dma16(wb + 64 * FF, (k == 0 || lane < 32) ? o : 0u, d + 6144);
        }
    };
    auto lds_add = [&](int off, unsigned v) { asm volatile("s_mov_b64 exec, 1\n\tds_add_u32 %0, %1\n\ts_mov_b64 exec, -1" :: "v"(lb + L_CNT + off), "v"(v) : "memory"); };
    auto send_flag = [&](int lane) {
        st4_sc1(ws, WS_MB + (unsigned)((gi * 8 + (lane & 7)) * 128 + gj * 4), lds_word(lb + L_CNT + 68));
    };
    auto tiles4 = [&](const Frag80 (&f)[4], const Frag80& b, unsigned long long mk) __attribute__((always_inline)) -> u32x4 {
        f32x4 c[4], d[4];
#pragma unroll
        for (int i = 0; i < 4; ++i) c[i] = mfma32(f[i].a0, b.a0, z4);
#pragma unroll
        for (int i = 0; i < 4; ++i) { c[i] = mfma32(f[i].a1, b.a1, c[i]); d[i] = mfma16(f[i].a2, b.a2, z4); }
        __builtin_amdgcn_sched_barrier(0);
        f32x2 lo, hi;
        asm volatile("s_nop 1\n\ts_mov_b64 exec, %18\n\tv_pk_add_f32 %0, %2, %10\n\tv_pk_add_f32 %1, %3, %11\n\t"
                     "s_lshl_b64 exec, exec, 4\n\tv_pk_add_f32 %0, %4, %12\n\tv_pk_add_f32 %1, %5, %13\n\t"
                     "s_lshl_b64 exec, exec, 4\n\tv_pk_add_f32 %0, %6, %14\n\tv_pk_add_f32 %1, %7, %15\n\t"
                     "s_lshl_b64 exec, exec, 4\n\tv_pk_add_f32 %0, %8, %16\n\tv_pk_add_f32 %1, %9, %17\n\ts_mov_b64 exec, -1"
                     : "=&v"(lo), "=&v"(hi) : "v"(LO2(c[0])), "v"(HI2(c[0])), "v"(LO2(c[1])), "v"(HI2(c[1])), "v"(LO2(c[2])), "v"(HI2(c[2])), "v"(LO2(c[3])), "v"(HI2(c[3])),
                       "v"(LO2(d[0])), "v"(HI2(d[0])), "v"(LO2(d[1])), "v"(HI2(d[1])), "v"(LO2(d[2])), "v"(HI2(d[2])), "v"(LO2(d[3])), "v"(HI2(d[3])), "s"(mk) : "scc");
        return asu(__builtin_shufflevector(lo, hi, 0, 1, 2, 3));
    };
    auto empty1 = [&](int half, unsigned S, int lane) {
        const unsigned o = WS_H1 + S + (unsigned)((gi * 4 + 2 * half) * 16384 + gj * 896 + min(lane, 55) * 16);
        st16_sc1(ws, o, (u32x4){~0u, ~0u, ~0u, ~0u}); st16_sc1(ws, o + 16384, (u32x4){~0u, ~0u, ~0u, ~0u});
    };
    auto report = [&]() { if (gj == (gi & 7)) st4_sc1(ws, WS_R + (unsigned)(gi * 4), lds_word(lb + L_CNT + 88)); };
    auto answer = [&](int lane, unsigned r) {
        int d = min(max((int)(r - tst), -400), 400) * 512 + (int)((r >> 1) & 15u);
        d += (int)dppu<0xB1>((unsigned)d); d += (int)dppu<0x4E>((unsigned)d); d += (int)dppu<0x141>((unsigned)d); d += (int)dppu<0x140>((unsigned)d);
        d = __builtin_amdgcn_readlane(d, 0) + __builtin_amdgcn_readlane(d, 16);
        st4_sc1(ws, WS_MB + (unsigned)(((lane & 31) * 8 + (lane & 7)) * 128 + 32), ((tst + (d >> 14) + TOFF) & ~3u) | ((d & 511) > 128 ? 3u : 1u));
        lds_set(lb + L_CNT + 36, 1u);
    };
    auto adopt = [&](unsigned t) {
        if (ADOPT && t - tst + 256u < 512u) lds_set(lb + L_CNT + 64, t);
        lds_set(lb + L_CNT + 80, lds_word(lb + L_CNT + 92) ^ (t & 2u) << 12);
        lds_set(lb + L_CNT + 84, (((t << 1) ^ lds_word(lb + L_CNT + 68)) & 4u) << 11);
        lds_set(lb + L_CNT + 52, 1u);
    };
    struct StatAddr { unsigned a16, b16, a8, b8, dst; };
    auto stats_addr = [&](int lane, unsigned dbase, int dstride) -> StatAddr {
        const int kq = lane >> 4, q4 = (lane >> 2) & 3, p4 = lane & 3;
        const int x1 = LANES(0x4444444444444444ull) ? L_WR + gj * 160 : L_XS + q4 * 160, xa = LANES(0xaaaaaaaaaaaaaaaaull) ? L_XS + 640 : x1;
        StatAddr r{lb + (unsigned)(xa + kq * 16), lb + (unsigned)(L_XS + p4 * 160 + kq * 16), lb + (unsigned)(xa + 128 + kq * 8), lb + (unsigned)(L_XS + p4 * 160 + 128 + kq * 8), dbase + (unsigned)(kq * dstride)};
        PIN3(r.a16, r.b16, r.dst); PIN2(r.a8, r.b8);
        return r;
    };
    auto stats = [&](const StatAddr& r) {
        const bf16x8 a0 = lds16(r.a16), b0 = lds16(r.b16), a1 = lds16(r.a16 + 64), b1 = lds16(r.b16 + 64);
        const bf16x4 a2 = lds8(r.a8), b2 = lds8(r.b8);
        unsigned long long mk = 0x0008000400020001ull; PINS(mk);
        __builtin_amdgcn_sched_barrier(0);
        st16m(ws, r.dst, asu(mfma32(a1, b1, mfma32(a0, b0, z4)) + mfma16(a2, b2, z4)), mk);
    };
    unsigned qa, pw, ma, idx, qm;
    auto qk_addr = [&](bool L1) {
        LANE_VARS();
        qa = lb + L_Q + (unsigned)(kq * 32 + (lane & 2) * 8);
        pw = lb + L_P + (unsigned)((7 + 8 * wave) * 192 + (kq >> 1) * 64 + (n >> 3) * 32 + (lane & 2) * 8 + (q4 & 1) * 8 + (kq & 1) * 4 + (lane & 1) * 2);
        ma = lb + L_M + (unsigned)(wave * 4);
        idx = (lane & 2) ? 0xeeeeu : 0x4444u;
        qm = lb + L_Q;
        PIN3(qa, pw, ma); PIN2(idx, qm);
        if (L1 && wave < 4) __builtin_amdgcn_s_setprio(2);
    };
    auto att_qk0 = [&]() {
        qk_addr(false);
#pragma unroll 1
        for (unsigned spins = 0; spins < (kSpinCap << 4); ++spins) {
            if (lds_word(lb + L_CNT + 4) != 0u) break;
            __builtin_amdgcn_s_sleep(1);
        }
        STAMP(3);
        const u32x4 q0 = *(const LAS u32x4*)(size_t)qa, q1 = *(const LAS u32x4*)(size_t)(qa + 128), q2 = *(const LAS u32x4*)(size_t)(qa + 256);
        f32x4 c0 = z4, c1 = z4, c2 = z4, c3 = z4;
#define SMF(c, q, r) "v_smfmac_f32_16x16x64_bf16 %" #c ", %" #q ", v[" #r "], %7\n\t"
        asm volatile("s_nop 1\n\t" SMF(0, 4, 64:71) SMF(1, 4, 88:95) SMF(2, 4, 112:119) SMF(3, 4, 136:143) SMF(0, 5, 72:79) SMF(1, 5, 96:103) SMF(2, 5, 120:127) SMF(3, 5, 144:151)
                     SMF(0, 6, 80:87) SMF(1, 6, 104:111) SMF(2, 6, 128:135) SMF(3, 6, 152:159) "s_nop 7"
                     : "+v"(c0), "+v"(c1), "+v"(c2), "+v"(c3) : "v"(q0), "v"(q1), "v"(q2), "v"(idx));
#undef SMF
        const float s0 = c0[0] + c0[2], s1 = c1[0] + c1[2], s2 = c2[0] + c2[2], s3 = c3[0] + c3[2];
        float m = fmax2(fmax3(s0, s1, s2), s3);
        m = fmax2(m, dpp<0xB1>(m)); m = fmax2(m, dpp<0x4E>(m)); m = fmax2(m, dpp<0x124>(m)); m = fmax2(m, dpp<0x128>(m));
        const float s01 = LANES(0xffff0000ffff0000ull) ? s1 : s0, s23 = LANES(0xffff0000ffff0000ull) ? s3 : s2;
        const float sv = LANES(0xffffffff00000000ull) ? s23 : s01;
        m = bfr(bfr(m) * SCALE);
        const float p = ex2((bfr(bfr(sv) * SCALE) - m) * LOG2E);
        *(LAS u16*)(size_t)pw = (u16)pk2(p, p);
        *(LAS float*)(size_t)ma = m;
        __builtin_amdgcn_sched_barrier(0);
    };
    unsigned pr, oa, oc, cma, cxa, cra, cso;
    auto att_addr = [&]() {
        LANE_VARS();
        pr = lb + L_P + (unsigned)((7 + 8 * wave - 2 * q4 - (lane & 1)) * 192 + kq * 32 + (lane & 2) * 8);
        oa = lb + L_OP + (unsigned)(wave * 1024 + (lane & 7) * 64 + kq * 16 + (n >> 3) * 8);
        oc = lb + L_OP + (unsigned)(wave * 1024 + 512 + (kq >> 1) * 256 + (lane & 7) * 32 + (kq & 1) * 16 + (n >> 3) * 8);
        PIN3(pr, oa, oc);
    };
    auto comb_out = [&](float u0, float u1, unsigned so, unsigned long long mk, bool both) __attribute__((always_inline)) {
        const unsigned w = min(pk2(u0, u1), 0xfffffffeu);
        const u32x4 w4 = {both ? dppu<0x00>(w) : w, dppu<0x55>(w), dppu<0xAA>(w), dppu<0xFF>(w)};
        st16m(ws, so, w4, mk);
    };
    auto max8 = [&](f32x4 m0, f32x4 m1) __attribute__((always_inline)) { return fmax3(fmax3(m0[0], m0[1], m0[2]), fmax3(m0[3], m1[0], m1[1]), fmax2(m1[2], m1[3])) * LOG2E; };
    auto comb_o0 = [&](f32x4 m0, f32x4 m1, f32x4 mm, f32x4 o0, f32x4 o1, f32x4 o2, f32x4 o3, unsigned so, unsigned long long mk, bool both) __attribute__((always_inline)) {
        const float M = max8(m0, m1);
        f32x4 f;
#pragma unroll
        for (int k = 0; k < 4; ++k) f[k] = ex2(__builtin_fmaf(mm[k], LOG2E, -M));
        const f32x4 o = f[3] * o3 + (f[2] * o2 + (f[1] * o1 + f[0] * o0));
        comb_out(rs32(o[0], o[2]), rs32(o[1], o[3]), so, mk, both);
    };
    auto comb_o1 = [&](f32x4 m0, f32x4 m1, f32x2 mm, f32x4 o0, f32x4 o1, unsigned so, unsigned long long mk, bool both) __attribute__((always_inline)) {
        const float M = max8(m0, m1);
        const f32x4 o = ex2(__builtin_fmaf(mm[1], LOG2E, -M)) * o1 + ex2(__builtin_fmaf(mm[0], LOG2E, -M)) * o0;
        comb_out(x16_add(rs32(o[0], o[2])), x16_add(rs32(o[1], o[3])), so, mk, both);
    };
    auto ml_addr = [&](int lane, unsigned& xa, unsigned& ra) {
        xa = lb + L_M + (unsigned)(((lane & 15) >> 1) * 4); ra = lb + L_P + (unsigned)((7 + 8 * ((lane & 15) >> 1)) * 192 + (lane & 1) * 64 + (lane >> 4) * 16);
    };
    auto comb_ml = [&](unsigned ma, unsigned xa, unsigned ra, unsigned so, bool both) __attribute__((always_inline)) {
        const u32x4 x4 = *(const LAS u32x4*)(size_t)ra;
        const f32x4 m0 = *(const LAS f32x4*)(size_t)ma, m1 = *(const LAS f32x4*)(size_t)(ma + 16);
        const float mw = *(const LAS float*)(size_t)xa;
        __builtin_amdgcn_sched_barrier(0);
        const f32x4 sp = mfma32u((u32x4){0x3f803f80u, 0x3f803f80u, 0x3f803f80u, 0x3f803f80u}, x4, z4);
        __builtin_amdgcn_sched_barrier(0);
        const float M = max8(m0, m1);
        float L = row16_sum(sp[0] * ex2(__builtin_fmaf(mw, LOG2E, -M)));
        if (both) L = dpp<0x00>(L);
        st16m(ws, so, asu((f32x4){M, L, M, L}), both ? 3ull : 1ull);
    };
    auto att_pv0 = [&]() {
        u32x4 bm[8];
        trans_v(IC<0>{}, IC<24>{}); trans_v(IC<1>{}, IC<16>{});
#pragma unroll
        for (int e = 0; e < 8; ++e) bm[e] = *(const LAS u32x4*)(size_t)(pr + e * 192);
        __builtin_amdgcn_sched_barrier(0);
        f32x4 da = z4, dc = z4;
        PIN2(da, dc);
        asm volatile("s_nop 1");
        __builtin_amdgcn_sched_barrier(0);
        f32x2 r;
        smf<RV>(da, bm[0], idx); smf<RV + 8>(da, bm[1], idx); smf<RV + 16>(da, bm[2], idx); smf<RV + 24>(da, bm[3], idx);
        smf<RV + 32>(da, bm[4], idx); smf<RV + 40>(da, bm[5], idx); smf<RV + 48>(da, bm[6], idx); smf<RV + 56>(da, bm[7], idx);
        asm volatile("s_nop 7\n\tv_pk_add_f32 %0, %1, %2\n\tds_write_b64 %3, %0\n\ts_mov_b64 exec, 1\n\tds_add_u32 %4, %5\n\ts_mov_b64 exec, -1"
                     : "=&v"(r) : "v"(LO2(da)), "v"(HI2(da)), "v"(oa), "v"(lb + L_CNT + 24), "v"(1u) : "memory");
        trans_w(IC<0>{}, IC<12>{}); trans_w(IC<1>{}, IC<8>{}); trans_w(IC<2>{}, IC<4>{}); trans_w(IC<3>{}, IC<0>{});
        STAMP(4);
        asm volatile("s_nop 1\n\tv_smfmac_f32_16x16x64_bf16 %0, %1, v[224:231], %5\n\tv_smfmac_f32_16x16x64_bf16 %0, %2, v[232:239], %5\n\t"
                     "v_smfmac_f32_16x16x64_bf16 %0, %3, v[240:247], %5\n\tv_smfmac_f32_16x16x64_bf16 %0, %4, v[248:255], %5\n\ts_nop 6"
                     : "+v"(dc) : "v"(bm[0]), "v"(bm[1]), "v"(bm[2]), "v"(bm[3]), "v"(idx));
        asm volatile("s_nop 0\n\tv_pk_add_f32 %0, %1, %2\n\tds_write_b64 %3, %0" : "=&v"(r) : "v"(LO2(dc)), "v"(HI2(dc)), "v"(oc) : "memory");
        STAMP(20);
    };
    unsigned long long ck1 = 0xffff0000ffff0000ull, ck2 = 0xffffffff00000000ull, csc = 0x3d93cd3a3d93cd3aull;
    auto att1 = [&]() {
        constexpr int layer = 1;
        {
#define SMF(c, q, r) "v_smfmac_f32_16x16x64_bf16 v[" #c "], v[" #q "], v[" #r "], %0\n\t"
#define MAXD(x) "v_max_f32_dpp v41, v41, v41 " x " row_mask:0xf bank_mask:0xf\n\t"
            asm volatile("s_waitcnt lgkmcnt(0)\n\ts_barrier\n\t"
                         "ds_read_b128 v[0:3], %1\n\tds_read2_b32 v[12:13], %2 offset0:96 offset1:100\n\tds_read_b128 v[4:7], %1 offset:128\n\tds_read_b128 v[8:11], %1 offset:256\n\t"
                         "v_mov_b64 v[16:17], 0\n\tv_mov_b64 v[18:19], 0\n\tv_mov_b64 v[20:21], 0\n\tv_mov_b64 v[22:23], 0\n\tv_mov_b64 v[24:25], 0\n\tv_mov_b64 v[26:27], 0\n\t"
                         "v_mov_b64 v[28:29], 0\n\tv_mov_b64 v[30:31], 0\n\tv_mov_b64 v[32:33], 0\n\tv_mov_b64 v[34:35], 0\n\tv_mov_b64 v[36:37], 0\n\tv_mov_b64 v[38:39], 0\n\t"
                         "s_waitcnt lgkmcnt(3)\n\t" SMF(16:19, 0:3, 64:71) SMF(20:23, 0:3, 88:95) "s_waitcnt lgkmcnt(1)\n\tv_mul_f32 v13, 0x3acccccd, v13\n\t"
                         SMF(24:27, 0:3, 112:119) "v_mul_f32 v12, 0x3acccccd, v12\n\t" SMF(28:31, 0:3, 136:143) "v_fma_f32 v12, -v13, v13, v12\n\t"
                         SMF(16:19, 4:7, 72:79) "v_maximum3_f32 v12, v12, 0, 0\n\t" SMF(20:23, 4:7, 96:103) "v_cvt_pk_bf16_f32 v12, 0, v12\n\t"
                         SMF(24:27, 4:7, 120:127) "v_add_f32 v12, 0x3727c5ac, v12\n\t" SMF(28:31, 4:7, 144:151) "v_cvt_pk_bf16_f32 v12, 0, v12\n\ts_waitcnt lgkmcnt(0)\n\t"
                         SMF(16:19, 8:11, 80:87) "v_sqrt_f32 v12, v12\n\t" SMF(20:23, 8:11, 104:111) "v_cvt_pk_bf16_f32 v12, 0, v12\n\t"
                         SMF(24:27, 8:11, 128:135) "v_rcp_f32 v12, v12\n\t" SMF(28:31, 8:11, 152:159)
                         "s_nop 2\n\tv_add_f32 v42, v16, v18\n\ts_nop 0\n\tv_add_f32 v43, v20, v22\n\tv_max_f32 v41, v42, v43\n\tv_add_f32 v44, v24, v26\n\tv_add_f32 v45, v28, v30\n\t"
                         "v_max3_f32 v41, v41, v44, v45\n\tv_cndmask_b32 v42, v42, v43, %8\n\tv_cndmask_b32 v44, v44, v45, %8\n\t"
                         MAXD("quad_perm:[1,0,3,2]") "v_cndmask_b32 v40, v42, v44, %9\n\ts_nop 0\n\t" MAXD("quad_perm:[2,3,0,1]") "s_nop 1\n\t" MAXD("row_ror:4") "s_nop 1\n\t" MAXD("row_ror:8")
                         "v_pk_mul_f32 v[40:41], v[12:13], v[40:41] op_sel_hi:[0,1]\n\tv_cvt_pk_bf16_f32 v40, 0, v40\n\tv_cvt_pk_bf16_f32 v41, 0, v41\n\t"
                         "v_pk_mul_f32 v[40:41], %10, v[40:41]\n\tv_cvt_pk_bf16_f32 v40, 0, v40\n\tv_cvt_pk_bf16_f32 v41, 0, v41\n\t"
                         "v_sub_f32 v40, v40, v41\n\tv_mul_f32 v40, 0x3fb8aa3b, v40\n\tv_exp_f32 v40, v40\n\ts_nop 0\n\tv_cvt_pk_bf16_f32 v40, v40, v40\n\tds_write_b16 %3, v40\n\t"
                         "ds_read_b128 v[0:3], %4\n\tds_read_b128 v[4:7], %4 offset:192\n\tds_read_b128 v[8:11], %4 offset:384\n\tds_read_b128 v[12:15], %4 offset:576\n\t"
                         "ds_read_b128 v[16:19], %4 offset:768\n\tds_read_b128 v[20:23], %4 offset:960\n\tds_read_b128 v[24:27], %4 offset:1152\n\tds_read_b128 v[28:31], %4 offset:1344\n\t"
                         "ds_write_b32 %5, v41\n\t"
                         "s_waitcnt lgkmcnt(8)\n\t" SMF(32:35, 0:3, 160:167) SMF(36:39, 0:3, 224:231) "s_waitcnt lgkmcnt(7)\n\t" SMF(32:35, 4:7, 168:175) SMF(36:39, 4:7, 232:239)
                         "s_waitcnt lgkmcnt(6)\n\t" SMF(32:35, 8:11, 176:183) SMF(36:39, 8:11, 240:247) "s_waitcnt lgkmcnt(4)\n\t" SMF(32:35, 12:15, 184:191) SMF(36:39, 12:15, 248:255)
                         SMF(32:35, 16:19, 192:199) "s_waitcnt lgkmcnt(1)\n\t" SMF(32:35, 20:23, 200:207) SMF(32:35, 24:27, 208:215) SMF(32:35, 28:31, 216:223)
                         "s_nop 2\n\tv_pk_add_f32 v[36:37], v[36:37], v[38:39]\n\tds_write_b64 %7, v[36:37]\n\ts_nop 2\n\tv_pk_add_f32 v[32:33], v[32:33], v[34:35]\n\tds_write_b64 %6, v[32:33]"
                         :: "v"(idx), "v"(qa), "v"(qm), "v"(pw), "v"(pr), "v"(ma), "v"(oa), "v"(oc), "s"(ck1), "s"(ck2), "s"(csc)
                         : "memory", "v0", "v1", "v2", "v3", "v4", "v5", "v6", "v7", "v8", "v9", "v10", "v11", "v12", "v13", "v14", "v15", "v16", "v17", "v18", "v19", "v20", "v21", "v22", "v23",
                           "v24", "v25", "v26", "v27", "v28", "v29", "v30", "v31", "v32", "v33", "v34", "v35", "v36", "v37", "v38", "v39", "v40", "v41", "v42", "v43", "v44", "v45");
#undef SMF
#undef MAXD
            STAMP(20 + 32 * layer);
            if (__builtin_expect(wave >= 3, 1)) BAR();
            else {
                __builtin_amdgcn_s_setprio(0);
                if (wave == 0) {
                    unsigned long long mk = 0x1111111111111111ull; PINS(mk);
                    BAR();
                    STAMP(5 + 32 * layer);
                    comb_o0(*(const LAS f32x4*)(size_t)cma, *(const LAS f32x4*)(size_t)(cma + 16), *(const LAS f32x4*)(size_t)cxa, *(const LAS f32x4*)(size_t)cra,
                            *(const LAS f32x4*)(size_t)(cra + 1024), *(const LAS f32x4*)(size_t)(cra + 2048), *(const LAS f32x4*)(size_t)(cra + 3072), cso, mk, false);
                } else if (wave == 1) {
                    unsigned long long mk = 0x0000111100001111ull; PINS(mk);
                    BAR();
                    comb_o1(*(const LAS f32x4*)(size_t)cma, *(const LAS f32x4*)(size_t)(cma + 16), *(const LAS f32x2*)(size_t)cxa, *(const LAS f32x4*)(size_t)cra, *(const LAS f32x4*)(size_t)(cra + 1024), cso, mk, false);
                } else {
                    BAR();
                    comb_ml(cma, cxa, cra, cso, false);
                }
                STAMP(21 + 32 * layer);
            }
        }
    };

    unsigned nj = wave == 3 ? WEARLY : 0u, rS = 0u, nS = 0u; int ord;
    {
        const int tid = threadIdx.x, lane = tid & 63;
        STAMP(0);
        unsigned pf = 0u, mb = 0u;
        const u16* kbase = kc + ((size_t)((gj >> 1) * HQ + gi) * SK + (SK - WIN) + (gj & 1) * 512) * DH;
        const u16* vbase = vc + ((size_t)((gj >> 1) * HQ + gi) * SK + (SK - WIN) + (gj & 1) * 512) * DH;
        unsigned pt0 = 0u;
        const unsigned S0 = ALT ? (unsigned)(gi & 1) * 8192u : 0u;
        if (wave == 7) {
            __builtin_amdgcn_s_setprio(3);
            for (int i = 0; i < 5; ++i) dma16p(x0, (unsigned)(i * 1024 + lane * 16), lb + L_X0 + i * 1024);
            pf = code_pf((g >> 3) % 11, lane);
            if (lane < 24) *reinterpret_cast<unsigned*>(smem + L_CNT + lane * 4) = lane == 16 ? tst : 0u;
            BAR();
            if (lane < 10) *reinterpret_cast<u32x4*>(smem + L_XS + 640 + lane * 16) = (u32x4){0x3f803f80u, 0x3f803f80u, 0x3f803f80u, 0x3f803f80u};
            if (lane < 16) { *reinterpret_cast<u32x4*>(smem + L_CS + 768 + lane * 16) = (u32x4){0u, 0u, 0u, 0u}; }
        } else {
            if (wave >= 4) {
                if (wave == 4) st16_sc1(ws, WS_OWN + (unsigned)(gi * 16384 + (lane >> 5) * 8192 + gj * 512 + (lane & 31) * 16), (u32x4){~0u, ~0u, ~0u, ~0u});
                else empty5(0, (unsigned)(wave - 5) * 8192u, lane);
                if (wave == 6) {
                    __builtin_amdgcn_s_setprio(3);
                    if (gi + gj == 0) st4_sc1(ws, WS_R + (lane & 31) * 4, 0u);
                    st4_sc1(ws, WS_MB + (unsigned)((gi * 8 + gj) * 128 + (lane & 31) * 4), 0u);
                }
            } else if (PROBE && wave >= 2) {
                unsigned long long c0, c1;
                asm volatile("s_memtime %0\n\ts_waitcnt lgkmcnt(0)\n\ts_load_dword %2, %3, %4\n\ts_waitcnt lgkmcnt(0)\n\ts_memtime %1\n\ts_waitcnt lgkmcnt(0)"
                             : "=&s"(c0), "=&s"(c1), "=&s"(pt0) : "s"(ws), "s"(WS_H4 + (unsigned)((32 + gi) * 32768 + ((wave ^ gj) & 1) * 8192 + gj * 512)) : "memory");
                pt0 = ((wave ^ gj) & 1) ? (unsigned)c1 - (unsigned)c0 : (unsigned)c0 - (unsigned)c1;
            }
            STAMP(32);
#pragma unroll 1
            for (int i = wave < 2 ? wave : wave < 4 ? 35 : wave - 2; i < 35; i += 5) {
                if (i >= 33) dma16p(wr, (unsigned)((i == 33 || lane < 16) ? (i - 33) * 1024 + lane * 16 : 0), lb + L_WR + (i - 33) * 1024);
                else {
                    const int c = i * 64 + lane; int row = c / 26, cc = c - 26 * row; if (cc > 23) cc = 23; if (row > 79) row = 79;
                    dma16(wq, (unsigned)(((gj * 80 + swz(row)) * QD + gi * DH + cc * 8) * 2), lb + L_WQ + i * 1024);
                }
            }
            STAMP(36);
            BAR();
            STAMP(40);
            *reinterpret_cast<u32x4*>(smem + L_P + tid * 16) = (u32x4){0u, 0u, 0u, 0u};
            if (tid < 72 * 12 - 448) *reinterpret_cast<u32x4*>(smem + L_P + 7168 + tid * 16) = (u32x4){0u, 0u, 0u, 0u};
            if (wave >= 4) {
                asm volatile("s_waitcnt vmcnt(0) lgkmcnt(0)" ::: "memory");
                lds_add(0, 1u);
            } else if (wave >= 2) lds_add(20, pt0);
        }
        u32x4 a[8];
        unsigned ko = (unsigned)((wave * 64 + (lane & 7)) * 384 + (((lane >> 3) & 1) * 4 + (lane >> 4)) * 16);
        PIN1(ko);
        auto k_group = [&](auto GC_) __attribute__((always_inline)) {
            constexpr int G = decltype(GC_)::value, R = RK + 24 * (G >> 1) + 4 * (G & 1);
            const unsigned o = ko + G * 3072;
            pace(3);
            ld16_nt<R, 0>(kbase, o); ld16_nt<R + 8, 128>(kbase, o); ld16_nt<R + 16, 256>(kbase, o);
            const int lane = lane_id();
            const unsigned ho = WS_H1 + S0 + (unsigned)((gi * 4 + (gj >> 1)) * 16384 + 3584 + (lane < 48 ? lane : 0) * 16);
            if (wave < 7) {
                asm volatile("; COND_BEGIN");
                if (G == 0 && wave < 4) {
                    asm volatile("s_waitcnt vmcnt(%0) lgkmcnt(0)" :: "i"(3) : "memory");
                    lds_add(0, 1u);
                    if (wave < 2) load_gu(wave, lane);
                    if (wave == 2) load_wd(lane);
                    if (wave == 3) {
#pragma unroll 1
                        for (int k = 5 - WEARLY; k < 5; ++k) load_wo(k, lane, true);
                    }
                }
                asm volatile("; COND_END");
            }
            if (wave == 7) {
                asm volatile("; COND_BEGIN");
                if (G == GP) {
                    VMW(3 * (GP + 1) + 1);
                    const unsigned char* p = smem + L_X0 + (lane >> 4) * 1280;
                    const float c0 = lo16(*reinterpret_cast<const u16*>(p));
                    const f32x2 c2 = {c0, c0};
                    f32x2 s2 = {0.f, 0.f}, q2 = s2;
#pragma unroll
                    for (int k = 0; k < 5; ++k) {
                        const u32x4 w = *reinterpret_cast<const u32x4*>(p + k * 256 + (lane & 15) * 16);
#pragma unroll
                        for (int e = 0; e < 4; ++e) { const f32x2 d = unpk(w[e]) - c2; s2 += d; q2 += d * d; }
                    }
                    const float s = row16_sum(s2[0] + s2[1]), qq = row16_sum(q2[0] + q2[1]);
                    const float ms = s * (1.f / DM);
                    const unsigned mv = pk2(c0 + ms, fmax2(qq - s * ms, 0.f) * (1.f / DM));
                    const float mb = lo16(mv), rden = frcp(bfr(__builtin_amdgcn_sqrtf(bfr(hi16(mv) + 1e-5f))));
                    const f32x2 mb2 = {mb, mb}, rd2 = {rden, rden};
                    if ((lane & 15) < 10) {
                        u32x4 w = *reinterpret_cast<const u32x4*>(p + gj * 160 + (lane & 15) * 16);
#pragma unroll
                        for (int e = 0; e < 4; ++e) w[e] = pk2v(unpk(pk2v(unpk(w[e]) - mb2)) * rd2);
                        *reinterpret_cast<u32x4*>(smem + L_XS + (lane >> 4) * 160 + (lane & 15) * 16) = w;
                    }
                    STAMP(30);
#pragma unroll 1
                    for (unsigned spins = 0; spins < (kSpinCap << 4); ++spins) {
                        if (lds_word(lb + L_CNT) >= 7u) break;
                        __builtin_amdgcn_s_sleep(1);
                    }
                    STAMP(26);
                }
                if (G == GP || G == GA) {
#pragma unroll 1
                    for (int blk = 0; blk < 3; ++blk) {
                        LANE_VARS();
                        const unsigned xb = lb + L_XS + (unsigned)((G == GP ? p4 : 4) * 160);
                        Frag80 f[4];
#pragma unroll
                        for (int i = 0; i < 4; ++i) f[i] = frag80(lb + L_WQ + (unsigned)(128 * blk + 32 * i + 8 * p4), 416, r0s, r1s, r2s);
                        const Frag80 b = rows80(xb + kq * 16, xb + 128 + kq * 8);
                        const u32x4 c = tiles4(f, b, 0x000f000f000f000full);
                        if (G == GP) st16_sc1(ws, WS_H1 + S0 + (unsigned)((gi * 4 + p4) * 16384 + gj * 896 + 256 * blk + q4 * 64 + kq * 16), c);
                        else *reinterpret_cast<u32x4*>(smem + L_CS + 256 * blk + q4 * 64 + kq * 16) = c;
                    }
                    if (G == GP) { STAMP(2); WAIT1(pf, 3 * (GP + 1) + 3); }
                }
                if (G == GA) {
                    const unsigned half = __builtin_amdgcn_s_getreg(2 << 6 | 20);
                    lds_set(lb + L_CNT + 68, 1u | (((int)lds_word(lb + L_CNT + 20) > 0) ^ half) << 1 | half << 2);
                    VMW(3 * (GA - GP));
                    send_flag(lane_id());
                    lds_set(lb + L_CNT + 8, 1u);
                    STAMP(17);
                    f32x2 r2 = {0.f, 0.f};
#pragma unroll
                    for (int k = 0; k < 5; ++k) r2 += unpk(*reinterpret_cast<const unsigned*>(smem + L_WR + k * 256 + lane_id() * 4));
                    *reinterpret_cast<float*>(smem + L_CNT + 12) = wave_sum(r2[0] + r2[1]);
                }
                asm volatile("; COND_END");
            }
            if (wave == 6) {
                asm volatile("; COND_BEGIN");
                if (G == GB) mb = ld4_sc1(ws, WS_MB + (unsigned)((gi * 8 + gj) * 128 + (lane & 7) * 4));
                if (G == GC) {
                    WAIT1(mb, 3 * (GC - GB));
#pragma unroll 1
                    for (unsigned spins = 0; spins < kSpinCap && !all_set(mb); ++spins) {
                        if ((spins & 7u) == 7u && lds_word(lb + L_CNT + 8)) send_flag(lane);
                        mb = ld4_sc1(ws, WS_MB + (unsigned)((gi * 8 + gj) * 128 + (lane & 7) * 4));
                        WAIT1(mb, 0);
                    }
                    lds_set(lb + L_CNT + 92, (__builtin_amdgcn_readlane(mb, gi & 7) & 4u) << 11);
                    lds_set(lb + L_CNT + 88, (tst & ~31u) | __builtin_popcount((unsigned)__builtin_amdgcn_ballot_w64((mb & 2u) != 0u) & 255u) << 1 | 1u);
                    report();
                    GATHER8(a, ho, 896);
                    pace(XCOST, false);
                    STAMP(31);
                }
                asm volatile("; COND_END");
            } else if (G == GC) { for (int k = 0; k < 8; ++k) asm volatile("" : "=v"(a[k])); }
        };
        k_group(IC<0>{}); k_group(IC<1>{}); k_group(IC<2>{}); k_group(IC<3>{}); k_group(IC<4>{}); k_group(IC<5>{}); k_group(IC<6>{}); k_group(IC<7>{});
        {
            const int lane = lane_id();
            const unsigned v0 = (unsigned)((wave * 64 + 4 * (lane >> 4)) * 384 + (lane & 15) * 16);
            const unsigned v1 = v0 + 256 - (unsigned)((lane & 15) * 8);
            auto v_group = [&](auto HC) __attribute__((always_inline)) {
                constexpr int h = decltype(HC)::value, R = h < 8 ? RV + 16 * (h & 3) + 4 * (h >> 2) : RV + 64 + 2 * (h - 8), NV = h < 8 ? 2 * h + 2 : 4 * h - 12;
                const unsigned o = (h < 8 ? v0 + (h >> 1) * 6144 : v1 + (h - 8) * 6144);
                pace(2);
                if (h < 8) { ld16_nt<R, (h & 1) * 768>(vbase, o); ld16_nt<R + 8, (h & 1) * 768 + 384>(vbase, o); }
                else { ld8_nt<R, 0>(vbase, o); ld8_nt<R + 8, 384>(vbase, o); ld8_nt<R + 16, 768>(vbase, o); ld8_nt<R + 24, 1152>(vbase, o); }
                if (wave == 7 && gi + gj == 0) {
                    asm volatile("; COND_BEGIN");
                    if (h == 3 || h == 6) mb = ld4_sc1(ws, WS_R + (unsigned)((lane & 31) * 4));
                    if (h == 4 || h == 7) {
                        WAIT1(mb, 2);
                        if (all_set(mb) && !lds_word(lb + L_CNT + 36)) answer(lane, mb);
                    }
                    asm volatile("; COND_END");
                }
                if (wave == 6) {
                    asm volatile("; COND_BEGIN");
                if (h == HD) {
                    WAIT8(a, 3 * (7 - GC) + NV);
                    const f32x4 sq = SUM8(a);
                    unsigned* qp = reinterpret_cast<unsigned*>(smem + L_Q + (lane >> 2) * 32 + (lane & 3) * 4);
                    qp[0] = pk2(sq[0], sq[1]); qp[4] = pk2(sq[2], sq[3]);
                    asm volatile("s_waitcnt lgkmcnt(0)\n\tds_write_b32 %0, %1" :: "v"(lb + L_CNT + 4), "v"(1u) : "memory");
                    STAMP(27);
                    lead = LEAD * PACE * 4;
                }
                    if (h == 6) mb = ld4_sc1(ws, WS_MB + (unsigned)((gi * 8 + (gi & 7)) * 128 + 32));
                    if (h == 8) {
                        WAIT1(mb, 6);
                        const unsigned r = __builtin_amdgcn_readfirstlane(mb);
                        if (r) { adopt(r); STAMP(29); }
                    }
                    asm volatile("; COND_END");
                }
                if constexpr (h < 6) {
                    constexpr int i = 2 * h;
                    fix_k1(IC<i>{}, IC<20 - 6 * (i / 3) - i % 3 + NV>{});
                    fix_k1(IC<i + 1>{}, IC<20 - 6 * ((i + 1) / 3) - (i + 1) % 3 + NV>{});
                }
                if (h == 5) {
                    STAMP(1);
                    __builtin_amdgcn_s_setprio(0);
                    att_qk0();
                }
            };
            v_group(IC<0>{}); v_group(IC<1>{}); v_group(IC<2>{}); v_group(IC<3>{}); v_group(IC<4>{}); v_group(IC<5>{});
            v_group(IC<6>{}); v_group(IC<7>{}); v_group(IC<8>{}); v_group(IC<9>{}); v_group(IC<10>{}); v_group(IC<11>{});
            lds_add(16, 1u);
        }
    }
    {
        STAMP(28);
        att_addr();
        const int lane = lane_id();
        cma = lb + L_M; cxa = lb + L_M + (unsigned)((lane >> 4) * 8); cra = lb + L_OP + (unsigned)(512 + (lane >> 4) * 2048 + (lane & 15) * 16);
        cso = WS_OWN + (unsigned)(gi * 16384 + gj * 512 + 256 + (lane >> 5) * 64 + (lane & 12) * 4 + (lane & 1) * 8192);
        PIN2(cma, cxa); PIN2(cra, cso);
        att_pv0();
    }
    {
        f32x4 m0, m1, o0, o1; f32x2 mm; unsigned r;
        asm volatile("s_mov_b64 exec, 1\n\tds_add_rtn_u32 %5, %6, %7 offset:%10\n\ts_mov_b64 exec, -1\n\t"
                     "ds_read_b128 %0, %6\n\tds_read_b128 %1, %6 offset:16\n\tds_read_b64 %2, %8\n\tds_read_b128 %3, %9\n\tds_read_b128 %4, %9 offset:1024\n\ts_waitcnt lgkmcnt(0)"
                     : "=&v"(m0), "=&v"(m1), "=&v"(mm), "=&v"(o0), "=&v"(o1), "=&v"(r)
                     : "v"(cma), "v"(1u), "v"(cxa), "v"(cra), "i"(L_CNT + 28 - L_M) : "memory");
        ord = __builtin_amdgcn_readfirstlane(r);
        if (ord == 7) { STAMP(5); comb_o1(m0, m1, mm, o0, o1, cso, 0x0000333300003333ull, true); lds_set(lb + L_CNT + 48, now() | 1u); STAMP(21); }
        const int lane = lane_id();
        auto wait_cnt = [&](int off, unsigned v) {
#pragma unroll 1
            for (unsigned spins = 0; spins < (kSpinCap << 4); ++spins) { if (lds_word(lb + L_CNT + off) >= v) break; __builtin_amdgcn_s_sleep(1); }
        };
        if (ord == 1) {
            wait_cnt(16, 8u);
            unsigned xa, ra; ml_addr(lane, xa, ra);
            comb_ml(cma, xa, ra, WS_OWN + (unsigned)(gi * 16384 + gj * 512 + 384 + (lane & 1) * 8192), true);
            STAMP(24);
        } else if (ord == 0 && !lds_word(lb + L_CNT + 52)) {
            if (gi + gj == 0 && !lds_word(lb + L_CNT + 36)) {
                unsigned r = 0u;
#pragma unroll 1
                for (unsigned spins = 0; spins < kSpinCap; ++spins) {
                    r = ld4_sc1(ws, WS_R + (unsigned)((lane & 31) * 4));
                    WAIT1(r, 0);
                    if (all_set(r)) break;
                    if ((spins & 7u) == 7u) send_flag(lane);
                }
                answer(lane, r);
            }
#pragma unroll 1
            for (unsigned spins = 0; spins < kSpinCap; ++spins) {
                unsigned r = ld4_sc1(ws, WS_MB + (unsigned)((gi * 8 + (gi & 7)) * 128 + 32));
                WAIT1(r, 0);
                r = __builtin_amdgcn_readfirstlane(r);
                if (r) { adopt(r); break; }
                if ((spins & 7u) == 7u) { send_flag(lane); report(); }
                __builtin_amdgcn_s_sleep(16);
            }
            STAMP(29);
        } else if (ord == 2) {
#pragma unroll 1
            for (unsigned spins = 0; spins < (kSpinCap << 6); ++spins) if (lds_word(lb + L_CNT + 24) >= 8u) break;
            const unsigned ra = lb + L_OP + (unsigned)((lane >> 5) * 4096 + (lane & 31) * 16);
            comb_o0(*(const LAS f32x4*)(size_t)cma, *(const LAS f32x4*)(size_t)(cma + 16), *(const LAS f32x4*)(size_t)(cma + (lane >> 5) * 16), *(const LAS f32x4*)(size_t)ra, *(const LAS f32x4*)(size_t)(ra + 1024),
                    *(const LAS f32x4*)(size_t)(ra + 2048), *(const LAS f32x4*)(size_t)(ra + 3072), WS_OWN + (unsigned)(gi * 16384 + gj * 512 + (lane & 60) * 4 + (lane & 1) * 8192), 0x3333333333333333ull, true);
            STAMP(22);
        }
#pragma unroll 1
        for (;;) {
            unsigned j;
            asm volatile("s_mov_b64 exec, 1\n\tds_add_rtn_u32 %0, %1, %2\n\ts_mov_b64 exec, -1\n\ts_waitcnt lgkmcnt(0)" : "=&v"(j) : "v"(lb + L_CNT + 40), "v"(1u) : "memory");
            j = __builtin_amdgcn_readfirstlane(j);
            if (j >= 5u - WEARLY) break;
            load_wo((int)j, lane, false); ++nj;
        }
        STAMP(23);
    }

    auto chain = [&](auto LC) {
        constexpr int layer = decltype(LC)::value;
        unsigned h4 = WS_H4 + (unsigned)((layer * 32 + gi) * 32768) + (layer ? rS ^ 8192u : 0u);
        auto sides = [&]() { rS = lds_word(lb + L_CNT + 80); nS = lds_word(lb + L_CNT + 84); h4 += rS ^ 8192u; };
        if (wave < 3) {
            LANE_VARS();
            if (layer == 0) {
                VMW(0);
                lds_add(44, nj);
                unsigned tf = 0u;
#pragma unroll 1
                for (unsigned spins = 0; spins < (kSpinCap << 8); ++spins) {
                    tf = lds_word(lb + L_CNT + (EMODE ? 48 : 24));
                    if ((EMODE ? tf != 0u : tf >= 8u) && lds_word(lb + L_CNT + 52)) break;
                }
                sides();
                if (EMODE == 1) until(tf + DG2 + 3);
                if (wave < 2) empty5(5, (unsigned)wave * 8192u, lane); else empty4(0, rS, lane);
                VMW(0);
                BAR();
#pragma unroll 1
                for (unsigned spins = 0; spins < (kSpinCap << 4); ++spins) if (lds_word(lb + L_CNT + 76) >= 5u) break;
                if (wave < 2) { empty1(wave, rS, lane); empty35(1, (unsigned)wave * 8192u, lane); }
                else { empty4(1, rS, lane); st16m(ws, WS_CS + (unsigned)((gi * 8 + gj) * 768 + lane * 16), *reinterpret_cast<const u32x4*>(smem + L_CS + lane * 16), 0xffffffffffffull); }
            } else BAR();
            {
                const unsigned im = lb + L_GU + (unsigned)(32 * wave + 8 * p4);
                Frag80 fg = frag80(im, 96, r0s, r1s, r2s), fu = frag80(im + 8192, 96, r0s, r1s, r2s);
                const unsigned xb = lb + L_XS + (unsigned)((n < 4 ? n : 4) * 160);
                unsigned b16 = xb + kq * 16, b8 = xb + 128 + kq * 8;
                unsigned sg = h4 + (unsigned)((n < 4 ? h4s(wave) + gj * 512 + n * 64 : H4C + wave * 1024 + gj * 128) + kq * 16), su = sg + (n < 4 ? 256u : 64u);
                unsigned o = h4 + h4s(wave) + (unsigned)(lane * 16);
                unsigned oc = h4 + (unsigned)(H4C + wave * 1024 + ((lane >> 5) * 4 + q4) * 128 + (kq & 1) * 64 + p4 * 16);
                unsigned os = h4 + (unsigned)(H4T + (p4 + 4 * (kq & 1)) * 128 + q4 * 16);
                unsigned ha = lb + L_H + (unsigned)(q4 * 96 + wave * 32 + p4 * 8 + kq * 2);
                f32x2 cs = {0.f, 0.f};
                if (layer != 0) cs = *reinterpret_cast<const f32x2*>(smem + L_CS2 + wave * 512 + lane * 8);
                PIN3(fg.a0, fg.a1, fg.a2); PIN3(fu.a0, fu.a1, fu.a2); PIN3(b16, b8, sg); PIN3(su, o, os); PIN2(ha, cs);
                unsigned long long mk = layer ? 0x000f000f000f000full : 0x001f001f001f001full;
                unsigned ta = WS_MB + (unsigned)((lane >> 1) * 1024 + 64 + (lane & 1) * 16);
                if (layer == 0) PIN2(oc, ta);
                PINS(mk);
                if (layer == 0) VMW(0);
                BAR();
                STAMP(10 + 32 * layer);
                u32x4 tl = {0u, 0u, 0u, 0u};
                if (layer == 0) tl = ld16_sc1(ws, ta);
                const Frag80 b = rows80(b16, b8);
                const f32x4 g0 = mfma32(fg.a0, b.a0, z4), u0 = mfma32(fu.a0, b.a0, z4), g1 = mfma32(fg.a1, b.a1, g0), g2 = mfma16(fg.a2, b.a2, z4), u1 = mfma32(fu.a1, b.a1, u0), u2 = mfma16(fu.a2, b.a2, z4);
                __builtin_amdgcn_sched_barrier(0);
                const f32x4 cg = g1 + g2, cu = u1 + u2;
                st16m2<0>(ws, sg, asu(cg), su, asu(cu), mk);
                STAMP(11 + 32 * layer);
                if (layer == 0) {
                    WAIT1(tl, 2);
#pragma unroll 1
                    for (unsigned spins = 0; spins < kSpinCap && !all_set(min(min(tl[0], tl[1]), min(tl[2], tl[3]))); ++spins) {
                        tl = ld16_sc1(ws, ta);
                        WAIT1(tl, 0);
                    }
                    const unsigned tn = now();
                    unsigned d = min(min(min(tn - tl[0], tn - tl[1]), min(tn - tl[2], tn - tl[3])), 800u);
                    d = min(d, dppu<0xB1>(d)); d = min(d, dppu<0x4E>(d)); d = min(d, dppu<0x124>(d)); d = min(d, dppu<0x128>(d));
                    unsigned tr = tn - min(min((unsigned)__builtin_amdgcn_readlane(d, 0), (unsigned)__builtin_amdgcn_readlane(d, 16)), min((unsigned)__builtin_amdgcn_readlane(d, 32), (unsigned)__builtin_amdgcn_readlane(d, 48)));
                    lds_set(lb + L_CNT + 56, tr | 1u);
                                        until(__builtin_amdgcn_readfirstlane(tr) + X4A);
                } else sched(X4, 12);
                STAMP(16 + 32 * layer);
                float hv; f32x2 st;
                u32x4 a0, a1, a2, a3, ac, as;
                auto load = [&]() __attribute__((always_inline)) {
                    as = ld16_sc1(ws, os);
                    if (layer == 0) ac = ld16_sc1(ws, oc);
                    a0 = ld16_sc1<0>(ws, o); a1 = ld16_sc1<1024>(ws, o); a2 = ld16_sc1<2048>(ws, o); a3 = ld16_sc1<3072>(ws, o);
                    if (layer == 0) asm volatile("s_waitcnt vmcnt(0)" : "+v"(a0), "+v"(a1), "+v"(a2), "+v"(a3), "+v"(ac), "+v"(as) :: "memory");
                    else { asm volatile("s_waitcnt vmcnt(0)" : "+v"(a0), "+v"(a1), "+v"(a2), "+v"(a3), "+v"(as) :: "memory"); ac = as; }
                };
                auto compute = [&]() __attribute__((always_inline)) {
                    float ss, sm;
                    asm volatile("v_add_f32_dpp %0, %2, %2 quad_perm:[1,0,3,2] row_mask:0xf bank_mask:0xf\n\tv_add_f32_dpp %1, %3, %3 quad_perm:[1,0,3,2] row_mask:0xf bank_mask:0xf\n\ts_nop 0\n\t"
                                 "v_add_f32_dpp %0, %0, %0 quad_perm:[2,3,0,1] row_mask:0xf bank_mask:0xf\n\tv_add_f32_dpp %1, %1, %1 quad_perm:[2,3,0,1] row_mask:0xf bank_mask:0xf\n\ts_nop 0"
                                 : "=&v"(ss), "=&v"(sm) : "v"(as[0]), "v"(as[1]));
                    ss = x16_add(ss); sm = x16_add(sm);
                    st = ln_stats(ss, sm);
                    const f32x4 t = (asf(a0) + asf(a1)) + (asf(a2) + asf(a3));
                    const f32x2 gu = sw16(rs32(t[0], t[2]), rs32(t[1], t[3]));
                    if (layer == 0) {
                        cs = sw16(rs32(asf(ac)[0], asf(ac)[2]), rs32(asf(ac)[1], asf(ac)[3]));
                        cs[0] += dpp<0x124>(cs[0]); cs[1] += dpp<0x124>(cs[1]); cs[0] += dpp<0x128>(cs[0]); cs[1] += dpp<0x128>(cs[1]);
                    }
                    const unsigned gw = pk2v((gu - st[0] * cs) * st[1]);
                    const float gv = lo16(gw);
                    hv = bfr(gv * frcp(1.f + ex2(gv * -LOG2E))) * hi16(gw);
                    *(LAS u16*)(size_t)ha = (u16)pk2(hv, hv);
                    __builtin_amdgcn_sched_barrier(0);
                };
                RETRY(load(), max(max(max(um4(a0), um4(a1)), max(um4(a2), um4(a3))), max(um4(ac), um4(as))), compute(), any_nan(hv, hv), layer ? X4 : X4A);
                BAR();
                STAMP(12 + 32 * layer);
                if (layer == 0) {
                    *reinterpret_cast<f32x2*>(smem + L_CS2 + wave * 512 + lane * 8) = cs;
                }
            }
            if (layer == 0) {
                if (wave == 1) {
                    const unsigned o = WS_CS + (unsigned)(gi * 6144 + 3072 + (lane < 48 ? lane : 0) * 16);
                    u32x4 a[8];
                    GATHER8(a, o, 768);
                    WAIT8(a, 0);
                    if (lane < 48) *reinterpret_cast<f32x4*>(smem + L_CS + lane * 16) = SUM8(a);
                }
                Frag80 f[4];
#pragma unroll
                for (int i = 0; i < 4; ++i) { f[i] = frag80(lb + L_WQ + (unsigned)(128 * wave + 32 * i + 8 * p4), 416, r0s, r1s, r2s); PIN3(f[i].a0, f[i].a1, f[i].a2); }
                unsigned b16 = lb + L_XS + (unsigned)(p4 * 160 + kq * 16), b8 = lb + L_XS + (unsigned)(p4 * 160 + 128 + kq * 8);
                unsigned so = WS_H1 + rS + (unsigned)((gi * 4 + p4) * 16384 + gj * 896 + 256 * wave + q4 * 64 + kq * 16);
                unsigned long long mk = 0x000f000f000f000full;
                PIN3(b16, b8, so); PINS(mk);
                BAR();
                STAMP(33);
                const Frag80 b = rows80(b16, b8);
                st16_sc1(ws, so, tiles4(f, b, mk));
                STAMP(34);
                qk_addr(true); att_addr();
                cma = lb + L_M; cxa = lb + L_M + (unsigned)(wave == 0 ? (lane >> 5) * 16 : (lane >> 4) * 8);
                cra = lb + L_OP + (unsigned)(wave == 0 ? (lane >> 5) * 4096 + (lane & 31) * 16 : 512 + (lane >> 4) * 2048 + (lane & 15) * 16);
                if (wave == 2) ml_addr(lane, cxa, cra);
                cso = WS_OWN + rS + (unsigned)(gi * 16384 + 4096 + gj * 512) + (unsigned)(wave == 0 ? lane * 4 : wave == 1 ? 256 + (lane >> 5) * 64 + (lane & 15) * 4 : 384);
                PIN2(cma, cxa); PIN2(cra, cso);
            }
        } else {
            const int t = wave - 3;
            StatAddr sa;
            auto gather32 = [&](unsigned o, int lane, unsigned X, auto mid, auto fin) __attribute__((always_inline)) {
                u32x4 a0 = ld16_sc1<-2048>(ws, o), a1 = ld16_sc1<-1024>(ws, o), a2 = ld16_sc1<0>(ws, o), a3 = ld16_sc1<1024>(ws, o), sel;
                const unsigned one = (lane & 1) ? 0x3f800000u : 0x3f80u;
#pragma unroll
                for (int k = 0; k < 4; ++k) sel[k] = ((lane >> 1) & 3) == k ? one : 0u;
                unsigned long long hi = 0xff00ff00ff00ff00ull;
                PIN1(sel); PINS(hi);
                mid();
                float v;
                auto sum = [&]() __attribute__((always_inline)) {
                    f32x4 d; float w;
                    asm volatile("s_waitcnt vmcnt(3)\n\tv_mfma_f32_16x16x32_bf16 %0, %1, %5, 0\n\ts_waitcnt vmcnt(2)\n\tv_mfma_f32_16x16x32_bf16 %0, %2, %5, %0\n\t"
                                 "s_waitcnt vmcnt(1)\n\tv_mfma_f32_16x16x32_bf16 %0, %3, %5, %0\n\ts_waitcnt vmcnt(0)\n\tv_mfma_f32_16x16x32_bf16 %0, %4, %5, %0"
                                 : "=&v"(d), "+v"(a0), "+v"(a1), "+v"(a2), "+v"(a3) : "v"(sel) : "memory");
                    asm volatile("s_nop 7\n\tv_cndmask_b32 %0, %2, %3, %6\n\tv_cndmask_b32 %1, %4, %5, %6\n\ts_nop 1\n\tv_permlane32_swap_b32 %0, %1\n\tv_add_f32 %0, %0, %1"
                                 : "=&v"(v), "=&v"(w) : "v"(d[0]), "v"(d[1]), "v"(d[2]), "v"(d[3]), "s"(hi));
                    fin(v);
                    __builtin_amdgcn_sched_barrier(0);
                };
                sum();
                if (__builtin_expect(any_nan(v, v) != 0ull, 0)) {
#pragma unroll 1
                    for (unsigned spins = 0; spins < kSpinCap; ++spins) {
                        slip(X);
                        again16<-2048>(a0, ws, o); again16<-1024>(a1, ws, o); again16<0>(a2, ws, o); again16<1024>(a3, ws, o);
                        asm volatile("s_waitcnt vmcnt(0)" : "+v"(a0), "+v"(a1), "+v"(a2), "+v"(a3) :: "memory");
                        if (!any_empty(max(max(um4(a0), um4(a1)), max(um4(a2), um4(a3))))) break;
                    }
                    sum();
                }
            };
            auto pack4 = [&](float c0, float c1, float c2, float c3) __attribute__((always_inline)) { const unsigned w0 = min(pk2(c0, c1), 0xfffffffeu), w1 = min(pk2(c2, c3), 0xfffffffeu); return (u32x2){w0, w1}; };
            unsigned xw;
            __builtin_amdgcn_s_setprio(0);
            {
                LANE_VARS();
                if (layer == 0) xe = lo16(*reinterpret_cast<const u16*>(smem + L_X0 + kq * 1280 + gj * 160 + t * 32 + n * 2));
                STAMP(6 + 32 * layer);
                if (layer == 0) nS = lds_word(lb + L_CNT + 84);
                unsigned lo = WS_OWN + (layer ? rS : nS) + (unsigned)(gi * 16384 + layer * 4096 + (lane >> 3) * 512 + t * 128 + (t == 3 ? 0 : (lane & 7) * 16));
                unsigned lw = lb + L_O2 + (unsigned)((lane >> 3) * 416 + t * 128 + (t == 3 ? 0 : (lane & 7) * 16));
                PIN2(lo, lw);
                u32x4 ov = {~0u, ~0u, ~0u, ~0u};
                if (layer != 0 && t < 4) { sched(X2, 20); STAMP(49); ov = ld16_sc1(ws, lo); }
                const unsigned wa = lb + L_WO + (unsigned)(r0s * 160 + 32 * t + 8 * p4), wa1 = lb + L_WO + (unsigned)(r1s * 160 + 32 * t + 8 * p4);
                bf16x8 af[6];
                if (layer == 0) {
                    if (t < 4) {
                        unsigned tf = 0u;
#pragma unroll 1
                        for (unsigned spins = 0; spins < (kSpinCap << 4); ++spins) { tf = lds_word(lb + L_CNT + 48); if (tf) break; }
                        until(tf + DG2);
                        ov = ld16_sc1(ws, lo);
                        if (nj) { WAIT1(ov, 1); lds_add(44, nj); }
                    } else { VMW(0); lds_add(44, nj); }
#pragma unroll 1
                    for (unsigned spins = 0; spins < (kSpinCap << 4); ++spins) if (lds_word(lb + L_CNT + 44) >= 5u) break;
                    STAMP(8);
                }
#pragma unroll
                for (int s = 0; s < 6; ++s) af[s] = cat8(tr64(wa + s * 32 * 160), tr64(wa1 + s * 32 * 160));
                const unsigned h3 = tile35(gj, 10 * layer + t);
                unsigned so = h3 + (unsigned)((lane & 1) * 8192 + gi * 128 + (lane & 2) * 32 + (lane & 4) * 8 + kq * 8), go = h3 + nS + (unsigned)(2048 + lane * 16);
                unsigned xs = lb + L_XS + (unsigned)(kq * 160 + t * 32 + n * 2);
                unsigned br = lb + L_O2 + (unsigned)((n & 7) * 416 + kq * 16), mr = lb + L_O2 + (unsigned)((n & 7) * 416 + 384);
                unsigned long long mk = 0x00ff00ff00ff00ffull;
                PIN3(af[0], af[1], af[2]); PIN3(af[3], af[4], af[5]); PIN3(br, mr, so); PIN2(go, xs); PINS(mk);
                if (t < 4) {
                    WAIT1(ov, 0);
                    *(LAS u32x4*)(size_t)lw = ov;
                    if (layer == 0 ? any_empty(um4(ov)) : __builtin_expect(any_empty(um4(ov)), 0)) {
#pragma unroll 1
                        for (unsigned spins = 0; spins < kSpinCap; ++spins) {
                            if (layer != 0) slip(X2);
                            ov = ld16_sc1(ws, lo);
                            WAIT1(ov, 0);
                            *(LAS u32x4*)(size_t)lw = ov;
                            if (!any_empty(um4(ov))) break;
                            if (layer == 0) { if ((spins & 15u) == 15u) send_flag(lane); __builtin_amdgcn_s_sleep(1); }
                        }
                    }
                }
                BAR();
                bf16x8 bf[6];
                const f32x2 ml = *(const LAS f32x2*)(size_t)mr;
#pragma unroll
                for (int s = 0; s < 6; ++s) bf[s] = lds16(br + s * 64);
                __builtin_amdgcn_sched_barrier(0);
                f32x4 ca = z4;
#pragma unroll
                for (int s = 0; s < 6; ++s) ca = mfma32(af[s], bf[s], ca);
                const float e = ex2(ml[0] - fmax2(ml[0], dpp<0xB1>(ml[0]))), el = e * ml[1];
                const float f = e * frcp(el + dpp<0xB1>(el));
                float c0, c1, c2, c3;
                asm volatile("s_nop 7\n\t"
                             "v_mul_f32 %0, %4, %8\n\tv_mul_f32 %1, %5, %8\n\tv_mul_f32 %2, %6, %8\n\tv_mul_f32 %3, %7, %8\n\t"
                             "v_add_f32_dpp %0, %0, %0 quad_perm:[1,0,3,2] row_mask:0xf bank_mask:0xf\n\tv_add_f32_dpp %1, %1, %1 quad_perm:[1,0,3,2] row_mask:0xf bank_mask:0xf\n\t"
                             "v_add_f32_dpp %2, %2, %2 quad_perm:[1,0,3,2] row_mask:0xf bank_mask:0xf\n\tv_add_f32_dpp %3, %3, %3 quad_perm:[1,0,3,2] row_mask:0xf bank_mask:0xf"
                             : "=&v"(c0), "=&v"(c1), "=&v"(c2), "=&v"(c3) : "v"(ca[0]), "v"(ca[1]), "v"(ca[2]), "v"(ca[3]), "v"(f));
                st8m(ws, so, pack4(c0, c1, c2, c3), mk);
                if (layer == 0 && wave == 7) st4_sc1(ws, WS_MB + (unsigned)(gi * 1024 + 64 + gj * 4), now() | 1u);
                if (layer == 0) { lds_add(76, 1u); go -= nS; sides(); go += nS; }
                STAMP(7 + 32 * layer);
                if (layer != 0) sched(X3, 12);
                else wait_word(ws, h3 + nS + (unsigned)(((gi * 7 + t * 5 + 3) & 31) * 128));
                STAMP(18 + 32 * layer);
                gather32(go, lane, layer ? X3 : 0u, [&]() { sa = stats_addr(lane, h4 + (unsigned)(H4T + gj * 128), 16); }, [&](float s) { xw = pk2(xe + bfr(s), 0.f); *(LAS u16*)(size_t)xs = (u16)xw; });
                xe = lo16(xw);
                STAMP(9 + 32 * layer);
            }
            BAR();
            if (wave == 3) stats(sa);
            STAMP(11 + 32 * layer);
            {
                LANE_VARS();
                const unsigned im = lb + L_WD + (unsigned)(32 * t + 8 * p4);
                bf16x8 wd0 = cat8(tr64(im + r0s * 160), tr64(im + r1s * 160)); bf16x4 wd1 = tr64(im + (32 + r2s) * 160);
                const unsigned h5 = tile35(gj, 10 * layer + 5 + t);
                unsigned h16 = lb + L_H + (unsigned)(p4 * 96 + kq * 16), h8 = lb + L_H + (unsigned)(p4 * 96 + 64 + kq * 8);
                unsigned so = h5 + (unsigned)((lane & 4) * 2048 + gi * 128 + (lane & 1) * 64 + (lane & 2) * 16 + kq * 8);
                unsigned long long mk = 0x00ff00ff00ff00ffull;
                PIN2(wd0, wd1); PIN3(h16, h8, so); PINS(mk);
                if (wave == 4) {
                    const unsigned os = h4 + (unsigned)(H4T + (p4 + 4 * (kq & 1)) * 128 + q4 * 16);
                    if (layer == 0) {
#pragma unroll 1
                        for (unsigned spins = 0; spins < (kSpinCap << 4); ++spins) if (lds_word(lb + L_CNT + 56)) break;
                    }
                    sched(layer ? X4 : X4A, 0);
                    u32x4 as;
#pragma unroll 1
                    for (unsigned spins = 0; spins < kSpinCap; ++spins) { as = ld16_sc1(ws, os); WAIT1(as, 0); if (!any_empty(um4(as))) break; }
                    float ss = __uint_as_float(as[0]), sm = __uint_as_float(as[1]), rd = __uint_as_float(as[2]);
                    ss += dpp<0xB1>(ss); sm += dpp<0xB1>(sm); rd += dpp<0xB1>(rd);
                    ss += dpp<0x4E>(ss); sm += dpp<0x4E>(sm); rd += dpp<0x4E>(rd);
                    ss = x16_add(ss); sm = x16_add(sm); rd = x16_add(rd);
                    if (LANES(0x1111ull)) *reinterpret_cast<f32x4*>(smem + L_ST + q4 * 16) = (f32x4){ss, sm, rd, 0.f};
                }
                BAR();
                STAMP(12 + 32 * layer);
                const bf16x8 hb0 = lds16(h16); const bf16x4 hb1 = lds8(h8);
                __builtin_amdgcn_sched_barrier(0);
                const f32x4 c = mfma32(wd0, hb0, z4) + mfma16(wd1, hb1, z4);
                st8m(ws, so, pack4(c[0], c[1], c[2], c[3]), mk);
                STAMP(13 + 33 * layer);
                if (layer == 0 || gi == 4 * t + (gj & 3)) {
                    float rs, n1;
                    auto prep = [&]() __attribute__((always_inline)) {
                        const f32x4 sr = *reinterpret_cast<const f32x4*>(smem + L_ST + kq * 16);
                        const float sw = *reinterpret_cast<const float*>(smem + L_CNT + 12);
                        const f32x2 st = ln_stats(sr[0], sr[1]);
                        const float r = bfr((sr[2] - st[0] * sw) * st[1]);
                        rs = bfr(frcp(1.f + ex2(r * -LOG2E)));
                        n1 = bfr(bfr(xe - st[0]) * st[1]);
                        PIN2(rs, n1);
                    };
                    if (layer == 0) {
                        unsigned go = h5 + nS + (unsigned)(2048 + lane * 16), xs = lb + L_XS + (unsigned)(kq * 160 + t * 32 + n * 2);
                        PIN2(go, xs);
                        sched(X5A, 8);
                        STAMP(15);
                        gather32(go, lane, X5A, [&]() { prep(); sa = stats_addr(lane, WS_H1 + rS + (unsigned)(gi * 65536 + gj * 896 + 768), 16384); },
                                 [&](float y) { xw = pk2(xe + bfr(n1 + bfr(bfr(y) * rs)), 0.f); *(LAS u16*)(size_t)xs = (u16)xw; });
                        xe = lo16(xw);
                        STAMP(14);
                    } else {
                        unsigned go = h5 + nS + (unsigned)(2048 + lane * 16), oo = (unsigned)(kq * 1280 + gj * 160 + t * 32 + n * 2);
                        PIN2(go, oo);
                        sched(X5B, 8);
                        STAMP(47);
                        gather32(go, lane, X5B, prep, [&](float y) { xw = pk2(xe + bfr(n1 + bfr(bfr(y) * rs)), 0.f); });
                        { const unsigned w = xw | (dppu<0xF5>(xw) << 16);
                          const u32x2 w2 = {w, dppu<0xAA>(w)};
                          asm volatile("s_mov_b64 exec, %3\n\tglobal_store_dwordx2 %0, %1, %2 sc0 sc1\n\ts_mov_b64 exec, -1" :: "v"(oo), "v"(w2), "s"(out), "s"(0x1111111111111111ull) : "memory"); }
                        STAMP(45);
                    }
                }
            }
            if (layer == 0) {
                BAR();
                if (wave == 3) { stats(sa); STAMP(34); }
                asm volatile("" : "=v"(cma), "=v"(cxa), "=v"(cra), "=v"(cso));
                if (wave != 7) { qk_addr(true); att_addr(); }
                else {
                    const int lane = lane_id();
                    unsigned o = WS_H1 + rS + (unsigned)((gi * 4 + (gj >> 1)) * 16384 + 3584 + (lane < 48 ? lane : 48) * 16), qw;
                    f32x4 cs;
                    PIN1(o);
                    sched(X1, 26);
                    STAMP(51);
                    f32x4 s; u32x4 a[8];
                    RETRY8(a, o, 896, s, { qw = lb + L_Q + (unsigned)((lane >> 2) * 32 + (lane & 3) * 4); cs = *reinterpret_cast<const f32x4*>(smem + L_CS + lane * 16); PIN2(qw, cs); qk_addr(true); att_addr(); }, { s -= bfr(__int_as_float(__builtin_amdgcn_readlane(__float_as_int(s[1]), 48)) * (1.f / DM)) * cs;
                                           u32x2 w; w[0] = pk2(s[0], s[1]); w[1] = pk2(s[2], s[3]);
                                           if (LANES(0xffff000000000000ull)) { w[0] = __float_as_uint(s[0]); w[1] = __float_as_uint(s[1]); }
                                           *(LAS unsigned*)(size_t)qw = w[0]; *(LAS unsigned*)(size_t)(qw + 16) = w[1]; __builtin_amdgcn_sched_barrier(0); }, any_nan(s[0] + s[2], s[1] + s[3]), X1);
                }
            }
        }
    };
    PINS(ck1); PINS(ck2); PINS(csc);
    chain(IC<0>{});
    STAMP(35);
    att1();
    chain(IC<1>{});
}

__device__ __attribute__((used, noinline)) void mega_code_end() { asm volatile(".Lmega_code_end:"); }

extern "C" void kernel_launch(void* const* d_in, const int* in_sizes, int n_in, void* d_out, int out_size, void* d_ws,
                              size_t ws_size, hipStream_t stream) {
    const u16* x0 = (const u16*)d_in[0];
    const u16* kc = (const u16*)d_in[1];
    const u16* vc = (const u16*)d_in[2];
    const u16* wq = (const u16*)d_in[3];
    const u16* wo = (const u16*)d_in[4];
    const u16* wr = (const u16*)d_in[6];
    const u16* wg = (const u16*)d_in[7];
    const u16* wu = (const u16*)d_in[8];
    const u16* wd = (const u16*)d_in[9];
    (void)hipFuncSetAttribute((const void*)mega_kernel, hipFuncAttributeMaxDynamicSharedMemorySize, L_END);
    mega_kernel<<<256, 512, L_END, stream>>>(x0, kc, vc, wq, wo, wr, wg, wu, wd, (u16*)d_out, (unsigned char*)d_ws
                                             );
}
```

```cpp
#define DM_ 3
#define PINIT 150
#define PACE 61
#include <hip/hip_runtime.h>
#include <hip/hip_bf16.h>
#include <stdint.h>

typedef unsigned short u16;
typedef unsigned u32x4 __attribute__((ext_vector_type(4)));
typedef unsigned u32x2 __attribute__((ext_vector_type(2)));
typedef float f32x4 __attribute__((ext_vector_type(4)));
typedef short s16x4 __attribute__((ext_vector_type(4)));
typedef __attribute__((ext_vector_type(4))) __bf16 bf16x4;
typedef __attribute__((ext_vector_type(8))) __bf16 bf16x8;
#define LAS __attribute__((address_space(3)))

constexpr int DM = 640, HQ = 32, SK = 4096, DH = 192, WIN = 1024, FF = 1536;
constexpr int QD = HQ * DH;
constexpr float SCALE = 0.07216878364870322f, LOG2E = 1.44269504088896341f;
constexpr unsigned kSpinCap = 1u << 16;
#define GJ(g) (((g) >> 3) & 7)
#define GI(g) (((g) & 7) * 4 + ((g) >> 6))

#define D4A 128
#define D5A 94
#define D1 94
#define D2 147
#define D3 74
#define D4 80
#define D5B 84
#define PMIN 50
constexpr unsigned X4A = D4A + DM_, X5A = X4A + D5A + DM_, X1 = X5A + D1 + DM_, X2 = X1 + D2 + DM_, X3 = X2 + D3 + DM_, X4 = X3 + D4 + DM_, X5B = X4 + D5B + DM_;
#define WOC 2
#define WOFF 6
#define WEARLY 1
#define DG2 15
#define XCOST 16
#define LEAD 12
#define TOFF 20
#define UG 360
#define SLOW 0
#define ADOPT 1
#define ALT 1
#define PROBE 1
constexpr unsigned NUNIT = 384 + XCOST + 27 + 6 * WEARLY, PBURST = 30;
#define GSTEPS GA = 1, GB = 6, GC = 7, HD = 1
#define EMODE 0
constexpr int GP = 0, GSTEPS;

constexpr unsigned WS_H1 = 0;
constexpr unsigned WS_H4 = 2u << 20;
constexpr unsigned H4C = 20480, H4T = H4C + 3072;
constexpr unsigned WS_H35 = 4u << 20;
constexpr unsigned WS_OWN = WS_H35 + 80 * 16384;
constexpr unsigned WS_CS = WS_OWN + 32 * 16384;
constexpr unsigned WS_MB = WS_CS + 256 * 768;
constexpr unsigned WS_R = WS_MB + 256 * 128;
constexpr unsigned WS_END = WS_R + 128;
static_assert(WS_END <= (6u << 20), "ws map");
__device__ __forceinline__ unsigned tile35(int j, int q) { return WS_H35 + (unsigned)((j * 10 + (q >> 1)) * 16384 + (q & 1) * 4096); }
__device__ __forceinline__ unsigned h4s(int s) { return (unsigned)((s >> 1) * 16384 + (s & 1) * 4096); }

constexpr int L_WQ = 0;
constexpr int L_WO = 33792;
constexpr int L_GU = L_WO + 30720;
constexpr int L_WD = L_GU + 16384;
constexpr int L_X0 = L_WD + 8192;
constexpr int L_WR = L_X0 + 5120;
constexpr int L_XS = L_WR + 2048;
constexpr int L_Q = L_XS + 1024;
constexpr int L_OP = L_Q + 512;
constexpr int L_M = L_OP + 8192;
constexpr int L_H = L_M + 64;
constexpr int L_ST = L_H + 384;
constexpr int L_CS = L_ST + 64;
constexpr int L_CS2 = L_CS + 1024;
constexpr int L_CNT = L_CS2 + 1536;
constexpr int L_P = L_CNT + 96;
constexpr int L_O2 = L_P + 72 * 192;
constexpr int L_DBG = L_O2 + 3328;
constexpr int L_END = L_DBG + 2048;
static_assert(L_END <= 163840 && L_END > 81920, "LDS");

typedef __attribute__((ext_vector_type(2))) __bf16 bf16x2;
typedef float f32x2 __attribute__((ext_vector_type(2)));
__device__ __forceinline__ unsigned pk2(float a, float b) { return __builtin_bit_cast(unsigned, __builtin_convertvector((f32x2){a, b}, bf16x2)); }
__device__ __forceinline__ float lo16(unsigned w) { return __uint_as_float(w << 16); }
__device__ __forceinline__ float hi16(unsigned w) { return __uint_as_float(w & 0xffff0000u); }
__device__ __forceinline__ float bfr(float f) { return __uint_as_float(pk2(0.f, f)); }
template <int CTRL> __device__ __forceinline__ float dpp(float x) {
    return __builtin_bit_cast(float, __builtin_amdgcn_mov_dpp(__builtin_bit_cast(int, x), CTRL, 0xf, 0xf, true));
}
template <int CTRL> __device__ __forceinline__ unsigned dppu(unsigned x) { return (unsigned)__builtin_amdgcn_mov_dpp((int)x, CTRL, 0xf, 0xf, true); }
__device__ __forceinline__ float row16_sum(float v) {
    v += dpp<0xB1>(v); v += dpp<0x4E>(v); v += dpp<0x141>(v); v += dpp<0x140>(v); return v;
}
__device__ __forceinline__ float fmax2(float a, float b) { return __builtin_elementwise_maximum(a, b); }
__device__ __forceinline__ float fmax3(float a, float b, float c) { return fmax2(fmax2(a, b), c); }
__device__ __forceinline__ float x16_add(float v) { auto s = __builtin_amdgcn_permlane16_swap(__float_as_uint(v), __float_as_uint(v), false, false); return __uint_as_float(s[0]) + __uint_as_float(s[1]); }
__device__ __forceinline__ float x32_add(float v) { auto s = __builtin_amdgcn_permlane32_swap(__float_as_uint(v), __float_as_uint(v), false, false); return __uint_as_float(s[0]) + __uint_as_float(s[1]); }
__device__ __forceinline__ float wave_sum(float v) { return x32_add(x16_add(row16_sum(v))); }
__device__ __forceinline__ float rs32(float a, float b) { auto s = __builtin_amdgcn_permlane32_swap(__float_as_uint(a), __float_as_uint(b), false, false); return __uint_as_float(s[0]) + __uint_as_float(s[1]); }
__device__ __forceinline__ f32x2 sw16(float a, float b) { auto s = __builtin_amdgcn_permlane16_swap(__float_as_uint(a), __float_as_uint(b), false, false); return (f32x2){__uint_as_float(s[0]), __uint_as_float(s[1])}; }

#define BAR() asm volatile("s_waitcnt lgkmcnt(0)\n\ts_barrier" ::: "memory")
#define VMW(n) asm volatile("s_waitcnt vmcnt(%0)" :: "i"(n) : "memory")

template <int OFF = 0> __device__ __forceinline__ u32x4 ld16_sc1(const void* sbase, unsigned voff) {
    u32x4 r; asm volatile("global_load_dwordx4 %0, %1, %2 offset:%3 sc1" : "=v"(r) : "v"(voff), "s"(sbase), "i"(OFF) : "memory"); return r;
}
__device__ __forceinline__ unsigned ld4_sc1(const void* sbase, unsigned voff) {
    unsigned r; asm volatile("global_load_dword %0, %1, %2 sc1" : "=v"(r) : "v"(voff), "s"(sbase) : "memory"); return r;
}
template <int OFF = 0> __device__ __forceinline__ void st16_sc1(void* sbase, unsigned voff, u32x4 v) {
    asm volatile("global_store_dwordx4 %0, %1, %2 offset:%3 sc1\n\ts_nop 1" :: "v"(voff), "v"(v), "s"(sbase), "i"(OFF) : "memory");
}
__device__ __forceinline__ void st4_sc1(void* sbase, unsigned voff, unsigned v) {
    asm volatile("global_store_dword %0, %1, %2 sc1" :: "v"(voff), "v"(v), "s"(sbase) : "memory");
}
__device__ __forceinline__ void st8(void* sbase, unsigned voff, u32x2 v) {
    asm volatile("global_store_dwordx2 %0, %1, %2" :: "v"(voff), "v"(v), "s"(sbase) : "memory");
}
__device__ __forceinline__ void st2(void* sbase, unsigned voff, unsigned v) {
    asm volatile("global_store_short %0, %1, %2" :: "v"(voff), "v"(v), "s"(sbase) : "memory");
}
constexpr int RK = 64, RV = 160;
template <int R, int IMM> __device__ __forceinline__ void ld16_nt(const void* sbase, unsigned voff) {
    asm volatile("global_load_dwordx4 v[%c3:%c4], %0, %1 offset:%2 nt" :: "v"(voff), "s"(sbase), "i"(IMM), "i"(R), "i"(R + 3) : "memory");
}
template <int R, int IMM> __device__ __forceinline__ void ld8_nt(const void* sbase, unsigned voff) {
    asm volatile("global_load_dwordx2 v[%c3:%c4], %0, %1 offset:%2 nt" :: "v"(voff), "s"(sbase), "i"(IMM), "i"(R), "i"(R + 1) : "memory");
}
template <int R> __device__ __forceinline__ void smf(f32x4& d, u32x4 a, unsigned idx) {
    asm volatile("v_smfmac_f32_16x16x64_bf16 %0, %1, v[%c3:%c4], %2" : "+v"(d) : "v"(a), "v"(idx), "i"(R), "i"(R + 7));
}
template <int A, int B> __device__ __forceinline__ void zip_in() {
    unsigned t;
    asm volatile("v_perm_b32 %0, v%c2, v%c1, %3\n\tv_perm_b32 v%c2, v%c2, v%c1, %4\n\tv_mov_b32 v%c1, %0" : "=&v"(t) : "i"(A), "i"(B), "s"(0x05040100u), "s"(0x07060302u));
}
template <int A, int B> __device__ __forceinline__ void zip_out(unsigned& lo, unsigned& hi) {
    asm volatile("v_perm_b32 %0, v%c3, v%c2, %4\n\tv_perm_b32 %1, v%c3, v%c2, %5" : "=&v"(lo), "=&v"(hi) : "i"(A), "i"(B), "s"(0x05040100u), "s"(0x07060302u));
}
template <int A, int B, int C, int D> __device__ __forceinline__ void zip_x(unsigned lo, unsigned hi) {
    asm volatile("v_perm_b32 v%c4, v%c3, v%c2, %6\n\tv_perm_b32 v%c5, v%c3, v%c2, %7\n\tv_mov_b32 v%c2, %0\n\tv_mov_b32 v%c3, %1"
                 :: "v"(lo), "v"(hi), "i"(A), "i"(B), "i"(C), "i"(D), "s"(0x05040100u), "s"(0x07060302u));
}
__device__ __forceinline__ void dma16(const void* sbase, unsigned voff, unsigned lds_dst) {
    unsigned keep;
    asm volatile("s_mov_b32 %0, m0\n\ts_mov_b32 m0, %3\n\ts_nop 0\n\tglobal_load_lds_dwordx4 %1, %2 nt\n\ts_mov_b32 m0, %0"
                 : "=&s"(keep) : "v"(voff), "s"(sbase), "s"(lds_dst) : "memory");
}
__device__ __forceinline__ void dma16p(const void* sbase, unsigned voff, unsigned lds_dst) {
    unsigned keep;
    asm volatile("s_mov_b32 %0, m0\n\ts_mov_b32 m0, %3\n\ts_nop 0\n\tglobal_load_lds_dwordx4 %1, %2\n\ts_mov_b32 m0, %0"
                 : "=&s"(keep) : "v"(voff), "s"(sbase), "s"(lds_dst) : "memory");
}
__device__ __forceinline__ unsigned um4(u32x4 a) { return max(max(a[0], a[1]), max(a[2], a[3])); }
__device__ __forceinline__ bool all_set(unsigned t) { return __builtin_amdgcn_ballot_w64(t == 0u) == 0ull; }
__device__ __forceinline__ bool any_empty(unsigned mx) { return __builtin_amdgcn_ballot_w64(mx == ~0u) != 0ull; }
__device__ __forceinline__ unsigned long long any_nan(float a, float b) { return __builtin_amdgcn_ballot_w64(__builtin_isunordered(a, b)); }
__device__ __forceinline__ f32x4 asf(u32x4 a) { return __builtin_bit_cast(f32x4, a); }
__device__ __forceinline__ u32x4 asu(f32x4 a) { return __builtin_bit_cast(u32x4, a); }
#define LANES(m) __builtin_amdgcn_inverse_ballot_w64(m)

__device__ __forceinline__ bf16x4 tr64(unsigned lds_addr) { return __builtin_amdgcn_ds_read_tr16_b64_v4bf16((LAS bf16x4*)(size_t)lds_addr); }
__device__ __forceinline__ bf16x8 cat8(bf16x4 a, bf16x4 b) { return __builtin_shufflevector(a, b, 0, 1, 2, 3, 4, 5, 6, 7); }
__device__ __forceinline__ bf16x8 lds16(unsigned a) { return *(const LAS bf16x8*)(size_t)a; }
__device__ __forceinline__ bf16x4 lds8(unsigned a) { return *(const LAS bf16x4*)(size_t)a; }
__device__ __forceinline__ f32x4 mfma32(bf16x8 a, bf16x8 b, f32x4 c) { return __builtin_amdgcn_mfma_f32_16x16x32_bf16(a, b, c, 0, 0, 0); }
__device__ __forceinline__ f32x4 mfma32u(u32x4 a, u32x4 b, f32x4 c) { return mfma32(__builtin_bit_cast(bf16x8, a), __builtin_bit_cast(bf16x8, b), c); }
__device__ __forceinline__ f32x4 mfma16(bf16x4 a, bf16x4 b, f32x4 c) {
    return __builtin_amdgcn_mfma_f32_16x16x16bf16_1k(__builtin_bit_cast(s16x4, a), __builtin_bit_cast(s16x4, b), c, 0, 0, 0);
}

#define STAMP(i) do {} while (0)

__device__ __forceinline__ void wait_word(const void* sbase, unsigned off) {
    unsigned t, cnt = kSpinCap;
    asm volatile("1:\n\tglobal_load_dword %0, %2, %3 sc1\n\ts_sub_u32 %1, %1, 1\n\ts_waitcnt vmcnt(0)\n\tv_cmp_eq_u32 vcc, -1, %0\n\ts_cbranch_vccz 2f\n\ts_cbranch_scc0 1b\n2:"
                 : "=&v"(t), "+s"(cnt) : "v"(off), "s"(sbase) : "memory", "vcc", "scc");
}
template <int OFF = 0> __device__ __forceinline__ void st16m(void* sbase, unsigned voff, u32x4 v, unsigned long long mask) {
    asm volatile("s_mov_b64 exec, %4\n\tglobal_store_dwordx4 %0, %1, %2 offset:%3 sc1\n\ts_mov_b64 exec, -1\n\ts_nop 0" :: "v"(voff), "v"(v), "s"(sbase), "i"(OFF), "s"(mask) : "memory");
}
__device__ __forceinline__ void st8m(void* sbase, unsigned voff, u32x2 v, unsigned long long mask) {
    asm volatile("s_mov_b64 exec, %3\n\tglobal_store_dwordx2 %0, %1, %2 sc1\n\ts_mov_b64 exec, -1" :: "v"(voff), "v"(v), "s"(sbase), "s"(mask) : "memory");
}
template <int OFF> __device__ __forceinline__ void st16m2(void* sbase, unsigned voff, u32x4 v, unsigned voff2, u32x4 v2, unsigned long long mask) {
    asm volatile("s_mov_b64 exec, %6\n\tglobal_store_dwordx4 %0, %1, %2 sc1\n\tglobal_store_dwordx4 %4, %5, %2 offset:%3 sc1\n\ts_mov_b64 exec, -1\n\ts_nop 0"
                 :: "v"(voff), "v"(v), "s"(sbase), "i"(OFF), "v"(voff2), "v"(v2), "s"(mask) : "memory");
}
#define PINS(a) asm volatile("" : "+s"(a))
#define RETRY(LOAD, EMPTY, COMPUTE, NAN_, X) do { LOAD; COMPUTE; if (__builtin_expect((NAN_) != 0ull, 0)) { \
    _Pragma("unroll 1") for (unsigned spins_ = 0; spins_ < kSpinCap; ++spins_) { slip(X); LOAD; if (!any_empty(EMPTY)) break; } COMPUTE; } } while (0)
__device__ __forceinline__ unsigned now() { return (unsigned)__builtin_amdgcn_s_memrealtime(); }
__device__ __forceinline__ void until(unsigned t) {
    asm volatile("1:\n\ts_memrealtime vcc\n\ts_waitcnt lgkmcnt(0)\n\ts_sub_u32 vcc_lo, %0, vcc_lo\n\ts_cmp_lt_u32 vcc_lo, 0x400\n\ts_cbranch_scc1 1b" :: "s"(t - 1u) : "vcc", "scc", "memory");
}
__device__ __forceinline__ unsigned lds_word(unsigned a) {
    unsigned t_; asm volatile("ds_read_b32 %0, %1\n\ts_waitcnt lgkmcnt(0)\n\ts_nop 0" : "=v"(t_) : "v"(a) : "memory");
    return __builtin_amdgcn_readfirstlane(t_);
}
__device__ __forceinline__ void lds_set(unsigned a, unsigned v) { asm volatile("ds_write_b32 %0, %1" :: "v"(a), "v"(v) : "memory"); }
__device__ __forceinline__ int swz(int r) { return r ^ (((r >> 3) & 1) << 2); }
__device__ __forceinline__ f32x2 unpk(unsigned w) { return (f32x2){lo16(w), hi16(w)}; }
__device__ __forceinline__ unsigned pk2v(f32x2 v) { return __builtin_bit_cast(unsigned, __builtin_convertvector(v, bf16x2)); }
__device__ __forceinline__ float frcp(float x) { return __builtin_amdgcn_rcpf(x); }
__device__ __forceinline__ float ex2(float x) { return __builtin_amdgcn_exp2f(x); }
__device__ __forceinline__ int lane_id() { int l; asm volatile("v_mbcnt_lo_u32_b32 %0, -1, 0\n\tv_mbcnt_hi_u32_b32 %0, -1, %0" : "=v"(l)); return l & 63; }
__device__ __forceinline__ f32x2 ln_stats(float ss, float s) {
    const float mean = s * (1.f / DM);
    const float var = fmax2(__builtin_fmaf(-mean, mean, ss * (1.f / DM)), 0.f);
    const unsigned mv = pk2(mean, var);
    return (f32x2){lo16(mv), frcp(bfr(__builtin_amdgcn_sqrtf(bfr(hi16(mv) + 1e-5f))))};
}
#define GATHER8(a, o, S) do { a[0] = ld16_sc1<-4 * (S)>(ws, o); a[1] = ld16_sc1<-3 * (S)>(ws, o); a[2] = ld16_sc1<-2 * (S)>(ws, o); a[3] = ld16_sc1<-(S)>(ws, o); \
    a[4] = ld16_sc1<0>(ws, o); a[5] = ld16_sc1<(S)>(ws, o); a[6] = ld16_sc1<2 * (S)>(ws, o); a[7] = ld16_sc1<3 * (S)>(ws, o); } while (0)
template <int OFF> __device__ __forceinline__ void again16(u32x4& r, const void* sbase, unsigned voff) {
    asm volatile("v_cmp_eq_u32 vcc, -1, %1\n\ts_mov_b64 exec, vcc\n\tglobal_load_dwordx4 %0, %2, %3 offset:%4 sc1\n\ts_mov_b64 exec, -1" : "+v"(r) : "v"(um4(r)), "v"(voff), "s"(sbase), "i"(OFF) : "memory", "vcc");
}
#define AGAIN8(a, o, S) do { again16<-4 * (S)>(a[0], ws, o); again16<-3 * (S)>(a[1], ws, o); again16<-2 * (S)>(a[2], ws, o); again16<-(S)>(a[3], ws, o); \
    again16<0>(a[4], ws, o); again16<(S)>(a[5], ws, o); again16<2 * (S)>(a[6], ws, o); again16<3 * (S)>(a[7], ws, o); } while (0)
#define WAIT8(a, N) asm volatile("s_waitcnt vmcnt(%8)" : "+v"((a)[0]), "+v"((a)[1]), "+v"((a)[2]), "+v"((a)[3]), "+v"((a)[4]), "+v"((a)[5]), "+v"((a)[6]), "+v"((a)[7]) : "i"(N) : "memory")
#define UM32(a) max(max(max(um4(a[0]), um4(a[1])), max(um4(a[2]), um4(a[3]))), max(max(um4(a[4]), um4(a[5])), max(um4(a[6]), um4(a[7]))))
#define SUM8(a) (((((((asf(a[0]) + asf(a[1])) + asf(a[2])) + asf(a[3])) + asf(a[4])) + asf(a[5])) + asf(a[6])) + asf(a[7]))
#define WAIT1(x, N) asm volatile("s_waitcnt vmcnt(%1)" : "+v"(x) : "i"(N) : "memory")
#define LO2(x) __builtin_shufflevector(x, x, 0, 1)
#define HI2(x) __builtin_shufflevector(x, x, 2, 3)
#define ACC8(a, s) do { f32x2 l_, h_; \
    asm volatile("s_waitcnt vmcnt(5)\n\tv_pk_add_f32 %0, %2, %4\n\tv_pk_add_f32 %1, %3, %5\n\tv_pk_add_f32 %0, %0, %6\n\tv_pk_add_f32 %1, %1, %7\n\t" \
                 "s_waitcnt vmcnt(3)\n\tv_pk_add_f32 %0, %0, %8\n\tv_pk_add_f32 %1, %1, %9\n\tv_pk_add_f32 %0, %0, %10\n\tv_pk_add_f32 %1, %1, %11\n\t" \
                 "s_waitcnt vmcnt(1)\n\tv_pk_add_f32 %0, %0, %12\n\tv_pk_add_f32 %1, %1, %13\n\tv_pk_add_f32 %0, %0, %14\n\tv_pk_add_f32 %1, %1, %15\n\t" \
                 "s_waitcnt vmcnt(0)\n\tv_pk_add_f32 %0, %0, %16\n\tv_pk_add_f32 %1, %1, %17\n\ts_nop 0" \
                 : "=&v"(l_), "=&v"(h_) : "v"(LO2(a[0])), "v"(HI2(a[0])), "v"(LO2(a[1])), "v"(HI2(a[1])), "v"(LO2(a[2])), "v"(HI2(a[2])), "v"(LO2(a[3])), "v"(HI2(a[3])), \
                   "v"(LO2(a[4])), "v"(HI2(a[4])), "v"(LO2(a[5])), "v"(HI2(a[5])), "v"(LO2(a[6])), "v"(HI2(a[6])), "v"(LO2(a[7])), "v"(HI2(a[7])) : "memory"); \
    s = __builtin_shufflevector(l_, h_, 0, 1, 2, 3); } while (0)
#define RETRY8(a, o, S, s, MID, POST, NAN_, X) do { GATHER8(a, o, S); MID; ACC8(a, s); POST; if (__builtin_expect((NAN_) != 0ull, 0)) { \
    _Pragma("unroll 1") for (unsigned spins_ = 0; spins_ < kSpinCap; ++spins_) { slip(X); AGAIN8(a, o, S); WAIT8(a, 0); if (!any_empty(UM32(a))) break; } s = SUM8(a); POST; } } while (0)
#define PIN1(a) asm volatile("" : "+v"(a))
#define PIN2(a, b) asm volatile("" : "+v"(a), "+v"(b))
#define PIN3(a, b, c) asm volatile("" : "+v"(a), "+v"(b), "+v"(c))
#define LANE_VARS() const int lane = lane_id(); \
    const int kq = lane >> 4, n = lane & 15, q4 = n >> 2, p4 = lane & 3; \
    const int r0s = 8 * kq + q4 + 4 * (kq & 1), r1s = 8 * kq + q4 + 4 * ((kq & 1) ^ 1);     \
    const int r2s = swz(4 * kq + q4);                                                       \
    (void)r0s; (void)r1s; (void)r2s; (void)p4
template <int V> struct IC { static constexpr int value = V; };
struct Frag80 { bf16x8 a0, a1; bf16x4 a2; };

__global__ void __launch_bounds__(512, 2) __attribute__((amdgpu_num_vgpr(32)))
mega_kernel(const u16* __restrict__ x0, const u16* __restrict__ kc, const u16* __restrict__ vc, const u16* __restrict__ wq,
            const u16* __restrict__ wo, const u16* __restrict__ wr, const u16* __restrict__ wg, const u16* __restrict__ wu,
            const u16* __restrict__ wd, u16* __restrict__ out, unsigned char* __restrict__ ws_in
            ) {
    extern __shared__ __attribute__((aligned(16))) unsigned char smem[];
    const unsigned lb = (unsigned)(size_t)smem;
    unsigned char* const ws = (unsigned char*)(((uintptr_t)ws_in + (32u << 20) - 1) & ~(uintptr_t)((16u << 20) - 1));
    const int wave = __builtin_amdgcn_readfirstlane(threadIdx.x >> 6);
    const int g = blockIdx.x, gj = GJ(g), gi = GI(g);
    const f32x4 z4 = {0.f, 0.f, 0.f, 0.f};
    asm volatile("" ::: "v255");
    auto sched = [&](unsigned X, unsigned gap) {
        unsigned v; unsigned long long c;
        asm volatile("s_memrealtime %1\n\tds_read_b32 %0, %2\n\ts_waitcnt lgkmcnt(0)" : "=v"(v), "=&s"(c) : "v"(lb + L_CNT + 56) : "memory");
        const unsigned n = (unsigned)c + gap;
        unsigned t = __builtin_amdgcn_readfirstlane(v) + X;
        if ((int)(n - t) > 0) t = n;
        until(t);
    };
    auto slip = [&](unsigned X) {
        if (X) lds_set(lb + L_CNT + 56, now() - X);
    };
    float xe = 0.f;

    auto code_pf = [&](int slice, int lane) {
        unsigned long long pc0; unsigned len, back, t;
        asm volatile("s_getpc_b64 %0\n.Lmega_pf%=:\n\ts_mov_b32 %1, .Lmega_code_end-.Lmega_code_begin\n\ts_mov_b32 %2, .Lmega_pf%=-.Lmega_code_begin" : "=s"(pc0), "=s"(len), "=s"(back));
        unsigned off = (unsigned)(slice * 4096 + lane * 64); if (off > len) off = len;
        asm volatile("global_load_dword %0, %1, %2" : "=v"(t) : "v"(off & ~3u), "s"(pc0 - back) : "memory");
        return t;
    };
    asm volatile(".Lmega_code_begin:");
    auto fix_k1 = [&](auto IC_, auto NC) {
        constexpr int A = RK + 8 * decltype(IC_)::value, B = A + 4;
        unsigned t0, t1, t2, t3;
        asm volatile("s_waitcnt vmcnt(%12)\n\tv_mov_b32 %0, v%c4\n\tv_mov_b32 %1, v%c5\n\tv_mov_b32 %2, v%c6\n\tv_mov_b32 %3, v%c7\n\t"
                     "v_mov_b32_dpp v%c4, v%c8 row_ror:8 row_mask:0xf bank_mask:0xc\n\tv_mov_b32_dpp v%c5, v%c9 row_ror:8 row_mask:0xf bank_mask:0xc\n\t"
                     "v_mov_b32_dpp v%c6, v%c10 row_ror:8 row_mask:0xf bank_mask:0xc\n\tv_mov_b32_dpp v%c7, v%c11 row_ror:8 row_mask:0xf bank_mask:0xc\n\t"
                     "v_mov_b32_dpp v%c8, %0 row_ror:8 row_mask:0xf bank_mask:0x3\n\tv_mov_b32_dpp v%c9, %1 row_ror:8 row_mask:0xf bank_mask:0x3\n\t"
                     "v_mov_b32_dpp v%c10, %2 row_ror:8 row_mask:0xf bank_mask:0x3\n\tv_mov_b32_dpp v%c11, %3 row_ror:8 row_mask:0xf bank_mask:0x3"
                     : "=&v"(t0), "=&v"(t1), "=&v"(t2), "=&v"(t3)
                     : "i"(A), "i"(A + 1), "i"(A + 2), "i"(A + 3), "i"(B), "i"(B + 1), "i"(B + 2), "i"(B + 3), "i"(decltype(NC)::value) : "memory");
    };
    auto trans_v = [&](auto UC, auto NC) {
        constexpr int R = RV + 4 * decltype(UC)::value, N = decltype(NC)::value;
#define CELL(r, c) R + 16 * (r) + (c), R + 16 * (r) + 8 + (c)
        unsigned a0, a1, b0, b1, c0, c1;
        VMW(N + 6); zip_in<CELL(0, 0)>(); zip_out<CELL(0, 1)>(a0, a1); zip_out<CELL(0, 2)>(b0, b1); zip_out<CELL(0, 3)>(c0, c1);
        VMW(N + 4); zip_x<CELL(1, 0), CELL(0, 1)>(a0, a1); zip_in<CELL(1, 1)>(); zip_out<CELL(1, 2)>(a0, a1);
        VMW(N + 2); zip_x<CELL(2, 0), CELL(0, 2)>(b0, b1); zip_x<CELL(2, 1), CELL(1, 2)>(a0, a1); zip_in<CELL(2, 2)>(); zip_out<CELL(1, 3)>(a0, a1); zip_out<CELL(2, 3)>(b0, b1);
        VMW(N); zip_x<CELL(3, 0), CELL(0, 3)>(c0, c1); zip_x<CELL(3, 1), CELL(1, 3)>(a0, a1); zip_x<CELL(3, 2), CELL(2, 3)>(b0, b1); zip_in<CELL(3, 3)>();
#undef CELL
    };
    auto trans_w = [&](auto UC, auto NC) {
        constexpr int R = RV + 64 + 2 * decltype(UC)::value, N = decltype(NC)::value;
        unsigned a0, a1;
        VMW(N + 2); zip_in<R, R + 8>(); zip_out<R + 1, R + 9>(a0, a1);
        VMW(N); zip_x<R + 16, R + 24, R + 1, R + 9>(a0, a1); zip_in<R + 17, R + 25>();
    };
    auto frag80 = [&](unsigned im, int rs, int r0s, int r1s, int r2s) -> Frag80 {
        return Frag80{cat8(tr64(im + r0s * rs), tr64(im + r1s * rs)), cat8(tr64(im + (r0s + 32) * rs), tr64(im + (r1s + 32) * rs)), tr64(im + (r2s + 64) * rs)};
    };
    auto mm80 = [&](const Frag80& a, const Frag80& b) -> f32x4 {
        return mfma32(a.a1, b.a1, mfma32(a.a0, b.a0, z4)) + mfma16(a.a2, b.a2, z4);
    };
    auto rows80 = [&](unsigned a16, unsigned a8) -> Frag80 { const Frag80 f{lds16(a16), lds16(a16 + 64), lds8(a8)}; __builtin_amdgcn_sched_barrier(0); return f; };
    auto empty35 = [&](int layer, unsigned S, int lane) {
        st16_sc1(ws, tile35(gj, 10 * layer + (lane >> 3)) + S + (unsigned)(gi * 128 + (lane & 7) * 16), (u32x4){~0u, ~0u, ~0u, ~0u});
        st16m(ws, S + (lane < 16 ? tile35(gj, 10 * layer + 8 + (lane >> 3)) + (unsigned)(gi * 128 + (lane & 7) * 16) : WS_OWN + (unsigned)(gi * 16384 + layer * 4096 + gj * 512 + (lane - 16) * 16)),
              (u32x4){~0u, ~0u, ~0u, ~0u}, 0xffffffffffffull);
    };
    auto empty5 = [&](int q0, unsigned S, int lane) {
        st16m(ws, tile35(gj, q0 + (lane >> 3)) + S + (unsigned)(gi * 128 + (lane & 7) * 16), (u32x4){~0u, ~0u, ~0u, ~0u}, 0xffffffffffull);
    };
    auto empty4 = [&](int layer, unsigned S, int lane) {
        const unsigned b = WS_H4 + (unsigned)((layer * 32 + gi) * 32768) + (S ^ 8192u);
        st16_sc1(ws, b + (unsigned)((lane >> 5) * 4096 + gj * 512 + (lane & 31) * 16), (u32x4){~0u, ~0u, ~0u, ~0u});
        st16m(ws, b + (unsigned)(lane < 32 ? 16384 + gj * 512 + lane * 16 : H4C + ((lane - 32) >> 3) * 1024 + gj * 128 + (lane & 7) * 16), (u32x4){~0u, ~0u, ~0u, ~0u},
              layer ? 0xff000000ffffffffull : ~0ull);
    };
    const unsigned tst = now();
    int lead = wave == 7 ? LEAD * PACE * 4 : 0;
    auto pace = [&](unsigned n, bool wait = true) {
        unsigned u, w, t0;
        asm volatile("s_mov_b64 exec, 1\n\tds_add_rtn_u32 %0, %3, %4\n\tds_max_u32 %3, %5 offset:12\n\tds_add_rtn_u32 %1, %3, %6 offset:12\n\tds_read_b32 %2, %3 offset:4\n\ts_mov_b64 exec, -1\n\ts_waitcnt lgkmcnt(0)"
                     : "=&v"(u), "=&v"(w), "=&v"(t0) : "v"(lb + L_CNT + 60), "v"(n), "v"((now() - tst) << 6), "v"(n * PMIN * 4u) : "memory");
        int tn = (int)(__builtin_amdgcn_readfirstlane(t0) - tst + PINIT) * 64 + __builtin_amdgcn_readfirstlane(u) * (PACE * 4);
        tn += max((int)__builtin_amdgcn_readfirstlane(u) - UG, 0) * (SLOW * 4);
        const int tw = __builtin_amdgcn_readfirstlane(w) - PBURST * 64;
        if (tw > tn) tn = tw;
        tn -= lead;
        if (wait) {
#pragma unroll 1
            for (int k = 0; k < 2048 && (int)((now() - tst) << 6) < tn; ++k) {}
        }
    };
    auto slot = [&](unsigned u) {
        const unsigned tn = lds_word(lb + L_CNT + 64) + PINIT + (u * PACE + (NUNIT - UG) * SLOW) / 16;
#pragma unroll 1
        for (int k = 0; k < 8192 && (int)(now() - tn) < 0; ++k) __builtin_amdgcn_s_sleep(1);
    };
    auto load_wo = [&](int k, int lane, bool early) {
        const int c = k * 64 + lane, pp = c / 10, cc = c - 10 * pp;
        const unsigned f = (unsigned)(swz(pp) * 1280 + cc * 16);
        const u16* wb = wo + (size_t)gi * DH * DM + gj * 80;
#pragma unroll
        for (int a2 = 0; a2 < 6; ++a2) { if (a2 % 3 == 0) { if (early) pace(3); else slot(NUNIT - WOFF + (2 * k + a2 / 3) * WOC); } dma16(wb + 32 * a2 * DM, f, lb + L_WO + (5 * a2 + k) * 1024); }
    };
    auto load_wd = [&](int lane) {
        const u16* db = wd + gi * 48 * DM + gj * 80;
#pragma unroll 1
        for (int k = 0; k < 5; ++k) {
            const int c = k * 64 + lane, pp = c / 10, cc = c - 10 * pp;
            const unsigned fd = (unsigned)(swz(pp) * 1280 + cc * 16);
            if (!(k & 1)) pace(3);
            dma16(db, fd, lb + L_WD + k * 1024);
            if (k < 3) dma16(db + 32 * DM, (k < 2 || lane < 32) ? fd : 0u, lb + L_WD + (5 + k) * 1024);
        }
    };
    auto load_gu = [&](int m, int lane) {
#pragma unroll
        for (int k = 0; k < 3; ++k) {
            const int c = k * 64 + lane, row = c / 6, cc = c - 6 * row;
            const unsigned o = (unsigned)((swz(row) * FF + cc * 8) * 2), d = lb + L_GU + m * 8192 + k * 1024;
            const u16* wb = (m == 0 ? wg : wu) + gj * 80 * FF + gi * 48;
            pace(3);
            dma16(wb, o, d); dma16(wb + 32 * FF, o, d + 3072);
            if (k < 2) dma16(wb + 64 * FF, (k == 0 || lane < 32) ? o : 0u, d + 6144);
        }
    };
    auto lds_add = [&](int off, unsigned v) { asm volatile("s_mov_b64 exec, 1\n\tds_add_u32 %0, %1\n\ts_mov_b64 exec, -1" :: "v"(lb + L_CNT + off), "v"(v) : "memory"); };
    auto send_flag = [&](int lane) {
        st4_sc1(ws, WS_MB + (unsigned)((gi * 8 + (lane & 7)) * 128 + gj * 4), lds_word(lb + L_CNT + 68));
    };
    auto tiles4 = [&](const Frag80 (&f)[4], const Frag80& b, unsigned long long mk) __attribute__((always_inline)) -> u32x4 {
        f32x4 c[4], d[4];
#pragma unroll
        for (int i = 0; i < 4; ++i) c[i] = mfma32(f[i].a0, b.a0, z4);
#pragma unroll
        for (int i = 0; i < 4; ++i) { c[i] = mfma32(f[i].a1, b.a1, c[i]); d[i] = mfma16(f[i].a2, b.a2, z4); }
        __builtin_amdgcn_sched_barrier(0);
        f32x2 lo, hi;
        asm volatile("s_nop 1\n\ts_mov_b64 exec, %18\n\tv_pk_add_f32 %0, %2, %10\n\tv_pk_add_f32 %1, %3, %11\n\t"
                     "s_lshl_b64 exec, exec, 4\n\tv_pk_add_f32 %0, %4, %12\n\tv_pk_add_f32 %1, %5, %13\n\t"
                     "s_lshl_b64 exec, exec, 4\n\tv_pk_add_f32 %0, %6, %14\n\tv_pk_add_f32 %1, %7, %15\n\t"
                     "s_lshl_b64 exec, exec, 4\n\tv_pk_add_f32 %0, %8, %16\n\tv_pk_add_f32 %1, %9, %17\n\ts_mov_b64 exec, -1"
                     : "=&v"(lo), "=&v"(hi) : "v"(LO2(c[0])), "v"(HI2(c[0])), "v"(LO2(c[1])), "v"(HI2(c[1])), "v"(LO2(c[2])), "v"(HI2(c[2])), "v"(LO2(c[3])), "v"(HI2(c[3])),
                       "v"(LO2(d[0])), "v"(HI2(d[0])), "v"(LO2(d[1])), "v"(HI2(d[1])), "v"(LO2(d[2])), "v"(HI2(d[2])), "v"(LO2(d[3])), "v"(HI2(d[3])), "s"(mk) : "scc");
        return asu(__builtin_shufflevector(lo, hi, 0, 1, 2, 3));
    };
    auto empty1 = [&](int half, unsigned S, int lane) {
        const unsigned o = WS_H1 + S + (unsigned)((gi * 4 + 2 * half) * 16384 + gj * 896 + min(lane, 55) * 16);
        st16_sc1(ws, o, (u32x4){~0u, ~0u, ~0u, ~0u}); st16_sc1(ws, o + 16384, (u32x4){~0u, ~0u, ~0u, ~0u});
    };
    auto report = [&]() { if (gj == (gi & 7)) st4_sc1(ws, WS_R + (unsigned)(gi * 4), lds_word(lb + L_CNT + 88)); };
    auto answer = [&](int lane, unsigned r) {
        int d = min(max((int)(r - tst), -400), 400) * 512 + (int)((r >> 1) & 15u);
        d += (int)dppu<0xB1>((unsigned)d); d += (int)dppu<0x4E>((unsigned)d); d += (int)dppu<0x141>((unsigned)d); d += (int)dppu<0x140>((unsigned)d);
        d = __builtin_amdgcn_readlane(d, 0) + __builtin_amdgcn_readlane(d, 16);
        st4_sc1(ws, WS_MB + (unsigned)(((lane & 31) * 8 + (lane & 7)) * 128 + 32), ((tst + (d >> 14) + TOFF) & ~3u) | ((d & 511) > 128 ? 3u : 1u));
        lds_set(lb + L_CNT + 36, 1u);
    };
    auto adopt = [&](unsigned t) {
        if (ADOPT && t - tst + 256u < 512u) lds_set(lb + L_CNT + 64, t);
        lds_set(lb + L_CNT + 80, lds_word(lb + L_CNT + 92) ^ (t & 2u) << 12);
        lds_set(lb + L_CNT + 84, (((t << 1) ^ lds_word(lb + L_CNT + 68)) & 4u) << 11);
        lds_set(lb + L_CNT + 52, 1u);
    };
    struct StatAddr { unsigned a16, b16, a8, b8, dst; };
    auto stats_addr = [&](int lane, unsigned dbase, int dstride) -> StatAddr {
        const int kq = lane >> 4, q4 = (lane >> 2) & 3, p4 = lane & 3;
        const int x1 = LANES(0x4444444444444444ull) ? L_WR + gj * 160 : L_XS + q4 * 160, xa = LANES(0xaaaaaaaaaaaaaaaaull) ? L_XS + 640 : x1;
        StatAddr r{lb + (unsigned)(xa + kq * 16), lb + (unsigned)(L_XS + p4 * 160 + kq * 16), lb + (unsigned)(xa + 128 + kq * 8), lb + (unsigned)(L_XS + p4 * 160 + 128 + kq * 8), dbase + (unsigned)(kq * dstride)};
        PIN3(r.a16, r.b16, r.dst); PIN2(r.a8, r.b8);
        return r;
    };
    auto stats = [&](const StatAddr& r) {
        const bf16x8 a0 = lds16(r.a16), b0 = lds16(r.b16), a1 = lds16(r.a16 + 64), b1 = lds16(r.b16 + 64);
        const bf16x4 a2 = lds8(r.a8), b2 = lds8(r.b8);
        unsigned long long mk = 0x0008000400020001ull; PINS(mk);
        __builtin_amdgcn_sched_barrier(0);
        st16m(ws, r.dst, asu(mfma32(a1, b1, mfma32(a0, b0, z4)) + mfma16(a2, b2, z4)), mk);
    };
    unsigned qa, pw, ma, idx, qm;
    auto qk_addr = [&](bool L1) {
        LANE_VARS();
        qa = lb + L_Q + (unsigned)(kq * 32 + (lane & 2) * 8);
        pw = lb + L_P + (unsigned)((7 + 8 * wave) * 192 + (kq >> 1) * 64 + (n >> 3) * 32 + (lane & 2) * 8 + (q4 & 1) * 8 + (kq & 1) * 4 + (lane & 1) * 2);
        ma = lb + L_M + (unsigned)(wave * 4);
        idx = (lane & 2) ? 0xeeeeu : 0x4444u;
        qm = lb + L_Q;
        PIN3(qa, pw, ma); PIN2(idx, qm);
        if (L1 && wave < 4) __builtin_amdgcn_s_setprio(2);
    };
    auto att_qk0 = [&]() {
        qk_addr(false);
#pragma unroll 1
        for (unsigned spins = 0; spins < (kSpinCap << 4); ++spins) {
            if (lds_word(lb + L_CNT + 4) != 0u) break;
            __builtin_amdgcn_s_sleep(1);
        }
        STAMP(3);
        const u32x4 q0 = *(const LAS u32x4*)(size_t)qa, q1 = *(const LAS u32x4*)(size_t)(qa + 128), q2 = *(const LAS u32x4*)(size_t)(qa + 256);
        f32x4 c0 = z4, c1 = z4, c2 = z4, c3 = z4;
#define SMF(c, q, r) "v_smfmac_f32_16x16x64_bf16 %" #c ", %" #q ", v[" #r "], %7\n\t"
        asm volatile("s_nop 1\n\t" SMF(0, 4, 64:71) SMF(1, 4, 88:95) SMF(2, 4, 112:119) SMF(3, 4, 136:143) SMF(0, 5, 72:79) SMF(1, 5, 96:103) SMF(2, 5, 120:127) SMF(3, 5, 144:151)
                     SMF(0, 6, 80:87) SMF(1, 6, 104:111) SMF(2, 6, 128:135) SMF(3, 6, 152:159) "s_nop 7"
                     : "+v"(c0), "+v"(c1), "+v"(c2), "+v"(c3) : "v"(q0), "v"(q1), "v"(q2), "v"(idx));
#undef SMF
        const float s0 = c0[0] + c0[2], s1 = c1[0] + c1[2], s2 = c2[0] + c2[2], s3 = c3[0] + c3[2];
        float m = fmax2(fmax3(s0, s1, s2), s3);
        m = fmax2(m, dpp<0xB1>(m)); m = fmax2(m, dpp<0x4E>(m)); m = fmax2(m, dpp<0x124>(m)); m = fmax2(m, dpp<0x128>(m));
        const float s01 = LANES(0xffff0000ffff0000ull) ? s1 : s0, s23 = LANES(0xffff0000ffff0000ull) ? s3 : s2;
        const float sv = LANES(0xffffffff00000000ull) ? s23 : s01;
        m = bfr(bfr(m) * SCALE);
        const float p = ex2((bfr(bfr(sv) * SCALE) - m) * LOG2E);
        *(LAS u16*)(size_t)pw = (u16)pk2(p, p);
        *(LAS float*)(size_t)ma = m;
        __builtin_amdgcn_sched_barrier(0);
    };
    unsigned pr, oa, oc, cma, cxa, cra, cso;
    auto att_addr = [&]() {
        LANE_VARS();
        pr = lb + L_P + (unsigned)((7 + 8 * wave - 2 * q4 - (lane & 1)) * 192 + kq * 32 + (lane & 2) * 8);
        oa = lb + L_OP + (unsigned)(wave * 1024 + (lane & 7) * 64 + kq * 16 + (n >> 3) * 8);
        oc = lb + L_OP + (unsigned)(wave * 1024 + 512 + (kq >> 1) * 256 + (lane & 7) * 32 + (kq & 1) * 16 + (n >> 3) * 8);
        PIN3(pr, oa, oc);
    };
    auto comb_out = [&](float u0, float u1, unsigned so, unsigned long long mk, bool both) __attribute__((always_inline)) {
        const unsigned w = min(pk2(u0, u1), 0xfffffffeu);
        const u32x4 w4 = {both ? dppu<0x00>(w) : w, dppu<0x55>(w), dppu<0xAA>(w), dppu<0xFF>(w)};
        st16m(ws, so, w4, mk);
    };
    auto max8 = [&](f32x4 m0, f32x4 m1) __attribute__((always_inline)) { return fmax3(fmax3(m0[0], m0[1], m0[2]), fmax3(m0[3], m1[0], m1[1]), fmax2(m1[2], m1[3])) * LOG2E; };
    auto comb_o0 = [&](f32x4 m0, f32x4 m1, f32x4 mm, f32x4 o0, f32x4 o1, f32x4 o2, f32x4 o3, unsigned so, unsigned long long mk, bool both) __attribute__((always_inline)) {
        const float M = max8(m0, m1);
        f32x4 f;
#pragma unroll
        for (int k = 0; k < 4; ++k) f[k] = ex2(__builtin_fmaf(mm[k], LOG2E, -M));
        const f32x4 o = f[3] * o3 + (f[2] * o2 + (f[1] * o1 + f[0] * o0));
        comb_out(rs32(o[0], o[2]), rs32(o[1], o[3]), so, mk, both);
    };
    auto comb_o1 = [&](f32x4 m0, f32x4 m1, f32x2 mm, f32x4 o0, f32x4 o1, unsigned so, unsigned long long mk, bool both) __attribute__((always_inline)) {
        const float M = max8(m0, m1);
        const f32x4 o = ex2(__builtin_fmaf(mm[1], LOG2E, -M)) * o1 + ex2(__builtin_fmaf(mm[0], LOG2E, -M)) * o0;
        comb_out(x16_add(rs32(o[0], o[2])), x16_add(rs32(o[1], o[3])), so, mk, both);
    };
    auto ml_addr = [&](int lane, unsigned& xa, unsigned& ra) {
        xa = lb + L_M + (unsigned)(((lane & 15) >> 1) * 4); ra = lb + L_P + (unsigned)((7 + 8 * ((lane & 15) >> 1)) * 192 + (lane & 1) * 64 + (lane >> 4) * 16);
    };
    auto comb_ml = [&](unsigned ma, unsigned xa, unsigned ra, unsigned so, bool both) __attribute__((always_inline)) {
        const u32x4 x4 = *(const LAS u32x4*)(size_t)ra;
        const f32x4 m0 = *(const LAS f32x4*)(size_t)ma, m1 = *(const LAS f32x4*)(size_t)(ma + 16);
        const float mw = *(const LAS float*)(size_t)xa;
        __builtin_amdgcn_sched_barrier(0);
        const f32x4 sp = mfma32u((u32x4){0x3f803f80u, 0x3f803f80u, 0x3f803f80u, 0x3f803f80u}, x4, z4);
        __builtin_amdgcn_sched_barrier(0);
        const float M = max8(m0, m1);
        float L = row16_sum(sp[0] * ex2(__builtin_fmaf(mw, LOG2E, -M)));
        if (both) L = dpp<0x00>(L);
        st16m(ws, so, asu((f32x4){M, L, M, L}), both ? 3ull : 1ull);
    };
    auto att_pv0 = [&]() {
        u32x4 bm[8];
        trans_v(IC<0>{}, IC<24>{}); trans_v(IC<1>{}, IC<16>{});
#pragma unroll
        for (int e = 0; e < 8; ++e) bm[e] = *(const LAS u32x4*)(size_t)(pr + e * 192);
        __builtin_amdgcn_sched_barrier(0);
        f32x4 da = z4, dc = z4;
        PIN2(da, dc);
        asm volatile("s_nop 1");
        __builtin_amdgcn_sched_barrier(0);
        f32x2 r;
        smf<RV>(da, bm[0], idx); smf<RV + 8>(da, bm[1], idx); smf<RV + 16>(da, bm[2], idx); smf<RV + 24>(da, bm[3], idx);
        smf<RV + 32>(da, bm[4], idx); smf<RV + 40>(da, bm[5], idx); smf<RV + 48>(da, bm[6], idx); smf<RV + 56>(da, bm[7], idx);
        asm volatile("s_nop 7\n\tv_pk_add_f32 %0, %1, %2\n\tds_write_b64 %3, %0\n\ts_mov_b64 exec, 1\n\tds_add_u32 %4, %5\n\ts_mov_b64 exec, -1"
                     : "=&v"(r) : "v"(LO2(da)), "v"(HI2(da)), "v"(oa), "v"(lb + L_CNT + 24), "v"(1u) : "memory");
        trans_w(IC<0>{}, IC<12>{}); trans_w(IC<1>{}, IC<8>{}); trans_w(IC<2>{}, IC<4>{}); trans_w(IC<3>{}, IC<0>{});
        STAMP(4);
        asm volatile("s_nop 1\n\tv_smfmac_f32_16x16x64_bf16 %0, %1, v[224:231], %5\n\tv_smfmac_f32_16x16x64_bf16 %0, %2, v[232:239], %5\n\t"
                     "v_smfmac_f32_16x16x64_bf16 %0, %3, v[240:247], %5\n\tv_smfmac_f32_16x16x64_bf16 %0, %4, v[248:255], %5\n\ts_nop 6"
                     : "+v"(dc) : "v"(bm[0]), "v"(bm[1]), "v"(bm[2]), "v"(bm[3]), "v"(idx));
        asm volatile("s_nop 0\n\tv_pk_add_f32 %0, %1, %2\n\tds_write_b64 %3, %0" : "=&v"(r) : "v"(LO2(dc)), "v"(HI2(dc)), "v"(oc) : "memory");
        STAMP(20);
    };
    unsigned long long ck1 = 0xffff0000ffff0000ull, ck2 = 0xffffffff00000000ull, csc = 0x3d93cd3a3d93cd3aull;
    auto att1 = [&]() {
        constexpr int layer = 1;
        {
#define SMF(c, q, r) "v_smfmac_f32_16x16x64_bf16 v[" #c "], v[" #q "], v[" #r "], %0\n\t"
#define MAXD(x) "v_max_f32_dpp v41, v41, v41 " x " row_mask:0xf bank_mask:0xf\n\t"
            asm volatile("s_waitcnt lgkmcnt(0)\n\ts_barrier\n\t"
                         "ds_read_b128 v[0:3], %1\n\tds_read2_b32 v[12:13], %2 offset0:96 offset1:100\n\tds_read_b128 v[4:7], %1 offset:128\n\tds_read_b128 v[8:11], %1 offset:256\n\t"
                         "v_mov_b64 v[16:17], 0\n\tv_mov_b64 v[18:19], 0\n\tv_mov_b64 v[20:21], 0\n\tv_mov_b64 v[22:23], 0\n\tv_mov_b64 v[24:25], 0\n\tv_mov_b64 v[26:27], 0\n\t"
                         "v_mov_b64 v[28:29], 0\n\tv_mov_b64 v[30:31], 0\n\tv_mov_b64 v[32:33], 0\n\tv_mov_b64 v[34:35], 0\n\tv_mov_b64 v[36:37], 0\n\tv_mov_b64 v[38:39], 0\n\t"
                         "s_waitcnt lgkmcnt(3)\n\t" SMF(16:19, 0:3, 64:71) SMF(20:23, 0:3, 88:95) "s_waitcnt lgkmcnt(1)\n\tv_mul_f32 v13, 0x3acccccd, v13\n\t"
                         SMF(24:27, 0:3, 112:119) "v_mul_f32 v12, 0x3acccccd, v12\n\t" SMF(28:31, 0:3, 136:143) "v_fma_f32 v12, -v13, v13, v12\n\t"
                         SMF(16:19, 4:7, 72:79) "v_maximum3_f32 v12, v12, 0, 0\n\t" SMF(20:23, 4:7, 96:103) "v_cvt_pk_bf16_f32 v12, 0, v12\n\t"
                         SMF(24:27, 4:7, 120:127) "v_add_f32 v12, 0x3727c5ac, v12\n\t" SMF(28:31, 4:7, 144:151) "v_cvt_pk_bf16_f32 v12, 0, v12\n\ts_waitcnt lgkmcnt(0)\n\t"
                         SMF(16:19, 8:11, 80:87) "v_sqrt_f32 v12, v12\n\t" SMF(20:23, 8:11, 104:111) "v_cvt_pk_bf16_f32 v12, 0, v12\n\t"
                         SMF(24:27, 8:11, 128:135) "v_rcp_f32 v12, v12\n\t" SMF(28:31, 8:11, 152:159)
                         "s_nop 2\n\tv_add_f32 v42, v16, v18\n\ts_nop 0\n\tv_add_f32 v43, v20, v22\n\tv_max_f32 v41, v42, v43\n\tv_add_f32 v44, v24, v26\n\tv_add_f32 v45, v28, v30\n\t"
                         "v_max3_f32 v41, v41, v44, v45\n\tv_cndmask_b32 v42, v42, v43, %8\n\tv_cndmask_b32 v44, v44, v45, %8\n\t"
                         MAXD("quad_perm:[1,0,3,2]") "v_cndmask_b32 v40, v42, v44, %9\n\ts_nop 0\n\t" MAXD("quad_perm:[2,3,0,1]") "s_nop 1\n\t" MAXD("row_ror:4") "s_nop 1\n\t" MAXD("row_ror:8")
                         "v_pk_mul_f32 v[40:41], v[12:13], v[40:41] op_sel_hi:[0,1]\n\tv_cvt_pk_bf16_f32 v40, 0, v40\n\tv_cvt_pk_bf16_f32 v41, 0, v41\n\t"
                         "v_pk_mul_f32 v[40:41], %10, v[40:41]\n\tv_cvt_pk_bf16_f32 v40, 0, v40\n\tv_cvt_pk_bf16_f32 v41, 0, v41\n\t"
                         "v_sub_f32 v40, v40, v41\n\tv_mul_f32 v40, 0x3fb8aa3b, v40\n\tv_exp_f32 v40, v40\n\ts_nop 0\n\tv_cvt_pk_bf16_f32 v40, v40, v40\n\tds_write_b16 %3, v40\n\t"
                         "ds_read_b128 v[0:3], %4\n\tds_read_b128 v[4:7], %4 offset:192\n\tds_read_b128 v[8:11], %4 offset:384\n\tds_read_b128 v[12:15], %4 offset:576\n\t"
                         "ds_read_b128 v[16:19], %4 offset:768\n\tds_read_b128 v[20:23], %4 offset:960\n\tds_read_b128 v[24:27], %4 offset:1152\n\tds_read_b128 v[28:31], %4 offset:1344\n\t"
                         "ds_write_b32 %5, v41\n\t"
                         "s_waitcnt lgkmcnt(8)\n\t" SMF(32:35, 0:3, 160:167) SMF(36:39, 0:3, 224:231) "s_waitcnt lgkmcnt(7)\n\t" SMF(32:35, 4:7, 168:175) SMF(36:39, 4:7, 232:239)
                         "s_waitcnt lgkmcnt(6)\n\t" SMF(32:35, 8:11, 176:183) SMF(36:39, 8:11, 240:247) "s_waitcnt lgkmcnt(4)\n\t" SMF(32:35, 12:15, 184:191) SMF(36:39, 12:15, 248:255)
                         SMF(32:35, 16:19, 192:199) "s_waitcnt lgkmcnt(1)\n\t" SMF(32:35, 20:23, 200:207) SMF(32:35, 24:27, 208:215) SMF(32:35, 28:31, 216:223)
                         "s_nop 2\n\tv_pk_add_f32 v[36:37], v[36:37], v[38:39]\n\tds_write_b64 %7, v[36:37]\n\ts_nop 2\n\tv_pk_add_f32 v[32:33], v[32:33], v[34:35]\n\tds_write_b64 %6, v[32:33]"
                         :: "v"(idx), "v"(qa), "v"(qm), "v"(pw), "v"(pr), "v"(ma), "v"(oa), "v"(oc), "s"(ck1), "s"(ck2), "s"(csc)
                         : "memory", "v0", "v1", "v2", "v3", "v4", "v5", "v6", "v7", "v8", "v9", "v10", "v11", "v12", "v13", "v14", "v15", "v16", "v17", "v18", "v19", "v20", "v21", "v22", "v23",
                           "v24", "v25", "v26", "v27", "v28", "v29", "v30", "v31", "v32", "v33", "v34", "v35", "v36", "v37", "v38", "v39", "v40", "v41", "v42", "v43", "v44", "v45");
#undef SMF
#undef MAXD
            STAMP(20 + 32 * layer);
            if (__builtin_expect(wave >= 3, 1)) BAR();
            else {
                __builtin_amdgcn_s_setprio(0);
                if (wave == 0) {
                    unsigned long long mk = 0x1111111111111111ull; PINS(mk);
                    BAR();
                    STAMP(5 + 32 * layer);
                    comb_o0(*(const LAS f32x4*)(size_t)cma, *(const LAS f32x4*)(size_t)(cma + 16), *(const LAS f32x4*)(size_t)cxa, *(const LAS f32x4*)(size_t)cra,
                            *(const LAS f32x4*)(size_t)(cra + 1024), *(const LAS f32x4*)(size_t)(cra + 2048), *(const LAS f32x4*)(size_t)(cra + 3072), cso, mk, false);
                } else if (wave == 1) {
                    unsigned long long mk = 0x0000111100001111ull; PINS(mk);
                    BAR();
                    comb_o1(*(const LAS f32x4*)(size_t)cma, *(const LAS f32x4*)(size_t)(cma + 16), *(const LAS f32x2*)(size_t)cxa, *(const LAS f32x4*)(size_t)cra, *(const LAS f32x4*)(size_t)(cra + 1024), cso, mk, false);
                } else {
                    BAR();
                    comb_ml(cma, cxa, cra, cso, false);
                }
                STAMP(21 + 32 * layer);
            }
        }
    };

    unsigned nj = wave == 3 ? WEARLY : 0u, rS = 0u, nS = 0u; int ord;
    {
        const int tid = threadIdx.x, lane = tid & 63;
        STAMP(0);
        unsigned pf = 0u, mb = 0u;
        const u16* kbase = kc + ((size_t)((gj >> 1) * HQ + gi) * SK + (SK - WIN) + (gj & 1) * 512) * DH;
        const u16* vbase = vc + ((size_t)((gj >> 1) * HQ + gi) * SK + (SK - WIN) + (gj & 1) * 512) * DH;
        unsigned pt0 = 0u;
        const unsigned S0 = ALT ? (unsigned)(gi & 1) * 8192u : 0u;
        if (wave == 7) {
            __builtin_amdgcn_s_setprio(3);
            for (int i = 0; i < 5; ++i) dma16p(x0, (unsigned)(i * 1024 + lane * 16), lb + L_X0 + i * 1024);
            pf = code_pf((g >> 3) % 11, lane);
            if (lane < 24) *reinterpret_cast<unsigned*>(smem + L_CNT + lane * 4) = lane == 16 ? tst : 0u;
            BAR();
            if (lane < 10) *reinterpret_cast<u32x4*>(smem + L_XS + 640 + lane * 16) = (u32x4){0x3f803f80u, 0x3f803f80u, 0x3f803f80u, 0x3f803f80u};
            if (lane < 16) { *reinterpret_cast<u32x4*>(smem + L_CS + 768 + lane * 16) = (u32x4){0u, 0u, 0u, 0u}; }
        } else {
            if (wave >= 4) {
                if (wave == 4) st16_sc1(ws, WS_OWN + (unsigned)(gi * 16384 + (lane >> 5) * 8192 + gj * 512 + (lane & 31) * 16), (u32x4){~0u, ~0u, ~0u, ~0u});
                else empty5(0, (unsigned)(wave - 5) * 8192u, lane);
                if (wave == 6) {
                    __builtin_amdgcn_s_setprio(3);
                    if (gi + gj == 0) st4_sc1(ws, WS_R + (lane & 31) * 4, 0u);
                    st4_sc1(ws, WS_MB + (unsigned)((gi * 8 + gj) * 128 + (lane & 31) * 4), 0u);
                }
            } else if (PROBE && wave >= 2) {
                unsigned long long c0, c1;
                asm volatile("s_memtime %0\n\ts_waitcnt lgkmcnt(0)\n\ts_load_dword %2, %3, %4\n\ts_waitcnt lgkmcnt(0)\n\ts_memtime %1\n\ts_waitcnt lgkmcnt(0)"
                             : "=&s"(c0), "=&s"(c1), "=&s"(pt0) : "s"(ws), "s"((6u << 20) + ((((tst + g * 977u) * 2654435761u) >> 11 & 0x1fdf80u) | ((wave ^ gj) & 1) * 8192)) : "memory");
                pt0 = ((wave ^ gj) & 1) ? (unsigned)c1 - (unsigned)c0 : (unsigned)c0 - (unsigned)c1;
            }
            STAMP(32);
#pragma unroll 1
            for (int i = wave < 2 ? wave : wave < 4 ? 35 : wave - 2; i < 35; i += 5) {
                if (i >= 33) dma16p(wr, (unsigned)((i == 33 || lane < 16) ? (i - 33) * 1024 + lane * 16 : 0), lb + L_WR + (i - 33) * 1024);
                else {
                    const int c = i * 64 + lane; int row = c / 26, cc = c - 26 * row; if (cc > 23) cc = 23; if (row > 79) row = 79;
                    dma16(wq, (unsigned)(((gj * 80 + swz(row)) * QD + gi * DH + cc * 8) * 2), lb + L_WQ + i * 1024);
                }
            }
            STAMP(36);
            BAR();
            STAMP(40);
            *reinterpret_cast<u32x4*>(smem + L_P + tid * 16) = (u32x4){0u, 0u, 0u, 0u};
            if (tid < 72 * 12 - 448) *reinterpret_cast<u32x4*>(smem + L_P + 7168 + tid * 16) = (u32x4){0u, 0u, 0u, 0u};
            if (wave >= 4) {
                asm volatile("s_waitcnt vmcnt(0) lgkmcnt(0)" ::: "memory");
                lds_add(0, 1u);
            } else if (wave >= 2) lds_add(20, pt0);
        }
        u32x4 a[8];
        unsigned ko = (unsigned)((wave * 64 + (lane & 7)) * 384 + (((lane >> 3) & 1) * 4 + (lane >> 4)) * 16);
        PIN1(ko);
        auto k_group = [&](auto GC_) __attribute__((always_inline)) {
            constexpr int G = decltype(GC_)::value, R = RK + 24 * (G >> 1) + 4 * (G & 1);
            const unsigned o = ko + G * 3072;
            pace(3);
            ld16_nt<R, 0>(kbase, o); ld16_nt<R + 8, 128>(kbase, o); ld16_nt<R + 16, 256>(kbase, o);
            const int lane = lane_id();
            const unsigned ho = WS_H1 + S0 + (unsigned)((gi * 4 + (gj >> 1)) * 16384 + 3584 + (lane < 48 ? lane : 0) * 16);
            if (wave < 7) {
                asm volatile("; COND_BEGIN");
                if (G == 0 && wave < 4) {
                    asm volatile("s_waitcnt vmcnt(%0) lgkmcnt(0)" :: "i"(3) : "memory");
                    lds_add(0, 1u);
                    if (wave < 2) load_gu(wave, lane);
                    if (wave == 2) load_wd(lane);
                    if (wave == 3) {
#pragma unroll 1
                        for (int k = 5 - WEARLY; k < 5; ++k) load_wo(k, lane, true);
                    }
                }
                asm volatile("; COND_END");
            }
            if (wave == 7) {
                asm volatile("; COND_BEGIN");
                if (G == GP) {
                    VMW(3 * (GP + 1) + 1);
                    const unsigned char* p = smem + L_X0 + (lane >> 4) * 1280;
                    const float c0 = lo16(*reinterpret_cast<const u16*>(p));
                    const f32x2 c2 = {c0, c0};
                    f32x2 s2 = {0.f, 0.f}, q2 = s2;
#pragma unroll
                    for (int k = 0; k < 5; ++k) {
                        const u32x4 w = *reinterpret_cast<const u32x4*>(p + k * 256 + (lane & 15) * 16);
#pragma unroll
                        for (int e = 0; e < 4; ++e) { const f32x2 d = unpk(w[e]) - c2; s2 += d; q2 += d * d; }
                    }
                    const float s = row16_sum(s2[0] + s2[1]), qq = row16_sum(q2[0] + q2[1]);
                    const float ms = s * (1.f / DM);
                    const unsigned mv = pk2(c0 + ms, fmax2(qq - s * ms, 0.f) * (1.f / DM));
                    const float mb = lo16(mv), rden = frcp(bfr(__builtin_amdgcn_sqrtf(bfr(hi16(mv) + 1e-5f))));
                    const f32x2 mb2 = {mb, mb}, rd2 = {rden, rden};
                    if ((lane & 15) < 10) {
                        u32x4 w = *reinterpret_cast<const u32x4*>(p + gj * 160 + (lane & 15) * 16);
#pragma unroll
                        for (int e = 0; e < 4; ++e) w[e] = pk2v(unpk(pk2v(unpk(w[e]) - mb2)) * rd2);
                        *reinterpret_cast<u32x4*>(smem + L_XS + (lane >> 4) * 160 + (lane & 15) * 16) = w;
                    }
                    STAMP(30);
#pragma unroll 1
                    for (unsigned spins = 0; spins < (kSpinCap << 4); ++spins) {
                        if (lds_word(lb + L_CNT) >= 7u) break;
                        __builtin_amdgcn_s_sleep(1);
                    }
                    STAMP(26);
                }
                if (G == GP || G == GA) {
#pragma unroll 1
                    for (int blk = 0; blk < 3; ++blk) {
                        LANE_VARS();
                        const unsigned xb = lb + L_XS + (unsigned)((G == GP ? p4 : 4) * 160);
                        Frag80 f[4];
#pragma unroll
                        for (int i = 0; i < 4; ++i) f[i] = frag80(lb + L_WQ + (unsigned)(128 * blk + 32 * i + 8 * p4), 416, r0s, r1s, r2s);
                        const Frag80 b = rows80(xb + kq * 16, xb + 128 + kq * 8);
                        const u32x4 c = tiles4(f, b, 0x000f000f000f000full);
                        if (G == GP) st16_sc1(ws, WS_H1 + S0 + (unsigned)((gi * 4 + p4) * 16384 + gj * 896 + 256 * blk + q4 * 64 + kq * 16), c);
                        else *reinterpret_cast<u32x4*>(smem + L_CS + 256 * blk + q4 * 64 + kq * 16) = c;
                    }
                    if (G == GP) { STAMP(2); WAIT1(pf, 3 * (GP + 1) + 3); }
                }
                if (G == GA) {
                    const unsigned half = __builtin_amdgcn_s_getreg(2 << 6 | 20);
                    lds_set(lb + L_CNT + 68, 1u | (((int)lds_word(lb + L_CNT + 20) > 0) ^ half) << 1 | half << 2);
                    VMW(3 * (GA - GP));
                    send_flag(lane_id());
                    lds_set(lb + L_CNT + 8, 1u);
                    STAMP(17);
                    f32x2 r2 = {0.f, 0.f};
#pragma unroll
                    for (int k = 0; k < 5; ++k) r2 += unpk(*reinterpret_cast<const unsigned*>(smem + L_WR + k * 256 + lane_id() * 4));
                    *reinterpret_cast<float*>(smem + L_CNT + 12) = wave_sum(r2[0] + r2[1]);
                }
                asm volatile("; COND_END");
            }
            if (wave == 6) {
                asm volatile("; COND_BEGIN");
                if (G == GB) mb = ld4_sc1(ws, WS_MB + (unsigned)((gi * 8 + gj) * 128 + (lane & 7) * 4));
                if (G == GC) {
                    WAIT1(mb, 3 * (GC - GB));
#pragma unroll 1
                    for (unsigned spins = 0; spins < kSpinCap && !all_set(mb); ++spins) {
                        if ((spins & 7u) == 7u && lds_word(lb + L_CNT + 8)) send_flag(lane);
                        mb = ld4_sc1(ws, WS_MB + (unsigned)((gi * 8 + gj) * 128 + (lane & 7) * 4));
                        WAIT1(mb, 0);
                    }
                    lds_set(lb + L_CNT + 92, (__builtin_amdgcn_readlane(mb, gi & 7) & 4u) << 11);
                    lds_set(lb + L_CNT + 88, (tst & ~31u) | __builtin_popcount((unsigned)__builtin_amdgcn_ballot_w64((mb & 2u) != 0u) & 255u) << 1 | 1u);
                    report();
                    GATHER8(a, ho, 896);
                    pace(XCOST, false);
                    STAMP(31);
                }
                asm volatile("; COND_END");
            } else if (G == GC) { for (int k = 0; k < 8; ++k) asm volatile("" : "=v"(a[k])); }
        };
        k_group(IC<0>{}); k_group(IC<1>{}); k_group(IC<2>{}); k_group(IC<3>{}); k_group(IC<4>{}); k_group(IC<5>{}); k_group(IC<6>{}); k_group(IC<7>{});
        {
            const int lane = lane_id();
            const unsigned v0 = (unsigned)((wave * 64 + 4 * (lane >> 4)) * 384 + (lane & 15) * 16);
            const unsigned v1 = v0 + 256 - (unsigned)((lane & 15) * 8);
            auto v_group = [&](auto HC) __attribute__((always_inline)) {
                constexpr int h = decltype(HC)::value, R = h < 8 ? RV + 16 * (h & 3) + 4 * (h >> 2) : RV + 64 + 2 * (h - 8), NV = h < 8 ? 2 * h + 2 : 4 * h - 12;
                const unsigned o = (h < 8 ? v0 + (h >> 1) * 6144 : v1 + (h - 8) * 6144);
                pace(2);
                if (h < 8) { ld16_nt<R, (h & 1) * 768>(vbase, o); ld16_nt<R + 8, (h & 1) * 768 + 384>(vbase, o); }
                else { ld8_nt<R, 0>(vbase, o); ld8_nt<R + 8, 384>(vbase, o); ld8_nt<R + 16, 768>(vbase, o); ld8_nt<R + 24, 1152>(vbase, o); }
                if (wave == 7 && gi + gj == 0) {
                    asm volatile("; COND_BEGIN");
                    if (h == 3 || h == 6) mb = ld4_sc1(ws, WS_R + (unsigned)((lane & 31) * 4));
                    if (h == 4 || h == 7) {
                        WAIT1(mb, 2);
                        if (all_set(mb) && !lds_word(lb + L_CNT + 36)) answer(lane, mb);
                    }
                    asm volatile("; COND_END");
                }
                if (wave == 6) {
                    asm volatile("; COND_BEGIN");
                if (h == HD) {
                    WAIT8(a, 3 * (7 - GC) + NV);
                    const f32x4 sq = SUM8(a);
                    unsigned* qp = reinterpret_cast<unsigned*>(smem + L_Q + (lane >> 2) * 32 + (lane & 3) * 4);
                    qp[0] = pk2(sq[0], sq[1]); qp[4] = pk2(sq[2], sq[3]);
                    asm volatile("s_waitcnt lgkmcnt(0)\n\tds_write_b32 %0, %1" :: "v"(lb + L_CNT + 4), "v"(1u) : "memory");
                    STAMP(27);
                    lead = LEAD * PACE * 4;
                }
                    if (h == 6) mb = ld4_sc1(ws, WS_MB + (unsigned)((gi * 8 + (gi & 7)) * 128 + 32));
                    if (h == 8) {
                        WAIT1(mb, 6);
                        const unsigned r = __builtin_amdgcn_readfirstlane(mb);
                        if (r) { adopt(r); STAMP(29); }
                    }
                    asm volatile("; COND_END");
                }
                if constexpr (h < 6) {
                    constexpr int i = 2 * h;
                    fix_k1(IC<i>{}, IC<20 - 6 * (i / 3) - i % 3 + NV>{});
                    fix_k1(IC<i + 1>{}, IC<20 - 6 * ((i + 1) / 3) - (i + 1) % 3 + NV>{});
                }
                if (h == 5) {
                    STAMP(1);
                    __builtin_amdgcn_s_setprio(0);
                    att_qk0();
                }
            };
            v_group(IC<0>{}); v_group(IC<1>{}); v_group(IC<2>{}); v_group(IC<3>{}); v_group(IC<4>{}); v_group(IC<5>{});
            v_group(IC<6>{}); v_group(IC<7>{}); v_group(IC<8>{}); v_group(IC<9>{}); v_group(IC<10>{}); v_group(IC<11>{});
            lds_add(16, 1u);
        }
    }
    {
        STAMP(28);
        att_addr();
        const int lane = lane_id();
        cma = lb + L_M; cxa = lb + L_M + (unsigned)((lane >> 4) * 8); cra = lb + L_OP + (unsigned)(512 + (lane >> 4) * 2048 + (lane & 15) * 16);
        cso = WS_OWN + (unsigned)(gi * 16384 + gj * 512 + 256 + (lane >> 5) * 64 + (lane & 12) * 4 + (lane & 1) * 8192);
        PIN2(cma, cxa); PIN2(cra, cso);
        att_pv0();
    }
    {
        f32x4 m0, m1, o0, o1; f32x2 mm; unsigned r;
        asm volatile("s_mov_b64 exec, 1\n\tds_add_rtn_u32 %5, %6, %7 offset:%10\n\ts_mov_b64 exec, -1\n\t"
                     "ds_read_b128 %0, %6\n\tds_read_b128 %1, %6 offset:16\n\tds_read_b64 %2, %8\n\tds_read_b128 %3, %9\n\tds_read_b128 %4, %9 offset:1024\n\ts_waitcnt lgkmcnt(0)"
                     : "=&v"(m0), "=&v"(m1), "=&v"(mm), "=&v"(o0), "=&v"(o1), "=&v"(r)
                     : "v"(cma), "v"(1u), "v"(cxa), "v"(cra), "i"(L_CNT + 28 - L_M) : "memory");
        ord = __builtin_amdgcn_readfirstlane(r);
        if (ord == 7) { STAMP(5); comb_o1(m0, m1, mm, o0, o1, cso, 0x0000333300003333ull, true); lds_set(lb + L_CNT + 48, now() | 1u); STAMP(21); }
        const int lane = lane_id();
        auto wait_cnt = [&](int off, unsigned v) {
#pragma unroll 1
            for (unsigned spins = 0; spins < (kSpinCap << 4); ++spins) { if (lds_word(lb + L_CNT + off) >= v) break; __builtin_amdgcn_s_sleep(1); }
        };
        if (ord == 1) {
            wait_cnt(16, 8u);
            unsigned xa, ra; ml_addr(lane, xa, ra);
            comb_ml(cma, xa, ra, WS_OWN + (unsigned)(gi * 16384 + gj * 512 + 384 + (lane & 1) * 8192), true);
            STAMP(24);
        } else if (ord == 0 && !lds_word(lb + L_CNT + 52)) {
            if (gi + gj == 0 && !lds_word(lb + L_CNT + 36)) {
                unsigned r = 0u;
#pragma unroll 1
                for (unsigned spins = 0; spins < kSpinCap; ++spins) {
                    r = ld4_sc1(ws, WS_R + (unsigned)((lane & 31) * 4));
                    WAIT1(r, 0);
                    if (all_set(r)) break;
                    if ((spins & 7u) == 7u) send_flag(lane);
                }
                answer(lane, r);
            }
#pragma unroll 1
            for (unsigned spins = 0; spins < kSpinCap; ++spins) {
                unsigned r = ld4_sc1(ws, WS_MB + (unsigned)((gi * 8 + (gi & 7)) * 128 + 32));
                WAIT1(r, 0);
                r = __builtin_amdgcn_readfirstlane(r);
                if (r) { adopt(r); break; }
                if ((spins & 7u) == 7u) { send_flag(lane); report(); }
                __builtin_amdgcn_s_sleep(16);
            }
            STAMP(29);
        } else if (ord == 2) {
#pragma unroll 1
            for (unsigned spins = 0; spins < (kSpinCap << 6); ++spins) if (lds_word(lb + L_CNT + 24) >= 8u) break;
            const unsigned ra = lb + L_OP + (unsigned)((lane >> 5) * 4096 + (lane & 31) * 16);
            comb_o0(*(const LAS f32x4*)(size_t)cma, *(const LAS f32x4*)(size_t)(cma + 16), *(const LAS f32x4*)(size_t)(cma + (lane >> 5) * 16), *(const LAS f32x4*)(size_t)ra, *(const LAS f32x4*)(size_t)(ra + 1024),
                    *(const LAS f32x4*)(size_t)(ra + 2048), *(const LAS f32x4*)(size_t)(ra + 3072), WS_OWN + (unsigned)(gi * 16384 + gj * 512 + (lane & 60) * 4 + (lane & 1) * 8192), 0x3333333333333333ull, true);
            STAMP(22);
        }
#pragma unroll 1
        for (;;) {
            unsigned j;
            asm volatile("s_mov_b64 exec, 1\n\tds_add_rtn_u32 %0, %1, %2\n\ts_mov_b64 exec, -1\n\ts_waitcnt lgkmcnt(0)" : "=&v"(j) : "v"(lb + L_CNT + 40), "v"(1u) : "memory");
            j = __builtin_amdgcn_readfirstlane(j);
            if (j >= 5u - WEARLY) break;
            load_wo((int)j, lane, false); ++nj;
        }
        STAMP(23);
    }

    auto chain = [&](auto LC) {
        constexpr int layer = decltype(LC)::value;
        unsigned h4 = WS_H4 + (unsigned)((layer * 32 + gi) * 32768) + (layer ? rS ^ 8192u : 0u);
        auto sides = [&]() { rS = lds_word(lb + L_CNT + 80); nS = lds_word(lb + L_CNT + 84); h4 += rS ^ 8192u; };
        if (wave < 3) {
            LANE_VARS();
            if (layer == 0) {
                VMW(0);
                lds_add(44, nj);
                unsigned tf = 0u;
#pragma unroll 1
                for (unsigned spins = 0; spins < (kSpinCap << 8); ++spins) {
                    tf = lds_word(lb + L_CNT + (EMODE ? 48 : 24));
                    if ((EMODE ? tf != 0u : tf >= 8u) && lds_word(lb + L_CNT + 52)) break;
                }
                sides();
                if (EMODE == 1) until(tf + DG2 + 3);
                if (wave < 2) empty5(5, (unsigned)wave * 8192u, lane); else empty4(0, rS, lane);
                VMW(0);
                BAR();
#pragma unroll 1
                for (unsigned spins = 0; spins < (kSpinCap << 4); ++spins) if (lds_word(lb + L_CNT + 76) >= 5u) break;
                if (wave < 2) { empty1(wave, rS, lane); empty35(1, (unsigned)wave * 8192u, lane); }
                else { empty4(1, rS, lane); st16m(ws, WS_CS + (unsigned)((gi * 8 + gj) * 768 + lane * 16), *reinterpret_cast<const u32x4*>(smem + L_CS + lane * 16), 0xffffffffffffull); }
            } else BAR();
            {
                const unsigned im = lb + L_GU + (unsigned)(32 * wave + 8 * p4);
                Frag80 fg = frag80(im, 96, r0s, r1s, r2s), fu = frag80(im + 8192, 96, r0s, r1s, r2s);
                const unsigned xb = lb + L_XS + (unsigned)((n < 4 ? n : 4) * 160);
                unsigned b16 = xb + kq * 16, b8 = xb + 128 + kq * 8;
                unsigned sg = h4 + (unsigned)((n < 4 ? h4s(wave) + gj * 512 + n * 64 : H4C + wave * 1024 + gj * 128) + kq * 16), su = sg + (n < 4 ? 256u : 64u);
                unsigned o = h4 + h4s(wave) + (unsigned)(lane * 16);
                unsigned oc = h4 + (unsigned)(H4C + wave * 1024 + ((lane >> 5) * 4 + q4) * 128 + (kq & 1) * 64 + p4 * 16);
                unsigned os = h4 + (unsigned)(H4T + (p4 + 4 * (kq & 1)) * 128 + q4 * 16);
                unsigned ha = lb + L_H + (unsigned)(q4 * 96 + wave * 32 + p4 * 8 + kq * 2);
                f32x2 cs = {0.f, 0.f};
                if (layer != 0) cs = *reinterpret_cast<const f32x2*>(smem + L_CS2 + wave * 512 + lane * 8);
                PIN3(fg.a0, fg.a1, fg.a2); PIN3(fu.a0, fu.a1, fu.a2); PIN3(b16, b8, sg); PIN3(su, o, os); PIN2(ha, cs);
                unsigned long long mk = layer ? 0x000f000f000f000full : 0x001f001f001f001full;
                unsigned ta = WS_MB + (unsigned)((lane >> 1) * 1024 + 64 + (lane & 1) * 16);
                if (layer == 0) PIN2(oc, ta);
                PINS(mk);
                if (layer == 0) VMW(0);
                BAR();
                STAMP(10 + 32 * layer);
                u32x4 tl = {0u, 0u, 0u, 0u};
                if (layer == 0) tl = ld16_sc1(ws, ta);
                const Frag80 b = rows80(b16, b8);
                const f32x4 g0 = mfma32(fg.a0, b.a0, z4), u0 = mfma32(fu.a0, b.a0, z4), g1 = mfma32(fg.a1, b.a1, g0), g2 = mfma16(fg.a2, b.a2, z4), u1 = mfma32(fu.a1, b.a1, u0), u2 = mfma16(fu.a2, b.a2, z4);
                __builtin_amdgcn_sched_barrier(0);
                const f32x4 cg = g1 + g2, cu = u1 + u2;
                st16m2<0>(ws, sg, asu(cg), su, asu(cu), mk);
                STAMP(11 + 32 * layer);
                if (layer == 0) {
                    WAIT1(tl, 2);
#pragma unroll 1
                    for (unsigned spins = 0; spins < kSpinCap && !all_set(min(min(tl[0], tl[1]), min(tl[2], tl[3]))); ++spins) {
                        tl = ld16_sc1(ws, ta);
                        WAIT1(tl, 0);
                    }
                    const unsigned tn = now();
                    unsigned d = min(min(min(tn - tl[0], tn - tl[1]), min(tn - tl[2], tn - tl[3])), 800u);
                    d = min(d, dppu<0xB1>(d)); d = min(d, dppu<0x4E>(d)); d = min(d, dppu<0x124>(d)); d = min(d, dppu<0x128>(d));
                    unsigned tr = tn - min(min((unsigned)__builtin_amdgcn_readlane(d, 0), (unsigned)__builtin_amdgcn_readlane(d, 16)), min((unsigned)__builtin_amdgcn_readlane(d, 32), (unsigned)__builtin_amdgcn_readlane(d, 48)));
                    lds_set(lb + L_CNT + 56, tr | 1u);
                                        until(__builtin_amdgcn_readfirstlane(tr) + X4A);
                } else sched(X4, 12);
                STAMP(16 + 32 * layer);
                float hv; f32x2 st;
                u32x4 a0, a1, a2, a3, ac, as;
                auto load = [&]() __attribute__((always_inline)) {
                    as = ld16_sc1(ws, os);
                    if (layer == 0) ac = ld16_sc1(ws, oc);
                    a0 = ld16_sc1<0>(ws, o); a1 = ld16_sc1<1024>(ws, o); a2 = ld16_sc1<2048>(ws, o); a3 = ld16_sc1<3072>(ws, o);
                    if (layer == 0) asm volatile("s_waitcnt vmcnt(0)" : "+v"(a0), "+v"(a1), "+v"(a2), "+v"(a3), "+v"(ac), "+v"(as) :: "memory");
                    else { asm volatile("s_waitcnt vmcnt(0)" : "+v"(a0), "+v"(a1), "+v"(a2), "+v"(a3), "+v"(as) :: "memory"); ac = as; }
                };
                auto compute = [&]() __attribute__((always_inline)) {
                    float ss, sm;
                    asm volatile("v_add_f32_dpp %0, %2, %2 quad_perm:[1,0,3,2] row_mask:0xf bank_mask:0xf\n\tv_add_f32_dpp %1, %3, %3 quad_perm:[1,0,3,2] row_mask:0xf bank_mask:0xf\n\ts_nop 0\n\t"
                                 "v_add_f32_dpp %0, %0, %0 quad_perm:[2,3,0,1] row_mask:0xf bank_mask:0xf\n\tv_add_f32_dpp %1, %1, %1 quad_perm:[2,3,0,1] row_mask:0xf bank_mask:0xf\n\ts_nop 0"
                                 : "=&v"(ss), "=&v"(sm) : "v"(as[0]), "v"(as[1]));
                    ss = x16_add(ss); sm = x16_add(sm);
                    st = ln_stats(ss, sm);
                    const f32x4 t = (asf(a0) + asf(a1)) + (asf(a2) + asf(a3));
                    const f32x2 gu = sw16(rs32(t[0], t[2]), rs32(t[1], t[3]));
                    if (layer == 0) {
                        cs = sw16(rs32(asf(ac)[0], asf(ac)[2]), rs32(asf(ac)[1], asf(ac)[3]));
                        cs[0] += dpp<0x124>(cs[0]); cs[1] += dpp<0x124>(cs[1]); cs[0] += dpp<0x128>(cs[0]); cs[1] += dpp<0x128>(cs[1]);
                    }
                    const unsigned gw = pk2v((gu - st[0] * cs) * st[1]);
                    const float gv = lo16(gw);
                    hv = bfr(gv * frcp(1.f + ex2(gv * -LOG2E))) * hi16(gw);
                    *(LAS u16*)(size_t)ha = (u16)pk2(hv, hv);
                    __builtin_amdgcn_sched_barrier(0);
                };
                RETRY(load(), max(max(max(um4(a0), um4(a1)), max(um4(a2), um4(a3))), max(um4(ac), um4(as))), compute(), any_nan(hv, hv), layer ? X4 : X4A);
                BAR();
                STAMP(12 + 32 * layer);
                if (layer == 0) {
                    *reinterpret_cast<f32x2*>(smem + L_CS2 + wave * 512 + lane * 8) = cs;
                }
            }
            if (layer == 0) {
                if (wave == 1) {
                    const unsigned o = WS_CS + (unsigned)(gi * 6144 + 3072 + (lane < 48 ? lane : 0) * 16);
                    u32x4 a[8];
                    GATHER8(a, o, 768);
                    WAIT8(a, 0);
                    if (lane < 48) *reinterpret_cast<f32x4*>(smem + L_CS + lane * 16) = SUM8(a);
                }
                Frag80 f[4];
#pragma unroll
                for (int i = 0; i < 4; ++i) { f[i] = frag80(lb + L_WQ + (unsigned)(128 * wave + 32 * i + 8 * p4), 416, r0s, r1s, r2s); PIN3(f[i].a0, f[i].a1, f[i].a2); }
                unsigned b16 = lb + L_XS + (unsigned)(p4 * 160 + kq * 16), b8 = lb + L_XS + (unsigned)(p4 * 160 + 128 + kq * 8);
                unsigned so = WS_H1 + rS + (unsigned)((gi * 4 + p4) * 16384 + gj * 896 + 256 * wave + q4 * 64 + kq * 16);
                unsigned long long mk = 0x000f000f000f000full;
                PIN3(b16, b8, so); PINS(mk);
                BAR();
                STAMP(33);
                const Frag80 b = rows80(b16, b8);
                st16_sc1(ws, so, tiles4(f, b, mk));
                STAMP(34);
                qk_addr(true); att_addr();
                cma = lb + L_M; cxa = lb + L_M + (unsigned)(wave == 0 ? (lane >> 5) * 16 : (lane >> 4) * 8);
                cra = lb + L_OP + (unsigned)(wave == 0 ? (lane >> 5) * 4096 + (lane & 31) * 16 : 512 + (lane >> 4) * 2048 + (lane & 15) * 16);
                if (wave == 2) ml_addr(lane, cxa, cra);
                cso = WS_OWN + rS + (unsigned)(gi * 16384 + 4096 + gj * 512) + (unsigned)(wave == 0 ? lane * 4 : wave == 1 ? 256 + (lane >> 5) * 64 + (lane & 15) * 4 : 384);
                PIN2(cma, cxa); PIN2(cra, cso);
            }
        } else {
            const int t = wave - 3;
            StatAddr sa;
            auto gather32 = [&](unsigned o, int lane, unsigned X, auto mid, auto fin) __attribute__((always_inline)) {
                u32x4 a0 = ld16_sc1<-2048>(ws, o), a1 = ld16_sc1<-1024>(ws, o), a2 = ld16_sc1<0>(ws, o), a3 = ld16_sc1<1024>(ws, o), sel;
                const unsigned one = (lane & 1) ? 0x3f800000u : 0x3f80u;
#pragma unroll
                for (int k = 0; k < 4; ++k) sel[k] = ((lane >> 1) & 3) == k ? one : 0u;
                unsigned long long hi = 0xff00ff00ff00ff00ull;
                PIN1(sel); PINS(hi);
                mid();
                float v;
                auto sum = [&]() __attribute__((always_inline)) {
                    f32x4 d; float w;
                    asm volatile("s_waitcnt vmcnt(3)\n\tv_mfma_f32_16x16x32_bf16 %0, %1, %5, 0\n\ts_waitcnt vmcnt(2)\n\tv_mfma_f32_16x16x32_bf16 %0, %2, %5, %0\n\t"
                                 "s_waitcnt vmcnt(1)\n\tv_mfma_f32_16x16x32_bf16 %0, %3, %5, %0\n\ts_waitcnt vmcnt(0)\n\tv_mfma_f32_16x16x32_bf16 %0, %4, %5, %0"
                                 : "=&v"(d), "+v"(a0), "+v"(a1), "+v"(a2), "+v"(a3) : "v"(sel) : "memory");
                    asm volatile("s_nop 7\n\tv_cndmask_b32 %0, %2, %3, %6\n\tv_cndmask_b32 %1, %4, %5, %6\n\ts_nop 1\n\tv_permlane32_swap_b32 %0, %1\n\tv_add_f32 %0, %0, %1"
                                 : "=&v"(v), "=&v"(w) : "v"(d[0]), "v"(d[1]), "v"(d[2]), "v"(d[3]), "s"(hi));
                    fin(v);
                    __builtin_amdgcn_sched_barrier(0);
                };
                sum();
                if (__builtin_expect(any_nan(v, v) != 0ull, 0)) {
#pragma unroll 1
                    for (unsigned spins = 0; spins < kSpinCap; ++spins) {
                        slip(X);
                        again16<-2048>(a0, ws, o); again16<-1024>(a1, ws, o); again16<0>(a2, ws, o); again16<1024>(a3, ws, o);
                        asm volatile("s_waitcnt vmcnt(0)" : "+v"(a0), "+v"(a1), "+v"(a2), "+v"(a3) :: "memory");
                        if (!any_empty(max(max(um4(a0), um4(a1)), max(um4(a2), um4(a3))))) break;
                    }
                    sum();
                }
            };
            auto pack4 = [&](float c0, float c1, float c2, float c3) __attribute__((always_inline)) { const unsigned w0 = min(pk2(c0, c1), 0xfffffffeu), w1 = min(pk2(c2, c3), 0xfffffffeu); return (u32x2){w0, w1}; };
            unsigned xw;
            __builtin_amdgcn_s_setprio(0);
            {
                LANE_VARS();
                if (layer == 0) xe = lo16(*reinterpret_cast<const u16*>(smem + L_X0 + kq * 1280 + gj * 160 + t * 32 + n * 2));
                STAMP(6 + 32 * layer);
                if (layer == 0) nS = lds_word(lb + L_CNT + 84);
                unsigned lo = WS_OWN + (layer ? rS : nS) + (unsigned)(gi * 16384 + layer * 4096 + (lane >> 3) * 512 + t * 128 + (t == 3 ? 0 : (lane & 7) * 16));
                unsigned lw = lb + L_O2 + (unsigned)((lane >> 3) * 416 + t * 128 + (t == 3 ? 0 : (lane & 7) * 16));
                PIN2(lo, lw);
                u32x4 ov = {~0u, ~0u, ~0u, ~0u};
                if (layer != 0 && t < 4) { sched(X2, 20); STAMP(49); ov = ld16_sc1(ws, lo); }
                const unsigned wa = lb + L_WO + (unsigned)(r0s * 160 + 32 * t + 8 * p4), wa1 = lb + L_WO + (unsigned)(r1s * 160 + 32 * t + 8 * p4);
                bf16x8 af[6];
                if (layer == 0) {
                    if (t < 4) {
                        unsigned tf = 0u;
#pragma unroll 1
                        for (unsigned spins = 0; spins < (kSpinCap << 4); ++spins) { tf = lds_word(lb + L_CNT + 48); if (tf) break; }
                        until(tf + DG2);
                        ov = ld16_sc1(ws, lo);
                        if (nj) { WAIT1(ov, 1); lds_add(44, nj); }
                    } else { VMW(0); lds_add(44, nj); }
#pragma unroll 1
                    for (unsigned spins = 0; spins < (kSpinCap << 4); ++spins) if (lds_word(lb + L_CNT + 44) >= 5u) break;
                    STAMP(8);
                }
#pragma unroll
                for (int s = 0; s < 6; ++s) af[s] = cat8(tr64(wa + s * 32 * 160), tr64(wa1 + s * 32 * 160));
                const unsigned h3 = tile35(gj, 10 * layer + t);
                unsigned so = h3 + (unsigned)((lane & 1) * 8192 + gi * 128 + (lane & 2) * 32 + (lane & 4) * 8 + kq * 8), go = h3 + nS + (unsigned)(2048 + lane * 16);
                unsigned xs = lb + L_XS + (unsigned)(kq * 160 + t * 32 + n * 2);
                unsigned br = lb + L_O2 + (unsigned)((n & 7) * 416 + kq * 16), mr = lb + L_O2 + (unsigned)((n & 7) * 416 + 384);
                unsigned long long mk = 0x00ff00ff00ff00ffull;
                PIN3(af[0], af[1], af[2]); PIN3(af[3], af[4], af[5]); PIN3(br, mr, so); PIN2(go, xs); PINS(mk);
                if (t < 4) {
                    WAIT1(ov, 0);
                    *(LAS u32x4*)(size_t)lw = ov;
                    if (layer == 0 ? any_empty(um4(ov)) : __builtin_expect(any_empty(um4(ov)), 0)) {
#pragma unroll 1
                        for (unsigned spins = 0; spins < kSpinCap; ++spins) {
                            if (layer != 0) slip(X2);
                            ov = ld16_sc1(ws, lo);
                            WAIT1(ov, 0);
                            *(LAS u32x4*)(size_t)lw = ov;
                            if (!any_empty(um4(ov))) break;
                            if (layer == 0) { if ((spins & 15u) == 15u) send_flag(lane); __builtin_amdgcn_s_sleep(1); }
                        }
                    }
                }
                BAR();
                bf16x8 bf[6];
                const f32x2 ml = *(const LAS f32x2*)(size_t)mr;
#pragma unroll
                for (int s = 0; s < 6; ++s) bf[s] = lds16(br + s * 64);
                __builtin_amdgcn_sched_barrier(0);
                f32x4 ca = z4;
#pragma unroll
                for (int s = 0; s < 6; ++s) ca = mfma32(af[s], bf[s], ca);
                const float e = ex2(ml[0] - fmax2(ml[0], dpp<0xB1>(ml[0]))), el = e * ml[1];
                const float f = e * frcp(el + dpp<0xB1>(el));
                float c0, c1, c2, c3;
                asm volatile("s_nop 7\n\t"
                             "v_mul_f32 %0, %4, %8\n\tv_mul_f32 %1, %5, %8\n\tv_mul_f32 %2, %6, %8\n\tv_mul_f32 %3, %7, %8\n\t"
                             "v_add_f32_dpp %0, %0, %0 quad_perm:[1,0,3,2] row_mask:0xf bank_mask:0xf\n\tv_add_f32_dpp %1, %1, %1 quad_perm:[1,0,3,2] row_mask:0xf bank_mask:0xf\n\t"
                             "v_add_f32_dpp %2, %2, %2 quad_perm:[1,0,3,2] row_mask:0xf bank_mask:0xf\n\tv_add_f32_dpp %3, %3, %3 quad_perm:[1,0,3,2] row_mask:0xf bank_mask:0xf"
                             : "=&v"(c0), "=&v"(c1), "=&v"(c2), "=&v"(c3) : "v"(ca[0]), "v"(ca[1]), "v"(ca[2]), "v"(ca[3]), "v"(f));
                st8m(ws, so, pack4(c0, c1, c2, c3), mk);
                if (layer == 0 && wave == 7) st4_sc1(ws, WS_MB + (unsigned)(gi * 1024 + 64 + gj * 4), now() | 1u);
                if (layer == 0) { lds_add(76, 1u); go -= nS; sides(); go += nS; }
                STAMP(7 + 32 * layer);
                if (layer != 0) sched(X3, 12);
                else wait_word(ws, h3 + nS + (unsigned)(((gi * 7 + t * 5 + 3) & 31) * 128));
                STAMP(18 + 32 * layer);
                gather32(go, lane, layer ? X3 : 0u, [&]() { sa = stats_addr(lane, h4 + (unsigned)(H4T + gj * 128), 16); }, [&](float s) { xw = pk2(xe + bfr(s), 0.f); *(LAS u16*)(size_t)xs = (u16)xw; });
                xe = lo16(xw);
                STAMP(9 + 32 * layer);
            }
            BAR();
            if (wave == 3) stats(sa);
            STAMP(11 + 32 * layer);
            {
                LANE_VARS();
                const unsigned im = lb + L_WD + (unsigned)(32 * t + 8 * p4);
                bf16x8 wd0 = cat8(tr64(im + r0s * 160), tr64(im + r1s * 160)); bf16x4 wd1 = tr64(im + (32 + r2s) * 160);
                const unsigned h5 = tile35(gj, 10 * layer + 5 + t);
                unsigned h16 = lb + L_H + (unsigned)(p4 * 96 + kq * 16), h8 = lb + L_H + (unsigned)(p4 * 96 + 64 + kq * 8);
                unsigned so = h5 + (unsigned)((lane & 4) * 2048 + gi * 128 + (lane & 1) * 64 + (lane & 2) * 16 + kq * 8);
                unsigned long long mk = 0x00ff00ff00ff00ffull;
                PIN2(wd0, wd1); PIN3(h16, h8, so); PINS(mk);
                if (wave == 4) {
                    const unsigned os = h4 + (unsigned)(H4T + (p4 + 4 * (kq & 1)) * 128 + q4 * 16);
                    if (layer == 0) {
#pragma unroll 1
                        for (unsigned spins = 0; spins < (kSpinCap << 4); ++spins) if (lds_word(lb + L_CNT + 56)) break;
                    }
                    sched(layer ? X4 : X4A, 0);
                    u32x4 as;
#pragma unroll 1
                    for (unsigned spins = 0; spins < kSpinCap; ++spins) { as = ld16_sc1(ws, os); WAIT1(as, 0); if (!any_empty(um4(as))) break; }
                    float ss = __uint_as_float(as[0]), sm = __uint_as_float(as[1]), rd = __uint_as_float(as[2]);
                    ss += dpp<0xB1>(ss); sm += dpp<0xB1>(sm); rd += dpp<0xB1>(rd);
                    ss += dpp<0x4E>(ss); sm += dpp<0x4E>(sm); rd += dpp<0x4E>(rd);
                    ss = x16_add(ss); sm = x16_add(sm); rd = x16_add(rd);
                    if (LANES(0x1111ull)) *reinterpret_cast<f32x4*>(smem + L_ST + q4 * 16) = (f32x4){ss, sm, rd, 0.f};
                }
                BAR();
                STAMP(12 + 32 * layer);
                const bf16x8 hb0 = lds16(h16); const bf16x4 hb1 = lds8(h8);
                __builtin_amdgcn_sched_barrier(0);
                const f32x4 c = mfma32(wd0, hb0, z4) + mfma16(wd1, hb1, z4);
                st8m(ws, so, pack4(c[0], c[1], c[2], c[3]), mk);
                STAMP(13 + 33 * layer);
                if (layer == 0 || gi == 4 * t + (gj & 3)) {
                    float rs, n1;
                    auto prep = [&]() __attribute__((always_inline)) {
                        const f32x4 sr = *reinterpret_cast<const f32x4*>(smem + L_ST + kq * 16);
                        const float sw = *reinterpret_cast<const float*>(smem + L_CNT + 12);
                        const f32x2 st = ln_stats(sr[0], sr[1]);
                        const float r = bfr((sr[2] - st[0] * sw) * st[1]);
                        rs = bfr(frcp(1.f + ex2(r * -LOG2E)));
                        n1 = bfr(bfr(xe - st[0]) * st[1]);
                        PIN2(rs, n1);
                    };
                    if (layer == 0) {
                        unsigned go = h5 + nS + (unsigned)(2048 + lane * 16), xs = lb + L_XS + (unsigned)(kq * 160 + t * 32 + n * 2);
                        PIN2(go, xs);
                        sched(X5A, 8);
                        STAMP(15);
                        gather32(go, lane, X5A, [&]() { prep(); sa = stats_addr(lane, WS_H1 + rS + (unsigned)(gi * 65536 + gj * 896 + 768), 16384); },
                                 [&](float y) { xw = pk2(xe + bfr(n1 + bfr(bfr(y) * rs)), 0.f); *(LAS u16*)(size_t)xs = (u16)xw; });
                        xe = lo16(xw);
                        STAMP(14);
                    } else {
                        unsigned go = h5 + nS + (unsigned)(2048 + lane * 16), oo = (unsigned)(kq * 1280 + gj * 160 + t * 32 + n * 2);
                        PIN2(go, oo);
                        sched(X5B, 8);
                        STAMP(47);
                        gather32(go, lane, X5B, prep, [&](float y) { xw = pk2(xe + bfr(n1 + bfr(bfr(y) * rs)), 0.f); });
                        { const unsigned w = xw | (dppu<0xF5>(xw) << 16);
                          const u32x2 w2 = {w, dppu<0xAA>(w)};
                          asm volatile("s_mov_b64 exec, %3\n\tglobal_store_dwordx2 %0, %1, %2 sc0 sc1\n\ts_mov_b64 exec, -1" :: "v"(oo), "v"(w2), "s"(out), "s"(0x1111111111111111ull) : "memory"); }
                        STAMP(45);
                    }
                }
            }
            if (layer == 0) {
                BAR();
                if (wave == 3) { stats(sa); STAMP(34); }
                asm volatile("" : "=v"(cma), "=v"(cxa), "=v"(cra), "=v"(cso));
                if (wave != 7) { qk_addr(true); att_addr(); }
                else {
                    const int lane = lane_id();
                    unsigned o = WS_H1 + rS + (unsigned)((gi * 4 + (gj >> 1)) * 16384 + 3584 + (lane < 48 ? lane : 48) * 16), qw;
                    f32x4 cs;
                    PIN1(o);
                    sched(X1, 26);
                    STAMP(51);
                    f32x4 s; u32x4 a[8];
                    RETRY8(a, o, 896, s, { qw = lb + L_Q + (unsigned)((lane >> 2) * 32 + (lane & 3) * 4); cs = *reinterpret_cast<const f32x4*>(smem + L_CS + lane * 16); PIN2(qw, cs); qk_addr(true); att_addr(); }, { s -= bfr(__int_as_float(__builtin_amdgcn_readlane(__float_as_int(s[1]), 48)) * (1.f / DM)) * cs;
                                           u32x2 w; w[0] = pk2(s[0], s[1]); w[1] = pk2(s[2], s[3]);
                                           if (LANES(0xffff000000000000ull)) { w[0] = __float_as_uint(s[0]); w[1] = __float_as_uint(s[1]); }
                                           *(LAS unsigned*)(size_t)qw = w[0]; *(LAS unsigned*)(size_t)(qw + 16) = w[1]; __builtin_amdgcn_sched_barrier(0); }, any_nan(s[0] + s[2], s[1] + s[3]), X1);
                }
            }
        }
    };
    PINS(ck1); PINS(ck2); PINS(csc);
    chain(IC<0>{});
    STAMP(35);
    att1();
    chain(IC<1>{});
}

__device__ __attribute__((used, noinline)) void mega_code_end() { asm volatile(".Lmega_code_end:"); }

extern "C" void kernel_launch(void* const* d_in, const int* in_sizes, int n_in, void* d_out, int out_size, void* d_ws,
                              size_t ws_size, hipStream_t stream) {
    const u16* x0 = (const u16*)d_in[0];
    const u16* kc = (const u16*)d_in[1];
    const u16* vc = (const u16*)d_in[2];
    const u16* wq = (const u16*)d_in[3];
    const u16* wo = (const u16*)d_in[4];
    const u16* wr = (const u16*)d_in[6];
    const u16* wg = (const u16*)d_in[7];
    const u16* wu = (const u16*)d_in[8];
    const u16* wd = (const u16*)d_in[9];
    (void)hipFuncSetAttribute((const void*)mega_kernel, hipFuncAttributeMaxDynamicSharedMemorySize, L_END);
    mega_kernel<<<256, 512, L_END, stream>>>(x0, kc, vc, wq, wo, wr, wg, wu, wd, (u16*)d_out, (unsigned char*)d_ws
                                             );
}
```

```cpp
#include <hip/hip_runtime.h>
#include <hip/hip_bf16.h>
#include <stdint.h>

typedef unsigned short u16;
typedef unsigned u32x4 __attribute__((ext_vector_type(4)));
typedef unsigned u32x2 __attribute__((ext_vector_type(2)));
typedef float f32x4 __attribute__((ext_vector_type(4)));
typedef short s16x4 __attribute__((ext_vector_type(4)));
typedef __attribute__((ext_vector_type(4))) __bf16 bf16x4;
typedef __attribute__((ext_vector_type(8))) __bf16 bf16x8;
#define LAS __attribute__((address_space(3)))

constexpr int DM = 640, HQ = 32, SK = 4096, DH = 192, WIN = 1024, FF = 1536;
constexpr int QD = HQ * DH;
constexpr float SCALE = 0.07216878364870322f, LOG2E = 1.44269504088896341f;
constexpr unsigned kSpinCap = 1u << 16;
#define GJ(g) (((g) >> 3) & 7)
#define GI(g) (((g) & 7) * 4 + ((g) >> 6))

#define D4A 128
#define D5A 94
#define D1 94
#define D2 147
#define D3 74
#define D4 80
#define D5B 84
#define PACE 60
#define PINIT 100
#define PMIN 50
#define DM_ 2
constexpr unsigned X4A = D4A + DM_, X5A = X4A + D5A + DM_, X1 = X5A + D1 + DM_, X2 = X1 + D2 + DM_, X3 = X2 + D3 + DM_, X4 = X3 + D4 + DM_, X5B = X4 + D5B + DM_;
#define WOC 2
#define WOFF 6
#define WEARLY 1
#define DG2 15
#define XCOST 16
#define LEAD 12
#define TOFF 20
#define UG 360
#define SLOW 0
#define TLAND 150
#define ADOPT 1
#define ALT 1
#define PROBE 1
constexpr unsigned NUNIT = 384 + XCOST + 27 + 6 * WEARLY, PBURST = 30;
#define GSTEPS GA = 1, GB = 6, GC = 7, HD = 1
#define EMODE 0
constexpr int GP = 0, GSTEPS;

constexpr unsigned WS_H1 = 0;
constexpr unsigned WS_H4 = 2u << 20;
constexpr unsigned H4C = 20480, H4T = H4C + 3072;
constexpr unsigned WS_H35 = 4u << 20;
constexpr unsigned WS_OWN = WS_H35 + 80 * 16384;
constexpr unsigned WS_CS = WS_OWN + 32 * 16384;
constexpr unsigned WS_MB = WS_CS + 256 * 768;
constexpr unsigned WS_R = WS_MB + 256 * 128;
constexpr unsigned WS_END = WS_R + 128;
static_assert(WS_END <= (6u << 20), "ws map");
__device__ __forceinline__ unsigned tile35(int j, int q) { return WS_H35 + (unsigned)((j * 10 + (q >> 1)) * 16384 + (q & 1) * 4096); }
__device__ __forceinline__ unsigned h4s(int s) { return (unsigned)((s >> 1) * 16384 + (s & 1) * 4096); }

constexpr int L_WQ = 0;
constexpr int L_WO = 33792;
constexpr int L_GU = L_WO + 30720;
constexpr int L_WD = L_GU + 16384;
constexpr int L_X0 = L_WD + 8192;
constexpr int L_WR = L_X0 + 5120;
constexpr int L_XS = L_WR + 2048;
constexpr int L_Q = L_XS + 1024;
constexpr int L_OP = L_Q + 512;
constexpr int L_M = L_OP + 8192;
constexpr int L_H = L_M + 64;
constexpr int L_ST = L_H + 384;
constexpr int L_CS = L_ST + 64;
constexpr int L_CS2 = L_CS + 1024;
constexpr int L_CNT = L_CS2 + 1536;
constexpr int L_P = L_CNT + 96;
constexpr int L_O2 = L_P + 72 * 192;
constexpr int L_DBG = L_O2 + 3328;
constexpr int L_END = L_DBG + 2048;
static_assert(L_END <= 163840 && L_END > 81920, "LDS");

typedef __attribute__((ext_vector_type(2))) __bf16 bf16x2;
typedef float f32x2 __attribute__((ext_vector_type(2)));
__device__ __forceinline__ unsigned pk2(float a, float b) { return __builtin_bit_cast(unsigned, __builtin_convertvector((f32x2){a, b}, bf16x2)); }
__device__ __forceinline__ float lo16(unsigned w) { return __uint_as_float(w << 16); }
__device__ __forceinline__ float hi16(unsigned w) { return __uint_as_float(w & 0xffff0000u); }
__device__ __forceinline__ float bfr(float f) { return __uint_as_float(pk2(0.f, f)); }
template <int CTRL> __device__ __forceinline__ float dpp(float x) {
    return __builtin_bit_cast(float, __builtin_amdgcn_mov_dpp(__builtin_bit_cast(int, x), CTRL, 0xf, 0xf, true));
}
template <int CTRL> __device__ __forceinline__ unsigned dppu(unsigned x) { return (unsigned)__builtin_amdgcn_mov_dpp((int)x, CTRL, 0xf, 0xf, true); }
__device__ __forceinline__ float row16_sum(float v) {
    v += dpp<0xB1>(v); v += dpp<0x4E>(v); v += dpp<0x141>(v); v += dpp<0x140>(v); return v;
}
__device__ __forceinline__ float fmax2(float a, float b) { return __builtin_elementwise_maximum(a, b); }
__device__ __forceinline__ float fmax3(float a, float b, float c) { return fmax2(fmax2(a, b), c); }
__device__ __forceinline__ float x16_add(float v) { auto s = __builtin_amdgcn_permlane16_swap(__float_as_uint(v), __float_as_uint(v), false, false); return __uint_as_float(s[0]) + __uint_as_float(s[1]); }
__device__ __forceinline__ float x32_add(float v) { auto s = __builtin_amdgcn_permlane32_swap(__float_as_uint(v), __float_as_uint(v), false, false); return __uint_as_float(s[0]) + __uint_as_float(s[1]); }
__device__ __forceinline__ float wave_sum(float v) { return x32_add(x16_add(row16_sum(v))); }
__device__ __forceinline__ float rs32(float a, float b) { auto s = __builtin_amdgcn_permlane32_swap(__float_as_uint(a), __float_as_uint(b), false, false); return __uint_as_float(s[0]) + __uint_as_float(s[1]); }
__device__ __forceinline__ f32x2 sw16(float a, float b) { auto s = __builtin_amdgcn_permlane16_swap(__float_as_uint(a), __float_as_uint(b), false, false); return (f32x2){__uint_as_float(s[0]), __uint_as_float(s[1])}; }

#define BAR() asm volatile("s_waitcnt lgkmcnt(0)\n\ts_barrier" ::: "memory")
#define VMW(n) asm volatile("s_waitcnt vmcnt(%0)" :: "i"(n) : "memory")

template <int OFF = 0> __device__ __forceinline__ u32x4 ld16_sc1(const void* sbase, unsigned voff) {
    u32x4 r; asm volatile("global_load_dwordx4 %0, %1, %2 offset:%3 sc1" : "=v"(r) : "v"(voff), "s"(sbase), "i"(OFF) : "memory"); return r;
}
__device__ __forceinline__ unsigned ld4_sc1(const void* sbase, unsigned voff) {
    unsigned r; asm volatile("global_load_dword %0, %1, %2 sc1" : "=v"(r) : "v"(voff), "s"(sbase) : "memory"); return r;
}
template <int OFF = 0> __device__ __forceinline__ void st16_sc1(void* sbase, unsigned voff, u32x4 v) {
    asm volatile("global_store_dwordx4 %0, %1, %2 offset:%3 sc1\n\ts_nop 1" :: "v"(voff), "v"(v), "s"(sbase), "i"(OFF) : "memory");
}
__device__ __forceinline__ void st4_sc1(void* sbase, unsigned voff, unsigned v) {
    asm volatile("global_store_dword %0, %1, %2 sc1" :: "v"(voff), "v"(v), "s"(sbase) : "memory");
}
__device__ __forceinline__ void st8(void* sbase, unsigned voff, u32x2 v) {
    asm volatile("global_store_dwordx2 %0, %1, %2" :: "v"(voff), "v"(v), "s"(sbase) : "memory");
}
__device__ __forceinline__ void st2(void* sbase, unsigned voff, unsigned v) {
    asm volatile("global_store_short %0, %1, %2" :: "v"(voff), "v"(v), "s"(sbase) : "memory");
}
constexpr int RK = 64, RV = 160;
template <int R, int IMM> __device__ __forceinline__ void ld16_nt(const void* sbase, unsigned voff) {
    asm volatile("global_load_dwordx4 v[%c3:%c4], %0, %1 offset:%2 nt" :: "v"(voff), "s"(sbase), "i"(IMM), "i"(R), "i"(R + 3) : "memory");
}
template <int R, int IMM> __device__ __forceinline__ void ld8_nt(const void* sbase, unsigned voff) {
    asm volatile("global_load_dwordx2 v[%c3:%c4], %0, %1 offset:%2 nt" :: "v"(voff), "s"(sbase), "i"(IMM), "i"(R), "i"(R + 1) : "memory");
}
template <int R> __device__ __forceinline__ void smf(f32x4& d, u32x4 a, unsigned idx) {
    asm volatile("v_smfmac_f32_16x16x64_bf16 %0, %1, v[%c3:%c4], %2" : "+v"(d) : "v"(a), "v"(idx), "i"(R), "i"(R + 7));
}
template <int A, int B> __device__ __forceinline__ void zip_in() {
    unsigned t;
    asm volatile("v_perm_b32 %0, v%c2, v%c1, %3\n\tv_perm_b32 v%c2, v%c2, v%c1, %4\n\tv_mov_b32 v%c1, %0" : "=&v"(t) : "i"(A), "i"(B), "s"(0x05040100u), "s"(0x07060302u));
}
template <int A, int B> __device__ __forceinline__ void zip_out(unsigned& lo, unsigned& hi) {
    asm volatile("v_perm_b32 %0, v%c3, v%c2, %4\n\tv_perm_b32 %1, v%c3, v%c2, %5" : "=&v"(lo), "=&v"(hi) : "i"(A), "i"(B), "s"(0x05040100u), "s"(0x07060302u));
}
template <int A, int B, int C, int D> __device__ __forceinline__ void zip_x(unsigned lo, unsigned hi) {
    asm volatile("v_perm_b32 v%c4, v%c3, v%c2, %6\n\tv_perm_b32 v%c5, v%c3, v%c2, %7\n\tv_mov_b32 v%c2, %0\n\tv_mov_b32 v%c3, %1"
                 :: "v"(lo), "v"(hi), "i"(A), "i"(B), "i"(C), "i"(D), "s"(0x05040100u), "s"(0x07060302u));
}
__device__ __forceinline__ void dma16(const void* sbase, unsigned voff, unsigned lds_dst) {
    unsigned keep;
    asm volatile("s_mov_b32 %0, m0\n\ts_mov_b32 m0, %3\n\ts_nop 0\n\tglobal_load_lds_dwordx4 %1, %2 nt\n\ts_mov_b32 m0, %0"
                 : "=&s"(keep) : "v"(voff), "s"(sbase), "s"(lds_dst) : "memory");
}
__device__ __forceinline__ void dma16p(const void* sbase, unsigned voff, unsigned lds_dst) {
    unsigned keep;
    asm volatile("s_mov_b32 %0, m0\n\ts_mov_b32 m0, %3\n\ts_nop 0\n\tglobal_load_lds_dwordx4 %1, %2\n\ts_mov_b32 m0, %0"
                 : "=&s"(keep) : "v"(voff), "s"(sbase), "s"(lds_dst) : "memory");
}
__device__ __forceinline__ unsigned um4(u32x4 a) { return max(max(a[0], a[1]), max(a[2], a[3])); }
__device__ __forceinline__ bool all_set(unsigned t) { return __builtin_amdgcn_ballot_w64(t == 0u) == 0ull; }
__device__ __forceinline__ bool any_empty(unsigned mx) { return __builtin_amdgcn_ballot_w64(mx == ~0u) != 0ull; }
__device__ __forceinline__ unsigned long long any_nan(float a, float b) { return __builtin_amdgcn_ballot_w64(__builtin_isunordered(a, b)); }
__device__ __forceinline__ f32x4 asf(u32x4 a) { return __builtin_bit_cast(f32x4, a); }
__device__ __forceinline__ u32x4 asu(f32x4 a) { return __builtin_bit_cast(u32x4, a); }
#define LANES(m) __builtin_amdgcn_inverse_ballot_w64(m)

__device__ __forceinline__ bf16x4 tr64(unsigned lds_addr) { return __builtin_amdgcn_ds_read_tr16_b64_v4bf16((LAS bf16x4*)(size_t)lds_addr); }
__device__ __forceinline__ bf16x8 cat8(bf16x4 a, bf16x4 b) { return __builtin_shufflevector(a, b, 0, 1, 2, 3, 4, 5, 6, 7); }
__device__ __forceinline__ bf16x8 lds16(unsigned a) { return *(const LAS bf16x8*)(size_t)a; }
__device__ __forceinline__ bf16x4 lds8(unsigned a) { return *(const LAS bf16x4*)(size_t)a; }
__device__ __forceinline__ f32x4 mfma32(bf16x8 a, bf16x8 b, f32x4 c) { return __builtin_amdgcn_mfma_f32_16x16x32_bf16(a, b, c, 0, 0, 0); }
__device__ __forceinline__ f32x4 mfma32u(u32x4 a, u32x4 b, f32x4 c) { return mfma32(__builtin_bit_cast(bf16x8, a), __builtin_bit_cast(bf16x8, b), c); }
__device__ __forceinline__ f32x4 mfma16(bf16x4 a, bf16x4 b, f32x4 c) {
    return __builtin_amdgcn_mfma_f32_16x16x16bf16_1k(__builtin_bit_cast(s16x4, a), __builtin_bit_cast(s16x4, b), c, 0, 0, 0);
}

#define STAMP(i) do {} while (0)

__device__ __forceinline__ void wait_word(const void* sbase, unsigned off) {
    unsigned t, cnt = kSpinCap;
    asm volatile("1:\n\tglobal_load_dword %0, %2, %3 sc1\n\ts_sub_u32 %1, %1, 1\n\ts_waitcnt vmcnt(0)\n\tv_cmp_eq_u32 vcc, -1, %0\n\ts_cbranch_vccz 2f\n\ts_cbranch_scc0 1b\n2:"
                 : "=&v"(t), "+s"(cnt) : "v"(off), "s"(sbase) : "memory", "vcc", "scc");
}
template <int OFF = 0> __device__ __forceinline__ void st16m(void* sbase, unsigned voff, u32x4 v, unsigned long long mask) {
    asm volatile("s_mov_b64 exec, %4\n\tglobal_store_dwordx4 %0, %1, %2 offset:%3 sc1\n\ts_mov_b64 exec, -1\n\ts_nop 0" :: "v"(voff), "v"(v), "s"(sbase), "i"(OFF), "s"(mask) : "memory");
}
__device__ __forceinline__ void st8m(void* sbase, unsigned voff, u32x2 v, unsigned long long mask) {
    asm volatile("s_mov_b64 exec, %3\n\tglobal_store_dwordx2 %0, %1, %2 sc1\n\ts_mov_b64 exec, -1" :: "v"(voff), "v"(v), "s"(sbase), "s"(mask) : "memory");
}
template <int OFF> __device__ __forceinline__ void st16m2(void* sbase, unsigned voff, u32x4 v, unsigned voff2, u32x4 v2, unsigned long long mask) {
    asm volatile("s_mov_b64 exec, %6\n\tglobal_store_dwordx4 %0, %1, %2 sc1\n\tglobal_store_dwordx4 %4, %5, %2 offset:%3 sc1\n\ts_mov_b64 exec, -1\n\ts_nop 0"
                 :: "v"(voff), "v"(v), "s"(sbase), "i"(OFF), "v"(voff2), "v"(v2), "s"(mask) : "memory");
}
#define PINS(a) asm volatile("" : "+s"(a))
#define RETRY(LOAD, EMPTY, COMPUTE, NAN_, X) do { LOAD; COMPUTE; if (__builtin_expect((NAN_) != 0ull, 0)) { \
    _Pragma("unroll 1") for (unsigned spins_ = 0; spins_ < kSpinCap; ++spins_) { slip(X); LOAD; if (!any_empty(EMPTY)) break; } COMPUTE; } } while (0)
__device__ __forceinline__ unsigned now() { return (unsigned)__builtin_amdgcn_s_memrealtime(); }
__device__ __forceinline__ void until(unsigned t) {
    asm volatile("1:\n\ts_memrealtime vcc\n\ts_waitcnt lgkmcnt(0)\n\ts_sub_u32 vcc_lo, %0, vcc_lo\n\ts_cmp_lt_u32 vcc_lo, 0x400\n\ts_cbranch_scc1 1b" :: "s"(t - 1u) : "vcc", "scc", "memory");
}
__device__ __forceinline__ unsigned lds_word(unsigned a) {
    unsigned t_; asm volatile("ds_read_b32 %0, %1\n\ts_waitcnt lgkmcnt(0)\n\ts_nop 0" : "=v"(t_) : "v"(a) : "memory");
    return __builtin_amdgcn_readfirstlane(t_);
}
__device__ __forceinline__ void lds_set(unsigned a, unsigned v) { asm volatile("ds_write_b32 %0, %1" :: "v"(a), "v"(v) : "memory"); }
__device__ __forceinline__ int swz(int r) { return r ^ (((r >> 3) & 1) << 2); }
__device__ __forceinline__ f32x2 unpk(unsigned w) { return (f32x2){lo16(w), hi16(w)}; }
__device__ __forceinline__ unsigned pk2v(f32x2 v) { return __builtin_bit_cast(unsigned, __builtin_convertvector(v, bf16x2)); }
__device__ __forceinline__ float frcp(float x) { return __builtin_amdgcn_rcpf(x); }
__device__ __forceinline__ float ex2(float x) { return __builtin_amdgcn_exp2f(x); }
__device__ __forceinline__ int lane_id() { int l; asm volatile("v_mbcnt_lo_u32_b32 %0, -1, 0\n\tv_mbcnt_hi_u32_b32 %0, -1, %0" : "=v"(l)); return l & 63; }
__device__ __forceinline__ f32x2 ln_stats(float ss, float s) {
    const float mean = s * (1.f / DM);
    const float var = fmax2(__builtin_fmaf(-mean, mean, ss * (1.f / DM)), 0.f);
    const unsigned mv = pk2(mean, var);
    return (f32x2){lo16(mv), frcp(bfr(__builtin_amdgcn_sqrtf(bfr(hi16(mv) + 1e-5f))))};
}
#define GATHER8(a, o, S) do { a[0] = ld16_sc1<-4 * (S)>(ws, o); a[1] = ld16_sc1<-3 * (S)>(ws, o); a[2] = ld16_sc1<-2 * (S)>(ws, o); a[3] = ld16_sc1<-(S)>(ws, o); \
    a[4] = ld16_sc1<0>(ws, o); a[5] = ld16_sc1<(S)>(ws, o); a[6] = ld16_sc1<2 * (S)>(ws, o); a[7] = ld16_sc1<3 * (S)>(ws, o); } while (0)
template <int OFF> __device__ __forceinline__ void again16(u32x4& r, const void* sbase, unsigned voff) {
    asm volatile("v_cmp_eq_u32 vcc, -1, %1\n\ts_mov_b64 exec, vcc\n\tglobal_load_dwordx4 %0, %2, %3 offset:%4 sc1\n\ts_mov_b64 exec, -1" : "+v"(r) : "v"(um4(r)), "v"(voff), "s"(sbase), "i"(OFF) : "memory", "vcc");
}
#define AGAIN8(a, o, S) do { again16<-4 * (S)>(a[0], ws, o); again16<-3 * (S)>(a[1], ws, o); again16<-2 * (S)>(a[2], ws, o); again16<-(S)>(a[3], ws, o); \
    again16<0>(a[4], ws, o); again16<(S)>(a[5], ws, o); again16<2 * (S)>(a[6], ws, o); again16<3 * (S)>(a[7], ws, o); } while (0)
#define WAIT8(a, N) asm volatile("s_waitcnt vmcnt(%8)" : "+v"((a)[0]), "+v"((a)[1]), "+v"((a)[2]), "+v"((a)[3]), "+v"((a)[4]), "+v"((a)[5]), "+v"((a)[6]), "+v"((a)[7]) : "i"(N) : "memory")
#define UM32(a) max(max(max(um4(a[0]), um4(a[1])), max(um4(a[2]), um4(a[3]))), max(max(um4(a[4]), um4(a[5])), max(um4(a[6]), um4(a[7]))))
#define SUM8(a) (((((((asf(a[0]) + asf(a[1])) + asf(a[2])) + asf(a[3])) + asf(a[4])) + asf(a[5])) + asf(a[6])) + asf(a[7]))
#define WAIT1(x, N) asm volatile("s_waitcnt vmcnt(%1)" : "+v"(x) : "i"(N) : "memory")
#define LO2(x) __builtin_shufflevector(x, x, 0, 1)
#define HI2(x) __builtin_shufflevector(x, x, 2, 3)
#define ACC8(a, s) do { f32x2 l_, h_; \
    asm volatile("s_waitcnt vmcnt(5)\n\tv_pk_add_f32 %0, %2, %4\n\tv_pk_add_f32 %1, %3, %5\n\tv_pk_add_f32 %0, %0, %6\n\tv_pk_add_f32 %1, %1, %7\n\t" \
                 "s_waitcnt vmcnt(3)\n\tv_pk_add_f32 %0, %0, %8\n\tv_pk_add_f32 %1, %1, %9\n\tv_pk_add_f32 %0, %0, %10\n\tv_pk_add_f32 %1, %1, %11\n\t" \
                 "s_waitcnt vmcnt(1)\n\tv_pk_add_f32 %0, %0, %12\n\tv_pk_add_f32 %1, %1, %13\n\tv_pk_add_f32 %0, %0, %14\n\tv_pk_add_f32 %1, %1, %15\n\t" \
                 "s_waitcnt vmcnt(0)\n\tv_pk_add_f32 %0, %0, %16\n\tv_pk_add_f32 %1, %1, %17\n\ts_nop 0" \
                 : "=&v"(l_), "=&v"(h_) : "v"(LO2(a[0])), "v"(HI2(a[0])), "v"(LO2(a[1])), "v"(HI2(a[1])), "v"(LO2(a[2])), "v"(HI2(a[2])), "v"(LO2(a[3])), "v"(HI2(a[3])), \
                   "v"(LO2(a[4])), "v"(HI2(a[4])), "v"(LO2(a[5])), "v"(HI2(a[5])), "v"(LO2(a[6])), "v"(HI2(a[6])), "v"(LO2(a[7])), "v"(HI2(a[7])) : "memory"); \
    s = __builtin_shufflevector(l_, h_, 0, 1, 2, 3); } while (0)
#define RETRY8(a, o, S, s, MID, POST, NAN_, X) do { GATHER8(a, o, S); MID; ACC8(a, s); POST; if (__builtin_expect((NAN_) != 0ull, 0)) { \
    _Pragma("unroll 1") for (unsigned spins_ = 0; spins_ < kSpinCap; ++spins_) { slip(X); AGAIN8(a, o, S); WAIT8(a, 0); if (!any_empty(UM32(a))) break; } s = SUM8(a); POST; } } while (0)
#define PIN1(a) asm volatile("" : "+v"(a))
#define PIN2(a, b) asm volatile("" : "+v"(a), "+v"(b))
#define PIN3(a, b, c) asm volatile("" : "+v"(a), "+v"(b), "+v"(c))
#define LANE_VARS() const int lane = lane_id(); \
    const int kq = lane >> 4, n = lane & 15, q4 = n >> 2, p4 = lane & 3; \
    const int r0s = 8 * kq + q4 + 4 * (kq & 1), r1s = 8 * kq + q4 + 4 * ((kq & 1) ^ 1);     \
    const int r2s = swz(4 * kq + q4);                                                       \
    (void)r0s; (void)r1s; (void)r2s; (void)p4
template <int V> struct IC { static constexpr int value = V; };
struct Frag80 { bf16x8 a0, a1; bf16x4 a2; };

__global__ void __launch_bounds__(512, 2) __attribute__((amdgpu_num_vgpr(32)))
mega_kernel(const u16* __restrict__ x0, const u16* __restrict__ kc, const u16* __restrict__ vc, const u16* __restrict__ wq,
            const u16* __restrict__ wo, const u16* __restrict__ wr, const u16* __restrict__ wg, const u16* __restrict__ wu,
            const u16* __restrict__ wd, u16* __restrict__ out, unsigned char* __restrict__ ws_in
            ) {
    extern __shared__ __attribute__((aligned(16))) unsigned char smem[];
    const unsigned lb = (unsigned)(size_t)smem;
    unsigned char* const ws = (unsigned char*)(((uintptr_t)ws_in + (32u << 20) - 1) & ~(uintptr_t)((16u << 20) - 1));
    const int wave = __builtin_amdgcn_readfirstlane(threadIdx.x >> 6);
    const int g = blockIdx.x, gj = GJ(g), gi = GI(g);
    const f32x4 z4 = {0.f, 0.f, 0.f, 0.f};
    asm volatile("" ::: "v255");
    auto sched = [&](unsigned X, unsigned gap) {
        unsigned v; unsigned long long c;
        asm volatile("s_memrealtime %1\n\tds_read_b32 %0, %2\n\ts_waitcnt lgkmcnt(0)" : "=v"(v), "=&s"(c) : "v"(lb + L_CNT + 56) : "memory");
        const unsigned n = (unsigned)c + gap;
        unsigned t = __builtin_amdgcn_readfirstlane(v) + X;
        if ((int)(n - t) > 0) t = n;
        until(t);
    };
    auto slip = [&](unsigned X) {
        if (X) lds_set(lb + L_CNT + 56, now() - X);
    };
    float xe = 0.f;

    auto code_pf = [&](int slice, int lane) {
        unsigned long long pc0; unsigned len, back, t;
        asm volatile("s_getpc_b64 %0\n.Lmega_pf%=:\n\ts_mov_b32 %1, .Lmega_code_end-.Lmega_code_begin\n\ts_mov_b32 %2, .Lmega_pf%=-.Lmega_code_begin" : "=s"(pc0), "=s"(len), "=s"(back));
        unsigned off = (unsigned)(slice * 4096 + lane * 64); if (off > len) off = len;
        asm volatile("global_load_dword %0, %1, %2" : "=v"(t) : "v"(off & ~3u), "s"(pc0 - back) : "memory");
        return t;
    };
    asm volatile(".Lmega_code_begin:");
    auto fix_k1 = [&](auto IC_, auto NC) {
        constexpr int A = RK + 8 * decltype(IC_)::value, B = A + 4;
        unsigned t0, t1, t2, t3;
        asm volatile("s_waitcnt vmcnt(%12)\n\tv_mov_b32 %0, v%c4\n\tv_mov_b32 %1, v%c5\n\tv_mov_b32 %2, v%c6\n\tv_mov_b32 %3, v%c7\n\t"
                     "v_mov_b32_dpp v%c4, v%c8 row_ror:8 row_mask:0xf bank_mask:0xc\n\tv_mov_b32_dpp v%c5, v%c9 row_ror:8 row_mask:0xf bank_mask:0xc\n\t"
                     "v_mov_b32_dpp v%c6, v%c10 row_ror:8 row_mask:0xf bank_mask:0xc\n\tv_mov_b32_dpp v%c7, v%c11 row_ror:8 row_mask:0xf bank_mask:0xc\n\t"
                     "v_mov_b32_dpp v%c8, %0 row_ror:8 row_mask:0xf bank_mask:0x3\n\tv_mov_b32_dpp v%c9, %1 row_ror:8 row_mask:0xf bank_mask:0x3\n\t"
                     "v_mov_b32_dpp v%c10, %2 row_ror:8 row_mask:0xf bank_mask:0x3\n\tv_mov_b32_dpp v%c11, %3 row_ror:8 row_mask:0xf bank_mask:0x3"
                     : "=&v"(t0), "=&v"(t1), "=&v"(t2), "=&v"(t3)
                     : "i"(A), "i"(A + 1), "i"(A + 2), "i"(A + 3), "i"(B), "i"(B + 1), "i"(B + 2), "i"(B + 3), "i"(decltype(NC)::value) : "memory");
    };
    auto trans_v = [&](auto UC, auto NC) {
        constexpr int R = RV + 4 * decltype(UC)::value, N = decltype(NC)::value;
#define CELL(r, c) R + 16 * (r) + (c), R + 16 * (r) + 8 + (c)
        unsigned a0, a1, b0, b1, c0, c1;
        VMW(N + 6); zip_in<CELL(0, 0)>(); zip_out<CELL(0, 1)>(a0, a1); zip_out<CELL(0, 2)>(b0, b1); zip_out<CELL(0, 3)>(c0, c1);
        VMW(N + 4); zip_x<CELL(1, 0), CELL(0, 1)>(a0, a1); zip_in<CELL(1, 1)>(); zip_out<CELL(1, 2)>(a0, a1);
        VMW(N + 2); zip_x<CELL(2, 0), CELL(0, 2)>(b0, b1); zip_x<CELL(2, 1), CELL(1, 2)>(a0, a1); zip_in<CELL(2, 2)>(); zip_out<CELL(1, 3)>(a0, a1); zip_out<CELL(2, 3)>(b0, b1);
        VMW(N); zip_x<CELL(3, 0), CELL(0, 3)>(c0, c1); zip_x<CELL(3, 1), CELL(1, 3)>(a0, a1); zip_x<CELL(3, 2), CELL(2, 3)>(b0, b1); zip_in<CELL(3, 3)>();
#undef CELL
    };
    auto trans_w = [&](auto UC, auto NC) {
        constexpr int R = RV + 64 + 2 * decltype(UC)::value, N = decltype(NC)::value;
        unsigned a0, a1;
        VMW(N + 2); zip_in<R, R + 8>(); zip_out<R + 1, R + 9>(a0, a1);
        VMW(N); zip_x<R + 16, R + 24, R + 1, R + 9>(a0, a1); zip_in<R + 17, R + 25>();
    };
    auto frag80 = [&](unsigned im, int rs, int r0s, int r1s, int r2s) -> Frag80 {
        return Frag80{cat8(tr64(im + r0s * rs), tr64(im + r1s * rs)), cat8(tr64(im + (r0s + 32) * rs), tr64(im + (r1s + 32) * rs)), tr64(im + (r2s + 64) * rs)};
    };
    auto mm80 = [&](const Frag80& a, const Frag80& b) -> f32x4 {
        return mfma32(a.a1, b.a1, mfma32(a.a0, b.a0, z4)) + mfma16(a.a2, b.a2, z4);
    };
    auto rows80 = [&](unsigned a16, unsigned a8) -> Frag80 { const Frag80 f{lds16(a16), lds16(a16 + 64), lds8(a8)}; __builtin_amdgcn_sched_barrier(0); return f; };
    auto empty35 = [&](int layer, unsigned S, int lane) {
        st16_sc1(ws, tile35(gj, 10 * layer + (lane >> 3)) + S + (unsigned)(gi * 128 + (lane & 7) * 16), (u32x4){~0u, ~0u, ~0u, ~0u});
        st16m(ws, S + (lane < 16 ? tile35(gj, 10 * layer + 8 + (lane >> 3)) + (unsigned)(gi * 128 + (lane & 7) * 16) : WS_OWN + (unsigned)(gi * 16384 + layer * 4096 + gj * 512 + (lane - 16) * 16)),
              (u32x4){~0u, ~0u, ~0u, ~0u}, 0xffffffffffffull);
    };
    auto empty5 = [&](int q0, unsigned S, int lane) {
        st16m(ws, tile35(gj, q0 + (lane >> 3)) + S + (unsigned)(gi * 128 + (lane & 7) * 16), (u32x4){~0u, ~0u, ~0u, ~0u}, 0xffffffffffull);
    };
    auto empty4 = [&](int layer, unsigned S, int lane) {
        const unsigned b = WS_H4 + (unsigned)((layer * 32 + gi) * 32768) + (S ^ 8192u);
        st16_sc1(ws, b + (unsigned)((lane >> 5) * 4096 + gj * 512 + (lane & 31) * 16), (u32x4){~0u, ~0u, ~0u, ~0u});
        st16m(ws, b + (unsigned)(lane < 32 ? 16384 + gj * 512 + lane * 16 : H4C + ((lane - 32) >> 3) * 1024 + gj * 128 + (lane & 7) * 16), (u32x4){~0u, ~0u, ~0u, ~0u},
              layer ? 0xff000000ffffffffull : ~0ull);
    };
    const unsigned tst = now();
    int lead = wave == 7 ? LEAD * PACE * 4 : 0;
    auto pace = [&](unsigned n, bool wait = true) {
        unsigned u, w, t0;
        asm volatile("s_mov_b64 exec, 1\n\tds_add_rtn_u32 %0, %3, %4\n\tds_max_u32 %3, %5 offset:12\n\tds_add_rtn_u32 %1, %3, %6 offset:12\n\tds_read_b32 %2, %3 offset:4\n\ts_mov_b64 exec, -1\n\ts_waitcnt lgkmcnt(0)"
                     : "=&v"(u), "=&v"(w), "=&v"(t0) : "v"(lb + L_CNT + 60), "v"(n), "v"((now() - tst) << 6), "v"(n * PMIN * 4u) : "memory");
        int tn = (int)(__builtin_amdgcn_readfirstlane(t0) - tst + PINIT) * 64 + __builtin_amdgcn_readfirstlane(u) * (PACE * 4);
        tn += max((int)__builtin_amdgcn_readfirstlane(u) - UG, 0) * (SLOW * 4);
        const int tw = __builtin_amdgcn_readfirstlane(w) - PBURST * 64;
        if (tw > tn) tn = tw;
        tn -= lead;
        if (wait) {
#pragma unroll 1
            for (int k = 0; k < 2048 && (int)((now() - tst) << 6) < tn; ++k) {}
        }
    };
    auto slot = [&](unsigned u) {
        const unsigned tn = lds_word(lb + L_CNT + 64) + PINIT + (u * PACE + (NUNIT - UG) * SLOW) / 16;
#pragma unroll 1
        for (int k = 0; k < 8192 && (int)(now() - tn) < 0; ++k) __builtin_amdgcn_s_sleep(1);
    };
    auto load_wo = [&](int k, int lane, bool early) {
        const int c = k * 64 + lane, pp = c / 10, cc = c - 10 * pp;
        const unsigned f = (unsigned)(swz(pp) * 1280 + cc * 16);
        const u16* wb = wo + (size_t)gi * DH * DM + gj * 80;
#pragma unroll
        for (int a2 = 0; a2 < 6; ++a2) { if (a2 % 3 == 0) { if (early) pace(3); else slot(NUNIT - WOFF + (2 * k + a2 / 3) * WOC); } dma16(wb + 32 * a2 * DM, f, lb + L_WO + (5 * a2 + k) * 1024); }
    };
    auto load_wd = [&](int lane) {
        const u16* db = wd + gi * 48 * DM + gj * 80;
#pragma unroll 1
        for (int k = 0; k < 5; ++k) {
            const int c = k * 64 + lane, pp = c / 10, cc = c - 10 * pp;
            const unsigned fd = (unsigned)(swz(pp) * 1280 + cc * 16);
            if (!(k & 1)) pace(3);
            dma16(db, fd, lb + L_WD + k * 1024);
            if (k < 3) dma16(db + 32 * DM, (k < 2 || lane < 32) ? fd : 0u, lb + L_WD + (5 + k) * 1024);
        }
    };
    auto load_gu = [&](int m, int lane) {
#pragma unroll
        for (int k = 0; k < 3; ++k) {
            const int c = k * 64 + lane, row = c / 6, cc = c - 6 * row;
            const unsigned o = (unsigned)((swz(row) * FF + cc * 8) * 2), d = lb + L_GU + m * 8192 + k * 1024;
            const u16* wb = (m == 0 ? wg : wu) + gj * 80 * FF + gi * 48;
            pace(3);
            dma16(wb, o, d); dma16(wb + 32 * FF, o, d + 3072);
            if (k < 2) dma16(wb + 64 * FF, (k == 0 || lane < 32) ? o : 0u, d + 6144);
        }
    };
    auto lds_add = [&](int off, unsigned v) { asm volatile("s_mov_b64 exec, 1\n\tds_add_u32 %0, %1\n\ts_mov_b64 exec, -1" :: "v"(lb + L_CNT + off), "v"(v) : "memory"); };
    auto landed = [&]() {
        asm volatile("ds_max_u32 %0, %1" :: "v"(lb + L_CNT + 64), "v"(now() - TLAND) : "memory");
        lds_add(0, 1u);
    };
    auto send_flag = [&](int lane) {
        st4_sc1(ws, WS_MB + (unsigned)((gi * 8 + (lane & 7)) * 128 + gj * 4), lds_word(lb + L_CNT + 68));
    };
    auto tiles4 = [&](const Frag80 (&f)[4], const Frag80& b, unsigned long long mk) __attribute__((always_inline)) -> u32x4 {
        f32x4 c[4], d[4];
#pragma unroll
        for (int i = 0; i < 4; ++i) c[i] = mfma32(f[i].a0, b.a0, z4);
#pragma unroll
        for (int i = 0; i < 4; ++i) { c[i] = mfma32(f[i].a1, b.a1, c[i]); d[i] = mfma16(f[i].a2, b.a2, z4); }
        __builtin_amdgcn_sched_barrier(0);
        f32x2 lo, hi;
        asm volatile("s_nop 1\n\ts_mov_b64 exec, %18\n\tv_pk_add_f32 %0, %2, %10\n\tv_pk_add_f32 %1, %3, %11\n\t"
                     "s_lshl_b64 exec, exec, 4\n\tv_pk_add_f32 %0, %4, %12\n\tv_pk_add_f32 %1, %5, %13\n\t"
                     "s_lshl_b64 exec, exec, 4\n\tv_pk_add_f32 %0, %6, %14\n\tv_pk_add_f32 %1, %7, %15\n\t"
                     "s_lshl_b64 exec, exec, 4\n\tv_pk_add_f32 %0, %8, %16\n\tv_pk_add_f32 %1, %9, %17\n\ts_mov_b64 exec, -1"
                     : "=&v"(lo), "=&v"(hi) : "v"(LO2(c[0])), "v"(HI2(c[0])), "v"(LO2(c[1])), "v"(HI2(c[1])), "v"(LO2(c[2])), "v"(HI2(c[2])), "v"(LO2(c[3])), "v"(HI2(c[3])),
                       "v"(LO2(d[0])), "v"(HI2(d[0])), "v"(LO2(d[1])), "v"(HI2(d[1])), "v"(LO2(d[2])), "v"(HI2(d[2])), "v"(LO2(d[3])), "v"(HI2(d[3])), "s"(mk) : "scc");
        return asu(__builtin_shufflevector(lo, hi, 0, 1, 2, 3));
    };
    auto empty1 = [&](int half, unsigned S, int lane) {
        const unsigned o = WS_H1 + S + (unsigned)((gi * 4 + 2 * half) * 16384 + gj * 896 + min(lane, 55) * 16);
        st16_sc1(ws, o, (u32x4){~0u, ~0u, ~0u, ~0u}); st16_sc1(ws, o + 16384, (u32x4){~0u, ~0u, ~0u, ~0u});
    };
    auto report = [&]() { if (gj == (gi & 7)) st4_sc1(ws, WS_R + (unsigned)(gi * 4), lds_word(lb + L_CNT + 88)); };
    auto answer = [&](int lane, unsigned r) {
        int d = min(max((int)(r - tst), -400), 400) * 512 + (int)((r >> 1) & 15u);
        d += (int)dppu<0xB1>((unsigned)d); d += (int)dppu<0x4E>((unsigned)d); d += (int)dppu<0x141>((unsigned)d); d += (int)dppu<0x140>((unsigned)d);
        d = __builtin_amdgcn_readlane(d, 0) + __builtin_amdgcn_readlane(d, 16);
        st4_sc1(ws, WS_MB + (unsigned)(((lane & 31) * 8 + (lane & 7)) * 128 + 32), ((tst + (d >> 14) + TOFF) & ~3u) | ((d & 511) > 128 ? 3u : 1u));
        lds_set(lb + L_CNT + 36, 1u);
    };
    auto adopt = [&](unsigned t) {
        if (ADOPT && t - tst + 256u < 768u) lds_set(lb + L_CNT + 64, t);
        lds_set(lb + L_CNT + 80, lds_word(lb + L_CNT + 92) ^ (t & 2u) << 12);
        lds_set(lb + L_CNT + 84, (((t << 1) ^ lds_word(lb + L_CNT + 68)) & 4u) << 11);
        lds_set(lb + L_CNT + 52, 1u);
    };
    struct StatAddr { unsigned a16, b16, a8, b8, dst; };
    auto stats_addr = [&](int lane, unsigned dbase, int dstride) -> StatAddr {
        const int kq = lane >> 4, q4 = (lane >> 2) & 3, p4 = lane & 3;
        const int x1 = LANES(0x4444444444444444ull) ? L_WR + gj * 160 : L_XS + q4 * 160, xa = LANES(0xaaaaaaaaaaaaaaaaull) ? L_XS + 640 : x1;
        StatAddr r{lb + (unsigned)(xa + kq * 16), lb + (unsigned)(L_XS + p4 * 160 + kq * 16), lb + (unsigned)(xa + 128 + kq * 8), lb + (unsigned)(L_XS + p4 * 160 + 128 + kq * 8), dbase + (unsigned)(kq * dstride)};
        PIN3(r.a16, r.b16, r.dst); PIN2(r.a8, r.b8);
        return r;
    };
    auto stats = [&](const StatAddr& r) {
        const bf16x8 a0 = lds16(r.a16), b0 = lds16(r.b16), a1 = lds16(r.a16 + 64), b1 = lds16(r.b16 + 64);
        const bf16x4 a2 = lds8(r.a8), b2 = lds8(r.b8);
        unsigned long long mk = 0x0008000400020001ull; PINS(mk);
        __builtin_amdgcn_sched_barrier(0);
        st16m(ws, r.dst, asu(mfma32(a1, b1, mfma32(a0, b0, z4)) + mfma16(a2, b2, z4)), mk);
    };
    unsigned qa, pw, ma, idx, qm;
    auto qk_addr = [&](bool L1) {
        LANE_VARS();
        qa = lb + L_Q + (unsigned)(kq * 32 + (lane & 2) * 8);
        pw = lb + L_P + (unsigned)((7 + 8 * wave) * 192 + (kq >> 1) * 64 + (n >> 3) * 32 + (lane & 2) * 8 + (q4 & 1) * 8 + (kq & 1) * 4 + (lane & 1) * 2);
        ma = lb + L_M + (unsigned)(wave * 4);
        idx = (lane & 2) ? 0xeeeeu : 0x4444u;
        qm = lb + L_Q;
        PIN3(qa, pw, ma); PIN2(idx, qm);
        if (L1 && wave < 4) __builtin_amdgcn_s_setprio(2);
    };
    auto att_qk0 = [&]() {
        qk_addr(false);
#pragma unroll 1
        for (unsigned spins = 0; spins < (kSpinCap << 4); ++spins) {
            if (lds_word(lb + L_CNT + 4) != 0u) break;
            __builtin_amdgcn_s_sleep(1);
        }
        STAMP(3);
        const u32x4 q0 = *(const LAS u32x4*)(size_t)qa, q1 = *(const LAS u32x4*)(size_t)(qa + 128), q2 = *(const LAS u32x4*)(size_t)(qa + 256);
        f32x4 c0 = z4, c1 = z4, c2 = z4, c3 = z4;
#define SMF(c, q, r) "v_smfmac_f32_16x16x64_bf16 %" #c ", %" #q ", v[" #r "], %7\n\t"
        asm volatile("s_nop 1\n\t" SMF(0, 4, 64:71) SMF(1, 4, 88:95) SMF(2, 4, 112:119) SMF(3, 4, 136:143) SMF(0, 5, 72:79) SMF(1, 5, 96:103) SMF(2, 5, 120:127) SMF(3, 5, 144:151)
                     SMF(0, 6, 80:87) SMF(1, 6, 104:111) SMF(2, 6, 128:135) SMF(3, 6, 152:159) "s_nop 7"
                     : "+v"(c0), "+v"(c1), "+v"(c2), "+v"(c3) : "v"(q0), "v"(q1), "v"(q2), "v"(idx));
#undef SMF
        const float s0 = c0[0] + c0[2], s1 = c1[0] + c1[2], s2 = c2[0] + c2[2], s3 = c3[0] + c3[2];
        float m = fmax2(fmax3(s0, s1, s2), s3);
        m = fmax2(m, dpp<0xB1>(m)); m = fmax2(m, dpp<0x4E>(m)); m = fmax2(m, dpp<0x124>(m)); m = fmax2(m, dpp<0x128>(m));
        const float s01 = LANES(0xffff0000ffff0000ull) ? s1 : s0, s23 = LANES(0xffff0000ffff0000ull) ? s3 : s2;
        const float sv = LANES(0xffffffff00000000ull) ? s23 : s01;
        m = bfr(bfr(m) * SCALE);
        const float p = ex2((bfr(bfr(sv) * SCALE) - m) * LOG2E);
        *(LAS u16*)(size_t)pw = (u16)pk2(p, p);
        *(LAS float*)(size_t)ma = m;
        __builtin_amdgcn_sched_barrier(0);
    };
    unsigned pr, oa, oc, cma, cxa, cra, cso;
    auto att_addr = [&]() {
        LANE_VARS();
        pr = lb + L_P + (unsigned)((7 + 8 * wave - 2 * q4 - (lane & 1)) * 192 + kq * 32 + (lane & 2) * 8);
        oa = lb + L_OP + (unsigned)(wave * 1024 + (lane & 7) * 64 + kq * 16 + (n >> 3) * 8);
        oc = lb + L_OP + (unsigned)(wave * 1024 + 512 + (kq >> 1) * 256 + (lane & 7) * 32 + (kq & 1) * 16 + (n >> 3) * 8);
        PIN3(pr, oa, oc);
    };
    auto comb_out = [&](float u0, float u1, unsigned so, unsigned long long mk, bool both) __attribute__((always_inline)) {
        const unsigned w = min(pk2(u0, u1), 0xfffffffeu);
        const u32x4 w4 = {both ? dppu<0x00>(w) : w, dppu<0x55>(w), dppu<0xAA>(w), dppu<0xFF>(w)};
        st16m(ws, so, w4, mk);
    };
    auto max8 = [&](f32x4 m0, f32x4 m1) __attribute__((always_inline)) { return fmax3(fmax3(m0[0], m0[1], m0[2]), fmax3(m0[3], m1[0], m1[1]), fmax2(m1[2], m1[3])) * LOG2E; };
    auto comb_o0 = [&](f32x4 m0, f32x4 m1, f32x4 mm, f32x4 o0, f32x4 o1, f32x4 o2, f32x4 o3, unsigned so, unsigned long long mk, bool both) __attribute__((always_inline)) {
        const float M = max8(m0, m1);
        f32x4 f;
#pragma unroll
        for (int k = 0; k < 4; ++k) f[k] = ex2(__builtin_fmaf(mm[k], LOG2E, -M));
        const f32x4 o = f[3] * o3 + (f[2] * o2 + (f[1] * o1 + f[0] * o0));
        comb_out(rs32(o[0], o[2]), rs32(o[1], o[3]), so, mk, both);
    };
    auto comb_o1 = [&](f32x4 m0, f32x4 m1, f32x2 mm, f32x4 o0, f32x4 o1, unsigned so, unsigned long long mk, bool both) __attribute__((always_inline)) {
        const float M = max8(m0, m1);
        const f32x4 o = ex2(__builtin_fmaf(mm[1], LOG2E, -M)) * o1 + ex2(__builtin_fmaf(mm[0], LOG2E, -M)) * o0;
        comb_out(x16_add(rs32(o[0], o[2])), x16_add(rs32(o[1], o[3])), so, mk, both);
    };
    auto ml_addr = [&](int lane, unsigned& xa, unsigned& ra) {
        xa = lb + L_M + (unsigned)(((lane & 15) >> 1) * 4); ra = lb + L_P + (unsigned)((7 + 8 * ((lane & 15) >> 1)) * 192 + (lane & 1) * 64 + (lane >> 4) * 16);
    };
    auto comb_ml = [&](unsigned ma, unsigned xa, unsigned ra, unsigned so, bool both) __attribute__((always_inline)) {
        const u32x4 x4 = *(const LAS u32x4*)(size_t)ra;
        const f32x4 m0 = *(const LAS f32x4*)(size_t)ma, m1 = *(const LAS f32x4*)(size_t)(ma + 16);
        const float mw = *(const LAS float*)(size_t)xa;
        __builtin_amdgcn_sched_barrier(0);
        const f32x4 sp = mfma32u((u32x4){0x3f803f80u, 0x3f803f80u, 0x3f803f80u, 0x3f803f80u}, x4, z4);
        __builtin_amdgcn_sched_barrier(0);
        const float M = max8(m0, m1);
        float L = row16_sum(sp[0] * ex2(__builtin_fmaf(mw, LOG2E, -M)));
        if (both) L = dpp<0x00>(L);
        st16m(ws, so, asu((f32x4){M, L, M, L}), both ? 3ull : 1ull);
    };
    auto att_pv0 = [&]() {
        u32x4 bm[8];
        trans_v(IC<0>{}, IC<24>{}); trans_v(IC<1>{}, IC<16>{});
#pragma unroll
        for (int e = 0; e < 8; ++e) bm[e] = *(const LAS u32x4*)(size_t)(pr + e * 192);
        __builtin_amdgcn_sched_barrier(0);
        f32x4 da = z4, dc = z4;
        PIN2(da, dc);
        asm volatile("s_nop 1");
        __builtin_amdgcn_sched_barrier(0);
        f32x2 r;
        smf<RV>(da, bm[0], idx); smf<RV + 8>(da, bm[1], idx); smf<RV + 16>(da, bm[2], idx); smf<RV + 24>(da, bm[3], idx);
        smf<RV + 32>(da, bm[4], idx); smf<RV + 40>(da, bm[5], idx); smf<RV + 48>(da, bm[6], idx); smf<RV + 56>(da, bm[7], idx);
        asm volatile("s_nop 7\n\tv_pk_add_f32 %0, %1, %2\n\tds_write_b64 %3, %0\n\ts_mov_b64 exec, 1\n\tds_add_u32 %4, %5\n\ts_mov_b64 exec, -1"
                     : "=&v"(r) : "v"(LO2(da)), "v"(HI2(da)), "v"(oa), "v"(lb + L_CNT + 24), "v"(1u) : "memory");
        trans_w(IC<0>{}, IC<12>{}); trans_w(IC<1>{}, IC<8>{}); trans_w(IC<2>{}, IC<4>{}); trans_w(IC<3>{}, IC<0>{});
        STAMP(4);
        asm volatile("s_nop 1\n\tv_smfmac_f32_16x16x64_bf16 %0, %1, v[224:231], %5\n\tv_smfmac_f32_16x16x64_bf16 %0, %2, v[232:239], %5\n\t"
                     "v_smfmac_f32_16x16x64_bf16 %0, %3, v[240:247], %5\n\tv_smfmac_f32_16x16x64_bf16 %0, %4, v[248:255], %5\n\ts_nop 6"
                     : "+v"(dc) : "v"(bm[0]), "v"(bm[1]), "v"(bm[2]), "v"(bm[3]), "v"(idx));
        asm volatile("s_nop 0\n\tv_pk_add_f32 %0, %1, %2\n\tds_write_b64 %3, %0" : "=&v"(r) : "v"(LO2(dc)), "v"(HI2(dc)), "v"(oc) : "memory");
        STAMP(20);
    };
    unsigned long long ck1 = 0xffff0000ffff0000ull, ck2 = 0xffffffff00000000ull, csc = 0x3d93cd3a3d93cd3aull;
    auto att1 = [&]() {
        constexpr int layer = 1;
        {
#define SMF(c, q, r) "v_smfmac_f32_16x16x64_bf16 v[" #c "], v[" #q "], v[" #r "], %0\n\t"
#define MAXD(x) "v_max_f32_dpp v41, v41, v41 " x " row_mask:0xf bank_mask:0xf\n\t"
            asm volatile("s_waitcnt lgkmcnt(0)\n\ts_barrier\n\t"
                         "ds_read_b128 v[0:3], %1\n\tds_read2_b32 v[12:13], %2 offset0:96 offset1:100\n\tds_read_b128 v[4:7], %1 offset:128\n\tds_read_b128 v[8:11], %1 offset:256\n\t"
                         "v_mov_b64 v[16:17], 0\n\tv_mov_b64 v[18:19], 0\n\tv_mov_b64 v[20:21], 0\n\tv_mov_b64 v[22:23], 0\n\tv_mov_b64 v[24:25], 0\n\tv_mov_b64 v[26:27], 0\n\t"
                         "v_mov_b64 v[28:29], 0\n\tv_mov_b64 v[30:31], 0\n\tv_mov_b64 v[32:33], 0\n\tv_mov_b64 v[34:35], 0\n\tv_mov_b64 v[36:37], 0\n\tv_mov_b64 v[38:39], 0\n\t"
                         "s_waitcnt lgkmcnt(3)\n\t" SMF(16:19, 0:3, 64:71) SMF(20:23, 0:3, 88:95) "s_waitcnt lgkmcnt(1)\n\tv_mul_f32 v13, 0x3acccccd, v13\n\t"
                         SMF(24:27, 0:3, 112:119) "v_mul_f32 v12, 0x3acccccd, v12\n\t" SMF(28:31, 0:3, 136:143) "v_fma_f32 v12, -v13, v13, v12\n\t"
                         SMF(16:19, 4:7, 72:79) "v_maximum3_f32 v12, v12, 0, 0\n\t" SMF(20:23, 4:7, 96:103) "v_cvt_pk_bf16_f32 v12, 0, v12\n\t"
                         SMF(24:27, 4:7, 120:127) "v_add_f32 v12, 0x3727c5ac, v12\n\t" SMF(28:31, 4:7, 144:151) "v_cvt_pk_bf16_f32 v12, 0, v12\n\ts_waitcnt lgkmcnt(0)\n\t"
                         SMF(16:19, 8:11, 80:87) "v_sqrt_f32 v12, v12\n\t" SMF(20:23, 8:11, 104:111) "v_cvt_pk_bf16_f32 v12, 0, v12\n\t"
                         SMF(24:27, 8:11, 128:135) "v_rcp_f32 v12, v12\n\t" SMF(28:31, 8:11, 152:159)
                         "s_nop 2\n\tv_add_f32 v42, v16, v18\n\ts_nop 0\n\tv_add_f32 v43, v20, v22\n\tv_max_f32 v41, v42, v43\n\tv_add_f32 v44, v24, v26\n\tv_add_f32 v45, v28, v30\n\t"
                         "v_max3_f32 v41, v41, v44, v45\n\tv_cndmask_b32 v42, v42, v43, %8\n\tv_cndmask_b32 v44, v44, v45, %8\n\t"
                         MAXD("quad_perm:[1,0,3,2]") "v_cndmask_b32 v40, v42, v44, %9\n\ts_nop 0\n\t" MAXD("quad_perm:[2,3,0,1]") "s_nop 1\n\t" MAXD("row_ror:4") "s_nop 1\n\t" MAXD("row_ror:8")
                         "v_pk_mul_f32 v[40:41], v[12:13], v[40:41] op_sel_hi:[0,1]\n\tv_cvt_pk_bf16_f32 v40, 0, v40\n\tv_cvt_pk_bf16_f32 v41, 0, v41\n\t"
                         "v_pk_mul_f32 v[40:41], %10, v[40:41]\n\tv_cvt_pk_bf16_f32 v40, 0, v40\n\tv_cvt_pk_bf16_f32 v41, 0, v41\n\t"
                         "v_sub_f32 v40, v40, v41\n\tv_mul_f32 v40, 0x3fb8aa3b, v40\n\tv_exp_f32 v40, v40\n\ts_nop 0\n\tv_cvt_pk_bf16_f32 v40, v40, v40\n\tds_write_b16 %3, v40\n\t"
                         "ds_read_b128 v[0:3], %4\n\tds_read_b128 v[4:7], %4 offset:192\n\tds_read_b128 v[8:11], %4 offset:384\n\tds_read_b128 v[12:15], %4 offset:576\n\t"
                         "ds_read_b128 v[16:19], %4 offset:768\n\tds_read_b128 v[20:23], %4 offset:960\n\tds_read_b128 v[24:27], %4 offset:1152\n\tds_read_b128 v[28:31], %4 offset:1344\n\t"
                         "ds_write_b32 %5, v41\n\t"
                         "s_waitcnt lgkmcnt(8)\n\t" SMF(32:35, 0:3, 160:167) SMF(36:39, 0:3, 224:231) "s_waitcnt lgkmcnt(7)\n\t" SMF(32:35, 4:7, 168:175) SMF(36:39, 4:7, 232:239)
                         "s_waitcnt lgkmcnt(6)\n\t" SMF(32:35, 8:11, 176:183) SMF(36:39, 8:11, 240:247) "s_waitcnt lgkmcnt(4)\n\t" SMF(32:35, 12:15, 184:191) SMF(36:39, 12:15, 248:255)
                         SMF(32:35, 16:19, 192:199) "s_waitcnt lgkmcnt(1)\n\t" SMF(32:35, 20:23, 200:207) SMF(32:35, 24:27, 208:215) SMF(32:35, 28:31, 216:223)
                         "s_nop 2\n\tv_pk_add_f32 v[36:37], v[36:37], v[38:39]\n\tds_write_b64 %7, v[36:37]\n\ts_nop 2\n\tv_pk_add_f32 v[32:33], v[32:33], v[34:35]\n\tds_write_b64 %6, v[32:33]"
                         :: "v"(idx), "v"(qa), "v"(qm), "v"(pw), "v"(pr), "v"(ma), "v"(oa), "v"(oc), "s"(ck1), "s"(ck2), "s"(csc)
                         : "memory", "v0", "v1", "v2", "v3", "v4", "v5", "v6", "v7", "v8", "v9", "v10", "v11", "v12", "v13", "v14", "v15", "v16", "v17", "v18", "v19", "v20", "v21", "v22", "v23",
                           "v24", "v25", "v26", "v27", "v28", "v29", "v30", "v31", "v32", "v33", "v34", "v35", "v36", "v37", "v38", "v39", "v40", "v41", "v42", "v43", "v44", "v45");
#undef SMF
#undef MAXD
            STAMP(20 + 32 * layer);
            if (__builtin_expect(wave >= 3, 1)) BAR();
            else {
                __builtin_amdgcn_s_setprio(0);
                if (wave == 0) {
                    unsigned long long mk = 0x1111111111111111ull; PINS(mk);
                    BAR();
                    STAMP(5 + 32 * layer);
                    comb_o0(*(const LAS f32x4*)(size_t)cma, *(const LAS f32x4*)(size_t)(cma + 16), *(const LAS f32x4*)(size_t)cxa, *(const LAS f32x4*)(size_t)cra,
                            *(const LAS f32x4*)(size_t)(cra + 1024), *(const LAS f32x4*)(size_t)(cra + 2048), *(const LAS f32x4*)(size_t)(cra + 3072), cso, mk, false);
                } else if (wave == 1) {
                    unsigned long long mk = 0x0000111100001111ull; PINS(mk);
                    BAR();
                    comb_o1(*(const LAS f32x4*)(size_t)cma, *(const LAS f32x4*)(size_t)(cma + 16), *(const LAS f32x2*)(size_t)cxa, *(const LAS f32x4*)(size_t)cra, *(const LAS f32x4*)(size_t)(cra + 1024), cso, mk, false);
                } else {
                    BAR();
                    comb_ml(cma, cxa, cra, cso, false);
                }
                STAMP(21 + 32 * layer);
            }
        }
    };

    unsigned nj = wave == 3 ? WEARLY : 0u, rS = 0u, nS = 0u; int ord;
    {
        const int tid = threadIdx.x, lane = tid & 63;
        STAMP(0);
        unsigned pf = 0u, mb = 0u;
        const u16* kbase = kc + ((size_t)((gj >> 1) * HQ + gi) * SK + (SK - WIN) + (gj & 1) * 512) * DH;
        const u16* vbase = vc + ((size_t)((gj >> 1) * HQ + gi) * SK + (SK - WIN) + (gj & 1) * 512) * DH;
        unsigned pt0 = 0u;
        const unsigned S0 = ALT ? (unsigned)(gi & 1) * 8192u : 0u;
        if (wave == 7) {
            __builtin_amdgcn_s_setprio(3);
            for (int i = 0; i < 5; ++i) dma16p(x0, (unsigned)(i * 1024 + lane * 16), lb + L_X0 + i * 1024);
            pf = code_pf((g >> 3) % 11, lane);
            if (lane < 24) *reinterpret_cast<unsigned*>(smem + L_CNT + lane * 4) = lane == 16 ? tst : 0u;
            BAR();
            if (lane < 10) *reinterpret_cast<u32x4*>(smem + L_XS + 640 + lane * 16) = (u32x4){0x3f803f80u, 0x3f803f80u, 0x3f803f80u, 0x3f803f80u};
            if (lane < 16) { *reinterpret_cast<u32x4*>(smem + L_CS + 768 + lane * 16) = (u32x4){0u, 0u, 0u, 0u}; }
        } else {
            if (wave >= 4) {
                if (wave == 4) st16_sc1(ws, WS_OWN + (unsigned)(gi * 16384 + (lane >> 5) * 8192 + gj * 512 + (lane & 31) * 16), (u32x4){~0u, ~0u, ~0u, ~0u});
                else empty5(0, (unsigned)(wave - 5) * 8192u, lane);
                if (wave == 6) {
                    __builtin_amdgcn_s_setprio(3);
                    if (gi + gj == 0) st4_sc1(ws, WS_R + (lane & 31) * 4, 0u);
                    st4_sc1(ws, WS_MB + (unsigned)((gi * 8 + gj) * 128 + (lane & 31) * 4), 0u);
                }
            } else if (PROBE && wave >= 2) {
                unsigned long long c0, c1;
                asm volatile("s_memtime %0\n\ts_waitcnt lgkmcnt(0)\n\ts_load_dword %2, %3, %4\n\ts_waitcnt lgkmcnt(0)\n\ts_memtime %1\n\ts_waitcnt lgkmcnt(0)"
                             : "=&s"(c0), "=&s"(c1), "=&s"(pt0) : "s"(ws), "s"((6u << 20) + ((((tst + g * 977u) * 2654435761u) >> 11 & 0x1fdf80u) | ((wave ^ gj) & 1) * 8192)) : "memory");
                pt0 = ((wave ^ gj) & 1) ? (unsigned)c1 - (unsigned)c0 : (unsigned)c0 - (unsigned)c1;
            }
            STAMP(32);
#pragma unroll 1
            for (int i = wave < 2 ? wave : wave < 4 ? 35 : wave - 2; i < 35; i += 5) {
                if (i >= 33) dma16p(wr, (unsigned)((i == 33 || lane < 16) ? (i - 33) * 1024 + lane * 16 : 0), lb + L_WR + (i - 33) * 1024);
                else {
                    const int c = i * 64 + lane; int row = c / 26, cc = c - 26 * row; if (cc > 23) cc = 23; if (row > 79) row = 79;
                    dma16(wq, (unsigned)(((gj * 80 + swz(row)) * QD + gi * DH + cc * 8) * 2), lb + L_WQ + i * 1024);
                }
            }
            STAMP(36);
            BAR();
            STAMP(40);
            *reinterpret_cast<u32x4*>(smem + L_P + tid * 16) = (u32x4){0u, 0u, 0u, 0u};
            if (tid < 72 * 12 - 448) *reinterpret_cast<u32x4*>(smem + L_P + 7168 + tid * 16) = (u32x4){0u, 0u, 0u, 0u};
            if (wave >= 4) {
                asm volatile("s_waitcnt vmcnt(0) lgkmcnt(0)" ::: "memory");
                landed();
            } else if (wave >= 2) lds_add(20, pt0);
        }
        u32x4 a[8];
        unsigned ko = (unsigned)((wave * 64 + (lane & 7)) * 384 + (((lane >> 3) & 1) * 4 + (lane >> 4)) * 16);
        PIN1(ko);
        auto k_group = [&](auto GC_) __attribute__((always_inline)) {
            constexpr int G = decltype(GC_)::value, R = RK + 24 * (G >> 1) + 4 * (G & 1);
            const unsigned o = ko + G * 3072;
            pace(3);
            ld16_nt<R, 0>(kbase, o); ld16_nt<R + 8, 128>(kbase, o); ld16_nt<R + 16, 256>(kbase, o);
            const int lane = lane_id();
            const unsigned ho = WS_H1 + S0 + (unsigned)((gi * 4 + (gj >> 1)) * 16384 + 3584 + (lane < 48 ? lane : 0) * 16);
            if (wave < 7) {
                asm volatile("; COND_BEGIN");
                if (G == 0 && wave < 4) {
                    asm volatile("s_waitcnt vmcnt(%0) lgkmcnt(0)" :: "i"(3) : "memory");
                    landed();
                    if (wave < 2) load_gu(wave, lane);
                    if (wave == 2) load_wd(lane);
                    if (wave == 3) {
#pragma unroll 1
                        for (int k = 5 - WEARLY; k < 5; ++k) load_wo(k, lane, true);
                    }
                }
                asm volatile("; COND_END");
            }
            if (wave == 7) {
                asm volatile("; COND_BEGIN");
                if (G == GP) {
                    VMW(3 * (GP + 1) + 1);
                    const unsigned char* p = smem + L_X0 + (lane >> 4) * 1280;
                    const float c0 = lo16(*reinterpret_cast<const u16*>(p));
                    const f32x2 c2 = {c0, c0};
                    f32x2 s2 = {0.f, 0.f}, q2 = s2;
#pragma unroll
                    for (int k = 0; k < 5; ++k) {
                        const u32x4 w = *reinterpret_cast<const u32x4*>(p + k * 256 + (lane & 15) * 16);
#pragma unroll
                        for (int e = 0; e < 4; ++e) { const f32x2 d = unpk(w[e]) - c2; s2 += d; q2 += d * d; }
                    }
                    const float s = row16_sum(s2[0] + s2[1]), qq = row16_sum(q2[0] + q2[1]);
                    const float ms = s * (1.f / DM);
                    const unsigned mv = pk2(c0 + ms, fmax2(qq - s * ms, 0.f) * (1.f / DM));
                    const float mb = lo16(mv), rden = frcp(bfr(__builtin_amdgcn_sqrtf(bfr(hi16(mv) + 1e-5f))));
                    const f32x2 mb2 = {mb, mb}, rd2 = {rden, rden};
                    if ((lane & 15) < 10) {
                        u32x4 w = *reinterpret_cast<const u32x4*>(p + gj * 160 + (lane & 15) * 16);
#pragma unroll
                        for (int e = 0; e < 4; ++e) w[e] = pk2v(unpk(pk2v(unpk(w[e]) - mb2)) * rd2);
                        *reinterpret_cast<u32x4*>(smem + L_XS + (lane >> 4) * 160 + (lane & 15) * 16) = w;
                    }
                    STAMP(30);
#pragma unroll 1
                    for (unsigned spins = 0; spins < (kSpinCap << 4); ++spins) {
                        if (lds_word(lb + L_CNT) >= 7u) break;
                        __builtin_amdgcn_s_sleep(1);
                    }
                    STAMP(26);
                }
                if (G == GP || G == GA) {
#pragma unroll 1
                    for (int blk = 0; blk < 3; ++blk) {
                        LANE_VARS();
                        const unsigned xb = lb + L_XS + (unsigned)((G == GP ? p4 : 4) * 160);
                        Frag80 f[4];
#pragma unroll
                        for (int i = 0; i < 4; ++i) f[i] = frag80(lb + L_WQ + (unsigned)(128 * blk + 32 * i + 8 * p4), 416, r0s, r1s, r2s);
                        const Frag80 b = rows80(xb + kq * 16, xb + 128 + kq * 8);
                        const u32x4 c = tiles4(f, b, 0x000f000f000f000full);
                        if (G == GP) st16_sc1(ws, WS_H1 + S0 + (unsigned)((gi * 4 + p4) * 16384 + gj * 896 + 256 * blk + q4 * 64 + kq * 16), c);
                        else *reinterpret_cast<u32x4*>(smem + L_CS + 256 * blk + q4 * 64 + kq * 16) = c;
                    }
                    if (G == GP) { STAMP(2); WAIT1(pf, 3 * (GP + 1) + 3); }
                }
                if (G == GA) {
                    const unsigned half = __builtin_amdgcn_s_getreg(2 << 6 | 20);
                    lds_set(lb + L_CNT + 68, 1u | (((int)lds_word(lb + L_CNT + 20) > 0) ^ half) << 1 | half << 2);
                    VMW(3 * (GA - GP));
                    send_flag(lane_id());
                    lds_set(lb + L_CNT + 8, 1u);
                    STAMP(17);
                    f32x2 r2 = {0.f, 0.f};
#pragma unroll
                    for (int k = 0; k < 5; ++k) r2 += unpk(*reinterpret_cast<const unsigned*>(smem + L_WR + k * 256 + lane_id() * 4));
                    *reinterpret_cast<float*>(smem + L_CNT + 12) = wave_sum(r2[0] + r2[1]);
                }
                asm volatile("; COND_END");
            }
            if (wave == 6) {
                asm volatile("; COND_BEGIN");
                if (G == GB) mb = ld4_sc1(ws, WS_MB + (unsigned)((gi * 8 + gj) * 128 + (lane & 7) * 4));
                if (G == GC) {
                    WAIT1(mb, 3 * (GC - GB));
#pragma unroll 1
                    for (unsigned spins = 0; spins < kSpinCap && !all_set(mb); ++spins) {
                        if ((spins & 7u) == 7u && lds_word(lb + L_CNT + 8)) send_flag(lane);
                        mb = ld4_sc1(ws, WS_MB + (unsigned)((gi * 8 + gj) * 128 + (lane & 7) * 4));
                        WAIT1(mb, 0);
                    }
                    lds_set(lb + L_CNT + 92, (__builtin_amdgcn_readlane(mb, gi & 7) & 4u) << 11);
                    lds_set(lb + L_CNT + 88, (lds_word(lb + L_CNT + 64) & ~31u) | __builtin_popcount((unsigned)__builtin_amdgcn_ballot_w64((mb & 2u) != 0u) & 255u) << 1 | 1u);
                    report();
                    GATHER8(a, ho, 896);
                    pace(XCOST, false);
                    STAMP(31);
                }
                asm volatile("; COND_END");
            } else if (G == GC) { for (int k = 0; k < 8; ++k) asm volatile("" : "=v"(a[k])); }
        };
        k_group(IC<0>{}); k_group(IC<1>{}); k_group(IC<2>{}); k_group(IC<3>{}); k_group(IC<4>{}); k_group(IC<5>{}); k_group(IC<6>{}); k_group(IC<7>{});
        {
            const int lane = lane_id();
            const unsigned v0 = (unsigned)((wave * 64 + 4 * (lane >> 4)) * 384 + (lane & 15) * 16);
            const unsigned v1 = v0 + 256 - (unsigned)((lane & 15) * 8);
            auto v_group = [&](auto HC) __attribute__((always_inline)) {
                constexpr int h = decltype(HC)::value, R = h < 8 ? RV + 16 * (h & 3) + 4 * (h >> 2) : RV + 64 + 2 * (h - 8), NV = h < 8 ? 2 * h + 2 : 4 * h - 12;
                const unsigned o = (h < 8 ? v0 + (h >> 1) * 6144 : v1 + (h - 8) * 6144);
                pace(2);
                if (h < 8) { ld16_nt<R, (h & 1) * 768>(vbase, o); ld16_nt<R + 8, (h & 1) * 768 + 384>(vbase, o); }
                else { ld8_nt<R, 0>(vbase, o); ld8_nt<R + 8, 384>(vbase, o); ld8_nt<R + 16, 768>(vbase, o); ld8_nt<R + 24, 1152>(vbase, o); }
                if (wave == 7 && gi + gj == 0) {
                    asm volatile("; COND_BEGIN");
                    if (h == 3 || h == 6) mb = ld4_sc1(ws, WS_R + (unsigned)((lane & 31) * 4));
                    if (h == 4 || h == 7) {
                        WAIT1(mb, 2);
                        if (all_set(mb) && !lds_word(lb + L_CNT + 36)) answer(lane, mb);
                    }
                    asm volatile("; COND_END");
                }
                if (wave == 6) {
                    asm volatile("; COND_BEGIN");
                if (h == HD) {
                    WAIT8(a, 3 * (7 - GC) + NV);
                    const f32x4 sq = SUM8(a);
                    unsigned* qp = reinterpret_cast<unsigned*>(smem + L_Q + (lane >> 2) * 32 + (lane & 3) * 4);
                    qp[0] = pk2(sq[0], sq[1]); qp[4] = pk2(sq[2], sq[3]);
                    asm volatile("s_waitcnt lgkmcnt(0)\n\tds_write_b32 %0, %1" :: "v"(lb + L_CNT + 4), "v"(1u) : "memory");
                    STAMP(27);
                    lead = LEAD * PACE * 4;
                }
                    if (h == 6) mb = ld4_sc1(ws, WS_MB + (unsigned)((gi * 8 + (gi & 7)) * 128 + 32));
                    if (h == 8) {
                        WAIT1(mb, 6);
                        const unsigned r = __builtin_amdgcn_readfirstlane(mb);
                        if (r) { adopt(r); STAMP(29); }
                    }
                    asm volatile("; COND_END");
                }
                if constexpr (h < 6) {
                    constexpr int i = 2 * h;
                    fix_k1(IC<i>{}, IC<20 - 6 * (i / 3) - i % 3 + NV>{});
                    fix_k1(IC<i + 1>{}, IC<20 - 6 * ((i + 1) / 3) - (i + 1) % 3 + NV>{});
                }
                if (h == 5) {
                    STAMP(1);
                    __builtin_amdgcn_s_setprio(0);
                    att_qk0();
                }
            };
            v_group(IC<0>{}); v_group(IC<1>{}); v_group(IC<2>{}); v_group(IC<3>{}); v_group(IC<4>{}); v_group(IC<5>{});
            v_group(IC<6>{}); v_group(IC<7>{}); v_group(IC<8>{}); v_group(IC<9>{}); v_group(IC<10>{}); v_group(IC<11>{});
            lds_add(16, 1u);
        }
    }
    {
        STAMP(28);
        att_addr();
        const int lane = lane_id();
        cma = lb + L_M; cxa = lb + L_M + (unsigned)((lane >> 4) * 8); cra = lb + L_OP + (unsigned)(512 + (lane >> 4) * 2048 + (lane & 15) * 16);
        cso = WS_OWN + (unsigned)(gi * 16384 + gj * 512 + 256 + (lane >> 5) * 64 + (lane & 12) * 4 + (lane & 1) * 8192);
        PIN2(cma, cxa); PIN2(cra, cso);
        att_pv0();
    }
    {
        f32x4 m0, m1, o0, o1; f32x2 mm; unsigned r;
        asm volatile("s_mov_b64 exec, 1\n\tds_add_rtn_u32 %5, %6, %7 offset:%10\n\ts_mov_b64 exec, -1\n\t"
                     "ds_read_b128 %0, %6\n\tds_read_b128 %1, %6 offset:16\n\tds_read_b64 %2, %8\n\tds_read_b128 %3, %9\n\tds_read_b128 %4, %9 offset:1024\n\ts_waitcnt lgkmcnt(0)"
                     : "=&v"(m0), "=&v"(m1), "=&v"(mm), "=&v"(o0), "=&v"(o1), "=&v"(r)
                     : "v"(cma), "v"(1u), "v"(cxa), "v"(cra), "i"(L_CNT + 28 - L_M) : "memory");
        ord = __builtin_amdgcn_readfirstlane(r);
        if (ord == 7) { STAMP(5); comb_o1(m0, m1, mm, o0, o1, cso, 0x0000333300003333ull, true); lds_set(lb + L_CNT + 48, now() | 1u); STAMP(21); }
        const int lane = lane_id();
        auto wait_cnt = [&](int off, unsigned v) {
#pragma unroll 1
            for (unsigned spins = 0; spins < (kSpinCap << 4); ++spins) { if (lds_word(lb + L_CNT + off) >= v) break; __builtin_amdgcn_s_sleep(1); }
        };
        if (ord == 1) {
            wait_cnt(16, 8u);
            unsigned xa, ra; ml_addr(lane, xa, ra);
            comb_ml(cma, xa, ra, WS_OWN + (unsigned)(gi * 16384 + gj * 512 + 384 + (lane & 1) * 8192), true);
            STAMP(24);
        } else if (ord == 0 && !lds_word(lb + L_CNT + 52)) {
            if (gi + gj == 0 && !lds_word(lb + L_CNT + 36)) {
                unsigned r = 0u;
#pragma unroll 1
                for (unsigned spins = 0; spins < kSpinCap; ++spins) {
                    r = ld4_sc1(ws, WS_R + (unsigned)((lane & 31) * 4));
                    WAIT1(r, 0);
                    if (all_set(r)) break;
                    if ((spins & 7u) == 7u) send_flag(lane);
                }
                answer(lane, r);
            }
#pragma unroll 1
            for (unsigned spins = 0; spins < kSpinCap; ++spins) {
                unsigned r = ld4_sc1(ws, WS_MB + (unsigned)((gi * 8 + (gi & 7)) * 128 + 32));
                WAIT1(r, 0);
                r = __builtin_amdgcn_readfirstlane(r);
                if (r) { adopt(r); break; }
                if ((spins & 7u) == 7u) { send_flag(lane); report(); }
                __builtin_amdgcn_s_sleep(16);
            }
            STAMP(29);
        } else if (ord == 2) {
#pragma unroll 1
            for (unsigned spins = 0; spins < (kSpinCap << 6); ++spins) if (lds_word(lb + L_CNT + 24) >= 8u) break;
            const unsigned ra = lb + L_OP + (unsigned)((lane >> 5) * 4096 + (lane & 31) * 16);
            comb_o0(*(const LAS f32x4*)(size_t)cma, *(const LAS f32x4*)(size_t)(cma + 16), *(const LAS f32x4*)(size_t)(cma + (lane >> 5) * 16), *(const LAS f32x4*)(size_t)ra, *(const LAS f32x4*)(size_t)(ra + 1024),
                    *(const LAS f32x4*)(size_t)(ra + 2048), *(const LAS f32x4*)(size_t)(ra + 3072), WS_OWN + (unsigned)(gi * 16384 + gj * 512 + (lane & 60) * 4 + (lane & 1) * 8192), 0x3333333333333333ull, true);
            STAMP(22);
        }
#pragma unroll 1
        for (;;) {
            unsigned j;
            asm volatile("s_mov_b64 exec, 1\n\tds_add_rtn_u32 %0, %1, %2\n\ts_mov_b64 exec, -1\n\ts_waitcnt lgkmcnt(0)" : "=&v"(j) : "v"(lb + L_CNT + 40), "v"(1u) : "memory");
            j = __builtin_amdgcn_readfirstlane(j);
            if (j >= 5u - WEARLY) break;
            load_wo((int)j, lane, false); ++nj;
        }
        STAMP(23);
    }

    auto chain = [&](auto LC) {
        constexpr int layer = decltype(LC)::value;
        unsigned h4 = WS_H4 + (unsigned)((layer * 32 + gi) * 32768) + (layer ? rS ^ 8192u : 0u);
        auto sides = [&]() { rS = lds_word(lb + L_CNT + 80); nS = lds_word(lb + L_CNT + 84); h4 += rS ^ 8192u; };
        if (wave < 3) {
            LANE_VARS();
            if (layer == 0) {
                VMW(0);
                lds_add(44, nj);
                unsigned tf = 0u;
#pragma unroll 1
                for (unsigned spins = 0; spins < (kSpinCap << 8); ++spins) {
                    tf = lds_word(lb + L_CNT + (EMODE ? 48 : 24));
                    if ((EMODE ? tf != 0u : tf >= 8u) && lds_word(lb + L_CNT + 52)) break;
                }
                sides();
                if (EMODE == 1) until(tf + DG2 + 3);
                if (wave < 2) empty5(5, (unsigned)wave * 8192u, lane); else empty4(0, rS, lane);
                VMW(0);
                BAR();
#pragma unroll 1
                for (unsigned spins = 0; spins < (kSpinCap << 4); ++spins) if (lds_word(lb + L_CNT + 76) >= 5u) break;
                if (wave < 2) { empty1(wave, rS, lane); empty35(1, (unsigned)wave * 8192u, lane); }
                else { empty4(1, rS, lane); st16m(ws, WS_CS + (unsigned)((gi * 8 + gj) * 768 + lane * 16), *reinterpret_cast<const u32x4*>(smem + L_CS + lane * 16), 0xffffffffffffull); }
            } else BAR();
            {
                const unsigned im = lb + L_GU + (unsigned)(32 * wave + 8 * p4);
                Frag80 fg = frag80(im, 96, r0s, r1s, r2s), fu = frag80(im + 8192, 96, r0s, r1s, r2s);
                const unsigned xb = lb + L_XS + (unsigned)((n < 4 ? n : 4) * 160);
                unsigned b16 = xb + kq * 16, b8 = xb + 128 + kq * 8;
                unsigned sg = h4 + (unsigned)((n < 4 ? h4s(wave) + gj * 512 + n * 64 : H4C + wave * 1024 + gj * 128) + kq * 16), su = sg + (n < 4 ? 256u : 64u);
                unsigned o = h4 + h4s(wave) + (unsigned)(lane * 16);
                unsigned oc = h4 + (unsigned)(H4C + wave * 1024 + ((lane >> 5) * 4 + q4) * 128 + (kq & 1) * 64 + p4 * 16);
                unsigned os = h4 + (unsigned)(H4T + (p4 + 4 * (kq & 1)) * 128 + q4 * 16);
                unsigned ha = lb + L_H + (unsigned)(q4 * 96 + wave * 32 + p4 * 8 + kq * 2);
                f32x2 cs = {0.f, 0.f};
                if (layer != 0) cs = *reinterpret_cast<const f32x2*>(smem + L_CS2 + wave * 512 + lane * 8);
                PIN3(fg.a0, fg.a1, fg.a2); PIN3(fu.a0, fu.a1, fu.a2); PIN3(b16, b8, sg); PIN3(su, o, os); PIN2(ha, cs);
                unsigned long long mk = layer ? 0x000f000f000f000full : 0x001f001f001f001full;
                unsigned ta = WS_MB + (unsigned)((lane >> 1) * 1024 + 64 + (lane & 1) * 16);
                if (layer == 0) PIN2(oc, ta);
                PINS(mk);
                if (layer == 0) VMW(0);
                BAR();
                STAMP(10 + 32 * layer);
                u32x4 tl = {0u, 0u, 0u, 0u};
                if (layer == 0) tl = ld16_sc1(ws, ta);
                const Frag80 b = rows80(b16, b8);
                const f32x4 g0 = mfma32(fg.a0, b.a0, z4), u0 = mfma32(fu.a0, b.a0, z4), g1 = mfma32(fg.a1, b.a1, g0), g2 = mfma16(fg.a2, b.a2, z4), u1 = mfma32(fu.a1, b.a1, u0), u2 = mfma16(fu.a2, b.a2, z4);
                __builtin_amdgcn_sched_barrier(0);
                const f32x4 cg = g1 + g2, cu = u1 + u2;
                st16m2<0>(ws, sg, asu(cg), su, asu(cu), mk);
                STAMP(11 + 32 * layer);
                if (layer == 0) {
                    WAIT1(tl, 2);
#pragma unroll 1
                    for (unsigned spins = 0; spins < kSpinCap && !all_set(min(min(tl[0], tl[1]), min(tl[2], tl[3]))); ++spins) {
                        tl = ld16_sc1(ws, ta);
                        WAIT1(tl, 0);
                    }
                    const unsigned tn = now();
                    unsigned d = min(min(min(tn - tl[0], tn - tl[1]), min(tn - tl[2], tn - tl[3])), 800u);
                    d = min(d, dppu<0xB1>(d)); d = min(d, dppu<0x4E>(d)); d = min(d, dppu<0x124>(d)); d = min(d, dppu<0x128>(d));
                    unsigned tr = tn - min(min((unsigned)__builtin_amdgcn_readlane(d, 0), (unsigned)__builtin_amdgcn_readlane(d, 16)), min((unsigned)__builtin_amdgcn_readlane(d, 32), (unsigned)__builtin_amdgcn_readlane(d, 48)));
                    lds_set(lb + L_CNT + 56, tr | 1u);
                                        until(__builtin_amdgcn_readfirstlane(tr) + X4A);
                } else sched(X4, 12);
                STAMP(16 + 32 * layer);
                float hv; f32x2 st;
                u32x4 a0, a1, a2, a3, ac, as;
                auto load = [&]() __attribute__((always_inline)) {
                    as = ld16_sc1(ws, os);
                    if (layer == 0) ac = ld16_sc1(ws, oc);
                    a0 = ld16_sc1<0>(ws, o); a1 = ld16_sc1<1024>(ws, o); a2 = ld16_sc1<2048>(ws, o); a3 = ld16_sc1<3072>(ws, o);
                    if (layer == 0) asm volatile("s_waitcnt vmcnt(0)" : "+v"(a0), "+v"(a1), "+v"(a2), "+v"(a3), "+v"(ac), "+v"(as) :: "memory");
                    else { asm volatile("s_waitcnt vmcnt(0)" : "+v"(a0), "+v"(a1), "+v"(a2), "+v"(a3), "+v"(as) :: "memory"); ac = as; }
                };
                auto compute = [&]() __attribute__((always_inline)) {
                    float ss, sm;
                    asm volatile("v_add_f32_dpp %0, %2, %2 quad_perm:[1,0,3,2] row_mask:0xf bank_mask:0xf\n\tv_add_f32_dpp %1, %3, %3 quad_perm:[1,0,3,2] row_mask:0xf bank_mask:0xf\n\ts_nop 0\n\t"
                                 "v_add_f32_dpp %0, %0, %0 quad_perm:[2,3,0,1] row_mask:0xf bank_mask:0xf\n\tv_add_f32_dpp %1, %1, %1 quad_perm:[2,3,0,1] row_mask:0xf bank_mask:0xf\n\ts_nop 0"
                                 : "=&v"(ss), "=&v"(sm) : "v"(as[0]), "v"(as[1]));
                    ss = x16_add(ss); sm = x16_add(sm);
                    st = ln_stats(ss, sm);
                    const f32x4 t = (asf(a0) + asf(a1)) + (asf(a2) + asf(a3));
                    const f32x2 gu = sw16(rs32(t[0], t[2]), rs32(t[1], t[3]));
                    if (layer == 0) {
                        cs = sw16(rs32(asf(ac)[0], asf(ac)[2]), rs32(asf(ac)[1], asf(ac)[3]));
                        cs[0] += dpp<0x124>(cs[0]); cs[1] += dpp<0x124>(cs[1]); cs[0] += dpp<0x128>(cs[0]); cs[1] += dpp<0x128>(cs[1]);
                    }
                    const unsigned gw = pk2v((gu - st[0] * cs) * st[1]);
                    const float gv = lo16(gw);
                    hv = bfr(gv * frcp(1.f + ex2(gv * -LOG2E))) * hi16(gw);
                    *(LAS u16*)(size_t)ha = (u16)pk2(hv, hv);
                    __builtin_amdgcn_sched_barrier(0);
                };
                RETRY(load(), max(max(max(um4(a0), um4(a1)), max(um4(a2), um4(a3))), max(um4(ac), um4(as))), compute(), any_nan(hv, hv), layer ? X4 : X4A);
                BAR();
                STAMP(12 + 32 * layer);
                if (layer == 0) {
                    *reinterpret_cast<f32x2*>(smem + L_CS2 + wave * 512 + lane * 8) = cs;
                }
            }
            if (layer == 0) {
                if (wave == 1) {
                    const unsigned o = WS_CS + (unsigned)(gi * 6144 + 3072 + (lane < 48 ? lane : 0) * 16);
                    u32x4 a[8];
                    GATHER8(a, o, 768);
                    WAIT8(a, 0);
                    if (lane < 48) *reinterpret_cast<f32x4*>(smem + L_CS + lane * 16) = SUM8(a);
                }
                Frag80 f[4];
#pragma unroll
                for (int i = 0; i < 4; ++i) { f[i] = frag80(lb + L_WQ + (unsigned)(128 * wave + 32 * i + 8 * p4), 416, r0s, r1s, r2s); PIN3(f[i].a0, f[i].a1, f[i].a2); }
                unsigned b16 = lb + L_XS + (unsigned)(p4 * 160 + kq * 16), b8 = lb + L_XS + (unsigned)(p4 * 160 + 128 + kq * 8);
                unsigned so = WS_H1 + rS + (unsigned)((gi * 4 + p4) * 16384 + gj * 896 + 256 * wave + q4 * 64 + kq * 16);
                unsigned long long mk = 0x000f000f000f000full;
                PIN3(b16, b8, so); PINS(mk);
                BAR();
                STAMP(33);
                const Frag80 b = rows80(b16, b8);
                st16_sc1(ws, so, tiles4(f, b, mk));
                STAMP(34);
                qk_addr(true); att_addr();
                cma = lb + L_M; cxa = lb + L_M + (unsigned)(wave == 0 ? (lane >> 5) * 16 : (lane >> 4) * 8);
                cra = lb + L_OP + (unsigned)(wave == 0 ? (lane >> 5) * 4096 + (lane & 31) * 16 : 512 + (lane >> 4) * 2048 + (lane & 15) * 16);
                if (wave == 2) ml_addr(lane, cxa, cra);
                cso = WS_OWN + rS + (unsigned)(gi * 16384 + 4096 + gj * 512) + (unsigned)(wave == 0 ? lane * 4 : wave == 1 ? 256 + (lane >> 5) * 64 + (lane & 15) * 4 : 384);
                PIN2(cma, cxa); PIN2(cra, cso);
            }
        } else {
            const int t = wave - 3;
            StatAddr sa;
            auto gather32 = [&](unsigned o, int lane, unsigned X, auto mid, auto fin) __attribute__((always_inline)) {
                u32x4 a0 = ld16_sc1<-2048>(ws, o), a1 = ld16_sc1<-1024>(ws, o), a2 = ld16_sc1<0>(ws, o), a3 = ld16_sc1<1024>(ws, o), sel;
                const unsigned one = (lane & 1) ? 0x3f800000u : 0x3f80u;
#pragma unroll
                for (int k = 0; k < 4; ++k) sel[k] = ((lane >> 1) & 3) == k ? one : 0u;
                unsigned long long hi = 0xff00ff00ff00ff00ull;
                PIN1(sel); PINS(hi);
                mid();
                float v;
                auto sum = [&]() __attribute__((always_inline)) {
                    f32x4 d; float w;
                    asm volatile("s_waitcnt vmcnt(3)\n\tv_mfma_f32_16x16x32_bf16 %0, %1, %5, 0\n\ts_waitcnt vmcnt(2)\n\tv_mfma_f32_16x16x32_bf16 %0, %2, %5, %0\n\t"
                                 "s_waitcnt vmcnt(1)\n\tv_mfma_f32_16x16x32_bf16 %0, %3, %5, %0\n\ts_waitcnt vmcnt(0)\n\tv_mfma_f32_16x16x32_bf16 %0, %4, %5, %0"
                                 : "=&v"(d), "+v"(a0), "+v"(a1), "+v"(a2), "+v"(a3) : "v"(sel) : "memory");
                    asm volatile("s_nop 7\n\tv_cndmask_b32 %0, %2, %3, %6\n\tv_cndmask_b32 %1, %4, %5, %6\n\ts_nop 1\n\tv_permlane32_swap_b32 %0, %1\n\tv_add_f32 %0, %0, %1"
                                 : "=&v"(v), "=&v"(w) : "v"(d[0]), "v"(d[1]), "v"(d[2]), "v"(d[3]), "s"(hi));
                    fin(v);
                    __builtin_amdgcn_sched_barrier(0);
                };
                sum();
                if (__builtin_expect(any_nan(v, v) != 0ull, 0)) {
#pragma unroll 1
                    for (unsigned spins = 0; spins < kSpinCap; ++spins) {
                        slip(X);
                        again16<-2048>(a0, ws, o); again16<-1024>(a1, ws, o); again16<0>(a2, ws, o); again16<1024>(a3, ws, o);
                        asm volatile("s_waitcnt vmcnt(0)" : "+v"(a0), "+v"(a1), "+v"(a2), "+v"(a3) :: "memory");
                        if (!any_empty(max(max(um4(a0), um4(a1)), max(um4(a2), um4(a3))))) break;
                    }
                    sum();
                }
            };
            auto pack4 = [&](float c0, float c1, float c2, float c3) __attribute__((always_inline)) { const unsigned w0 = min(pk2(c0, c1), 0xfffffffeu), w1 = min(pk2(c2, c3), 0xfffffffeu); return (u32x2){w0, w1}; };
            unsigned xw;
            __builtin_amdgcn_s_setprio(0);
            {
                LANE_VARS();
                if (layer == 0) xe = lo16(*reinterpret_cast<const u16*>(smem + L_X0 + kq * 1280 + gj * 160 + t * 32 + n * 2));
                STAMP(6 + 32 * layer);
                if (layer == 0) nS = lds_word(lb + L_CNT + 84);
                unsigned lo = WS_OWN + (layer ? rS : nS) + (unsigned)(gi * 16384 + layer * 4096 + (lane >> 3) * 512 + t * 128 + (t == 3 ? 0 : (lane & 7) * 16));
                unsigned lw = lb + L_O2 + (unsigned)((lane >> 3) * 416 + t * 128 + (t == 3 ? 0 : (lane & 7) * 16));
                PIN2(lo, lw);
                u32x4 ov = {~0u, ~0u, ~0u, ~0u};
                if (layer != 0 && t < 4) { sched(X2, 20); STAMP(49); ov = ld16_sc1(ws, lo); }
                const unsigned wa = lb + L_WO + (unsigned)(r0s * 160 + 32 * t + 8 * p4), wa1 = lb + L_WO + (unsigned)(r1s * 160 + 32 * t + 8 * p4);
                bf16x8 af[6];
                if (layer == 0) {
                    if (t < 4) {
                        unsigned tf = 0u;
#pragma unroll 1
                        for (unsigned spins = 0; spins < (kSpinCap << 4); ++spins) { tf = lds_word(lb + L_CNT + 48); if (tf) break; }
                        until(tf + DG2);
                        ov = ld16_sc1(ws, lo);
                        if (nj) { WAIT1(ov, 1); lds_add(44, nj); }
                    } else { VMW(0); lds_add(44, nj); }
#pragma unroll 1
                    for (unsigned spins = 0; spins < (kSpinCap << 4); ++spins) if (lds_word(lb + L_CNT + 44) >= 5u) break;
                    STAMP(8);
                }
#pragma unroll
                for (int s = 0; s < 6; ++s) af[s] = cat8(tr64(wa + s * 32 * 160), tr64(wa1 + s * 32 * 160));
                const unsigned h3 = tile35(gj, 10 * layer + t);
                unsigned so = h3 + (unsigned)((lane & 1) * 8192 + gi * 128 + (lane & 2) * 32 + (lane & 4) * 8 + kq * 8), go = h3 + nS + (unsigned)(2048 + lane * 16);
                unsigned xs = lb + L_XS + (unsigned)(kq * 160 + t * 32 + n * 2);
                unsigned br = lb + L_O2 + (unsigned)((n & 7) * 416 + kq * 16), mr = lb + L_O2 + (unsigned)((n & 7) * 416 + 384);
                unsigned long long mk = 0x00ff00ff00ff00ffull;
                PIN3(af[0], af[1], af[2]); PIN3(af[3], af[4], af[5]); PIN3(br, mr, so); PIN2(go, xs); PINS(mk);
                if (t < 4) {
                    WAIT1(ov, 0);
                    *(LAS u32x4*)(size_t)lw = ov;
                    if (layer == 0 ? any_empty(um4(ov)) : __builtin_expect(any_empty(um4(ov)), 0)) {
#pragma unroll 1
                        for (unsigned spins = 0; spins < kSpinCap; ++spins) {
                            if (layer != 0) slip(X2);
                            ov = ld16_sc1(ws, lo);
                            WAIT1(ov, 0);
                            *(LAS u32x4*)(size_t)lw = ov;
                            if (!any_empty(um4(ov))) break;
                            if (layer == 0) { if ((spins & 15u) == 15u) send_flag(lane); __builtin_amdgcn_s_sleep(1); }
                        }
                    }
                }
                BAR();
                bf16x8 bf[6];
                const f32x2 ml = *(const LAS f32x2*)(size_t)mr;
#pragma unroll
                for (int s = 0; s < 6; ++s) bf[s] = lds16(br + s * 64);
                __builtin_amdgcn_sched_barrier(0);
                f32x4 ca = z4;
#pragma unroll
                for (int s = 0; s < 6; ++s) ca = mfma32(af[s], bf[s], ca);
                const float e = ex2(ml[0] - fmax2(ml[0], dpp<0xB1>(ml[0]))), el = e * ml[1];
                const float f = e * frcp(el + dpp<0xB1>(el));
                float c0, c1, c2, c3;
                asm volatile("s_nop 7\n\t"
                             "v_mul_f32 %0, %4, %8\n\tv_mul_f32 %1, %5, %8\n\tv_mul_f32 %2, %6, %8\n\tv_mul_f32 %3, %7, %8\n\t"
                             "v_add_f32_dpp %0, %0, %0 quad_perm:[1,0,3,2] row_mask:0xf bank_mask:0xf\n\tv_add_f32_dpp %1, %1, %1 quad_perm:[1,0,3,2] row_mask:0xf bank_mask:0xf\n\t"
                             "v_add_f32_dpp %2, %2, %2 quad_perm:[1,0,3,2] row_mask:0xf bank_mask:0xf\n\tv_add_f32_dpp %3, %3, %3 quad_perm:[1,0,3,2] row_mask:0xf bank_mask:0xf"
                             : "=&v"(c0), "=&v"(c1), "=&v"(c2), "=&v"(c3) : "v"(ca[0]), "v"(ca[1]), "v"(ca[2]), "v"(ca[3]), "v"(f));
                st8m(ws, so, pack4(c0, c1, c2, c3), mk);
                if (layer == 0 && wave == 7) st4_sc1(ws, WS_MB + (unsigned)(gi * 1024 + 64 + gj * 4), now() | 1u);
                if (layer == 0) { lds_add(76, 1u); go -= nS; sides(); go += nS; }
                STAMP(7 + 32 * layer);
                if (layer != 0) sched(X3, 12);
                else wait_word(ws, h3 + nS + (unsigned)(((gi * 7 + t * 5 + 3) & 31) * 128));
                STAMP(18 + 32 * layer);
                gather32(go, lane, layer ? X3 : 0u, [&]() { sa = stats_addr(lane, h4 + (unsigned)(H4T + gj * 128), 16); }, [&](float s) { xw = pk2(xe + bfr(s), 0.f); *(LAS u16*)(size_t)xs = (u16)xw; });
                xe = lo16(xw);
                STAMP(9 + 32 * layer);
            }
            BAR();
            if (wave == 3) stats(sa);
            STAMP(11 + 32 * layer);
            {
                LANE_VARS();
                const unsigned im = lb + L_WD + (unsigned)(32 * t + 8 * p4);
                bf16x8 wd0 = cat8(tr64(im + r0s * 160), tr64(im + r1s * 160)); bf16x4 wd1 = tr64(im + (32 + r2s) * 160);
                const unsigned h5 = tile35(gj, 10 * layer + 5 + t);
                unsigned h16 = lb + L_H + (unsigned)(p4 * 96 + kq * 16), h8 = lb + L_H + (unsigned)(p4 * 96 + 64 + kq * 8);
                unsigned so = h5 + (unsigned)((lane & 4) * 2048 + gi * 128 + (lane & 1) * 64 + (lane & 2) * 16 + kq * 8);
                unsigned long long mk = 0x00ff00ff00ff00ffull;
                PIN2(wd0, wd1); PIN3(h16, h8, so); PINS(mk);
                if (wave == 4) {
                    const unsigned os = h4 + (unsigned)(H4T + (p4 + 4 * (kq & 1)) * 128 + q4 * 16);
                    if (layer == 0) {
#pragma unroll 1
                        for (unsigned spins = 0; spins < (kSpinCap << 4); ++spins) if (lds_word(lb + L_CNT + 56)) break;
                    }
                    sched(layer ? X4 : X4A, 0);
                    u32x4 as;
#pragma unroll 1
                    for (unsigned spins = 0; spins < kSpinCap; ++spins) { as = ld16_sc1(ws, os); WAIT1(as, 0); if (!any_empty(um4(as))) break; }
                    float ss = __uint_as_float(as[0]), sm = __uint_as_float(as[1]), rd = __uint_as_float(as[2]);
                    ss += dpp<0xB1>(ss); sm += dpp<0xB1>(sm); rd += dpp<0xB1>(rd);
                    ss += dpp<0x4E>(ss); sm += dpp<0x4E>(sm); rd += dpp<0x4E>(rd);
                    ss = x16_add(ss); sm = x16_add(sm); rd = x16_add(rd);
                    if (LANES(0x1111ull)) *reinterpret_cast<f32x4*>(smem + L_ST + q4 * 16) = (f32x4){ss, sm, rd, 0.f};
                }
                BAR();
                STAMP(12 + 32 * layer);
                const bf16x8 hb0 = lds16(h16); const bf16x4 hb1 = lds8(h8);
                __builtin_amdgcn_sched_barrier(0);
                const f32x4 c = mfma32(wd0, hb0, z4) + mfma16(wd1, hb1, z4);
                st8m(ws, so, pack4(c[0], c[1], c[2], c[3]), mk);
                STAMP(13 + 33 * layer);
                if (layer == 0 || gi == 4 * t + (gj & 3)) {
                    float rs, n1;
                    auto prep = [&]() __attribute__((always_inline)) {
                        const f32x4 sr = *reinterpret_cast<const f32x4*>(smem + L_ST + kq * 16);
                        const float sw = *reinterpret_cast<const float*>(smem + L_CNT + 12);
                        const f32x2 st = ln_stats(sr[0], sr[1]);
                        const float r = bfr((sr[2] - st[0] * sw) * st[1]);
                        rs = bfr(frcp(1.f + ex2(r * -LOG2E)));
                        n1 = bfr(bfr(xe - st[0]) * st[1]);
                        PIN2(rs, n1);
                    };
                    if (layer == 0) {
                        unsigned go = h5 + nS + (unsigned)(2048 + lane * 16), xs = lb + L_XS + (unsigned)(kq * 160 + t * 32 + n * 2);
                        PIN2(go, xs);
                        sched(X5A, 8);
                        STAMP(15);
                        gather32(go, lane, X5A, [&]() { prep(); sa = stats_addr(lane, WS_H1 + rS + (unsigned)(gi * 65536 + gj * 896 + 768), 16384); },
                                 [&](float y) { xw = pk2(xe + bfr(n1 + bfr(bfr(y) * rs)), 0.f); *(LAS u16*)(size_t)xs = (u16)xw; });
                        xe = lo16(xw);
                        STAMP(14);
                    } else {
                        unsigned go = h5 + nS + (unsigned)(2048 + lane * 16), oo = (unsigned)(kq * 1280 + gj * 160 + t * 32 + n * 2);
                        PIN2(go, oo);
                        sched(X5B, 8);
                        STAMP(47);
                        gather32(go, lane, X5B, prep, [&](float y) { xw = pk2(xe + bfr(n1 + bfr(bfr(y) * rs)), 0.f); });
                        { const unsigned w = xw | (dppu<0xF5>(xw) << 16);
                          const u32x2 w2 = {w, dppu<0xAA>(w)};
                          asm volatile("s_mov_b64 exec, %3\n\tglobal_store_dwordx2 %0, %1, %2 sc0 sc1\n\ts_mov_b64 exec, -1" :: "v"(oo), "v"(w2), "s"(out), "s"(0x1111111111111111ull) : "memory"); }
                        STAMP(45);
                    }
                }
            }
            if (layer == 0) {
                BAR();
                if (wave == 3) { stats(sa); STAMP(34); }
                asm volatile("" : "=v"(cma), "=v"(cxa), "=v"(cra), "=v"(cso));
                if (wave != 7) { qk_addr(true); att_addr(); }
                else {
                    const int lane = lane_id();
                    unsigned o = WS_H1 + rS + (unsigned)((gi * 4 + (gj >> 1)) * 16384 + 3584 + (lane < 48 ? lane : 48) * 16), qw;
                    f32x4 cs;
                    PIN1(o);
                    sched(X1, 26);
                    STAMP(51);
                    f32x4 s; u32x4 a[8];
                    RETRY8(a, o, 896, s, { qw = lb + L_Q + (unsigned)((lane >> 2) * 32 + (lane & 3) * 4); cs = *reinterpret_cast<const f32x4*>(smem + L_CS + lane * 16); PIN2(qw, cs); qk_addr(true); att_addr(); }, { s -= bfr(__int_as_float(__builtin_amdgcn_readlane(__float_as_int(s[1]), 48)) * (1.f / DM)) * cs;
                                           u32x2 w; w[0] = pk2(s[0], s[1]); w[1] = pk2(s[2], s[3]);
                                           if (LANES(0xffff000000000000ull)) { w[0] = __float_as_uint(s[0]); w[1] = __float_as_uint(s[1]); }
                                           *(LAS unsigned*)(size_t)qw = w[0]; *(LAS unsigned*)(size_t)(qw + 16) = w[1]; __builtin_amdgcn_sched_barrier(0); }, any_nan(s[0] + s[2], s[1] + s[3]), X1);
                }
            }
        }
    };
    PINS(ck1); PINS(ck2); PINS(csc);
    chain(IC<0>{});
    STAMP(35);
    att1();
    chain(IC<1>{});
}

__device__ __attribute__((used, noinline)) void mega_code_end() { asm volatile(".Lmega_code_end:"); }

extern "C" void kernel_launch(void* const* d_in, const int* in_sizes, int n_in, void* d_out, int out_size, void* d_ws,
                              size_t ws_size, hipStream_t stream) {
    const u16* x0 = (const u16*)d_in[0];
    const u16* kc = (const u16*)d_in[1];
    const u16* vc = (const u16*)d_in[2];
    const u16* wq = (const u16*)d_in[3];
    const u16* wo = (const u16*)d_in[4];
    const u16* wr = (const u16*)d_in[6];
    const u16* wg = (const u16*)d_in[7];
    const u16* wu = (const u16*)d_in[8];
    const u16* wd = (const u16*)d_in[9];
    (void)hipFuncSetAttribute((const void*)mega_kernel, hipFuncAttributeMaxDynamicSharedMemorySize, L_END);
    mega_kernel<<<256, 512, L_END, stream>>>(x0, kc, vc, wq, wo, wr, wg, wu, wd, (u16*)d_out, (unsigned char*)d_ws
                                             );
}
```

```cpp
#define DM_ 2
#include <hip/hip_runtime.h>
#include <hip/hip_bf16.h>
#include <stdint.h>

typedef unsigned short u16;
typedef unsigned u32x4 __attribute__((ext_vector_type(4)));
typedef unsigned u32x2 __attribute__((ext_vector_type(2)));
typedef float f32x4 __attribute__((ext_vector_type(4)));
typedef short s16x4 __attribute__((ext_vector_type(4)));
typedef __attribute__((ext_vector_type(4))) __bf16 bf16x4;
typedef __attribute__((ext_vector_type(8))) __bf16 bf16x8;
#define LAS __attribute__((address_space(3)))

constexpr int DM = 640, HQ = 32, SK = 4096, DH = 192, WIN = 1024, FF = 1536;
constexpr int QD = HQ * DH;
constexpr float SCALE = 0.07216878364870322f, LOG2E = 1.44269504088896341f;
constexpr unsigned kSpinCap = 1u << 16;
#define GJ(g) (((g) >> 3) & 7)
#define GI(g) (((g) & 7) * 4 + ((g) >> 6))

#define D4A 124
#define D5A 94
#define D1 90
#define D2 134
#define D3 69
#define D4 76
#define D5B 81
#define PACE 60
#define PINIT 100
#define PMIN 50
constexpr unsigned X4A = D4A + DM_, X5A = X4A + D5A + DM_, X1 = X5A + D1 + DM_, X2 = X1 + D2 + DM_, X3 = X2 + D3 + DM_, X4 = X3 + D4 + DM_, X5B = X4 + D5B + DM_;
#define WOC 2
#define WOFF 6
#define WEARLY 1
#define DG2 12
#define XCOST 10
#define LEAD 12
#define TOFF 20
#define UG 360
#define SLOW 0
#define TLAND 160
#define PRET 1
#define ROWM 1
#define W3 15
#define ADOPT 1
#define ALT 1
#define PROBE 1
constexpr unsigned NUNIT = 384 + XCOST + 27 + 6 * WEARLY, PBURST = 30;
#define GSTEPS GA = 1, GB = 6, GC = 7, HD = 1
#define OML 2
#define EMODE (OML + 1)
constexpr int GP = 0, GSTEPS;

constexpr unsigned WS_H1 = 0;
constexpr unsigned WS_H4 = 2u << 20;
constexpr unsigned H4C = 20480, H4T = H4C + 3072;
constexpr unsigned WS_H35 = 4u << 20;
constexpr unsigned WS_OWN = WS_H35 + 80 * 16384;
constexpr unsigned WS_CS = WS_OWN + 32 * 16384;
constexpr unsigned WS_MB = WS_CS + 256 * 768;
constexpr unsigned WS_R = WS_MB + 256 * 128;
constexpr unsigned WS_END = WS_R + 128;
static_assert(WS_END <= (6u << 20), "ws map");
__device__ __forceinline__ unsigned tile35(int j, int q) { return WS_H35 + (unsigned)((j * 10 + (q >> 1)) * 16384 + (q & 1) * 4096); }
__device__ __forceinline__ unsigned h4s(int s) { return (unsigned)((s >> 1) * 16384 + (s & 1) * 4096); }

constexpr int L_WQ = 0;
constexpr int L_WO = 33792;
constexpr int L_GU = L_WO + 30720;
constexpr int L_WD = L_GU + 16384;
constexpr int L_X0 = L_WD + 8192;
constexpr int L_WR = L_X0 + 5120;
constexpr int L_XS = L_WR + 2048;
constexpr int L_Q = L_XS + 1024;
constexpr int L_OP = L_Q + 512;
constexpr int L_M = L_OP + 8192;
constexpr int L_H = L_M + 64;
constexpr int L_ST = L_H + 384;
constexpr int L_CS = L_ST + 64;
constexpr int L_CS2 = L_CS + 1024;
constexpr int L_CNT = L_CS2 + 1536;
constexpr int L_P = L_CNT + 96;
constexpr int L_O2 = L_P + 72 * 192;
constexpr int L_DBG = L_O2 + 3328;
constexpr int L_END = L_DBG + 2048;
static_assert(L_END <= 163840 && L_END > 81920, "LDS");

typedef __attribute__((ext_vector_type(2))) __bf16 bf16x2;
typedef float f32x2 __attribute__((ext_vector_type(2)));
__device__ __forceinline__ unsigned pk2(float a, float b) { return __builtin_bit_cast(unsigned, __builtin_convertvector((f32x2){a, b}, bf16x2)); }
__device__ __forceinline__ float lo16(unsigned w) { return __uint_as_float(w << 16); }
__device__ __forceinline__ float hi16(unsigned w) { return __uint_as_float(w & 0xffff0000u); }
__device__ __forceinline__ float bfr(float f) { return __uint_as_float(pk2(0.f, f)); }
template <int CTRL> __device__ __forceinline__ float dpp(float x) {
    return __builtin_bit_cast(float, __builtin_amdgcn_mov_dpp(__builtin_bit_cast(int, x), CTRL, 0xf, 0xf, true));
}
template <int CTRL> __device__ __forceinline__ unsigned dppu(unsigned x) { return (unsigned)__builtin_amdgcn_mov_dpp((int)x, CTRL, 0xf, 0xf, true); }
__device__ __forceinline__ float row16_sum(float v) {
    v += dpp<0xB1>(v); v += dpp<0x4E>(v); v += dpp<0x141>(v); v += dpp<0x140>(v); return v;
}
__device__ __forceinline__ float fmax2(float a, float b) { return __builtin_elementwise_maximum(a, b); }
__device__ __forceinline__ float fmax3(float a, float b, float c) { return fmax2(fmax2(a, b), c); }
__device__ __forceinline__ float x16_add(float v) { auto s = __builtin_amdgcn_permlane16_swap(__float_as_uint(v), __float_as_uint(v), false, false); return __uint_as_float(s[0]) + __uint_as_float(s[1]); }
__device__ __forceinline__ float x32_add(float v) { auto s = __builtin_amdgcn_permlane32_swap(__float_as_uint(v), __float_as_uint(v), false, false); return __uint_as_float(s[0]) + __uint_as_float(s[1]); }
__device__ __forceinline__ float wave_sum(float v) { return x32_add(x16_add(row16_sum(v))); }
__device__ __forceinline__ float rs32(float a, float b) { auto s = __builtin_amdgcn_permlane32_swap(__float_as_uint(a), __float_as_uint(b), false, false); return __uint_as_float(s[0]) + __uint_as_float(s[1]); }
__device__ __forceinline__ f32x2 sw16(float a, float b) { auto s = __builtin_amdgcn_permlane16_swap(__float_as_uint(a), __float_as_uint(b), false, false); return (f32x2){__uint_as_float(s[0]), __uint_as_float(s[1])}; }

#define GUARD(n) asm volatile("s_branch 1f\n\t.p2align 12,," #n "\n1:")
#define MARK(n) do {} while (0)
#define BAR() asm volatile("s_waitcnt lgkmcnt(0)\n\ts_barrier" ::: "memory")
#define VMW(n) asm volatile("s_waitcnt vmcnt(%0)" :: "i"(n) : "memory")

template <int OFF = 0> __device__ __forceinline__ u32x4 ld16_sc1(const void* sbase, unsigned voff) {
    u32x4 r; asm volatile("global_load_dwordx4 %0, %1, %2 offset:%3 sc1" : "=v"(r) : "v"(voff), "s"(sbase), "i"(OFF) : "memory"); return r;
}
__device__ __forceinline__ unsigned ld4_sc1(const void* sbase, unsigned voff) {
    unsigned r; asm volatile("global_load_dword %0, %1, %2 sc1" : "=v"(r) : "v"(voff), "s"(sbase) : "memory"); return r;
}
template <int OFF = 0> __device__ __forceinline__ void st16_sc1(void* sbase, unsigned voff, u32x4 v) {
    asm volatile("global_store_dwordx4 %0, %1, %2 offset:%3 sc1\n\ts_nop 1" :: "v"(voff), "v"(v), "s"(sbase), "i"(OFF) : "memory");
}
__device__ __forceinline__ void st4_sc1(void* sbase, unsigned voff, unsigned v) {
    asm volatile("global_store_dword %0, %1, %2 sc1" :: "v"(voff), "v"(v), "s"(sbase) : "memory");
}
__device__ __forceinline__ void st8(void* sbase, unsigned voff, u32x2 v) {
    asm volatile("global_store_dwordx2 %0, %1, %2" :: "v"(voff), "v"(v), "s"(sbase) : "memory");
}
__device__ __forceinline__ void st2(void* sbase, unsigned voff, unsigned v) {
    asm volatile("global_store_short %0, %1, %2" :: "v"(voff), "v"(v), "s"(sbase) : "memory");
}
constexpr int RK = 64, RV = 160;
template <int R, int IMM> __device__ __forceinline__ void ld16_nt(const void* sbase, unsigned voff) {
    asm volatile("global_load_dwordx4 v[%c3:%c4], %0, %1 offset:%2 nt" :: "v"(voff), "s"(sbase), "i"(IMM), "i"(R), "i"(R + 3) : "memory");
}
template <int R, int IMM> __device__ __forceinline__ void ld8_nt(const void* sbase, unsigned voff) {
    asm volatile("global_load_dwordx2 v[%c3:%c4], %0, %1 offset:%2 nt" :: "v"(voff), "s"(sbase), "i"(IMM), "i"(R), "i"(R + 1) : "memory");
}
template <int R> __device__ __forceinline__ void smf(f32x4& d, u32x4 a, unsigned idx) {
    asm volatile("v_smfmac_f32_16x16x64_bf16 %0, %1, v[%c3:%c4], %2" : "+v"(d) : "v"(a), "v"(idx), "i"(R), "i"(R + 7));
}
template <int A, int B> __device__ __forceinline__ void zip_in() {
    unsigned t;
    asm volatile("v_perm_b32 %0, v%c2, v%c1, %3\n\tv_perm_b32 v%c2, v%c2, v%c1, %4\n\tv_mov_b32 v%c1, %0" : "=&v"(t) : "i"(A), "i"(B), "s"(0x05040100u), "s"(0x07060302u));
}
template <int A, int B> __device__ __forceinline__ void zip_out(unsigned& lo, unsigned& hi) {
    asm volatile("v_perm_b32 %0, v%c3, v%c2, %4\n\tv_perm_b32 %1, v%c3, v%c2, %5" : "=&v"(lo), "=&v"(hi) : "i"(A), "i"(B), "s"(0x05040100u), "s"(0x07060302u));
}
template <int A, int B, int C, int D> __device__ __forceinline__ void zip_x(unsigned lo, unsigned hi) {
    asm volatile("v_perm_b32 v%c4, v%c3, v%c2, %6\n\tv_perm_b32 v%c5, v%c3, v%c2, %7\n\tv_mov_b32 v%c2, %0\n\tv_mov_b32 v%c3, %1"
                 :: "v"(lo), "v"(hi), "i"(A), "i"(B), "i"(C), "i"(D), "s"(0x05040100u), "s"(0x07060302u));
}
__device__ __forceinline__ void dma16(const void* sbase, unsigned voff, unsigned lds_dst) {
    unsigned keep;
    asm volatile("s_mov_b32 %0, m0\n\ts_mov_b32 m0, %3\n\ts_nop 0\n\tglobal_load_lds_dwordx4 %1, %2 nt\n\ts_mov_b32 m0, %0"
                 : "=&s"(keep) : "v"(voff), "s"(sbase), "s"(lds_dst) : "memory");
}
__device__ __forceinline__ void dma16p(const void* sbase, unsigned voff, unsigned lds_dst) {
    unsigned keep;
    asm volatile("s_mov_b32 %0, m0\n\ts_mov_b32 m0, %3\n\ts_nop 0\n\tglobal_load_lds_dwordx4 %1, %2\n\ts_mov_b32 m0, %0"
                 : "=&s"(keep) : "v"(voff), "s"(sbase), "s"(lds_dst) : "memory");
}
__device__ __forceinline__ unsigned um4(u32x4 a) { return max(max(a[0], a[1]), max(a[2], a[3])); }
__device__ __forceinline__ bool all_set(unsigned t) { return __builtin_amdgcn_ballot_w64(t == 0u) == 0ull; }
__device__ __forceinline__ bool any_empty(unsigned mx) { return __builtin_amdgcn_ballot_w64(mx == ~0u) != 0ull; }
__device__ __forceinline__ unsigned long long any_nan(float a, float b) { return __builtin_amdgcn_ballot_w64(__builtin_isunordered(a, b)); }
__device__ __forceinline__ f32x4 asf(u32x4 a) { return __builtin_bit_cast(f32x4, a); }
__device__ __forceinline__ u32x4 asu(f32x4 a) { return __builtin_bit_cast(u32x4, a); }
#define LANES(m) __builtin_amdgcn_inverse_ballot_w64(m)

__device__ __forceinline__ bf16x4 tr64(unsigned lds_addr) { return __builtin_amdgcn_ds_read_tr16_b64_v4bf16((LAS bf16x4*)(size_t)lds_addr); }
__device__ __forceinline__ bf16x4 tr64o(unsigned lds_addr, int off) { return __builtin_amdgcn_ds_read_tr16_b64_v4bf16((LAS bf16x4*)((LAS char*)(size_t)lds_addr + off)); }
__device__ __forceinline__ bf16x8 cat8(bf16x4 a, bf16x4 b) { return __builtin_shufflevector(a, b, 0, 1, 2, 3, 4, 5, 6, 7); }
__device__ __forceinline__ bf16x8 lds16(unsigned a) { return *(const LAS bf16x8*)(size_t)a; }
__device__ __forceinline__ bf16x4 lds8(unsigned a) { return *(const LAS bf16x4*)(size_t)a; }
__device__ __forceinline__ f32x4 mfma32(bf16x8 a, bf16x8 b, f32x4 c) { return __builtin_amdgcn_mfma_f32_16x16x32_bf16(a, b, c, 0, 0, 0); }
__device__ __forceinline__ f32x4 mfma32u(u32x4 a, u32x4 b, f32x4 c) { return mfma32(__builtin_bit_cast(bf16x8, a), __builtin_bit_cast(bf16x8, b), c); }
__device__ __forceinline__ f32x4 mfma16(bf16x4 a, bf16x4 b, f32x4 c) {
    return __builtin_amdgcn_mfma_f32_16x16x16bf16_1k(__builtin_bit_cast(s16x4, a), __builtin_bit_cast(s16x4, b), c, 0, 0, 0);
}

#define STAMP(i) do {} while (0)

__device__ __forceinline__ void wait_word(const void* sbase, unsigned off) {
    unsigned t, cnt = kSpinCap;
    asm volatile("1:\n\tglobal_load_dword %0, %2, %3 sc1\n\ts_sub_u32 %1, %1, 1\n\ts_waitcnt vmcnt(0)\n\tv_cmp_eq_u32 vcc, -1, %0\n\ts_cbranch_vccz 2f\n\ts_cbranch_scc0 1b\n2:"
                 : "=&v"(t), "+s"(cnt) : "v"(off), "s"(sbase) : "memory", "vcc", "scc");
}
template <int OFF = 0> __device__ __forceinline__ void st16m(void* sbase, unsigned voff, u32x4 v, unsigned long long mask) {
    asm volatile("s_mov_b64 exec, %4\n\tglobal_store_dwordx4 %0, %1, %2 offset:%3 sc1\n\ts_mov_b64 exec, -1\n\ts_nop 0" :: "v"(voff), "v"(v), "s"(sbase), "i"(OFF), "s"(mask) : "memory");
}
__device__ __forceinline__ void st8m(void* sbase, unsigned voff, u32x2 v, unsigned long long mask) {
    asm volatile("s_mov_b64 exec, %3\n\tglobal_store_dwordx2 %0, %1, %2 sc1\n\ts_mov_b64 exec, -1" :: "v"(voff), "v"(v), "s"(sbase), "s"(mask) : "memory");
}
template <int OFF> __device__ __forceinline__ void st16m2(void* sbase, unsigned voff, u32x4 v, unsigned voff2, u32x4 v2, unsigned long long mask) {
    asm volatile("s_mov_b64 exec, %6\n\tglobal_store_dwordx4 %0, %1, %2 sc1\n\tglobal_store_dwordx4 %4, %5, %2 offset:%3 sc1\n\ts_mov_b64 exec, -1\n\ts_nop 0"
                 :: "v"(voff), "v"(v), "s"(sbase), "i"(OFF), "v"(voff2), "v"(v2), "s"(mask) : "memory");
}
#define PINS(a) asm volatile("" : "+s"(a))
#define RETRY(LOAD, EMPTY, COMPUTE, NAN_, X) do { LOAD; COMPUTE; if (__builtin_expect((NAN_) != 0ull, 0)) { \
    _Pragma("unroll 1") for (unsigned spins_ = 0; spins_ < kSpinCap; ++spins_) { slip(X); LOAD; if (!any_empty(EMPTY)) break; } COMPUTE; } } while (0)
__device__ __forceinline__ unsigned now() { return (unsigned)__builtin_amdgcn_s_memrealtime(); }
__device__ __forceinline__ void until(unsigned t) {
    asm volatile("1:\n\ts_memrealtime vcc\n\ts_waitcnt lgkmcnt(0)\n\ts_sub_u32 vcc_lo, %0, vcc_lo\n\ts_cmp_lt_u32 vcc_lo, 0x400\n\ts_cbranch_scc1 1b" :: "s"(t - 1u) : "vcc", "scc", "memory");
}
__device__ __forceinline__ unsigned lds_word(unsigned a) {
    unsigned t_; asm volatile("ds_read_b32 %0, %1\n\ts_waitcnt lgkmcnt(0)\n\ts_nop 0" : "=v"(t_) : "v"(a) : "memory");
    return __builtin_amdgcn_readfirstlane(t_);
}
__device__ __forceinline__ void lds_set(unsigned a, unsigned v) { asm volatile("ds_write_b32 %0, %1" :: "v"(a), "v"(v) : "memory"); }
__device__ __forceinline__ int swz(int r) { return r ^ (((r >> 3) & 1) << 2); }
__device__ __forceinline__ f32x2 unpk(unsigned w) { return (f32x2){lo16(w), hi16(w)}; }
__device__ __forceinline__ unsigned pk2v(f32x2 v) { return __builtin_bit_cast(unsigned, __builtin_convertvector(v, bf16x2)); }
__device__ __forceinline__ float frcp(float x) { return __builtin_amdgcn_rcpf(x); }
__device__ __forceinline__ float ex2(float x) { return __builtin_amdgcn_exp2f(x); }
__device__ __forceinline__ int lane_id() { int l; asm volatile("v_mbcnt_lo_u32_b32 %0, -1, 0\n\tv_mbcnt_hi_u32_b32 %0, -1, %0" : "=v"(l)); return l & 63; }
__device__ __forceinline__ f32x2 ln_stats(float ss, float s) {
    const float mean = s * (1.f / DM);
    const float var = fmax2(__builtin_fmaf(-mean, mean, ss * (1.f / DM)), 0.f);
    return (f32x2){bfr(mean), frcp(bfr(__builtin_amdgcn_sqrtf(bfr(bfr(var) + 1e-5f))))};
}
#define GATHER8(a, o, S) do { a[0] = ld16_sc1<-4 * (S)>(ws, o); a[1] = ld16_sc1<-3 * (S)>(ws, o); a[2] = ld16_sc1<-2 * (S)>(ws, o); a[3] = ld16_sc1<-(S)>(ws, o); \
    a[4] = ld16_sc1<0>(ws, o); a[5] = ld16_sc1<(S)>(ws, o); a[6] = ld16_sc1<2 * (S)>(ws, o); a[7] = ld16_sc1<3 * (S)>(ws, o); } while (0)
template <int OFF> __device__ __forceinline__ void again16(u32x4& r, const void* sbase, unsigned voff) {
    asm volatile("v_cmp_eq_u32 vcc, -1, %1\n\ts_mov_b64 exec, vcc\n\tglobal_load_dwordx4 %0, %2, %3 offset:%4 sc1\n\ts_mov_b64 exec, -1" : "+v"(r) : "v"(um4(r)), "v"(voff), "s"(sbase), "i"(OFF) : "memory", "vcc");
}
#define AGAIN8(a, o, S) do { again16<-4 * (S)>(a[0], ws, o); again16<-3 * (S)>(a[1], ws, o); again16<-2 * (S)>(a[2], ws, o); again16<-(S)>(a[3], ws, o); \
    again16<0>(a[4], ws, o); again16<(S)>(a[5], ws, o); again16<2 * (S)>(a[6], ws, o); again16<3 * (S)>(a[7], ws, o); } while (0)
#define WAIT8(a, N) asm volatile("s_waitcnt vmcnt(%8)" : "+v"((a)[0]), "+v"((a)[1]), "+v"((a)[2]), "+v"((a)[3]), "+v"((a)[4]), "+v"((a)[5]), "+v"((a)[6]), "+v"((a)[7]) : "i"(N) : "memory")
#define UM32(a) max(max(max(um4(a[0]), um4(a[1])), max(um4(a[2]), um4(a[3]))), max(max(um4(a[4]), um4(a[5])), max(um4(a[6]), um4(a[7]))))
#define SUM8(a) (((((((asf(a[0]) + asf(a[1])) + asf(a[2])) + asf(a[3])) + asf(a[4])) + asf(a[5])) + asf(a[6])) + asf(a[7]))
#define WAIT1(x, N) asm volatile("s_waitcnt vmcnt(%1)" : "+v"(x) : "i"(N) : "memory")
#define LO2(x) __builtin_shufflevector(x, x, 0, 1)
#define HI2(x) __builtin_shufflevector(x, x, 2, 3)
#define ACC8(a, s) do { f32x2 l_, h_; \
    asm volatile("s_waitcnt vmcnt(5)\n\tv_pk_add_f32 %0, %2, %4\n\tv_pk_add_f32 %1, %3, %5\n\tv_pk_add_f32 %0, %0, %6\n\tv_pk_add_f32 %1, %1, %7\n\t" \
                 "s_waitcnt vmcnt(3)\n\tv_pk_add_f32 %0, %0, %8\n\tv_pk_add_f32 %1, %1, %9\n\tv_pk_add_f32 %0, %0, %10\n\tv_pk_add_f32 %1, %1, %11\n\t" \
                 "s_waitcnt vmcnt(1)\n\tv_pk_add_f32 %0, %0, %12\n\tv_pk_add_f32 %1, %1, %13\n\tv_pk_add_f32 %0, %0, %14\n\tv_pk_add_f32 %1, %1, %15\n\t" \
                 "s_waitcnt vmcnt(0)\n\tv_pk_add_f32 %0, %0, %16\n\tv_pk_add_f32 %1, %1, %17\n\ts_nop 0" \
                 : "=&v"(l_), "=&v"(h_) : "v"(LO2(a[0])), "v"(HI2(a[0])), "v"(LO2(a[1])), "v"(HI2(a[1])), "v"(LO2(a[2])), "v"(HI2(a[2])), "v"(LO2(a[3])), "v"(HI2(a[3])), \
                   "v"(LO2(a[4])), "v"(HI2(a[4])), "v"(LO2(a[5])), "v"(HI2(a[5])), "v"(LO2(a[6])), "v"(HI2(a[6])), "v"(LO2(a[7])), "v"(HI2(a[7])) : "memory"); \
    s = __builtin_shufflevector(l_, h_, 0, 1, 2, 3); } while (0)
#define RETRY8(a, o, S, s, MID, POST, NAN_, X) do { GATHER8(a, o, S); MID; ACC8(a, s); POST; if (__builtin_expect((NAN_) != 0ull, 0)) { \
    _Pragma("unroll 1") for (unsigned spins_ = 0; spins_ < kSpinCap; ++spins_) { slip(X); AGAIN8(a, o, S); WAIT8(a, 0); if (!any_empty(UM32(a))) break; } s = SUM8(a); POST; } } while (0)
#define PIN1(a) asm volatile("" : "+v"(a))
#define PIN2(a, b) asm volatile("" : "+v"(a), "+v"(b))
#define PIN3(a, b, c) asm volatile("" : "+v"(a), "+v"(b), "+v"(c))
#define LANE_VARS() const int lane = lane_id(); \
    const int kq = lane >> 4, n = lane & 15, q4 = n >> 2, p4 = lane & 3; \
    const int r0s = 8 * kq + q4 + 4 * (kq & 1), r1s = 8 * kq + q4 + 4 * ((kq & 1) ^ 1);     \
    const int r2s = swz(4 * kq + q4);                                                       \
    (void)r0s; (void)r1s; (void)r2s; (void)p4
template <int V> struct IC { static constexpr int value = V; };
struct Frag80 { bf16x8 a0, a1; bf16x4 a2; };

__global__ void __launch_bounds__(512, 2) __attribute__((amdgpu_num_vgpr(32)))
mega_kernel(const u16* __restrict__ x0, const u16* __restrict__ kc, const u16* __restrict__ vc, const u16* __restrict__ wq,
            const u16* __restrict__ wo, const u16* __restrict__ wr, const u16* __restrict__ wg, const u16* __restrict__ wu,
            const u16* __restrict__ wd, u16* __restrict__ out, unsigned char* __restrict__ ws_in
            ) {
    extern __shared__ __attribute__((aligned(16))) unsigned char smem[];
    const unsigned lb = (unsigned)(size_t)smem;
    unsigned char* const ws = (unsigned char*)(((uintptr_t)ws_in + (32u << 20) - 1) & ~(uintptr_t)((16u << 20) - 1));
    const int wave = __builtin_amdgcn_readfirstlane(threadIdx.x >> 6);
    const int g = blockIdx.x, gj = GJ(g), gi = GI(g);
    const f32x4 z4 = {0.f, 0.f, 0.f, 0.f};
    asm volatile("" ::: "v255");
    auto sched = [&](unsigned X, unsigned gap) __attribute__((always_inline)) {
        unsigned v; unsigned long long c;
        asm volatile("s_memrealtime %1\n\tds_read_b32 %0, %2\n\ts_waitcnt lgkmcnt(0)" : "=v"(v), "=&s"(c) : "v"(lb + L_CNT + 56) : "memory");
        const unsigned n = (unsigned)c + gap;
        unsigned t = __builtin_amdgcn_readfirstlane(v) + X;
        if ((int)(n - t) > 0) t = n;
        until(t);
    };
    auto slip = [&](unsigned X) __attribute__((always_inline)) {
        if (X) lds_set(lb + L_CNT + 56, now() - X);
    };
    float xe = 0.f;

    auto code_pf = [&](int slice, int lane) __attribute__((always_inline)) {
        unsigned long long pc0; unsigned len, back, t;
        asm volatile("s_getpc_b64 %0\n.Lmega_pf%=:\n\ts_mov_b32 %1, .Lmega_code_end-.Lmega_code_begin\n\ts_mov_b32 %2, .Lmega_pf%=-.Lmega_code_begin" : "=s"(pc0), "=s"(len), "=s"(back));
        unsigned off = (unsigned)(slice * 4096 + lane * 64); if (off > len) off = len;
        asm volatile("global_load_dword %0, %1, %2" : "=v"(t) : "v"(off & ~3u), "s"(pc0 - back) : "memory");
        return t;
    };
    asm volatile(".Lmega_code_begin:");
    auto fix_k1 = [&](auto IC_, auto NC) __attribute__((always_inline)) {
        constexpr int A = RK + 8 * decltype(IC_)::value, B = A + 4;
        unsigned t0, t1, t2, t3;
        asm volatile("s_waitcnt vmcnt(%12)\n\tv_mov_b32 %0, v%c4\n\tv_mov_b32 %1, v%c5\n\tv_mov_b32 %2, v%c6\n\tv_mov_b32 %3, v%c7\n\t"
                     "v_mov_b32_dpp v%c4, v%c8 row_ror:8 row_mask:0xf bank_mask:0xc\n\tv_mov_b32_dpp v%c5, v%c9 row_ror:8 row_mask:0xf bank_mask:0xc\n\t"
                     "v_mov_b32_dpp v%c6, v%c10 row_ror:8 row_mask:0xf bank_mask:0xc\n\tv_mov_b32_dpp v%c7, v%c11 row_ror:8 row_mask:0xf bank_mask:0xc\n\t"
                     "v_mov_b32_dpp v%c8, %0 row_ror:8 row_mask:0xf bank_mask:0x3\n\tv_mov_b32_dpp v%c9, %1 row_ror:8 row_mask:0xf bank_mask:0x3\n\t"
                     "v_mov_b32_dpp v%c10, %2 row_ror:8 row_mask:0xf bank_mask:0x3\n\tv_mov_b32_dpp v%c11, %3 row_ror:8 row_mask:0xf bank_mask:0x3"
                     : "=&v"(t0), "=&v"(t1), "=&v"(t2), "=&v"(t3)
                     : "i"(A), "i"(A + 1), "i"(A + 2), "i"(A + 3), "i"(B), "i"(B + 1), "i"(B + 2), "i"(B + 3), "i"(decltype(NC)::value) : "memory");
    };
    auto trans_v = [&](auto UC, auto NC) __attribute__((always_inline)) {
        constexpr int R = RV + 4 * decltype(UC)::value, N = decltype(NC)::value;
#define CELL(r, c) R + 16 * (r) + (c), R + 16 * (r) + 8 + (c)
        unsigned a0, a1, b0, b1, c0, c1;
        VMW(N + 6); zip_in<CELL(0, 0)>(); zip_out<CELL(0, 1)>(a0, a1); zip_out<CELL(0, 2)>(b0, b1); zip_out<CELL(0, 3)>(c0, c1);
        VMW(N + 4); zip_x<CELL(1, 0), CELL(0, 1)>(a0, a1); zip_in<CELL(1, 1)>(); zip_out<CELL(1, 2)>(a0, a1);
        VMW(N + 2); zip_x<CELL(2, 0), CELL(0, 2)>(b0, b1); zip_x<CELL(2, 1), CELL(1, 2)>(a0, a1); zip_in<CELL(2, 2)>(); zip_out<CELL(1, 3)>(a0, a1); zip_out<CELL(2, 3)>(b0, b1);
        VMW(N); zip_x<CELL(3, 0), CELL(0, 3)>(c0, c1); zip_x<CELL(3, 1), CELL(1, 3)>(a0, a1); zip_x<CELL(3, 2), CELL(2, 3)>(b0, b1); zip_in<CELL(3, 3)>();
#undef CELL
    };
    auto trans_w = [&](auto UC, auto NC) __attribute__((always_inline)) {
        constexpr int R = RV + 64 + 2 * decltype(UC)::value, N = decltype(NC)::value;
        unsigned a0, a1;
        VMW(N + 2); zip_in<R, R + 8>(); zip_out<R + 1, R + 9>(a0, a1);
        VMW(N); zip_x<R + 16, R + 24, R + 1, R + 9>(a0, a1); zip_in<R + 17, R + 25>();
    };
    auto frag80 = [&](unsigned im, int rs, int r0s, int r1s, int r2s) __attribute__((always_inline)) -> Frag80 {
        return Frag80{cat8(tr64(im + r0s * rs), tr64(im + r1s * rs)), cat8(tr64(im + (r0s + 32) * rs), tr64(im + (r1s + 32) * rs)), tr64(im + (r2s + 64) * rs)};
    };
    auto mm80 = [&](const Frag80& a, const Frag80& b) __attribute__((always_inline)) -> f32x4 {
        return mfma32(a.a1, b.a1, mfma32(a.a0, b.a0, z4)) + mfma16(a.a2, b.a2, z4);
    };
    auto rows80 = [&](unsigned a16, unsigned a8) __attribute__((always_inline)) -> Frag80 { const Frag80 f{lds16(a16), lds16(a16 + 64), lds8(a8)}; __builtin_amdgcn_sched_barrier(0); return f; };
    auto empty35 = [&](int layer, unsigned S, int lane) __attribute__((always_inline)) {
        st16_sc1(ws, tile35(gj, 10 * layer + (lane >> 3)) + S + (unsigned)(gi * 128 + (lane & 7) * 16), (u32x4){~0u, ~0u, ~0u, ~0u});
        st16m(ws, S + (lane < 16 ? tile35(gj, 10 * layer + 8 + (lane >> 3)) + (unsigned)(gi * 128 + (lane & 7) * 16) : WS_OWN + (unsigned)(gi * 16384 + layer * 4096 + gj * 512 + (lane - 16) * 16)),
              (u32x4){~0u, ~0u, ~0u, ~0u}, 0xffffffffffffull);
    };
    auto empty5 = [&](int q0, unsigned S, int lane) __attribute__((always_inline)) {
        st16m(ws, tile35(gj, q0 + (lane >> 3)) + S + (unsigned)(gi * 128 + (lane & 7) * 16), (u32x4){~0u, ~0u, ~0u, ~0u}, 0xffffffffffull);
    };
    auto empty4 = [&](int layer, unsigned S, int lane) __attribute__((always_inline)) {
        const unsigned b = WS_H4 + (unsigned)((layer * 32 + gi) * 32768) + (S ^ 8192u);
        st16_sc1(ws, b + (unsigned)((lane >> 5) * 4096 + gj * 512 + (lane & 31) * 16), (u32x4){~0u, ~0u, ~0u, ~0u});
        st16m(ws, b + (unsigned)(lane < 32 ? 16384 + gj * 512 + lane * 16 : H4C + ((lane - 32) >> 3) * 1024 + gj * 128 + (lane & 7) * 16), (u32x4){~0u, ~0u, ~0u, ~0u},
              layer ? 0xff000000ffffffffull : ~0ull);
    };
    const unsigned tst = now();
    int lead = wave == 7 ? LEAD * PACE * 4 : 0;
    auto pace = [&](unsigned n, bool wait = true) __attribute__((always_inline)) {
        unsigned u, w, t0;
        asm volatile("s_mov_b64 exec, 1\n\tds_add_rtn_u32 %0, %3, %4\n\tds_max_u32 %3, %5 offset:12\n\tds_add_rtn_u32 %1, %3, %6 offset:12\n\tds_read_b32 %2, %3 offset:4\n\ts_mov_b64 exec, -1\n\ts_waitcnt lgkmcnt(0)"
                     : "=&v"(u), "=&v"(w), "=&v"(t0) : "v"(lb + L_CNT + 60), "v"(n), "v"((now() - tst) << 6), "v"(n * PMIN * 4u) : "memory");
        int tn = (int)(__builtin_amdgcn_readfirstlane(t0) - tst + PINIT) * 64 + __builtin_amdgcn_readfirstlane(u) * (PACE * 4);
        tn += max((int)__builtin_amdgcn_readfirstlane(u) - UG, 0) * (SLOW * 4);
        const int tw = __builtin_amdgcn_readfirstlane(w) - PBURST * 64;
        if (tw > tn) tn = tw;
        tn -= lead;
        if (wait) {
#pragma unroll 1
            for (int k = 0; k < 2048 && (int)((now() - tst) << 6) < tn; ++k) {}
        }
    };
    auto slot = [&](unsigned u) __attribute__((always_inline)) {
        const unsigned tn = lds_word(lb + L_CNT + 64) + PINIT + (u * PACE + (NUNIT - UG) * SLOW) / 16;
#pragma unroll 1
        for (int k = 0; k < 8192 && (int)(now() - tn) < 0; ++k) __builtin_amdgcn_s_sleep(1);
    };
    auto load_wo = [&](int k, int lane, bool early) __attribute__((always_inline)) {
        const int c = k * 64 + lane, pp = c / 10, cc = c - 10 * pp;
        const unsigned f = (unsigned)(swz(pp) * 1280 + cc * 16);
        const u16* wb = wo + (size_t)gi * DH * DM + gj * 80;
#pragma unroll
        for (int a2 = 0; a2 < 6; ++a2) { if (a2 % 3 == 0) { if (early) pace(3); else slot(NUNIT - WOFF + (2 * k + a2 / 3) * WOC); } dma16(wb + 32 * a2 * DM, f, lb + L_WO + (5 * a2 + k) * 1024); }
    };
    auto load_wd = [&](int lane) __attribute__((always_inline)) {
        const u16* db = wd + gi * 48 * DM + gj * 80;
#pragma unroll 1
        for (int k = 0; k < 5; ++k) {
            const int c = k * 64 + lane, pp = c / 10, cc = c - 10 * pp;
            const unsigned fd = (unsigned)(swz(pp) * 1280 + cc * 16);
            if (!(k & 1)) pace(3);
            dma16(db, fd, lb + L_WD + k * 1024);
            if (k < 3) dma16(db + 32 * DM, (k < 2 || lane < 32) ? fd : 0u, lb + L_WD + (5 + k) * 1024);
        }
    };
    auto load_gu = [&](int m, int lane) __attribute__((always_inline)) {
#pragma unroll
        for (int k = 0; k < 3; ++k) {
            const int c = k * 64 + lane, row = c / 6, cc = c - 6 * row;
            const unsigned o = (unsigned)((swz(row) * FF + cc * 8) * 2), d = lb + L_GU + m * 8192 + k * 1024;
            const u16* wb = (m == 0 ? wg : wu) + gj * 80 * FF + gi * 48;
            pace(3);
            dma16(wb, o, d); dma16(wb + 32 * FF, o, d + 3072);
            if (k < 2) dma16(wb + 64 * FF, (k == 0 || lane < 32) ? o : 0u, d + 6144);
        }
    };
    auto lds_add = [&](int off, unsigned v) __attribute__((always_inline)) { asm volatile("s_mov_b64 exec, 1\n\tds_add_u32 %0, %1\n\ts_mov_b64 exec, -1" :: "v"(lb + L_CNT + off), "v"(v) : "memory"); };
    auto landed = [&]() __attribute__((always_inline)) {
        asm volatile("ds_max_u32 %0, %1" :: "v"(lb + L_CNT + 64), "v"(now() - TLAND) : "memory");
        lds_add(0, 1u);
    };
    auto send_flag = [&](int lane) __attribute__((always_inline)) {
        st4_sc1(ws, WS_MB + (unsigned)((gi * 8 + ((gj == (gi & 7) ? lane : gi) & 7)) * 128 + gj * 4), lds_word(lb + L_CNT + 68));
    };
    auto tiles4 = [&](const Frag80 (&f)[4], const Frag80& b, unsigned long long mk) __attribute__((always_inline)) -> u32x4 {
        f32x4 c[4], d[4];
#pragma unroll
        for (int i = 0; i < 4; ++i) c[i] = mfma32(f[i].a0, b.a0, z4);
#pragma unroll
        for (int i = 0; i < 4; ++i) { c[i] = mfma32(f[i].a1, b.a1, c[i]); d[i] = mfma16(f[i].a2, b.a2, z4); }
        __builtin_amdgcn_sched_barrier(0);
        f32x2 lo, hi;
        asm volatile("s_nop 1\n\ts_mov_b64 exec, %18\n\tv_pk_add_f32 %0, %2, %10\n\tv_pk_add_f32 %1, %3, %11\n\t"
                     "s_lshl_b64 exec, exec, 4\n\tv_pk_add_f32 %0, %4, %12\n\tv_pk_add_f32 %1, %5, %13\n\t"
                     "s_lshl_b64 exec, exec, 4\n\tv_pk_add_f32 %0, %6, %14\n\tv_pk_add_f32 %1, %7, %15\n\t"
                     "s_lshl_b64 exec, exec, 4\n\tv_pk_add_f32 %0, %8, %16\n\tv_pk_add_f32 %1, %9, %17\n\ts_mov_b64 exec, -1"
                     : "=&v"(lo), "=&v"(hi) : "v"(LO2(c[0])), "v"(HI2(c[0])), "v"(LO2(c[1])), "v"(HI2(c[1])), "v"(LO2(c[2])), "v"(HI2(c[2])), "v"(LO2(c[3])), "v"(HI2(c[3])),
                       "v"(LO2(d[0])), "v"(HI2(d[0])), "v"(LO2(d[1])), "v"(HI2(d[1])), "v"(LO2(d[2])), "v"(HI2(d[2])), "v"(LO2(d[3])), "v"(HI2(d[3])), "s"(mk) : "scc");
        return asu(__builtin_shufflevector(lo, hi, 0, 1, 2, 3));
    };
    auto empty1 = [&](int half, unsigned S, int lane) __attribute__((always_inline)) {
        const unsigned o = WS_H1 + S + (unsigned)((gi * 4 + 2 * half) * 16384 + gj * 896 + min(lane, 55) * 16);
        st16_sc1(ws, o, (u32x4){~0u, ~0u, ~0u, ~0u}); st16_sc1(ws, o + 16384, (u32x4){~0u, ~0u, ~0u, ~0u});
    };
    auto report = [&]() __attribute__((always_inline)) { if (gj == (gi & 7)) st4_sc1(ws, WS_R + (unsigned)(gi * 4), lds_word(lb + L_CNT + 88)); };
    auto answer = [&](int lane, unsigned r) __attribute__((always_inline)) {
        int d = min(max((int)(r - tst), -400), 600) * 512 + (int)((r >> 1) & 15u);
        d += (int)dppu<0xB1>((unsigned)d); d += (int)dppu<0x4E>((unsigned)d); d += (int)dppu<0x141>((unsigned)d); d += (int)dppu<0x140>((unsigned)d);
        d = __builtin_amdgcn_readlane(d, 0) + __builtin_amdgcn_readlane(d, 16);
        st4_sc1(ws, WS_MB + (unsigned)(((lane & 31) * 8 + (lane & 7)) * 128 + 32), ((tst + (d >> 14) + TOFF) & ~3u) | ((d & 511) > 128 ? 3u : 1u));
        lds_set(lb + L_CNT + 36, 1u);
    };
    auto adopt = [&](unsigned t) __attribute__((always_inline)) {
        if (ADOPT && t - tst + 256u < 768u) lds_set(lb + L_CNT + 64, t);
        lds_set(lb + L_CNT + 80, lds_word(lb + L_CNT + 92) ^ (t & 2u) << 12);
        lds_set(lb + L_CNT + 84, (((t << 1) ^ lds_word(lb + L_CNT + 68)) & 4u) << 11);
        lds_set(lb + L_CNT + 52, 1u);
    };
    struct StatAddr { unsigned a16, b16, a8, b8, dst; };
    auto stats_addr = [&](int lane, unsigned dbase, int dstride) __attribute__((always_inline)) -> StatAddr {
        const int kq = lane >> 4, q4 = (lane >> 2) & 3, p4 = lane & 3;
        const int x1 = LANES(0x4444444444444444ull) ? L_WR + gj * 160 : L_XS + q4 * 160, xa = LANES(0xaaaaaaaaaaaaaaaaull) ? L_XS + 640 : x1;
        StatAddr r{lb + (unsigned)(xa + kq * 16), lb + (unsigned)(L_XS + p4 * 160 + kq * 16), lb + (unsigned)(xa + 128 + kq * 8), lb + (unsigned)(L_XS + p4 * 160 + 128 + kq * 8), dbase + (unsigned)(kq * dstride)};
        PIN3(r.a16, r.b16, r.dst); PIN2(r.a8, r.b8);
        return r;
    };
    auto stats = [&](const StatAddr& r) __attribute__((always_inline)) {
        const bf16x8 a0 = lds16(r.a16), b0 = lds16(r.b16), a1 = lds16(r.a16 + 64), b1 = lds16(r.b16 + 64);
        const bf16x4 a2 = lds8(r.a8), b2 = lds8(r.b8);
        unsigned long long mk = 0x0008000400020001ull; PINS(mk);
        __builtin_amdgcn_sched_barrier(0);
        st16m(ws, r.dst, asu(mfma32(a1, b1, mfma32(a0, b0, z4)) + mfma16(a2, b2, z4)), mk);
    };
    unsigned qa, pw, ma, idx, qm;
    auto qk_addr = [&](bool L1) __attribute__((always_inline)) {
        LANE_VARS();
        qa = lb + L_Q + (unsigned)(kq * 32 + (lane & 2) * 8);
        pw = lb + L_P + (unsigned)((7 + 8 * wave) * 192 + (kq >> 1) * 64 + (n >> 3) * 32 + (lane & 2) * 8 + (q4 & 1) * 8 + (kq & 1) * 4 + (lane & 1) * 2);
        ma = lb + L_M + (unsigned)(wave * 4);
        idx = (lane & 2) ? 0xeeeeu : 0x4444u;
        qm = lb + L_Q;
        PIN3(qa, pw, ma); PIN2(idx, qm);
        if (L1 && wave < 4) __builtin_amdgcn_s_setprio(2);
    };
    auto att_qk0 = [&]() __attribute__((always_inline)) {
        qk_addr(false);
#pragma unroll 1
        for (unsigned spins = 0; spins < (kSpinCap << 4); ++spins) {
            if (lds_word(lb + L_CNT + 4) != 0u) break;
            __builtin_amdgcn_s_sleep(1);
        }
        STAMP(3);
        const u32x4 q0 = *(const LAS u32x4*)(size_t)qa, q1 = *(const LAS u32x4*)(size_t)(qa + 128), q2 = *(const LAS u32x4*)(size_t)(qa + 256);
        f32x4 c0 = z4, c1 = z4, c2 = z4, c3 = z4;
#define SMF(c, q, r) "v_smfmac_f32_16x16x64_bf16 %" #c ", %" #q ", v[" #r "], %7\n\t"
        asm volatile("s_nop 1\n\t" SMF(0, 4, 64:71) SMF(1, 4, 88:95) SMF(2, 4, 112:119) SMF(3, 4, 136:143) SMF(0, 5, 72:79) SMF(1, 5, 96:103) SMF(2, 5, 120:127) SMF(3, 5, 144:151)
                     SMF(0, 6, 80:87) SMF(1, 6, 104:111) SMF(2, 6, 128:135) SMF(3, 6, 152:159) "s_nop 7"
                     : "+v"(c0), "+v"(c1), "+v"(c2), "+v"(c3) : "v"(q0), "v"(q1), "v"(q2), "v"(idx));
#undef SMF
        const float s0 = c0[0] + c0[2], s1 = c1[0] + c1[2], s2 = c2[0] + c2[2], s3 = c3[0] + c3[2];
        float m = fmax2(fmax3(s0, s1, s2), s3);
        m = fmax2(m, dpp<0xB1>(m)); m = fmax2(m, dpp<0x4E>(m)); m = fmax2(m, dpp<0x124>(m)); m = fmax2(m, dpp<0x128>(m));
        const float s01 = LANES(0xffff0000ffff0000ull) ? s1 : s0, s23 = LANES(0xffff0000ffff0000ull) ? s3 : s2;
        const float sv = LANES(0xffffffff00000000ull) ? s23 : s01;
        m = bfr(bfr(m) * SCALE);
        const float p = ex2((bfr(bfr(sv) * SCALE) - m) * LOG2E);
        *(LAS u16*)(size_t)pw = (u16)pk2(p, p);
        *(LAS float*)(size_t)ma = m;
        __builtin_amdgcn_sched_barrier(0);
    };
    unsigned pr, oa, oc, cma, cxa, cra, cso;
    auto att_addr = [&]() __attribute__((always_inline)) {
        LANE_VARS();
        pr = lb + L_P + (unsigned)((7 + 8 * wave - 2 * q4 - (lane & 1)) * 192 + kq * 32 + (lane & 2) * 8);
        oa = lb + L_OP + (unsigned)(wave * 1024 + (lane & 7) * 64 + kq * 16 + (n >> 3) * 8);
        oc = lb + L_OP + (unsigned)(wave * 1024 + 512 + (kq >> 1) * 256 + (lane & 7) * 32 + (kq & 1) * 16 + (n >> 3) * 8);
        PIN3(pr, oa, oc);
    };
    auto comb_out = [&](float u0, float u1, unsigned so, unsigned long long mk, bool both) __attribute__((always_inline)) {
        const unsigned w = min(pk2(u0, u1), 0xfffffffeu);
        const u32x4 w4 = {both ? dppu<0x00>(w) : w, dppu<0x55>(w), dppu<0xAA>(w), dppu<0xFF>(w)};
        st16m(ws, so, w4, mk);
    };
    auto max8 = [&](f32x4 m0, f32x4 m1) __attribute__((always_inline)) { return fmax3(fmax3(m0[0], m0[1], m0[2]), fmax3(m0[3], m1[0], m1[1]), fmax2(m1[2], m1[3])) * LOG2E; };
    auto comb_o0 = [&](f32x4 m0, f32x4 m1, f32x4 mm, f32x4 o0, f32x4 o1, f32x4 o2, f32x4 o3, unsigned so, unsigned long long mk, bool both) __attribute__((always_inline)) {
        const float M = max8(m0, m1);
        f32x4 f;
#pragma unroll
        for (int k = 0; k < 4; ++k) f[k] = ex2(__builtin_fmaf(mm[k], LOG2E, -M));
        const f32x4 o = f[3] * o3 + (f[2] * o2 + (f[1] * o1 + f[0] * o0));
        comb_out(rs32(o[0], o[2]), rs32(o[1], o[3]), so, mk, both);
    };
    auto comb_o1 = [&](f32x4 m0, f32x4 m1, f32x2 mm, f32x4 o0, f32x4 o1, unsigned so, unsigned long long mk, bool both) __attribute__((always_inline)) {
        const float M = max8(m0, m1);
        const f32x4 o = ex2(__builtin_fmaf(mm[1], LOG2E, -M)) * o1 + ex2(__builtin_fmaf(mm[0], LOG2E, -M)) * o0;
        comb_out(x16_add(rs32(o[0], o[2])), x16_add(rs32(o[1], o[3])), so, mk, both);
    };
    auto ml_addr = [&](int lane, unsigned& xa, unsigned& ra) __attribute__((always_inline)) {
        xa = lb + L_M + (unsigned)(((lane & 15) >> 1) * 4); ra = lb + L_P + (unsigned)((7 + 8 * ((lane & 15) >> 1)) * 192 + (lane & 1) * 64 + (lane >> 4) * 16);
    };
    auto comb_ml = [&](unsigned ma, unsigned xa, unsigned ra, unsigned so, bool both) __attribute__((always_inline)) {
        const u32x4 x4 = *(const LAS u32x4*)(size_t)ra;
        const f32x4 m0 = *(const LAS f32x4*)(size_t)ma, m1 = *(const LAS f32x4*)(size_t)(ma + 16);
        const float mw = *(const LAS float*)(size_t)xa;
        __builtin_amdgcn_sched_barrier(0);
        const f32x4 sp = mfma32u((u32x4){0x3f803f80u, 0x3f803f80u, 0x3f803f80u, 0x3f803f80u}, x4, z4);
        __builtin_amdgcn_sched_barrier(0);
        const float M = max8(m0, m1);
        float L = row16_sum(sp[0] * ex2(__builtin_fmaf(mw, LOG2E, -M)));
        if (both) L = dpp<0x00>(L);
        st16m(ws, so, asu((f32x4){M, L, M, L}), both ? 3ull : 1ull);
    };
    auto att_pv0 = [&]() __attribute__((always_inline)) {
        u32x4 bm[8];
        trans_v(IC<0>{}, IC<24>{}); trans_v(IC<1>{}, IC<16>{});
#pragma unroll
        for (int e = 0; e < 8; ++e) bm[e] = *(const LAS u32x4*)(size_t)(pr + e * 192);
        __builtin_amdgcn_sched_barrier(0);
        f32x4 da = z4, dc = z4;
        PIN2(da, dc);
        asm volatile("s_nop 1");
        __builtin_amdgcn_sched_barrier(0);
        f32x2 r;
        smf<RV>(da, bm[0], idx); smf<RV + 8>(da, bm[1], idx); smf<RV + 16>(da, bm[2], idx); smf<RV + 24>(da, bm[3], idx);
        smf<RV + 32>(da, bm[4], idx); smf<RV + 40>(da, bm[5], idx); smf<RV + 48>(da, bm[6], idx); smf<RV + 56>(da, bm[7], idx);
        asm volatile("s_nop 7\n\tv_pk_add_f32 %0, %1, %2\n\tds_write_b64 %3, %0\n\ts_mov_b64 exec, 1\n\tds_add_u32 %4, %5\n\ts_mov_b64 exec, -1"
                     : "=&v"(r) : "v"(LO2(da)), "v"(HI2(da)), "v"(oa), "v"(lb + L_CNT + 24), "v"(1u) : "memory");
        GUARD(900);
        MARK(b_t5);
        trans_w(IC<0>{}, IC<12>{}); trans_w(IC<1>{}, IC<8>{}); trans_w(IC<2>{}, IC<4>{}); trans_w(IC<3>{}, IC<0>{});
        STAMP(4);
        asm volatile("s_nop 1\n\tv_smfmac_f32_16x16x64_bf16 %0, %1, v[224:231], %5\n\tv_smfmac_f32_16x16x64_bf16 %0, %2, v[232:239], %5\n\t"
                     "v_smfmac_f32_16x16x64_bf16 %0, %3, v[240:247], %5\n\tv_smfmac_f32_16x16x64_bf16 %0, %4, v[248:255], %5\n\ts_nop 6"
                     : "+v"(dc) : "v"(bm[0]), "v"(bm[1]), "v"(bm[2]), "v"(bm[3]), "v"(idx));
        asm volatile("s_nop 0\n\tv_pk_add_f32 %0, %1, %2\n\tds_write_b64 %3, %0" : "=&v"(r) : "v"(LO2(dc)), "v"(HI2(dc)), "v"(oc) : "memory");
        STAMP(20);
    };
    unsigned long long ck1 = 0xffff0000ffff0000ull, ck2 = 0xffffffff00000000ull, csc = 0x3d93cd3a3d93cd3aull;
    auto att1 = [&]() __attribute__((always_inline)) {
        constexpr int layer = 1;
        {
#define SMF(c, q, r) "v_smfmac_f32_16x16x64_bf16 v[" #c "], v[" #q "], v[" #r "], %0\n\t"
#define MAXD(x) "v_max_f32_dpp v41, v41, v41 " x " row_mask:0xf bank_mask:0xf\n\t"
            asm volatile("s_waitcnt lgkmcnt(0)\n\ts_barrier\n\t"
                         "ds_read_b128 v[0:3], %1\n\tds_read2_b32 v[12:13], %2 offset0:96 offset1:100\n\tds_read_b128 v[4:7], %1 offset:128\n\tds_read_b128 v[8:11], %1 offset:256\n\t"
                         "v_mov_b64 v[16:17], 0\n\tv_mov_b64 v[18:19], 0\n\tv_mov_b64 v[20:21], 0\n\tv_mov_b64 v[22:23], 0\n\tv_mov_b64 v[24:25], 0\n\tv_mov_b64 v[26:27], 0\n\t"
                         "v_mov_b64 v[28:29], 0\n\tv_mov_b64 v[30:31], 0\n\tv_mov_b64 v[32:33], 0\n\tv_mov_b64 v[34:35], 0\n\tv_mov_b64 v[36:37], 0\n\tv_mov_b64 v[38:39], 0\n\t"
                         "s_waitcnt lgkmcnt(3)\n\t" SMF(16:19, 0:3, 64:71) SMF(20:23, 0:3, 88:95) "s_waitcnt lgkmcnt(1)\n\tv_mul_f32 v13, 0x3acccccd, v13\n\t"
                         SMF(24:27, 0:3, 112:119) "v_mul_f32 v12, 0x3acccccd, v12\n\t" SMF(28:31, 0:3, 136:143) "v_fma_f32 v12, -v13, v13, v12\n\t"
                         SMF(16:19, 4:7, 72:79) "v_maximum3_f32 v12, v12, 0, 0\n\t" SMF(20:23, 4:7, 96:103) "v_cvt_pk_bf16_f32 v12, 0, v12\n\t"
                         SMF(24:27, 4:7, 120:127) "v_add_f32 v12, 0x3727c5ac, v12\n\t" SMF(28:31, 4:7, 144:151) "v_cvt_pk_bf16_f32 v12, 0, v12\n\ts_waitcnt lgkmcnt(0)\n\t"
                         SMF(16:19, 8:11, 80:87) "v_sqrt_f32 v12, v12\n\t" SMF(20:23, 8:11, 104:111) "v_cvt_pk_bf16_f32 v12, 0, v12\n\t"
                         SMF(24:27, 8:11, 128:135) "v_rcp_f32 v12, v12\n\t" SMF(28:31, 8:11, 152:159)
                         "s_nop 2\n\tv_add_f32 v42, v16, v18\n\ts_nop 0\n\tv_add_f32 v43, v20, v22\n\tv_max_f32 v41, v42, v43\n\tv_add_f32 v44, v24, v26\n\tv_add_f32 v45, v28, v30\n\t"
                         "v_max3_f32 v41, v41, v44, v45\n\tv_cndmask_b32 v42, v42, v43, %8\n\tv_cndmask_b32 v44, v44, v45, %8\n\t"
                         MAXD("quad_perm:[1,0,3,2]") "v_cndmask_b32 v40, v42, v44, %9\n\ts_nop 0\n\t" MAXD("quad_perm:[2,3,0,1]") "s_nop 1\n\t" MAXD("row_ror:4") "s_nop 1\n\t" MAXD("row_ror:8")
                         "v_pk_mul_f32 v[40:41], v[12:13], v[40:41] op_sel_hi:[0,1]\n\tv_cvt_pk_bf16_f32 v40, 0, v40\n\tv_cvt_pk_bf16_f32 v41, 0, v41\n\t"
                         "v_pk_mul_f32 v[40:41], %10, v[40:41]\n\tv_cvt_pk_bf16_f32 v40, 0, v40\n\tv_cvt_pk_bf16_f32 v41, 0, v41\n\t"
                         "v_sub_f32 v40, v40, v41\n\tv_mul_f32 v40, 0x3fb8aa3b, v40\n\tv_exp_f32 v40, v40\n\ts_nop 0\n\tv_cvt_pk_bf16_f32 v40, v40, v40\n\tds_write_b16 %3, v40\n\t"
                         "ds_read_b128 v[0:3], %4\n\tds_read_b128 v[4:7], %4 offset:192\n\tds_read_b128 v[8:11], %4 offset:384\n\tds_read_b128 v[12:15], %4 offset:576\n\t"
                         "ds_read_b128 v[16:19], %4 offset:768\n\tds_read_b128 v[20:23], %4 offset:960\n\tds_read_b128 v[24:27], %4 offset:1152\n\tds_read_b128 v[28:31], %4 offset:1344\n\t"
                         "ds_write_b32 %5, v41\n\t"
                         "s_waitcnt lgkmcnt(8)\n\t" SMF(32:35, 0:3, 160:167) SMF(36:39, 0:3, 224:231) "s_waitcnt lgkmcnt(7)\n\t" SMF(32:35, 4:7, 168:175) SMF(36:39, 4:7, 232:239)
                         "s_waitcnt lgkmcnt(6)\n\t" SMF(32:35, 8:11, 176:183) SMF(36:39, 8:11, 240:247) "s_waitcnt lgkmcnt(4)\n\t" SMF(32:35, 12:15, 184:191) SMF(36:39, 12:15, 248:255)
                         SMF(32:35, 16:19, 192:199) "s_waitcnt lgkmcnt(1)\n\t" SMF(32:35, 20:23, 200:207) SMF(32:35, 24:27, 208:215) SMF(32:35, 28:31, 216:223)
                         "s_nop 2\n\tv_pk_add_f32 v[36:37], v[36:37], v[38:39]\n\tds_write_b64 %7, v[36:37]\n\ts_nop 2\n\tv_pk_add_f32 v[32:33], v[32:33], v[34:35]\n\tds_write_b64 %6, v[32:33]"
                         :: "v"(idx), "v"(qa), "v"(qm), "v"(pw), "v"(pr), "v"(ma), "v"(oa), "v"(oc), "s"(ck1), "s"(ck2), "s"(csc)
                         : "memory", "v0", "v1", "v2", "v3", "v4", "v5", "v6", "v7", "v8", "v9", "v10", "v11", "v12", "v13", "v14", "v15", "v16", "v17", "v18", "v19", "v20", "v21", "v22", "v23",
                           "v24", "v25", "v26", "v27", "v28", "v29", "v30", "v31", "v32", "v33", "v34", "v35", "v36", "v37", "v38", "v39", "v40", "v41", "v42", "v43", "v44", "v45");
#undef SMF
#undef MAXD
            STAMP(20 + 32 * layer);
            BAR();
            if (__builtin_expect(wave == 0, 1)) {
                STAMP(5 + 32 * layer);
                comb_o0(*(const LAS f32x4*)(size_t)cma, *(const LAS f32x4*)(size_t)(cma + 16), *(const LAS f32x4*)(size_t)cxa, *(const LAS f32x4*)(size_t)cra,
                        *(const LAS f32x4*)(size_t)(cra + 1024), *(const LAS f32x4*)(size_t)(cra + 2048), *(const LAS f32x4*)(size_t)(cra + 3072), cso, 0x1111111111111111ull, false);
            }
            __builtin_amdgcn_sched_barrier(0);
            if (__builtin_expect(wave == 1, 1))
                comb_o1(*(const LAS f32x4*)(size_t)cma, *(const LAS f32x4*)(size_t)(cma + 16), *(const LAS f32x2*)(size_t)cxa, *(const LAS f32x4*)(size_t)cra, *(const LAS f32x4*)(size_t)(cra + 1024), cso, 0x0000111100001111ull, false);
            __builtin_amdgcn_sched_barrier(0);
            if (__builtin_expect(wave == 2, 1)) comb_ml(cma, cxa, cra, cso, false);
            MARK(e_q);
            if (wave < 3) STAMP(21 + 32 * layer);
            __builtin_amdgcn_s_setprio(0);
        }
    };

    unsigned nj = 0u, rS = 0u, nS = 0u; int ord;
    {
        const int tid = threadIdx.x, lane = tid & 63;
        if (tid < 24) *reinterpret_cast<unsigned*>(smem + L_CNT + tid * 4) = tid == 16 ? tst : 0u;
        BAR();
        STAMP(0);
        unsigned pf = 0u, mb = 0u;
        const u16* kbase = kc + ((size_t)((gj >> 1) * HQ + gi) * SK + (SK - WIN) + (gj & 1) * 512) * DH;
        const u16* vbase = vc + ((size_t)((gj >> 1) * HQ + gi) * SK + (SK - WIN) + (gj & 1) * 512) * DH;
        unsigned pt0 = 0u, pt1 = 0u;
        const unsigned S0 = ALT ? (unsigned)(gi & 1) * 8192u : 0u;
        if (wave == 7) {
            __builtin_amdgcn_s_setprio(3);
            for (int i = 0; i < 5; ++i) dma16p(x0, (unsigned)(i * 1024 + lane * 16), lb + L_X0 + i * 1024);
            pf = code_pf((g >> 3) % 13, lane);
            if (lane < 10) *reinterpret_cast<u32x4*>(smem + L_XS + 640 + lane * 16) = (u32x4){0x3f803f80u, 0x3f803f80u, 0x3f803f80u, 0x3f803f80u};
            if (lane < 16) { *reinterpret_cast<u32x4*>(smem + L_CS + 768 + lane * 16) = (u32x4){0u, 0u, 0u, 0u}; }
        } else {
            if (wave >= 4) {
                if (wave == 4) st16_sc1(ws, WS_OWN + (unsigned)(gi * 16384 + (lane >> 5) * 8192 + gj * 512 + (lane & 31) * 16), (u32x4){~0u, ~0u, ~0u, ~0u});
                else empty5(0, (unsigned)(wave - 5) * 8192u, lane);
                if (wave == 6) {
                    __builtin_amdgcn_s_setprio(3);
                    if (gi + gj == 0) st4_sc1(ws, WS_R + (lane & 31) * 4, 0u);
                    st4_sc1(ws, WS_MB + (unsigned)((gi * 8 + gj) * 128 + (lane & 31) * 4), 0u);
                }
            } else if (PROBE && wave >= 2) {
                unsigned long long c0, c1;
                asm volatile("s_memtime %0\n\ts_waitcnt lgkmcnt(0)\n\ts_load_dword %2, %3, %4\n\ts_waitcnt lgkmcnt(0)\n\ts_memtime %1\n\ts_waitcnt lgkmcnt(0)"
                             : "=&s"(c0), "=&s"(c1), "=&s"(pt0) : "s"(ws), "s"((6u << 20) + ((((tst + g * 977u) * 2654435761u) >> 11 & 0x1fdf80u) | ((wave ^ gj) & 1) * 8192)) : "memory");
                pt0 = ((wave ^ gj) & 1) ? (unsigned)c1 - (unsigned)c0 : (unsigned)c0 - (unsigned)c1;
                if (PRET) {
                    if (wave == 2) asm volatile("s_load_dword %0, %1, 0\n\ts_load_dword %0, %2, 0\n\ts_load_dword %0, %3, %4" : "=&s"(pt1) : "s"(vbase), "s"(kbase), "s"(wo), "s"(gi * (DH * DM * 2)) : "memory");
                    else asm volatile("s_load_dword %0, %1, %5\n\ts_load_dword %0, %2, %5\n\ts_load_dword %0, %3, %5\n\ts_load_dword %0, %4, 0" : "=&s"(pt1) : "s"(wg), "s"(wu), "s"(wd), "s"(out), "s"(g * 4096) : "memory");
                }
            }
            STAMP(32);
#pragma unroll 1
            for (int i = wave < 2 ? wave : wave < 4 ? 35 : wave - 2; i < 35; i += 5) {
                if (i >= 33) dma16p(wr, (unsigned)((i == 33 || lane < 16) ? (i - 33) * 1024 + lane * 16 : 0), lb + L_WR + (i - 33) * 1024);
                else {
                    const int c = i * 64 + lane; int row = c / 26, cc = c - 26 * row; if (cc > 23) cc = 23; if (row > 79) row = 79;
                    dma16(wq, (unsigned)(((gj * 80 + swz(row)) * QD + gi * DH + cc * 8) * 2), lb + L_WQ + i * 1024);
                }
            }
            STAMP(36);
            *reinterpret_cast<u32x4*>(smem + L_P + tid * 16) = (u32x4){0u, 0u, 0u, 0u};
            if (tid < 72 * 12 - 448) *reinterpret_cast<u32x4*>(smem + L_P + 7168 + tid * 16) = (u32x4){0u, 0u, 0u, 0u};
            if (wave >= 4) {
                asm volatile("s_waitcnt vmcnt(0) lgkmcnt(0)" ::: "memory");
                landed();
            } else if (wave >= 2) { lds_add(20, pt0); asm volatile("s_waitcnt lgkmcnt(0)" :: "s"(pt1) : "memory"); }
        }
        u32x4 a[8];
        unsigned ko = (unsigned)((wave * 64 + (lane & 7)) * 384 + (((lane >> 3) & 1) * 4 + (lane >> 4)) * 16);
        PIN1(ko);
        auto k_group = [&](auto GC_) __attribute__((always_inline)) {
            constexpr int G = decltype(GC_)::value, R = RK + 24 * (G >> 1) + 4 * (G & 1);
            const unsigned o = ko + G * 3072;
            pace(3);
            ld16_nt<R, 0>(kbase, o); ld16_nt<R + 8, 128>(kbase, o); ld16_nt<R + 16, 256>(kbase, o);
            const int lane = lane_id();
            const unsigned ho = WS_H1 + S0 + (unsigned)((gi * 4 + (gj >> 1)) * 16384 + 3584 + (lane < 48 ? lane : 0) * 16);
            if (wave < 7) {
                asm volatile("; COND_BEGIN");
                if (G == 0 && wave < 4) {
                    asm volatile("s_waitcnt vmcnt(%0) lgkmcnt(0)" :: "i"(3) : "memory");
                    landed();
                    if (wave < 2) load_gu(wave, lane);
                    if (wave == 2) load_wd(lane);
                    if (wave == 3) {
#pragma unroll 1
                        for (int k = 5 - WEARLY; k < 5; ++k) load_wo(k, lane, true);
                    }
                }
                asm volatile("; COND_END");
            }
            if (wave == 7) {
                asm volatile("; COND_BEGIN");
                if (G == GP) {
                    VMW(3 * (GP + 1) + 1);
                    const unsigned char* p = smem + L_X0 + (lane >> 4) * 1280;
                    const float c0 = lo16(*reinterpret_cast<const u16*>(p));
                    const f32x2 c2 = {c0, c0};
                    f32x2 s2 = {0.f, 0.f}, q2 = s2;
#pragma unroll
                    for (int k = 0; k < 5; ++k) {
                        const u32x4 w = *reinterpret_cast<const u32x4*>(p + k * 256 + (lane & 15) * 16);
#pragma unroll
                        for (int e = 0; e < 4; ++e) { const f32x2 d = unpk(w[e]) - c2; s2 += d; q2 += d * d; }
                    }
                    const float s = row16_sum(s2[0] + s2[1]), qq = row16_sum(q2[0] + q2[1]);
                    const float ms = s * (1.f / DM);
                    const unsigned mv = pk2(c0 + ms, fmax2(qq - s * ms, 0.f) * (1.f / DM));
                    const float mb = lo16(mv), rden = frcp(bfr(__builtin_amdgcn_sqrtf(bfr(hi16(mv) + 1e-5f))));
                    const f32x2 mb2 = {mb, mb}, rd2 = {rden, rden};
                    if ((lane & 15) < 10) {
                        u32x4 w = *reinterpret_cast<const u32x4*>(p + gj * 160 + (lane & 15) * 16);
#pragma unroll
                        for (int e = 0; e < 4; ++e) w[e] = pk2v(unpk(pk2v(unpk(w[e]) - mb2)) * rd2);
                        *reinterpret_cast<u32x4*>(smem + L_XS + (lane >> 4) * 160 + (lane & 15) * 16) = w;
                    }
                    STAMP(30);
#pragma unroll 1
                    for (unsigned spins = 0; spins < (kSpinCap << 4); ++spins) {
                        if (lds_word(lb + L_CNT) >= 7u) break;
                        __builtin_amdgcn_s_sleep(1);
                    }
                    STAMP(26);
                }
                if (G == GP || G == GA) {
#pragma unroll 1
                    for (int blk = 0; blk < 3; ++blk) {
                        LANE_VARS();
                        const unsigned xb = lb + L_XS + (unsigned)((G == GP ? p4 : 4) * 160);
                        Frag80 f[4];
#pragma unroll
                        for (int i = 0; i < 4; ++i) f[i] = frag80(lb + L_WQ + (unsigned)(128 * blk + 32 * i + 8 * p4), 416, r0s, r1s, r2s);
                        const Frag80 b = rows80(xb + kq * 16, xb + 128 + kq * 8);
                        const u32x4 c = tiles4(f, b, 0x000f000f000f000full);
                        if (G == GP) st16_sc1(ws, WS_H1 + S0 + (unsigned)((gi * 4 + p4) * 16384 + gj * 896 + 256 * blk + q4 * 64 + kq * 16), c);
                        else *reinterpret_cast<u32x4*>(smem + L_CS + 256 * blk + q4 * 64 + kq * 16) = c;
                    }
                    if (G == GP) { STAMP(2); WAIT1(pf, 3 * (GP + 1) + 3); }
                }
                if (G == GA) {
                    const unsigned half = __builtin_amdgcn_s_getreg(2 << 6 | 20);
                    lds_set(lb + L_CNT + 68, (lds_word(lb + L_CNT + 64) & ~7u) | 1u | (((int)lds_word(lb + L_CNT + 20) > 0) ^ half) << 1 | half << 2);
                    VMW(3 * (GA - GP));
                    send_flag(lane_id());
                    lds_set(lb + L_CNT + 8, 1u);
                    STAMP(17);
                    f32x2 r2 = {0.f, 0.f};
#pragma unroll
                    for (int k = 0; k < 5; ++k) r2 += unpk(*reinterpret_cast<const unsigned*>(smem + L_WR + k * 256 + lane_id() * 4));
                    *reinterpret_cast<float*>(smem + L_CNT + 12) = wave_sum(r2[0] + r2[1]);
                }
                asm volatile("; COND_END");
            }
            if (wave == 6) {
                asm volatile("; COND_BEGIN");
                if (G == GB - 1) mb = ld4_sc1(ws, WS_MB + (unsigned)((gi * 8 + gj) * 128 + (gi & 7) * 4));
                if (G == GB) {
                    WAIT1(mb, 3);
#pragma unroll 1
                    for (unsigned spins = 0; spins < kSpinCap && !all_set(mb); ++spins) {
                        if ((spins & 7u) == 7u && lds_word(lb + L_CNT + 8)) send_flag(lane);
                        mb = ld4_sc1(ws, WS_MB + (unsigned)((gi * 8 + gj) * 128 + (gi & 7) * 4));
                        WAIT1(mb, 0);
                    }
                    mb = ld4_sc1(ws, WS_MB + (unsigned)((gi * 8 + (gi & 7)) * 128 + (lane & 7) * 4));
                }
                if (G == GC) {
                    WAIT1(mb, 3 * (GC - GB));
#pragma unroll 1
                    for (unsigned spins = 0; spins < kSpinCap && !all_set(mb); ++spins) {
                        if ((spins & 7u) == 7u && lds_word(lb + L_CNT + 8)) send_flag(lane);
                        mb = ld4_sc1(ws, WS_MB + (unsigned)((gi * 8 + (gi & 7)) * 128 + (lane & 7) * 4));
                        WAIT1(mb, 0);
                    }
                    lds_set(lb + L_CNT + 92, (__builtin_amdgcn_readlane(mb, gi & 7) & 4u) << 11);
                    int d = min(max((int)(mb - tst), -400), 600);
                    d += (int)dppu<0xB1>((unsigned)d); d += (int)dppu<0x4E>((unsigned)d); d += (int)dppu<0x141>((unsigned)d);
                    const unsigned tm = tst + (unsigned)(__builtin_amdgcn_readfirstlane(d) >> 3);
                    if (ROWM) lds_set(lb + L_CNT + 64, tm);
                    lds_set(lb + L_CNT + 88, (tm & ~31u) | __builtin_popcount((unsigned)__builtin_amdgcn_ballot_w64((mb & 2u) != 0u) & 255u) << 1 | 1u);
                    report();
                    GATHER8(a, ho, 896);
                    pace(XCOST, false);
                    STAMP(31);
                }
                asm volatile("; COND_END");
            } else if (G == GC) { for (int k = 0; k < 8; ++k) asm volatile("" : "=v"(a[k])); }
        };
        k_group(IC<0>{}); k_group(IC<1>{}); k_group(IC<2>{}); k_group(IC<3>{}); k_group(IC<4>{}); k_group(IC<5>{}); k_group(IC<6>{}); k_group(IC<7>{});
        {
            const int lane = lane_id();
            const unsigned v0 = (unsigned)((wave * 64 + 4 * (lane >> 4)) * 384 + (lane & 15) * 16);
            const unsigned v1 = v0 + 256 - (unsigned)((lane & 15) * 8);
            auto v_group = [&](auto HC) __attribute__((always_inline)) {
                constexpr int h = decltype(HC)::value, R = h < 8 ? RV + 16 * (h & 3) + 4 * (h >> 2) : RV + 64 + 2 * (h - 8), NV = h < 8 ? 2 * h + 2 : 4 * h - 12;
                const unsigned o = (h < 8 ? v0 + (h >> 1) * 6144 : v1 + (h - 8) * 6144);
                pace(2);
                if (h < 8) { ld16_nt<R, (h & 1) * 768>(vbase, o); ld16_nt<R + 8, (h & 1) * 768 + 384>(vbase, o); }
                else { ld8_nt<R, 0>(vbase, o); ld8_nt<R + 8, 384>(vbase, o); ld8_nt<R + 16, 768>(vbase, o); ld8_nt<R + 24, 1152>(vbase, o); }
                if (wave == 7 && gi + gj == 0) {
                    asm volatile("; COND_BEGIN");
                    if (h == 3 || h == 6) mb = ld4_sc1(ws, WS_R + (unsigned)((lane & 31) * 4));
                    if (h == 4 || h == 7) {
                        WAIT1(mb, 2);
                        if (all_set(mb) && !lds_word(lb + L_CNT + 36)) answer(lane, mb);
                    }
                    asm volatile("; COND_END");
                }
                if (wave == 6) {
                    asm volatile("; COND_BEGIN");
                if (h == HD) {
                    WAIT8(a, 3 * (7 - GC) + NV);
                    const f32x4 sq = SUM8(a);
                    unsigned* qp = reinterpret_cast<unsigned*>(smem + L_Q + (lane >> 2) * 32 + (lane & 3) * 4);
                    qp[0] = pk2(sq[0], sq[1]); qp[4] = pk2(sq[2], sq[3]);
                    asm volatile("s_waitcnt lgkmcnt(0)\n\tds_write_b32 %0, %1" :: "v"(lb + L_CNT + 4), "v"(1u) : "memory");
                    STAMP(27);
                    lead = LEAD * PACE * 4;
                }
                    if (h == 6) mb = ld4_sc1(ws, WS_MB + (unsigned)((gi * 8 + (gi & 7)) * 128 + 32));
                    if (h == 8) {
                        WAIT1(mb, 6);
                        const unsigned r = __builtin_amdgcn_readfirstlane(mb);
                        if (r) { adopt(r); STAMP(29); }
                    }
                    asm volatile("; COND_END");
                }
                if constexpr (h < 6) {
                    constexpr int i = 2 * h;
                    fix_k1(IC<i>{}, IC<20 - 6 * (i / 3) - i % 3 + NV>{});
                    fix_k1(IC<i + 1>{}, IC<20 - 6 * ((i + 1) / 3) - (i + 1) % 3 + NV>{});
                }
                if (h == 5) {
                    STAMP(1);
                    __builtin_amdgcn_s_setprio(0);
                    att_qk0();
                }
            };
            v_group(IC<0>{}); v_group(IC<1>{}); v_group(IC<2>{}); v_group(IC<3>{}); v_group(IC<4>{}); v_group(IC<5>{});
            v_group(IC<6>{}); v_group(IC<7>{}); v_group(IC<8>{}); v_group(IC<9>{}); v_group(IC<10>{}); v_group(IC<11>{});
            lds_add(16, 1u);
            if (wave == 3) lds_add(44, WEARLY);
        }
    }
    {
        STAMP(28);
        att_addr();
        const int lane = lane_id();
        cma = lb + L_M; cxa = lb + L_M + (unsigned)((lane >> 4) * 8); cra = lb + L_OP + (unsigned)(512 + (lane >> 4) * 2048 + (lane & 15) * 16);
        cso = WS_OWN + (unsigned)(gi * 16384 + gj * 512 + 256 + (lane >> 5) * 64 + (lane & 12) * 4 + (lane & 1) * 8192);
        PIN2(cma, cxa); PIN2(cra, cso);
        att_pv0();
    }
    {
        f32x4 m0, m1, o0, o1; f32x2 mm; unsigned r;
        asm volatile("s_mov_b64 exec, 1\n\tds_add_rtn_u32 %5, %6, %7 offset:%10\n\ts_mov_b64 exec, -1\n\t"
                     "ds_read_b128 %0, %6\n\tds_read_b128 %1, %6 offset:16\n\tds_read_b64 %2, %8\n\tds_read_b128 %3, %9\n\tds_read_b128 %4, %9 offset:1024\n\ts_waitcnt lgkmcnt(0)"
                     : "=&v"(m0), "=&v"(m1), "=&v"(mm), "=&v"(o0), "=&v"(o1), "=&v"(r)
                     : "v"(cma), "v"(1u), "v"(cxa), "v"(cra), "i"(L_CNT + 28 - L_M) : "memory");
        ord = __builtin_amdgcn_readfirstlane(r);
        if (ord == 7) { STAMP(5); comb_o1(m0, m1, mm, o0, o1, cso, 0x0000333300003333ull, true); lds_set(lb + L_CNT + 48, now() | 1u); STAMP(21); }
        MARK(e_t5);
        const int lane = lane_id();
        auto wait_cnt = [&](int off, unsigned v) __attribute__((always_inline)) {
#pragma unroll 1
            for (unsigned spins = 0; spins < (kSpinCap << 4); ++spins) { if (lds_word(lb + L_CNT + off) >= v) break; __builtin_amdgcn_s_sleep(1); }
        };
        if (ord == OML) {
            wait_cnt(16, 8u);
            unsigned xa, ra; ml_addr(lane, xa, ra);
            comb_ml(cma, xa, ra, WS_OWN + (unsigned)(gi * 16384 + gj * 512 + 384 + (lane & 1) * 8192), true);
            STAMP(24);
        } else if (ord == 0 && !lds_word(lb + L_CNT + 52)) {
            if (gi + gj == 0 && !lds_word(lb + L_CNT + 36)) {
                unsigned r = 0u;
#pragma unroll 1
                for (unsigned spins = 0; spins < kSpinCap; ++spins) {
                    r = ld4_sc1(ws, WS_R + (unsigned)((lane & 31) * 4));
                    WAIT1(r, 0);
                    if (all_set(r)) break;
                    if ((spins & 7u) == 7u) send_flag(lane);
                }
                answer(lane, r);
            }
#pragma unroll 1
            for (unsigned spins = 0; spins < kSpinCap; ++spins) {
                unsigned r = ld4_sc1(ws, WS_MB + (unsigned)((gi * 8 + (gi & 7)) * 128 + 32));
                WAIT1(r, 0);
                r = __builtin_amdgcn_readfirstlane(r);
                if (r) { adopt(r); break; }
                if ((spins & 7u) == 7u) { send_flag(lane); report(); }
                __builtin_amdgcn_s_sleep(16);
            }
            STAMP(29);
        } else if (ord == OML + 1) {
#pragma unroll 1
            for (unsigned spins = 0; spins < (kSpinCap << 6); ++spins) if (lds_word(lb + L_CNT + 24) >= 8u) break;
            const unsigned ra = lb + L_OP + (unsigned)((lane >> 5) * 4096 + (lane & 31) * 16);
            comb_o0(*(const LAS f32x4*)(size_t)cma, *(const LAS f32x4*)(size_t)(cma + 16), *(const LAS f32x4*)(size_t)(cma + (lane >> 5) * 16), *(const LAS f32x4*)(size_t)ra, *(const LAS f32x4*)(size_t)(ra + 1024),
                    *(const LAS f32x4*)(size_t)(ra + 2048), *(const LAS f32x4*)(size_t)(ra + 3072), WS_OWN + (unsigned)(gi * 16384 + gj * 512 + (lane & 60) * 4 + (lane & 1) * 8192), 0x3333333333333333ull, true);
            STAMP(22);
        }
        if (ord == EMODE) {
#pragma unroll 1
            for (unsigned spins = 0; spins < (kSpinCap << 8); ++spins) { if (lds_word(lb + L_CNT + 52)) break; __builtin_amdgcn_s_sleep(1); }
            empty4(0, lds_word(lb + L_CNT + 80), lane); empty5(5, 0u, lane); empty5(5, 8192u, lane);
        }
#pragma unroll 1
        for (;;) {
            unsigned j;
            asm volatile("s_mov_b64 exec, 1\n\tds_add_rtn_u32 %0, %1, %2\n\ts_mov_b64 exec, -1\n\ts_waitcnt lgkmcnt(0)" : "=&v"(j) : "v"(lb + L_CNT + 40), "v"(1u) : "memory");
            j = __builtin_amdgcn_readfirstlane(j);
            if (j >= 5u - WEARLY) break;
            load_wo((int)j, lane, false); ++nj;
        }
        STAMP(23);
    }

    unsigned omr, owa, owb;
    auto op_addr = [&](int layer) __attribute__((always_inline)) {
        LANE_VARS();
        const int t = wave - 3;
        cma = WS_OWN + rS + (unsigned)(gi * 16384 + layer * 4096 + (lane >> 3) * 512 + t * 128 + (t == 3 ? 0 : (lane & 7) * 16));
        cxa = lb + L_O2 + (unsigned)((lane >> 3) * 416 + t * 128 + (t == 3 ? 0 : (lane & 7) * 16));
        cso = tile35(gj, 10 * layer + t) + (unsigned)((lane & 1) * 8192 + gi * 128 + (lane & 2) * 32 + (lane & 4) * 8 + kq * 8);
        cra = lb + L_O2 + (unsigned)((n & 7) * 416 + kq * 16); omr = lb + L_O2 + (unsigned)((n & 7) * 416 + 384);
        owa = lb + L_WO + (unsigned)(r0s * 160 + 32 * t + 8 * p4); owb = lb + L_WO + (unsigned)(r1s * 160 + 32 * t + 8 * p4);
        if (layer) PIN2(cma, cxa);
        PIN2(cso, cra); PIN3(omr, owa, owb);
    };
    auto chain = [&](auto LC) __attribute__((always_inline)) {
        constexpr int layer = decltype(LC)::value;
        unsigned h4 = WS_H4 + (unsigned)((layer * 32 + gi) * 32768) + (layer ? rS ^ 8192u : 0u);
        auto sides = [&]() __attribute__((always_inline)) { rS = lds_word(lb + L_CNT + 80); nS = lds_word(lb + L_CNT + 84); h4 += rS ^ 8192u; };
        if (wave < 3) {
            LANE_VARS();
            if (layer == 0) {
                const int l0 = wave == 0 ? 2 : wave - 1;
                const unsigned lo = WS_OWN + lds_word(lb + L_CNT + 84) + (unsigned)(gi * 16384 + (lane >> 3) * 512), lw = lb + L_O2 + (unsigned)((lane >> 3) * 416);
                unsigned lo0 = lo + (unsigned)(l0 * 128 + (lane & 7) * 16), lo1 = wave == 1 ? lo + 384 : lo0, lw0 = lw + (unsigned)(l0 * 128 + (lane & 7) * 16), lw1 = wave == 1 ? lw + 384 : lw0;
                PIN2(lo0, lo1); PIN2(lw0, lw1);
                GUARD(600);
                MARK(b_g2);
                unsigned tf = 0u;
#pragma unroll 1
                for (unsigned spins = 0; spins < (kSpinCap << 4); ++spins) { tf = lds_word(lb + L_CNT + 48); if (tf) break; }
                if (wave == 0) until(tf + DG2);
#pragma unroll 1
                for (unsigned spins = 0; spins < kSpinCap; ++spins) {
                    u32x4 o0 = ld16_sc1(ws, lo0), o1;
                    if (wave == 1) o1 = ld16_sc1(ws, lo1); else asm volatile("" : "=v"(o1));
                    if (nj) { if (wave == 1) VMW(2); else VMW(1); lds_add(44, nj); nj = 0u; }
                    asm volatile("s_waitcnt vmcnt(0)" : "+v"(o0), "+v"(o1) :: "memory");
                    if (wave != 1) o1 = o0;
                    *(LAS u32x4*)(size_t)lw0 = o0; *(LAS u32x4*)(size_t)lw1 = o1;
                    if (!any_empty(max(um4(o0), um4(o1)))) break;
                    if ((spins & 15u) == 15u) send_flag(lane);
                }
                MARK(e_g2);
                STAMP(25);
                BAR();
#pragma unroll 1
                for (unsigned spins = 0; spins < (kSpinCap << 4); ++spins) { __builtin_amdgcn_s_sleep(4); if (lds_word(lb + L_CNT + 76) >= 5u) break; }
                sides();
                if (wave < 2) { empty1(wave, rS, lane); empty35(1, (unsigned)wave * 8192u, lane); }
                else { empty4(1, rS, lane); st16m(ws, WS_CS + (unsigned)((gi * 8 + gj) * 768 + lane * 16), *reinterpret_cast<const u32x4*>(smem + L_CS + lane * 16), 0xffffffffffffull); }
            } else BAR();
            {
                const unsigned im = lb + L_GU + (unsigned)(32 * wave + 8 * p4);
                Frag80 fg = frag80(im, 96, r0s, r1s, r2s), fu = frag80(im + 8192, 96, r0s, r1s, r2s);
                const unsigned xb = lb + L_XS + (unsigned)((n < 4 ? n : 4) * 160);
                unsigned b16 = xb + kq * 16, b8 = xb + 128 + kq * 8;
                unsigned sg = h4 + (unsigned)((n < 4 ? h4s(wave) + gj * 512 + n * 64 : H4C + wave * 1024 + gj * 128) + kq * 16), su = sg + (n < 4 ? 256u : 64u);
                unsigned o = h4 + h4s(wave) + (unsigned)(lane * 16);
                unsigned oc = h4 + (unsigned)(H4C + wave * 1024 + ((lane >> 5) * 4 + q4) * 128 + (kq & 1) * 64 + p4 * 16);
                unsigned os = h4 + (unsigned)(H4T + (p4 + 4 * (kq & 1)) * 128 + q4 * 16);
                unsigned ha = lb + L_H + (unsigned)(q4 * 96 + wave * 32 + p4 * 8 + kq * 2);
                f32x2 cs = {0.f, 0.f};
                if (layer != 0) cs = *reinterpret_cast<const f32x2*>(smem + L_CS2 + wave * 512 + lane * 8);
                PIN3(fg.a0, fg.a1, fg.a2); PIN3(fu.a0, fu.a1, fu.a2); PIN3(b16, b8, sg); PIN3(su, o, os); PIN2(ha, cs);
                unsigned long long mk = layer ? 0x000f000f000f000full : 0x001f001f001f001full;
                unsigned ta = WS_MB + (unsigned)((lane >> 1) * 1024 + 64 + (lane & 1) * 16);
                if (layer == 0) PIN2(oc, ta);
                PINS(mk);
                if (layer == 0) VMW(0);
                if (layer == 0) GUARD(1200); else GUARD(800);
                MARK(b_gu);
                BAR();
                STAMP(10 + 32 * layer);
                u32x4 tl = {0u, 0u, 0u, 0u};
                if (layer == 0) tl = ld16_sc1(ws, ta);
                const Frag80 b = rows80(b16, b8);
                const f32x4 g0 = mfma32(fg.a0, b.a0, z4), u0 = mfma32(fu.a0, b.a0, z4), g1 = mfma32(fg.a1, b.a1, g0), g2 = mfma16(fg.a2, b.a2, z4), u1 = mfma32(fu.a1, b.a1, u0), u2 = mfma16(fu.a2, b.a2, z4);
                __builtin_amdgcn_sched_barrier(0);
                const f32x4 cg = g1 + g2, cu = u1 + u2;
                st16m2<0>(ws, sg, asu(cg), su, asu(cu), mk);
                STAMP(11 + 32 * layer);
                if (layer == 0) {
                    WAIT1(tl, 2);
#pragma unroll 1
                    for (unsigned spins = 0; spins < kSpinCap && !all_set(min(min(tl[0], tl[1]), min(tl[2], tl[3]))); ++spins) {
                        tl = ld16_sc1(ws, ta);
                        WAIT1(tl, 0);
                    }
                    const unsigned tn = now();
                    unsigned d = min(min(min(tn - tl[0], tn - tl[1]), min(tn - tl[2], tn - tl[3])), 800u);
                    d = min(d, dppu<0xB1>(d)); d = min(d, dppu<0x4E>(d)); d = min(d, dppu<0x124>(d)); d = min(d, dppu<0x128>(d));
                    unsigned tr = tn - min(min((unsigned)__builtin_amdgcn_readlane(d, 0), (unsigned)__builtin_amdgcn_readlane(d, 16)), min((unsigned)__builtin_amdgcn_readlane(d, 32), (unsigned)__builtin_amdgcn_readlane(d, 48)));
                    lds_set(lb + L_CNT + 56, tr | 1u);
                                        until(__builtin_amdgcn_readfirstlane(tr) + X4A);
                } else sched(X4, 0);
                STAMP(16 + 32 * layer);
                float hv; f32x2 st;
                u32x4 a0, a1, a2, a3, ac, as;
                auto load = [&]() __attribute__((always_inline)) {
                    as = ld16_sc1(ws, os);
                    if (layer == 0) ac = ld16_sc1(ws, oc);
                    a0 = ld16_sc1<0>(ws, o); a1 = ld16_sc1<1024>(ws, o); a2 = ld16_sc1<2048>(ws, o); a3 = ld16_sc1<3072>(ws, o);
                };
                auto all_in = [&]() __attribute__((always_inline)) {
                    if (layer == 0) asm volatile("s_waitcnt vmcnt(0)" : "+v"(a0), "+v"(a1), "+v"(a2), "+v"(a3), "+v"(ac), "+v"(as) :: "memory");
                    else { asm volatile("s_waitcnt vmcnt(0)" : "+v"(a0), "+v"(a1), "+v"(a2), "+v"(a3), "+v"(as) :: "memory"); ac = as; }
                };
                auto compute = [&]() __attribute__((always_inline)) {
                    float ss, sm;
                    WAIT1(as, layer ? 4 : 5);
                    asm volatile("v_add_f32_dpp %0, %2, %2 quad_perm:[1,0,3,2] row_mask:0xf bank_mask:0xf\n\tv_add_f32_dpp %1, %3, %3 quad_perm:[1,0,3,2] row_mask:0xf bank_mask:0xf\n\ts_nop 0\n\t"
                                 "v_add_f32_dpp %0, %0, %0 quad_perm:[2,3,0,1] row_mask:0xf bank_mask:0xf\n\tv_add_f32_dpp %1, %1, %1 quad_perm:[2,3,0,1] row_mask:0xf bank_mask:0xf\n\ts_nop 0"
                                 : "=&v"(ss), "=&v"(sm) : "v"(as[0]), "v"(as[1]));
                    ss = x16_add(ss); sm = x16_add(sm);
                    float mean = sm * (1.f / DM), var = fmax2(__builtin_fmaf(-mean, mean, ss * (1.f / DM)), 0.f);
                    PIN2(mean, var);
                    if (layer == 0) {
                        WAIT1(ac, 4);
                        cs = sw16(rs32(asf(ac)[0], asf(ac)[2]), rs32(asf(ac)[1], asf(ac)[3]));
                        cs[0] += dpp<0x124>(cs[0]); cs[1] += dpp<0x124>(cs[1]); cs[0] += dpp<0x128>(cs[0]); cs[1] += dpp<0x128>(cs[1]);
                        PIN1(cs);
                    }
                    all_in();
                    st = (f32x2){bfr(mean), frcp(bfr(__builtin_amdgcn_sqrtf(bfr(bfr(var) + 1e-5f))))};
                    const f32x4 t = (asf(a0) + asf(a1)) + (asf(a2) + asf(a3));
                    const f32x2 gu = sw16(rs32(t[0], t[2]), rs32(t[1], t[3]));
                    const f32x2 gn = (gu - st[0] * cs) * st[1];
                    const float gv = bfr(gn[0]);
                    hv = bfr(gv * frcp(1.f + ex2(gv * -LOG2E))) * bfr(gn[1]);
                    *(LAS u16*)(size_t)ha = (u16)pk2(hv, hv);
                    __builtin_amdgcn_sched_barrier(0);
                };
                RETRY(load(), (all_in(), max(max(max(um4(a0), um4(a1)), max(um4(a2), um4(a3))), max(um4(ac), um4(as)))), compute(), any_nan(hv, hv), layer ? X4 : X4A);
                BAR();
                MARK(e_gu);
                STAMP(12 + 32 * layer);
                if (layer == 0) {
                    *reinterpret_cast<f32x2*>(smem + L_CS2 + wave * 512 + lane * 8) = cs;
                }
            }
            if (layer == 0) {
                if (wave == 1) {
                    const unsigned o = WS_CS + (unsigned)(gi * 6144 + 3072 + (lane < 48 ? lane : 0) * 16);
                    u32x4 a[8];
                    GATHER8(a, o, 768);
                    WAIT8(a, 0);
                    if (lane < 48) *reinterpret_cast<f32x4*>(smem + L_CS + lane * 16) = SUM8(a);
                }
                Frag80 f[4];
#pragma unroll
                for (int i = 0; i < 4; ++i) { f[i] = frag80(lb + L_WQ + (unsigned)(128 * wave + 32 * i + 8 * p4), 416, r0s, r1s, r2s); PIN3(f[i].a0, f[i].a1, f[i].a2); }
                unsigned b16 = lb + L_XS + (unsigned)(p4 * 160 + kq * 16), b8 = lb + L_XS + (unsigned)(p4 * 160 + 128 + kq * 8);
                unsigned so = WS_H1 + rS + (unsigned)((gi * 4 + p4) * 16384 + gj * 896 + 256 * wave + q4 * 64 + kq * 16);
                unsigned long long mk = 0x000f000f000f000full;
                PIN3(b16, b8, so); PINS(mk);
                GUARD(3800);
                MARK(b_q);
                BAR();
                STAMP(33);
                const Frag80 b = rows80(b16, b8);
                st16_sc1(ws, so, tiles4(f, b, mk));
                STAMP(34);
                qk_addr(true); att_addr();
                cma = lb + L_M; cxa = lb + L_M + (unsigned)(wave == 0 ? (lane >> 5) * 16 : (lane >> 4) * 8);
                cra = lb + L_OP + (unsigned)(wave == 0 ? (lane >> 5) * 4096 + (lane & 31) * 16 : 512 + (lane >> 4) * 2048 + (lane & 15) * 16);
                if (wave == 2) ml_addr(lane, cxa, cra);
                cso = WS_OWN + rS + (unsigned)(gi * 16384 + 4096 + gj * 512) + (unsigned)(wave == 0 ? lane * 4 : wave == 1 ? 256 + (lane >> 5) * 64 + (lane & 15) * 4 : 384);
                PIN2(cma, cxa); PIN2(cra, cso);
                asm volatile("" : "=v"(omr), "=v"(owa), "=v"(owb));
            }
        } else {
            const int t = wave - 3;
            StatAddr sa;
            u32x4 sel;
            unsigned long long hi = 0xff00ff00ff00ff00ull;
            auto gather32 = [&](unsigned o, unsigned X, auto fin) __attribute__((always_inline)) {
                u32x4 a0 = ld16_sc1<-2048>(ws, o), a1 = ld16_sc1<-1024>(ws, o), a2 = ld16_sc1<0>(ws, o), a3 = ld16_sc1<1024>(ws, o);
                float v;
                auto sum = [&]() __attribute__((always_inline)) {
                    asm volatile("s_waitcnt vmcnt(3)\n\tv_mfma_f32_16x16x32_bf16 v[40:43], %1, %5, 0\n\ts_waitcnt vmcnt(2)\n\tv_mfma_f32_16x16x32_bf16 v[40:43], %2, %5, v[40:43]\n\t"
                                 "s_waitcnt vmcnt(1)\n\tv_mfma_f32_16x16x32_bf16 v[40:43], %3, %5, v[40:43]\n\ts_waitcnt vmcnt(0)\n\tv_mfma_f32_16x16x32_bf16 v[40:43], %4, %5, v[40:43]\n\t"
                                 "s_nop 7\n\tv_cndmask_b32 v40, v40, v41, %6\n\tv_cndmask_b32 v42, v42, v43, %6\n\ts_nop 1\n\tv_permlane32_swap_b32 v40, v42\n\tv_add_f32 v40, v40, v42\n\t"
                                 "v_cvt_pk_bf16_f32 %0, 0, v40"
                                 : "=&v"(v), "+v"(a0), "+v"(a1), "+v"(a2), "+v"(a3) : "v"(sel), "s"(hi) : "memory", "v40", "v41", "v42", "v43");
                    fin(v);
                    __builtin_amdgcn_sched_barrier(0);
                };
                sum();
                if (__builtin_expect(any_nan(v, v) != 0ull, 0)) {
#pragma unroll 1
                    for (unsigned spins = 0; spins < kSpinCap; ++spins) {
                        slip(X);
                        again16<-2048>(a0, ws, o); again16<-1024>(a1, ws, o); again16<0>(a2, ws, o); again16<1024>(a3, ws, o);
                        asm volatile("s_waitcnt vmcnt(0)" : "+v"(a0), "+v"(a1), "+v"(a2), "+v"(a3) :: "memory");
                        if (!any_empty(max(max(um4(a0), um4(a1)), max(um4(a2), um4(a3))))) break;
                    }
                    sum();
                }
            };
            auto pack4 = [&](float c0, float c1, float c2, float c3) __attribute__((always_inline)) { const unsigned w0 = min(pk2(c0, c1), 0xfffffffeu), w1 = min(pk2(c2, c3), 0xfffffffeu); return (u32x2){w0, w1}; };
            unsigned xw, qo, qw, qc;
            __builtin_amdgcn_s_setprio(0);
            {
                STAMP(6 + 32 * layer);
                bf16x8 af[6];
                unsigned long long mk = 0x00ff00ff00ff00ffull;
                if (layer == 0) {
                    const int lane = lane_id();
                    xe = lo16(*reinterpret_cast<const u16*>(smem + L_X0 + (lane >> 4) * 1280 + gj * 160 + t * 32 + (lane & 15) * 2));
                    op_addr(0);
                    GUARD(1500);
                    MARK(b_op);
                    if (nj | (ord == EMODE)) { VMW(0); lds_add(44, nj); }
#pragma unroll 1
                    for (unsigned spins = 0; spins < (kSpinCap << 4); ++spins) if (lds_word(lb + L_CNT + 44) >= 5u) break;
                    STAMP(8);
                } else MARK(b_op);
                const unsigned lo = cma, lw = cxa, so = cso, br = cra, mr = omr, h3 = tile35(gj, 10 * layer + t);
                unsigned go, xs;
#pragma unroll
                for (int s = 0; s < 6; ++s) af[s] = cat8(tr64o(owa, s * 32 * 160), tr64o(owb, s * 32 * 160));
                PINS(mk); PINS(hi);
                if (layer != 0 && t < 4) {
                    sched(X2, 0);
                    STAMP(49);
                    u32x4 ov = ld16_sc1(ws, lo);
                    WAIT1(ov, 0);
                    *(LAS u32x4*)(size_t)lw = ov;
                    if (__builtin_expect(any_empty(um4(ov)), 0)) {
#pragma unroll 1
                        for (unsigned spins = 0; spins < kSpinCap; ++spins) {
                            slip(X2);
                            ov = ld16_sc1(ws, lo);
                            WAIT1(ov, 0);
                            *(LAS u32x4*)(size_t)lw = ov;
                            if (!any_empty(um4(ov))) break;
                        }
                    }
                }
                {
                    bf16x8 b0, b1, b2, b3, b4, b5;
#define QP " quad_perm:[1,0,3,2] row_mask:0xf bank_mask:0xf\n\t"
                    asm volatile("s_waitcnt lgkmcnt(0)\n\ts_barrier\n\t"
                                 "ds_read_b128 %0, %12\n\tds_read_b64 v[36:37], %13\n\tds_read_b128 %1, %12 offset:64\n\tds_read_b128 %2, %12 offset:128\n\tds_read_b128 %3, %12 offset:192\n\t"
                                 "ds_read_b128 %4, %12 offset:256\n\tds_read_b128 %5, %12 offset:320\n\t"
                                 "s_waitcnt lgkmcnt(5)\n\tv_mfma_f32_16x16x32_bf16 v[32:35], %6, %0, 0\n\tv_max_f32_dpp v38, v36, v36" QP "v_sub_f32 v38, v36, v38\n\t"
                                 "s_waitcnt lgkmcnt(4)\n\tv_mfma_f32_16x16x32_bf16 v[32:35], %7, %1, v[32:35]\n\tv_exp_f32 v38, v38\n\t"
                                 "s_waitcnt lgkmcnt(3)\n\tv_mfma_f32_16x16x32_bf16 v[32:35], %8, %2, v[32:35]\n\tv_mul_f32 v37, v37, v38\n\t"
                                 "s_waitcnt lgkmcnt(2)\n\tv_mfma_f32_16x16x32_bf16 v[32:35], %9, %3, v[32:35]\n\tv_add_f32_dpp v39, v37, v37" QP
                                 "s_waitcnt lgkmcnt(1)\n\tv_mfma_f32_16x16x32_bf16 v[32:35], %10, %4, v[32:35]\n\tv_rcp_f32 v39, v39\n\t"
                                 "s_waitcnt lgkmcnt(0)\n\tv_mfma_f32_16x16x32_bf16 v[32:35], %11, %5, v[32:35]\n\tv_mul_f32 v38, v38, v39\n\ts_nop 6\n\t"
                                 "v_pk_mul_f32 v[32:33], v[32:33], v[38:39] op_sel_hi:[1,0]\n\tv_pk_mul_f32 v[34:35], v[34:35], v[38:39] op_sel_hi:[1,0]\n\ts_nop 0\n\t"
                                 "v_add_f32_dpp v32, v32, v32" QP "v_add_f32_dpp v33, v33, v33" QP "v_add_f32_dpp v34, v34, v34" QP "v_add_f32_dpp v35, v35, v35" QP
                                 "v_cvt_pk_bf16_f32 v32, v32, v33\n\tv_cvt_pk_bf16_f32 v33, v34, v35\n\ts_mov_b64 exec, %16\n\tv_min_u32 v32, -2, v32\n\tv_min_u32 v33, -2, v33\n\t"
                                 "global_store_dwordx2 %14, v[32:33], %15 sc1\n\ts_mov_b64 exec, -1"
                                 : "=&v"(b0), "=&v"(b1), "=&v"(b2), "=&v"(b3), "=&v"(b4), "=&v"(b5)
                                 : "v"(af[0]), "v"(af[1]), "v"(af[2]), "v"(af[3]), "v"(af[4]), "v"(af[5]), "v"(br), "v"(mr), "v"(so), "s"(ws), "s"(mk)
                                 : "memory", "v32", "v33", "v34", "v35", "v36", "v37", "v38", "v39");
#undef QP
                }
                if (layer == 0 && wave == 7) st4_sc1(ws, WS_MB + (unsigned)(gi * 1024 + 64 + gj * 4), now() | 1u);
                const unsigned tp = layer ? 0u : now();
                if (layer == 0) { lds_add(76, 1u); sides(); }
                STAMP(7 + 32 * layer);
                {
                    const int ln = lane_id();
#pragma unroll
                    for (int k = 0; k < 4; ++k) sel[k] = ((ln >> 1) & 3) == k ? (ln & 1) ? 0x3f800000u : 0x3f80u : 0u;
                    go = h3 + nS + (unsigned)(2048 + ln * 16); xs = lb + L_XS + (unsigned)((ln >> 4) * 160 + t * 32 + (ln & 15) * 2);
                    PIN3(sel, go, xs);
                    sa = stats_addr(ln, h4 + (unsigned)(H4T + gj * 128), 16);
                }
                if (layer != 0) sched(X3, 0);
                else if (W3 == 0) wait_word(ws, h3 + nS + (unsigned)(((gi * 7 + t * 5 + 3) & 31) * 128));
                else until(tp + W3);
                STAMP(18 + 32 * layer);
                gather32(go, layer ? X3 : 0u, [&](float s) { xw = pk2(xe + s, 0.f); *(LAS u16*)(size_t)xs = (u16)xw; });
                xe = lo16(xw);
                STAMP(9 + 32 * layer);
            }
            BAR();
            if (wave == 3) stats(sa);
            MARK(e_op);
            STAMP(11 + 32 * layer);
            {
                LANE_VARS();
                const unsigned im = lb + L_WD + (unsigned)(32 * t + 8 * p4);
                bf16x8 wd0 = cat8(tr64(im + r0s * 160), tr64(im + r1s * 160)); bf16x4 wd1 = tr64(im + (32 + r2s) * 160);
                const unsigned h5 = tile35(gj, 10 * layer + 5 + t);
                unsigned h16 = lb + L_H + (unsigned)(p4 * 96 + kq * 16), h8 = lb + L_H + (unsigned)(p4 * 96 + 64 + kq * 8);
                unsigned so = h5 + (unsigned)((lane & 4) * 2048 + gi * 128 + (lane & 1) * 64 + (lane & 2) * 16 + kq * 8);
                unsigned long long mk = 0x00ff00ff00ff00ffull;
                unsigned go = h5 + nS + (unsigned)(2048 + lane * 16), xs = layer ? (unsigned)(kq * 1280 + gj * 160 + t * 32 + n * 2) : lb + L_XS + (unsigned)(kq * 160 + t * 32 + n * 2);
                unsigned sta = lb + L_ST + (unsigned)(kq * 16);
                if (layer == 0 && wave == 4) {
                    asm volatile("" : "=v"(sa.a16), "=v"(sa.b16), "=v"(sa.a8), "=v"(sa.b8), "=v"(sa.dst), "=v"(qo), "=v"(qw), "=v"(qc), "=v"(cma), "=v"(cxa), "=v"(cra), "=v"(cso), "=v"(omr), "=v"(owa), "=v"(owb));
                    asm volatile("" : "=v"(qa), "=v"(pw), "=v"(ma), "=v"(idx), "=v"(qm), "=v"(pr), "=v"(oa), "=v"(oc));
                } else if (layer == 0) {
                    sa = stats_addr(lane, WS_H1 + rS + (unsigned)(gi * 65536 + gj * 896 + 768), 16384);
                    op_addr(1); qk_addr(false); att_addr();
                    const int l5 = lane & 31, c = l5 < 24 ? 24 * (wave - 6) + l5 : 48;
                    qo = WS_H1 + rS + (unsigned)((gi * 4 + (gj >> 1)) * 16384 + 3584 + (lane >> 5) * 896 + c * 16);
                    qw = lb + L_Q + (unsigned)(l5 < 24 ? (c >> 2) * 32 + (c & 3) * 4 + (lane >> 5) * 16 : lane < 26 ? 384 + (lane & 1) * 16 : 448);
                    qc = lb + L_CS + (unsigned)(c * 16 + (lane >> 5) * 8);
                    PIN3(qo, qw, qc);
                }
                PIN2(wd0, wd1); PIN3(h16, h8, so); PIN3(go, xs, sta); PINS(mk);
                GUARD(1700);
                MARK(b_dn);
                if (wave == 4) {
                    unsigned os = h4 + (unsigned)(H4T + (p4 + 4 * (kq & 1)) * 128 + q4 * 16);
                    float sw = *reinterpret_cast<const float*>(smem + L_CNT + 12);
                    PIN2(os, sw);
                    if (layer == 0) {
#pragma unroll 1
                        for (unsigned spins = 0; spins < (kSpinCap << 4); ++spins) if (lds_word(lb + L_CNT + 56)) break;
                    }
                    sched(layer ? X4 : X4A, 0);
                    u32x4 as;
#pragma unroll 1
                    for (unsigned spins = 0; spins < kSpinCap; ++spins) { as = ld16_sc1(ws, os); WAIT1(as, 0); if (!any_empty(um4(as))) break; }
                    float ss = __uint_as_float(as[0]), sm = __uint_as_float(as[1]), rd = __uint_as_float(as[2]);
                    ss += dpp<0xB1>(ss); sm += dpp<0xB1>(sm); rd += dpp<0xB1>(rd);
                    ss += dpp<0x4E>(ss); sm += dpp<0x4E>(sm); rd += dpp<0x4E>(rd);
                    ss = x16_add(ss); sm = x16_add(sm); rd = x16_add(rd);
                    if (LANES(0x1111ull)) *reinterpret_cast<f32x4*>(smem + L_ST + q4 * 16) = (f32x4){ss, sm, rd, sw};
                }
                BAR();
                STAMP(12 + 32 * layer);
                const bf16x8 hb0 = lds16(h16); const bf16x4 hb1 = lds8(h8);
                __builtin_amdgcn_sched_barrier(0);
                const f32x4 c = mfma32(wd0, hb0, z4) + mfma16(wd1, hb1, z4);
                st8m(ws, so, pack4(c[0], c[1], c[2], c[3]), mk);
                STAMP(13 + 33 * layer);
                if (layer == 0 || gi == 4 * t + (gj & 3)) {
                    const f32x4 sr = *(const LAS f32x4*)(size_t)sta;
                    const f32x2 st = ln_stats(sr[0], sr[1]);
                    float rs = bfr(frcp(1.f + ex2(bfr((sr[2] - st[0] * sr[3]) * st[1]) * -LOG2E))), n1 = bfr(bfr(xe - st[0]) * st[1]);
                    PIN2(rs, n1);
                    if (layer == 0) {
                        sched(X5A, 0);
                        STAMP(15);
                        gather32(go, X5A, [&](float y) { xw = pk2(xe + bfr(n1 + bfr(y * rs)), 0.f); *(LAS u16*)(size_t)xs = (u16)xw; });
                        xe = lo16(xw);
                        STAMP(14);
                    } else {
                        unsigned long long mo = 0x1111111111111111ull; PINS(mo);
                        sched(X5B, 0);
                        STAMP(47);
                        gather32(go, X5B, [&](float y) { xw = pk2(xe + bfr(n1 + bfr(y * rs)), 0.f); });
                        { const unsigned w = xw | (dppu<0xF5>(xw) << 16);
                          const u32x2 w2 = {w, dppu<0xAA>(w)};
                          asm volatile("s_mov_b64 exec, %3\n\tglobal_store_dwordx2 %0, %1, %2 sc0 sc1\n\ts_mov_b64 exec, -1" :: "v"(xs), "v"(w2), "s"(out), "s"(mo) : "memory"); }
                        MARK(e_dn);
                        STAMP(45);
                    }
                }
            }
            if (layer == 0) {
                BAR();
                if (wave == 3) { stats(sa); STAMP(34); }
                MARK(e_dn);
                if (wave == 3) __builtin_amdgcn_s_setprio(2);
                if (__builtin_expect(wave == 4, 0)) { op_addr(1); qk_addr(false); att_addr(); }
                if (wave >= 6) {
                    const unsigned o = qo;
                    f32x2 cs = -*(const LAS f32x2*)(size_t)qc;
                    float rn = 1.f / DM;
                    unsigned long long m0 = 1ull << 24, m1 = 1ull << 25;
                    PIN2(cs, rn); PINS(m0); PINS(m1);
                    MARK(b_qg);
                    sched(X1, 0);
                    STAMP(51);
                    u32x4 a0, a1, a2, a3; f32x2 u;
                    RETRY({ a0 = ld16_sc1<-3584>(ws, o); a1 = ld16_sc1<-1792>(ws, o); a2 = ld16_sc1<0>(ws, o); a3 = ld16_sc1<1792>(ws, o); },
                          ({ asm volatile("s_waitcnt vmcnt(0)" : "+v"(a0), "+v"(a1), "+v"(a2), "+v"(a3) :: "memory"); max(max(um4(a0), um4(a1)), max(um4(a2), um4(a3))); }),
                          { unsigned sm;
                            asm volatile("s_waitcnt vmcnt(2)\n\tv_pk_add_f32 v[40:41], %2, %4\n\tv_pk_add_f32 v[42:43], %3, %5\n\t"
                                         "s_waitcnt vmcnt(1)\n\tv_pk_add_f32 v[40:41], v[40:41], %6\n\tv_pk_add_f32 v[42:43], v[42:43], %7\n\t"
                                         "s_waitcnt vmcnt(0)\n\tv_pk_add_f32 v[40:41], v[40:41], %8\n\tv_pk_add_f32 v[42:43], v[42:43], %9\n\ts_nop 1\n\t"
                                         "v_permlane32_swap_b32 v40, v42\n\tv_permlane32_swap_b32 v41, v43\n\tv_pk_add_f32 v[40:41], v[40:41], v[42:43]\n\ts_nop 0\n\t"
                                         "v_readlane_b32 %1, v41, 24\n\ts_nop 1\n\tv_mul_f32 v42, %1, %11\n\tv_cvt_pk_bf16_f32 v42, 0, v42\n\tv_pk_fma_f32 %0, %10, v[42:43], v[40:41] op_sel_hi:[1,0,1]"
                                         : "=&v"(u), "=&s"(sm) : "v"(LO2(a0)), "v"(HI2(a0)), "v"(LO2(a1)), "v"(HI2(a1)), "v"(LO2(a2)), "v"(HI2(a2)), "v"(LO2(a3)), "v"(HI2(a3)), "v"(cs), "v"(rn)
                                         : "memory", "v40", "v41", "v42", "v43");
                            unsigned w = pk2v(u);
                            if (LANES(m0)) w = __float_as_uint(u[0]);
                            if (LANES(m1)) w = __float_as_uint(u[1]);
                            *(LAS unsigned*)(size_t)qw = w; __builtin_amdgcn_sched_barrier(0); }, any_nan(u[0], u[1]), X1);
                    MARK(e_qg);
                }
            }
        }
    };
    PINS(ck1); PINS(ck2); PINS(csc);
    chain(IC<0>{});
    STAMP(35);
    att1();
    chain(IC<1>{});
}

__device__ __attribute__((used, noinline)) void mega_code_end() { asm volatile(".Lmega_code_end:"); }

extern "C" void kernel_launch(void* const* d_in, const int* in_sizes, int n_in, void* d_out, int out_size, void* d_ws,
                              size_t ws_size, hipStream_t stream) {
    const u16* x0 = (const u16*)d_in[0];
    const u16* kc = (const u16*)d_in[1];
    const u16* vc = (const u16*)d_in[2];
    const u16* wq = (const u16*)d_in[3];
    const u16* wo = (const u16*)d_in[4];
    const u16* wr = (const u16*)d_in[6];
    const u16* wg = (const u16*)d_in[7];
    const u16* wu = (const u16*)d_in[8];
    const u16* wd = (const u16*)d_in[9];
    (void)hipFuncSetAttribute((const void*)mega_kernel, hipFuncAttributeMaxDynamicSharedMemorySize, L_END);
    mega_kernel<<<256, 512, L_END, stream>>>(x0, kc, vc, wq, wo, wr, wg, wu, wd, (u16*)d_out, (unsigned char*)d_ws
                                             );
}
```

```cpp
#include <hip/hip_runtime.h>
#include <hip/hip_bf16.h>
#include <stdint.h>

typedef unsigned short u16;
typedef unsigned u32x4 __attribute__((ext_vector_type(4)));
typedef unsigned u32x2 __attribute__((ext_vector_type(2)));
typedef float f32x4 __attribute__((ext_vector_type(4)));
typedef short s16x4 __attribute__((ext_vector_type(4)));
typedef __attribute__((ext_vector_type(4))) __bf16 bf16x4;
typedef __attribute__((ext_vector_type(8))) __bf16 bf16x8;
#define LAS __attribute__((address_space(3)))

constexpr int DM = 640, HQ = 32, SK = 4096, DH = 192, WIN = 1024, FF = 1536;
constexpr int QD = HQ * DH;
constexpr float SCALE = 0.07216878364870322f, LOG2E = 1.44269504088896341f;
constexpr unsigned kSpinCap = 1u << 16;
#define GJ(g) (((g) >> 3) & 7)
#define GI(g) (((g) & 7) * 4 + ((g) >> 6))

#define D4A 118
#define D5A 96
#define D1 90
#define D2 142
#define D3 67
#define D4 78
#define D5B 79
#define PACE 60
#define PINIT 100
#define PMIN 50
#define DM_ 0
constexpr unsigned X4A = D4A + DM_, X5A = X4A + D5A + DM_, X1 = X5A + D1 + DM_, X2 = X1 + D2 + DM_, X3 = X2 + D3 + DM_, X4 = X3 + D4 + DM_, X5B = X4 + D5B + DM_;
#define WOC 2
#define WOFF 6
#define WEARLY 1
#define DG2 12
#define XCOST 10
#define GUC 3
#define XS 0
#define LEAD 12
#define TOFF 20
#define UG 360
#define SLOW 0
#define TLAND 160
#define PRET 1
#define ROWM 1
#define W3 15
#define PW 1
#define ROT gj
#define GROT 1
#define QROT gi
#define CMODE 1
#define ADOPT 1
constexpr unsigned NUNIT = 384 + XCOST + XS + GUC * (9 + 2 * WEARLY), PBURST = 30;
#define GSTEPS GA = 1, GB = 6, GC = 7, HD = 1
#define OML 2
#define EMODE (OML + 1)
constexpr int GP = 0, GSTEPS;

constexpr unsigned WS_H1 = 0;
constexpr unsigned WS_H4 = 2u << 20;
constexpr unsigned H4C = 20480, H4T = H4C + 3072;
constexpr unsigned WS_H35 = 4u << 20;
constexpr unsigned WS_OWN = WS_H35 + 80 * 16384;
constexpr unsigned WS_CS = WS_OWN + 32 * 16384;
constexpr unsigned WS_MB = WS_CS + 256 * 768;
constexpr unsigned WS_R = WS_MB + 256 * 128;
constexpr unsigned WS_END = WS_R + 128;
constexpr unsigned WS_Q0 = 8u << 20;
static_assert(WS_END <= (6u << 20), "ws map");
__device__ __forceinline__ unsigned tile35(int j, int q) { return WS_H35 + (unsigned)((j * 10 + (q >> 1)) * 16384 + (q & 1) * 4096); }
__device__ __forceinline__ unsigned h4s(int s) { return (unsigned)((s >> 1) * 16384 + (s & 1) * 4096); }

constexpr int L_WQ = 0;
constexpr int L_WO = 33792;
constexpr int L_GU = L_WO + 30720;
constexpr int L_WD = L_GU + 16384;
constexpr int L_X0 = L_WD + 8192;
constexpr int L_WR = L_X0 + 5120;
constexpr int L_XS = L_WR + 2048;
constexpr int L_Q = L_XS + 1024;
constexpr int L_OP = L_Q + 512;
constexpr int L_M = L_OP + 8192;
constexpr int L_H = L_M + 64;
constexpr int L_ST = L_H + 384;
constexpr int L_CS = L_ST + 64;
constexpr int L_CS2 = L_CS + 1024;
constexpr int L_CNT = L_CS2 + 1536;
constexpr int L_P = L_CNT + 96;
constexpr int L_O2 = L_P + 72 * 192;
constexpr int L_DBG = L_O2 + 3328;
constexpr int L_END = L_DBG + 2048;
static_assert(L_END <= 163840 && L_END > 81920, "LDS");

typedef __attribute__((ext_vector_type(2))) __bf16 bf16x2;
typedef float f32x2 __attribute__((ext_vector_type(2)));
__device__ __forceinline__ unsigned pk2(float a, float b) { return __builtin_bit_cast(unsigned, __builtin_convertvector((f32x2){a, b}, bf16x2)); }
__device__ __forceinline__ float lo16(unsigned w) { return __uint_as_float(w << 16); }
__device__ __forceinline__ float hi16(unsigned w) { return __uint_as_float(w & 0xffff0000u); }
__device__ __forceinline__ float bfr(float f) { return __uint_as_float(pk2(0.f, f)); }
template <int CTRL> __device__ __forceinline__ float dpp(float x) {
    return __builtin_bit_cast(float, __builtin_amdgcn_mov_dpp(__builtin_bit_cast(int, x), CTRL, 0xf, 0xf, true));
}
template <int CTRL> __device__ __forceinline__ unsigned dppu(unsigned x) { return (unsigned)__builtin_amdgcn_mov_dpp((int)x, CTRL, 0xf, 0xf, true); }
__device__ __forceinline__ float row16_sum(float v) {
    v += dpp<0xB1>(v); v += dpp<0x4E>(v); v += dpp<0x141>(v); v += dpp<0x140>(v); return v;
}
__device__ __forceinline__ float fmax2(float a, float b) { return __builtin_elementwise_maximum(a, b); }
__device__ __forceinline__ float fmax3(float a, float b, float c) { return fmax2(fmax2(a, b), c); }
__device__ __forceinline__ float x16_add(float v) { auto s = __builtin_amdgcn_permlane16_swap(__float_as_uint(v), __float_as_uint(v), false, false); return __uint_as_float(s[0]) + __uint_as_float(s[1]); }
__device__ __forceinline__ float x32_add(float v) { auto s = __builtin_amdgcn_permlane32_swap(__float_as_uint(v), __float_as_uint(v), false, false); return __uint_as_float(s[0]) + __uint_as_float(s[1]); }
__device__ __forceinline__ float wave_sum(float v) { return x32_add(x16_add(row16_sum(v))); }
__device__ __forceinline__ float rs32(float a, float b) { auto s = __builtin_amdgcn_permlane32_swap(__float_as_uint(a), __float_as_uint(b), false, false); return __uint_as_float(s[0]) + __uint_as_float(s[1]); }
__device__ __forceinline__ f32x2 sw16(float a, float b) { auto s = __builtin_amdgcn_permlane16_swap(__float_as_uint(a), __float_as_uint(b), false, false); return (f32x2){__uint_as_float(s[0]), __uint_as_float(s[1])}; }

#define GUARD(n) asm volatile("s_branch 1f\n\t.p2align 12,," #n "\n1:")
#define MARK(n) do {} while (0)
#define BAR() asm volatile("s_waitcnt lgkmcnt(0)\n\ts_barrier" ::: "memory")
#define VMW(n) asm volatile("s_waitcnt vmcnt(%0)" :: "i"(n) : "memory")

template <int OFF = 0> __device__ __forceinline__ u32x4 ld16_sc1(const void* sbase, unsigned voff) {
    u32x4 r; asm volatile("global_load_dwordx4 %0, %1, %2 offset:%3 sc1" : "=v"(r) : "v"(voff), "s"(sbase), "i"(OFF) : "memory"); return r;
}
__device__ __forceinline__ unsigned ld4_sc1(const void* sbase, unsigned voff) {
    unsigned r; asm volatile("global_load_dword %0, %1, %2 sc1" : "=v"(r) : "v"(voff), "s"(sbase) : "memory"); return r;
}
template <int OFF = 0> __device__ __forceinline__ void st16_sc1(void* sbase, unsigned voff, u32x4 v) {
    asm volatile("global_store_dwordx4 %0, %1, %2 offset:%3 sc1\n\ts_nop 1" :: "v"(voff), "v"(v), "s"(sbase), "i"(OFF) : "memory");
}
__device__ __forceinline__ void st4_sc1(void* sbase, unsigned voff, unsigned v) {
    asm volatile("global_store_dword %0, %1, %2 sc1" :: "v"(voff), "v"(v), "s"(sbase) : "memory");
}
__device__ __forceinline__ void st8(void* sbase, unsigned voff, u32x2 v) {
    asm volatile("global_store_dwordx2 %0, %1, %2" :: "v"(voff), "v"(v), "s"(sbase) : "memory");
}
__device__ __forceinline__ void st2(void* sbase, unsigned voff, unsigned v) {
    asm volatile("global_store_short %0, %1, %2" :: "v"(voff), "v"(v), "s"(sbase) : "memory");
}
constexpr int RK = 64, RV = 160;
template <int R, int IMM> __device__ __forceinline__ void ld16_nt(const void* sbase, unsigned voff) {
    asm volatile("global_load_dwordx4 v[%c3:%c4], %0, %1 offset:%2 nt" :: "v"(voff), "s"(sbase), "i"(IMM), "i"(R), "i"(R + 3) : "memory");
}
template <int R, int IMM> __device__ __forceinline__ void ld8_nt(const void* sbase, unsigned voff) {
    asm volatile("global_load_dwordx2 v[%c3:%c4], %0, %1 offset:%2 nt" :: "v"(voff), "s"(sbase), "i"(IMM), "i"(R), "i"(R + 1) : "memory");
}
template <int R> __device__ __forceinline__ void smf(f32x4& d, u32x4 a, unsigned idx) {
    asm volatile("v_smfmac_f32_16x16x64_bf16 %0, %1, v[%c3:%c4], %2" : "+v"(d) : "v"(a), "v"(idx), "i"(R), "i"(R + 7));
}
template <int A, int B> __device__ __forceinline__ void zip_in() {
    unsigned t;
    asm volatile("v_perm_b32 %0, v%c2, v%c1, %3\n\tv_perm_b32 v%c2, v%c2, v%c1, %4\n\tv_mov_b32 v%c1, %0" : "=&v"(t) : "i"(A), "i"(B), "s"(0x05040100u), "s"(0x07060302u));
}
template <int A, int B> __device__ __forceinline__ void zip_out(unsigned& lo, unsigned& hi) {
    asm volatile("v_perm_b32 %0, v%c3, v%c2, %4\n\tv_perm_b32 %1, v%c3, v%c2, %5" : "=&v"(lo), "=&v"(hi) : "i"(A), "i"(B), "s"(0x05040100u), "s"(0x07060302u));
}
template <int A, int B, int C, int D> __device__ __forceinline__ void zip_x(unsigned lo, unsigned hi) {
    asm volatile("v_perm_b32 v%c4, v%c3, v%c2, %6\n\tv_perm_b32 v%c5, v%c3, v%c2, %7\n\tv_mov_b32 v%c2, %0\n\tv_mov_b32 v%c3, %1"
                 :: "v"(lo), "v"(hi), "i"(A), "i"(B), "i"(C), "i"(D), "s"(0x05040100u), "s"(0x07060302u));
}
__device__ __forceinline__ void dma16(const void* sbase, unsigned voff, unsigned lds_dst) {
    unsigned keep;
    asm volatile("s_mov_b32 %0, m0\n\ts_mov_b32 m0, %3\n\ts_nop 0\n\tglobal_load_lds_dwordx4 %1, %2 nt\n\ts_mov_b32 m0, %0"
                 : "=&s"(keep) : "v"(voff), "s"(sbase), "s"(lds_dst) : "memory");
}
__device__ __forceinline__ void dma16p(const void* sbase, unsigned voff, unsigned lds_dst) {
    unsigned keep;
    asm volatile("s_mov_b32 %0, m0\n\ts_mov_b32 m0, %3\n\ts_nop 0\n\tglobal_load_lds_dwordx4 %1, %2\n\ts_mov_b32 m0, %0"
                 : "=&s"(keep) : "v"(voff), "s"(sbase), "s"(lds_dst) : "memory");
}
__device__ __forceinline__ unsigned um4(u32x4 a) { return max(max(a[0], a[1]), max(a[2], a[3])); }
__device__ __forceinline__ bool all_set(unsigned t) { return __builtin_amdgcn_ballot_w64(t == 0u) == 0ull; }
__device__ __forceinline__ bool any_empty(unsigned mx) { return __builtin_amdgcn_ballot_w64(mx == ~0u) != 0ull; }
__device__ __forceinline__ unsigned long long any_nan(float a, float b) { return __builtin_amdgcn_ballot_w64(__builtin_isunordered(a, b)); }
__device__ __forceinline__ f32x4 asf(u32x4 a) { return __builtin_bit_cast(f32x4, a); }
__device__ __forceinline__ u32x4 asu(f32x4 a) { return __builtin_bit_cast(u32x4, a); }
#define LANES(m) __builtin_amdgcn_inverse_ballot_w64(m)

__device__ __forceinline__ bf16x4 tr64(unsigned lds_addr) { return __builtin_amdgcn_ds_read_tr16_b64_v4bf16((LAS bf16x4*)(size_t)lds_addr); }
__device__ __forceinline__ bf16x4 tr64o(unsigned lds_addr, int off) { return __builtin_amdgcn_ds_read_tr16_b64_v4bf16((LAS bf16x4*)((LAS char*)(size_t)lds_addr + off)); }
__device__ __forceinline__ bf16x8 cat8(bf16x4 a, bf16x4 b) { return __builtin_shufflevector(a, b, 0, 1, 2, 3, 4, 5, 6, 7); }
__device__ __forceinline__ bf16x8 lds16(unsigned a) { return *(const LAS bf16x8*)(size_t)a; }
__device__ __forceinline__ bf16x4 lds8(unsigned a) { return *(const LAS bf16x4*)(size_t)a; }
__device__ __forceinline__ f32x4 mfma32(bf16x8 a, bf16x8 b, f32x4 c) { return __builtin_amdgcn_mfma_f32_16x16x32_bf16(a, b, c, 0, 0, 0); }
__device__ __forceinline__ f32x4 mfma32u(u32x4 a, u32x4 b, f32x4 c) { return mfma32(__builtin_bit_cast(bf16x8, a), __builtin_bit_cast(bf16x8, b), c); }
__device__ __forceinline__ f32x4 mfma16(bf16x4 a, bf16x4 b, f32x4 c) {
    return __builtin_amdgcn_mfma_f32_16x16x16bf16_1k(__builtin_bit_cast(s16x4, a), __builtin_bit_cast(s16x4, b), c, 0, 0, 0);
}

#define STAMP(i) do {} while (0)

template <int OFF = 0> __device__ __forceinline__ void st16m(void* sbase, unsigned voff, u32x4 v, unsigned long long mask) {
    asm volatile("s_mov_b64 exec, %4\n\tglobal_store_dwordx4 %0, %1, %2 offset:%3 sc1\n\ts_mov_b64 exec, -1\n\ts_nop 0" :: "v"(voff), "v"(v), "s"(sbase), "i"(OFF), "s"(mask) : "memory");
}
__device__ __forceinline__ void st8m(void* sbase, unsigned voff, u32x2 v, unsigned long long mask) {
    asm volatile("s_mov_b64 exec, %3\n\tglobal_store_dwordx2 %0, %1, %2 sc1\n\ts_mov_b64 exec, -1" :: "v"(voff), "v"(v), "s"(sbase), "s"(mask) : "memory");
}
template <int OFF> __device__ __forceinline__ void st16m2(void* sbase, unsigned voff, u32x4 v, unsigned voff2, u32x4 v2, unsigned long long mask) {
    asm volatile("s_mov_b64 exec, %6\n\tglobal_store_dwordx4 %0, %1, %2 sc1\n\tglobal_store_dwordx4 %4, %5, %2 offset:%3 sc1\n\ts_mov_b64 exec, -1\n\ts_nop 0"
                 :: "v"(voff), "v"(v), "s"(sbase), "i"(OFF), "v"(voff2), "v"(v2), "s"(mask) : "memory");
}
#define PINS(a) asm volatile("" : "+s"(a))
#define RETRY(LOAD, EMPTY, COMPUTE, NAN_, X) do { LOAD; COMPUTE; if (__builtin_expect((NAN_) != 0ull, 0)) { \
    _Pragma("unroll 1") for (unsigned spins_ = 0; spins_ < kSpinCap; ++spins_) { slip(X); LOAD; if (!any_empty(EMPTY)) break; } COMPUTE; } } while (0)
__device__ __forceinline__ unsigned now() { return (unsigned)__builtin_amdgcn_s_memrealtime(); }
__device__ __forceinline__ void until(unsigned t) {
    asm volatile("1:\n\ts_memrealtime vcc\n\ts_waitcnt lgkmcnt(0)\n\ts_sub_u32 vcc_lo, %0, vcc_lo\n\ts_cmp_lt_u32 vcc_lo, 0x400\n\ts_cbranch_scc1 1b" :: "s"(t - 1u) : "vcc", "scc", "memory");
}
__device__ __forceinline__ unsigned lds_word(unsigned a) {
    unsigned t_; asm volatile("ds_read_b32 %0, %1\n\ts_waitcnt lgkmcnt(0)\n\ts_nop 0" : "=v"(t_) : "v"(a) : "memory");
    return __builtin_amdgcn_readfirstlane(t_);
}
__device__ __forceinline__ void lds_set(unsigned a, unsigned v) { asm volatile("ds_write_b32 %0, %1" :: "v"(a), "v"(v) : "memory"); }
__device__ __forceinline__ int swz(int r) { return r ^ (((r >> 3) & 1) << 2); }
__device__ __forceinline__ f32x2 unpk(unsigned w) { return (f32x2){lo16(w), hi16(w)}; }
__device__ __forceinline__ unsigned pk2v(f32x2 v) { return __builtin_bit_cast(unsigned, __builtin_convertvector(v, bf16x2)); }
__device__ __forceinline__ float frcp(float x) { return __builtin_amdgcn_rcpf(x); }
__device__ __forceinline__ float ex2(float x) { return __builtin_amdgcn_exp2f(x); }
__device__ __forceinline__ int lane_id() { int l; asm volatile("v_mbcnt_lo_u32_b32 %0, -1, 0\n\tv_mbcnt_hi_u32_b32 %0, -1, %0" : "=v"(l)); return l & 63; }
__device__ __forceinline__ f32x2 ln_stats(float ss, float s) {
    const float mean = s * (1.f / DM);
    const float var = fmax2(__builtin_fmaf(-mean, mean, ss * (1.f / DM)), 0.f);
    return (f32x2){bfr(mean), frcp(bfr(__builtin_amdgcn_sqrtf(bfr(bfr(var) + 1e-5f))))};
}
#define GATHER8(a, o, S) do { a[0] = ld16_sc1<-4 * (S)>(ws, o); a[1] = ld16_sc1<-3 * (S)>(ws, o); a[2] = ld16_sc1<-2 * (S)>(ws, o); a[3] = ld16_sc1<-(S)>(ws, o); \
    a[4] = ld16_sc1<0>(ws, o); a[5] = ld16_sc1<(S)>(ws, o); a[6] = ld16_sc1<2 * (S)>(ws, o); a[7] = ld16_sc1<3 * (S)>(ws, o); } while (0)
template <int OFF> __device__ __forceinline__ void again16(u32x4& r, const void* sbase, unsigned voff) {
    asm volatile("v_cmp_eq_u32 vcc, -1, %1\n\ts_mov_b64 exec, vcc\n\tglobal_load_dwordx4 %0, %2, %3 offset:%4 sc1\n\ts_mov_b64 exec, -1" : "+v"(r) : "v"(um4(r)), "v"(voff), "s"(sbase), "i"(OFF) : "memory", "vcc");
}
#define AGAIN8(a, o, S) do { again16<-4 * (S)>(a[0], ws, o); again16<-3 * (S)>(a[1], ws, o); again16<-2 * (S)>(a[2], ws, o); again16<-(S)>(a[3], ws, o); \
    again16<0>(a[4], ws, o); again16<(S)>(a[5], ws, o); again16<2 * (S)>(a[6], ws, o); again16<3 * (S)>(a[7], ws, o); } while (0)
#define WAIT8(a, N) asm volatile("s_waitcnt vmcnt(%8)" : "+v"((a)[0]), "+v"((a)[1]), "+v"((a)[2]), "+v"((a)[3]), "+v"((a)[4]), "+v"((a)[5]), "+v"((a)[6]), "+v"((a)[7]) : "i"(N) : "memory")
#define UM32(a) max(max(max(um4(a[0]), um4(a[1])), max(um4(a[2]), um4(a[3]))), max(max(um4(a[4]), um4(a[5])), max(um4(a[6]), um4(a[7]))))
#define SUM8(a) (((((((asf(a[0]) + asf(a[1])) + asf(a[2])) + asf(a[3])) + asf(a[4])) + asf(a[5])) + asf(a[6])) + asf(a[7]))
#define WAIT1(x, N) asm volatile("s_waitcnt vmcnt(%1)" : "+v"(x) : "i"(N) : "memory")
#define LO2(x) __builtin_shufflevector(x, x, 0, 1)
#define HI2(x) __builtin_shufflevector(x, x, 2, 3)
#define ACC8(a, s) do { f32x2 l_, h_; \
    asm volatile("s_waitcnt vmcnt(5)\n\tv_pk_add_f32 %0, %2, %4\n\tv_pk_add_f32 %1, %3, %5\n\tv_pk_add_f32 %0, %0, %6\n\tv_pk_add_f32 %1, %1, %7\n\t" \
                 "s_waitcnt vmcnt(3)\n\tv_pk_add_f32 %0, %0, %8\n\tv_pk_add_f32 %1, %1, %9\n\tv_pk_add_f32 %0, %0, %10\n\tv_pk_add_f32 %1, %1, %11\n\t" \
                 "s_waitcnt vmcnt(1)\n\tv_pk_add_f32 %0, %0, %12\n\tv_pk_add_f32 %1, %1, %13\n\tv_pk_add_f32 %0, %0, %14\n\tv_pk_add_f32 %1, %1, %15\n\t" \
                 "s_waitcnt vmcnt(0)\n\tv_pk_add_f32 %0, %0, %16\n\tv_pk_add_f32 %1, %1, %17\n\ts_nop 0" \
                 : "=&v"(l_), "=&v"(h_) : "v"(LO2(a[0])), "v"(HI2(a[0])), "v"(LO2(a[1])), "v"(HI2(a[1])), "v"(LO2(a[2])), "v"(HI2(a[2])), "v"(LO2(a[3])), "v"(HI2(a[3])), \
                   "v"(LO2(a[4])), "v"(HI2(a[4])), "v"(LO2(a[5])), "v"(HI2(a[5])), "v"(LO2(a[6])), "v"(HI2(a[6])), "v"(LO2(a[7])), "v"(HI2(a[7])) : "memory"); \
    s = __builtin_shufflevector(l_, h_, 0, 1, 2, 3); } while (0)
#define RETRY8(a, o, S, s, MID, POST, NAN_, X) do { GATHER8(a, o, S); MID; ACC8(a, s); POST; if (__builtin_expect((NAN_) != 0ull, 0)) { \
    _Pragma("unroll 1") for (unsigned spins_ = 0; spins_ < kSpinCap; ++spins_) { slip(X); AGAIN8(a, o, S); WAIT8(a, 0); if (!any_empty(UM32(a))) break; } s = SUM8(a); POST; } } while (0)
#define PIN1(a) asm volatile("" : "+v"(a))
#define PIN2(a, b) asm volatile("" : "+v"(a), "+v"(b))
#define PIN3(a, b, c) asm volatile("" : "+v"(a), "+v"(b), "+v"(c))
#define LANE_VARS() const int lane = lane_id(); \
    const int kq = lane >> 4, n = lane & 15, q4 = n >> 2, p4 = lane & 3; \
    const int r0s = 8 * kq + q4 + 4 * (kq & 1), r1s = 8 * kq + q4 + 4 * ((kq & 1) ^ 1);     \
    const int r2s = swz(4 * kq + q4);                                                       \
    (void)r0s; (void)r1s; (void)r2s; (void)p4
template <int V> struct IC { static constexpr int value = V; };
struct Frag80 { bf16x8 a0, a1; bf16x4 a2; };

__global__ void __launch_bounds__(512, 2) __attribute__((amdgpu_num_vgpr(32)))
mega_kernel(const u16* __restrict__ x0, const u16* __restrict__ kc, const u16* __restrict__ vc, const u16* __restrict__ wq,
            const u16* __restrict__ wo, const u16* __restrict__ wr, const u16* __restrict__ wg, const u16* __restrict__ wu,
            const u16* __restrict__ wd, u16* __restrict__ out, unsigned char* __restrict__ ws_in
            ) {
    extern __shared__ __attribute__((aligned(16))) unsigned char smem[];
    const unsigned lb = (unsigned)(size_t)smem;
    unsigned char* const ws = (unsigned char*)(((uintptr_t)ws_in + (32u << 20) - 1) & ~(uintptr_t)((16u << 20) - 1));
    const int wave = __builtin_amdgcn_readfirstlane(threadIdx.x >> 6);
    const int g = blockIdx.x, gj = GJ(g), gi = GI(g);
    const f32x4 z4 = {0.f, 0.f, 0.f, 0.f};
    asm volatile("" ::: "v255");
    auto sched = [&](unsigned X, unsigned gap) __attribute__((always_inline)) {
        unsigned v; unsigned long long c;
        asm volatile("s_memrealtime %1\n\tds_read_b32 %0, %2\n\ts_waitcnt lgkmcnt(0)" : "=v"(v), "=&s"(c) : "v"(lb + L_CNT + 56) : "memory");
        const unsigned n = (unsigned)c + gap;
        unsigned t = __builtin_amdgcn_readfirstlane(v) + X;
        if ((int)(n - t) > 0) t = n;
        until(t);
    };
    auto slip = [&](unsigned X) __attribute__((always_inline)) {
        if (X) lds_set(lb + L_CNT + 56, now() - X);
    };
    float xe = 0.f;

    auto code_pf = [&](int slice, int lane) __attribute__((always_inline)) {
        unsigned long long pc0; unsigned len, back, t;
        asm volatile("s_getpc_b64 %0\n.Lmega_pf%=:\n\ts_mov_b32 %1, .Lmega_code_end-.Lmega_code_begin\n\ts_mov_b32 %2, .Lmega_pf%=-.Lmega_code_begin" : "=s"(pc0), "=s"(len), "=s"(back));
        unsigned off = (unsigned)(slice * 4096 + lane * 64); if (off > len) off = len;
        asm volatile("global_load_dword %0, %1, %2" : "=v"(t) : "v"(off & ~3u), "s"(pc0 - back) : "memory");
        return t;
    };
    asm volatile(".Lmega_code_begin:");
    auto fix_k1 = [&](auto IC_, auto NC) __attribute__((always_inline)) {
        constexpr int A = RK + 8 * decltype(IC_)::value, B = A + 4;
        unsigned t0, t1, t2, t3;
        asm volatile("s_waitcnt vmcnt(%12)\n\tv_mov_b32 %0, v%c4\n\tv_mov_b32 %1, v%c5\n\tv_mov_b32 %2, v%c6\n\tv_mov_b32 %3, v%c7\n\t"
                     "v_mov_b32_dpp v%c4, v%c8 row_ror:8 row_mask:0xf bank_mask:0xc\n\tv_mov_b32_dpp v%c5, v%c9 row_ror:8 row_mask:0xf bank_mask:0xc\n\t"
                     "v_mov_b32_dpp v%c6, v%c10 row_ror:8 row_mask:0xf bank_mask:0xc\n\tv_mov_b32_dpp v%c7, v%c11 row_ror:8 row_mask:0xf bank_mask:0xc\n\t"
                     "v_mov_b32_dpp v%c8, %0 row_ror:8 row_mask:0xf bank_mask:0x3\n\tv_mov_b32_dpp v%c9, %1 row_ror:8 row_mask:0xf bank_mask:0x3\n\t"
                     "v_mov_b32_dpp v%c10, %2 row_ror:8 row_mask:0xf bank_mask:0x3\n\tv_mov_b32_dpp v%c11, %3 row_ror:8 row_mask:0xf bank_mask:0x3"
                     : "=&v"(t0), "=&v"(t1), "=&v"(t2), "=&v"(t3)
                     : "i"(A), "i"(A + 1), "i"(A + 2), "i"(A + 3), "i"(B), "i"(B + 1), "i"(B + 2), "i"(B + 3), "i"(decltype(NC)::value) : "memory");
    };
    auto trans_v = [&](auto UC, auto NC) __attribute__((always_inline)) {
        constexpr int R = RV + 4 * decltype(UC)::value, N = decltype(NC)::value;
#define CELL(r, c) R + 16 * (r) + (c), R + 16 * (r) + 8 + (c)
        unsigned a0, a1, b0, b1, c0, c1;
        VMW(N + 6); zip_in<CELL(0, 0)>(); zip_out<CELL(0, 1)>(a0, a1); zip_out<CELL(0, 2)>(b0, b1); zip_out<CELL(0, 3)>(c0, c1);
        VMW(N + 4); zip_x<CELL(1, 0), CELL(0, 1)>(a0, a1); zip_in<CELL(1, 1)>(); zip_out<CELL(1, 2)>(a0, a1);
        VMW(N + 2); zip_x<CELL(2, 0), CELL(0, 2)>(b0, b1); zip_x<CELL(2, 1), CELL(1, 2)>(a0, a1); zip_in<CELL(2, 2)>(); zip_out<CELL(1, 3)>(a0, a1); zip_out<CELL(2, 3)>(b0, b1);
        VMW(N); zip_x<CELL(3, 0), CELL(0, 3)>(c0, c1); zip_x<CELL(3, 1), CELL(1, 3)>(a0, a1); zip_x<CELL(3, 2), CELL(2, 3)>(b0, b1); zip_in<CELL(3, 3)>();
#undef CELL
    };
    auto trans_w = [&](auto UC, auto NC) __attribute__((always_inline)) {
        constexpr int R = RV + 64 + 2 * decltype(UC)::value, N = decltype(NC)::value;
        unsigned a0, a1;
        VMW(N + 2); zip_in<R, R + 8>(); zip_out<R + 1, R + 9>(a0, a1);
        VMW(N); zip_x<R + 16, R + 24, R + 1, R + 9>(a0, a1); zip_in<R + 17, R + 25>();
    };
    auto frag80 = [&](unsigned im, int rs, int r0s, int r1s, int r2s) __attribute__((always_inline)) -> Frag80 {
        return Frag80{cat8(tr64(im + r0s * rs), tr64(im + r1s * rs)), cat8(tr64(im + (r0s + 32) * rs), tr64(im + (r1s + 32) * rs)), tr64(im + (r2s + 64) * rs)};
    };
    auto mm80 = [&](const Frag80& a, const Frag80& b) __attribute__((always_inline)) -> f32x4 {
        return mfma32(a.a1, b.a1, mfma32(a.a0, b.a0, z4)) + mfma16(a.a2, b.a2, z4);
    };
    auto rows80 = [&](unsigned a16, unsigned a8) __attribute__((always_inline)) -> Frag80 { const Frag80 f{lds16(a16), lds16(a16 + 64), lds8(a8)}; __builtin_amdgcn_sched_barrier(0); return f; };
    auto empty5 = [&](int q0, unsigned S, int lane) __attribute__((always_inline)) {
        st16m(ws, tile35(gj, q0 + (lane >> 3)) + S + (unsigned)(gi * 128 + (lane & 7) * 16), (u32x4){~0u, ~0u, ~0u, ~0u}, 0xffffffffffull);
    };
    auto empty4 = [&](int layer, unsigned S, int lane) __attribute__((always_inline)) {
        const unsigned b = WS_H4 + (unsigned)((layer * 32 + gi) * 32768) + (S ^ 8192u);
        st16_sc1(ws, b + (unsigned)((lane >> 5) * 4096 + gj * 512 + (lane & 31) * 16), (u32x4){~0u, ~0u, ~0u, ~0u});
        st16m(ws, b + (unsigned)(lane < 32 ? 16384 + gj * 512 + lane * 16 : H4C + ((lane - 32) >> 3) * 1024 + gj * 128 + (lane & 7) * 16), (u32x4){~0u, ~0u, ~0u, ~0u},
              layer ? 0xff000000ffffffffull : ~0ull);
    };
    const unsigned tst = now();
    int lead = wave == 7 ? LEAD * PACE * 4 : 0;
    auto pace = [&](unsigned n, bool wait = true) __attribute__((always_inline)) {
        unsigned u, w, t0;
        asm volatile("s_mov_b64 exec, 1\n\tds_add_rtn_u32 %0, %3, %4\n\tds_max_u32 %3, %5 offset:12\n\tds_add_rtn_u32 %1, %3, %6 offset:12\n\tds_read_b32 %2, %3 offset:4\n\ts_mov_b64 exec, -1\n\ts_waitcnt lgkmcnt(0)"
                     : "=&v"(u), "=&v"(w), "=&v"(t0) : "v"(lb + L_CNT + 60), "v"(n), "v"((now() - tst) << 6), "v"(n * PMIN * 4u) : "memory");
        int tn = (int)(__builtin_amdgcn_readfirstlane(t0) - tst + PINIT) * 64 + __builtin_amdgcn_readfirstlane(u) * (PACE * 4);
        tn += max((int)__builtin_amdgcn_readfirstlane(u) - UG, 0) * (SLOW * 4);
        const int tw = __builtin_amdgcn_readfirstlane(w) - PBURST * 64;
        if (tw > tn) tn = tw;
        tn -= lead;
        if (wait) {
#pragma unroll 1
            for (int k = 0; k < 2048 && (int)((now() - tst) << 6) < tn; ++k) {}
        }
    };
    auto slot = [&](unsigned u) __attribute__((always_inline)) {
        const unsigned tn = lds_word(lb + L_CNT + 64) + PINIT + (u * PACE + (NUNIT - UG) * SLOW) / 16;
#pragma unroll 1
        for (int k = 0; k < 8192 && (int)(now() - tn) < 0; ++k) __builtin_amdgcn_s_sleep(1);
    };
    auto load_wo = [&](int k, int lane, bool early) __attribute__((always_inline)) {
        const int c = k * 64 + lane, pp = c / 10, cc = c - 10 * pp;
        const unsigned f = (unsigned)(swz(pp) * 1280 + cc * 16);
        const u16* wb = wo + (size_t)gi * DH * DM + gj * 80;
#pragma unroll
        for (int a2 = 0; a2 < 6; ++a2) { if (a2 % 3 == 0) { if (early) pace(GUC); else slot(NUNIT - WOFF + (2 * k + a2 / 3) * WOC); } dma16(wb + 32 * a2 * DM, f, lb + L_WO + (5 * a2 + k) * 1024); }
    };
    auto load_wd = [&](int lane) __attribute__((always_inline)) {
        const u16* db = wd + gi * 48 * DM + gj * 80;
#pragma unroll 1
        for (int k = 0; k < 5; ++k) {
            const int c = k * 64 + lane, pp = c / 10, cc = c - 10 * pp;
            const unsigned fd = (unsigned)(swz(pp) * 1280 + cc * 16);
            if (!(k & 1)) pace(GUC);
            dma16(db, fd, lb + L_WD + k * 1024);
            if (k < 3) dma16(db + 32 * DM, (k < 2 || lane < 32) ? fd : 0u, lb + L_WD + (5 + k) * 1024);
        }
    };
    auto load_gu = [&](int m, int lane) __attribute__((always_inline)) {
#pragma unroll 1
        for (int k0 = 0; k0 < 3; ++k0) {
            const int k = (k0 + (GROT ? g & 7 : 0)) % 3, c = k * 64 + lane, row = c / 6, cc = c - 6 * row;
            const unsigned o = (unsigned)((swz(row) * FF + cc * 8) * 2), d = lb + L_GU + m * 8192 + k * 1024;
            const u16* wb = (m == 0 ? wg : wu) + gj * 80 * FF + gi * 48;
            pace(GUC);
            dma16(wb, o, d); dma16(wb + 32 * FF, o, d + 3072);
            if (k < 2) dma16(wb + 64 * FF, (k == 0 || lane < 32) ? o : 0u, d + 6144);
        }
    };
    auto lds_add = [&](int off, unsigned v) __attribute__((always_inline)) { asm volatile("s_mov_b64 exec, 1\n\tds_add_u32 %0, %1\n\ts_mov_b64 exec, -1" :: "v"(lb + L_CNT + off), "v"(v) : "memory"); };
    auto landed = [&]() __attribute__((always_inline)) {
        asm volatile("ds_max_u32 %0, %1" :: "v"(lb + L_CNT + 64), "v"(now() - TLAND) : "memory");
        lds_add(0, 1u);
    };
    auto send_flag = [&](int lane) __attribute__((always_inline)) {
        st4_sc1(ws, WS_MB + (unsigned)((gi * 8 + ((gj == (gi & 7) ? lane : gi) & 7)) * 128 + gj * 4), lds_word(lb + L_CNT + 68));
    };
    auto tiles4 = [&](const Frag80 (&f)[4], const Frag80& b, unsigned long long mk) __attribute__((always_inline)) -> u32x4 {
        f32x4 c[4], d[4];
#pragma unroll
        for (int i = 0; i < 4; ++i) c[i] = mfma32(f[i].a0, b.a0, z4);
#pragma unroll
        for (int i = 0; i < 4; ++i) { c[i] = mfma32(f[i].a1, b.a1, c[i]); d[i] = mfma16(f[i].a2, b.a2, z4); }
        __builtin_amdgcn_sched_barrier(0);
        f32x2 lo, hi;
        asm volatile("s_nop 1\n\ts_mov_b64 exec, %18\n\tv_pk_add_f32 %0, %2, %10\n\tv_pk_add_f32 %1, %3, %11\n\t"
                     "s_lshl_b64 exec, exec, 1\n\tv_pk_add_f32 %0, %4, %12\n\tv_pk_add_f32 %1, %5, %13\n\t"
                     "s_lshl_b64 exec, exec, 1\n\tv_pk_add_f32 %0, %6, %14\n\tv_pk_add_f32 %1, %7, %15\n\t"
                     "s_lshl_b64 exec, exec, 1\n\tv_pk_add_f32 %0, %8, %16\n\tv_pk_add_f32 %1, %9, %17\n\ts_mov_b64 exec, -1"
                     : "=&v"(lo), "=&v"(hi) : "v"(LO2(c[0])), "v"(HI2(c[0])), "v"(LO2(c[1])), "v"(HI2(c[1])), "v"(LO2(c[2])), "v"(HI2(c[2])), "v"(LO2(c[3])), "v"(HI2(c[3])),
                       "v"(LO2(d[0])), "v"(HI2(d[0])), "v"(LO2(d[1])), "v"(HI2(d[1])), "v"(LO2(d[2])), "v"(HI2(d[2])), "v"(LO2(d[3])), "v"(HI2(d[3])), "s"(mk) : "scc");
        return asu(__builtin_shufflevector(lo, hi, 0, 1, 2, 3));
    };
    auto empty1 = [&](int half, unsigned S, int lane) __attribute__((always_inline)) {
        const unsigned o = WS_H1 + S + (unsigned)((gi * 4 + 2 * half) * 16384 + gj * 896 + min(lane, 55) * 16);
        st16_sc1(ws, o, (u32x4){~0u, ~0u, ~0u, ~0u}); st16_sc1(ws, o + 16384, (u32x4){~0u, ~0u, ~0u, ~0u});
    };
    auto report = [&]() __attribute__((always_inline)) { if (gj == (gi & 7)) st4_sc1(ws, WS_R + (unsigned)(gi * 4), lds_word(lb + L_CNT + 88)); };
    auto answer = [&](int lane, unsigned r) __attribute__((always_inline)) {
        int d = min(max((int)(r - tst), -400), 600) * 512 + (int)((r >> 1) & 15u);
        d += (int)dppu<0xB1>((unsigned)d); d += (int)dppu<0x4E>((unsigned)d); d += (int)dppu<0x141>((unsigned)d); d += (int)dppu<0x140>((unsigned)d);
        d = __builtin_amdgcn_readlane(d, 0) + __builtin_amdgcn_readlane(d, 16);
        st4_sc1(ws, WS_MB + (unsigned)(((lane & 31) * 8 + (lane & 7)) * 128 + 32), ((tst + (d >> 14) + TOFF) & ~3u) | ((d & 511) > 128 ? 3u : 1u));
        lds_set(lb + L_CNT + 36, 1u);
    };
    auto adopt = [&](unsigned t) __attribute__((always_inline)) {
        if (ADOPT && t - tst + 256u < 768u) lds_set(lb + L_CNT + 64, t);
        lds_set(lb + L_CNT + 80, lds_word(lb + L_CNT + 92) ^ (t & 2u) << 12);
        lds_set(lb + L_CNT + 84, (((t << 1) ^ lds_word(lb + L_CNT + 68)) & 4u) << 11);
        lds_set(lb + L_CNT + 52, 1u);
    };
    struct StatAddr { unsigned a16, b16, a8, b8, dst; };
    auto stats_addr = [&](int lane, unsigned dbase, int dstride) __attribute__((always_inline)) -> StatAddr {
        const int kq = lane >> 4, q4 = (lane >> 2) & 3, p4 = lane & 3;
        const int x1 = LANES(0x4444444444444444ull) ? L_WR + gj * 160 : L_XS + q4 * 160, xa = LANES(0xaaaaaaaaaaaaaaaaull) ? L_XS + 640 : x1;
        StatAddr r{lb + (unsigned)(xa + kq * 16), lb + (unsigned)(L_XS + p4 * 160 + kq * 16), lb + (unsigned)(xa + 128 + kq * 8), lb + (unsigned)(L_XS + p4 * 160 + 128 + kq * 8), dbase + (unsigned)(kq * dstride)};
        PIN3(r.a16, r.b16, r.dst); PIN2(r.a8, r.b8);
        return r;
    };
    auto stats = [&](const StatAddr& r) __attribute__((always_inline)) {
        const bf16x8 a0 = lds16(r.a16), b0 = lds16(r.b16), a1 = lds16(r.a16 + 64), b1 = lds16(r.b16 + 64);
        const bf16x4 a2 = lds8(r.a8), b2 = lds8(r.b8);
        unsigned long long mk = 0x0008000400020001ull; PINS(mk);
        __builtin_amdgcn_sched_barrier(0);
        st16m(ws, r.dst, asu(mfma32(a1, b1, mfma32(a0, b0, z4)) + mfma16(a2, b2, z4)), mk);
    };
    unsigned qa, pw, ma, idx, qm;
    auto qk_addr = [&](bool L1) __attribute__((always_inline)) {
        LANE_VARS();
        qa = lb + L_Q + (unsigned)(kq * 32 + (lane & 2) * 8);
        pw = lb + L_P + (unsigned)((7 + 8 * wave) * 192 + (kq >> 1) * 64 + (n >> 3) * 32 + (lane & 2) * 8 + (q4 & 1) * 8 + (kq & 1) * 4 + (lane & 1) * 2);
        ma = lb + L_M + (unsigned)(wave * 4);
        idx = (lane & 2) ? 0xeeeeu : 0x4444u;
        qm = lb + L_Q;
        PIN3(qa, pw, ma); PIN2(idx, qm);
        if (L1 && wave < 4) __builtin_amdgcn_s_setprio(2);
    };
    auto att_qk0 = [&]() __attribute__((always_inline)) {
        qk_addr(false);
#pragma unroll 1
        for (unsigned spins = 0; spins < (kSpinCap << 4); ++spins) {
            if (lds_word(lb + L_CNT + 4) != 0u) break;
            __builtin_amdgcn_s_sleep(1);
        }
        STAMP(3);
        const u32x4 q0 = *(const LAS u32x4*)(size_t)qa, q1 = *(const LAS u32x4*)(size_t)(qa + 128), q2 = *(const LAS u32x4*)(size_t)(qa + 256);
        f32x4 c0 = z4, c1 = z4, c2 = z4, c3 = z4;
#define SMF(c, q, r) "v_smfmac_f32_16x16x64_bf16 %" #c ", %" #q ", v[" #r "], %7\n\t"
        asm volatile("s_nop 1\n\t" SMF(0, 4, 64:71) SMF(1, 4, 88:95) SMF(2, 4, 112:119) SMF(3, 4, 136:143) SMF(0, 5, 72:79) SMF(1, 5, 96:103) SMF(2, 5, 120:127) SMF(3, 5, 144:151)
                     SMF(0, 6, 80:87) SMF(1, 6, 104:111) SMF(2, 6, 128:135) SMF(3, 6, 152:159) "s_nop 7"
                     : "+v"(c0), "+v"(c1), "+v"(c2), "+v"(c3) : "v"(q0), "v"(q1), "v"(q2), "v"(idx));
#undef SMF
        const float s0 = c0[0] + c0[2], s1 = c1[0] + c1[2], s2 = c2[0] + c2[2], s3 = c3[0] + c3[2];
        float m = fmax2(fmax3(s0, s1, s2), s3);
        m = fmax2(m, dpp<0xB1>(m)); m = fmax2(m, dpp<0x4E>(m)); m = fmax2(m, dpp<0x124>(m)); m = fmax2(m, dpp<0x128>(m));
        const float s01 = LANES(0xffff0000ffff0000ull) ? s1 : s0, s23 = LANES(0xffff0000ffff0000ull) ? s3 : s2;
        const float sv = LANES(0xffffffff00000000ull) ? s23 : s01;
        m = bfr(bfr(m) * SCALE);
        const float p = ex2((bfr(bfr(sv) * SCALE) - m) * LOG2E);
        *(LAS u16*)(size_t)pw = (u16)pk2(p, p);
        *(LAS float*)(size_t)ma = m;
        __builtin_amdgcn_sched_barrier(0);
    };
    unsigned pr, oa, oc, cma, cxa, cra, cso;
    auto att_addr = [&]() __attribute__((always_inline)) {
        LANE_VARS();
        pr = lb + L_P + (unsigned)((7 + 8 * wave - 2 * q4 - (lane & 1)) * 192 + kq * 32 + (lane & 2) * 8);
        oa = lb + L_OP + (unsigned)(wave * 1024 + (lane & 7) * 64 + kq * 16 + (n >> 3) * 8);
        oc = lb + L_OP + (unsigned)(wave * 1024 + 512 + (kq >> 1) * 256 + (lane & 7) * 32 + (kq & 1) * 16 + (n >> 3) * 8);
        PIN3(pr, oa, oc);
    };
    auto comb_out = [&](float u0, float u1, unsigned so, unsigned long long mk, bool both) __attribute__((always_inline)) {
        const unsigned w = min(pk2(u0, u1), 0xfffffffeu);
        if (CMODE && !both) { st4_sc1(ws, so, w); return; }
        const u32x4 w4 = {both ? dppu<0x00>(w) : w, dppu<0x55>(w), dppu<0xAA>(w), dppu<0xFF>(w)};
        st16m(ws, so, w4, mk);
    };
    auto max8 = [&](f32x4 m0, f32x4 m1) __attribute__((always_inline)) { return fmax3(fmax3(m0[0], m0[1], m0[2]), fmax3(m0[3], m1[0], m1[1]), fmax2(m1[2], m1[3])) * LOG2E; };
    auto comb_o0 = [&](f32x4 m0, f32x4 m1, f32x4 mm, f32x4 o0, f32x4 o1, f32x4 o2, f32x4 o3, unsigned so, unsigned long long mk, bool both) __attribute__((always_inline)) {
        const float M = max8(m0, m1);
        f32x4 f;
#pragma unroll
        for (int k = 0; k < 4; ++k) f[k] = ex2(__builtin_fmaf(mm[k], LOG2E, -M));
        const f32x4 o = f[3] * o3 + (f[2] * o2 + (f[1] * o1 + f[0] * o0));
        comb_out(rs32(o[0], o[2]), rs32(o[1], o[3]), so, mk, both);
    };
    auto comb_o1 = [&](f32x4 m0, f32x4 m1, f32x2 mm, f32x4 o0, f32x4 o1, unsigned so, unsigned long long mk, bool both) __attribute__((always_inline)) {
        const float M = max8(m0, m1);
        const f32x4 o = ex2(__builtin_fmaf(mm[1], LOG2E, -M)) * o1 + ex2(__builtin_fmaf(mm[0], LOG2E, -M)) * o0;
        comb_out(x16_add(rs32(o[0], o[2])), x16_add(rs32(o[1], o[3])), so, mk, both);
    };
    auto ml_addr = [&](int lane, unsigned& xa, unsigned& ra) __attribute__((always_inline)) {
        xa = lb + L_M + (unsigned)(((lane & 15) >> 1) * 4); ra = lb + L_P + (unsigned)((7 + 8 * ((lane & 15) >> 1)) * 192 + (lane & 1) * 64 + (lane >> 4) * 16);
    };
    auto comb_ml = [&](unsigned ma, unsigned xa, unsigned ra, unsigned so, bool both) __attribute__((always_inline)) {
        const u32x4 x4 = *(const LAS u32x4*)(size_t)ra;
        const f32x4 m0 = *(const LAS f32x4*)(size_t)ma, m1 = *(const LAS f32x4*)(size_t)(ma + 16);
        const float mw = *(const LAS float*)(size_t)xa;
        __builtin_amdgcn_sched_barrier(0);
        const f32x4 sp = mfma32u((u32x4){0x3f803f80u, 0x3f803f80u, 0x3f803f80u, 0x3f803f80u}, x4, z4);
        __builtin_amdgcn_sched_barrier(0);
        const float M = max8(m0, m1);
        float L = row16_sum(sp[0] * ex2(__builtin_fmaf(mw, LOG2E, -M)));
        if (both) L = dpp<0x00>(L);
        st16m(ws, so, asu((f32x4){M, L, M, L}), both ? 3ull : 1ull);
    };
    auto att_pv0 = [&]() __attribute__((always_inline)) {
        u32x4 bm[8];
        trans_v(IC<0>{}, IC<24>{}); trans_v(IC<1>{}, IC<16>{});
#pragma unroll
        for (int e = 0; e < 8; ++e) bm[e] = *(const LAS u32x4*)(size_t)(pr + e * 192);
        __builtin_amdgcn_sched_barrier(0);
        f32x4 da = z4, dc = z4;
        PIN2(da, dc);
        asm volatile("s_nop 1");
        __builtin_amdgcn_sched_barrier(0);
        f32x2 r;
        smf<RV>(da, bm[0], idx); smf<RV + 8>(da, bm[1], idx); smf<RV + 16>(da, bm[2], idx); smf<RV + 24>(da, bm[3], idx);
        smf<RV + 32>(da, bm[4], idx); smf<RV + 40>(da, bm[5], idx); smf<RV + 48>(da, bm[6], idx); smf<RV + 56>(da, bm[7], idx);
        asm volatile("s_nop 7\n\tv_pk_add_f32 %0, %1, %2\n\tds_write_b64 %3, %0\n\ts_mov_b64 exec, 1\n\tds_add_u32 %4, %5\n\ts_mov_b64 exec, -1"
                     : "=&v"(r) : "v"(LO2(da)), "v"(HI2(da)), "v"(oa), "v"(lb + L_CNT + 24), "v"(1u) : "memory");
        GUARD(900);
        MARK(b_t5);
        trans_w(IC<0>{}, IC<12>{}); trans_w(IC<1>{}, IC<8>{}); trans_w(IC<2>{}, IC<4>{}); trans_w(IC<3>{}, IC<0>{});
        STAMP(4);
        asm volatile("s_nop 1\n\tv_smfmac_f32_16x16x64_bf16 %0, %1, v[224:231], %5\n\tv_smfmac_f32_16x16x64_bf16 %0, %2, v[232:239], %5\n\t"
                     "v_smfmac_f32_16x16x64_bf16 %0, %3, v[240:247], %5\n\tv_smfmac_f32_16x16x64_bf16 %0, %4, v[248:255], %5\n\ts_nop 6"
                     : "+v"(dc) : "v"(bm[0]), "v"(bm[1]), "v"(bm[2]), "v"(bm[3]), "v"(idx));
        asm volatile("s_nop 0\n\tv_pk_add_f32 %0, %1, %2\n\tds_write_b64 %3, %0" : "=&v"(r) : "v"(LO2(dc)), "v"(HI2(dc)), "v"(oc) : "memory");
        STAMP(20);
    };
    unsigned long long ck1 = 0xffff0000ffff0000ull, ck2 = 0xffffffff00000000ull, csc = 0x3d93cd3a3d93cd3aull;
    auto att1 = [&]() __attribute__((always_inline)) {
        constexpr int layer = 1;
        {
#define SMF(c, q, r) "v_smfmac_f32_16x16x64_bf16 v[" #c "], v[" #q "], v[" #r "], %0\n\t"
#define MAXD(x) "v_max_f32_dpp v41, v41, v41 " x " row_mask:0xf bank_mask:0xf\n\t"
            asm volatile("s_waitcnt lgkmcnt(0)\n\ts_barrier\n\t"
                         "ds_read_b128 v[0:3], %1\n\tds_read2_b32 v[12:13], %2 offset0:96 offset1:100\n\tds_read_b128 v[4:7], %1 offset:128\n\tds_read_b128 v[8:11], %1 offset:256\n\t"
                         "v_mov_b64 v[16:17], 0\n\tv_mov_b64 v[18:19], 0\n\tv_mov_b64 v[20:21], 0\n\tv_mov_b64 v[22:23], 0\n\tv_mov_b64 v[24:25], 0\n\tv_mov_b64 v[26:27], 0\n\t"
                         "v_mov_b64 v[28:29], 0\n\tv_mov_b64 v[30:31], 0\n\tv_mov_b64 v[32:33], 0\n\tv_mov_b64 v[34:35], 0\n\tv_mov_b64 v[36:37], 0\n\tv_mov_b64 v[38:39], 0\n\t"
                         "s_waitcnt lgkmcnt(3)\n\t" SMF(16:19, 0:3, 64:71) SMF(20:23, 0:3, 88:95) "s_waitcnt lgkmcnt(1)\n\tv_mul_f32 v13, 0x3acccccd, v13\n\t"
                         SMF(24:27, 0:3, 112:119) "v_mul_f32 v12, 0x3acccccd, v12\n\t" SMF(28:31, 0:3, 136:143) "v_fma_f32 v12, -v13, v13, v12\n\t"
                         SMF(16:19, 4:7, 72:79) "v_maximum3_f32 v12, v12, 0, 0\n\t" SMF(20:23, 4:7, 96:103) "v_cvt_pk_bf16_f32 v12, 0, v12\n\t"
                         SMF(24:27, 4:7, 120:127) "v_add_f32 v12, 0x3727c5ac, v12\n\t" SMF(28:31, 4:7, 144:151) "v_cvt_pk_bf16_f32 v12, 0, v12\n\ts_waitcnt lgkmcnt(0)\n\t"
                         SMF(16:19, 8:11, 80:87) "v_sqrt_f32 v12, v12\n\t" SMF(20:23, 8:11, 104:111) "v_cvt_pk_bf16_f32 v12, 0, v12\n\t"
                         SMF(24:27, 8:11, 128:135) "v_rcp_f32 v12, v12\n\t" SMF(28:31, 8:11, 152:159)
                         "s_nop 2\n\tv_add_f32 v42, v16, v18\n\ts_nop 0\n\tv_add_f32 v43, v20, v22\n\tv_max_f32 v41, v42, v43\n\tv_add_f32 v44, v24, v26\n\tv_add_f32 v45, v28, v30\n\t"
                         "v_max3_f32 v41, v41, v44, v45\n\tv_cndmask_b32 v42, v42, v43, %8\n\tv_cndmask_b32 v44, v44, v45, %8\n\t"
                         MAXD("quad_perm:[1,0,3,2]") "v_cndmask_b32 v40, v42, v44, %9\n\ts_nop 0\n\t" MAXD("quad_perm:[2,3,0,1]") "s_nop 1\n\t" MAXD("row_ror:4") "s_nop 1\n\t" MAXD("row_ror:8")
                         "v_pk_mul_f32 v[40:41], v[12:13], v[40:41] op_sel_hi:[0,1]\n\tv_cvt_pk_bf16_f32 v40, 0, v40\n\tv_cvt_pk_bf16_f32 v41, 0, v41\n\t"
                         "v_pk_mul_f32 v[40:41], %10, v[40:41]\n\tv_cvt_pk_bf16_f32 v40, 0, v40\n\tv_cvt_pk_bf16_f32 v41, 0, v41\n\t"
                         "v_sub_f32 v40, v40, v41\n\tv_mul_f32 v40, 0x3fb8aa3b, v40\n\tv_exp_f32 v40, v40\n\ts_nop 0\n\tv_cvt_pk_bf16_f32 v40, v40, v40\n\tds_write_b16 %3, v40\n\t"
                         "ds_read_b128 v[0:3], %4\n\tds_read_b128 v[4:7], %4 offset:192\n\tds_read_b128 v[8:11], %4 offset:384\n\tds_read_b128 v[12:15], %4 offset:576\n\t"
                         "ds_read_b128 v[16:19], %4 offset:768\n\tds_read_b128 v[20:23], %4 offset:960\n\tds_read_b128 v[24:27], %4 offset:1152\n\tds_read_b128 v[28:31], %4 offset:1344\n\t"
                         "ds_write_b32 %5, v41\n\t"
                         "s_waitcnt lgkmcnt(8)\n\t" SMF(32:35, 0:3, 160:167) SMF(36:39, 0:3, 224:231) "s_waitcnt lgkmcnt(7)\n\t" SMF(32:35, 4:7, 168:175) SMF(36:39, 4:7, 232:239)
                         "s_waitcnt lgkmcnt(6)\n\t" SMF(32:35, 8:11, 176:183) SMF(36:39, 8:11, 240:247) "s_waitcnt lgkmcnt(4)\n\t" SMF(32:35, 12:15, 184:191) SMF(36:39, 12:15, 248:255)
                         SMF(32:35, 16:19, 192:199) "s_waitcnt lgkmcnt(1)\n\t" SMF(32:35, 20:23, 200:207) SMF(32:35, 24:27, 208:215) SMF(32:35, 28:31, 216:223)
                         "s_nop 2\n\tv_pk_add_f32 v[36:37], v[36:37], v[38:39]\n\tds_write_b64 %7, v[36:37]\n\ts_nop 2\n\tv_pk_add_f32 v[32:33], v[32:33], v[34:35]\n\tds_write_b64 %6, v[32:33]"
                         :: "v"(idx), "v"(qa), "v"(qm), "v"(pw), "v"(pr), "v"(ma), "v"(oa), "v"(oc), "s"(ck1), "s"(ck2), "s"(csc)
                         : "memory", "v0", "v1", "v2", "v3", "v4", "v5", "v6", "v7", "v8", "v9", "v10", "v11", "v12", "v13", "v14", "v15", "v16", "v17", "v18", "v19", "v20", "v21", "v22", "v23",
                           "v24", "v25", "v26", "v27", "v28", "v29", "v30", "v31", "v32", "v33", "v34", "v35", "v36", "v37", "v38", "v39", "v40", "v41", "v42", "v43", "v44", "v45");
#undef SMF
#undef MAXD
            STAMP(20 + 32 * layer);
            BAR();
            if (__builtin_expect(wave == 0, 1)) {
                STAMP(5 + 32 * layer);
                comb_o0(*(const LAS f32x4*)(size_t)cma, *(const LAS f32x4*)(size_t)(cma + 16), *(const LAS f32x4*)(size_t)cxa, *(const LAS f32x4*)(size_t)cra,
                        *(const LAS f32x4*)(size_t)(cra + 1024), *(const LAS f32x4*)(size_t)(cra + 2048), *(const LAS f32x4*)(size_t)(cra + 3072), cso, 0x1111111111111111ull, false);
            }
            __builtin_amdgcn_sched_barrier(0);
            if (__builtin_expect(wave == 1, 1))
                comb_o1(*(const LAS f32x4*)(size_t)cma, *(const LAS f32x4*)(size_t)(cma + 16), *(const LAS f32x2*)(size_t)cxa, *(const LAS f32x4*)(size_t)cra, *(const LAS f32x4*)(size_t)(cra + 1024), cso, 0x0000111100001111ull, false);
            __builtin_amdgcn_sched_barrier(0);
            if (__builtin_expect(wave == 2, 1)) comb_ml(cma, cxa, cra, cso, false);
            MARK(e_q);
            if (wave < 3) STAMP(21 + 32 * layer);
            __builtin_amdgcn_s_setprio(0);
        }
    };

    unsigned nj = 0u, rS = 0u, nS = 0u; int ord;
    {
        const int tid = threadIdx.x, lane = tid & 63;
        if (tid < 24) *reinterpret_cast<unsigned*>(smem + L_CNT + tid * 4) = tid == 16 ? tst : 0u;
        asm volatile("" :: "s"(x0), "s"(wq), "s"(wr));
        BAR();
        STAMP(0);
        unsigned pf = 0u, mb = 0u;
        const u16* kbase = kc + ((size_t)((gj >> 1) * HQ + gi) * SK + (SK - WIN) + (gj & 1) * 512) * DH;
        const u16* vbase = vc + ((size_t)((gj >> 1) * HQ + gi) * SK + (SK - WIN) + (gj & 1) * 512) * DH;
        unsigned pt0 = 0u, pt1 = 0u;
        if (wave == 7) {
            __builtin_amdgcn_s_setprio(3);
            for (int i = 0; i < 5; ++i) dma16p(x0, (unsigned)(i * 1024 + lane * 16), lb + L_X0 + i * 1024);
            pf = code_pf((g >> 3) % 13, lane);
            if (lane < 10) *reinterpret_cast<u32x4*>(smem + L_XS + 640 + lane * 16) = (u32x4){0x3f803f80u, 0x3f803f80u, 0x3f803f80u, 0x3f803f80u};
            if (lane < 16) { *reinterpret_cast<u32x4*>(smem + L_CS + 768 + lane * 16) = (u32x4){0u, 0u, 0u, 0u}; }
        } else {
            if (wave >= 4) {
                if (wave == 4) st16_sc1(ws, WS_OWN + (unsigned)(gi * 16384 + (lane >> 5) * 8192 + gj * 512 + (lane & 31) * 16), (u32x4){~0u, ~0u, ~0u, ~0u});
                else empty5(0, (unsigned)(wave - 5) * 8192u, lane);
                if (wave == 6) {
                    __builtin_amdgcn_s_setprio(3);
                    if (gi + gj == 0) st4_sc1(ws, WS_R + (lane & 31) * 4, 0u);
                    st4_sc1(ws, WS_MB + (unsigned)((gi * 8 + gj) * 128 + (lane & 31) * 4), 0u);
                }
            } else if (wave >= 2) {
                unsigned long long c0, c1;
                if (PW) asm volatile("s_load_dword %0, %1, %2\n\ts_waitcnt lgkmcnt(0)" : "=&s"(pt0) : "s"(ws), "s"(WS_CS + g * 768 + (wave & 1) * 128) : "memory");
                asm volatile("s_memtime %0\n\ts_waitcnt lgkmcnt(0)\n\ts_load_dword %2, %3, %4\n\ts_waitcnt lgkmcnt(0)\n\ts_memtime %1\n\ts_waitcnt lgkmcnt(0)"
                             : "=&s"(c0), "=&s"(c1), "=&s"(pt0) : "s"(ws), "s"((6u << 20) + ((((tst + g * 977u) * 2654435761u) >> 11 & 0x1fdf80u) | ((wave ^ gj) & 1) * 8192)) : "memory");
                pt0 = ((wave ^ gj) & 1) ? (unsigned)c1 - (unsigned)c0 : (unsigned)c0 - (unsigned)c1;
            } else if (PRET) {
                if (wave == 0) asm volatile("s_load_dword %0, %1, %6\n\ts_load_dword %0, %2, %6\n\ts_load_dword %0, %3, 0\n\ts_load_dword %0, %4, 0\n\ts_load_dword %0, %5, %7"
                                            : "=&s"(pt1) : "s"(x0), "s"(wr), "s"(kbase), "s"(vbase), "s"(wq), "s"(gj * 128), "s"((gj * 80 + 79) * (QD * 2)) : "memory");
                else asm volatile("s_load_dword %0, %1, %6\n\ts_load_dword %0, %2, %6\n\ts_load_dword %0, %3, %6\n\ts_load_dword %0, %4, %7\n\ts_load_dword %0, %5, %8"
                                  : "=&s"(pt1) : "s"(wg), "s"(wu), "s"(wd), "s"(wo), "s"(out), "s"(g * 4096), "s"(gi * (DH * DM * 2)), "s"(gj * 128) : "memory");
            }
            STAMP(32);
#pragma unroll 1
            for (int i = wave < 2 ? wave + 1 : wave < 4 ? 35 : wave - 6; i < 33; i += 5) {
                if (i < 0) dma16p(wr, (unsigned)((i == -2 || lane < 16) ? (i + 2) * 1024 + lane * 16 : 0), lb + L_WR + (i + 2) * 1024);
                else {
                    int j = i + QROT; if (j >= 33) j -= 33;
                    const int c = j * 64 + lane; int row = c / 26, cc = c - 26 * row; if (cc > 23) cc = 23; if (row > 79) row = 79;
                    dma16(wq, (unsigned)(((gj * 80 + swz(row)) * QD + gi * DH + cc * 8) * 2), lb + L_WQ + j * 1024);
                }
            }
            STAMP(36);
            *reinterpret_cast<u32x4*>(smem + L_P + tid * 16) = (u32x4){0u, 0u, 0u, 0u};
            if (tid < 72 * 12 - 448) *reinterpret_cast<u32x4*>(smem + L_P + 7168 + tid * 16) = (u32x4){0u, 0u, 0u, 0u};
            if (wave >= 4) {
                asm volatile("s_waitcnt vmcnt(0) lgkmcnt(0)" ::: "memory");
                landed();
            } else if (wave >= 2) lds_add(20, pt0);
            else asm volatile("s_waitcnt lgkmcnt(0)" :: "s"(pt1) : "memory");
        }
        const int rot = ROT;
        u32x4 a[8];
        unsigned ko = (unsigned)((wave * 64 + (lane & 7)) * 384 + (((lane >> 3) & 1) * 4 + (lane >> 4)) * 16);
        PIN1(ko);
        auto k_group = [&](auto GC_) __attribute__((always_inline)) {
            constexpr int G = decltype(GC_)::value, R = RK + 24 * (G >> 1) + 4 * (G & 1);
            const unsigned o = ko + (unsigned)((((G >> 1) + rot) & 3) * 6144 + (G & 1) * 3072);
            pace(3);
            ld16_nt<R, 0>(kbase, o); ld16_nt<R + 8, 128>(kbase, o); ld16_nt<R + 16, 256>(kbase, o);
            const int lane = lane_id();
            const unsigned ho = WS_Q0 + (unsigned)((gi * 4 + (gj >> 1)) * 6144 + 3072 + (lane < 48 ? lane : 0) * 16);
            if (wave < 7) {
                asm volatile("; COND_BEGIN");
                if (G == 0 && wave < 4) {
                    asm volatile("s_waitcnt vmcnt(%0) lgkmcnt(0)" :: "i"(3) : "memory");
                    landed();
                    if (wave < 2) load_gu(wave, lane);
                    if (wave == 2) load_wd(lane);
                    if (wave == 3) {
#pragma unroll 1
                        for (int k = 5 - WEARLY; k < 5; ++k) load_wo(k, lane, true);
                    }
                }
                asm volatile("; COND_END");
            }
            if (wave == 7) {
                asm volatile("; COND_BEGIN");
                if (G == GP) {
                    VMW(3 * (GP + 1) + 1);
                    const unsigned char* p = smem + L_X0 + (lane >> 4) * 1280;
                    const float c0 = lo16(*reinterpret_cast<const u16*>(p));
                    const f32x2 c2 = {c0, c0};
                    f32x2 s2 = {0.f, 0.f}, q2 = s2;
#pragma unroll
                    for (int k = 0; k < 5; ++k) {
                        const u32x4 w = *reinterpret_cast<const u32x4*>(p + k * 256 + (lane & 15) * 16);
#pragma unroll
                        for (int e = 0; e < 4; ++e) { const f32x2 d = unpk(w[e]) - c2; s2 += d; q2 += d * d; }
                    }
                    const float s = row16_sum(s2[0] + s2[1]), qq = row16_sum(q2[0] + q2[1]);
                    const float ms = s * (1.f / DM);
                    const unsigned mv = pk2(c0 + ms, fmax2(qq - s * ms, 0.f) * (1.f / DM));
                    const float mb = lo16(mv), rden = frcp(bfr(__builtin_amdgcn_sqrtf(bfr(hi16(mv) + 1e-5f))));
                    const f32x2 mb2 = {mb, mb}, rd2 = {rden, rden};
                    if ((lane & 15) < 10) {
                        u32x4 w = *reinterpret_cast<const u32x4*>(p + gj * 160 + (lane & 15) * 16);
#pragma unroll
                        for (int e = 0; e < 4; ++e) w[e] = pk2v(unpk(pk2v(unpk(w[e]) - mb2)) * rd2);
                        *reinterpret_cast<u32x4*>(smem + L_XS + (lane >> 4) * 160 + (lane & 15) * 16) = w;
                    }
                    STAMP(30);
#pragma unroll 1
                    for (unsigned spins = 0; spins < (kSpinCap << 4); ++spins) {
                        if (lds_word(lb + L_CNT) >= 7u) break;
                        __builtin_amdgcn_s_sleep(1);
                    }
                    STAMP(26);
                }
                if (G == GP || G == GA) {
#pragma unroll 1
                    for (int blk = 0; blk < 3; ++blk) {
                        LANE_VARS();
                        const unsigned xb = lb + L_XS + (unsigned)((G == GP ? q4 : 4) * 160);
                        Frag80 f[4];
#pragma unroll
                        for (int i = 0; i < 4; ++i) f[i] = frag80(lb + L_WQ + (unsigned)(128 * blk + 32 * i + 8 * p4), 416, r0s, r1s, r2s);
                        const Frag80 b = rows80(xb + kq * 16, xb + 128 + kq * 8);
                        const u32x4 c = tiles4(f, b, 0x1111111111111111ull);
                        if (G == GP) st16_sc1(ws, WS_Q0 + (unsigned)((gi * 4 + q4) * 6144 + gj * 768 + 256 * blk + kq * 64 + p4 * 16), c);
                        else *reinterpret_cast<u32x4*>(smem + L_CS + 256 * blk + kq * 64 + p4 * 16) = c;
                    }
                    if (G == GP) { STAMP(2); WAIT1(pf, 3 * (GP + 1) + 3); }
                }
                if (G == GA) {
                    const unsigned half = __builtin_amdgcn_s_getreg(2 << 6 | 20);
                    lds_set(lb + L_CNT + 68, (lds_word(lb + L_CNT + 64) & ~7u) | 1u | (((int)lds_word(lb + L_CNT + 20) > 0) ^ half) << 1 | half << 2);
                    VMW(3 * (GA - GP));
                    send_flag(lane_id());
                    lds_set(lb + L_CNT + 8, 1u);
                    if (XS) pace(XS, false);
                    STAMP(17);
                    f32x2 r2 = {0.f, 0.f};
#pragma unroll
                    for (int k = 0; k < 5; ++k) r2 += unpk(*reinterpret_cast<const unsigned*>(smem + L_WR + k * 256 + lane_id() * 4));
                    *reinterpret_cast<float*>(smem + L_CNT + 12) = wave_sum(r2[0] + r2[1]);
                }
                asm volatile("; COND_END");
            }
            if (wave == 6) {
                asm volatile("; COND_BEGIN");
                if (G == GB - 1) mb = ld4_sc1(ws, WS_MB + (unsigned)((gi * 8 + gj) * 128 + (gi & 7) * 4));
                if (G == GB) {
                    WAIT1(mb, 3);
#pragma unroll 1
                    for (unsigned spins = 0; spins < kSpinCap && !all_set(mb); ++spins) {
                        if ((spins & 7u) == 7u && lds_word(lb + L_CNT + 8)) send_flag(lane);
                        mb = ld4_sc1(ws, WS_MB + (unsigned)((gi * 8 + gj) * 128 + (gi & 7) * 4));
                        WAIT1(mb, 0);
                    }
                    mb = ld4_sc1(ws, WS_MB + (unsigned)((gi * 8 + (gi & 7)) * 128 + (lane & 7) * 4));
                }
                if (G == GC) {
                    WAIT1(mb, 3 * (GC - GB));
#pragma unroll 1
                    for (unsigned spins = 0; spins < kSpinCap && !all_set(mb); ++spins) {
                        if ((spins & 7u) == 7u && lds_word(lb + L_CNT + 8)) send_flag(lane);
                        mb = ld4_sc1(ws, WS_MB + (unsigned)((gi * 8 + (gi & 7)) * 128 + (lane & 7) * 4));
                        WAIT1(mb, 0);
                    }
                    lds_set(lb + L_CNT + 92, (__builtin_amdgcn_readlane(mb, gi & 7) & 4u) << 11);
                    int d = min(max((int)(mb - tst), -400), 600);
                    d += (int)dppu<0xB1>((unsigned)d); d += (int)dppu<0x4E>((unsigned)d); d += (int)dppu<0x141>((unsigned)d);
                    const unsigned tm = tst + (unsigned)(__builtin_amdgcn_readfirstlane(d) >> 3);
                    if (ROWM) lds_set(lb + L_CNT + 64, tm);
                    lds_set(lb + L_CNT + 88, (tm & ~31u) | __builtin_popcount((unsigned)__builtin_amdgcn_ballot_w64((mb & 2u) != 0u) & 255u) << 1 | 1u);
                    report();
                    GATHER8(a, ho, 768);
                    pace(XCOST, false);
                    STAMP(31);
                }
                asm volatile("; COND_END");
            } else if (G == GC) { for (int k = 0; k < 8; ++k) asm volatile("" : "=v"(a[k])); }
        };
        k_group(IC<0>{}); k_group(IC<1>{}); k_group(IC<2>{}); k_group(IC<3>{}); k_group(IC<4>{}); k_group(IC<5>{}); k_group(IC<6>{}); k_group(IC<7>{});
        {
            const int lane = lane_id();
            const unsigned v0 = (unsigned)((wave * 64 + 4 * (lane >> 4)) * 384 + (lane & 15) * 16);
            const unsigned v1 = v0 + 256 - (unsigned)((lane & 15) * 8);
            auto v_group = [&](auto HC) __attribute__((always_inline)) {
                constexpr int h = decltype(HC)::value, R = h < 8 ? RV + 16 * (h & 3) + 4 * (h >> 2) : RV + 64 + 2 * (h - 8), NV = h < 8 ? 2 * h + 2 : 4 * h - 12;
                const unsigned o = (h < 8 ? v0 : v1) + (unsigned)((((h < 8 ? h >> 1 : h) + rot) & 3) * 6144);
                pace(2);
                if (h < 8) { ld16_nt<R, (h & 1) * 768>(vbase, o); ld16_nt<R + 8, (h & 1) * 768 + 384>(vbase, o); }
                else { ld8_nt<R, 0>(vbase, o); ld8_nt<R + 8, 384>(vbase, o); ld8_nt<R + 16, 768>(vbase, o); ld8_nt<R + 24, 1152>(vbase, o); }
                if (wave == 7 && gi + gj == 0) {
                    asm volatile("; COND_BEGIN");
                    if (h == 3 || h == 6) mb = ld4_sc1(ws, WS_R + (unsigned)((lane & 31) * 4));
                    if (h == 4 || h == 7) {
                        WAIT1(mb, 2);
                        if (all_set(mb) && !lds_word(lb + L_CNT + 36)) answer(lane, mb);
                    }
                    asm volatile("; COND_END");
                }
                if (wave == 6) {
                    asm volatile("; COND_BEGIN");
                if (h == HD) {
                    WAIT8(a, 3 * (7 - GC) + NV);
                    const f32x4 sq = SUM8(a);
                    unsigned* qp = reinterpret_cast<unsigned*>(smem + L_Q + (lane >> 4) * 128 + (lane & 3) * 32 + (lane & 12));
                    qp[0] = pk2(sq[0], sq[1]); qp[4] = pk2(sq[2], sq[3]);
                    asm volatile("s_waitcnt lgkmcnt(0)\n\tds_write_b32 %0, %1" :: "v"(lb + L_CNT + 4), "v"(1u) : "memory");
                    STAMP(27);
                    lead = LEAD * PACE * 4;
                }
                    if (h == 6) mb = ld4_sc1(ws, WS_MB + (unsigned)((gi * 8 + (gi & 7)) * 128 + 32));
                    if (h == 8) {
                        WAIT1(mb, 6);
                        const unsigned r = __builtin_amdgcn_readfirstlane(mb);
                        if (r) { adopt(r); STAMP(29); }
                    }
                    asm volatile("; COND_END");
                }
                if constexpr (h < 6) {
                    constexpr int i = 2 * h;
                    fix_k1(IC<i>{}, IC<20 - 6 * (i / 3) - i % 3 + NV>{});
                    fix_k1(IC<i + 1>{}, IC<20 - 6 * ((i + 1) / 3) - (i + 1) % 3 + NV>{});
                }
                if (h == 5) {
                    STAMP(1);
                    __builtin_amdgcn_s_setprio(0);
                    att_qk0();
                }
            };
            v_group(IC<0>{}); v_group(IC<1>{}); v_group(IC<2>{}); v_group(IC<3>{}); v_group(IC<4>{}); v_group(IC<5>{});
            v_group(IC<6>{}); v_group(IC<7>{}); v_group(IC<8>{}); v_group(IC<9>{}); v_group(IC<10>{}); v_group(IC<11>{});
            lds_add(16, 1u);
            if (wave == 3) lds_add(44, WEARLY);
        }
    }
    {
        STAMP(28);
        att_addr();
        const int lane = lane_id();
        cma = lb + L_M; cxa = lb + L_M + (unsigned)((lane >> 4) * 8); cra = lb + L_OP + (unsigned)(512 + (lane >> 4) * 2048 + (lane & 15) * 16);
        cso = WS_OWN + (unsigned)(gi * 16384 + gj * 512 + 256 + (lane >> 5) * 64 + (lane & 12) * 4 + (lane & 1) * 8192);
        PIN2(cma, cxa); PIN2(cra, cso);
        att_pv0();
    }
    {
        f32x4 m0, m1, o0, o1; f32x2 mm; unsigned r;
        asm volatile("s_mov_b64 exec, 1\n\tds_add_rtn_u32 %5, %6, %7 offset:%10\n\ts_mov_b64 exec, -1\n\t"
                     "ds_read_b128 %0, %6\n\tds_read_b128 %1, %6 offset:16\n\tds_read_b64 %2, %8\n\tds_read_b128 %3, %9\n\tds_read_b128 %4, %9 offset:1024\n\ts_waitcnt lgkmcnt(0)"
                     : "=&v"(m0), "=&v"(m1), "=&v"(mm), "=&v"(o0), "=&v"(o1), "=&v"(r)
                     : "v"(cma), "v"(1u), "v"(cxa), "v"(cra), "i"(L_CNT + 28 - L_M) : "memory");
        ord = __builtin_amdgcn_readfirstlane(r);
        if (ord == 7) { STAMP(5); comb_o1(m0, m1, mm, o0, o1, cso, 0x0000333300003333ull, true); lds_set(lb + L_CNT + 48, now() | 1u); STAMP(21); }
        MARK(e_t5);
        const int lane = lane_id();
        auto wait_cnt = [&](int off, unsigned v) __attribute__((always_inline)) {
#pragma unroll 1
            for (unsigned spins = 0; spins < (kSpinCap << 4); ++spins) { if (lds_word(lb + L_CNT + off) >= v) break; __builtin_amdgcn_s_sleep(1); }
        };
        if (ord == OML) {
            wait_cnt(16, 8u);
            unsigned xa, ra; ml_addr(lane, xa, ra);
            comb_ml(cma, xa, ra, WS_OWN + (unsigned)(gi * 16384 + gj * 512 + 384 + (lane & 1) * 8192), true);
            STAMP(24);
        } else if (ord == 0 && !lds_word(lb + L_CNT + 52)) {
            if (gi + gj == 0 && !lds_word(lb + L_CNT + 36)) {
                unsigned r = 0u;
#pragma unroll 1
                for (unsigned spins = 0; spins < kSpinCap; ++spins) {
                    r = ld4_sc1(ws, WS_R + (unsigned)((lane & 31) * 4));
                    WAIT1(r, 0);
                    if (all_set(r)) break;
                    if ((spins & 7u) == 7u) send_flag(lane);
                }
                answer(lane, r);
            }
#pragma unroll 1
            for (unsigned spins = 0; spins < kSpinCap; ++spins) {
                unsigned r = ld4_sc1(ws, WS_MB + (unsigned)((gi * 8 + (gi & 7)) * 128 + 32));
                WAIT1(r, 0);
                r = __builtin_amdgcn_readfirstlane(r);
                if (r) { adopt(r); break; }
                if ((spins & 7u) == 7u) { send_flag(lane); report(); }
                __builtin_amdgcn_s_sleep(16);
            }
            STAMP(29);
        } else if (ord == OML + 1) {
#pragma unroll 1
            for (unsigned spins = 0; spins < (kSpinCap << 6); ++spins) if (lds_word(lb + L_CNT + 24) >= 8u) break;
            const unsigned ra = lb + L_OP + (unsigned)((lane >> 5) * 4096 + (lane & 31) * 16);
            comb_o0(*(const LAS f32x4*)(size_t)cma, *(const LAS f32x4*)(size_t)(cma + 16), *(const LAS f32x4*)(size_t)(cma + (lane >> 5) * 16), *(const LAS f32x4*)(size_t)ra, *(const LAS f32x4*)(size_t)(ra + 1024),
                    *(const LAS f32x4*)(size_t)(ra + 2048), *(const LAS f32x4*)(size_t)(ra + 3072), WS_OWN + (unsigned)(gi * 16384 + gj * 512 + (lane & 60) * 4 + (lane & 1) * 8192), 0x3333333333333333ull, true);
            STAMP(22);
        }
        if (ord == EMODE) {
#pragma unroll 1
            for (unsigned spins = 0; spins < (kSpinCap << 8); ++spins) { if (lds_word(lb + L_CNT + 52)) break; __builtin_amdgcn_s_sleep(1); }
            const unsigned S = lds_word(lb + L_CNT + 80);
            empty4(0, S, lane); empty5(5, 0u, lane); empty5(5, 8192u, lane);
        }
#pragma unroll 1
        for (;;) {
            unsigned j;
            asm volatile("s_mov_b64 exec, 1\n\tds_add_rtn_u32 %0, %1, %2\n\ts_mov_b64 exec, -1\n\ts_waitcnt lgkmcnt(0)" : "=&v"(j) : "v"(lb + L_CNT + 40), "v"(1u) : "memory");
            j = __builtin_amdgcn_readfirstlane(j);
            if (j >= 5u - WEARLY) break;
            load_wo((int)j, lane, false); ++nj;
        }
        STAMP(23);
    }

    unsigned omr, owa, owb;
    auto op_addr = [&](int layer) __attribute__((always_inline)) {
        LANE_VARS();
        const int t = wave - 3;
        cma = WS_OWN + rS + (unsigned)(gi * 16384 + layer * 4096 + (lane >> 3) * 512 + t * 128 + (t == 3 ? 0 : (lane & 7) * 16));
        cxa = lb + L_O2 + (unsigned)((lane >> 3) * 416 + t * 128 + (t == 3 ? 0 : (lane & 7) * 16));
        const int pb = (lane & 4) ? 3 - p4 : p4, jp = 2 * pb + ((lane >> 2) & 1);
        cso = tile35(gj, 10 * layer + t) + (unsigned)((lane & 4) * 2048 + gi * 128 + (kq & 1) * 64 + (kq >> 1) * 32 + pb * 8);
        cra = lb + L_O2 + (unsigned)(jp * 416 + kq * 16); omr = lb + L_O2 + (unsigned)(jp * 416 + 384);
        owa = lb + L_WO + (unsigned)(r0s * 160 + 32 * t + 8 * p4); owb = lb + L_WO + (unsigned)(r1s * 160 + 32 * t + 8 * p4);
        if (layer) PIN2(cma, cxa);
        PIN2(cso, cra); PIN3(omr, owa, owb);
    };
    auto chain = [&](auto LC) __attribute__((always_inline)) {
        constexpr int layer = decltype(LC)::value;
        unsigned h4 = WS_H4 + (unsigned)((layer * 32 + gi) * 32768) + (layer ? rS ^ 8192u : 0u);
        auto sides = [&]() __attribute__((always_inline)) { rS = lds_word(lb + L_CNT + 80); nS = lds_word(lb + L_CNT + 84); h4 += rS ^ 8192u; };
        if (wave < 3) {
            LANE_VARS();
            if (layer == 0) {
                const int l0 = wave == 0 ? 2 : wave - 1;
                const unsigned lo = WS_OWN + lds_word(lb + L_CNT + 84) + (unsigned)(gi * 16384 + (lane >> 3) * 512), lw = lb + L_O2 + (unsigned)((lane >> 3) * 416);
                unsigned lo0 = lo + (unsigned)(l0 * 128 + (lane & 7) * 16), lo1 = wave == 1 ? lo + 384 : lo0, lw0 = lw + (unsigned)(l0 * 128 + (lane & 7) * 16), lw1 = wave == 1 ? lw + 384 : lw0;
                PIN2(lo0, lo1); PIN2(lw0, lw1);
                GUARD(600);
                MARK(b_g2);
                unsigned tf = 0u;
#pragma unroll 1
                for (unsigned spins = 0; spins < (kSpinCap << 4); ++spins) { tf = lds_word(lb + L_CNT + 48); if (tf) break; }
                if (wave == 0) until(tf + DG2);
#pragma unroll 1
                for (unsigned spins = 0; spins < kSpinCap; ++spins) {
                    u32x4 o0 = ld16_sc1(ws, lo0), o1;
                    if (wave == 1) o1 = ld16_sc1(ws, lo1); else asm volatile("" : "=v"(o1));
                    if (nj) { if (wave == 1) VMW(2); else VMW(1); lds_add(44, nj); nj = 0u; }
                    asm volatile("s_waitcnt vmcnt(0)" : "+v"(o0), "+v"(o1) :: "memory");
                    if (wave != 1) o1 = o0;
                    *(LAS u32x4*)(size_t)lw0 = o0; *(LAS u32x4*)(size_t)lw1 = o1;
                    if (!any_empty(max(um4(o0), um4(o1)))) break;
                    if ((spins & 15u) == 15u) send_flag(lane);
                }
                MARK(e_g2);
                STAMP(25);
                BAR();
                sides();
                if (wave < 2) empty1(wave, rS, lane);
                else {
                    st16m(ws, WS_CS + (unsigned)((gi * 8 + gj) * 768 + lane * 16), *reinterpret_cast<const u32x4*>(smem + L_CS + lane * 16), 0xffffffffffffull);
                    empty5(10, 0u, lane); empty5(10, 8192u, lane);
                }
            } else BAR();
            {
                const unsigned im = lb + L_GU + (unsigned)(32 * wave + 8 * p4);
                Frag80 fg = frag80(im, 96, r0s, r1s, r2s), fu = frag80(im + 8192, 96, r0s, r1s, r2s);
                const unsigned xb = lb + L_XS + (unsigned)((n < 4 ? n : 4) * 160);
                unsigned b16 = xb + kq * 16, b8 = xb + 128 + kq * 8;
                unsigned sg = h4 + (unsigned)(n < 4 ? h4s(wave) + gj * 512 + kq * 64 + n * 16 : H4C + wave * 1024 + gj * 128 + kq * 16), su = sg + (n < 4 ? 256u : 64u);
                unsigned o = h4 + h4s(wave) + (unsigned)(lane * 16);
                unsigned oc = h4 + (unsigned)(H4C + wave * 1024 + ((lane >> 5) * 4 + p4) * 128 + (kq & 1) * 64 + q4 * 16);
                unsigned os = h4 + (unsigned)(H4T + (q4 + 4 * (kq & 1)) * 128 + p4 * 16);
                unsigned ha = lb + L_H + (unsigned)(p4 * 96 + wave * 32 + q4 * 8 + kq * 2);
                f32x2 cs = {0.f, 0.f};
                if (layer != 0) cs = *reinterpret_cast<const f32x2*>(smem + L_CS2 + wave * 512 + lane * 8);
                PIN3(fg.a0, fg.a1, fg.a2); PIN3(fu.a0, fu.a1, fu.a2); PIN3(b16, b8, sg); PIN3(su, o, os); PIN2(ha, cs);
                unsigned long long mk = layer ? 0x000f000f000f000full : 0x001f001f001f001full;
                unsigned ta = WS_MB + (unsigned)((lane >> 1) * 1024 + 64 + (lane & 1) * 16);
                if (layer == 0) PIN2(oc, ta);
                PINS(mk);
                if (layer == 0) VMW(0);
                if (layer == 0) GUARD(1200); else GUARD(800);
                MARK(b_gu);
                BAR();
                STAMP(10 + 32 * layer);
                u32x4 tl = {0u, 0u, 0u, 0u};
                if (layer == 0) tl = ld16_sc1(ws, ta);
                const Frag80 b = rows80(b16, b8);
                const f32x4 g0 = mfma32(fg.a0, b.a0, z4), u0 = mfma32(fu.a0, b.a0, z4), g1 = mfma32(fg.a1, b.a1, g0), g2 = mfma16(fg.a2, b.a2, z4), u1 = mfma32(fu.a1, b.a1, u0), u2 = mfma16(fu.a2, b.a2, z4);
                __builtin_amdgcn_sched_barrier(0);
                const f32x4 cg = g1 + g2, cu = u1 + u2;
                st16m2<0>(ws, sg, asu(cg), su, asu(cu), mk);
                STAMP(11 + 32 * layer);
                if (layer == 0) {
                    WAIT1(tl, 2);
#pragma unroll 1
                    for (unsigned spins = 0; spins < kSpinCap && !all_set(min(min(tl[0], tl[1]), min(tl[2], tl[3]))); ++spins) {
                        tl = ld16_sc1(ws, ta);
                        WAIT1(tl, 0);
                    }
                    const unsigned tn = now();
                    unsigned d = min(min(min(tn - tl[0], tn - tl[1]), min(tn - tl[2], tn - tl[3])), 800u);
                    d = min(d, dppu<0xB1>(d)); d = min(d, dppu<0x4E>(d)); d = min(d, dppu<0x124>(d)); d = min(d, dppu<0x128>(d));
                    unsigned tr = tn - min(min((unsigned)__builtin_amdgcn_readlane(d, 0), (unsigned)__builtin_amdgcn_readlane(d, 16)), min((unsigned)__builtin_amdgcn_readlane(d, 32), (unsigned)__builtin_amdgcn_readlane(d, 48)));
                    lds_set(lb + L_CNT + 56, tr | 1u);
                                        until(__builtin_amdgcn_readfirstlane(tr) + X4A);
                } else sched(X4, 0);
                STAMP(16 + 32 * layer);
                float hv; f32x2 st;
                u32x4 a0, a1, a2, a3, ac, as;
                auto load = [&]() __attribute__((always_inline)) {
                    as = ld16_sc1(ws, os);
                    if (layer == 0) ac = ld16_sc1(ws, oc);
                    a0 = ld16_sc1<0>(ws, o); a1 = ld16_sc1<1024>(ws, o); a2 = ld16_sc1<2048>(ws, o); a3 = ld16_sc1<3072>(ws, o);
                };
                auto all_in = [&]() __attribute__((always_inline)) {
                    if (layer == 0) asm volatile("s_waitcnt vmcnt(0)" : "+v"(a0), "+v"(a1), "+v"(a2), "+v"(a3), "+v"(ac), "+v"(as) :: "memory");
                    else { asm volatile("s_waitcnt vmcnt(0)" : "+v"(a0), "+v"(a1), "+v"(a2), "+v"(a3), "+v"(as) :: "memory"); ac = as; }
                };
                auto compute = [&]() __attribute__((always_inline)) {
                    float ss, sm;
                    WAIT1(as, layer ? 4 : 5);
                    asm volatile("v_add_f32_dpp %0, %2, %2 row_ror:8 row_mask:0xf bank_mask:0xf\n\tv_add_f32_dpp %1, %3, %3 row_ror:8 row_mask:0xf bank_mask:0xf\n\ts_nop 0\n\t"
                                 "v_add_f32_dpp %0, %0, %0 row_ror:4 row_mask:0xf bank_mask:0xf\n\tv_add_f32_dpp %1, %1, %1 row_ror:4 row_mask:0xf bank_mask:0xf\n\ts_nop 0"
                                 : "=&v"(ss), "=&v"(sm) : "v"(as[0]), "v"(as[1]));
                    ss = x16_add(ss); sm = x16_add(sm);
                    float mean = sm * (1.f / DM), var = fmax2(__builtin_fmaf(-mean, mean, ss * (1.f / DM)), 0.f);
                    PIN2(mean, var);
                    if (layer == 0) {
                        WAIT1(ac, 4);
                        cs = sw16(rs32(asf(ac)[0], asf(ac)[2]), rs32(asf(ac)[1], asf(ac)[3]));
                        cs[0] += dpp<0xB1>(cs[0]); cs[1] += dpp<0xB1>(cs[1]); cs[0] += dpp<0x4E>(cs[0]); cs[1] += dpp<0x4E>(cs[1]);
                        PIN1(cs);
                    }
                    all_in();
                    st = (f32x2){bfr(mean), frcp(bfr(__builtin_amdgcn_sqrtf(bfr(bfr(var) + 1e-5f))))};
                    const f32x4 t = (asf(a0) + asf(a1)) + (asf(a2) + asf(a3));
                    const f32x2 gu = sw16(rs32(t[0], t[2]), rs32(t[1], t[3]));
                    const f32x2 gn = (gu - st[0] * cs) * st[1];
                    const float gv = bfr(gn[0]);
                    hv = bfr(gv * frcp(1.f + ex2(gv * -LOG2E))) * bfr(gn[1]);
                    *(LAS u16*)(size_t)ha = (u16)pk2(hv, hv);
                    __builtin_amdgcn_sched_barrier(0);
                };
                RETRY(load(), (all_in(), max(max(max(um4(a0), um4(a1)), max(um4(a2), um4(a3))), max(um4(ac), um4(as)))), compute(), any_nan(hv, hv), layer ? X4 : X4A);
                BAR();
                MARK(e_gu);
                STAMP(12 + 32 * layer);
                if (layer == 0) {
                    *reinterpret_cast<f32x2*>(smem + L_CS2 + wave * 512 + lane * 8) = cs;
                }
            }
            if (layer == 0) {
                if (wave == 1) {
                    const unsigned o = WS_CS + (unsigned)(gi * 6144 + 3072 + (lane < 48 ? lane : 0) * 16);
                    u32x4 a[8];
                    GATHER8(a, o, 768);
                    WAIT8(a, 0);
                    if (lane < 48) *reinterpret_cast<f32x4*>(smem + L_CS + lane * 16) = SUM8(a);
                }
                Frag80 f[4];
#pragma unroll
                for (int i = 0; i < 4; ++i) { f[i] = frag80(lb + L_WQ + (unsigned)(128 * wave + 32 * i + 8 * p4), 416, r0s, r1s, r2s); PIN3(f[i].a0, f[i].a1, f[i].a2); }
                unsigned b16 = lb + L_XS + (unsigned)(q4 * 160 + kq * 16), b8 = lb + L_XS + (unsigned)(q4 * 160 + 128 + kq * 8);
                unsigned so = WS_H1 + rS + (unsigned)((gi * 4 + q4) * 16384 + gj * 896 + 256 * wave + kq * 64 + p4 * 16);
                unsigned long long mk = 0x1111111111111111ull;
                PIN3(b16, b8, so); PINS(mk);
                GUARD(3800);
                MARK(b_q);
                BAR();
                STAMP(33);
                const Frag80 b = rows80(b16, b8);
                st16_sc1(ws, so, tiles4(f, b, mk));
                STAMP(34);
                qk_addr(true); att_addr();
                cma = lb + L_M; cxa = lb + L_M + (unsigned)(wave == 0 ? (lane >> 5) * 16 : (lane >> 4) * 8);
                cra = lb + L_OP + (unsigned)(wave == 0 ? (lane >> 5) * 4096 + (lane & 31) * 16 : 512 + (lane >> 4) * 2048 + (lane & 15) * 16);
                if (wave == 2) ml_addr(lane, cxa, cra);
                cso = WS_OWN + rS + (unsigned)(gi * 16384 + 4096 + gj * 512) + (unsigned)(wave == 0 ? lane * 4 : wave == 1 ? 256 + (lane >> 5) * 64 + (lane & 15) * 4 : 384);
                PIN2(cma, cxa); PIN2(cra, cso);
                asm volatile("" : "=v"(omr), "=v"(owa), "=v"(owb));
            }
        } else {
            const int t = wave - 3;
            StatAddr sa;
            u32x4 sel;
            unsigned long long hi = 0xff00ff00ff00ff00ull;
            auto gather32 = [&](unsigned o, unsigned X, auto fin) __attribute__((always_inline)) {
                u32x4 a0 = ld16_sc1<-2048>(ws, o), a1 = ld16_sc1<-1024>(ws, o), a2 = ld16_sc1<0>(ws, o), a3 = ld16_sc1<1024>(ws, o);
                float v;
                auto sum = [&]() __attribute__((always_inline)) {
                    asm volatile("s_waitcnt vmcnt(3)\n\tv_mfma_f32_16x16x32_bf16 v[40:43], %1, %5, 0\n\ts_waitcnt vmcnt(2)\n\tv_mfma_f32_16x16x32_bf16 v[40:43], %2, %5, v[40:43]\n\t"
                                 "s_waitcnt vmcnt(1)\n\tv_mfma_f32_16x16x32_bf16 v[40:43], %3, %5, v[40:43]\n\ts_waitcnt vmcnt(0)\n\tv_mfma_f32_16x16x32_bf16 v[40:43], %4, %5, v[40:43]\n\t"
                                 "s_nop 7\n\tv_cndmask_b32 v40, v40, v41, %6\n\tv_cndmask_b32 v42, v42, v43, %6\n\ts_nop 1\n\tv_permlane32_swap_b32 v40, v42\n\tv_add_f32 v40, v40, v42\n\t"
                                 "v_cvt_pk_bf16_f32 %0, 0, v40"
                                 : "=&v"(v), "+v"(a0), "+v"(a1), "+v"(a2), "+v"(a3) : "v"(sel), "s"(hi) : "memory", "v40", "v41", "v42", "v43");
                    fin(v);
                    __builtin_amdgcn_sched_barrier(0);
                };
                sum();
                if (__builtin_expect(any_nan(v, v) != 0ull, 0)) {
#pragma unroll 1
                    for (unsigned spins = 0; spins < kSpinCap; ++spins) {
                        slip(X);
                        again16<-2048>(a0, ws, o); again16<-1024>(a1, ws, o); again16<0>(a2, ws, o); again16<1024>(a3, ws, o);
                        asm volatile("s_waitcnt vmcnt(0)" : "+v"(a0), "+v"(a1), "+v"(a2), "+v"(a3) :: "memory");
                        if (!any_empty(max(max(um4(a0), um4(a1)), max(um4(a2), um4(a3))))) break;
                    }
                    sum();
                }
            };
            auto pack4 = [&](float c0, float c1, float c2, float c3) __attribute__((always_inline)) { const unsigned w0 = min(pk2(c0, c1), 0xfffffffeu), w1 = min(pk2(c2, c3), 0xfffffffeu); return (u32x2){w0, w1}; };
            unsigned xw, qo, qw, qc;
            __builtin_amdgcn_s_setprio(0);
            {
                STAMP(6 + 32 * layer);
                bf16x8 af[6];
                unsigned long long mk = 0x00ff00ff00ff00ffull;
                if (layer == 0) {
                    const int lane = lane_id();
                    xe = lo16(*reinterpret_cast<const u16*>(smem + L_X0 + ((lane >> 2) & 3) * 1280 + gj * 160 + t * 32 + (lane >> 4) * 8 + (lane & 3) * 2));
                    op_addr(0);
                    GUARD(1500);
                    MARK(b_op);
                    if (nj | (ord == EMODE)) { VMW(0); lds_add(44, nj); }
#pragma unroll 1
                    for (unsigned spins = 0; spins < (kSpinCap << 4); ++spins) if (lds_word(lb + L_CNT + 44) >= 5u) break;
                    STAMP(8);
                } else MARK(b_op);
                const unsigned lo = cma, lw = cxa, so = cso, br = cra, mr = omr, h3 = tile35(gj, 10 * layer + t);
                unsigned go, xs;
#pragma unroll
                for (int s = 0; s < 6; ++s) af[s] = cat8(tr64o(owa, s * 32 * 160), tr64o(owb, s * 32 * 160));
                PINS(mk); PINS(hi);
                if (layer != 0 && t < 4) {
                    sched(X2, 0);
                    STAMP(49);
                    u32x4 ov = ld16_sc1(ws, lo);
                    WAIT1(ov, 0);
                    *(LAS u32x4*)(size_t)lw = ov;
                    if (__builtin_expect(any_empty(um4(ov)), 0)) {
#pragma unroll 1
                        for (unsigned spins = 0; spins < kSpinCap; ++spins) {
                            slip(X2);
                            ov = ld16_sc1(ws, lo);
                            WAIT1(ov, 0);
                            *(LAS u32x4*)(size_t)lw = ov;
                            if (!any_empty(um4(ov))) break;
                        }
                    }
                }
                {
                    bf16x8 b0, b1, b2, b3, b4, b5;
#define QP " row_half_mirror row_mask:0xf bank_mask:0xf\n\t"
                    asm volatile("s_waitcnt lgkmcnt(0)\n\ts_barrier\n\t"
                                 "ds_read_b128 %0, %12\n\tds_read_b64 v[36:37], %13\n\tds_read_b128 %1, %12 offset:64\n\tds_read_b128 %2, %12 offset:128\n\tds_read_b128 %3, %12 offset:192\n\t"
                                 "ds_read_b128 %4, %12 offset:256\n\tds_read_b128 %5, %12 offset:320\n\t"
                                 "s_waitcnt lgkmcnt(5)\n\tv_mfma_f32_16x16x32_bf16 v[32:35], %6, %0, 0\n\tv_max_f32_dpp v38, v36, v36" QP "v_sub_f32 v38, v36, v38\n\t"
                                 "s_waitcnt lgkmcnt(4)\n\tv_mfma_f32_16x16x32_bf16 v[32:35], %7, %1, v[32:35]\n\tv_exp_f32 v38, v38\n\t"
                                 "s_waitcnt lgkmcnt(3)\n\tv_mfma_f32_16x16x32_bf16 v[32:35], %8, %2, v[32:35]\n\tv_mul_f32 v37, v37, v38\n\t"
                                 "s_waitcnt lgkmcnt(2)\n\tv_mfma_f32_16x16x32_bf16 v[32:35], %9, %3, v[32:35]\n\tv_add_f32_dpp v39, v37, v37" QP
                                 "s_waitcnt lgkmcnt(1)\n\tv_mfma_f32_16x16x32_bf16 v[32:35], %10, %4, v[32:35]\n\tv_rcp_f32 v39, v39\n\t"
                                 "s_waitcnt lgkmcnt(0)\n\tv_mfma_f32_16x16x32_bf16 v[32:35], %11, %5, v[32:35]\n\tv_mul_f32 v38, v38, v39\n\ts_nop 6\n\t"
                                 "v_pk_mul_f32 v[32:33], v[32:33], v[38:39] op_sel_hi:[1,0]\n\tv_pk_mul_f32 v[34:35], v[34:35], v[38:39] op_sel_hi:[1,0]\n\ts_nop 0\n\t"
                                 "v_add_f32_dpp v32, v32, v32" QP "v_add_f32_dpp v33, v33, v33" QP "v_add_f32_dpp v34, v34, v34" QP "v_add_f32_dpp v35, v35, v35" QP
                                 "v_cvt_pk_bf16_f32 v32, v32, v33\n\tv_cvt_pk_bf16_f32 v33, v34, v35\n\ts_mov_b64 exec, %16\n\tv_min_u32 v32, -2, v32\n\tv_min_u32 v33, -2, v33\n\t"
                                 "global_store_dwordx2 %14, v[32:33], %15 sc1\n\ts_mov_b64 exec, -1"
                                 : "=&v"(b0), "=&v"(b1), "=&v"(b2), "=&v"(b3), "=&v"(b4), "=&v"(b5)
                                 : "v"(af[0]), "v"(af[1]), "v"(af[2]), "v"(af[3]), "v"(af[4]), "v"(af[5]), "v"(br), "v"(mr), "v"(so), "s"(ws), "s"(mk)
                                 : "memory", "v32", "v33", "v34", "v35", "v36", "v37", "v38", "v39");
#undef QP
                }
                if (layer == 0 && wave == 7) st4_sc1(ws, WS_MB + (unsigned)(gi * 1024 + 64 + gj * 4), now() | 1u);
                const unsigned tp = layer ? 0u : now();
                if (layer == 0) sides();
                STAMP(7 + 32 * layer);
                {
                    const int ln = lane_id();
#pragma unroll
                    for (int k = 0; k < 4; ++k) sel[k] = ((ln >> 1) & 3) == k ? (ln & 1) ? 0x3f800000u : 0x3f80u : 0u;
                    go = h3 + nS + (unsigned)(2048 + ln * 16); xs = lb + L_XS + (unsigned)(((ln >> 2) & 3) * 160 + t * 32 + (ln >> 4) * 8 + (ln & 3) * 2);
                    PIN3(sel, go, xs);
                    sa = stats_addr(ln, h4 + (unsigned)(H4T + gj * 128), 16);
                }
                if (layer != 0) sched(X3, 0);
                else until(tp + W3);
                STAMP(18 + 32 * layer);
                if (layer != 0) gather32(go, X3, [&](float s) { xw = pk2(xe + s, 0.f); *(LAS u16*)(size_t)xs = (u16)xw; });
                else {
                    unsigned long long m0, m1, m2, m3; unsigned cnt = kSpinCap;
#define CHK(r, m) "v_max3_u32 v44, v" #r ", v[" #r "+1], v[" #r "+2]\n\tv_max_u32 v44, v44, v[" #r "+3]\n\tv_cmp_eq_u32 %" #m ", -1, v44\n\t"
                    asm volatile("s_mov_b64 %1, -1\n\ts_mov_b64 %2, -1\n\ts_mov_b64 %3, -1\n\ts_mov_b64 %4, -1\n"
                                 "1:\n\ts_mov_b64 exec, %1\n\tglobal_load_dwordx4 v[0:3], %6, %7 offset:-2048 sc1\n\ts_mov_b64 exec, %2\n\tglobal_load_dwordx4 v[4:7], %6, %7 offset:-1024 sc1\n\t"
                                 "s_mov_b64 exec, %3\n\tglobal_load_dwordx4 v[8:11], %6, %7 sc1\n\ts_mov_b64 exec, %4\n\tglobal_load_dwordx4 v[12:15], %6, %7 offset:1024 sc1\n\ts_mov_b64 exec, -1\n\t"
                                 "s_sub_u32 %5, %5, 1\n\ts_cselect_b64 vcc, 0, -1\n\ts_waitcnt vmcnt(0)\n\t"
                                 "v_mfma_f32_16x16x32_bf16 v[40:43], v[0:3], %8, 0\n\t" CHK(0, 1) "v_mfma_f32_16x16x32_bf16 v[40:43], v[4:7], %8, v[40:43]\n\t" CHK(4, 2)
                                 "v_mfma_f32_16x16x32_bf16 v[40:43], v[8:11], %8, v[40:43]\n\t" CHK(8, 3) "v_mfma_f32_16x16x32_bf16 v[40:43], v[12:15], %8, v[40:43]\n\t" CHK(12, 4)
                                 "s_or_b64 exec, %1, %2\n\ts_or_b64 exec, exec, %3\n\ts_or_b64 exec, exec, %4\n\ts_and_b64 vcc, exec, vcc\n\ts_cbranch_scc1 1b\n\t"
                                 "s_mov_b64 exec, -1\n\tv_cndmask_b32 v40, v40, v41, %9\n\tv_cndmask_b32 v42, v42, v43, %9\n\ts_nop 1\n\tv_permlane32_swap_b32 v40, v42\n\tv_add_f32 v40, v40, v42\n\t"
                                 "v_cvt_pk_bf16_f32 v40, 0, v40\n\tv_add_f32 v40, %10, v40\n\tv_cvt_pk_bf16_f32 %0, v40, 0\n\tds_write_b16 %11, %0"
                                 : "=&v"(xw), "=&s"(m0), "=&s"(m1), "=&s"(m2), "=&s"(m3), "+s"(cnt) : "v"(go), "s"(ws), "v"(sel), "s"(hi), "v"(xe), "v"(xs)
                                 : "memory", "vcc", "scc", "v0", "v1", "v2", "v3", "v4", "v5", "v6", "v7", "v8", "v9", "v10", "v11", "v12", "v13", "v14", "v15", "v40", "v41", "v42", "v43", "v44");
#undef CHK
                }
                xe = lo16(xw);
                STAMP(9 + 32 * layer);
            }
            BAR();
            if (wave == 3) stats(sa);
            MARK(e_op);
            STAMP(11 + 32 * layer);
            {
                LANE_VARS();
                const unsigned im = lb + L_WD + (unsigned)(32 * t + 8 * p4);
                bf16x8 wd0 = cat8(tr64(im + r0s * 160), tr64(im + r1s * 160)); bf16x4 wd1 = tr64(im + (32 + r2s) * 160);
                const unsigned h5 = tile35(gj, 10 * layer + 5 + t);
                unsigned h16 = lb + L_H + (unsigned)(p4 * 96 + kq * 16), h8 = lb + L_H + (unsigned)(p4 * 96 + 64 + kq * 8);
                unsigned so = h5 + (unsigned)((lane & 4) * 2048 + gi * 128 + (kq & 1) * 64 + (kq >> 1) * 32 + p4 * 8);
                unsigned long long mk = 0x00ff00ff00ff00ffull;
                unsigned go = h5 + nS + (unsigned)(2048 + lane * 16), xs = (layer ? (unsigned)(q4 * 1280 + gj * 160) : lb + L_XS + (unsigned)(q4 * 160)) + (unsigned)(t * 32 + kq * 8 + p4 * 2);
                unsigned sta = lb + L_ST + (unsigned)(q4 * 16);
                if (layer == 0 && wave == 4) {
                    asm volatile("" : "=v"(sa.a16), "=v"(sa.b16), "=v"(sa.a8), "=v"(sa.b8), "=v"(sa.dst), "=v"(qo), "=v"(qw), "=v"(qc), "=v"(cma), "=v"(cxa), "=v"(cra), "=v"(cso), "=v"(omr), "=v"(owa), "=v"(owb));
                    asm volatile("" : "=v"(qa), "=v"(pw), "=v"(ma), "=v"(idx), "=v"(qm), "=v"(pr), "=v"(oa), "=v"(oc));
                } else if (layer == 0) {
                    sa = stats_addr(lane, WS_H1 + rS + (unsigned)(gi * 65536 + gj * 896 + 768), 16384);
                    op_addr(1); qk_addr(false); att_addr();
                    const int l5 = lane & 31, c = l5 < 24 ? 24 * (wave - 6) + l5 : 48;
                    qo = WS_H1 + rS + (unsigned)((gi * 4 + (gj >> 1)) * 16384 + 3584 + (lane >> 5) * 896 + c * 16);
                    qw = lb + L_Q + (unsigned)(l5 < 24 ? (c >> 4) * 128 + (c & 3) * 32 + (c & 12) + (lane >> 5) * 16 : lane < 26 ? 384 + (lane & 1) * 16 : 448);
                    qc = lb + L_CS + (unsigned)(c * 16 + (lane >> 5) * 8);
                    PIN3(qo, qw, qc);
                }
                PIN2(wd0, wd1); PIN3(h16, h8, so); PIN3(go, xs, sta); PINS(mk);
                GUARD(2000);
                MARK(b_dn);
                if (wave == 4) {
                    unsigned os = h4 + (unsigned)(H4T + (p4 + 4 * (kq & 1)) * 128 + q4 * 16);
                    float sw = *reinterpret_cast<const float*>(smem + L_CNT + 12);
                    PIN2(os, sw);
                    if (layer == 0) {
#pragma unroll 1
                        for (unsigned spins = 0; spins < (kSpinCap << 4); ++spins) if (lds_word(lb + L_CNT + 56)) break;
                    }
                    sched(layer ? X4 : X4A, 0);
                    u32x4 as;
#pragma unroll 1
                    for (unsigned spins = 0; spins < kSpinCap; ++spins) { as = ld16_sc1(ws, os); WAIT1(as, 0); if (!any_empty(um4(as))) break; }
                    float ss = __uint_as_float(as[0]), sm = __uint_as_float(as[1]), rd = __uint_as_float(as[2]);
                    ss += dpp<0x4E>(ss); sm += dpp<0x4E>(sm); rd += dpp<0x4E>(rd);
                    ss += dpp<0xB1>(ss); sm += dpp<0xB1>(sm); rd += dpp<0xB1>(rd);
                    ss = x16_add(ss); sm = x16_add(sm); rd = x16_add(rd);
                    if (LANES(0x1111ull)) *reinterpret_cast<f32x4*>(smem + L_ST + q4 * 16) = (f32x4){ss, sm, rd, sw};
                }
                if (layer == 0 && wave > 4) {
#pragma unroll 1
                    for (unsigned spins = 0; spins < (kSpinCap << 4); ++spins) { if (lds_word(lb + L_CNT + 56)) break; __builtin_amdgcn_s_sleep(1); }
                    sched(X4A + 5, 0);
                    if (wave == 5) empty4(1, rS, lane);
                    else st16m(ws, lane < 40 ? tile35(gj, 15 + (lane >> 3)) + (unsigned)((wave - 6) * 8192 + gi * 128 + (lane & 7) * 16)
                                             : WS_OWN + rS + (unsigned)(gi * 16384 + 4096 + gj * 512 + (wave - 6) * 256 + (lane - 40) * 16), (u32x4){~0u, ~0u, ~0u, ~0u}, 0xffffffffffffffull);
                }
                BAR();
                STAMP(12 + 32 * layer);
                const bf16x8 hb0 = lds16(h16); const bf16x4 hb1 = lds8(h8);
                __builtin_amdgcn_sched_barrier(0);
                const f32x4 c = mfma32(wd0, hb0, z4) + mfma16(wd1, hb1, z4);
                st8m(ws, so, pack4(c[0], c[1], c[2], c[3]), mk);
                STAMP(13 + 33 * layer);
                if (layer == 0 || gi == 4 * t + (gj & 3)) {
                    const f32x4 sr = *(const LAS f32x4*)(size_t)sta;
                    const f32x2 st = ln_stats(sr[0], sr[1]);
                    float rs = bfr(frcp(1.f + ex2(bfr((sr[2] - st[0] * sr[3]) * st[1]) * -LOG2E))), n1 = bfr(bfr(xe - st[0]) * st[1]);
                    PIN2(rs, n1);
                    if (layer == 0) {
                        sched(X5A, 0);
                        STAMP(15);
                        gather32(go, X5A, [&](float y) { xw = pk2(xe + bfr(n1 + bfr(y * rs)), 0.f); *(LAS u16*)(size_t)xs = (u16)xw; });
                        xe = lo16(xw);
                        STAMP(14);
                    } else {
                        unsigned long long mo = 0x1111111111111111ull; PINS(mo);
                        sched(X5B, 0);
                        STAMP(47);
                        gather32(go, X5B, [&](float y) { xw = pk2(xe + bfr(n1 + bfr(y * rs)), 0.f); });
                        { const unsigned w = xw | (dppu<0xF5>(xw) << 16);
                          const u32x2 w2 = {w, dppu<0xAA>(w)};
                          asm volatile("s_mov_b64 exec, %3\n\tglobal_store_dwordx2 %0, %1, %2 sc0 sc1\n\ts_mov_b64 exec, -1" :: "v"(xs), "v"(w2), "s"(out), "s"(mo) : "memory"); }
                        MARK(e_dn);
                        STAMP(45);
                    }
                }
            }
            if (layer == 0) {
                BAR();
                if (wave == 3) { stats(sa); STAMP(34); }
                MARK(e_dn);
                if (wave == 3) __builtin_amdgcn_s_setprio(2);
                if (__builtin_expect(wave == 4, 0)) { op_addr(1); qk_addr(false); att_addr(); }
                if (wave >= 6) {
                    const unsigned o = qo;
                    f32x2 cs = -*(const LAS f32x2*)(size_t)qc;
                    float rn = 1.f / DM;
                    unsigned long long m0 = 1ull << 24, m1 = 1ull << 25;
                    PIN2(cs, rn); PINS(m0); PINS(m1);
                    MARK(b_qg);
                    sched(X1, 0);
                    STAMP(51);
                    u32x4 a0, a1, a2, a3; f32x2 u;
                    RETRY({ a0 = ld16_sc1<-3584>(ws, o); a1 = ld16_sc1<-1792>(ws, o); a2 = ld16_sc1<0>(ws, o); a3 = ld16_sc1<1792>(ws, o); },
                          ({ asm volatile("s_waitcnt vmcnt(0)" : "+v"(a0), "+v"(a1), "+v"(a2), "+v"(a3) :: "memory"); max(max(um4(a0), um4(a1)), max(um4(a2), um4(a3))); }),
                          { unsigned sm;
                            asm volatile("s_waitcnt vmcnt(2)\n\tv_pk_add_f32 v[40:41], %2, %4\n\tv_pk_add_f32 v[42:43], %3, %5\n\t"
                                         "s_waitcnt vmcnt(1)\n\tv_pk_add_f32 v[40:41], v[40:41], %6\n\tv_pk_add_f32 v[42:43], v[42:43], %7\n\t"
                                         "s_waitcnt vmcnt(0)\n\tv_pk_add_f32 v[40:41], v[40:41], %8\n\tv_pk_add_f32 v[42:43], v[42:43], %9\n\ts_nop 1\n\t"
                                         "v_permlane32_swap_b32 v40, v42\n\tv_permlane32_swap_b32 v41, v43\n\tv_pk_add_f32 v[40:41], v[40:41], v[42:43]\n\ts_nop 0\n\t"
                                         "v_readlane_b32 %1, v41, 24\n\ts_nop 1\n\tv_mul_f32 v42, %1, %11\n\tv_cvt_pk_bf16_f32 v42, 0, v42\n\tv_pk_fma_f32 %0, %10, v[42:43], v[40:41] op_sel_hi:[1,0,1]"
                                         : "=&v"(u), "=&s"(sm) : "v"(LO2(a0)), "v"(HI2(a0)), "v"(LO2(a1)), "v"(HI2(a1)), "v"(LO2(a2)), "v"(HI2(a2)), "v"(LO2(a3)), "v"(HI2(a3)), "v"(cs), "v"(rn)
                                         : "memory", "v40", "v41", "v42", "v43");
                            unsigned w = pk2v(u);
                            if (LANES(m0)) w = __float_as_uint(u[0]);
                            if (LANES(m1)) w = __float_as_uint(u[1]);
                            *(LAS unsigned*)(size_t)qw = w; __builtin_amdgcn_sched_barrier(0); }, any_nan(u[0], u[1]), X1);
                    MARK(e_qg);
                }
            }
        }
    };
    PINS(ck1); PINS(ck2); PINS(csc);
    chain(IC<0>{});
    STAMP(35);
    att1();
    chain(IC<1>{});
}

__device__ __attribute__((used, noinline)) void mega_code_end() { asm volatile(".Lmega_code_end:"); }

extern "C" void kernel_launch(void* const* d_in, const int* in_sizes, int n_in, void* d_out, int out_size, void* d_ws,
                              size_t ws_size, hipStream_t stream) {
    const u16* x0 = (const u16*)d_in[0];
    const u16* kc = (const u16*)d_in[1];
    const u16* vc = (const u16*)d_in[2];
    const u16* wq = (const u16*)d_in[3];
    const u16* wo = (const u16*)d_in[4];
    const u16* wr = (const u16*)d_in[6];
    const u16* wg = (const u16*)d_in[7];
    const u16* wu = (const u16*)d_in[8];
    const u16* wd = (const u16*)d_in[9];
    (void)hipFuncSetAttribute((const void*)mega_kernel, hipFuncAttributeMaxDynamicSharedMemorySize, L_END);
    mega_kernel<<<256, 512, L_END, stream>>>(x0, kc, vc, wq, wo, wr, wg, wu, wd, (u16*)d_out, (unsigned char*)d_ws
                                             );
}
```

```cpp
#include <hip/hip_runtime.h>
#include <hip/hip_bf16.h>
#include <stdint.h>

typedef unsigned short u16;
typedef unsigned u32x4 __attribute__((ext_vector_type(4)));
typedef unsigned u32x2 __attribute__((ext_vector_type(2)));
typedef unsigned long long u64x8 __attribute__((ext_vector_type(8)));
typedef unsigned long long u64x2 __attribute__((ext_vector_type(2)));
typedef float f32x4 __attribute__((ext_vector_type(4)));
typedef short s16x4 __attribute__((ext_vector_type(4)));
typedef __attribute__((ext_vector_type(4))) __bf16 bf16x4;
typedef __attribute__((ext_vector_type(8))) __bf16 bf16x8;
#define LAS __attribute__((address_space(3)))

constexpr int DM = 640, HQ = 32, SK = 4096, DH = 192, WIN = 1024, FF = 1536;
constexpr int QD = HQ * DH;
constexpr float SCALE = 0.07216878364870322f, LOG2E = 1.44269504088896341f;
constexpr unsigned kSpinCap = 1u << 16;
#define GJ(g) (((g) >> 3) & 7)
#define GI(g) (((g) & 7) * 4 + ((g) >> 6))

#define D4A 118
#define D5A 96
#define D1 90
#define D2 142
#define D3 67
#define D4 78
#define D5B 79
#define PACE 59
#define PINIT 100
#define PMIN 50
#define DM_ 0
constexpr unsigned X4A = D4A + DM_, X5A = X4A + D5A + DM_, X1 = X5A + D1 + DM_, X2 = X1 + D2 + DM_, X3 = X2 + D3 + DM_, X4 = X3 + D4 + DM_, X5B = X4 + D5B + DM_;
#define WOC 2
#define WOFF 6
#define WEARLY 1
#define DG2 12
#define XCOST 10
#define GUC 3
#define XS 0
#define LEAD 12
#define TOFF 20
#define UG 360
#define SLOW 0
#define TLAND 125
#define ROWM 1
#define W3 15
#define ROT gj
#define GROT 1
#define CMODE 1
#define ADOPT 1
constexpr unsigned NUNIT = 384 + XCOST + XS + GUC * (9 + 2 * WEARLY), PBURST = 30;
#define GSTEPS GA = 1, GB = 6, GC = 7, HD = 1
#define OML 2
#define EMODE (OML + 1)
constexpr int GP = 0, GSTEPS;

constexpr unsigned WS_H1 = 0;
constexpr unsigned WS_H4 = 2u << 20;
constexpr unsigned H4C = 20480, H4T = H4C + 3072;
constexpr unsigned WS_H35 = 4u << 20;
constexpr unsigned WS_OWN = WS_H35 + 80 * 16384;
constexpr unsigned WS_CS = WS_OWN + 32 * 16384;
constexpr unsigned WS_MB = WS_CS + 256 * 768;
constexpr unsigned WS_R = WS_MB + 256 * 128;
constexpr unsigned WS_END = WS_R + 128;
constexpr unsigned WS_Q0 = 8u << 20;
static_assert(WS_END <= (6u << 20), "ws map");
__device__ __forceinline__ unsigned tile35(int j, int q) { return WS_H35 + (unsigned)((j * 10 + (q >> 1)) * 16384 + (q & 1) * 4096); }
__device__ __forceinline__ unsigned h4s(int s) { return (unsigned)((s >> 1) * 16384 + (s & 1) * 4096); }

constexpr int L_WQ = 0;
constexpr int L_WO = 33792;
constexpr int L_GU = L_WO + 30720;
constexpr int L_WD = L_GU + 16384;
constexpr int L_X0 = L_WD + 8192;
constexpr int L_WR = L_X0 + 5120;
constexpr int L_XS = L_WR + 2048;
constexpr int L_Q = L_XS + 1024;
constexpr int L_OP = L_Q + 512;
constexpr int L_M = L_OP + 8192;
constexpr int L_H = L_M + 64;
constexpr int L_ST = L_H + 384;
constexpr int L_CS = L_ST + 64;
constexpr int L_CS2 = L_CS + 1024;
constexpr int L_CNT = L_CS2 + 1536;
constexpr int L_P = L_CNT + 96;
constexpr int L_O2 = L_P + 72 * 192;
constexpr int L_DBG = L_O2 + 3328;
constexpr int L_END = L_DBG + 2048;
static_assert(L_END <= 163840 && L_END > 81920, "LDS");

typedef __attribute__((ext_vector_type(2))) __bf16 bf16x2;
typedef float f32x2 __attribute__((ext_vector_type(2)));
__device__ __forceinline__ unsigned pk2(float a, float b) { return __builtin_bit_cast(unsigned, __builtin_convertvector((f32x2){a, b}, bf16x2)); }
__device__ __forceinline__ unsigned fexp(float x) { const unsigned e = (__float_as_uint(x) >> 23) & 255u; return e == 255u ? 0u : e; }
__device__ __forceinline__ u32x2 enc21(float a, float b, float c) {
    const float lim = 1048575.f;
    const unsigned i = (unsigned)(int)__builtin_rintf(__builtin_amdgcn_fmed3f(a, -lim, lim)) & 0x1fffffu, j = (unsigned)(int)__builtin_rintf(__builtin_amdgcn_fmed3f(b, -lim, lim)) & 0x1fffffu;
    const unsigned k = (unsigned)(int)__builtin_rintf(__builtin_amdgcn_fmed3f(c, -lim, lim)) & 0x1fffffu;
    return (u32x2){i | j << 21, j >> 11 | k << 10 | (__builtin_fabsf(a) + __builtin_fabsf(b) + __builtin_fabsf(c) < 1e30f ? 0u : 0x80000000u)};
}
__device__ __forceinline__ float lo16(unsigned w) { return __uint_as_float(w << 16); }
__device__ __forceinline__ float hi16(unsigned w) { return __uint_as_float(w & 0xffff0000u); }
__device__ __forceinline__ float bfr(float f) { return __uint_as_float(pk2(0.f, f)); }
template <int CTRL> __device__ __forceinline__ float dpp(float x) {
    return __builtin_bit_cast(float, __builtin_amdgcn_mov_dpp(__builtin_bit_cast(int, x), CTRL, 0xf, 0xf, true));
}
template <int CTRL> __device__ __forceinline__ unsigned dppu(unsigned x) { return (unsigned)__builtin_amdgcn_mov_dpp((int)x, CTRL, 0xf, 0xf, true); }
__device__ __forceinline__ float row16_sum(float v) {
    v += dpp<0xB1>(v); v += dpp<0x4E>(v); v += dpp<0x141>(v); v += dpp<0x140>(v); return v;
}
__device__ __forceinline__ float fmax2(float a, float b) { return __builtin_elementwise_maximum(a, b); }
__device__ __forceinline__ float fmax3(float a, float b, float c) { return fmax2(fmax2(a, b), c); }
__device__ __forceinline__ float x16_add(float v) { auto s = __builtin_amdgcn_permlane16_swap(__float_as_uint(v), __float_as_uint(v), false, false); return __uint_as_float(s[0]) + __uint_as_float(s[1]); }
__device__ __forceinline__ float x32_add(float v) { auto s = __builtin_amdgcn_permlane32_swap(__float_as_uint(v), __float_as_uint(v), false, false); return __uint_as_float(s[0]) + __uint_as_float(s[1]); }
__device__ __forceinline__ float wave_sum(float v) { return x32_add(x16_add(row16_sum(v))); }
__device__ __forceinline__ float rs32(float a, float b) { auto s = __builtin_amdgcn_permlane32_swap(__float_as_uint(a), __float_as_uint(b), false, false); return __uint_as_float(s[0]) + __uint_as_float(s[1]); }
__device__ __forceinline__ f32x2 sw16(float a, float b) { auto s = __builtin_amdgcn_permlane16_swap(__float_as_uint(a), __float_as_uint(b), false, false); return (f32x2){__uint_as_float(s[0]), __uint_as_float(s[1])}; }

#define GUARD(n) asm volatile("s_branch 1f\n\t.p2align 12,," #n "\n1:")
#define MARK(n) do {} while (0)
#define BAR() asm volatile("s_waitcnt lgkmcnt(0)\n\ts_barrier" ::: "memory")
#define VMW(n) asm volatile("s_waitcnt vmcnt(%0)" :: "i"(n) : "memory")

template <int OFF = 0> __device__ __forceinline__ u32x4 ld16_sc1(const void* sbase, unsigned voff) {
    u32x4 r; asm volatile("global_load_dwordx4 %0, %1, %2 offset:%3 sc1" : "=v"(r) : "v"(voff), "s"(sbase), "i"(OFF) : "memory"); return r;
}
__device__ __forceinline__ unsigned ld4_sc1(const void* sbase, unsigned voff) {
    unsigned r; asm volatile("global_load_dword %0, %1, %2 sc1" : "=v"(r) : "v"(voff), "s"(sbase) : "memory"); return r;
}
template <int OFF = 0> __device__ __forceinline__ void st16_sc1(void* sbase, unsigned voff, u32x4 v) {
    asm volatile("global_store_dwordx4 %0, %1, %2 offset:%3 sc1\n\ts_nop 1" :: "v"(voff), "v"(v), "s"(sbase), "i"(OFF) : "memory");
}
__device__ __forceinline__ void st4_sc1(void* sbase, unsigned voff, unsigned v) {
    asm volatile("global_store_dword %0, %1, %2 sc1" :: "v"(voff), "v"(v), "s"(sbase) : "memory");
}
__device__ __forceinline__ void st8(void* sbase, unsigned voff, u32x2 v) {
    asm volatile("global_store_dwordx2 %0, %1, %2" :: "v"(voff), "v"(v), "s"(sbase) : "memory");
}
__device__ __forceinline__ void st2(void* sbase, unsigned voff, unsigned v) {
    asm volatile("global_store_short %0, %1, %2" :: "v"(voff), "v"(v), "s"(sbase) : "memory");
}
constexpr int RK = 64, RV = 160;
template <int R, int IMM> __device__ __forceinline__ void ld16_nt(const void* sbase, unsigned voff) {
    asm volatile("global_load_dwordx4 v[%c3:%c4], %0, %1 offset:%2 nt" :: "v"(voff), "s"(sbase), "i"(IMM), "i"(R), "i"(R + 3) : "memory");
}
template <int R, int IMM> __device__ __forceinline__ void ld8_nt(const void* sbase, unsigned voff) {
    asm volatile("global_load_dwordx2 v[%c3:%c4], %0, %1 offset:%2 nt" :: "v"(voff), "s"(sbase), "i"(IMM), "i"(R), "i"(R + 1) : "memory");
}
template <int R> __device__ __forceinline__ void smf(f32x4& d, u32x4 a, unsigned idx) {
    asm volatile("v_smfmac_f32_16x16x64_bf16 %0, %1, v[%c3:%c4], %2" : "+v"(d) : "v"(a), "v"(idx), "i"(R), "i"(R + 7));
}
template <int A, int B> __device__ __forceinline__ void zip_in() {
    unsigned t;
    asm volatile("v_perm_b32 %0, v%c2, v%c1, %3\n\tv_perm_b32 v%c2, v%c2, v%c1, %4\n\tv_mov_b32 v%c1, %0" : "=&v"(t) : "i"(A), "i"(B), "s"(0x05040100u), "s"(0x07060302u));
}
template <int A, int B> __device__ __forceinline__ void zip_out(unsigned& lo, unsigned& hi) {
    asm volatile("v_perm_b32 %0, v%c3, v%c2, %4\n\tv_perm_b32 %1, v%c3, v%c2, %5" : "=&v"(lo), "=&v"(hi) : "i"(A), "i"(B), "s"(0x05040100u), "s"(0x07060302u));
}
template <int A, int B, int C, int D> __device__ __forceinline__ void zip_x(unsigned lo, unsigned hi) {
    asm volatile("v_perm_b32 v%c4, v%c3, v%c2, %6\n\tv_perm_b32 v%c5, v%c3, v%c2, %7\n\tv_mov_b32 v%c2, %0\n\tv_mov_b32 v%c3, %1"
                 :: "v"(lo), "v"(hi), "i"(A), "i"(B), "i"(C), "i"(D), "s"(0x05040100u), "s"(0x07060302u));
}
__device__ __forceinline__ void dma16(const void* sbase, unsigned voff, unsigned lds_dst) {
    unsigned keep;
    asm volatile("s_mov_b32 %0, m0\n\ts_mov_b32 m0, %3\n\ts_nop 0\n\tglobal_load_lds_dwordx4 %1, %2 nt\n\ts_mov_b32 m0, %0"
                 : "=&s"(keep) : "v"(voff), "s"(sbase), "s"(lds_dst) : "memory");
}
__device__ __forceinline__ void dma16p(const void* sbase, unsigned voff, unsigned lds_dst) {
    unsigned keep;
    asm volatile("s_mov_b32 %0, m0\n\ts_mov_b32 m0, %3\n\ts_nop 0\n\tglobal_load_lds_dwordx4 %1, %2\n\ts_mov_b32 m0, %0"
                 : "=&s"(keep) : "v"(voff), "s"(sbase), "s"(lds_dst) : "memory");
}
__device__ __forceinline__ unsigned um4(u32x4 a) { return max(max(a[0], a[1]), max(a[2], a[3])); }
__device__ __forceinline__ bool all_set(unsigned t) { return __builtin_amdgcn_ballot_w64(t == 0u) == 0ull; }
__device__ __forceinline__ bool any_empty(unsigned mx) { return __builtin_amdgcn_ballot_w64(mx == ~0u) != 0ull; }
__device__ __forceinline__ unsigned long long any_nan(float a, float b) { return __builtin_amdgcn_ballot_w64(__builtin_isunordered(a, b)); }
__device__ __forceinline__ f32x4 asf(u32x4 a) { return __builtin_bit_cast(f32x4, a); }
__device__ __forceinline__ u32x4 asu(f32x4 a) { return __builtin_bit_cast(u32x4, a); }
#define LANES(m) __builtin_amdgcn_inverse_ballot_w64(m)

__device__ __forceinline__ bf16x4 tr64(unsigned lds_addr) { return __builtin_amdgcn_ds_read_tr16_b64_v4bf16((LAS bf16x4*)(size_t)lds_addr); }
__device__ __forceinline__ bf16x4 tr64o(unsigned lds_addr, int off) { return __builtin_amdgcn_ds_read_tr16_b64_v4bf16((LAS bf16x4*)((LAS char*)(size_t)lds_addr + off)); }
__device__ __forceinline__ bf16x8 cat8(bf16x4 a, bf16x4 b) { return __builtin_shufflevector(a, b, 0, 1, 2, 3, 4, 5, 6, 7); }
__device__ __forceinline__ bf16x8 lds16(unsigned a) { return *(const LAS bf16x8*)(size_t)a; }
__device__ __forceinline__ bf16x4 lds8(unsigned a) { return *(const LAS bf16x4*)(size_t)a; }
__device__ __forceinline__ f32x4 mfma32(bf16x8 a, bf16x8 b, f32x4 c) { return __builtin_amdgcn_mfma_f32_16x16x32_bf16(a, b, c, 0, 0, 0); }
__device__ __forceinline__ f32x4 mfma32u(u32x4 a, u32x4 b, f32x4 c) { return mfma32(__builtin_bit_cast(bf16x8, a), __builtin_bit_cast(bf16x8, b), c); }
__device__ __forceinline__ f32x4 mfma16(bf16x4 a, bf16x4 b, f32x4 c) {
    return __builtin_amdgcn_mfma_f32_16x16x16bf16_1k(__builtin_bit_cast(s16x4, a), __builtin_bit_cast(s16x4, b), c, 0, 0, 0);
}

#define STAMP(i) do {} while (0)

template <int OFF = 0> __device__ __forceinline__ void st16m(void* sbase, unsigned voff, u32x4 v, unsigned long long mask) {
    asm volatile("s_mov_b64 exec, %4\n\tglobal_store_dwordx4 %0, %1, %2 offset:%3 sc1\n\ts_mov_b64 exec, -1\n\ts_nop 0" :: "v"(voff), "v"(v), "s"(sbase), "i"(OFF), "s"(mask) : "memory");
}
template <int OFF> __device__ __forceinline__ void st16m2(void* sbase, unsigned voff, u32x4 v, unsigned voff2, u32x4 v2, unsigned long long mask) {
    asm volatile("s_mov_b64 exec, %6\n\tglobal_store_dwordx4 %0, %1, %2 sc1\n\tglobal_store_dwordx4 %4, %5, %2 offset:%3 sc1\n\ts_mov_b64 exec, -1\n\ts_nop 0"
                 :: "v"(voff), "v"(v), "s"(sbase), "i"(OFF), "v"(voff2), "v"(v2), "s"(mask) : "memory");
}
#define PINS(a) asm volatile("" : "+s"(a))
#define RETRY(LOAD, EMPTY, COMPUTE, NAN_, X) do { LOAD; COMPUTE; if (__builtin_expect((NAN_) != 0ull, 0)) { \
    _Pragma("unroll 1") for (unsigned spins_ = 0; spins_ < kSpinCap; ++spins_) { slip(X); LOAD; if (!any_empty(EMPTY)) break; } COMPUTE; } } while (0)
__device__ __forceinline__ unsigned now() { return (unsigned)__builtin_amdgcn_s_memrealtime(); }
__device__ __forceinline__ void until(unsigned t) {
    asm volatile("1:\n\ts_memrealtime vcc\n\ts_waitcnt lgkmcnt(0)\n\ts_sub_u32 vcc_lo, %0, vcc_lo\n\ts_cmp_lt_u32 vcc_lo, 0x400\n\ts_cbranch_scc1 1b" :: "s"(t - 1u) : "vcc", "scc", "memory");
}
__device__ __forceinline__ unsigned lds_word(unsigned a) {
    unsigned t_; asm volatile("ds_read_b32 %0, %1\n\ts_waitcnt lgkmcnt(0)\n\ts_nop 0" : "=v"(t_) : "v"(a) : "memory");
    return __builtin_amdgcn_readfirstlane(t_);
}
__device__ __forceinline__ void lds_set(unsigned a, unsigned v) { asm volatile("ds_write_b32 %0, %1" :: "v"(a), "v"(v) : "memory"); }
__device__ __forceinline__ int xrow(int lane) { return (lane >> 5) * 2 + ((lane >> 3) & 1); }
__device__ __forceinline__ int xcol(int lane) { return ((lane >> 4) & 1) * 8 + (lane & 7); }
__device__ __forceinline__ int swz(int r) { return r ^ (((r >> 3) & 1) << 2); }
__device__ __forceinline__ f32x2 unpk(unsigned w) { return (f32x2){lo16(w), hi16(w)}; }
__device__ __forceinline__ unsigned pk2v(f32x2 v) { return __builtin_bit_cast(unsigned, __builtin_convertvector(v, bf16x2)); }
__device__ __forceinline__ float frcp(float x) { return __builtin_amdgcn_rcpf(x); }
__device__ __forceinline__ float ex2(float x) { return __builtin_amdgcn_exp2f(x); }
__device__ __forceinline__ int lane_id() { int l; asm volatile("v_mbcnt_lo_u32_b32 %0, -1, 0\n\tv_mbcnt_hi_u32_b32 %0, -1, %0" : "=v"(l)); return l & 63; }
__device__ __forceinline__ f32x2 ln_stats(float ss, float s) {
    const float mean = s * (1.f / DM);
    const float var = fmax2(__builtin_fmaf(-mean, mean, ss * (1.f / DM)), 0.f);
    return (f32x2){bfr(mean), frcp(bfr(__builtin_amdgcn_sqrtf(bfr(bfr(var) + 1e-5f))))};
}
#define GATHER8(a, o, S) do { a[0] = ld16_sc1<-4 * (S)>(ws, o); a[1] = ld16_sc1<-3 * (S)>(ws, o); a[2] = ld16_sc1<-2 * (S)>(ws, o); a[3] = ld16_sc1<-(S)>(ws, o); \
    a[4] = ld16_sc1<0>(ws, o); a[5] = ld16_sc1<(S)>(ws, o); a[6] = ld16_sc1<2 * (S)>(ws, o); a[7] = ld16_sc1<3 * (S)>(ws, o); } while (0)
template <int OFF> __device__ __forceinline__ void again16(u32x4& r, const void* sbase, unsigned voff) {
    asm volatile("v_cmp_eq_u32 vcc, -1, %1\n\ts_mov_b64 exec, vcc\n\tglobal_load_dwordx4 %0, %2, %3 offset:%4 sc1\n\ts_mov_b64 exec, -1" : "+v"(r) : "v"(um4(r)), "v"(voff), "s"(sbase), "i"(OFF) : "memory", "vcc");
}
#define AGAIN8(a, o, S) do { again16<-4 * (S)>(a[0], ws, o); again16<-3 * (S)>(a[1], ws, o); again16<-2 * (S)>(a[2], ws, o); again16<-(S)>(a[3], ws, o); \
    again16<0>(a[4], ws, o); again16<(S)>(a[5], ws, o); again16<2 * (S)>(a[6], ws, o); again16<3 * (S)>(a[7], ws, o); } while (0)
#define WAIT8(a, N) asm volatile("s_waitcnt vmcnt(%8)" : "+v"((a)[0]), "+v"((a)[1]), "+v"((a)[2]), "+v"((a)[3]), "+v"((a)[4]), "+v"((a)[5]), "+v"((a)[6]), "+v"((a)[7]) : "i"(N) : "memory")
#define UM32(a) max(max(max(um4(a[0]), um4(a[1])), max(um4(a[2]), um4(a[3]))), max(max(um4(a[4]), um4(a[5])), max(um4(a[6]), um4(a[7]))))
#define SUM8(a) (((((((asf(a[0]) + asf(a[1])) + asf(a[2])) + asf(a[3])) + asf(a[4])) + asf(a[5])) + asf(a[6])) + asf(a[7]))
#define WAIT1(x, N) asm volatile("s_waitcnt vmcnt(%1)" : "+v"(x) : "i"(N) : "memory")
#define LO2(x) __builtin_shufflevector(x, x, 0, 1)
#define HI2(x) __builtin_shufflevector(x, x, 2, 3)
#define ACC8(a, s) do { f32x2 l_, h_; \
    asm volatile("s_waitcnt vmcnt(5)\n\tv_pk_add_f32 %0, %2, %4\n\tv_pk_add_f32 %1, %3, %5\n\tv_pk_add_f32 %0, %0, %6\n\tv_pk_add_f32 %1, %1, %7\n\t" \
                 "s_waitcnt vmcnt(3)\n\tv_pk_add_f32 %0, %0, %8\n\tv_pk_add_f32 %1, %1, %9\n\tv_pk_add_f32 %0, %0, %10\n\tv_pk_add_f32 %1, %1, %11\n\t" \
                 "s_waitcnt vmcnt(1)\n\tv_pk_add_f32 %0, %0, %12\n\tv_pk_add_f32 %1, %1, %13\n\tv_pk_add_f32 %0, %0, %14\n\tv_pk_add_f32 %1, %1, %15\n\t" \
                 "s_waitcnt vmcnt(0)\n\tv_pk_add_f32 %0, %0, %16\n\tv_pk_add_f32 %1, %1, %17\n\ts_nop 0" \
                 : "=&v"(l_), "=&v"(h_) : "v"(LO2(a[0])), "v"(HI2(a[0])), "v"(LO2(a[1])), "v"(HI2(a[1])), "v"(LO2(a[2])), "v"(HI2(a[2])), "v"(LO2(a[3])), "v"(HI2(a[3])), \
                   "v"(LO2(a[4])), "v"(HI2(a[4])), "v"(LO2(a[5])), "v"(HI2(a[5])), "v"(LO2(a[6])), "v"(HI2(a[6])), "v"(LO2(a[7])), "v"(HI2(a[7])) : "memory"); \
    s = __builtin_shufflevector(l_, h_, 0, 1, 2, 3); } while (0)
#define RETRY8(a, o, S, s, MID, POST, NAN_, X) do { GATHER8(a, o, S); MID; ACC8(a, s); POST; if (__builtin_expect((NAN_) != 0ull, 0)) { \
    _Pragma("unroll 1") for (unsigned spins_ = 0; spins_ < kSpinCap; ++spins_) { slip(X); AGAIN8(a, o, S); WAIT8(a, 0); if (!any_empty(UM32(a))) break; } s = SUM8(a); POST; } } while (0)
#define PIN1(a) asm volatile("" : "+v"(a))
#define PIN2(a, b) asm volatile("" : "+v"(a), "+v"(b))
#define PIN3(a, b, c) asm volatile("" : "+v"(a), "+v"(b), "+v"(c))
#define LANE_VARS() const int lane = lane_id(); \
    const int kq = lane >> 4, n = lane & 15, q4 = n >> 2, p4 = lane & 3; \
    const int r0s = 8 * kq + q4 + 4 * (kq & 1), r1s = 8 * kq + q4 + 4 * ((kq & 1) ^ 1);     \
    const int r2s = swz(4 * kq + q4);                                                       \
    (void)r0s; (void)r1s; (void)r2s; (void)p4
template <int V> struct IC { static constexpr int value = V; };
struct Frag80 { bf16x8 a0, a1; bf16x4 a2; };

__global__ void __launch_bounds__(512, 2) __attribute__((amdgpu_num_vgpr(32)))
mega_kernel(const u16*, const u16*, const u16*, const u16*, const u16*, const u16*, const u16*, const u16*, const u16*, u16*, unsigned char*
            ) {
    extern __shared__ __attribute__((aligned(16))) unsigned char smem[];
    const unsigned lb = (unsigned)(size_t)smem;
    const int wave = __builtin_amdgcn_readfirstlane(threadIdx.x >> 6);
    u64x8 ar0; u64x2 ar1; unsigned long long ar2, tp, tst64;
    asm volatile("s_load_dwordx16 %0, %5, 0\n\ts_load_dwordx4 %1, %5, 0x40\n\ts_load_dwordx2 %2, %5, 0x50\n\ts_load_dwordx2 %3, %5, %6\n\ts_memrealtime %4"
                 : "=&s"(ar0), "=&s"(ar1), "=&s"(ar2), "=&s"(tp), "=&s"(tst64) : "s"(__builtin_amdgcn_kernarg_segment_ptr()), "s"((0xa9876421u >> (4 * wave) & 15u) * 8u) : "memory");
    const int g = blockIdx.x, gj = GJ(g), gi = GI(g);
    const f32x4 z4 = {0.f, 0.f, 0.f, 0.f};
    asm volatile("" ::: "v255");
    auto sched = [&](unsigned X, unsigned gap) __attribute__((always_inline)) {
        unsigned v; unsigned long long c;
        asm volatile("s_memrealtime %1\n\tds_read_b32 %0, %2\n\ts_waitcnt lgkmcnt(0)" : "=v"(v), "=&s"(c) : "v"(lb + L_CNT + 56) : "memory");
        const unsigned n = (unsigned)c + gap;
        unsigned t = __builtin_amdgcn_readfirstlane(v) + X;
        if ((int)(n - t) > 0) t = n;
        until(t);
    };
    auto slip = [&](unsigned X) __attribute__((always_inline)) {
        if (X) lds_set(lb + L_CNT + 56, now() - X);
    };
    float xe = 0.f;

    auto code_pf = [&](int slice, int lane) __attribute__((always_inline)) {
        unsigned long long pc0; unsigned len, back, t;
        asm volatile("s_getpc_b64 %0\n.Lmega_pf%=:\n\ts_mov_b32 %1, .Lmega_code_end-.Lmega_code_begin\n\ts_mov_b32 %2, .Lmega_pf%=-.Lmega_code_begin" : "=s"(pc0), "=s"(len), "=s"(back));
        unsigned off = (unsigned)(slice * 4096 + lane * 64); if (off > len) off = len;
        asm volatile("global_load_dword %0, %1, %2" : "=v"(t) : "v"(off & ~3u), "s"(pc0 - back) : "memory");
        return t;
    };
    asm volatile(".Lmega_code_begin:");
    unsigned pf = 0u, pt1;
    const unsigned wofs = (unsigned)((((gj >> 1) * HQ + gi) * SK + (SK - WIN) + (gj & 1) * 512) * (DH * 2));
    {
        const int lane = threadIdx.x & 63, hi = lane >= 26;
        if (threadIdx.x < 24) lds_set(lb + L_CNT + threadIdx.x * 4, 0u);
        unsigned t2 = (unsigned)(gi * (DH * DM * 2)), t7 = (32u << 20) + (unsigned)(g * 128), to = (unsigned)(gj * 128), w2 = wofs;
        PINS(t2); PINS(t7); PINS(to); PINS(w2);
        to = wave == 7 ? t7 : to; to = wave == 2 ? t2 : to; to = wave < 2 ? w2 : to;
        if (wave == 7) pf = code_pf((g >> 3) & 15, lane);
        unsigned vo = (unsigned)((gj * 80 + swz(2 * wave) + hi) * (QD * 2) + gi * (DH * 2) + (lane - 26 * hi) * 16), d = lb + L_WQ + wave * 832, keep;
        unsigned xo = (0x11043210u >> (4 * wave) & 15u) * 1024 + (unsigned)(lane & (wave < 6 ? 63 : 15)) * 16, xd = lb + L_X0 + min(wave, 6) * 1024;
        PIN2(vo, xo); PINS(d); PINS(xd); PINS(to);
        __builtin_amdgcn_sched_barrier(0);
        asm volatile("s_waitcnt lgkmcnt(0)\n\ts_load_dword %0, %4, %6" : "=&s"(pt1), "+s"(ar0), "+s"(ar1), "+s"(ar2), "+s"(tp), "+s"(tst64) : "s"(to) : "memory");
        dma16p((const void*)(wave < 5 ? ar0[0] : ar0[5]), xo, xd);
        asm volatile("s_mov_b32 %0, m0\n\ts_mov_b32 m0, %2\n\ts_mov_b64 exec, %4\n\tglobal_load_lds_dwordx4 %1, %3 nt\n\t"
                     ".rept 4\n\ts_add_u32 m0, m0, 6656\n\tv_add_u32 %1, 0x30000, %1\n\tglobal_load_lds_dwordx4 %1, %3 nt\n\t.endr\n\t"
                     "s_mov_b64 exec, -1\n\ts_mov_b32 m0, %0\n\ts_barrier"
                     : "=&s"(keep), "+v"(vo) : "s"(d), "s"(ar0[3]), "s"(0x0003fffffcffffffull) : "memory", "scc");
        __builtin_amdgcn_sched_barrier(0);
    }
    const u16 *const kc = (const u16*)ar0[1], *const vc = (const u16*)ar0[2], *const wo = (const u16*)ar0[4], *const wg = (const u16*)ar0[6], *const wu = (const u16*)ar0[7], *const wd = (const u16*)ar1[0];
    u16* const out = (u16*)ar1[1];
    unsigned char* const ws = (unsigned char*)((ar2 + (32u << 20) - 1) & ~(unsigned long long)((16u << 20) - 1));
    const unsigned tst = (unsigned)tst64;
    auto fix_k1 = [&](auto IC_, auto NC) __attribute__((always_inline)) {
        constexpr int A = RK + 8 * decltype(IC_)::value, B = A + 4;
        unsigned t0, t1, t2, t3;
        asm volatile("s_waitcnt vmcnt(%12)\n\tv_mov_b32 %0, v%c4\n\tv_mov_b32 %1, v%c5\n\tv_mov_b32 %2, v%c6\n\tv_mov_b32 %3, v%c7\n\t"
                     "v_mov_b32_dpp v%c4, v%c8 row_ror:8 row_mask:0xf bank_mask:0xc\n\tv_mov_b32_dpp v%c5, v%c9 row_ror:8 row_mask:0xf bank_mask:0xc\n\t"
                     "v_mov_b32_dpp v%c6, v%c10 row_ror:8 row_mask:0xf bank_mask:0xc\n\tv_mov_b32_dpp v%c7, v%c11 row_ror:8 row_mask:0xf bank_mask:0xc\n\t"
                     "v_mov_b32_dpp v%c8, %0 row_ror:8 row_mask:0xf bank_mask:0x3\n\tv_mov_b32_dpp v%c9, %1 row_ror:8 row_mask:0xf bank_mask:0x3\n\t"
                     "v_mov_b32_dpp v%c10, %2 row_ror:8 row_mask:0xf bank_mask:0x3\n\tv_mov_b32_dpp v%c11, %3 row_ror:8 row_mask:0xf bank_mask:0x3"
                     : "=&v"(t0), "=&v"(t1), "=&v"(t2), "=&v"(t3)
                     : "i"(A), "i"(A + 1), "i"(A + 2), "i"(A + 3), "i"(B), "i"(B + 1), "i"(B + 2), "i"(B + 3), "i"(decltype(NC)::value) : "memory");
    };
    auto trans_v = [&](auto UC, auto NC) __attribute__((always_inline)) {
        constexpr int R = RV + 4 * decltype(UC)::value, N = decltype(NC)::value;
#define CELL(r, c) R + 16 * (r) + (c), R + 16 * (r) + 8 + (c)
        unsigned a0, a1, b0, b1, c0, c1;
        VMW(N + 6); zip_in<CELL(0, 0)>(); zip_out<CELL(0, 1)>(a0, a1); zip_out<CELL(0, 2)>(b0, b1); zip_out<CELL(0, 3)>(c0, c1);
        VMW(N + 4); zip_x<CELL(1, 0), CELL(0, 1)>(a0, a1); zip_in<CELL(1, 1)>(); zip_out<CELL(1, 2)>(a0, a1);
        VMW(N + 2); zip_x<CELL(2, 0), CELL(0, 2)>(b0, b1); zip_x<CELL(2, 1), CELL(1, 2)>(a0, a1); zip_in<CELL(2, 2)>(); zip_out<CELL(1, 3)>(a0, a1); zip_out<CELL(2, 3)>(b0, b1);
        VMW(N); zip_x<CELL(3, 0), CELL(0, 3)>(c0, c1); zip_x<CELL(3, 1), CELL(1, 3)>(a0, a1); zip_x<CELL(3, 2), CELL(2, 3)>(b0, b1); zip_in<CELL(3, 3)>();
#undef CELL
    };
    auto trans_w = [&](auto UC, auto NC) __attribute__((always_inline)) {
        constexpr int R = RV + 64 + 2 * decltype(UC)::value, N = decltype(NC)::value;
        unsigned a0, a1;
        VMW(N + 2); zip_in<R, R + 8>(); zip_out<R + 1, R + 9>(a0, a1);
        VMW(N); zip_x<R + 16, R + 24, R + 1, R + 9>(a0, a1); zip_in<R + 17, R + 25>();
    };
    auto frag80 = [&](unsigned im, int rs, int r0s, int r1s, int r2s) __attribute__((always_inline)) -> Frag80 {
        return Frag80{cat8(tr64(im + r0s * rs), tr64(im + r1s * rs)), cat8(tr64(im + (r0s + 32) * rs), tr64(im + (r1s + 32) * rs)), tr64(im + (r2s + 64) * rs)};
    };
    auto mm80 = [&](const Frag80& a, const Frag80& b) __attribute__((always_inline)) -> f32x4 {
        return mfma32(a.a1, b.a1, mfma32(a.a0, b.a0, z4)) + mfma16(a.a2, b.a2, z4);
    };
    auto rows80 = [&](unsigned a16, unsigned a8) __attribute__((always_inline)) -> Frag80 { const Frag80 f{lds16(a16), lds16(a16 + 64), lds8(a8)}; __builtin_amdgcn_sched_barrier(0); return f; };
    auto empty5 = [&](int q0, unsigned S, int lane) __attribute__((always_inline)) {
        st16m(ws, tile35(gj, q0 + (lane >> 3)) + S + (unsigned)(gi * 128 + (lane & 7) * 16), (u32x4){~0u, ~0u, ~0u, ~0u}, 0xffffffffffull);
    };
    auto empty4 = [&](int layer, unsigned S, int lane) __attribute__((always_inline)) {
        const unsigned b = WS_H4 + (unsigned)((layer * 32 + gi) * 32768) + (S ^ 8192u);
        st16_sc1(ws, b + (unsigned)((lane >> 5) * 4096 + gj * 512 + (lane & 31) * 16), (u32x4){~0u, ~0u, ~0u, ~0u});
        st16m(ws, b + (unsigned)(lane < 32 ? 16384 + gj * 512 + lane * 16 : H4C + ((lane - 32) >> 3) * 1024 + gj * 128 + (lane & 7) * 16), (u32x4){~0u, ~0u, ~0u, ~0u},
              layer ? 0xff000000ffffffffull : ~0ull);
    };
    int lead = wave == 7 ? LEAD * PACE * 4 : 0;
    auto pace = [&](unsigned n, bool wait = true) __attribute__((always_inline)) {
        unsigned u, w, t0;
        asm volatile("s_mov_b64 exec, 1\n\tds_add_rtn_u32 %0, %3, %4\n\tds_max_u32 %3, %5 offset:12\n\tds_add_rtn_u32 %1, %3, %6 offset:12\n\tds_read_b32 %2, %3 offset:4\n\ts_mov_b64 exec, -1\n\ts_waitcnt lgkmcnt(0)"
                     : "=&v"(u), "=&v"(w), "=&v"(t0) : "v"(lb + L_CNT + 60), "v"(n), "v"((now() - tst) << 6), "v"(n * PMIN * 4u) : "memory");
        int tn = (int)(__builtin_amdgcn_readfirstlane(t0) - tst + PINIT) * 64 + __builtin_amdgcn_readfirstlane(u) * (PACE * 4);
        tn += max((int)__builtin_amdgcn_readfirstlane(u) - UG, 0) * (SLOW * 4);
        const int tw = __builtin_amdgcn_readfirstlane(w) - PBURST * 64;
        if (tw > tn) tn = tw;
        tn -= lead;
        if (wait) {
#pragma unroll 1
            for (int k = 0; k < 2048 && (int)((now() - tst) << 6) < tn; ++k) {}
        }
    };
    auto slot = [&](unsigned u) __attribute__((always_inline)) {
        const unsigned tn = lds_word(lb + L_CNT + 64) + PINIT + (u * PACE + (NUNIT - UG) * SLOW) / 16;
#pragma unroll 1
        for (int k = 0; k < 8192 && (int)(now() - tn) < 0; ++k) __builtin_amdgcn_s_sleep(1);
    };
    auto load_wo = [&](int k, int lane, bool early) __attribute__((always_inline)) {
        const int c = k * 64 + lane, pp = c / 10, cc = c - 10 * pp;
        const unsigned f = (unsigned)(swz(pp) * 1280 + cc * 16);
        const u16* wb = wo + (size_t)gi * DH * DM + gj * 80;
#pragma unroll
        for (int a2 = 0; a2 < 6; ++a2) { if (a2 % 3 == 0) { if (early) pace(GUC); else slot(NUNIT - WOFF + (2 * k + a2 / 3) * WOC); } dma16(wb + 32 * a2 * DM, f, lb + L_WO + (5 * a2 + k) * 1024); }
    };
    auto load_wd = [&](int lane) __attribute__((always_inline)) {
        const u16* db = wd + gi * 48 * DM + gj * 80;
#pragma unroll 1
        for (int k = 0; k < 5; ++k) {
            const int c = k * 64 + lane, pp = c / 10, cc = c - 10 * pp;
            const unsigned fd = (unsigned)(swz(pp) * 1280 + cc * 16);
            if (!(k & 1)) pace(GUC);
            dma16(db, fd, lb + L_WD + k * 1024);
            if (k < 3) dma16(db + 32 * DM, (k < 2 || lane < 32) ? fd : 0u, lb + L_WD + (5 + k) * 1024);
        }
    };
    auto load_gu = [&](int m, int lane) __attribute__((always_inline)) {
#pragma unroll 1
        for (int k0 = 0; k0 < 3; ++k0) {
            const int k = (k0 + (GROT ? g & 7 : 0)) % 3, c = k * 64 + lane, row = c / 6, cc = c - 6 * row;
            const unsigned o = (unsigned)((swz(row) * FF + cc * 8) * 2), d = lb + L_GU + m * 8192 + k * 1024;
            const u16* wb = (m == 0 ? wg : wu) + gj * 80 * FF + gi * 48;
            pace(GUC);
            dma16(wb, o, d); dma16(wb + 32 * FF, o, d + 3072);
            if (k < 2) dma16(wb + 64 * FF, (k == 0 || lane < 32) ? o : 0u, d + 6144);
        }
    };
    auto lds_add = [&](int off, unsigned v) __attribute__((always_inline)) { asm volatile("s_mov_b64 exec, 1\n\tds_add_u32 %0, %1\n\ts_mov_b64 exec, -1" :: "v"(lb + L_CNT + off), "v"(v) : "memory"); };
    auto send_flag = [&](int lane) __attribute__((always_inline)) {
        st4_sc1(ws, WS_MB + (unsigned)((gi * 8 + ((gj == (gi & 7) ? lane : gi) & 7)) * 128 + gj * 4), lds_word(lb + L_CNT + 68));
    };
    auto tiles4 = [&](const Frag80 (&f)[4], const Frag80& b, unsigned long long mk) __attribute__((always_inline)) -> u32x4 {
        f32x4 c[4], d[4];
#pragma unroll
        for (int i = 0; i < 4; ++i) c[i] = mfma32(f[i].a0, b.a0, z4);
#pragma unroll
        for (int i = 0; i < 4; ++i) { c[i] = mfma32(f[i].a1, b.a1, c[i]); d[i] = mfma16(f[i].a2, b.a2, z4); }
        __builtin_amdgcn_sched_barrier(0);
        f32x2 lo, hi;
        asm volatile("s_nop 1\n\ts_mov_b64 exec, %18\n\tv_pk_add_f32 %0, %2, %10\n\tv_pk_add_f32 %1, %3, %11\n\t"
                     "s_lshl_b64 exec, exec, 1\n\tv_pk_add_f32 %0, %4, %12\n\tv_pk_add_f32 %1, %5, %13\n\t"
                     "s_lshl_b64 exec, exec, 1\n\tv_pk_add_f32 %0, %6, %14\n\tv_pk_add_f32 %1, %7, %15\n\t"
                     "s_lshl_b64 exec, exec, 1\n\tv_pk_add_f32 %0, %8, %16\n\tv_pk_add_f32 %1, %9, %17\n\ts_mov_b64 exec, -1"
                     : "=&v"(lo), "=&v"(hi) : "v"(LO2(c[0])), "v"(HI2(c[0])), "v"(LO2(c[1])), "v"(HI2(c[1])), "v"(LO2(c[2])), "v"(HI2(c[2])), "v"(LO2(c[3])), "v"(HI2(c[3])),
                       "v"(LO2(d[0])), "v"(HI2(d[0])), "v"(LO2(d[1])), "v"(HI2(d[1])), "v"(LO2(d[2])), "v"(HI2(d[2])), "v"(LO2(d[3])), "v"(HI2(d[3])), "s"(mk) : "scc");
        return asu(__builtin_shufflevector(lo, hi, 0, 1, 2, 3));
    };
    auto empty1 = [&](int half, unsigned S, int lane) __attribute__((always_inline)) {
        const unsigned o = WS_H1 + S + (unsigned)((gi * 4 + 2 * half) * 16384 + gj * 896 + min(lane, 55) * 16);
        st16_sc1(ws, o, (u32x4){~0u, ~0u, ~0u, ~0u}); st16_sc1(ws, o + 16384, (u32x4){~0u, ~0u, ~0u, ~0u});
    };
    auto report = [&]() __attribute__((always_inline)) { if (gj == (gi & 7)) st4_sc1(ws, WS_R + (unsigned)(gi * 4), lds_word(lb + L_CNT + 88)); };
    auto answer = [&](int lane, unsigned r) __attribute__((always_inline)) {
        int d = min(max((int)(r - tst), -400), 600) * 512 + (int)((r >> 1) & 15u);
        d += (int)dppu<0xB1>((unsigned)d); d += (int)dppu<0x4E>((unsigned)d); d += (int)dppu<0x141>((unsigned)d); d += (int)dppu<0x140>((unsigned)d);
        d = __builtin_amdgcn_readlane(d, 0) + __builtin_amdgcn_readlane(d, 16);
        st4_sc1(ws, WS_MB + (unsigned)(((lane & 31) * 8 + (lane & 7)) * 128 + 32), ((tst + (d >> 14) + TOFF) & ~3u) | ((d & 511) > 128 ? 3u : 1u));
        lds_set(lb + L_CNT + 36, 1u);
    };
    auto adopt = [&](unsigned t) __attribute__((always_inline)) {
        if (ADOPT && t - tst + 256u < 768u) lds_set(lb + L_CNT + 64, t);
        lds_set(lb + L_CNT + 80, lds_word(lb + L_CNT + 92) ^ (t & 2u) << 12);
        lds_set(lb + L_CNT + 84, (((t << 1) ^ lds_word(lb + L_CNT + 68)) & 4u) << 11);
        lds_set(lb + L_CNT + 52, 1u);
    };
    struct StatAddr { unsigned a16, b16, a8, b8, dst; };
    auto stats_addr = [&](int lane, unsigned dbase, int dstride) __attribute__((always_inline)) -> StatAddr {
        const int kq = lane >> 4, q4 = (lane >> 2) & 3, p4 = lane & 3;
        const int x1 = LANES(0x4444444444444444ull) ? L_WR + gj * 160 : L_XS + q4 * 160, xa = LANES(0xaaaaaaaaaaaaaaaaull) ? L_XS + 640 : x1;
        StatAddr r{lb + (unsigned)(xa + kq * 16), lb + (unsigned)(L_XS + p4 * 160 + kq * 16), lb + (unsigned)(xa + 128 + kq * 8), lb + (unsigned)(L_XS + p4 * 160 + 128 + kq * 8), dbase + (unsigned)(kq * dstride)};
        PIN3(r.a16, r.b16, r.dst); PIN2(r.a8, r.b8);
        return r;
    };
    auto stats = [&](const StatAddr& r) __attribute__((always_inline)) {
        const bf16x8 a0 = lds16(r.a16), b0 = lds16(r.b16), a1 = lds16(r.a16 + 64), b1 = lds16(r.b16 + 64);
        const bf16x4 a2 = lds8(r.a8), b2 = lds8(r.b8);
        unsigned long long mk = 0x0008000400020001ull; PINS(mk);
        __builtin_amdgcn_sched_barrier(0);
        st16m(ws, r.dst, asu(mfma32(a1, b1, mfma32(a0, b0, z4)) + mfma16(a2, b2, z4)), mk);
    };
    unsigned qa, pw, ma, idx, qm;
    auto qk_addr = [&](bool L1) __attribute__((always_inline)) {
        LANE_VARS();
        qa = lb + L_Q + (unsigned)(kq * 32 + (lane & 2) * 8);
        pw = lb + L_P + (unsigned)((7 + 8 * wave) * 192 + (kq >> 1) * 64 + (n >> 3) * 32 + (lane & 2) * 8 + (q4 & 1) * 8 + (kq & 1) * 4 + (lane & 1) * 2);
        ma = lb + L_M + (unsigned)(wave * 4);
        idx = (lane & 2) ? 0xeeeeu : 0x4444u;
        qm = lb + L_Q;
        PIN3(qa, pw, ma); PIN2(idx, qm);
        if (L1 && wave < 4) __builtin_amdgcn_s_setprio(2);
    };
    auto att_qk0 = [&]() __attribute__((always_inline)) {
        qk_addr(false);
#pragma unroll 1
        for (unsigned spins = 0; spins < (kSpinCap << 4); ++spins) {
            if (lds_word(lb + L_CNT + 4) != 0u) break;
            __builtin_amdgcn_s_sleep(1);
        }
        STAMP(3);
        const u32x4 q0 = *(const LAS u32x4*)(size_t)qa, q1 = *(const LAS u32x4*)(size_t)(qa + 128), q2 = *(const LAS u32x4*)(size_t)(qa + 256);
        f32x4 c0 = z4, c1 = z4, c2 = z4, c3 = z4;
#define SMF(c, q, r) "v_smfmac_f32_16x16x64_bf16 %" #c ", %" #q ", v[" #r "], %7\n\t"
        asm volatile("s_nop 1\n\t" SMF(0, 4, 64:71) SMF(1, 4, 88:95) SMF(2, 4, 112:119) SMF(3, 4, 136:143) SMF(0, 5, 72:79) SMF(1, 5, 96:103) SMF(2, 5, 120:127) SMF(3, 5, 144:151)
                     SMF(0, 6, 80:87) SMF(1, 6, 104:111) SMF(2, 6, 128:135) SMF(3, 6, 152:159) "s_nop 7"
                     : "+v"(c0), "+v"(c1), "+v"(c2), "+v"(c3) : "v"(q0), "v"(q1), "v"(q2), "v"(idx));
#undef SMF
        const float s0 = c0[0] + c0[2], s1 = c1[0] + c1[2], s2 = c2[0] + c2[2], s3 = c3[0] + c3[2];
        float m = fmax2(fmax3(s0, s1, s2), s3);
        m = fmax2(m, dpp<0xB1>(m)); m = fmax2(m, dpp<0x4E>(m)); m = fmax2(m, dpp<0x124>(m)); m = fmax2(m, dpp<0x128>(m));
        const float s01 = LANES(0xffff0000ffff0000ull) ? s1 : s0, s23 = LANES(0xffff0000ffff0000ull) ? s3 : s2;
        const float sv = LANES(0xffffffff00000000ull) ? s23 : s01;
        m = bfr(bfr(m) * SCALE);
        const float p = ex2((bfr(bfr(sv) * SCALE) - m) * LOG2E);
        *(LAS u16*)(size_t)pw = (u16)pk2(p, p);
        *(LAS float*)(size_t)ma = m;
        __builtin_amdgcn_sched_barrier(0);
    };
    unsigned pr, oa, oc, cma, cxa, cra, cso;
    auto att_addr = [&]() __attribute__((always_inline)) {
        LANE_VARS();
        pr = lb + L_P + (unsigned)((7 + 8 * wave - 2 * q4 - (lane & 1)) * 192 + kq * 32 + (lane & 2) * 8);
        oa = lb + L_OP + (unsigned)(wave * 1024 + (lane & 7) * 64 + kq * 16 + (n >> 3) * 8);
        oc = lb + L_OP + (unsigned)(wave * 1024 + 512 + (kq >> 1) * 256 + (lane & 7) * 32 + (kq & 1) * 16 + (n >> 3) * 8);
        PIN3(pr, oa, oc);
    };
    auto comb_out = [&](float u0, float u1, unsigned so, unsigned long long mk, bool both) __attribute__((always_inline)) {
        const unsigned w = min(pk2(u0, u1), 0xfffffffeu);
        if (CMODE && !both) { st4_sc1(ws, so, w); return; }
        const u32x4 w4 = {both ? dppu<0x00>(w) : w, dppu<0x55>(w), dppu<0xAA>(w), dppu<0xFF>(w)};
        st16m(ws, so, w4, mk);
    };
    auto max8 = [&](f32x4 m0, f32x4 m1) __attribute__((always_inline)) { return fmax3(fmax3(m0[0], m0[1], m0[2]), fmax3(m0[3], m1[0], m1[1]), fmax2(m1[2], m1[3])) * LOG2E; };
    auto comb_o0 = [&](f32x4 m0, f32x4 m1, f32x4 mm, f32x4 o0, f32x4 o1, f32x4 o2, f32x4 o3, unsigned so, unsigned long long mk, bool both) __attribute__((always_inline)) {
        const float M = max8(m0, m1);
        f32x4 f;
#pragma unroll
        for (int k = 0; k < 4; ++k) f[k] = ex2(__builtin_fmaf(mm[k], LOG2E, -M));
        const f32x4 o = f[3] * o3 + (f[2] * o2 + (f[1] * o1 + f[0] * o0));
        comb_out(rs32(o[0], o[2]), rs32(o[1], o[3]), so, mk, both);
    };
    auto comb_o1 = [&](f32x4 m0, f32x4 m1, f32x2 mm, f32x4 o0, f32x4 o1, unsigned so, unsigned long long mk, bool both) __attribute__((always_inline)) {
        const float M = max8(m0, m1);
        const f32x4 o = ex2(__builtin_fmaf(mm[1], LOG2E, -M)) * o1 + ex2(__builtin_fmaf(mm[0], LOG2E, -M)) * o0;
        comb_out(x16_add(rs32(o[0], o[2])), x16_add(rs32(o[1], o[3])), so, mk, both);
    };
    auto ml_addr = [&](int lane, unsigned& xa, unsigned& ra) __attribute__((always_inline)) {
        xa = lb + L_M + (unsigned)(((lane & 15) >> 1) * 4); ra = lb + L_P + (unsigned)((7 + 8 * ((lane & 15) >> 1)) * 192 + (lane & 1) * 64 + (lane >> 4) * 16);
    };
    auto comb_ml = [&](unsigned ma, unsigned xa, unsigned ra, unsigned so, bool both) __attribute__((always_inline)) {
        const u32x4 x4 = *(const LAS u32x4*)(size_t)ra;
        const f32x4 m0 = *(const LAS f32x4*)(size_t)ma, m1 = *(const LAS f32x4*)(size_t)(ma + 16);
        const float mw = *(const LAS float*)(size_t)xa;
        __builtin_amdgcn_sched_barrier(0);
        const f32x4 sp = mfma32u((u32x4){0x3f803f80u, 0x3f803f80u, 0x3f803f80u, 0x3f803f80u}, x4, z4);
        __builtin_amdgcn_sched_barrier(0);
        const float M = max8(m0, m1);
        float L = row16_sum(sp[0] * ex2(__builtin_fmaf(mw, LOG2E, -M)));
        if (both) L = dpp<0x00>(L);
        st16m(ws, so, asu((f32x4){M, L, M, L}), both ? 3ull : 1ull);
    };
    auto att_pv0 = [&]() __attribute__((always_inline)) {
        u32x4 bm[8];
        trans_v(IC<0>{}, IC<24>{}); trans_v(IC<1>{}, IC<16>{});
#pragma unroll
        for (int e = 0; e < 8; ++e) bm[e] = *(const LAS u32x4*)(size_t)(pr + e * 192);
        __builtin_amdgcn_sched_barrier(0);
        f32x4 da = z4, dc = z4;
        PIN2(da, dc);
        asm volatile("s_nop 1");
        __builtin_amdgcn_sched_barrier(0);
        f32x2 r;
        smf<RV>(da, bm[0], idx); smf<RV + 8>(da, bm[1], idx); smf<RV + 16>(da, bm[2], idx); smf<RV + 24>(da, bm[3], idx);
        smf<RV + 32>(da, bm[4], idx); smf<RV + 40>(da, bm[5], idx); smf<RV + 48>(da, bm[6], idx); smf<RV + 56>(da, bm[7], idx);
        asm volatile("s_nop 7\n\tv_pk_add_f32 %0, %1, %2\n\tds_write_b64 %3, %0\n\ts_mov_b64 exec, 1\n\tds_add_u32 %4, %5\n\ts_mov_b64 exec, -1"
                     : "=&v"(r) : "v"(LO2(da)), "v"(HI2(da)), "v"(oa), "v"(lb + L_CNT + 24), "v"(1u) : "memory");
        GUARD(900);
        MARK(b_t5);
        trans_w(IC<0>{}, IC<12>{}); trans_w(IC<1>{}, IC<8>{}); trans_w(IC<2>{}, IC<4>{}); trans_w(IC<3>{}, IC<0>{});
        STAMP(4);
        asm volatile("s_nop 1\n\tv_smfmac_f32_16x16x64_bf16 %0, %1, v[224:231], %5\n\tv_smfmac_f32_16x16x64_bf16 %0, %2, v[232:239], %5\n\t"
                     "v_smfmac_f32_16x16x64_bf16 %0, %3, v[240:247], %5\n\tv_smfmac_f32_16x16x64_bf16 %0, %4, v[248:255], %5\n\ts_nop 6"
                     : "+v"(dc) : "v"(bm[0]), "v"(bm[1]), "v"(bm[2]), "v"(bm[3]), "v"(idx));
        asm volatile("s_nop 0\n\tv_pk_add_f32 %0, %1, %2\n\tds_write_b64 %3, %0" : "=&v"(r) : "v"(LO2(dc)), "v"(HI2(dc)), "v"(oc) : "memory");
        STAMP(20);
    };
    unsigned long long ck1 = 0xffff0000ffff0000ull, ck2 = 0xffffffff00000000ull, csc = 0x3d93cd3a3d93cd3aull;
    auto att1 = [&]() __attribute__((always_inline)) {
        constexpr int layer = 1;
        {
#define SMF(c, q, r) "v_smfmac_f32_16x16x64_bf16 v[" #c "], v[" #q "], v[" #r "], %0\n\t"
#define MAXD(x) "v_max_f32_dpp v41, v41, v41 " x " row_mask:0xf bank_mask:0xf\n\t"
            asm volatile("s_waitcnt lgkmcnt(0)\n\ts_barrier\n\t"
                         "ds_read_b128 v[0:3], %1\n\tds_read2_b32 v[12:13], %2 offset0:96 offset1:100\n\tds_read_b128 v[4:7], %1 offset:128\n\tds_read_b128 v[8:11], %1 offset:256\n\t"
                         "v_mov_b64 v[16:17], 0\n\tv_mov_b64 v[18:19], 0\n\tv_mov_b64 v[20:21], 0\n\tv_mov_b64 v[22:23], 0\n\tv_mov_b64 v[24:25], 0\n\tv_mov_b64 v[26:27], 0\n\t"
                         "v_mov_b64 v[28:29], 0\n\tv_mov_b64 v[30:31], 0\n\tv_mov_b64 v[32:33], 0\n\tv_mov_b64 v[34:35], 0\n\tv_mov_b64 v[36:37], 0\n\tv_mov_b64 v[38:39], 0\n\t"
                         "s_waitcnt lgkmcnt(3)\n\t" SMF(16:19, 0:3, 64:71) SMF(20:23, 0:3, 88:95) "s_waitcnt lgkmcnt(1)\n\tv_mul_f32 v13, 0x3acccccd, v13\n\t"
                         SMF(24:27, 0:3, 112:119) "v_mul_f32 v12, 0x3acccccd, v12\n\t" SMF(28:31, 0:3, 136:143) "v_fma_f32 v12, -v13, v13, v12\n\t"
                         SMF(16:19, 4:7, 72:79) "v_maximum3_f32 v12, v12, 0, 0\n\t" SMF(20:23, 4:7, 96:103) "v_cvt_pk_bf16_f32 v12, 0, v12\n\t"
                         SMF(24:27, 4:7, 120:127) "v_add_f32 v12, 0x3727c5ac, v12\n\t" SMF(28:31, 4:7, 144:151) "v_cvt_pk_bf16_f32 v12, 0, v12\n\ts_waitcnt lgkmcnt(0)\n\t"
                         SMF(16:19, 8:11, 80:87) "v_sqrt_f32 v12, v12\n\t" SMF(20:23, 8:11, 104:111) "v_cvt_pk_bf16_f32 v12, 0, v12\n\t"
                         SMF(24:27, 8:11, 128:135) "v_rcp_f32 v12, v12\n\t" SMF(28:31, 8:11, 152:159)
                         "s_nop 2\n\tv_add_f32 v42, v16, v18\n\ts_nop 0\n\tv_add_f32 v43, v20, v22\n\tv_max_f32 v41, v42, v43\n\tv_add_f32 v44, v24, v26\n\tv_add_f32 v45, v28, v30\n\t"
                         "v_max3_f32 v41, v41, v44, v45\n\tv_cndmask_b32 v42, v42, v43, %8\n\tv_cndmask_b32 v44, v44, v45, %8\n\t"
                         MAXD("quad_perm:[1,0,3,2]") "v_cndmask_b32 v40, v42, v44, %9\n\ts_nop 0\n\t" MAXD("quad_perm:[2,3,0,1]") "s_nop 1\n\t" MAXD("row_ror:4") "s_nop 1\n\t" MAXD("row_ror:8")
                         "v_pk_mul_f32 v[40:41], v[12:13], v[40:41] op_sel_hi:[0,1]\n\tv_cvt_pk_bf16_f32 v40, 0, v40\n\tv_cvt_pk_bf16_f32 v41, 0, v41\n\t"
                         "v_pk_mul_f32 v[40:41], %10, v[40:41]\n\tv_cvt_pk_bf16_f32 v40, 0, v40\n\tv_cvt_pk_bf16_f32 v41, 0, v41\n\t"
                         "v_sub_f32 v40, v40, v41\n\tv_mul_f32 v40, 0x3fb8aa3b, v40\n\tv_exp_f32 v40, v40\n\ts_nop 0\n\tv_cvt_pk_bf16_f32 v40, v40, v40\n\tds_write_b16 %3, v40\n\t"
                         "ds_read_b128 v[0:3], %4\n\tds_read_b128 v[4:7], %4 offset:192\n\tds_read_b128 v[8:11], %4 offset:384\n\tds_read_b128 v[12:15], %4 offset:576\n\t"
                         "ds_read_b128 v[16:19], %4 offset:768\n\tds_read_b128 v[20:23], %4 offset:960\n\tds_read_b128 v[24:27], %4 offset:1152\n\tds_read_b128 v[28:31], %4 offset:1344\n\t"
                         "ds_write_b32 %5, v41\n\t"
                         "s_waitcnt lgkmcnt(8)\n\t" SMF(32:35, 0:3, 160:167) SMF(36:39, 0:3, 224:231) "s_waitcnt lgkmcnt(7)\n\t" SMF(32:35, 4:7, 168:175) SMF(36:39, 4:7, 232:239)
                         "s_waitcnt lgkmcnt(6)\n\t" SMF(32:35, 8:11, 176:183) SMF(36:39, 8:11, 240:247) "s_waitcnt lgkmcnt(4)\n\t" SMF(32:35, 12:15, 184:191) SMF(36:39, 12:15, 248:255)
                         SMF(32:35, 16:19, 192:199) "s_waitcnt lgkmcnt(1)\n\t" SMF(32:35, 20:23, 200:207) SMF(32:35, 24:27, 208:215) SMF(32:35, 28:31, 216:223)
                         "s_nop 2\n\tv_pk_add_f32 v[36:37], v[36:37], v[38:39]\n\tds_write_b64 %7, v[36:37]\n\ts_nop 2\n\tv_pk_add_f32 v[32:33], v[32:33], v[34:35]\n\tds_write_b64 %6, v[32:33]"
                         :: "v"(idx), "v"(qa), "v"(qm), "v"(pw), "v"(pr), "v"(ma), "v"(oa), "v"(oc), "s"(ck1), "s"(ck2), "s"(csc)
                         : "memory", "v0", "v1", "v2", "v3", "v4", "v5", "v6", "v7", "v8", "v9", "v10", "v11", "v12", "v13", "v14", "v15", "v16", "v17", "v18", "v19", "v20", "v21", "v22", "v23",
                           "v24", "v25", "v26", "v27", "v28", "v29", "v30", "v31", "v32", "v33", "v34", "v35", "v36", "v37", "v38", "v39", "v40", "v41", "v42", "v43", "v44", "v45");
#undef SMF
#undef MAXD
            STAMP(20 + 32 * layer);
            BAR();
            if (__builtin_expect(wave == 0, 1)) {
                STAMP(5 + 32 * layer);
                comb_o0(*(const LAS f32x4*)(size_t)cma, *(const LAS f32x4*)(size_t)(cma + 16), *(const LAS f32x4*)(size_t)cxa, *(const LAS f32x4*)(size_t)cra,
                        *(const LAS f32x4*)(size_t)(cra + 1024), *(const LAS f32x4*)(size_t)(cra + 2048), *(const LAS f32x4*)(size_t)(cra + 3072), cso, 0x1111111111111111ull, false);
            }
            __builtin_amdgcn_sched_barrier(0);
            if (__builtin_expect(wave == 1, 1))
                comb_o1(*(const LAS f32x4*)(size_t)cma, *(const LAS f32x4*)(size_t)(cma + 16), *(const LAS f32x2*)(size_t)cxa, *(const LAS f32x4*)(size_t)cra, *(const LAS f32x4*)(size_t)(cra + 1024), cso, 0x0000111100001111ull, false);
            __builtin_amdgcn_sched_barrier(0);
            if (__builtin_expect(wave == 2, 1)) comb_ml(cma, cxa, cra, cso, false);
            MARK(e_q);
            if (wave < 3) STAMP(21 + 32 * layer);
            __builtin_amdgcn_s_setprio(0);
        }
    };

    unsigned nj = 0u, rS = 0u, nS = 0u; int ord;
    {
        const int tid = threadIdx.x, lane = tid & 63;
        asm volatile("ds_max_u32 %0, %1" :: "v"(lb + L_CNT + 64), "v"(tst) : "memory");
        unsigned mb = 0u;
        const u16* kbase = kc + wofs / 2;
        const u16* vbase = vc + wofs / 2;
        unsigned pt0 = 0u;
        if (wave == 7) {
            __builtin_amdgcn_s_setprio(3);
            if (lane < 10) *reinterpret_cast<u32x4*>(smem + L_XS + 640 + lane * 16) = (u32x4){0x3f803f80u, 0x3f803f80u, 0x3f803f80u, 0x3f803f80u};
            if (lane < 16) { *reinterpret_cast<u32x4*>(smem + L_CS + 768 + lane * 16) = (u32x4){0u, 0u, 0u, 0u}; }
        } else {
            if (wave >= 4) {
                if (wave == 4) st16_sc1(ws, WS_OWN + (unsigned)(gi * 16384 + (lane >> 5) * 8192 + gj * 512 + (lane & 31) * 16), (u32x4){~0u, ~0u, ~0u, ~0u});
                else empty5(0, (unsigned)(wave - 5) * 8192u, lane);
                if (wave == 6) {
                    __builtin_amdgcn_s_setprio(3);
                    if (gi + gj == 0) st4_sc1(ws, WS_R + (lane & 31) * 4, 0u);
                    st4_sc1(ws, WS_MB + (unsigned)((gi * 8 + gj) * 128 + (lane & 31) * 4), 0u);
                }
            } else if (wave >= 2) {
                unsigned long long c0, c1;
                asm volatile("s_waitcnt lgkmcnt(0)\n\ts_memtime %0\n\ts_waitcnt lgkmcnt(0)\n\ts_load_dword %2, %3, %4\n\ts_waitcnt lgkmcnt(0)\n\ts_memtime %1\n\ts_waitcnt lgkmcnt(0)"
                             : "=&s"(c0), "=&s"(c1), "=&s"(pt0) : "s"(ws), "s"((6u << 20) + ((((tst + g * 977u) * 2654435761u) >> 11 & 0x1fdf80u) | ((wave ^ gj) & 1) * 8192)) : "memory");
                pt0 = ((wave ^ gj) & 1) ? (unsigned)c1 - (unsigned)c0 : (unsigned)c0 - (unsigned)c1;
            }
            STAMP(36);
            *reinterpret_cast<u32x4*>(smem + L_P + tid * 16) = (u32x4){0u, 0u, 0u, 0u};
            if (tid < 72 * 12 - 448) *reinterpret_cast<u32x4*>(smem + L_P + 7168 + tid * 16) = (u32x4){0u, 0u, 0u, 0u};
            if (wave >= 4) {
                if (wave == 6) VMW(2); else VMW(1);
                asm volatile("ds_max_u32 %0, %1" :: "v"(lb + L_CNT + 64), "v"(now() - TLAND) : "memory");
                STAMP(40);
                lds_add(0, 1u);
            } else if (wave >= 2) { lds_add(20, pt0); VMW(0); lds_add(0, 1u); }
        }
        asm volatile("s_waitcnt lgkmcnt(0)" :: "s"(pt1) : "memory");
        const int rot = ROT;
        u32x4 a[4];
        unsigned ko = (unsigned)((wave * 64 + (lane & 7)) * 384 + (((lane >> 3) & 1) * 4 + (lane >> 4)) * 16);
        PIN1(ko);
        auto k_group = [&](auto GC_) __attribute__((always_inline)) {
            constexpr int G = decltype(GC_)::value, R = RK + 24 * (G >> 1) + 4 * (G & 1);
            const unsigned o = ko + (unsigned)((((G >> 1) + rot) & 3) * 6144 + (G & 1) * 3072);
            pace(3);
            ld16_nt<R, 0>(kbase, o); ld16_nt<R + 8, 128>(kbase, o); ld16_nt<R + 16, 256>(kbase, o);
            const int lane = lane_id();
            const unsigned ho = WS_Q0 + (unsigned)((gi * 4 + (gj >> 1)) * 4096 + lane * 16);
            if (wave < 7) {
                asm volatile("; COND_BEGIN");
                if (G == 0 && wave >= 4) { VMW(3); lds_add(32, 1u); }
                if (G == 0 && wave < 4) {
                    if (wave < 2) { VMW(3); lds_add(0, 1u); load_gu(wave, lane); }
                    if (wave == 2) load_wd(lane);
                    if (wave == 3) {
#pragma unroll 1
                        for (int k = 5 - WEARLY; k < 5; ++k) load_wo(k, lane, true);
                    }
                }
                asm volatile("; COND_END");
            }
            if (wave == 7) {
                asm volatile("; COND_BEGIN");
                if (G == GP) {
                    VMW(3 * (GP + 1));
#pragma unroll 1
                    for (unsigned spins = 0; spins < (kSpinCap << 4); ++spins) {
                        if (lds_word(lb + L_CNT) >= 7u) break;
                        __builtin_amdgcn_s_sleep(1);
                    }
                    STAMP(26);
                    const unsigned char* p = smem + L_X0 + (lane >> 4) * 1280;
                    const float c0 = lo16(*reinterpret_cast<const u16*>(p));
                    const f32x2 c2 = {c0, c0};
                    f32x2 s2 = {0.f, 0.f}, q2 = s2;
#pragma unroll
                    for (int k = 0; k < 5; ++k) {
                        const u32x4 w = *reinterpret_cast<const u32x4*>(p + k * 256 + (lane & 15) * 16);
#pragma unroll
                        for (int e = 0; e < 4; ++e) { const f32x2 d = unpk(w[e]) - c2; s2 += d; q2 += d * d; }
                    }
                    const float s = row16_sum(s2[0] + s2[1]), qq = row16_sum(q2[0] + q2[1]);
                    const float ms = s * (1.f / DM);
                    const unsigned mv = pk2(c0 + ms, fmax2(qq - s * ms, 0.f) * (1.f / DM));
                    const float mb = lo16(mv), rden = frcp(bfr(__builtin_amdgcn_sqrtf(bfr(hi16(mv) + 1e-5f))));
                    const f32x2 mb2 = {mb, mb}, rd2 = {rden, rden};
                    if ((lane & 15) < 10) {
                        u32x4 w = *reinterpret_cast<const u32x4*>(p + gj * 160 + (lane & 15) * 16);
#pragma unroll
                        for (int e = 0; e < 4; ++e) w[e] = pk2v(unpk(pk2v(unpk(w[e]) - mb2)) * rd2);
                        *reinterpret_cast<u32x4*>(smem + L_XS + (lane >> 4) * 160 + (lane & 15) * 16) = w;
                    }
                    STAMP(30);
                }
                if (G == GP || G == GA) {
#pragma unroll 1
                    for (int blk = 0; blk < 3; ++blk) {
                        LANE_VARS();
                        const unsigned xb = lb + L_XS + (unsigned)((G == GP ? q4 : 4) * 160);
                        Frag80 f[4];
#pragma unroll
                        for (int i = 0; i < 4; ++i) f[i] = frag80(lb + L_WQ + (unsigned)(128 * blk + 32 * i + 8 * p4), 416, r0s, r1s, r2s);
                        const Frag80 b = rows80(xb + kq * 16, xb + 128 + kq * 8);
                        const u32x4 c = tiles4(f, b, 0x1111111111111111ull);
                        if (G == GP) *reinterpret_cast<u32x4*>(smem + L_OP + q4 * 768 + 256 * blk + p4 * 64 + kq * 16) = c;
                        else *reinterpret_cast<u32x4*>(smem + L_CS + 256 * blk + kq * 64 + p4 * 16) = c;
                    }
                    if (G == GP) {
                        asm volatile("" ::: "memory");
                        const int ln = lane_id();
                        f32x2 c[6];
                        unsigned m = 20u;
#pragma unroll
                        for (int k = 0; k < 6; ++k) {
                            c[k] = *reinterpret_cast<const f32x2*>(smem + L_OP + (k / 3) * 1536 + ln * 24 + (k % 3) * 8);
                            m = max(m, max(fexp(c[k][0]), fexp(c[k][1])));
                        }
                        m = max(m, dppu<0xB1>(m)); m = max(m, dppu<0x4E>(m)); m = max(m, dppu<0x141>(m)); m = max(m, dppu<0x140>(m));
                        m = max(max((unsigned)__builtin_amdgcn_readlane(m, 0), (unsigned)__builtin_amdgcn_readlane(m, 16)), max((unsigned)__builtin_amdgcn_readlane(m, 32), (unsigned)__builtin_amdgcn_readlane(m, 48)));
                        lds_set(lb + L_CNT + 76, m);
                        const float f = __uint_as_float((273u - m) << 23);
#pragma unroll
                        for (int k = 0; k < 2; ++k) {
                            const u32x2 w0 = enc21(c[3 * k][0] * f, c[3 * k][1] * f, c[3 * k + 1][0] * f), w1 = enc21(c[3 * k + 1][1] * f, c[3 * k + 2][0] * f, c[3 * k + 2][1] * f);
                            st16_sc1(ws, WS_Q0 + (unsigned)((gi * 4 + 2 * k + (ln >> 5)) * 4096 + gj * 512 + (ln & 31) * 16), (u32x4){w0[0], w0[1], w1[0], w1[1]});
                        }
                        STAMP(2);
                        WAIT1(pf, 3 * (GP + 1) + 2);
                    }
                }
                if (G == GA) {
                    const unsigned half = __builtin_amdgcn_s_getreg(2 << 6 | 20);
                    lds_set(lb + L_CNT + 68, lds_word(lb + L_CNT + 64) << 11 | lds_word(lb + L_CNT + 76) << 3 | 1u | (((int)lds_word(lb + L_CNT + 20) > 0) ^ half) << 1 | half << 2);
                    VMW(3 * (GA - GP));
#pragma unroll 1
                    for (unsigned spins = 0; spins < (kSpinCap << 4); ++spins) if (lds_word(lb + L_CNT + 32) >= 3u) break;
                    send_flag(lane_id());
                    lds_set(lb + L_CNT + 8, 1u);
                    if (XS) pace(XS, false);
                    STAMP(17);
                    f32x2 r2 = {0.f, 0.f};
#pragma unroll
                    for (int k = 0; k < 5; ++k) r2 += unpk(*reinterpret_cast<const unsigned*>(smem + L_WR + k * 256 + lane_id() * 4));
                    *reinterpret_cast<float*>(smem + L_CNT + 12) = wave_sum(r2[0] + r2[1]);
                }
                asm volatile("; COND_END");
            }
            if (wave == 6) {
                asm volatile("; COND_BEGIN");
                if (G == GB - 1) mb = ld4_sc1(ws, WS_MB + (unsigned)((gi * 8 + gj) * 128 + (gi & 7) * 4));
                if (G == GB) {
                    WAIT1(mb, 3);
#pragma unroll 1
                    for (unsigned spins = 0; spins < kSpinCap && !all_set(mb); ++spins) {
                        if ((spins & 7u) == 7u && lds_word(lb + L_CNT + 8)) send_flag(lane);
                        mb = ld4_sc1(ws, WS_MB + (unsigned)((gi * 8 + gj) * 128 + (gi & 7) * 4));
                        WAIT1(mb, 0);
                    }
                    mb = ld4_sc1(ws, WS_MB + (unsigned)((gi * 8 + (gi & 7)) * 128 + (lane & 7) * 4));
                }
                if (G == GC) {
                    WAIT1(mb, 3 * (GC - GB));
#pragma unroll 1
                    for (unsigned spins = 0; spins < kSpinCap && !all_set(mb); ++spins) {
                        if ((spins & 7u) == 7u && lds_word(lb + L_CNT + 8)) send_flag(lane);
                        mb = ld4_sc1(ws, WS_MB + (unsigned)((gi * 8 + (gi & 7)) * 128 + (lane & 7) * 4));
                        WAIT1(mb, 0);
                    }
                    lds_set(lb + L_CNT + 92, (__builtin_amdgcn_readlane(mb, gi & 7) & 4u) << 11);
                    int d = min(max((int)(mb - (tst << 11)) >> 11, -400), 600);
                    d += (int)dppu<0xB1>((unsigned)d); d += (int)dppu<0x4E>((unsigned)d); d += (int)dppu<0x141>((unsigned)d);
                    const unsigned tm = tst + (unsigned)(__builtin_amdgcn_readfirstlane(d) >> 3);
                    if (ROWM) lds_set(lb + L_CNT + 64, tm);
                    lds_set(lb + L_CNT + 88, (tm & ~31u) | __builtin_popcount((unsigned)__builtin_amdgcn_ballot_w64((mb & 2u) != 0u) & 255u) << 1 | 1u);
                    report();
                    a[0] = ld16_sc1<0>(ws, ho); a[1] = ld16_sc1<1024>(ws, ho); a[2] = ld16_sc1<2048>(ws, ho); a[3] = ld16_sc1<3072>(ws, ho);
                    pace(XCOST, false);
                    STAMP(31);
                }
                asm volatile("; COND_END");
            } else if (G == GC) { for (int k = 0; k < 4; ++k) asm volatile("" : "=v"(a[k])); }
        };
        k_group(IC<0>{}); k_group(IC<1>{}); k_group(IC<2>{}); k_group(IC<3>{}); k_group(IC<4>{}); k_group(IC<5>{}); k_group(IC<6>{}); k_group(IC<7>{});
        {
            const int lane = lane_id();
            const unsigned v0 = (unsigned)((wave * 64 + 4 * (lane >> 4)) * 384 + (lane & 15) * 16);
            const unsigned v1 = v0 + 256 - (unsigned)((lane & 15) * 8);
            auto v_group = [&](auto HC) __attribute__((always_inline)) {
                constexpr int h = decltype(HC)::value, R = h < 8 ? RV + 16 * (h & 3) + 4 * (h >> 2) : RV + 64 + 2 * (h - 8), NV = h < 8 ? 2 * h + 2 : 4 * h - 12;
                const unsigned o = (h < 8 ? v0 : v1) + (unsigned)((((h < 8 ? h >> 1 : h) + rot) & 3) * 6144);
                pace(2);
                if (h < 8) { ld16_nt<R, (h & 1) * 768>(vbase, o); ld16_nt<R + 8, (h & 1) * 768 + 384>(vbase, o); }
                else { ld8_nt<R, 0>(vbase, o); ld8_nt<R + 8, 384>(vbase, o); ld8_nt<R + 16, 768>(vbase, o); ld8_nt<R + 24, 1152>(vbase, o); }
                if (wave == 7 && gi + gj == 0) {
                    asm volatile("; COND_BEGIN");
                    if (h == 3 || h == 6) mb = ld4_sc1(ws, WS_R + (unsigned)((lane & 31) * 4));
                    if (h == 4 || h == 7) {
                        WAIT1(mb, 2);
                        if (all_set(mb) && !lds_word(lb + L_CNT + 36)) answer(lane, mb);
                    }
                    asm volatile("; COND_END");
                }
                if (wave == 6) {
                    asm volatile("; COND_BEGIN");
                if (h == HD) {
                    asm volatile("s_waitcnt vmcnt(%4)" : "+v"(a[0]), "+v"(a[1]), "+v"(a[2]), "+v"(a[3]) : "i"(3 * (7 - GC) + NV) : "memory");
                    float sq[6] = {0.f, 0.f, 0.f, 0.f, 0.f, 0.f};
#pragma unroll
                    for (int k = 0; k < 4; ++k) {
                        const float f = __uint_as_float(((((unsigned)__builtin_amdgcn_ds_bpermute((2 * k + (lane >> 5)) * 4, (int)mb) >> 3) & 255u) - 19u) << 23);
#pragma unroll
                        for (int e = 0; e < 2; ++e) {
                            const unsigned lo = a[k][2 * e], hi = a[k][2 * e + 1];
                            const float fe = (int)hi < 0 ? __builtin_nanf("") : f;
                            sq[3 * e] += (float)((int)(lo << 11) >> 11) * fe;
                            sq[3 * e + 1] += (float)((int)(__builtin_amdgcn_alignbit(hi, lo, 21) << 11) >> 11) * fe;
                            sq[3 * e + 2] += (float)((int)(hi << 1) >> 11) * fe;
                        }
                    }
#pragma unroll
                    for (int e = 0; e < 3; ++e) {
                        const int pi = 3 * (lane & 31) + e;
                        *reinterpret_cast<unsigned*>(smem + L_Q + (pi >> 3) * 32 + (pi & 6) * 2 + (pi & 1) * 16) = pk2(x32_add(sq[2 * e]), x32_add(sq[2 * e + 1]));
                    }
                    asm volatile("s_waitcnt lgkmcnt(0)\n\tds_write_b32 %0, %1" :: "v"(lb + L_CNT + 4), "v"(1u) : "memory");
                    STAMP(27);
                    lead = LEAD * PACE * 4;
                }
                    if (h == 6) mb = ld4_sc1(ws, WS_MB + (unsigned)((gi * 8 + (gi & 7)) * 128 + 32));
                    if (h == 8) {
                        WAIT1(mb, 6);
                        const unsigned r = __builtin_amdgcn_readfirstlane(mb);
                        if (r) { adopt(r); STAMP(29); }
                    }
                    asm volatile("; COND_END");
                }
                if constexpr (h < 6) {
                    constexpr int i = 2 * h;
                    fix_k1(IC<i>{}, IC<20 - 6 * (i / 3) - i % 3 + NV>{});
                    fix_k1(IC<i + 1>{}, IC<20 - 6 * ((i + 1) / 3) - (i + 1) % 3 + NV>{});
                }
                if (h == 5) {
                    STAMP(1);
                    __builtin_amdgcn_s_setprio(0);
                    att_qk0();
                }
            };
            v_group(IC<0>{}); v_group(IC<1>{}); v_group(IC<2>{}); v_group(IC<3>{}); v_group(IC<4>{}); v_group(IC<5>{});
            v_group(IC<6>{}); v_group(IC<7>{}); v_group(IC<8>{}); v_group(IC<9>{}); v_group(IC<10>{}); v_group(IC<11>{});
            lds_add(16, 1u);
            if (wave == 3) lds_add(44, WEARLY);
        }
    }
    {
        STAMP(28);
        att_addr();
        const int lane = lane_id();
        cma = lb + L_M; cxa = lb + L_M + (unsigned)((lane >> 4) * 8); cra = lb + L_OP + (unsigned)(512 + (lane >> 4) * 2048 + (lane & 15) * 16);
        cso = WS_OWN + (unsigned)(gi * 16384 + gj * 512 + 256 + (lane >> 5) * 64 + (lane & 12) * 4 + (lane & 1) * 8192);
        PIN2(cma, cxa); PIN2(cra, cso);
        att_pv0();
    }
    {
        f32x4 m0, m1, o0, o1; f32x2 mm; unsigned r;
        asm volatile("s_mov_b64 exec, 1\n\tds_add_rtn_u32 %5, %6, %7 offset:%10\n\ts_mov_b64 exec, -1\n\t"
                     "ds_read_b128 %0, %6\n\tds_read_b128 %1, %6 offset:16\n\tds_read_b64 %2, %8\n\tds_read_b128 %3, %9\n\tds_read_b128 %4, %9 offset:1024\n\ts_waitcnt lgkmcnt(0)"
                     : "=&v"(m0), "=&v"(m1), "=&v"(mm), "=&v"(o0), "=&v"(o1), "=&v"(r)
                     : "v"(cma), "v"(1u), "v"(cxa), "v"(cra), "i"(L_CNT + 28 - L_M) : "memory");
        ord = __builtin_amdgcn_readfirstlane(r);
        if (ord == 7) { STAMP(5); comb_o1(m0, m1, mm, o0, o1, cso, 0x0000333300003333ull, true); lds_set(lb + L_CNT + 48, now() | 1u); STAMP(21); }
        MARK(e_t5);
        const int lane = lane_id();
        auto wait_cnt = [&](int off, unsigned v) __attribute__((always_inline)) {
#pragma unroll 1
            for (unsigned spins = 0; spins < (kSpinCap << 4); ++spins) { if (lds_word(lb + L_CNT + off) >= v) break; __builtin_amdgcn_s_sleep(1); }
        };
        if (ord == OML) {
            wait_cnt(16, 8u);
            unsigned xa, ra; ml_addr(lane, xa, ra);
            comb_ml(cma, xa, ra, WS_OWN + (unsigned)(gi * 16384 + gj * 512 + 384 + (lane & 1) * 8192), true);
            STAMP(24);
        } else if (ord == 0 && !lds_word(lb + L_CNT + 52)) {
            if (gi + gj == 0 && !lds_word(lb + L_CNT + 36)) {
                unsigned r = 0u;
#pragma unroll 1
                for (unsigned spins = 0; spins < kSpinCap; ++spins) {
                    r = ld4_sc1(ws, WS_R + (unsigned)((lane & 31) * 4));
                    WAIT1(r, 0);
                    if (all_set(r)) break;
                    if ((spins & 7u) == 7u) send_flag(lane);
                }
                answer(lane, r);
            }
#pragma unroll 1
            for (unsigned spins = 0; spins < kSpinCap; ++spins) {
                unsigned r = ld4_sc1(ws, WS_MB + (unsigned)((gi * 8 + (gi & 7)) * 128 + 32));
                WAIT1(r, 0);
                r = __builtin_amdgcn_readfirstlane(r);
                if (r) { adopt(r); break; }
                if ((spins & 7u) == 7u) { send_flag(lane); report(); }
                __builtin_amdgcn_s_sleep(16);
            }
            STAMP(29);
        } else if (ord == OML + 1) {
#pragma unroll 1
            for (unsigned spins = 0; spins < (kSpinCap << 6); ++spins) if (lds_word(lb + L_CNT + 24) >= 8u) break;
            const unsigned ra = lb + L_OP + (unsigned)((lane >> 5) * 4096 + (lane & 31) * 16);
            comb_o0(*(const LAS f32x4*)(size_t)cma, *(const LAS f32x4*)(size_t)(cma + 16), *(const LAS f32x4*)(size_t)(cma + (lane >> 5) * 16), *(const LAS f32x4*)(size_t)ra, *(const LAS f32x4*)(size_t)(ra + 1024),
                    *(const LAS f32x4*)(size_t)(ra + 2048), *(const LAS f32x4*)(size_t)(ra + 3072), WS_OWN + (unsigned)(gi * 16384 + gj * 512 + (lane & 60) * 4 + (lane & 1) * 8192), 0x3333333333333333ull, true);
            STAMP(22);
        }
        if (ord == EMODE) {
#pragma unroll 1
            for (unsigned spins = 0; spins < (kSpinCap << 8); ++spins) { if (lds_word(lb + L_CNT + 52)) break; __builtin_amdgcn_s_sleep(1); }
            const unsigned S = lds_word(lb + L_CNT + 80);
            empty4(0, S, lane); empty5(5, 0u, lane); empty5(5, 8192u, lane);
        }
#pragma unroll 1
        for (;;) {
            unsigned j;
            asm volatile("s_mov_b64 exec, 1\n\tds_add_rtn_u32 %0, %1, %2\n\ts_mov_b64 exec, -1\n\ts_waitcnt lgkmcnt(0)" : "=&v"(j) : "v"(lb + L_CNT + 40), "v"(1u) : "memory");
            j = __builtin_amdgcn_readfirstlane(j);
            if (j >= 5u - WEARLY) break;
            load_wo((int)j, lane, false); ++nj;
        }
        STAMP(23);
    }

    unsigned omr, owa, owb;
    auto op_addr = [&](int layer) __attribute__((always_inline)) {
        LANE_VARS();
        const int t = wave - 3;
        cma = WS_OWN + rS + (unsigned)(gi * 16384 + layer * 4096 + (lane >> 3) * 512 + t * 128 + (t == 3 ? 0 : (lane & 7) * 16));
        cxa = lb + L_O2 + (unsigned)((lane >> 3) * 416 + t * 128 + (t == 3 ? 0 : (lane & 7) * 16));
        const int pb = (lane & 4) ? 3 - p4 : p4, jp = 2 * pb + ((lane >> 2) & 1);
        cso = tile35(gj, 10 * layer + t) + (unsigned)((kq & 1) * 8192 + gi * 128 + (kq >> 1) * 64 + p4 * 16);
        cra = lb + L_O2 + (unsigned)(jp * 416 + kq * 16); omr = lb + L_O2 + (unsigned)(jp * 416 + 384);
        owa = lb + L_WO + (unsigned)(r0s * 160 + 32 * t + 8 * p4); owb = lb + L_WO + (unsigned)(r1s * 160 + 32 * t + 8 * p4);
        if (layer) PIN2(cma, cxa);
        PIN2(cso, cra); PIN3(omr, owa, owb);
    };
    auto chain = [&](auto LC) __attribute__((always_inline)) {
        constexpr int layer = decltype(LC)::value;
        unsigned h4 = WS_H4 + (unsigned)((layer * 32 + gi) * 32768) + (layer ? rS ^ 8192u : 0u);
        auto sides = [&]() __attribute__((always_inline)) { rS = lds_word(lb + L_CNT + 80); nS = lds_word(lb + L_CNT + 84); h4 += rS ^ 8192u; };
        if (wave < 3) {
            LANE_VARS();
            if (layer == 0) {
                const int l0 = wave == 0 ? 2 : wave - 1;
                const unsigned lo = WS_OWN + lds_word(lb + L_CNT + 84) + (unsigned)(gi * 16384 + (lane >> 3) * 512), lw = lb + L_O2 + (unsigned)((lane >> 3) * 416);
                unsigned lo0 = lo + (unsigned)(l0 * 128 + (lane & 7) * 16), lo1 = wave == 1 ? lo + 384 : lo0, lw0 = lw + (unsigned)(l0 * 128 + (lane & 7) * 16), lw1 = wave == 1 ? lw + 384 : lw0;
                PIN2(lo0, lo1); PIN2(lw0, lw1);
                GUARD(600);
                MARK(b_g2);
                unsigned tf = 0u;
#pragma unroll 1
                for (unsigned spins = 0; spins < (kSpinCap << 4); ++spins) { tf = lds_word(lb + L_CNT + 48); if (tf) break; }
                if (wave == 0) until(tf + DG2);
#pragma unroll 1
                for (unsigned spins = 0; spins < kSpinCap; ++spins) {
                    u32x4 o0 = ld16_sc1(ws, lo0), o1;
                    if (wave == 1) o1 = ld16_sc1(ws, lo1); else asm volatile("" : "=v"(o1));
                    if (nj) { if (wave == 1) VMW(2); else VMW(1); lds_add(44, nj); nj = 0u; }
                    asm volatile("s_waitcnt vmcnt(0)" : "+v"(o0), "+v"(o1) :: "memory");
                    if (wave != 1) o1 = o0;
                    *(LAS u32x4*)(size_t)lw0 = o0; *(LAS u32x4*)(size_t)lw1 = o1;
                    if (!any_empty(max(um4(o0), um4(o1)))) break;
                    if ((spins & 15u) == 15u) send_flag(lane);
                }
                MARK(e_g2);
                STAMP(25);
                BAR();
                sides();
                if (wave < 2) empty1(wave, rS, lane);
                else {
                    st16m(ws, WS_CS + (unsigned)((gi * 8 + gj) * 768 + lane * 16), *reinterpret_cast<const u32x4*>(smem + L_CS + lane * 16), 0xffffffffffffull);
                    empty5(10, 0u, lane); empty5(10, 8192u, lane);
                }
            } else BAR();
            {
                const unsigned im = lb + L_GU + (unsigned)(32 * wave + 8 * p4);
                Frag80 fg = frag80(im, 96, r0s, r1s, r2s), fu = frag80(im + 8192, 96, r0s, r1s, r2s);
                const unsigned xb = lb + L_XS + (unsigned)((n < 4 ? n : 4) * 160);
                unsigned b16 = xb + kq * 16, b8 = xb + 128 + kq * 8;
                unsigned sg = h4 + (unsigned)(n < 4 ? h4s(wave) + gj * 512 + kq * 64 + n * 16 : H4C + wave * 1024 + gj * 128 + kq * 16), su = sg + (n < 4 ? 256u : 64u);
                unsigned o = h4 + h4s(wave) + (unsigned)(lane * 16);
                unsigned oc = h4 + (unsigned)(H4C + wave * 1024 + ((lane >> 5) * 4 + p4) * 128 + (kq & 1) * 64 + q4 * 16);
                unsigned os = h4 + (unsigned)(H4T + (q4 + 4 * (kq & 1)) * 128 + p4 * 16);
                unsigned ha = lb + L_H + (unsigned)(p4 * 96 + wave * 32 + q4 * 8 + kq * 2);
                f32x2 cs = {0.f, 0.f};
                if (layer != 0) cs = *reinterpret_cast<const f32x2*>(smem + L_CS2 + wave * 512 + lane * 8);
                PIN3(fg.a0, fg.a1, fg.a2); PIN3(fu.a0, fu.a1, fu.a2); PIN3(b16, b8, sg); PIN3(su, o, os); PIN2(ha, cs);
                unsigned long long mk = layer ? 0x000f000f000f000full : 0x001f001f001f001full;
                unsigned ta = WS_MB + (unsigned)((lane >> 1) * 1024 + 64 + (lane & 1) * 16);
                if (layer == 0) PIN2(oc, ta);
                PINS(mk);
                if (layer == 0) VMW(0);
                if (layer == 0) GUARD(1200); else GUARD(800);
                MARK(b_gu);
                BAR();
                STAMP(10 + 32 * layer);
                u32x4 tl = {0u, 0u, 0u, 0u};
                if (layer == 0) tl = ld16_sc1(ws, ta);
                const Frag80 b = rows80(b16, b8);
                const f32x4 g0 = mfma32(fg.a0, b.a0, z4), u0 = mfma32(fu.a0, b.a0, z4), g1 = mfma32(fg.a1, b.a1, g0), g2 = mfma16(fg.a2, b.a2, z4), u1 = mfma32(fu.a1, b.a1, u0), u2 = mfma16(fu.a2, b.a2, z4);
                __builtin_amdgcn_sched_barrier(0);
                const f32x4 cg = g1 + g2, cu = u1 + u2;
                st16m2<0>(ws, sg, asu(cg), su, asu(cu), mk);
                STAMP(11 + 32 * layer);
                if (layer == 0) {
                    WAIT1(tl, 2);
#pragma unroll 1
                    for (unsigned spins = 0; spins < kSpinCap && !all_set(min(min(tl[0], tl[1]), min(tl[2], tl[3]))); ++spins) {
                        tl = ld16_sc1(ws, ta);
                        WAIT1(tl, 0);
                    }
                    const unsigned tn = now();
                    unsigned d = min(min(min(tn - tl[0], tn - tl[1]), min(tn - tl[2], tn - tl[3])), 800u);
                    d = min(d, dppu<0xB1>(d)); d = min(d, dppu<0x4E>(d)); d = min(d, dppu<0x124>(d)); d = min(d, dppu<0x128>(d));
                    unsigned tr = tn - min(min((unsigned)__builtin_amdgcn_readlane(d, 0), (unsigned)__builtin_amdgcn_readlane(d, 16)), min((unsigned)__builtin_amdgcn_readlane(d, 32), (unsigned)__builtin_amdgcn_readlane(d, 48)));
                    lds_set(lb + L_CNT + 56, tr | 1u);
                                        until(__builtin_amdgcn_readfirstlane(tr) + X4A);
                } else sched(X4, 0);
                STAMP(16 + 32 * layer);
                float hv; f32x2 st;
                u32x4 a0, a1, a2, a3, ac, as;
                auto load = [&]() __attribute__((always_inline)) {
                    as = ld16_sc1(ws, os);
                    if (layer == 0) ac = ld16_sc1(ws, oc);
                    a0 = ld16_sc1<0>(ws, o); a1 = ld16_sc1<1024>(ws, o); a2 = ld16_sc1<2048>(ws, o); a3 = ld16_sc1<3072>(ws, o);
                };
                auto all_in = [&]() __attribute__((always_inline)) {
                    if (layer == 0) asm volatile("s_waitcnt vmcnt(0)" : "+v"(a0), "+v"(a1), "+v"(a2), "+v"(a3), "+v"(ac), "+v"(as) :: "memory");
                    else { asm volatile("s_waitcnt vmcnt(0)" : "+v"(a0), "+v"(a1), "+v"(a2), "+v"(a3), "+v"(as) :: "memory"); ac = as; }
                };
                auto compute = [&]() __attribute__((always_inline)) {
                    float ss, sm;
                    WAIT1(as, layer ? 4 : 5);
                    asm volatile("v_add_f32_dpp %0, %2, %2 row_ror:8 row_mask:0xf bank_mask:0xf\n\tv_add_f32_dpp %1, %3, %3 row_ror:8 row_mask:0xf bank_mask:0xf\n\ts_nop 0\n\t"
                                 "v_add_f32_dpp %0, %0, %0 row_ror:4 row_mask:0xf bank_mask:0xf\n\tv_add_f32_dpp %1, %1, %1 row_ror:4 row_mask:0xf bank_mask:0xf\n\ts_nop 0"
                                 : "=&v"(ss), "=&v"(sm) : "v"(as[0]), "v"(as[1]));
                    ss = x16_add(ss); sm = x16_add(sm);
                    float mean = sm * (1.f / DM), var = fmax2(__builtin_fmaf(-mean, mean, ss * (1.f / DM)), 0.f);
                    PIN2(mean, var);
                    if (layer == 0) {
                        WAIT1(ac, 4);
                        cs = sw16(rs32(asf(ac)[0], asf(ac)[2]), rs32(asf(ac)[1], asf(ac)[3]));
                        cs[0] += dpp<0xB1>(cs[0]); cs[1] += dpp<0xB1>(cs[1]); cs[0] += dpp<0x4E>(cs[0]); cs[1] += dpp<0x4E>(cs[1]);
                        PIN1(cs);
                    }
                    all_in();
                    st = (f32x2){bfr(mean), frcp(bfr(__builtin_amdgcn_sqrtf(bfr(bfr(var) + 1e-5f))))};
                    const f32x4 t = (asf(a0) + asf(a1)) + (asf(a2) + asf(a3));
                    const f32x2 gu = sw16(rs32(t[0], t[2]), rs32(t[1], t[3]));
                    const f32x2 gn = (gu - st[0] * cs) * st[1];
                    const float gv = bfr(gn[0]);
                    hv = bfr(gv * frcp(1.f + ex2(gv * -LOG2E))) * bfr(gn[1]);
                    *(LAS u16*)(size_t)ha = (u16)pk2(hv, hv);
                    __builtin_amdgcn_sched_barrier(0);
                };
                RETRY(load(), (all_in(), max(max(max(um4(a0), um4(a1)), max(um4(a2), um4(a3))), max(um4(ac), um4(as)))), compute(), any_nan(hv, hv), layer ? X4 : X4A);
                BAR();
                MARK(e_gu);
                STAMP(12 + 32 * layer);
                if (layer == 0) {
                    *reinterpret_cast<f32x2*>(smem + L_CS2 + wave * 512 + lane * 8) = cs;
                }
            }
            if (layer == 0) {
                if (wave == 1) {
                    const unsigned o = WS_CS + (unsigned)(gi * 6144 + 3072 + (lane < 48 ? lane : 0) * 16);
                    u32x4 a[8];
                    GATHER8(a, o, 768);
                    WAIT8(a, 0);
                    if (lane < 48) *reinterpret_cast<f32x4*>(smem + L_CS + lane * 16) = SUM8(a);
                }
                Frag80 f[4];
#pragma unroll
                for (int i = 0; i < 4; ++i) { f[i] = frag80(lb + L_WQ + (unsigned)(128 * wave + 32 * i + 8 * p4), 416, r0s, r1s, r2s); PIN3(f[i].a0, f[i].a1, f[i].a2); }
                unsigned b16 = lb + L_XS + (unsigned)(q4 * 160 + kq * 16), b8 = lb + L_XS + (unsigned)(q4 * 160 + 128 + kq * 8);
                unsigned so = WS_H1 + rS + (unsigned)((gi * 4 + q4) * 16384 + gj * 896 + 256 * wave + kq * 64 + p4 * 16);
                unsigned long long mk = 0x1111111111111111ull;
                PIN3(b16, b8, so); PINS(mk);
                GUARD(3800);
                MARK(b_q);
                BAR();
                STAMP(33);
                const Frag80 b = rows80(b16, b8);
                st16_sc1(ws, so, tiles4(f, b, mk));
                STAMP(34);
                qk_addr(true); att_addr();
                cma = lb + L_M; cxa = lb + L_M + (unsigned)(wave == 0 ? (lane >> 5) * 16 : (lane >> 4) * 8);
                cra = lb + L_OP + (unsigned)(wave == 0 ? (lane >> 5) * 4096 + (lane & 31) * 16 : 512 + (lane >> 4) * 2048 + (lane & 15) * 16);
                if (wave == 2) ml_addr(lane, cxa, cra);
                cso = WS_OWN + rS + (unsigned)(gi * 16384 + 4096 + gj * 512) + (unsigned)(wave == 0 ? lane * 4 : wave == 1 ? 256 + (lane >> 5) * 64 + (lane & 15) * 4 : 384);
                PIN2(cma, cxa); PIN2(cra, cso);
                asm volatile("" : "=v"(omr), "=v"(owa), "=v"(owb));
            }
        } else {
            const int t = wave - 3;
            StatAddr sa;
            u32x4 sel;
            unsigned long long hi = 0xff00ff00ff00ff00ull;
            auto gather32 = [&](unsigned o, unsigned X, auto fin) __attribute__((always_inline)) {
                u32x4 a0 = ld16_sc1<-2048>(ws, o), a1 = ld16_sc1<-1024>(ws, o), a2 = ld16_sc1<0>(ws, o), a3 = ld16_sc1<1024>(ws, o);
                float v;
                auto sum = [&]() __attribute__((always_inline)) {
                    asm volatile("s_waitcnt vmcnt(3)\n\tv_mfma_f32_16x16x32_bf16 v[40:43], %1, %5, 0\n\ts_waitcnt vmcnt(2)\n\tv_mfma_f32_16x16x32_bf16 v[40:43], %2, %5, v[40:43]\n\t"
                                 "s_waitcnt vmcnt(1)\n\tv_mfma_f32_16x16x32_bf16 v[40:43], %3, %5, v[40:43]\n\ts_waitcnt vmcnt(0)\n\tv_mfma_f32_16x16x32_bf16 v[40:43], %4, %5, v[40:43]\n\t"
                                 "s_nop 7\n\tv_cndmask_b32 v40, v40, v41, %6\n\tv_cndmask_b32 v42, v42, v43, %6\n\ts_nop 1\n\tv_permlane32_swap_b32 v40, v42\n\tv_add_f32 v40, v40, v42\n\t"
                                 "v_cvt_pk_bf16_f32 %0, 0, v40"
                                 : "=&v"(v), "+v"(a0), "+v"(a1), "+v"(a2), "+v"(a3) : "v"(sel), "s"(hi) : "memory", "v40", "v41", "v42", "v43");
                    fin(v);
                    __builtin_amdgcn_sched_barrier(0);
                };
                sum();
                if (__builtin_expect(any_nan(v, v) != 0ull, 0)) {
#pragma unroll 1
                    for (unsigned spins = 0; spins < kSpinCap; ++spins) {
                        slip(X);
                        again16<-2048>(a0, ws, o); again16<-1024>(a1, ws, o); again16<0>(a2, ws, o); again16<1024>(a3, ws, o);
                        asm volatile("s_waitcnt vmcnt(0)" : "+v"(a0), "+v"(a1), "+v"(a2), "+v"(a3) :: "memory");
                        if (!any_empty(max(max(um4(a0), um4(a1)), max(um4(a2), um4(a3))))) break;
                    }
                    sum();
                }
            };
#define PUT8(A, B, M) "v_min_u32 v34, -2, v32\n\tv_min_u32 v32, -2, v32\n\tv_min_u32 v35, -2, v33\n\tv_min_u32 v33, -2, v33\n\tv_permlane16_swap_b32 v32, v34\n\ts_nop 0\n\t" \
                      "v_permlane16_swap_b32 v33, v35\n\ts_mov_b64 exec, " M "\n\tglobal_store_dwordx4 " A ", v[32:35], " B " sc1\n\ts_mov_b64 exec, -1\n\ts_nop 0"
            unsigned xw, qo, qw, qc;
            __builtin_amdgcn_s_setprio(0);
            {
                STAMP(6 + 32 * layer);
                bf16x8 af[6];
                unsigned long long mk = 0x000f000f000f000full;
                if (layer == 0) {
                    const int lane = lane_id();
                    xe = lo16(*reinterpret_cast<const u16*>(smem + L_X0 + xrow(lane) * 1280 + gj * 160 + t * 32 + xcol(lane) * 2));
                    op_addr(0);
                    GUARD(1500);
                    MARK(b_op);
                    if (nj | (ord == EMODE)) { VMW(0); lds_add(44, nj); }
#pragma unroll 1
                    for (unsigned spins = 0; spins < (kSpinCap << 4); ++spins) if (lds_word(lb + L_CNT + 44) >= 5u) break;
                    STAMP(8);
                } else MARK(b_op);
                const unsigned lo = cma, lw = cxa, so = cso, br = cra, mr = omr, h3 = tile35(gj, 10 * layer + t);
                unsigned go, xs;
#pragma unroll
                for (int s = 0; s < 6; ++s) af[s] = cat8(tr64o(owa, s * 32 * 160), tr64o(owb, s * 32 * 160));
                PINS(mk); PINS(hi);
                if (layer != 0 && t < 4) {
                    sched(X2, 0);
                    STAMP(49);
                    u32x4 ov = ld16_sc1(ws, lo);
                    WAIT1(ov, 0);
                    *(LAS u32x4*)(size_t)lw = ov;
                    if (__builtin_expect(any_empty(um4(ov)), 0)) {
#pragma unroll 1
                        for (unsigned spins = 0; spins < kSpinCap; ++spins) {
                            slip(X2);
                            ov = ld16_sc1(ws, lo);
                            WAIT1(ov, 0);
                            *(LAS u32x4*)(size_t)lw = ov;
                            if (!any_empty(um4(ov))) break;
                        }
                    }
                }
                {
                    bf16x8 b0, b1, b2, b3, b4, b5;
#define QP " row_half_mirror row_mask:0xf bank_mask:0xf\n\t"
                    asm volatile("s_waitcnt lgkmcnt(0)\n\ts_barrier\n\t"
                                 "ds_read_b128 %0, %12\n\tds_read_b64 v[36:37], %13\n\tds_read_b128 %1, %12 offset:64\n\tds_read_b128 %2, %12 offset:128\n\tds_read_b128 %3, %12 offset:192\n\t"
                                 "ds_read_b128 %4, %12 offset:256\n\tds_read_b128 %5, %12 offset:320\n\t"
                                 "s_waitcnt lgkmcnt(5)\n\tv_mfma_f32_16x16x32_bf16 v[32:35], %6, %0, 0\n\tv_max_f32_dpp v38, v36, v36" QP "v_sub_f32 v38, v36, v38\n\t"
                                 "s_waitcnt lgkmcnt(4)\n\tv_mfma_f32_16x16x32_bf16 v[32:35], %7, %1, v[32:35]\n\tv_exp_f32 v38, v38\n\t"
                                 "s_waitcnt lgkmcnt(3)\n\tv_mfma_f32_16x16x32_bf16 v[32:35], %8, %2, v[32:35]\n\tv_mul_f32 v37, v37, v38\n\t"
                                 "s_waitcnt lgkmcnt(2)\n\tv_mfma_f32_16x16x32_bf16 v[32:35], %9, %3, v[32:35]\n\tv_add_f32_dpp v39, v37, v37" QP
                                 "s_waitcnt lgkmcnt(1)\n\tv_mfma_f32_16x16x32_bf16 v[32:35], %10, %4, v[32:35]\n\tv_rcp_f32 v39, v39\n\t"
                                 "s_waitcnt lgkmcnt(0)\n\tv_mfma_f32_16x16x32_bf16 v[32:35], %11, %5, v[32:35]\n\tv_mul_f32 v38, v38, v39\n\ts_nop 6\n\t"
                                 "v_pk_mul_f32 v[32:33], v[32:33], v[38:39] op_sel_hi:[1,0]\n\tv_pk_mul_f32 v[34:35], v[34:35], v[38:39] op_sel_hi:[1,0]\n\ts_nop 0\n\t"
                                 "v_add_f32_dpp v32, v32, v32" QP "v_add_f32_dpp v33, v33, v33" QP "v_add_f32_dpp v34, v34, v34" QP "v_add_f32_dpp v35, v35, v35" QP
                                 "v_cvt_pk_bf16_f32 v32, v32, v33\n\tv_cvt_pk_bf16_f32 v33, v34, v35\n\t" PUT8("%14", "%15", "%16")
                                 : "=&v"(b0), "=&v"(b1), "=&v"(b2), "=&v"(b3), "=&v"(b4), "=&v"(b5)
                                 : "v"(af[0]), "v"(af[1]), "v"(af[2]), "v"(af[3]), "v"(af[4]), "v"(af[5]), "v"(br), "v"(mr), "v"(so), "s"(ws), "s"(mk)
                                 : "memory", "v32", "v33", "v34", "v35", "v36", "v37", "v38", "v39");
#undef QP
                }
                if (layer == 0 && wave == 7) st4_sc1(ws, WS_MB + (unsigned)(gi * 1024 + 64 + gj * 4), now() | 1u);
                const unsigned tp = layer ? 0u : now();
                if (layer == 0) sides();
                STAMP(7 + 32 * layer);
                {
                    const int ln = lane_id();
#pragma unroll
                    for (int k = 0; k < 4; ++k) sel[k] = ((ln >> 1) & 3) == k ? (ln & 1) ? 0x3f800000u : 0x3f80u : 0u;
                    go = h3 + nS + (unsigned)(2048 + ln * 16); xs = lb + L_XS + (unsigned)(xrow(ln) * 160 + t * 32 + xcol(ln) * 2);
                    PIN3(sel, go, xs);
                    sa = stats_addr(ln, h4 + (unsigned)(H4T + gj * 128), 16);
                }
                if (layer != 0) sched(X3, 0);
                else until(tp + W3);
                STAMP(18 + 32 * layer);
                if (layer != 0) gather32(go, X3, [&](float s) { xw = pk2(xe + s, 0.f); *(LAS u16*)(size_t)xs = (u16)xw; });
                else {
                    unsigned long long m0, m1, m2, m3; unsigned cnt = kSpinCap;
#define CHK(r, m) "v_max3_u32 v44, v" #r ", v[" #r "+1], v[" #r "+2]\n\tv_max_u32 v44, v44, v[" #r "+3]\n\tv_cmp_eq_u32 %" #m ", -1, v44\n\t"
                    asm volatile("s_mov_b64 %1, -1\n\ts_mov_b64 %2, -1\n\ts_mov_b64 %3, -1\n\ts_mov_b64 %4, -1\n"
                                 "1:\n\ts_mov_b64 exec, %1\n\tglobal_load_dwordx4 v[0:3], %6, %7 offset:-2048 sc1\n\ts_mov_b64 exec, %2\n\tglobal_load_dwordx4 v[4:7], %6, %7 offset:-1024 sc1\n\t"
                                 "s_mov_b64 exec, %3\n\tglobal_load_dwordx4 v[8:11], %6, %7 sc1\n\ts_mov_b64 exec, %4\n\tglobal_load_dwordx4 v[12:15], %6, %7 offset:1024 sc1\n\ts_mov_b64 exec, -1\n\t"
                                 "s_sub_u32 %5, %5, 1\n\ts_cselect_b64 vcc, 0, -1\n\ts_waitcnt vmcnt(0)\n\t"
                                 "v_mfma_f32_16x16x32_bf16 v[40:43], v[0:3], %8, 0\n\t" CHK(0, 1) "v_mfma_f32_16x16x32_bf16 v[40:43], v[4:7], %8, v[40:43]\n\t" CHK(4, 2)
                                 "v_mfma_f32_16x16x32_bf16 v[40:43], v[8:11], %8, v[40:43]\n\t" CHK(8, 3) "v_mfma_f32_16x16x32_bf16 v[40:43], v[12:15], %8, v[40:43]\n\t" CHK(12, 4)
                                 "s_or_b64 exec, %1, %2\n\ts_or_b64 exec, exec, %3\n\ts_or_b64 exec, exec, %4\n\ts_and_b64 vcc, exec, vcc\n\ts_cbranch_scc1 1b\n\t"
                                 "s_mov_b64 exec, -1\n\tv_cndmask_b32 v40, v40, v41, %9\n\tv_cndmask_b32 v42, v42, v43, %9\n\ts_nop 1\n\tv_permlane32_swap_b32 v40, v42\n\tv_add_f32 v40, v40, v42\n\t"
                                 "v_cvt_pk_bf16_f32 v40, 0, v40\n\tv_add_f32 v40, %10, v40\n\tv_cvt_pk_bf16_f32 %0, v40, 0\n\tds_write_b16 %11, %0"
                                 : "=&v"(xw), "=&s"(m0), "=&s"(m1), "=&s"(m2), "=&s"(m3), "+s"(cnt) : "v"(go), "s"(ws), "v"(sel), "s"(hi), "v"(xe), "v"(xs)
                                 : "memory", "vcc", "scc", "v0", "v1", "v2", "v3", "v4", "v5", "v6", "v7", "v8", "v9", "v10", "v11", "v12", "v13", "v14", "v15", "v40", "v41", "v42", "v43", "v44");
#undef CHK
                }
                xe = lo16(xw);
                STAMP(9 + 32 * layer);
            }
            BAR();
            if (wave == 3) stats(sa);
            MARK(e_op);
            STAMP(11 + 32 * layer);
            {
                LANE_VARS();
                const unsigned im = lb + L_WD + (unsigned)(32 * t + 8 * p4);
                bf16x8 wd0 = cat8(tr64(im + r0s * 160), tr64(im + r1s * 160)); bf16x4 wd1 = tr64(im + (32 + r2s) * 160);
                const unsigned h5 = tile35(gj, 10 * layer + 5 + t);
                unsigned h16 = lb + L_H + (unsigned)(p4 * 96 + kq * 16), h8 = lb + L_H + (unsigned)(p4 * 96 + 64 + kq * 8);
                unsigned so = h5 + (unsigned)((kq & 1) * 8192 + gi * 128 + (kq >> 1) * 64 + p4 * 16);
                unsigned long long mk = 0x000f000f000f000full;
                unsigned go = h5 + nS + (unsigned)(2048 + lane * 16), xs = (layer ? (unsigned)(xrow(lane) * 1280 + gj * 160) : lb + L_XS + (unsigned)(xrow(lane) * 160)) + (unsigned)(t * 32 + xcol(lane) * 2);
                unsigned sta = lb + L_ST + (unsigned)(xrow(lane) * 16);
                if (layer == 0 && wave == 4) {
                    asm volatile("" : "=v"(sa.a16), "=v"(sa.b16), "=v"(sa.a8), "=v"(sa.b8), "=v"(sa.dst), "=v"(qo), "=v"(qw), "=v"(qc), "=v"(cma), "=v"(cxa), "=v"(cra), "=v"(cso), "=v"(omr), "=v"(owa), "=v"(owb));
                    asm volatile("" : "=v"(qa), "=v"(pw), "=v"(ma), "=v"(idx), "=v"(qm), "=v"(pr), "=v"(oa), "=v"(oc));
                } else if (layer == 0) {
                    sa = stats_addr(lane, WS_H1 + rS + (unsigned)(gi * 65536 + gj * 896 + 768), 16384);
                    op_addr(1); qk_addr(false); att_addr();
                    const int l5 = lane & 31, c = l5 < 24 ? 24 * (wave - 6) + l5 : 48;
                    qo = WS_H1 + rS + (unsigned)((gi * 4 + (gj >> 1)) * 16384 + 3584 + (lane >> 5) * 896 + c * 16);
                    qw = lb + L_Q + (unsigned)(l5 < 24 ? (c >> 4) * 128 + (c & 3) * 32 + (c & 12) + (lane >> 5) * 16 : lane < 26 ? 384 + (lane & 1) * 16 : 448);
                    qc = lb + L_CS + (unsigned)(c * 16 + (lane >> 5) * 8);
                    PIN3(qo, qw, qc);
                }
                PIN2(wd0, wd1); PIN3(h16, h8, so); PIN3(go, xs, sta); PINS(mk);
                GUARD(2000);
                MARK(b_dn);
                if (wave == 4) {
                    unsigned os = h4 + (unsigned)(H4T + (p4 + 4 * (kq & 1)) * 128 + q4 * 16);
                    float sw = *reinterpret_cast<const float*>(smem + L_CNT + 12);
                    PIN2(os, sw);
                    if (layer == 0) {
#pragma unroll 1
                        for (unsigned spins = 0; spins < (kSpinCap << 4); ++spins) if (lds_word(lb + L_CNT + 56)) break;
                    }
                    sched(layer ? X4 : X4A, 0);
                    u32x4 as;
#pragma unroll 1
                    for (unsigned spins = 0; spins < kSpinCap; ++spins) { as = ld16_sc1(ws, os); WAIT1(as, 0); if (!any_empty(um4(as))) break; }
                    float ss = __uint_as_float(as[0]), sm = __uint_as_float(as[1]), rd = __uint_as_float(as[2]);
                    ss += dpp<0x4E>(ss); sm += dpp<0x4E>(sm); rd += dpp<0x4E>(rd);
                    ss += dpp<0xB1>(ss); sm += dpp<0xB1>(sm); rd += dpp<0xB1>(rd);
                    ss = x16_add(ss); sm = x16_add(sm); rd = x16_add(rd);
                    if (LANES(0x1111ull)) *reinterpret_cast<f32x4*>(smem + L_ST + q4 * 16) = (f32x4){ss, sm, rd, sw};
                }
                if (layer == 0 && wave > 4) {
#pragma unroll 1
                    for (unsigned spins = 0; spins < (kSpinCap << 4); ++spins) { if (lds_word(lb + L_CNT + 56)) break; __builtin_amdgcn_s_sleep(1); }
                    sched(X4A + 5, 0);
                    if (wave == 5) empty4(1, rS, lane);
                    else st16m(ws, lane < 40 ? tile35(gj, 15 + (lane >> 3)) + (unsigned)((wave - 6) * 8192 + gi * 128 + (lane & 7) * 16)
                                             : WS_OWN + rS + (unsigned)(gi * 16384 + 4096 + gj * 512 + (wave - 6) * 256 + (lane - 40) * 16), (u32x4){~0u, ~0u, ~0u, ~0u}, 0xffffffffffffffull);
                }
                asm volatile("s_waitcnt lgkmcnt(0)\n\ts_barrier\n\tds_read_b128 v[36:39], %2\n\tds_read_b64 v[40:41], %3\n\t"
                             "s_waitcnt lgkmcnt(1)\n\tv_mfma_f32_16x16x32_bf16 v[32:35], %0, v[36:39], 0\n\ts_waitcnt lgkmcnt(0)\n\tv_mfma_f32_16x16x16_bf16 v[42:45], %1, v[40:41], 0\n\ts_nop 7\n\t"
                             "v_pk_add_f32 v[32:33], v[32:33], v[42:43]\n\tv_pk_add_f32 v[34:35], v[34:35], v[44:45]\n\t"
                             "v_cvt_pk_bf16_f32 v32, v32, v33\n\tv_cvt_pk_bf16_f32 v33, v34, v35\n\t" PUT8("%4", "%5", "%6")
                             :: "v"(wd0), "v"(wd1), "v"(h16), "v"(h8), "v"(so), "s"(ws), "s"(mk)
                             : "memory", "v32", "v33", "v34", "v35", "v36", "v37", "v38", "v39", "v40", "v41", "v42", "v43", "v44", "v45");
                STAMP(13 + 33 * layer);
                if (layer == 0 || gi == 4 * t + (gj & 3)) {
                    const f32x4 sr = *(const LAS f32x4*)(size_t)sta;
                    const f32x2 st = ln_stats(sr[0], sr[1]);
                    float rs = bfr(frcp(1.f + ex2(bfr((sr[2] - st[0] * sr[3]) * st[1]) * -LOG2E))), n1 = bfr(bfr(xe - st[0]) * st[1]);
                    PIN2(rs, n1);
                    if (layer == 0) {
                        sched(X5A, 0);
                        STAMP(15);
                        gather32(go, X5A, [&](float y) { xw = pk2(xe + bfr(n1 + bfr(y * rs)), 0.f); *(LAS u16*)(size_t)xs = (u16)xw; });
                        xe = lo16(xw);
                        STAMP(14);
                    } else {
                        unsigned long long mo = 0x1111111111111111ull; PINS(mo);
                        sched(X5B, 0);
                        STAMP(47);
                        gather32(go, X5B, [&](float y) { xw = pk2(xe + bfr(n1 + bfr(y * rs)), 0.f); });
                        { const unsigned w = xw | (dppu<0xF5>(xw) << 16);
                          const u32x2 w2 = {w, dppu<0xAA>(w)};
                          asm volatile("s_mov_b64 exec, %3\n\tglobal_store_dwordx2 %0, %1, %2 sc0 sc1\n\ts_mov_b64 exec, -1" :: "v"(xs), "v"(w2), "s"(out), "s"(mo) : "memory"); }
                        MARK(e_dn);
                        STAMP(45);
                    }
                }
            }
            if (layer == 0) {
                BAR();
                if (wave == 3) { stats(sa); STAMP(34); }
                MARK(e_dn);
                if (wave == 3) __builtin_amdgcn_s_setprio(2);
                if (__builtin_expect(wave == 4, 0)) { op_addr(1); qk_addr(false); att_addr(); }
                if (wave >= 6) {
                    const unsigned o = qo;
                    f32x2 cs = -*(const LAS f32x2*)(size_t)qc;
                    float rn = 1.f / DM;
                    unsigned long long m0 = 1ull << 24, m1 = 1ull << 25;
                    PIN2(cs, rn); PINS(m0); PINS(m1);
                    MARK(b_qg);
                    sched(X1, 0);
                    STAMP(51);
                    u32x4 a0, a1, a2, a3; f32x2 u;
                    RETRY({ a0 = ld16_sc1<-3584>(ws, o); a1 = ld16_sc1<-1792>(ws, o); a2 = ld16_sc1<0>(ws, o); a3 = ld16_sc1<1792>(ws, o); },
                          ({ asm volatile("s_waitcnt vmcnt(0)" : "+v"(a0), "+v"(a1), "+v"(a2), "+v"(a3) :: "memory"); max(max(um4(a0), um4(a1)), max(um4(a2), um4(a3))); }),
                          { unsigned sm;
                            asm volatile("s_waitcnt vmcnt(2)\n\tv_pk_add_f32 v[40:41], %2, %4\n\tv_pk_add_f32 v[42:43], %3, %5\n\t"
                                         "s_waitcnt vmcnt(1)\n\tv_pk_add_f32 v[40:41], v[40:41], %6\n\tv_pk_add_f32 v[42:43], v[42:43], %7\n\t"
                                         "s_waitcnt vmcnt(0)\n\tv_pk_add_f32 v[40:41], v[40:41], %8\n\tv_pk_add_f32 v[42:43], v[42:43], %9\n\ts_nop 1\n\t"
                                         "v_permlane32_swap_b32 v40, v42\n\tv_permlane32_swap_b32 v41, v43\n\tv_pk_add_f32 v[40:41], v[40:41], v[42:43]\n\ts_nop 0\n\t"
                                         "v_readlane_b32 %1, v41, 24\n\ts_nop 1\n\tv_mul_f32 v42, %1, %11\n\tv_cvt_pk_bf16_f32 v42, 0, v42\n\tv_pk_fma_f32 %0, %10, v[42:43], v[40:41] op_sel_hi:[1,0,1]"
                                         : "=&v"(u), "=&s"(sm) : "v"(LO2(a0)), "v"(HI2(a0)), "v"(LO2(a1)), "v"(HI2(a1)), "v"(LO2(a2)), "v"(HI2(a2)), "v"(LO2(a3)), "v"(HI2(a3)), "v"(cs), "v"(rn)
                                         : "memory", "v40", "v41", "v42", "v43");
                            unsigned w = pk2v(u);
                            if (LANES(m0)) w = __float_as_uint(u[0]);
                            if (LANES(m1)) w = __float_as_uint(u[1]);
                            *(LAS unsigned*)(size_t)qw = w; __builtin_amdgcn_sched_barrier(0); }, any_nan(u[0], u[1]), X1);
                    MARK(e_qg);
                }
            }
        }
    };
    PINS(ck1); PINS(ck2); PINS(csc);
    chain(IC<0>{});
    STAMP(35);
    att1();
    chain(IC<1>{});
}

__device__ __attribute__((used, noinline)) void mega_code_end() { asm volatile(".Lmega_code_end:"); }

extern "C" void kernel_launch(void* const* d_in, const int* in_sizes, int n_in, void* d_out, int out_size, void* d_ws,
                              size_t ws_size, hipStream_t stream) {
    const u16* x0 = (const u16*)d_in[0];
    const u16* kc = (const u16*)d_in[1];
    const u16* vc = (const u16*)d_in[2];
    const u16* wq = (const u16*)d_in[3];
    const u16* wo = (const u16*)d_in[4];
    const u16* wr = (const u16*)d_in[6];
    const u16* wg = (const u16*)d_in[7];
    const u16* wu = (const u16*)d_in[8];
    const u16* wd = (const u16*)d_in[9];
    (void)hipFuncSetAttribute((const void*)mega_kernel, hipFuncAttributeMaxDynamicSharedMemorySize, L_END);
    mega_kernel<<<256, 512, L_END, stream>>>(x0, kc, vc, wq, wo, wr, wg, wu, wd, (u16*)d_out, (unsigned char*)d_ws
                                             );
}
```

```cpp
#include <hip/hip_runtime.h>
#include <hip/hip_bf16.h>
#include <stdint.h>

typedef unsigned short u16;
typedef unsigned u32x4 __attribute__((ext_vector_type(4)));
typedef unsigned u32x2 __attribute__((ext_vector_type(2)));
typedef unsigned long long u64x8 __attribute__((ext_vector_type(8)));
typedef unsigned long long u64x2 __attribute__((ext_vector_type(2)));
typedef float f32x4 __attribute__((ext_vector_type(4)));
typedef short s16x4 __attribute__((ext_vector_type(4)));
typedef __attribute__((ext_vector_type(4))) __bf16 bf16x4;
typedef __attribute__((ext_vector_type(8))) __bf16 bf16x8;
#define LAS __attribute__((address_space(3)))

constexpr int DM = 640, HQ = 32, SK = 4096, DH = 192, WIN = 1024, FF = 1536;
constexpr int QD = HQ * DH;
constexpr float SCALE = 0.07216878364870322f, LOG2E = 1.44269504088896341f;
constexpr unsigned kSpinCap = 1u << 16;
#define GJ(g) (((g) >> 3) & 7)
#define GI(g) (((g) & 7) * 4 + ((g) >> 6))

#define D4A 111
#define D5A 92
#define D1 84
#define D2 133
#define D3 66
#define D4 76
#define D5B 75
#define PACE 59
#define PINIT 100
#define PMIN 50
#define DM_ 0
constexpr unsigned X4A = D4A + DM_, X5A = X4A + D5A + DM_, X1 = X5A + D1 + DM_, X2 = X1 + D2 + DM_, X3 = X2 + D3 + DM_, X4 = X3 + D4 + DM_, X5B = X4 + D5B + DM_;
#define WOC 2
#define WOFF 6
#define WEARLY 1
#define DG2 12
#define XCOST 10
#define GUC 3
#define XS 0
#define LEAD 12
#define TOFF 12
#define UG 360
#define SLOW 0
#define TLAND 125
#define ROWM 1
#define W3 15
#define ROT gj
#define GROT 1
#define CMODE 1
#define BETA 11
#define ADOPT 1
constexpr unsigned NUNIT = 384 + XCOST + XS + GUC * (9 + 2 * WEARLY), PBURST = 30;
#define GSTEPS GA = 1, GB = 6, GC = 7, HD = 1
#define OML 2
#define EMODE (OML + 1)
constexpr int GP = 0, GSTEPS;

constexpr unsigned WS_H1 = 0;
constexpr unsigned WS_H4 = 2u << 20;
constexpr unsigned H4C = 20480, H4T = H4C + 3072;
constexpr unsigned WS_H35 = 4u << 20;
constexpr unsigned WS_OWN = WS_H35 + 80 * 16384;
constexpr unsigned WS_CS = WS_OWN + 32 * 16384;
constexpr unsigned WS_MB = WS_CS + 256 * 768;
constexpr unsigned WS_R = WS_MB + 256 * 128;
constexpr unsigned WS_END = WS_R + 128;
constexpr unsigned WS_Q0 = 8u << 20;
static_assert(WS_END <= (6u << 20), "ws map");
__device__ __forceinline__ unsigned tile35(int j, int q) { return WS_H35 + (unsigned)((j * 10 + (q >> 1)) * 16384 + (q & 1) * 4096); }
__device__ __forceinline__ unsigned h4s(int s) { return (unsigned)((s >> 1) * 16384 + (s & 1) * 4096); }

constexpr int L_WQ = 0;
constexpr int L_WO = 33792;
constexpr int L_GU = L_WO + 30720;
constexpr int L_WD = L_GU + 16384;
constexpr int L_X0 = L_WD + 8192;
constexpr int L_WR = L_X0 + 5120;
constexpr int L_XS = L_WR + 2048;
constexpr int L_Q = L_XS + 1024;
constexpr int L_OP = L_Q + 512;
constexpr int L_M = L_OP + 8192;
constexpr int L_H = L_M + 64;
constexpr int L_ST = L_H + 384;
constexpr int L_CS = L_ST + 64;
constexpr int L_CS2 = L_CS + 1024;
constexpr int L_CNT = L_CS2 + 1536;
constexpr int L_P = L_CNT + 96;
constexpr int L_O2 = L_P + 72 * 192;
constexpr int L_DBG = L_O2 + 3328;
constexpr int L_END = L_DBG + 2048;
static_assert(L_END <= 163840 && L_END > 81920, "LDS");

typedef __attribute__((ext_vector_type(2))) __bf16 bf16x2;
typedef float f32x2 __attribute__((ext_vector_type(2)));
__device__ __forceinline__ unsigned pk2(float a, float b) { return __builtin_bit_cast(unsigned, __builtin_convertvector((f32x2){a, b}, bf16x2)); }
__device__ __forceinline__ unsigned fexp(float x) { const unsigned e = (__float_as_uint(x) >> 23) & 255u; return e == 255u ? 0u : e; }
__device__ __forceinline__ u32x2 enc21(float a, float b, float c) {
    const float lim = 1048575.f;
    const unsigned i = (unsigned)(int)__builtin_rintf(__builtin_amdgcn_fmed3f(a, -lim, lim)) & 0x1fffffu, j = (unsigned)(int)__builtin_rintf(__builtin_amdgcn_fmed3f(b, -lim, lim)) & 0x1fffffu;
    const unsigned k = (unsigned)(int)__builtin_rintf(__builtin_amdgcn_fmed3f(c, -lim, lim)) & 0x1fffffu;
    return (u32x2){i | j << 21, j >> 11 | k << 10 | (__builtin_fabsf(a) + __builtin_fabsf(b) + __builtin_fabsf(c) < 1e30f ? 0u : 0x80000000u)};
}
__device__ __forceinline__ float lo16(unsigned w) { return __uint_as_float(w << 16); }
__device__ __forceinline__ float hi16(unsigned w) { return __uint_as_float(w & 0xffff0000u); }
__device__ __forceinline__ float bfr(float f) { return __uint_as_float(pk2(0.f, f)); }
template <int CTRL> __device__ __forceinline__ float dpp(float x) {
    return __builtin_bit_cast(float, __builtin_amdgcn_mov_dpp(__builtin_bit_cast(int, x), CTRL, 0xf, 0xf, true));
}
template <int CTRL> __device__ __forceinline__ unsigned dppu(unsigned x) { return (unsigned)__builtin_amdgcn_mov_dpp((int)x, CTRL, 0xf, 0xf, true); }
__device__ __forceinline__ float row16_sum(float v) {
    v += dpp<0xB1>(v); v += dpp<0x4E>(v); v += dpp<0x141>(v); v += dpp<0x140>(v); return v;
}
__device__ __forceinline__ float fmax2(float a, float b) { return __builtin_elementwise_maximum(a, b); }
__device__ __forceinline__ float fmax3(float a, float b, float c) { return fmax2(fmax2(a, b), c); }
__device__ __forceinline__ float x16_add(float v) { auto s = __builtin_amdgcn_permlane16_swap(__float_as_uint(v), __float_as_uint(v), false, false); return __uint_as_float(s[0]) + __uint_as_float(s[1]); }
__device__ __forceinline__ float x32_add(float v) { auto s = __builtin_amdgcn_permlane32_swap(__float_as_uint(v), __float_as_uint(v), false, false); return __uint_as_float(s[0]) + __uint_as_float(s[1]); }
__device__ __forceinline__ float wave_sum(float v) { return x32_add(x16_add(row16_sum(v))); }
__device__ __forceinline__ float rs32(float a, float b) { auto s = __builtin_amdgcn_permlane32_swap(__float_as_uint(a), __float_as_uint(b), false, false); return __uint_as_float(s[0]) + __uint_as_float(s[1]); }
__device__ __forceinline__ f32x2 sw16(float a, float b) { auto s = __builtin_amdgcn_permlane16_swap(__float_as_uint(a), __float_as_uint(b), false, false); return (f32x2){__uint_as_float(s[0]), __uint_as_float(s[1])}; }

#define GUARD(n) asm volatile("s_branch 1f\n\t.p2align 12,," #n "\n1:")
#define MARK(n) do {} while (0)
#define BAR() asm volatile("s_waitcnt lgkmcnt(0)\n\ts_barrier" ::: "memory")
#define VMW(n) asm volatile("s_waitcnt vmcnt(%0)" :: "i"(n) : "memory")

template <int OFF = 0> __device__ __forceinline__ u32x4 ld16_sc1(const void* sbase, unsigned voff) {
    u32x4 r; asm volatile("global_load_dwordx4 %0, %1, %2 offset:%3 sc1" : "=v"(r) : "v"(voff), "s"(sbase), "i"(OFF) : "memory"); return r;
}
__device__ __forceinline__ unsigned ld4_sc1(const void* sbase, unsigned voff) {
    unsigned r; asm volatile("global_load_dword %0, %1, %2 sc1" : "=v"(r) : "v"(voff), "s"(sbase) : "memory"); return r;
}
template <int OFF = 0> __device__ __forceinline__ void st16_sc1(void* sbase, unsigned voff, u32x4 v) {
    asm volatile("global_store_dwordx4 %0, %1, %2 offset:%3 sc1\n\ts_nop 1" :: "v"(voff), "v"(v), "s"(sbase), "i"(OFF) : "memory");
}
__device__ __forceinline__ void st4_sc1(void* sbase, unsigned voff, unsigned v) {
    asm volatile("global_store_dword %0, %1, %2 sc1" :: "v"(voff), "v"(v), "s"(sbase) : "memory");
}
__device__ __forceinline__ void st8(void* sbase, unsigned voff, u32x2 v) {
    asm volatile("global_store_dwordx2 %0, %1, %2" :: "v"(voff), "v"(v), "s"(sbase) : "memory");
}
__device__ __forceinline__ void st2(void* sbase, unsigned voff, unsigned v) {
    asm volatile("global_store_short %0, %1, %2" :: "v"(voff), "v"(v), "s"(sbase) : "memory");
}
constexpr int RK = 64, RV = 160;
template <int R, int IMM> __device__ __forceinline__ void ld16_nt(const void* sbase, unsigned voff) {
    asm volatile("global_load_dwordx4 v[%c3:%c4], %0, %1 offset:%2 nt" :: "v"(voff), "s"(sbase), "i"(IMM), "i"(R), "i"(R + 3) : "memory");
}
template <int R, int IMM> __device__ __forceinline__ void ld8_nt(const void* sbase, unsigned voff) {
    asm volatile("global_load_dwordx2 v[%c3:%c4], %0, %1 offset:%2 nt" :: "v"(voff), "s"(sbase), "i"(IMM), "i"(R), "i"(R + 1) : "memory");
}
template <int R> __device__ __forceinline__ void smf(f32x4& d, u32x4 a, unsigned idx) {
    asm volatile("v_smfmac_f32_16x16x64_bf16 %0, %1, v[%c3:%c4], %2" : "+v"(d) : "v"(a), "v"(idx), "i"(R), "i"(R + 7));
}
template <int A, int B> __device__ __forceinline__ void zip_in() {
    unsigned t;
    asm volatile("v_perm_b32 %0, v%c2, v%c1, %3\n\tv_perm_b32 v%c2, v%c2, v%c1, %4\n\tv_mov_b32 v%c1, %0" : "=&v"(t) : "i"(A), "i"(B), "s"(0x05040100u), "s"(0x07060302u));
}
template <int A, int B> __device__ __forceinline__ void zip_out(unsigned& lo, unsigned& hi) {
    asm volatile("v_perm_b32 %0, v%c3, v%c2, %4\n\tv_perm_b32 %1, v%c3, v%c2, %5" : "=&v"(lo), "=&v"(hi) : "i"(A), "i"(B), "s"(0x05040100u), "s"(0x07060302u));
}
template <int A, int B, int C, int D> __device__ __forceinline__ void zip_x(unsigned lo, unsigned hi) {
    asm volatile("v_perm_b32 v%c4, v%c3, v%c2, %6\n\tv_perm_b32 v%c5, v%c3, v%c2, %7\n\tv_mov_b32 v%c2, %0\n\tv_mov_b32 v%c3, %1"
                 :: "v"(lo), "v"(hi), "i"(A), "i"(B), "i"(C), "i"(D), "s"(0x05040100u), "s"(0x07060302u));
}
__device__ __forceinline__ void dma16(const void* sbase, unsigned voff, unsigned lds_dst) {
    unsigned keep;
    asm volatile("s_mov_b32 %0, m0\n\ts_mov_b32 m0, %3\n\ts_nop 0\n\tglobal_load_lds_dwordx4 %1, %2 nt\n\ts_mov_b32 m0, %0"
                 : "=&s"(keep) : "v"(voff), "s"(sbase), "s"(lds_dst) : "memory");
}
__device__ __forceinline__ void dma16p(const void* sbase, unsigned voff, unsigned lds_dst) {
    unsigned keep;
    asm volatile("s_mov_b32 %0, m0\n\ts_mov_b32 m0, %3\n\ts_nop 0\n\tglobal_load_lds_dwordx4 %1, %2\n\ts_mov_b32 m0, %0"
                 : "=&s"(keep) : "v"(voff), "s"(sbase), "s"(lds_dst) : "memory");
}
__device__ __forceinline__ unsigned um4(u32x4 a) { return max(max(a[0], a[1]), max(a[2], a[3])); }
__device__ __forceinline__ bool all_set(unsigned t) { return __builtin_amdgcn_ballot_w64(t == 0u) == 0ull; }
__device__ __forceinline__ bool any_empty(unsigned mx) { return __builtin_amdgcn_ballot_w64(mx == ~0u) != 0ull; }
__device__ __forceinline__ unsigned long long any_nan(float a, float b) { return __builtin_amdgcn_ballot_w64(__builtin_isunordered(a, b)); }
__device__ __forceinline__ f32x4 asf(u32x4 a) { return __builtin_bit_cast(f32x4, a); }
__device__ __forceinline__ u32x4 asu(f32x4 a) { return __builtin_bit_cast(u32x4, a); }
#define LANES(m) __builtin_amdgcn_inverse_ballot_w64(m)

__device__ __forceinline__ bf16x4 tr64(unsigned lds_addr) { return __builtin_amdgcn_ds_read_tr16_b64_v4bf16((LAS bf16x4*)(size_t)lds_addr); }
__device__ __forceinline__ bf16x4 tr64o(unsigned lds_addr, int off) { return __builtin_amdgcn_ds_read_tr16_b64_v4bf16((LAS bf16x4*)((LAS char*)(size_t)lds_addr + off)); }
__device__ __forceinline__ bf16x8 cat8(bf16x4 a, bf16x4 b) { return __builtin_shufflevector(a, b, 0, 1, 2, 3, 4, 5, 6, 7); }
__device__ __forceinline__ bf16x8 lds16(unsigned a) { return *(const LAS bf16x8*)(size_t)a; }
__device__ __forceinline__ bf16x4 lds8(unsigned a) { return *(const LAS bf16x4*)(size_t)a; }
__device__ __forceinline__ f32x4 mfma32(bf16x8 a, bf16x8 b, f32x4 c) { return __builtin_amdgcn_mfma_f32_16x16x32_bf16(a, b, c, 0, 0, 0); }
__device__ __forceinline__ f32x4 mfma32u(u32x4 a, u32x4 b, f32x4 c) { return mfma32(__builtin_bit_cast(bf16x8, a), __builtin_bit_cast(bf16x8, b), c); }
__device__ __forceinline__ f32x4 mfma16(bf16x4 a, bf16x4 b, f32x4 c) {
    return __builtin_amdgcn_mfma_f32_16x16x16bf16_1k(__builtin_bit_cast(s16x4, a), __builtin_bit_cast(s16x4, b), c, 0, 0, 0);
}

#define STAMP(i) do {} while (0)

template <int OFF = 0> __device__ __forceinline__ void st16m(void* sbase, unsigned voff, u32x4 v, unsigned long long mask) {
    asm volatile("s_mov_b64 exec, %4\n\tglobal_store_dwordx4 %0, %1, %2 offset:%3 sc1\n\ts_mov_b64 exec, -1\n\ts_nop 0" :: "v"(voff), "v"(v), "s"(sbase), "i"(OFF), "s"(mask) : "memory");
}
template <int OFF> __device__ __forceinline__ void st16m2(void* sbase, unsigned voff, u32x4 v, unsigned voff2, u32x4 v2, unsigned long long mask) {
    asm volatile("s_mov_b64 exec, %6\n\tglobal_store_dwordx4 %0, %1, %2 sc1\n\tglobal_store_dwordx4 %4, %5, %2 offset:%3 sc1\n\ts_mov_b64 exec, -1\n\ts_nop 0"
                 :: "v"(voff), "v"(v), "s"(sbase), "i"(OFF), "v"(voff2), "v"(v2), "s"(mask) : "memory");
}
#define PINS(a) asm volatile("" : "+s"(a))
#define RETRY(LOAD, EMPTY, COMPUTE, NAN_, X) do { LOAD; COMPUTE; if (__builtin_expect((NAN_) != 0ull, 0)) { \
    _Pragma("unroll 1") for (unsigned spins_ = 0; spins_ < kSpinCap; ++spins_) { slip(X); LOAD; if (!any_empty(EMPTY)) break; } COMPUTE; } } while (0)
__device__ __forceinline__ unsigned now() { return (unsigned)__builtin_amdgcn_s_memrealtime(); }
__device__ __forceinline__ void until(unsigned t) {
    asm volatile("1:\n\ts_memrealtime vcc\n\ts_waitcnt lgkmcnt(0)\n\ts_sub_u32 vcc_lo, %0, vcc_lo\n\ts_cmp_lt_u32 vcc_lo, 0x400\n\ts_cbranch_scc1 1b" :: "s"(t - 1u) : "vcc", "scc", "memory");
}
__device__ __forceinline__ unsigned lds_word(unsigned a) {
    unsigned t_; asm volatile("ds_read_b32 %0, %1\n\ts_waitcnt lgkmcnt(0)\n\ts_nop 0" : "=v"(t_) : "v"(a) : "memory");
    return __builtin_amdgcn_readfirstlane(t_);
}
__device__ __forceinline__ void lds_set(unsigned a, unsigned v) { asm volatile("ds_write_b32 %0, %1" :: "v"(a), "v"(v) : "memory"); }
__device__ __forceinline__ int xrow(int lane) { return (lane >> 5) * 2 + ((lane >> 3) & 1); }
__device__ __forceinline__ int xcol(int lane) { return ((lane >> 4) & 1) * 8 + (lane & 7); }
__device__ __forceinline__ int swz(int r) { return r ^ (((r >> 3) & 1) << 2); }
__device__ __forceinline__ f32x2 unpk(unsigned w) { return (f32x2){lo16(w), hi16(w)}; }
__device__ __forceinline__ unsigned pk2v(f32x2 v) { return __builtin_bit_cast(unsigned, __builtin_convertvector(v, bf16x2)); }
__device__ __forceinline__ float frcp(float x) { return __builtin_amdgcn_rcpf(x); }
__device__ __forceinline__ float ex2(float x) { return __builtin_amdgcn_exp2f(x); }
__device__ __forceinline__ int lane_id() { int l; asm volatile("v_mbcnt_lo_u32_b32 %0, -1, 0\n\tv_mbcnt_hi_u32_b32 %0, -1, %0" : "=v"(l)); return l & 63; }
__device__ __forceinline__ f32x2 ln_stats(float ss, float s) {
    const float mean = s * (1.f / DM);
    const float var = fmax2(__builtin_fmaf(-mean, mean, ss * (1.f / DM)), 0.f);
    return (f32x2){bfr(mean), frcp(bfr(__builtin_amdgcn_sqrtf(bfr(bfr(var) + 1e-5f))))};
}
#define GATHER8(a, o, S) do { a[0] = ld16_sc1<-4 * (S)>(ws, o); a[1] = ld16_sc1<-3 * (S)>(ws, o); a[2] = ld16_sc1<-2 * (S)>(ws, o); a[3] = ld16_sc1<-(S)>(ws, o); \
    a[4] = ld16_sc1<0>(ws, o); a[5] = ld16_sc1<(S)>(ws, o); a[6] = ld16_sc1<2 * (S)>(ws, o); a[7] = ld16_sc1<3 * (S)>(ws, o); } while (0)
template <int OFF> __device__ __forceinline__ void again16(u32x4& r, const void* sbase, unsigned voff) {
    asm volatile("v_cmp_eq_u32 vcc, -1, %1\n\ts_mov_b64 exec, vcc\n\tglobal_load_dwordx4 %0, %2, %3 offset:%4 sc1\n\ts_mov_b64 exec, -1" : "+v"(r) : "v"(um4(r)), "v"(voff), "s"(sbase), "i"(OFF) : "memory", "vcc");
}
#define AGAIN8(a, o, S) do { again16<-4 * (S)>(a[0], ws, o); again16<-3 * (S)>(a[1], ws, o); again16<-2 * (S)>(a[2], ws, o); again16<-(S)>(a[3], ws, o); \
    again16<0>(a[4], ws, o); again16<(S)>(a[5], ws, o); again16<2 * (S)>(a[6], ws, o); again16<3 * (S)>(a[7], ws, o); } while (0)
#define WAIT8(a, N) asm volatile("s_waitcnt vmcnt(%8)" : "+v"((a)[0]), "+v"((a)[1]), "+v"((a)[2]), "+v"((a)[3]), "+v"((a)[4]), "+v"((a)[5]), "+v"((a)[6]), "+v"((a)[7]) : "i"(N) : "memory")
#define UM32(a) max(max(max(um4(a[0]), um4(a[1])), max(um4(a[2]), um4(a[3]))), max(max(um4(a[4]), um4(a[5])), max(um4(a[6]), um4(a[7]))))
#define SUM8(a) (((((((asf(a[0]) + asf(a[1])) + asf(a[2])) + asf(a[3])) + asf(a[4])) + asf(a[5])) + asf(a[6])) + asf(a[7]))
#define WAIT1(x, N) asm volatile("s_waitcnt vmcnt(%1)" : "+v"(x) : "i"(N) : "memory")
#define LO2(x) __builtin_shufflevector(x, x, 0, 1)
#define HI2(x) __builtin_shufflevector(x, x, 2, 3)
#define ACC8(a, s) do { f32x2 l_, h_; \
    asm volatile("s_waitcnt vmcnt(5)\n\tv_pk_add_f32 %0, %2, %4\n\tv_pk_add_f32 %1, %3, %5\n\tv_pk_add_f32 %0, %0, %6\n\tv_pk_add_f32 %1, %1, %7\n\t" \
                 "s_waitcnt vmcnt(3)\n\tv_pk_add_f32 %0, %0, %8\n\tv_pk_add_f32 %1, %1, %9\n\tv_pk_add_f32 %0, %0, %10\n\tv_pk_add_f32 %1, %1, %11\n\t" \
                 "s_waitcnt vmcnt(1)\n\tv_pk_add_f32 %0, %0, %12\n\tv_pk_add_f32 %1, %1, %13\n\tv_pk_add_f32 %0, %0, %14\n\tv_pk_add_f32 %1, %1, %15\n\t" \
                 "s_waitcnt vmcnt(0)\n\tv_pk_add_f32 %0, %0, %16\n\tv_pk_add_f32 %1, %1, %17\n\ts_nop 0" \
                 : "=&v"(l_), "=&v"(h_) : "v"(LO2(a[0])), "v"(HI2(a[0])), "v"(LO2(a[1])), "v"(HI2(a[1])), "v"(LO2(a[2])), "v"(HI2(a[2])), "v"(LO2(a[3])), "v"(HI2(a[3])), \
                   "v"(LO2(a[4])), "v"(HI2(a[4])), "v"(LO2(a[5])), "v"(HI2(a[5])), "v"(LO2(a[6])), "v"(HI2(a[6])), "v"(LO2(a[7])), "v"(HI2(a[7])) : "memory"); \
    s = __builtin_shufflevector(l_, h_, 0, 1, 2, 3); } while (0)
#define RETRY8(a, o, S, s, MID, POST, NAN_, X) do { GATHER8(a, o, S); MID; ACC8(a, s); POST; if (__builtin_expect((NAN_) != 0ull, 0)) { \
    _Pragma("unroll 1") for (unsigned spins_ = 0; spins_ < kSpinCap; ++spins_) { slip(X); AGAIN8(a, o, S); WAIT8(a, 0); if (!any_empty(UM32(a))) break; } s = SUM8(a); POST; } } while (0)
#define PIN1(a) asm volatile("" : "+v"(a))
#define PIN2(a, b) asm volatile("" : "+v"(a), "+v"(b))
#define PIN3(a, b, c) asm volatile("" : "+v"(a), "+v"(b), "+v"(c))
#define LANE_VARS() const int lane = lane_id(); \
    const int kq = lane >> 4, n = lane & 15, q4 = n >> 2, p4 = lane & 3; \
    const int r0s = 8 * kq + q4 + 4 * (kq & 1), r1s = 8 * kq + q4 + 4 * ((kq & 1) ^ 1);     \
    const int r2s = swz(4 * kq + q4);                                                       \
    (void)r0s; (void)r1s; (void)r2s; (void)p4
template <int V> struct IC { static constexpr int value = V; };
struct Frag80 { bf16x8 a0, a1; bf16x4 a2; };

__global__ void __launch_bounds__(512, 2) __attribute__((amdgpu_num_vgpr(32)))
mega_kernel(const u16*, const u16*, const u16*, const u16*, const u16*, const u16*, const u16*, const u16*, const u16*, u16*, unsigned char*
            ) {
    extern __shared__ __attribute__((aligned(16))) unsigned char smem[];
    const unsigned lb = (unsigned)(size_t)smem;
    const int wave = __builtin_amdgcn_readfirstlane(threadIdx.x >> 6);
    u64x8 ar0; u64x2 ar1; unsigned long long ar2, tp, tst64;
    asm volatile("s_load_dwordx16 %0, %5, 0\n\ts_load_dwordx4 %1, %5, 0x40\n\ts_load_dwordx2 %2, %5, 0x50\n\ts_load_dwordx2 %3, %5, %6\n\ts_memrealtime %4"
                 : "=&s"(ar0), "=&s"(ar1), "=&s"(ar2), "=&s"(tp), "=&s"(tst64) : "s"(__builtin_amdgcn_kernarg_segment_ptr()), "s"((0xa9876421u >> (4 * wave) & 15u) * 8u) : "memory");
    const int g = blockIdx.x, gj = GJ(g), gi = GI(g);
    const f32x4 z4 = {0.f, 0.f, 0.f, 0.f};
    asm volatile("" ::: "v255");
    auto sched = [&](unsigned X, unsigned add) __attribute__((always_inline)) {
        u32x2 v;
        asm volatile("ds_read2_b32 %0, %1 offset0:14 offset1:5\n\ts_waitcnt lgkmcnt(0)" : "=v"(v) : "v"(lb + L_CNT) : "memory");
        until(__builtin_amdgcn_readfirstlane(v[0]) + (X * __builtin_amdgcn_readfirstlane(v[1]) >> 12) + add);
    };
    auto slip = [&](unsigned X) __attribute__((always_inline)) {
        if (X) lds_set(lb + L_CNT + 56, now() - (X * lds_word(lb + L_CNT + 20) >> 12));
    };
    float xe = 0.f;

    auto code_pf = [&](int slice, int lane) __attribute__((always_inline)) {
        unsigned long long pc0; unsigned len, back, t;
        asm volatile("s_getpc_b64 %0\n.Lmega_pf%=:\n\ts_mov_b32 %1, .Lmega_code_end-.Lmega_code_begin\n\ts_mov_b32 %2, .Lmega_pf%=-.Lmega_code_begin" : "=s"(pc0), "=s"(len), "=s"(back));
        unsigned off = (unsigned)(slice * 4096 + lane * 64); if (off > len) off = len;
        asm volatile("global_load_dword %0, %1, %2" : "=v"(t) : "v"(off & ~3u), "s"(pc0 - back) : "memory");
        return t;
    };
    asm volatile(".Lmega_code_begin:");
    unsigned pf = 0u, pt1;
    const unsigned wofs = (unsigned)((((gj >> 1) * HQ + gi) * SK + (SK - WIN) + (gj & 1) * 512) * (DH * 2));
    {
        const int lane = threadIdx.x & 63, hi = lane >= 26;
        if (threadIdx.x < 24) lds_set(lb + L_CNT + threadIdx.x * 4, 0u);
        unsigned t2 = (unsigned)(gi * (DH * DM * 2)), t7 = (32u << 20) + (unsigned)(g * 128), to = (unsigned)(gj * 128), w2 = wofs;
        PINS(t2); PINS(t7); PINS(to); PINS(w2);
        to = wave == 7 ? t7 : to; to = wave == 2 ? t2 : to; to = wave < 2 ? w2 : to;
        if (wave == 7) pf = code_pf((g >> 3) & 15, lane);
        unsigned vo = (unsigned)((gj * 80 + swz(2 * wave) + hi) * (QD * 2) + gi * (DH * 2) + (lane - 26 * hi) * 16), d = lb + L_WQ + wave * 832, keep;
        unsigned xo = (0x11043210u >> (4 * wave) & 15u) * 1024 + (unsigned)(lane & (wave < 6 ? 63 : 15)) * 16, xd = lb + L_X0 + min(wave, 6) * 1024;
        PIN2(vo, xo); PINS(d); PINS(xd); PINS(to);
        __builtin_amdgcn_sched_barrier(0);
        asm volatile("s_waitcnt lgkmcnt(0)\n\ts_load_dword %0, %4, %6" : "=&s"(pt1), "+s"(ar0), "+s"(ar1), "+s"(ar2), "+s"(tp), "+s"(tst64) : "s"(to) : "memory");
        dma16p((const void*)(wave < 5 ? ar0[0] : ar0[5]), xo, xd);
        asm volatile("s_mov_b32 %0, m0\n\ts_mov_b32 m0, %2\n\ts_mov_b64 exec, %4\n\tglobal_load_lds_dwordx4 %1, %3 nt\n\t"
                     ".rept 4\n\ts_add_u32 m0, m0, 6656\n\tv_add_u32 %1, 0x30000, %1\n\tglobal_load_lds_dwordx4 %1, %3 nt\n\t.endr\n\t"
                     "s_mov_b64 exec, -1\n\ts_mov_b32 m0, %0\n\ts_barrier"
                     : "=&s"(keep), "+v"(vo) : "s"(d), "s"(ar0[3]), "s"(0x0003fffffcffffffull) : "memory", "scc");
        __builtin_amdgcn_sched_barrier(0);
    }
    const u16 *const kc = (const u16*)ar0[1], *const vc = (const u16*)ar0[2], *const wo = (const u16*)ar0[4], *const wg = (const u16*)ar0[6], *const wu = (const u16*)ar0[7], *const wd = (const u16*)ar1[0];
    u16* const out = (u16*)ar1[1];
    unsigned char* const ws = (unsigned char*)((ar2 + (32u << 20) - 1) & ~(unsigned long long)((16u << 20) - 1));
    const unsigned tst = (unsigned)tst64;
    auto fix_k1 = [&](auto IC_, auto NC) __attribute__((always_inline)) {
        constexpr int A = RK + 8 * decltype(IC_)::value, B = A + 4;
        unsigned t0, t1, t2, t3;
        asm volatile("s_waitcnt vmcnt(%12)\n\tv_mov_b32 %0, v%c4\n\tv_mov_b32 %1, v%c5\n\tv_mov_b32 %2, v%c6\n\tv_mov_b32 %3, v%c7\n\t"
                     "v_mov_b32_dpp v%c4, v%c8 row_ror:8 row_mask:0xf bank_mask:0xc\n\tv_mov_b32_dpp v%c5, v%c9 row_ror:8 row_mask:0xf bank_mask:0xc\n\t"
                     "v_mov_b32_dpp v%c6, v%c10 row_ror:8 row_mask:0xf bank_mask:0xc\n\tv_mov_b32_dpp v%c7, v%c11 row_ror:8 row_mask:0xf bank_mask:0xc\n\t"
                     "v_mov_b32_dpp v%c8, %0 row_ror:8 row_mask:0xf bank_mask:0x3\n\tv_mov_b32_dpp v%c9, %1 row_ror:8 row_mask:0xf bank_mask:0x3\n\t"
                     "v_mov_b32_dpp v%c10, %2 row_ror:8 row_mask:0xf bank_mask:0x3\n\tv_mov_b32_dpp v%c11, %3 row_ror:8 row_mask:0xf bank_mask:0x3"
                     : "=&v"(t0), "=&v"(t1), "=&v"(t2), "=&v"(t3)
                     : "i"(A), "i"(A + 1), "i"(A + 2), "i"(A + 3), "i"(B), "i"(B + 1), "i"(B + 2), "i"(B + 3), "i"(decltype(NC)::value) : "memory");
    };
    auto trans_v = [&](auto UC, auto NC) __attribute__((always_inline)) {
        constexpr int R = RV + 4 * decltype(UC)::value, N = decltype(NC)::value;
#define CELL(r, c) R + 16 * (r) + (c), R + 16 * (r) + 8 + (c)
        unsigned a0, a1, b0, b1, c0, c1;
        VMW(N + 6); zip_in<CELL(0, 0)>(); zip_out<CELL(0, 1)>(a0, a1); zip_out<CELL(0, 2)>(b0, b1); zip_out<CELL(0, 3)>(c0, c1);
        VMW(N + 4); zip_x<CELL(1, 0), CELL(0, 1)>(a0, a1); zip_in<CELL(1, 1)>(); zip_out<CELL(1, 2)>(a0, a1);
        VMW(N + 2); zip_x<CELL(2, 0), CELL(0, 2)>(b0, b1); zip_x<CELL(2, 1), CELL(1, 2)>(a0, a1); zip_in<CELL(2, 2)>(); zip_out<CELL(1, 3)>(a0, a1); zip_out<CELL(2, 3)>(b0, b1);
        VMW(N); zip_x<CELL(3, 0), CELL(0, 3)>(c0, c1); zip_x<CELL(3, 1), CELL(1, 3)>(a0, a1); zip_x<CELL(3, 2), CELL(2, 3)>(b0, b1); zip_in<CELL(3, 3)>();
#undef CELL
    };
    auto trans_w = [&](auto UC, auto NC) __attribute__((always_inline)) {
        constexpr int R = RV + 64 + 2 * decltype(UC)::value, N = decltype(NC)::value;
        unsigned a0, a1;
        VMW(N + 2); zip_in<R, R + 8>(); zip_out<R + 1, R + 9>(a0, a1);
        VMW(N); zip_x<R + 16, R + 24, R + 1, R + 9>(a0, a1); zip_in<R + 17, R + 25>();
    };
    auto frag80 = [&](unsigned im, int rs, int r0s, int r1s, int r2s) __attribute__((always_inline)) -> Frag80 {
        return Frag80{cat8(tr64(im + r0s * rs), tr64(im + r1s * rs)), cat8(tr64(im + (r0s + 32) * rs), tr64(im + (r1s + 32) * rs)), tr64(im + (r2s + 64) * rs)};
    };
    auto mm80 = [&](const Frag80& a, const Frag80& b) __attribute__((always_inline)) -> f32x4 {
        return mfma32(a.a1, b.a1, mfma32(a.a0, b.a0, z4)) + mfma16(a.a2, b.a2, z4);
    };
    auto rows80 = [&](unsigned a16, unsigned a8) __attribute__((always_inline)) -> Frag80 { const Frag80 f{lds16(a16), lds16(a16 + 64), lds8(a8)}; __builtin_amdgcn_sched_barrier(0); return f; };
    auto empty5 = [&](int q0, unsigned S, int lane) __attribute__((always_inline)) {
        st16m(ws, tile35(gj, q0 + (lane >> 3)) + S + (unsigned)(gi * 128 + (lane & 7) * 16), (u32x4){~0u, ~0u, ~0u, ~0u}, 0xffffffffffull);
    };
    auto empty4 = [&](int layer, unsigned S, int lane) __attribute__((always_inline)) {
        const unsigned b = WS_H4 + (unsigned)((layer * 32 + gi) * 32768) + (S ^ 8192u);
        st16_sc1(ws, b + (unsigned)((lane >> 5) * 4096 + gj * 512 + (lane & 31) * 16), (u32x4){~0u, ~0u, ~0u, ~0u});
        st16m(ws, b + (unsigned)(lane < 32 ? 16384 + gj * 512 + lane * 16 : H4C + ((lane - 32) >> 3) * 1024 + gj * 128 + (lane & 7) * 16), (u32x4){~0u, ~0u, ~0u, ~0u},
              layer ? 0xff000000ffffffffull : ~0ull);
    };
    int lead = wave == 7 ? LEAD * PACE * 4 : 0;
    auto pace = [&](unsigned n, bool wait = true) __attribute__((always_inline)) {
        unsigned u, w, t0;
        asm volatile("s_mov_b64 exec, 1\n\tds_add_rtn_u32 %0, %3, %4\n\tds_max_u32 %3, %5 offset:12\n\tds_add_rtn_u32 %1, %3, %6 offset:12\n\tds_read_b32 %2, %3 offset:4\n\ts_mov_b64 exec, -1\n\ts_waitcnt lgkmcnt(0)"
                     : "=&v"(u), "=&v"(w), "=&v"(t0) : "v"(lb + L_CNT + 60), "v"(n), "v"((now() - tst) << 6), "v"(n * PMIN * 4u) : "memory");
        int tn = (int)(__builtin_amdgcn_readfirstlane(t0) - tst + PINIT) * 64 + __builtin_amdgcn_readfirstlane(u) * (PACE * 4);
        tn += max((int)__builtin_amdgcn_readfirstlane(u) - UG, 0) * (SLOW * 4);
        const int tw = __builtin_amdgcn_readfirstlane(w) - PBURST * 64;
        if (tw > tn) tn = tw;
        tn -= lead;
        if (wait) {
#pragma unroll 1
            for (int k = 0; k < 2048 && (int)((now() - tst) << 6) < tn; ++k) {}
        }
    };
    auto slot = [&](unsigned u) __attribute__((always_inline)) {
        const unsigned tn = lds_word(lb + L_CNT + 64) + PINIT + (u * PACE + (NUNIT - UG) * SLOW) / 16;
#pragma unroll 1
        for (int k = 0; k < 8192 && (int)(now() - tn) < 0; ++k) __builtin_amdgcn_s_sleep(1);
    };
    auto load_wo = [&](int k, int lane, bool early) __attribute__((always_inline)) {
        const int c = k * 64 + lane, pp = c / 10, cc = c - 10 * pp;
        const unsigned f = (unsigned)(swz(pp) * 1280 + cc * 16);
        const u16* wb = wo + (size_t)gi * DH * DM + gj * 80;
#pragma unroll
        for (int a2 = 0; a2 < 6; ++a2) { if (a2 % 3 == 0) { if (early) pace(GUC); else slot(NUNIT - WOFF + (2 * k + a2 / 3) * WOC); } dma16(wb + 32 * a2 * DM, f, lb + L_WO + (5 * a2 + k) * 1024); }
    };
    auto load_wd = [&](int lane) __attribute__((always_inline)) {
        const u16* db = wd + gi * 48 * DM + gj * 80;
#pragma unroll 1
        for (int k = 0; k < 5; ++k) {
            const int c = k * 64 + lane, pp = c / 10, cc = c - 10 * pp;
            const unsigned fd = (unsigned)(swz(pp) * 1280 + cc * 16);
            if (!(k & 1)) pace(GUC);
            dma16(db, fd, lb + L_WD + k * 1024);
            if (k < 3) dma16(db + 32 * DM, (k < 2 || lane < 32) ? fd : 0u, lb + L_WD + (5 + k) * 1024);
        }
    };
    auto load_gu = [&](int m, int lane) __attribute__((always_inline)) {
#pragma unroll 1
        for (int k0 = 0; k0 < 3; ++k0) {
            const int k = (k0 + (GROT ? g & 7 : 0)) % 3, c = k * 64 + lane, row = c / 6, cc = c - 6 * row;
            const unsigned o = (unsigned)((swz(row) * FF + cc * 8) * 2), d = lb + L_GU + m * 8192 + k * 1024;
            const u16* wb = (m == 0 ? wg : wu) + gj * 80 * FF + gi * 48;
            pace(GUC);
            dma16(wb, o, d); dma16(wb + 32 * FF, o, d + 3072);
            if (k < 2) dma16(wb + 64 * FF, (k == 0 || lane < 32) ? o : 0u, d + 6144);
        }
    };
    auto lds_add = [&](int off, unsigned v) __attribute__((always_inline)) { asm volatile("s_mov_b64 exec, 1\n\tds_add_u32 %0, %1\n\ts_mov_b64 exec, -1" :: "v"(lb + L_CNT + off), "v"(v) : "memory"); };
    auto send_flag = [&](int lane) __attribute__((always_inline)) {
        st4_sc1(ws, WS_MB + (unsigned)((gi * 8 + ((gj == (gi & 7) ? lane : gi) & 7)) * 128 + gj * 4), lds_word(lb + L_CNT + 68));
    };
    auto tiles4 = [&](const Frag80 (&f)[4], const Frag80& b, unsigned long long mk) __attribute__((always_inline)) -> u32x4 {
        f32x4 c[4], d[4];
#pragma unroll
        for (int i = 0; i < 4; ++i) c[i] = mfma32(f[i].a0, b.a0, z4);
#pragma unroll
        for (int i = 0; i < 4; ++i) { c[i] = mfma32(f[i].a1, b.a1, c[i]); d[i] = mfma16(f[i].a2, b.a2, z4); }
        __builtin_amdgcn_sched_barrier(0);
        f32x2 lo, hi;
        asm volatile("s_nop 1\n\ts_mov_b64 exec, %18\n\tv_pk_add_f32 %0, %2, %10\n\tv_pk_add_f32 %1, %3, %11\n\t"
                     "s_lshl_b64 exec, exec, 1\n\tv_pk_add_f32 %0, %4, %12\n\tv_pk_add_f32 %1, %5, %13\n\t"
                     "s_lshl_b64 exec, exec, 1\n\tv_pk_add_f32 %0, %6, %14\n\tv_pk_add_f32 %1, %7, %15\n\t"
                     "s_lshl_b64 exec, exec, 1\n\tv_pk_add_f32 %0, %8, %16\n\tv_pk_add_f32 %1, %9, %17\n\ts_mov_b64 exec, -1"
                     : "=&v"(lo), "=&v"(hi) : "v"(LO2(c[0])), "v"(HI2(c[0])), "v"(LO2(c[1])), "v"(HI2(c[1])), "v"(LO2(c[2])), "v"(HI2(c[2])), "v"(LO2(c[3])), "v"(HI2(c[3])),
                       "v"(LO2(d[0])), "v"(HI2(d[0])), "v"(LO2(d[1])), "v"(HI2(d[1])), "v"(LO2(d[2])), "v"(HI2(d[2])), "v"(LO2(d[3])), "v"(HI2(d[3])), "s"(mk) : "scc");
        return asu(__builtin_shufflevector(lo, hi, 0, 1, 2, 3));
    };
    auto empty1 = [&](int half, unsigned S, int lane) __attribute__((always_inline)) {
        const unsigned o = WS_H1 + S + (unsigned)((gi * 4 + 2 * half) * 16384 + gj * 896 + min(lane, 55) * 16);
        st16_sc1(ws, o, (u32x4){~0u, ~0u, ~0u, ~0u}); st16_sc1(ws, o + 16384, (u32x4){~0u, ~0u, ~0u, ~0u});
    };
    auto report = [&]() __attribute__((always_inline)) { if (gj == (gi & 7)) st4_sc1(ws, WS_R + (unsigned)(gi * 4), lds_word(lb + L_CNT + 88)); };
    auto answer = [&](int lane, unsigned r) __attribute__((always_inline)) {
        int d = min(max((int)(r - (tst << 11)) >> 11, -400), 600) * 512 + (int)((r >> 1) & 15u);
        unsigned k = (r >> 5) & 63u;
        d += (int)dppu<0xB1>((unsigned)d); d += (int)dppu<0x4E>((unsigned)d); d += (int)dppu<0x141>((unsigned)d); d += (int)dppu<0x140>((unsigned)d);
        k = max(k, dppu<0xB1>(k)); k = max(k, dppu<0x4E>(k)); k = max(k, dppu<0x141>(k)); k = max(k, dppu<0x140>(k));
        d = __builtin_amdgcn_readlane(d, 0) + __builtin_amdgcn_readlane(d, 16);
        k = max((unsigned)__builtin_amdgcn_readlane(k, 0), (unsigned)__builtin_amdgcn_readlane(k, 16));
        st4_sc1(ws, WS_MB + (unsigned)(((lane & 31) * 8 + (lane & 7)) * 128 + 32), (tst + (d >> 14) + TOFF) << 8 | k << 2 | ((d & 511) > 128 ? 3u : 1u));
        lds_set(lb + L_CNT + 36, 1u);
    };
    auto adopt = [&](unsigned t) __attribute__((always_inline)) {
        const int dt = (int)(t - (tst << 8)) >> 8;
        if (ADOPT && dt > -256 && dt < 512) lds_set(lb + L_CNT + 64, tst + dt);
        lds_set(lb + L_CNT + 20, 4096u + ((t >> 2) & 63u) * BETA);
        lds_set(lb + L_CNT + 80, lds_word(lb + L_CNT + 92) ^ (t & 2u) << 12);
        lds_set(lb + L_CNT + 84, (((t << 1) ^ lds_word(lb + L_CNT + 68)) & 4u) << 11);
        lds_set(lb + L_CNT + 52, 1u);
    };
    struct StatAddr { unsigned a16, b16, a8, b8, dst; };
    auto stats_addr = [&](int lane, unsigned dbase, int dstride) __attribute__((always_inline)) -> StatAddr {
        const int kq = lane >> 4, q4 = (lane >> 2) & 3, p4 = lane & 3;
        const int x1 = LANES(0x4444444444444444ull) ? L_WR + gj * 160 : L_XS + q4 * 160, xa = LANES(0xaaaaaaaaaaaaaaaaull) ? L_XS + 640 : x1;
        StatAddr r{lb + (unsigned)(xa + kq * 16), lb + (unsigned)(L_XS + p4 * 160 + kq * 16), lb + (unsigned)(xa + 128 + kq * 8), lb + (unsigned)(L_XS + p4 * 160 + 128 + kq * 8), dbase + (unsigned)(kq * dstride)};
        PIN3(r.a16, r.b16, r.dst); PIN2(r.a8, r.b8);
        return r;
    };
    auto stats = [&](const StatAddr& r) __attribute__((always_inline)) {
        const bf16x8 a0 = lds16(r.a16), b0 = lds16(r.b16), a1 = lds16(r.a16 + 64), b1 = lds16(r.b16 + 64);
        const bf16x4 a2 = lds8(r.a8), b2 = lds8(r.b8);
        unsigned long long mk = 0x0008000400020001ull; PINS(mk);
        __builtin_amdgcn_sched_barrier(0);
        st16m(ws, r.dst, asu(mfma32(a1, b1, mfma32(a0, b0, z4)) + mfma16(a2, b2, z4)), mk);
    };
    unsigned qa, pw, ma, idx, qm;
    auto qk_addr = [&](bool L1) __attribute__((always_inline)) {
        LANE_VARS();
        qa = lb + L_Q + (unsigned)(kq * 32 + (lane & 2) * 8);
        pw = lb + L_P + (unsigned)((7 + 8 * wave) * 192 + (kq >> 1) * 64 + (n >> 3) * 32 + (lane & 2) * 8 + (q4 & 1) * 8 + (kq & 1) * 4 + (lane & 1) * 2);
        ma = lb + L_M + (unsigned)(wave * 4);
        idx = (lane & 2) ? 0xeeeeu : 0x4444u;
        qm = lb + L_Q;
        PIN3(qa, pw, ma); PIN2(idx, qm);
        if (L1 && wave < 4) __builtin_amdgcn_s_setprio(2);
    };
    auto att_qk0 = [&]() __attribute__((always_inline)) {
        qk_addr(false);
#pragma unroll 1
        for (unsigned spins = 0; spins < (kSpinCap << 4); ++spins) {
            if (lds_word(lb + L_CNT + 4) != 0u) break;
            __builtin_amdgcn_s_sleep(1);
        }
        STAMP(3);
        const u32x4 q0 = *(const LAS u32x4*)(size_t)qa, q1 = *(const LAS u32x4*)(size_t)(qa + 128), q2 = *(const LAS u32x4*)(size_t)(qa + 256);
        f32x4 c0 = z4, c1 = z4, c2 = z4, c3 = z4;
#define SMF(c, q, r) "v_smfmac_f32_16x16x64_bf16 %" #c ", %" #q ", v[" #r "], %7\n\t"
        asm volatile("s_nop 1\n\t" SMF(0, 4, 64:71) SMF(1, 4, 88:95) SMF(2, 4, 112:119) SMF(3, 4, 136:143) SMF(0, 5, 72:79) SMF(1, 5, 96:103) SMF(2, 5, 120:127) SMF(3, 5, 144:151)
                     SMF(0, 6, 80:87) SMF(1, 6, 104:111) SMF(2, 6, 128:135) SMF(3, 6, 152:159) "s_nop 7"
                     : "+v"(c0), "+v"(c1), "+v"(c2), "+v"(c3) : "v"(q0), "v"(q1), "v"(q2), "v"(idx));
#undef SMF
        const float s0 = c0[0] + c0[2], s1 = c1[0] + c1[2], s2 = c2[0] + c2[2], s3 = c3[0] + c3[2];
        float m = fmax2(fmax3(s0, s1, s2), s3);
        m = fmax2(m, dpp<0xB1>(m)); m = fmax2(m, dpp<0x4E>(m)); m = fmax2(m, dpp<0x124>(m)); m = fmax2(m, dpp<0x128>(m));
        const float s01 = LANES(0xffff0000ffff0000ull) ? s1 : s0, s23 = LANES(0xffff0000ffff0000ull) ? s3 : s2;
        const float sv = LANES(0xffffffff00000000ull) ? s23 : s01;
        m = bfr(bfr(m) * SCALE);
        const float p = ex2((bfr(bfr(sv) * SCALE) - m) * LOG2E);
        *(LAS u16*)(size_t)pw = (u16)pk2(p, p);
        *(LAS float*)(size_t)ma = m;
        __builtin_amdgcn_sched_barrier(0);
    };
    unsigned pr, oa, oc, cma, cxa, cra, cso;
    auto att_addr = [&]() __attribute__((always_inline)) {
        LANE_VARS();
        pr = lb + L_P + (unsigned)((7 + 8 * wave - 2 * q4 - (lane & 1)) * 192 + kq * 32 + (lane & 2) * 8);
        oa = lb + L_OP + (unsigned)(wave * 1024 + (lane & 7) * 64 + kq * 16 + (n >> 3) * 8);
        oc = lb + L_OP + (unsigned)(wave * 1024 + 512 + (kq >> 1) * 256 + (lane & 7) * 32 + (kq & 1) * 16 + (n >> 3) * 8);
        PIN3(pr, oa, oc);
    };
    auto comb_out = [&](float u0, float u1, unsigned so, unsigned long long mk, bool both) __attribute__((always_inline)) {
        const unsigned w = min(pk2(u0, u1), 0xfffffffeu);
        if (CMODE && !both) { st4_sc1(ws, so, w); return; }
        const u32x4 w4 = {both ? dppu<0x00>(w) : w, dppu<0x55>(w), dppu<0xAA>(w), dppu<0xFF>(w)};
        st16m(ws, so, w4, mk);
    };
    auto max8 = [&](f32x4 m0, f32x4 m1) __attribute__((always_inline)) { return fmax3(fmax3(m0[0], m0[1], m0[2]), fmax3(m0[3], m1[0], m1[1]), fmax2(m1[2], m1[3])) * LOG2E; };
    auto comb_o0 = [&](f32x4 m0, f32x4 m1, f32x4 mm, f32x4 o0, f32x4 o1, f32x4 o2, f32x4 o3, unsigned so, unsigned long long mk, bool both) __attribute__((always_inline)) {
        const float M = max8(m0, m1);
        f32x4 f;
#pragma unroll
        for (int k = 0; k < 4; ++k) f[k] = ex2(__builtin_fmaf(mm[k], LOG2E, -M));
        const f32x4 o = f[3] * o3 + (f[2] * o2 + (f[1] * o1 + f[0] * o0));
        comb_out(rs32(o[0], o[2]), rs32(o[1], o[3]), so, mk, both);
    };
    auto comb_o1 = [&](f32x4 m0, f32x4 m1, f32x2 mm, f32x4 o0, f32x4 o1, unsigned so, unsigned long long mk, bool both) __attribute__((always_inline)) {
        const float M = max8(m0, m1);
        const f32x4 o = ex2(__builtin_fmaf(mm[1], LOG2E, -M)) * o1 + ex2(__builtin_fmaf(mm[0], LOG2E, -M)) * o0;
        comb_out(x16_add(rs32(o[0], o[2])), x16_add(rs32(o[1], o[3])), so, mk, both);
    };
    auto ml_addr = [&](int lane, unsigned& xa, unsigned& ra) __attribute__((always_inline)) {
        xa = lb + L_M + (unsigned)(((lane & 15) >> 1) * 4); ra = lb + L_P + (unsigned)((7 + 8 * ((lane & 15) >> 1)) * 192 + (lane & 1) * 64 + (lane >> 4) * 16);
    };
    auto comb_ml = [&](unsigned ma, unsigned xa, unsigned ra, unsigned so, bool both) __attribute__((always_inline)) {
        const u32x4 x4 = *(const LAS u32x4*)(size_t)ra;
        const f32x4 m0 = *(const LAS f32x4*)(size_t)ma, m1 = *(const LAS f32x4*)(size_t)(ma + 16);
        const float mw = *(const LAS float*)(size_t)xa;
        __builtin_amdgcn_sched_barrier(0);
        const f32x4 sp = mfma32u((u32x4){0x3f803f80u, 0x3f803f80u, 0x3f803f80u, 0x3f803f80u}, x4, z4);
        __builtin_amdgcn_sched_barrier(0);
        const float M = max8(m0, m1);
        float L = row16_sum(sp[0] * ex2(__builtin_fmaf(mw, LOG2E, -M)));
        if (both) L = dpp<0x00>(L);
        st16m(ws, so, asu((f32x4){M, L, M, L}), both ? 3ull : 1ull);
    };
    auto att_pv0 = [&]() __attribute__((always_inline)) {
        u32x4 bm[8];
        trans_v(IC<0>{}, IC<24>{}); trans_v(IC<1>{}, IC<16>{});
#pragma unroll
        for (int e = 0; e < 8; ++e) bm[e] = *(const LAS u32x4*)(size_t)(pr + e * 192);
        __builtin_amdgcn_sched_barrier(0);
        f32x4 da = z4, dc = z4;
        PIN2(da, dc);
        asm volatile("s_nop 1");
        __builtin_amdgcn_sched_barrier(0);
        f32x2 r;
        smf<RV>(da, bm[0], idx); smf<RV + 8>(da, bm[1], idx); smf<RV + 16>(da, bm[2], idx); smf<RV + 24>(da, bm[3], idx);
        smf<RV + 32>(da, bm[4], idx); smf<RV + 40>(da, bm[5], idx); smf<RV + 48>(da, bm[6], idx); smf<RV + 56>(da, bm[7], idx);
        asm volatile("s_nop 7\n\tv_pk_add_f32 %0, %1, %2\n\tds_write_b64 %3, %0\n\ts_mov_b64 exec, 1\n\tds_add_u32 %4, %5\n\ts_mov_b64 exec, -1"
                     : "=&v"(r) : "v"(LO2(da)), "v"(HI2(da)), "v"(oa), "v"(lb + L_CNT + 24), "v"(1u) : "memory");
        GUARD(900);
        MARK(b_t5);
        trans_w(IC<0>{}, IC<12>{}); trans_w(IC<1>{}, IC<8>{}); trans_w(IC<2>{}, IC<4>{}); trans_w(IC<3>{}, IC<0>{});
        STAMP(4);
        asm volatile("s_nop 1\n\tv_smfmac_f32_16x16x64_bf16 %0, %1, v[224:231], %5\n\tv_smfmac_f32_16x16x64_bf16 %0, %2, v[232:239], %5\n\t"
                     "v_smfmac_f32_16x16x64_bf16 %0, %3, v[240:247], %5\n\tv_smfmac_f32_16x16x64_bf16 %0, %4, v[248:255], %5\n\ts_nop 6"
                     : "+v"(dc) : "v"(bm[0]), "v"(bm[1]), "v"(bm[2]), "v"(bm[3]), "v"(idx));
        asm volatile("s_nop 0\n\tv_pk_add_f32 %0, %1, %2\n\tds_write_b64 %3, %0" : "=&v"(r) : "v"(LO2(dc)), "v"(HI2(dc)), "v"(oc) : "memory");
        STAMP(20);
    };
    unsigned long long ck1 = 0xffff0000ffff0000ull, ck2 = 0xffffffff00000000ull, csc = 0x3d93cd3a3d93cd3aull;
    auto att1 = [&]() __attribute__((always_inline)) {
        constexpr int layer = 1;
        {
#define SMF(c, q, r) "v_smfmac_f32_16x16x64_bf16 v[" #c "], v[" #q "], v[" #r "], %0\n\t"
#define MAXD(x) "v_max_f32_dpp v41, v41, v41 " x " row_mask:0xf bank_mask:0xf\n\t"
            asm volatile("s_waitcnt lgkmcnt(0)\n\ts_barrier\n\t"
                         "ds_read_b128 v[0:3], %1\n\tds_read2_b32 v[12:13], %2 offset0:96 offset1:100\n\tds_read_b128 v[4:7], %1 offset:128\n\tds_read_b128 v[8:11], %1 offset:256\n\t"
                         "v_mov_b64 v[16:17], 0\n\tv_mov_b64 v[18:19], 0\n\tv_mov_b64 v[20:21], 0\n\tv_mov_b64 v[22:23], 0\n\tv_mov_b64 v[24:25], 0\n\tv_mov_b64 v[26:27], 0\n\t"
                         "v_mov_b64 v[28:29], 0\n\tv_mov_b64 v[30:31], 0\n\tv_mov_b64 v[32:33], 0\n\tv_mov_b64 v[34:35], 0\n\tv_mov_b64 v[36:37], 0\n\tv_mov_b64 v[38:39], 0\n\t"
                         "s_waitcnt lgkmcnt(3)\n\t" SMF(16:19, 0:3, 64:71) SMF(20:23, 0:3, 88:95) "s_waitcnt lgkmcnt(1)\n\tv_mul_f32 v13, 0x3acccccd, v13\n\t"
                         SMF(24:27, 0:3, 112:119) "v_mul_f32 v12, 0x3acccccd, v12\n\t" SMF(28:31, 0:3, 136:143) "v_fma_f32 v12, -v13, v13, v12\n\t"
                         SMF(16:19, 4:7, 72:79) "v_maximum3_f32 v12, v12, 0, 0\n\t" SMF(20:23, 4:7, 96:103) "v_cvt_pk_bf16_f32 v12, 0, v12\n\t"
                         SMF(24:27, 4:7, 120:127) "v_add_f32 v12, 0x3727c5ac, v12\n\t" SMF(28:31, 4:7, 144:151) "v_cvt_pk_bf16_f32 v12, 0, v12\n\ts_waitcnt lgkmcnt(0)\n\t"
                         SMF(16:19, 8:11, 80:87) "v_sqrt_f32 v12, v12\n\t" SMF(20:23, 8:11, 104:111) "v_cvt_pk_bf16_f32 v12, 0, v12\n\t"
                         SMF(24:27, 8:11, 128:135) "v_rcp_f32 v12, v12\n\t" SMF(28:31, 8:11, 152:159)
                         "s_nop 2\n\tv_add_f32 v42, v16, v18\n\ts_nop 0\n\tv_add_f32 v43, v20, v22\n\tv_max_f32 v41, v42, v43\n\tv_add_f32 v44, v24, v26\n\tv_add_f32 v45, v28, v30\n\t"
                         "v_max3_f32 v41, v41, v44, v45\n\tv_cndmask_b32 v42, v42, v43, %8\n\tv_cndmask_b32 v44, v44, v45, %8\n\t"
                         MAXD("quad_perm:[1,0,3,2]") "v_cndmask_b32 v40, v42, v44, %9\n\ts_nop 0\n\t" MAXD("quad_perm:[2,3,0,1]") "s_nop 1\n\t" MAXD("row_ror:4") "s_nop 1\n\t" MAXD("row_ror:8")
                         "v_pk_mul_f32 v[40:41], v[12:13], v[40:41] op_sel_hi:[0,1]\n\tv_cvt_pk_bf16_f32 v40, 0, v40\n\tv_cvt_pk_bf16_f32 v41, 0, v41\n\t"
                         "v_pk_mul_f32 v[40:41], %10, v[40:41]\n\tv_cvt_pk_bf16_f32 v40, 0, v40\n\tv_cvt_pk_bf16_f32 v41, 0, v41\n\t"
                         "v_sub_f32 v40, v40, v41\n\tv_mul_f32 v40, 0x3fb8aa3b, v40\n\tv_exp_f32 v40, v40\n\ts_nop 0\n\tv_cvt_pk_bf16_f32 v40, v40, v40\n\tds_write_b16 %3, v40\n\t"
                         "ds_read_b128 v[0:3], %4\n\tds_read_b128 v[4:7], %4 offset:192\n\tds_read_b128 v[8:11], %4 offset:384\n\tds_read_b128 v[12:15], %4 offset:576\n\t"
                         "ds_read_b128 v[16:19], %4 offset:768\n\tds_read_b128 v[20:23], %4 offset:960\n\tds_read_b128 v[24:27], %4 offset:1152\n\tds_read_b128 v[28:31], %4 offset:1344\n\t"
                         "ds_write_b32 %5, v41\n\t"
                         "s_waitcnt lgkmcnt(8)\n\t" SMF(32:35, 0:3, 160:167) SMF(36:39, 0:3, 224:231) "s_waitcnt lgkmcnt(7)\n\t" SMF(32:35, 4:7, 168:175) SMF(36:39, 4:7, 232:239)
                         "s_waitcnt lgkmcnt(6)\n\t" SMF(32:35, 8:11, 176:183) SMF(36:39, 8:11, 240:247) "s_waitcnt lgkmcnt(4)\n\t" SMF(32:35, 12:15, 184:191) SMF(36:39, 12:15, 248:255)
                         SMF(32:35, 16:19, 192:199) "s_waitcnt lgkmcnt(1)\n\t" SMF(32:35, 20:23, 200:207) SMF(32:35, 24:27, 208:215) SMF(32:35, 28:31, 216:223)
                         "s_nop 2\n\tv_pk_add_f32 v[36:37], v[36:37], v[38:39]\n\tds_write_b64 %7, v[36:37]\n\ts_nop 2\n\tv_pk_add_f32 v[32:33], v[32:33], v[34:35]\n\tds_write_b64 %6, v[32:33]"
                         :: "v"(idx), "v"(qa), "v"(qm), "v"(pw), "v"(pr), "v"(ma), "v"(oa), "v"(oc), "s"(ck1), "s"(ck2), "s"(csc)
                         : "memory", "v0", "v1", "v2", "v3", "v4", "v5", "v6", "v7", "v8", "v9", "v10", "v11", "v12", "v13", "v14", "v15", "v16", "v17", "v18", "v19", "v20", "v21", "v22", "v23",
                           "v24", "v25", "v26", "v27", "v28", "v29", "v30", "v31", "v32", "v33", "v34", "v35", "v36", "v37", "v38", "v39", "v40", "v41", "v42", "v43", "v44", "v45");
#undef SMF
#undef MAXD
            STAMP(20 + 32 * layer);
            BAR();
            if (__builtin_expect(wave == 0, 1)) {
                STAMP(5 + 32 * layer);
                comb_o0(*(const LAS f32x4*)(size_t)cma, *(const LAS f32x4*)(size_t)(cma + 16), *(const LAS f32x4*)(size_t)cxa, *(const LAS f32x4*)(size_t)cra,
                        *(const LAS f32x4*)(size_t)(cra + 1024), *(const LAS f32x4*)(size_t)(cra + 2048), *(const LAS f32x4*)(size_t)(cra + 3072), cso, 0x1111111111111111ull, false);
            }
            __builtin_amdgcn_sched_barrier(0);
            if (__builtin_expect(wave == 1, 1))
                comb_o1(*(const LAS f32x4*)(size_t)cma, *(const LAS f32x4*)(size_t)(cma + 16), *(const LAS f32x2*)(size_t)cxa, *(const LAS f32x4*)(size_t)cra, *(const LAS f32x4*)(size_t)(cra + 1024), cso, 0x0000111100001111ull, false);
            __builtin_amdgcn_sched_barrier(0);
            if (__builtin_expect(wave == 2, 1)) comb_ml(cma, cxa, cra, cso, false);
            MARK(e_q);
            if (wave < 3) STAMP(21 + 32 * layer);
            __builtin_amdgcn_s_setprio(0);
        }
    };

    unsigned nj = 0u, rS = 0u, nS = 0u; int ord;
    {
        const int tid = threadIdx.x, lane = tid & 63;
        asm volatile("ds_max_u32 %0, %1" :: "v"(lb + L_CNT + 64), "v"(tst) : "memory");
        unsigned mb = 0u;
        const u16* kbase = kc + wofs / 2;
        const u16* vbase = vc + wofs / 2;
        unsigned pt0 = 0u;
        if (wave == 7) {
            __builtin_amdgcn_s_setprio(3);
            if (lane < 10) *reinterpret_cast<u32x4*>(smem + L_XS + 640 + lane * 16) = (u32x4){0x3f803f80u, 0x3f803f80u, 0x3f803f80u, 0x3f803f80u};
            if (lane < 16) { *reinterpret_cast<u32x4*>(smem + L_CS + 768 + lane * 16) = (u32x4){0u, 0u, 0u, 0u}; }
        } else {
            if (wave >= 4) {
                if (wave == 4) st16_sc1(ws, WS_OWN + (unsigned)(gi * 16384 + (lane >> 5) * 8192 + gj * 512 + (lane & 31) * 16), (u32x4){~0u, ~0u, ~0u, ~0u});
                else empty5(0, (unsigned)(wave - 5) * 8192u, lane);
                if (wave == 6) {
                    __builtin_amdgcn_s_setprio(3);
                    if (gi + gj == 0) st4_sc1(ws, WS_R + (lane & 31) * 4, 0u);
                    st4_sc1(ws, WS_MB + (unsigned)((gi * 8 + gj) * 128 + (lane & 31) * 4), 0u);
                }
            } else if (wave >= 2) {
                unsigned long long c0, c1;
                asm volatile("s_waitcnt lgkmcnt(0)\n\ts_memtime %0\n\ts_waitcnt lgkmcnt(0)\n\ts_load_dword %2, %3, %4\n\ts_waitcnt lgkmcnt(0)\n\ts_memtime %1\n\ts_waitcnt lgkmcnt(0)"
                             : "=&s"(c0), "=&s"(c1), "=&s"(pt0) : "s"(ws), "s"((6u << 20) + ((((tst + g * 977u) * 2654435761u) >> 11 & 0x1fdf80u) | ((wave ^ gj) & 1) * 8192)) : "memory");
                pt0 = ((wave ^ gj) & 1) ? (unsigned)c1 - (unsigned)c0 : (unsigned)c0 - (unsigned)c1;
            }
            STAMP(36);
            *reinterpret_cast<u32x4*>(smem + L_P + tid * 16) = (u32x4){0u, 0u, 0u, 0u};
            if (tid < 72 * 12 - 448) *reinterpret_cast<u32x4*>(smem + L_P + 7168 + tid * 16) = (u32x4){0u, 0u, 0u, 0u};
            if (wave >= 4) {
                if (wave == 6) VMW(2); else VMW(1);
                asm volatile("ds_max_u32 %0, %1" :: "v"(lb + L_CNT + 64), "v"(now() - TLAND) : "memory");
                STAMP(40);
                lds_add(0, 1u);
            } else if (wave >= 2) { lds_add(20, pt0); VMW(0); lds_add(0, 1u); }
        }
        asm volatile("s_waitcnt lgkmcnt(0)" :: "s"(pt1) : "memory");
        const int rot = ROT;
        const unsigned r0 = now(), c0 = (unsigned)__builtin_amdgcn_s_memtime();
        u32x4 a[4];
        unsigned ko = (unsigned)((wave * 64 + (lane & 7)) * 384 + (((lane >> 3) & 1) * 4 + (lane >> 4)) * 16);
        PIN1(ko);
        auto k_group = [&](auto GC_) __attribute__((always_inline)) {
            constexpr int G = decltype(GC_)::value, R = RK + 24 * (G >> 1) + 4 * (G & 1);
            const unsigned o = ko + (unsigned)((((G >> 1) + rot) & 3) * 6144 + (G & 1) * 3072);
            pace(3);
            ld16_nt<R, 0>(kbase, o); ld16_nt<R + 8, 128>(kbase, o); ld16_nt<R + 16, 256>(kbase, o);
            const int lane = lane_id();
            const unsigned ho = WS_Q0 + (unsigned)((gi * 4 + (gj >> 1)) * 4096 + lane * 16);
            if (wave < 7) {
                asm volatile("; COND_BEGIN");
                if (G == 0 && wave >= 4) { VMW(3); lds_add(32, 1u); }
                if (G == 0 && wave < 4) {
                    if (wave < 2) { VMW(3); lds_add(0, 1u); load_gu(wave, lane); }
                    if (wave == 2) load_wd(lane);
                    if (wave == 3) {
#pragma unroll 1
                        for (int k = 5 - WEARLY; k < 5; ++k) load_wo(k, lane, true);
                    }
                }
                asm volatile("; COND_END");
            }
            if (wave == 7) {
                asm volatile("; COND_BEGIN");
                if (G == GP) {
                    VMW(3 * (GP + 1));
#pragma unroll 1
                    for (unsigned spins = 0; spins < (kSpinCap << 4); ++spins) {
                        if (lds_word(lb + L_CNT) >= 7u) break;
                        __builtin_amdgcn_s_sleep(1);
                    }
                    STAMP(26);
                    const unsigned char* p = smem + L_X0 + (lane >> 4) * 1280;
                    const float c0 = lo16(*reinterpret_cast<const u16*>(p));
                    const f32x2 c2 = {c0, c0};
                    f32x2 s2 = {0.f, 0.f}, q2 = s2;
#pragma unroll
                    for (int k = 0; k < 5; ++k) {
                        const u32x4 w = *reinterpret_cast<const u32x4*>(p + k * 256 + (lane & 15) * 16);
#pragma unroll
                        for (int e = 0; e < 4; ++e) { const f32x2 d = unpk(w[e]) - c2; s2 += d; q2 += d * d; }
                    }
                    const float s = row16_sum(s2[0] + s2[1]), qq = row16_sum(q2[0] + q2[1]);
                    const float ms = s * (1.f / DM);
                    const unsigned mv = pk2(c0 + ms, fmax2(qq - s * ms, 0.f) * (1.f / DM));
                    const float mb = lo16(mv), rden = frcp(bfr(__builtin_amdgcn_sqrtf(bfr(hi16(mv) + 1e-5f))));
                    const f32x2 mb2 = {mb, mb}, rd2 = {rden, rden};
                    if ((lane & 15) < 10) {
                        u32x4 w = *reinterpret_cast<const u32x4*>(p + gj * 160 + (lane & 15) * 16);
#pragma unroll
                        for (int e = 0; e < 4; ++e) w[e] = pk2v(unpk(pk2v(unpk(w[e]) - mb2)) * rd2);
                        *reinterpret_cast<u32x4*>(smem + L_XS + (lane >> 4) * 160 + (lane & 15) * 16) = w;
                    }
                    STAMP(30);
                }
                if (G == GP || G == GA) {
#pragma unroll 1
                    for (int blk = 0; blk < 3; ++blk) {
                        LANE_VARS();
                        const unsigned xb = lb + L_XS + (unsigned)((G == GP ? q4 : 4) * 160);
                        Frag80 f[4];
#pragma unroll
                        for (int i = 0; i < 4; ++i) f[i] = frag80(lb + L_WQ + (unsigned)(128 * blk + 32 * i + 8 * p4), 416, r0s, r1s, r2s);
                        const Frag80 b = rows80(xb + kq * 16, xb + 128 + kq * 8);
                        const u32x4 c = tiles4(f, b, 0x1111111111111111ull);
                        if (G == GP) *reinterpret_cast<u32x4*>(smem + L_OP + q4 * 768 + 256 * blk + p4 * 64 + kq * 16) = c;
                        else *reinterpret_cast<u32x4*>(smem + L_CS + 256 * blk + kq * 64 + p4 * 16) = c;
                    }
                    if (G == GP) {
                        asm volatile("" ::: "memory");
                        const int ln = lane_id();
                        f32x2 c[6];
                        unsigned m = 20u;
#pragma unroll
                        for (int k = 0; k < 6; ++k) {
                            c[k] = *reinterpret_cast<const f32x2*>(smem + L_OP + (k / 3) * 1536 + ln * 24 + (k % 3) * 8);
                            m = max(m, max(fexp(c[k][0]), fexp(c[k][1])));
                        }
                        m = max(m, dppu<0xB1>(m)); m = max(m, dppu<0x4E>(m)); m = max(m, dppu<0x141>(m)); m = max(m, dppu<0x140>(m));
                        m = max(max((unsigned)__builtin_amdgcn_readlane(m, 0), (unsigned)__builtin_amdgcn_readlane(m, 16)), max((unsigned)__builtin_amdgcn_readlane(m, 32), (unsigned)__builtin_amdgcn_readlane(m, 48)));
                        lds_set(lb + L_CNT + 76, m);
                        const float f = __uint_as_float((273u - m) << 23);
#pragma unroll
                        for (int k = 0; k < 2; ++k) {
                            const u32x2 w0 = enc21(c[3 * k][0] * f, c[3 * k][1] * f, c[3 * k + 1][0] * f), w1 = enc21(c[3 * k + 1][1] * f, c[3 * k + 2][0] * f, c[3 * k + 2][1] * f);
                            st16_sc1(ws, WS_Q0 + (unsigned)((gi * 4 + 2 * k + (ln >> 5)) * 4096 + gj * 512 + (ln & 31) * 16), (u32x4){w0[0], w0[1], w1[0], w1[1]});
                        }
                        STAMP(2);
                        WAIT1(pf, 3 * (GP + 1) + 2);
                    }
                }
                if (G == GA) {
                    const unsigned half = __builtin_amdgcn_s_getreg(2 << 6 | 20);
                    lds_set(lb + L_CNT + 68, lds_word(lb + L_CNT + 64) << 11 | lds_word(lb + L_CNT + 76) << 3 | 1u | (((int)lds_word(lb + L_CNT + 20) > 0) ^ half) << 1 | half << 2);
                    VMW(3 * (GA - GP));
#pragma unroll 1
                    for (unsigned spins = 0; spins < (kSpinCap << 4); ++spins) if (lds_word(lb + L_CNT + 32) >= 3u) break;
                    send_flag(lane_id());
                    lds_set(lb + L_CNT + 8, 1u);
                    if (XS) pace(XS, false);
                    STAMP(17);
                    f32x2 r2 = {0.f, 0.f};
#pragma unroll
                    for (int k = 0; k < 5; ++k) r2 += unpk(*reinterpret_cast<const unsigned*>(smem + L_WR + k * 256 + lane_id() * 4));
                    *reinterpret_cast<float*>(smem + L_CNT + 12) = wave_sum(r2[0] + r2[1]);
                }
                asm volatile("; COND_END");
            }
            if (wave == 6) {
                asm volatile("; COND_BEGIN");
                if (G == GB - 1) mb = ld4_sc1(ws, WS_MB + (unsigned)((gi * 8 + gj) * 128 + (gi & 7) * 4));
                if (G == GB) {
                    WAIT1(mb, 3);
#pragma unroll 1
                    for (unsigned spins = 0; spins < kSpinCap && !all_set(mb); ++spins) {
                        if ((spins & 7u) == 7u && lds_word(lb + L_CNT + 8)) send_flag(lane);
                        mb = ld4_sc1(ws, WS_MB + (unsigned)((gi * 8 + gj) * 128 + (gi & 7) * 4));
                        WAIT1(mb, 0);
                    }
                    mb = ld4_sc1(ws, WS_MB + (unsigned)((gi * 8 + (gi & 7)) * 128 + (lane & 7) * 4));
                }
                if (G == GC) {
                    WAIT1(mb, 3 * (GC - GB));
#pragma unroll 1
                    for (unsigned spins = 0; spins < kSpinCap && !all_set(mb); ++spins) {
                        if ((spins & 7u) == 7u && lds_word(lb + L_CNT + 8)) send_flag(lane);
                        mb = ld4_sc1(ws, WS_MB + (unsigned)((gi * 8 + (gi & 7)) * 128 + (lane & 7) * 4));
                        WAIT1(mb, 0);
                    }
                    lds_set(lb + L_CNT + 92, (__builtin_amdgcn_readlane(mb, gi & 7) & 4u) << 11);
                    int d = min(max((int)(mb - (tst << 11)) >> 11, -400), 600);
                    d += (int)dppu<0xB1>((unsigned)d); d += (int)dppu<0x4E>((unsigned)d); d += (int)dppu<0x141>((unsigned)d);
                    const unsigned tm = tst + (unsigned)(__builtin_amdgcn_readfirstlane(d) >> 3);
                    if (ROWM) lds_set(lb + L_CNT + 64, tm);
                    const float slow = (float)(now() - r0) * 24.f * 256.f * frcp((float)((unsigned)__builtin_amdgcn_s_memtime() - c0)) - 256.f;
                    lds_set(lb + L_CNT + 88, tm << 11 | (unsigned)__builtin_amdgcn_fmed3f(slow, 0.f, 63.f) << 5 | __builtin_popcount((unsigned)__builtin_amdgcn_ballot_w64((mb & 2u) != 0u) & 255u) << 1 | 1u);
                    report();
                    a[0] = ld16_sc1<0>(ws, ho); a[1] = ld16_sc1<1024>(ws, ho); a[2] = ld16_sc1<2048>(ws, ho); a[3] = ld16_sc1<3072>(ws, ho);
                    pace(XCOST, false);
                    STAMP(31);
                }
                asm volatile("; COND_END");
            } else if (G == GC) { for (int k = 0; k < 4; ++k) asm volatile("" : "=v"(a[k])); }
        };
        k_group(IC<0>{}); k_group(IC<1>{}); k_group(IC<2>{}); k_group(IC<3>{}); k_group(IC<4>{}); k_group(IC<5>{}); k_group(IC<6>{}); k_group(IC<7>{});
        {
            const int lane = lane_id();
            const unsigned v0 = (unsigned)((wave * 64 + 4 * (lane >> 4)) * 384 + (lane & 15) * 16);
            const unsigned v1 = v0 + 256 - (unsigned)((lane & 15) * 8);
            auto v_group = [&](auto HC) __attribute__((always_inline)) {
                constexpr int h = decltype(HC)::value, R = h < 8 ? RV + 16 * (h & 3) + 4 * (h >> 2) : RV + 64 + 2 * (h - 8), NV = h < 8 ? 2 * h + 2 : 4 * h - 12;
                const unsigned o = (h < 8 ? v0 : v1) + (unsigned)((((h < 8 ? h >> 1 : h) + rot) & 3) * 6144);
                pace(2);
                if (h < 8) { ld16_nt<R, (h & 1) * 768>(vbase, o); ld16_nt<R + 8, (h & 1) * 768 + 384>(vbase, o); }
                else { ld8_nt<R, 0>(vbase, o); ld8_nt<R + 8, 384>(vbase, o); ld8_nt<R + 16, 768>(vbase, o); ld8_nt<R + 24, 1152>(vbase, o); }
                if (wave == 7 && gi + gj == 0) {
                    asm volatile("; COND_BEGIN");
                    if (h == 3 || h == 6) mb = ld4_sc1(ws, WS_R + (unsigned)((lane & 31) * 4));
                    if (h == 4 || h == 7) {
                        WAIT1(mb, 2);
                        if (all_set(mb) && !lds_word(lb + L_CNT + 36)) answer(lane, mb);
                    }
                    asm volatile("; COND_END");
                }
                if (wave == 6) {
                    asm volatile("; COND_BEGIN");
                if (h == HD) {
                    asm volatile("s_waitcnt vmcnt(%4)" : "+v"(a[0]), "+v"(a[1]), "+v"(a[2]), "+v"(a[3]) : "i"(3 * (7 - GC) + NV) : "memory");
                    float sq[6] = {0.f, 0.f, 0.f, 0.f, 0.f, 0.f};
#pragma unroll
                    for (int k = 0; k < 4; ++k) {
                        const float f = __uint_as_float(((((unsigned)__builtin_amdgcn_ds_bpermute((2 * k + (lane >> 5)) * 4, (int)mb) >> 3) & 255u) - 19u) << 23);
#pragma unroll
                        for (int e = 0; e < 2; ++e) {
                            const unsigned lo = a[k][2 * e], hi = a[k][2 * e + 1];
                            const float fe = (int)hi < 0 ? __builtin_nanf("") : f;
                            sq[3 * e] += (float)((int)(lo << 11) >> 11) * fe;
                            sq[3 * e + 1] += (float)((int)(__builtin_amdgcn_alignbit(hi, lo, 21) << 11) >> 11) * fe;
                            sq[3 * e + 2] += (float)((int)(hi << 1) >> 11) * fe;
                        }
                    }
#pragma unroll
                    for (int e = 0; e < 3; ++e) {
                        const int pi = 3 * (lane & 31) + e;
                        *reinterpret_cast<unsigned*>(smem + L_Q + (pi >> 3) * 32 + (pi & 6) * 2 + (pi & 1) * 16) = pk2(x32_add(sq[2 * e]), x32_add(sq[2 * e + 1]));
                    }
                    asm volatile("s_waitcnt lgkmcnt(0)\n\tds_write_b32 %0, %1" :: "v"(lb + L_CNT + 4), "v"(1u) : "memory");
                    STAMP(27);
                    lead = LEAD * PACE * 4;
                }
                    if (h == 6) mb = ld4_sc1(ws, WS_MB + (unsigned)((gi * 8 + (gi & 7)) * 128 + 32));
                    if (h == 8) {
                        WAIT1(mb, 6);
                        const unsigned r = __builtin_amdgcn_readfirstlane(mb);
                        if (r) { adopt(r); STAMP(29); }
                    }
                    asm volatile("; COND_END");
                }
                if constexpr (h < 6) {
                    constexpr int i = 2 * h;
                    fix_k1(IC<i>{}, IC<20 - 6 * (i / 3) - i % 3 + NV>{});
                    fix_k1(IC<i + 1>{}, IC<20 - 6 * ((i + 1) / 3) - (i + 1) % 3 + NV>{});
                }
                if (h == 5) {
                    STAMP(1);
                    __builtin_amdgcn_s_setprio(0);
                    att_qk0();
                }
            };
            v_group(IC<0>{}); v_group(IC<1>{}); v_group(IC<2>{}); v_group(IC<3>{}); v_group(IC<4>{}); v_group(IC<5>{});
            v_group(IC<6>{}); v_group(IC<7>{}); v_group(IC<8>{}); v_group(IC<9>{}); v_group(IC<10>{}); v_group(IC<11>{});
            lds_add(16, 1u);
            if (wave == 3) lds_add(44, WEARLY);
        }
    }
    {
        STAMP(28);
        att_addr();
        const int lane = lane_id();
        cma = lb + L_M; cxa = lb + L_M + (unsigned)((lane >> 4) * 8); cra = lb + L_OP + (unsigned)(512 + (lane >> 4) * 2048 + (lane & 15) * 16);
        cso = WS_OWN + (unsigned)(gi * 16384 + gj * 512 + 256 + (lane >> 5) * 64 + (lane & 12) * 4 + (lane & 1) * 8192);
        PIN2(cma, cxa); PIN2(cra, cso);
        att_pv0();
    }
    {
        f32x4 m0, m1, o0, o1; f32x2 mm; unsigned r;
        asm volatile("s_mov_b64 exec, 1\n\tds_add_rtn_u32 %5, %6, %7 offset:%10\n\ts_mov_b64 exec, -1\n\t"
                     "ds_read_b128 %0, %6\n\tds_read_b128 %1, %6 offset:16\n\tds_read_b64 %2, %8\n\tds_read_b128 %3, %9\n\tds_read_b128 %4, %9 offset:1024\n\ts_waitcnt lgkmcnt(0)"
                     : "=&v"(m0), "=&v"(m1), "=&v"(mm), "=&v"(o0), "=&v"(o1), "=&v"(r)
                     : "v"(cma), "v"(1u), "v"(cxa), "v"(cra), "i"(L_CNT + 28 - L_M) : "memory");
        ord = __builtin_amdgcn_readfirstlane(r);
        if (ord == 7) { STAMP(5); comb_o1(m0, m1, mm, o0, o1, cso, 0x0000333300003333ull, true); lds_set(lb + L_CNT + 48, now() | 1u); STAMP(21); }
        MARK(e_t5);
        const int lane = lane_id();
        auto wait_cnt = [&](int off, unsigned v) __attribute__((always_inline)) {
#pragma unroll 1
            for (unsigned spins = 0; spins < (kSpinCap << 4); ++spins) { if (lds_word(lb + L_CNT + off) >= v) break; __builtin_amdgcn_s_sleep(1); }
        };
        if (ord == OML) {
            wait_cnt(16, 8u);
            unsigned xa, ra; ml_addr(lane, xa, ra);
            comb_ml(cma, xa, ra, WS_OWN + (unsigned)(gi * 16384 + gj * 512 + 384 + (lane & 1) * 8192), true);
            STAMP(24);
        } else if (ord == 0 && !lds_word(lb + L_CNT + 52)) {
            if (gi + gj == 0 && !lds_word(lb + L_CNT + 36)) {
                unsigned r = 0u;
#pragma unroll 1
                for (unsigned spins = 0; spins < kSpinCap; ++spins) {
                    r = ld4_sc1(ws, WS_R + (unsigned)((lane & 31) * 4));
                    WAIT1(r, 0);
                    if (all_set(r)) break;
                    if ((spins & 7u) == 7u) send_flag(lane);
                }
                answer(lane, r);
            }
#pragma unroll 1
            for (unsigned spins = 0; spins < kSpinCap; ++spins) {
                unsigned r = ld4_sc1(ws, WS_MB + (unsigned)((gi * 8 + (gi & 7)) * 128 + 32));
                WAIT1(r, 0);
                r = __builtin_amdgcn_readfirstlane(r);
                if (r) { adopt(r); break; }
                if ((spins & 7u) == 7u) { send_flag(lane); report(); }
                __builtin_amdgcn_s_sleep(16);
            }
            STAMP(29);
        } else if (ord == OML + 1) {
#pragma unroll 1
            for (unsigned spins = 0; spins < (kSpinCap << 6); ++spins) if (lds_word(lb + L_CNT + 24) >= 8u) break;
            const unsigned ra = lb + L_OP + (unsigned)((lane >> 5) * 4096 + (lane & 31) * 16);
            comb_o0(*(const LAS f32x4*)(size_t)cma, *(const LAS f32x4*)(size_t)(cma + 16), *(const LAS f32x4*)(size_t)(cma + (lane >> 5) * 16), *(const LAS f32x4*)(size_t)ra, *(const LAS f32x4*)(size_t)(ra + 1024),
                    *(const LAS f32x4*)(size_t)(ra + 2048), *(const LAS f32x4*)(size_t)(ra + 3072), WS_OWN + (unsigned)(gi * 16384 + gj * 512 + (lane & 60) * 4 + (lane & 1) * 8192), 0x3333333333333333ull, true);
            STAMP(22);
        }
        if (ord == EMODE) {
#pragma unroll 1
            for (unsigned spins = 0; spins < (kSpinCap << 8); ++spins) { if (lds_word(lb + L_CNT + 52)) break; __builtin_amdgcn_s_sleep(1); }
            const unsigned S = lds_word(lb + L_CNT + 80);
            empty4(0, S, lane); empty5(5, 0u, lane); empty5(5, 8192u, lane);
        }
#pragma unroll 1
        for (;;) {
            unsigned j;
            asm volatile("s_mov_b64 exec, 1\n\tds_add_rtn_u32 %0, %1, %2\n\ts_mov_b64 exec, -1\n\ts_waitcnt lgkmcnt(0)" : "=&v"(j) : "v"(lb + L_CNT + 40), "v"(1u) : "memory");
            j = __builtin_amdgcn_readfirstlane(j);
            if (j >= 5u - WEARLY) break;
            load_wo((int)j, lane, false); ++nj;
        }
        STAMP(23);
    }

    unsigned omr, owa, owb;
    auto op_addr = [&](int layer) __attribute__((always_inline)) {
        LANE_VARS();
        const int t = wave - 3;
        cma = WS_OWN + rS + (unsigned)(gi * 16384 + layer * 4096 + (lane >> 3) * 512 + t * 128 + (t == 3 ? 0 : (lane & 7) * 16));
        cxa = lb + L_O2 + (unsigned)((lane >> 3) * 416 + t * 128 + (t == 3 ? 0 : (lane & 7) * 16));
        const int pb = (lane & 4) ? 3 - p4 : p4, jp = 2 * pb + ((lane >> 2) & 1);
        cso = tile35(gj, 10 * layer + t) + (unsigned)((kq & 1) * 8192 + gi * 128 + (kq >> 1) * 64 + p4 * 16);
        cra = lb + L_O2 + (unsigned)(jp * 416 + kq * 16); omr = lb + L_O2 + (unsigned)(jp * 416 + 384);
        owa = lb + L_WO + (unsigned)(r0s * 160 + 32 * t + 8 * p4); owb = lb + L_WO + (unsigned)(r1s * 160 + 32 * t + 8 * p4);
        if (layer) PIN2(cma, cxa);
        PIN2(cso, cra); PIN3(omr, owa, owb);
    };
    auto chain = [&](auto LC) __attribute__((always_inline)) {
        constexpr int layer = decltype(LC)::value;
        unsigned h4 = WS_H4 + (unsigned)((layer * 32 + gi) * 32768) + (layer ? rS ^ 8192u : 0u);
        auto sides = [&]() __attribute__((always_inline)) { rS = lds_word(lb + L_CNT + 80); nS = lds_word(lb + L_CNT + 84); h4 += rS ^ 8192u; };
        if (wave < 3) {
            LANE_VARS();
            if (layer == 0) {
                const int l0 = wave == 0 ? 2 : wave - 1;
                const unsigned lo = WS_OWN + lds_word(lb + L_CNT + 84) + (unsigned)(gi * 16384 + (lane >> 3) * 512), lw = lb + L_O2 + (unsigned)((lane >> 3) * 416);
                unsigned lo0 = lo + (unsigned)(l0 * 128 + (lane & 7) * 16), lo1 = wave == 1 ? lo + 384 : lo0, lw0 = lw + (unsigned)(l0 * 128 + (lane & 7) * 16), lw1 = wave == 1 ? lw + 384 : lw0;
                PIN2(lo0, lo1); PIN2(lw0, lw1);
                GUARD(600);
                MARK(b_g2);
                unsigned tf = 0u;
#pragma unroll 1
                for (unsigned spins = 0; spins < (kSpinCap << 4); ++spins) { tf = lds_word(lb + L_CNT + 48); if (tf) break; }
                if (wave == 0) until(tf + DG2);
#pragma unroll 1
                for (unsigned spins = 0; spins < kSpinCap; ++spins) {
                    u32x4 o0 = ld16_sc1(ws, lo0), o1;
                    if (wave == 1) o1 = ld16_sc1(ws, lo1); else asm volatile("" : "=v"(o1));
                    if (nj) { if (wave == 1) VMW(2); else VMW(1); lds_add(44, nj); nj = 0u; }
                    asm volatile("s_waitcnt vmcnt(0)" : "+v"(o0), "+v"(o1) :: "memory");
                    if (wave != 1) o1 = o0;
                    *(LAS u32x4*)(size_t)lw0 = o0; *(LAS u32x4*)(size_t)lw1 = o1;
                    if (!any_empty(max(um4(o0), um4(o1)))) break;
                    if ((spins & 15u) == 15u) send_flag(lane);
                }
                MARK(e_g2);
                STAMP(25);
                BAR();
                sides();
                if (wave < 2) empty1(wave, rS, lane);
                else {
                    st16m(ws, WS_CS + (unsigned)((gi * 8 + gj) * 768 + lane * 16), *reinterpret_cast<const u32x4*>(smem + L_CS + lane * 16), 0xffffffffffffull);
                    empty5(10, 0u, lane); empty5(10, 8192u, lane);
                }
            } else BAR();
            {
                const unsigned im = lb + L_GU + (unsigned)(32 * wave + 8 * p4);
                Frag80 fg = frag80(im, 96, r0s, r1s, r2s), fu = frag80(im + 8192, 96, r0s, r1s, r2s);
                const unsigned xb = lb + L_XS + (unsigned)((n < 4 ? n : 4) * 160);
                unsigned b16 = xb + kq * 16, b8 = xb + 128 + kq * 8;
                unsigned sg = h4 + (unsigned)(n < 4 ? h4s(wave) + gj * 512 + kq * 64 + n * 16 : H4C + wave * 1024 + gj * 128 + kq * 16), su = sg + (n < 4 ? 256u : 64u);
                unsigned o = h4 + h4s(wave) + (unsigned)(lane * 16);
                unsigned oc = h4 + (unsigned)(H4C + wave * 1024 + ((lane >> 5) * 4 + p4) * 128 + (kq & 1) * 64 + q4 * 16);
                unsigned os = h4 + (unsigned)(H4T + (q4 + 4 * (kq & 1)) * 128 + p4 * 16);
                unsigned ha = lb + L_H + (unsigned)(p4 * 96 + wave * 32 + q4 * 8 + kq * 2);
                f32x2 cs = {0.f, 0.f};
                if (layer != 0) cs = *reinterpret_cast<const f32x2*>(smem + L_CS2 + wave * 512 + lane * 8);
                PIN3(fg.a0, fg.a1, fg.a2); PIN3(fu.a0, fu.a1, fu.a2); PIN3(b16, b8, sg); PIN3(su, o, os); PIN2(ha, cs);
                unsigned long long mk = layer ? 0x000f000f000f000full : 0x001f001f001f001full;
                unsigned ta = WS_MB + (unsigned)((lane >> 1) * 1024 + 64 + (lane & 1) * 16);
                if (layer == 0) PIN2(oc, ta);
                unsigned x4a = layer ? 0u : X4A * lds_word(lb + L_CNT + 20) >> 12;
                PINS(x4a);
                unsigned long long rdm = 0x3acccccd3acccccdull;
                PINS(mk); PINS(rdm);
                if (layer == 0) VMW(0);
                if (layer == 0) GUARD(1200); else GUARD(800);
                MARK(b_gu);
                BAR();
                STAMP(10 + 32 * layer);
                u32x4 tl = {0u, 0u, 0u, 0u};
                if (layer == 0) tl = ld16_sc1(ws, ta);
                const Frag80 b = rows80(b16, b8);
                const f32x4 g0 = mfma32(fg.a0, b.a0, z4), u0 = mfma32(fu.a0, b.a0, z4), g1 = mfma32(fg.a1, b.a1, g0), g2 = mfma16(fg.a2, b.a2, z4), u1 = mfma32(fu.a1, b.a1, u0), u2 = mfma16(fu.a2, b.a2, z4);
                __builtin_amdgcn_sched_barrier(0);
                const f32x4 cg = g1 + g2, cu = u1 + u2;
                st16m2<0>(ws, sg, asu(cg), su, asu(cu), mk);
                STAMP(11 + 32 * layer);
                if (layer == 0) {
                    WAIT1(tl, 2);
#pragma unroll 1
                    for (unsigned spins = 0; spins < kSpinCap && !all_set(min(min(tl[0], tl[1]), min(tl[2], tl[3]))); ++spins) {
                        tl = ld16_sc1(ws, ta);
                        WAIT1(tl, 0);
                    }
                    const unsigned tn = now();
                    unsigned d = min(min(min(tn - tl[0], tn - tl[1]), min(tn - tl[2], tn - tl[3])), 800u);
                    d = min(d, dppu<0xB1>(d)); d = min(d, dppu<0x4E>(d)); d = min(d, dppu<0x124>(d)); d = min(d, dppu<0x128>(d));
                    unsigned tr = tn - min(min((unsigned)__builtin_amdgcn_readlane(d, 0), (unsigned)__builtin_amdgcn_readlane(d, 16)), min((unsigned)__builtin_amdgcn_readlane(d, 32), (unsigned)__builtin_amdgcn_readlane(d, 48)));
                    lds_set(lb + L_CNT + 56, tr | 1u);
                    until(__builtin_amdgcn_readfirstlane(tr | 1u) + x4a);
                } else sched(X4, 0);
                STAMP(16 + 32 * layer);
                float hv;
                u32x4 a0, a1, a2, a3, ac, as;
                auto load = [&]() __attribute__((always_inline)) {
                    as = ld16_sc1(ws, os);
                    if (layer == 0) ac = ld16_sc1(ws, oc);
                    a0 = ld16_sc1<0>(ws, o); a1 = ld16_sc1<1024>(ws, o); a2 = ld16_sc1<2048>(ws, o); a3 = ld16_sc1<3072>(ws, o);
                };
                auto all_in = [&]() __attribute__((always_inline)) {
                    if (layer == 0) asm volatile("s_waitcnt vmcnt(0)" : "+v"(a0), "+v"(a1), "+v"(a2), "+v"(a3), "+v"(ac), "+v"(as) :: "memory");
                    else { asm volatile("s_waitcnt vmcnt(0)" : "+v"(a0), "+v"(a1), "+v"(a2), "+v"(a3), "+v"(as) :: "memory"); ac = as; }
                };
                auto compute = [&]() __attribute__((always_inline)) {
#define ROR(n) " row_ror:" #n " row_mask:0xf bank_mask:0xf\n\t"
#define QD2(p) " quad_perm:" p " row_mask:0xf bank_mask:0xf\n\t"
                    asm volatile("s_waitcnt vmcnt(%16)\n\tv_add_f32_dpp v40, %2, %2" ROR(8) "v_add_f32_dpp v41, %3, %3" ROR(8) "s_nop 0\n\tv_add_f32_dpp v40, v40, v40" ROR(4) "v_add_f32_dpp v41, v41, v41" ROR(4)
                                 "v_mov_b32 v42, v40\n\tv_mov_b32 v43, v41\n\ts_nop 0\n\tv_permlane16_swap_b32 v40, v42\n\tv_permlane16_swap_b32 v41, v43\n\t"
                                 "v_pk_add_f32 v[40:41], v[40:41], v[42:43]\n\tv_pk_mul_f32 v[40:41], v[40:41], %14\n\t"
                                 ".if %17 == 0\n\ts_waitcnt vmcnt(4)\n\tv_mov_b64 v[44:45], %12\n\tv_mov_b64 v[46:47], %13\n\t.endif\n\t"
                                 "v_fma_f32 v40, -v41, v41, v40\n\tv_cvt_pk_bf16_f32 v42, 0, v41\n\tv_maximum3_f32 v40, v40, 0, 0\n\t"
                                 ".if %17 == 0\n\tv_permlane32_swap_b32 v44, v46\n\tv_permlane32_swap_b32 v45, v47\n\tv_pk_add_f32 v[44:45], v[44:45], v[46:47]\n\t.endif\n\t"
                                 "v_cvt_pk_bf16_f32 v40, 0, v40\n\ts_waitcnt vmcnt(2)\n\tv_pk_add_f32 v[32:33], %4, %6\n\tv_add_f32 v40, 0x3727c5ac, v40\n\tv_pk_add_f32 v[34:35], %5, %7\n\t"
                                 ".if %17 == 0\n\tv_permlane16_swap_b32 v44, v45\n\t.endif\n\t"
                                 "v_cvt_pk_bf16_f32 v40, 0, v40\n\ts_waitcnt vmcnt(0)\n\tv_pk_add_f32 v[36:37], %8, %10\n\tv_sqrt_f32 v40, v40\n\tv_pk_add_f32 v[38:39], %9, %11\n\t"
                                 ".if %17 == 0\n\tv_add_f32_dpp v44, v44, v44" QD2("[1,0,3,2]") "v_add_f32_dpp v45, v45, v45" QD2("[1,0,3,2]") ".endif\n\t"
                                 "v_pk_add_f32 v[32:33], v[32:33], v[36:37]\n\tv_pk_add_f32 v[34:35], v[34:35], v[38:39]\n\tv_cvt_pk_bf16_f32 v40, 0, v40\n\t"
                                 ".if %17 == 0\n\tv_add_f32_dpp v44, v44, v44" QD2("[2,3,0,1]") "v_add_f32_dpp v45, v45, v45" QD2("[2,3,0,1]") "v_mov_b64 %1, v[44:45]\n\t.else\n\ts_nop 0\n\t.endif\n\t"
                                 "v_permlane32_swap_b32 v32, v34\n\tv_permlane32_swap_b32 v33, v35\n\tv_rcp_f32 v40, v40\n\tv_pk_add_f32 v[32:33], v[32:33], v[34:35]\n\ts_nop 1\n\t"
                                 "v_permlane16_swap_b32 v32, v33\n\t"
                                 "v_pk_fma_f32 v[32:33], %1, v[42:43], v[32:33] op_sel_hi:[1,0,1] neg_lo:[1,0,0] neg_hi:[1,0,0]\n\tv_pk_mul_f32 v[32:33], v[32:33], v[40:41] op_sel_hi:[1,0]\n\t"
                                 "v_cvt_pk_bf16_f32 v32, 0, v32\n\tv_mul_f32 v34, 0xbfb8aa3b, v32\n\tv_exp_f32 v34, v34\n\tv_cvt_pk_bf16_f32 v33, 0, v33\n\tv_add_f32 v34, 1.0, v34\n\t"
                                 "v_rcp_f32 v34, v34\n\ts_nop 0\n\tv_mul_f32 v32, v34, v32\n\tv_cvt_pk_bf16_f32 v32, 0, v32\n\tv_mul_f32 %0, v33, v32\n\tv_cvt_pk_bf16_f32 v32, %0, %0\n\tds_write_b16 %15, v32"
                                 : "=&v"(hv), "+v"(cs)
                                 : "v"(as[0]), "v"(as[1]), "v"(LO2(a0)), "v"(HI2(a0)), "v"(LO2(a1)), "v"(HI2(a1)), "v"(LO2(a2)), "v"(HI2(a2)), "v"(LO2(a3)), "v"(HI2(a3)),
                                   "v"(LO2(layer ? as : ac)), "v"(HI2(layer ? as : ac)), "s"(rdm), "v"(ha), "i"(layer ? 4 : 5), "i"(layer), "v"(as)
                                 : "memory", "v32", "v33", "v34", "v35", "v36", "v37", "v38", "v39", "v40", "v41", "v42", "v43", "v44", "v45", "v46", "v47");
#undef ROR
#undef QD2
                };
                RETRY(load(), (all_in(), max(max(max(um4(a0), um4(a1)), max(um4(a2), um4(a3))), max(um4(ac), um4(as)))), compute(), any_nan(hv, hv), layer ? X4 : X4A);
                BAR();
                MARK(e_gu);
                STAMP(12 + 32 * layer);
                if (layer == 0) {
                    *reinterpret_cast<f32x2*>(smem + L_CS2 + wave * 512 + lane * 8) = cs;
                }
            }
            if (layer == 0) {
                if (wave == 1) {
                    const unsigned o = WS_CS + (unsigned)(gi * 6144 + 3072 + (lane < 48 ? lane : 0) * 16);
                    u32x4 a[8];
                    GATHER8(a, o, 768);
                    WAIT8(a, 0);
                    if (lane < 48) *reinterpret_cast<f32x4*>(smem + L_CS + lane * 16) = SUM8(a);
                }
                Frag80 f[4];
#pragma unroll
                for (int i = 0; i < 4; ++i) { f[i] = frag80(lb + L_WQ + (unsigned)(128 * wave + 32 * i + 8 * p4), 416, r0s, r1s, r2s); PIN3(f[i].a0, f[i].a1, f[i].a2); }
                unsigned b16 = lb + L_XS + (unsigned)(q4 * 160 + kq * 16), b8 = lb + L_XS + (unsigned)(q4 * 160 + 128 + kq * 8);
                unsigned so = WS_H1 + rS + (unsigned)((gi * 4 + q4) * 16384 + gj * 896 + 256 * wave + kq * 64 + p4 * 16);
                unsigned long long mk = 0x1111111111111111ull;
                PIN3(b16, b8, so); PINS(mk);
                GUARD(3800);
                MARK(b_q);
                BAR();
                STAMP(33);
                const Frag80 b = rows80(b16, b8);
                st16_sc1(ws, so, tiles4(f, b, mk));
                STAMP(34);
                qk_addr(true); att_addr();
                cma = lb + L_M; cxa = lb + L_M + (unsigned)(wave == 0 ? (lane >> 5) * 16 : (lane >> 4) * 8);
                cra = lb + L_OP + (unsigned)(wave == 0 ? (lane >> 5) * 4096 + (lane & 31) * 16 : 512 + (lane >> 4) * 2048 + (lane & 15) * 16);
                if (wave == 2) ml_addr(lane, cxa, cra);
                cso = WS_OWN + rS + (unsigned)(gi * 16384 + 4096 + gj * 512) + (unsigned)(wave == 0 ? lane * 4 : wave == 1 ? 256 + (lane >> 5) * 64 + (lane & 15) * 4 : 384);
                PIN2(cma, cxa); PIN2(cra, cso);
                asm volatile("" : "=v"(omr), "=v"(owa), "=v"(owb));
            }
        } else {
            const int t = wave - 3;
            StatAddr sa;
            u32x4 sel;
            unsigned long long hi = 0xff00ff00ff00ff00ull;
            auto gather32 = [&](unsigned o, unsigned X, auto fin) __attribute__((always_inline)) {
                u32x4 a0 = ld16_sc1<-2048>(ws, o), a1 = ld16_sc1<-1024>(ws, o), a2 = ld16_sc1<0>(ws, o), a3 = ld16_sc1<1024>(ws, o);
                float v;
                auto sum = [&]() __attribute__((always_inline)) {
                    asm volatile("s_waitcnt vmcnt(3)\n\tv_mfma_f32_16x16x32_bf16 v[40:43], %1, %5, 0\n\ts_waitcnt vmcnt(2)\n\tv_mfma_f32_16x16x32_bf16 v[40:43], %2, %5, v[40:43]\n\t"
                                 "s_waitcnt vmcnt(1)\n\tv_mfma_f32_16x16x32_bf16 v[40:43], %3, %5, v[40:43]\n\ts_waitcnt vmcnt(0)\n\tv_mfma_f32_16x16x32_bf16 v[40:43], %4, %5, v[40:43]\n\t"
                                 "s_nop 7\n\tv_cndmask_b32 v40, v40, v41, %6\n\tv_cndmask_b32 v42, v42, v43, %6\n\ts_nop 1\n\tv_permlane32_swap_b32 v40, v42\n\tv_add_f32 v40, v40, v42\n\t"
                                 "v_cvt_pk_bf16_f32 %0, 0, v40"
                                 : "=&v"(v), "+v"(a0), "+v"(a1), "+v"(a2), "+v"(a3) : "v"(sel), "s"(hi) : "memory", "v40", "v41", "v42", "v43");
                    fin(v);
                    __builtin_amdgcn_sched_barrier(0);
                };
                sum();
                if (__builtin_expect(any_nan(v, v) != 0ull, 0)) {
#pragma unroll 1
                    for (unsigned spins = 0; spins < kSpinCap; ++spins) {
                        slip(X);
                        again16<-2048>(a0, ws, o); again16<-1024>(a1, ws, o); again16<0>(a2, ws, o); again16<1024>(a3, ws, o);
                        asm volatile("s_waitcnt vmcnt(0)" : "+v"(a0), "+v"(a1), "+v"(a2), "+v"(a3) :: "memory");
                        if (!any_empty(max(max(um4(a0), um4(a1)), max(um4(a2), um4(a3))))) break;
                    }
                    sum();
                }
            };
#define PUT8(A, B, M) "v_min_u32 v34, -2, v32\n\tv_min_u32 v32, -2, v32\n\tv_min_u32 v35, -2, v33\n\tv_min_u32 v33, -2, v33\n\tv_permlane16_swap_b32 v32, v34\n\ts_nop 0\n\t" \
                      "v_permlane16_swap_b32 v33, v35\n\ts_mov_b64 exec, " M "\n\tglobal_store_dwordx4 " A ", v[32:35], " B " sc1\n\ts_mov_b64 exec, -1\n\ts_nop 0"
            unsigned xw, qo, qw, qc;
            __builtin_amdgcn_s_setprio(0);
            {
                STAMP(6 + 32 * layer);
                bf16x8 af[6];
                unsigned long long mk = 0x000f000f000f000full;
                if (layer == 0) {
                    const int lane = lane_id();
                    xe = lo16(*reinterpret_cast<const u16*>(smem + L_X0 + xrow(lane) * 1280 + gj * 160 + t * 32 + xcol(lane) * 2));
                    op_addr(0);
                    GUARD(1500);
                    MARK(b_op);
                    if (nj | (ord == EMODE)) { VMW(0); lds_add(44, nj); }
#pragma unroll 1
                    for (unsigned spins = 0; spins < (kSpinCap << 4); ++spins) if (lds_word(lb + L_CNT + 44) >= 5u) break;
                    STAMP(8);
                } else MARK(b_op);
                const unsigned lo = cma, lw = cxa, so = cso, br = cra, mr = omr, h3 = tile35(gj, 10 * layer + t);
                unsigned go, xs;
#pragma unroll
                for (int s = 0; s < 6; ++s) af[s] = cat8(tr64o(owa, s * 32 * 160), tr64o(owb, s * 32 * 160));
                PINS(mk); PINS(hi);
                if (layer != 0 && t < 4) {
                    sched(X2, 0);
                    STAMP(49);
                    u32x4 ov = ld16_sc1(ws, lo);
                    WAIT1(ov, 0);
                    *(LAS u32x4*)(size_t)lw = ov;
                    if (__builtin_expect(any_empty(um4(ov)), 0)) {
#pragma unroll 1
                        for (unsigned spins = 0; spins < kSpinCap; ++spins) {
                            slip(X2);
                            ov = ld16_sc1(ws, lo);
                            WAIT1(ov, 0);
                            *(LAS u32x4*)(size_t)lw = ov;
                            if (!any_empty(um4(ov))) break;
                        }
                    }
                }
                {
                    bf16x8 b0, b1, b2, b3, b4, b5;
#define QP " row_half_mirror row_mask:0xf bank_mask:0xf\n\t"
                    asm volatile("s_waitcnt lgkmcnt(0)\n\ts_barrier\n\t"
                                 "ds_read_b128 %0, %12\n\tds_read_b64 v[36:37], %13\n\tds_read_b128 %1, %12 offset:64\n\tds_read_b128 %2, %12 offset:128\n\tds_read_b128 %3, %12 offset:192\n\t"
                                 "ds_read_b128 %4, %12 offset:256\n\tds_read_b128 %5, %12 offset:320\n\t"
                                 "s_waitcnt lgkmcnt(5)\n\tv_mfma_f32_16x16x32_bf16 v[32:35], %6, %0, 0\n\tv_max_f32_dpp v38, v36, v36" QP "v_sub_f32 v38, v36, v38\n\t"
                                 "s_waitcnt lgkmcnt(4)\n\tv_mfma_f32_16x16x32_bf16 v[32:35], %7, %1, v[32:35]\n\tv_exp_f32 v38, v38\n\t"
                                 "s_waitcnt lgkmcnt(3)\n\tv_mfma_f32_16x16x32_bf16 v[32:35], %8, %2, v[32:35]\n\tv_mul_f32 v37, v37, v38\n\t"
                                 "s_waitcnt lgkmcnt(2)\n\tv_mfma_f32_16x16x32_bf16 v[32:35], %9, %3, v[32:35]\n\tv_add_f32_dpp v39, v37, v37" QP
                                 "s_waitcnt lgkmcnt(1)\n\tv_mfma_f32_16x16x32_bf16 v[32:35], %10, %4, v[32:35]\n\tv_rcp_f32 v39, v39\n\t"
                                 "s_waitcnt lgkmcnt(0)\n\tv_mfma_f32_16x16x32_bf16 v[32:35], %11, %5, v[32:35]\n\tv_mul_f32 v38, v38, v39\n\ts_nop 6\n\t"
                                 "v_pk_mul_f32 v[32:33], v[32:33], v[38:39] op_sel_hi:[1,0]\n\tv_pk_mul_f32 v[34:35], v[34:35], v[38:39] op_sel_hi:[1,0]\n\ts_nop 0\n\t"
                                 "v_add_f32_dpp v32, v32, v32" QP "v_add_f32_dpp v33, v33, v33" QP "v_add_f32_dpp v34, v34, v34" QP "v_add_f32_dpp v35, v35, v35" QP
                                 "v_cvt_pk_bf16_f32 v32, v32, v33\n\tv_cvt_pk_bf16_f32 v33, v34, v35\n\t" PUT8("%14", "%15", "%16")
                                 : "=&v"(b0), "=&v"(b1), "=&v"(b2), "=&v"(b3), "=&v"(b4), "=&v"(b5)
                                 : "v"(af[0]), "v"(af[1]), "v"(af[2]), "v"(af[3]), "v"(af[4]), "v"(af[5]), "v"(br), "v"(mr), "v"(so), "s"(ws), "s"(mk)
                                 : "memory", "v32", "v33", "v34", "v35", "v36", "v37", "v38", "v39");
#undef QP
                }
                if (layer == 0 && wave == 7) st4_sc1(ws, WS_MB + (unsigned)(gi * 1024 + 64 + gj * 4), now() | 1u);
                const unsigned tp = layer ? 0u : now();
                if (layer == 0) sides();
                STAMP(7 + 32 * layer);
                {
                    const int ln = lane_id();
#pragma unroll
                    for (int k = 0; k < 4; ++k) sel[k] = ((ln >> 1) & 3) == k ? (ln & 1) ? 0x3f800000u : 0x3f80u : 0u;
                    go = h3 + nS + (unsigned)(2048 + ln * 16); xs = lb + L_XS + (unsigned)(xrow(ln) * 160 + t * 32 + xcol(ln) * 2);
                    PIN3(sel, go, xs);
                    sa = stats_addr(ln, h4 + (unsigned)(H4T + gj * 128), 16);
                }
                if (layer != 0) sched(X3, 0);
                else until(tp + W3);
                STAMP(18 + 32 * layer);
                if (layer != 0) gather32(go, X3, [&](float s) { xw = pk2(xe + s, 0.f); *(LAS u16*)(size_t)xs = (u16)xw; });
                else {
                    unsigned long long m0, m1, m2, m3; unsigned cnt = kSpinCap;
#define CHK(r, m) "v_max3_u32 v44, v" #r ", v[" #r "+1], v[" #r "+2]\n\tv_max_u32 v44, v44, v[" #r "+3]\n\tv_cmp_eq_u32 %" #m ", -1, v44\n\t"
                    asm volatile("s_mov_b64 %1, -1\n\ts_mov_b64 %2, -1\n\ts_mov_b64 %3, -1\n\ts_mov_b64 %4, -1\n"
                                 "1:\n\ts_mov_b64 exec, %1\n\tglobal_load_dwordx4 v[0:3], %6, %7 offset:-2048 sc1\n\ts_mov_b64 exec, %2\n\tglobal_load_dwordx4 v[4:7], %6, %7 offset:-1024 sc1\n\t"
                                 "s_mov_b64 exec, %3\n\tglobal_load_dwordx4 v[8:11], %6, %7 sc1\n\ts_mov_b64 exec, %4\n\tglobal_load_dwordx4 v[12:15], %6, %7 offset:1024 sc1\n\ts_mov_b64 exec, -1\n\t"
                                 "s_sub_u32 %5, %5, 1\n\ts_cselect_b64 vcc, 0, -1\n\ts_waitcnt vmcnt(0)\n\t"
                                 "v_mfma_f32_16x16x32_bf16 v[40:43], v[0:3], %8, 0\n\t" CHK(0, 1) "v_mfma_f32_16x16x32_bf16 v[40:43], v[4:7], %8, v[40:43]\n\t" CHK(4, 2)
                                 "v_mfma_f32_16x16x32_bf16 v[40:43], v[8:11], %8, v[40:43]\n\t" CHK(8, 3) "v_mfma_f32_16x16x32_bf16 v[40:43], v[12:15], %8, v[40:43]\n\t" CHK(12, 4)
                                 "s_or_b64 exec, %1, %2\n\ts_or_b64 exec, exec, %3\n\ts_or_b64 exec, exec, %4\n\ts_and_b64 vcc, exec, vcc\n\ts_cbranch_scc1 1b\n\t"
                                 "s_mov_b64 exec, -1\n\tv_cndmask_b32 v40, v40, v41, %9\n\tv_cndmask_b32 v42, v42, v43, %9\n\ts_nop 1\n\tv_permlane32_swap_b32 v40, v42\n\tv_add_f32 v40, v40, v42\n\t"
                                 "v_cvt_pk_bf16_f32 v40, 0, v40\n\tv_add_f32 v40, %10, v40\n\tv_cvt_pk_bf16_f32 %0, v40, 0\n\tds_write_b16 %11, %0"
                                 : "=&v"(xw), "=&s"(m0), "=&s"(m1), "=&s"(m2), "=&s"(m3), "+s"(cnt) : "v"(go), "s"(ws), "v"(sel), "s"(hi), "v"(xe), "v"(xs)
                                 : "memory", "vcc", "scc", "v0", "v1", "v2", "v3", "v4", "v5", "v6", "v7", "v8", "v9", "v10", "v11", "v12", "v13", "v14", "v15", "v40", "v41", "v42", "v43", "v44");
#undef CHK
                }
                xe = lo16(xw);
                STAMP(9 + 32 * layer);
            }
            BAR();
            if (wave == 3) stats(sa);
            MARK(e_op);
            STAMP(11 + 32 * layer);
            {
                LANE_VARS();
                const unsigned im = lb + L_WD + (unsigned)(32 * t + 8 * p4);
                bf16x8 wd0 = cat8(tr64(im + r0s * 160), tr64(im + r1s * 160)); bf16x4 wd1 = tr64(im + (32 + r2s) * 160);
                const unsigned h5 = tile35(gj, 10 * layer + 5 + t);
                unsigned h16 = lb + L_H + (unsigned)(p4 * 96 + kq * 16), h8 = lb + L_H + (unsigned)(p4 * 96 + 64 + kq * 8);
                unsigned so = h5 + (unsigned)((kq & 1) * 8192 + gi * 128 + (kq >> 1) * 64 + p4 * 16);
                unsigned long long mk = 0x000f000f000f000full;
                unsigned go = h5 + nS + (unsigned)(2048 + lane * 16), xs = (layer ? (unsigned)(xrow(lane) * 1280 + gj * 160) : lb + L_XS + (unsigned)(xrow(lane) * 160)) + (unsigned)(t * 32 + xcol(lane) * 2);
                unsigned sta = lb + L_ST + (unsigned)(xrow(lane) * 16);
                if (layer == 0 && wave == 4) {
                    asm volatile("" : "=v"(sa.a16), "=v"(sa.b16), "=v"(sa.a8), "=v"(sa.b8), "=v"(sa.dst), "=v"(qo), "=v"(qw), "=v"(qc), "=v"(cma), "=v"(cxa), "=v"(cra), "=v"(cso), "=v"(omr), "=v"(owa), "=v"(owb));
                    asm volatile("" : "=v"(qa), "=v"(pw), "=v"(ma), "=v"(idx), "=v"(qm), "=v"(pr), "=v"(oa), "=v"(oc));
                } else if (layer == 0) {
                    sa = stats_addr(lane, WS_H1 + rS + (unsigned)(gi * 65536 + gj * 896 + 768), 16384);
                    op_addr(1); qk_addr(false); att_addr();
                    const int l5 = lane & 31, c = l5 < 24 ? 24 * (wave - 6) + l5 : 48;
                    qo = WS_H1 + rS + (unsigned)((gi * 4 + (gj >> 1)) * 16384 + 3584 + (lane >> 5) * 896 + c * 16);
                    qw = lb + L_Q + (unsigned)(l5 < 24 ? (c >> 4) * 128 + (c & 3) * 32 + (c & 12) + (lane >> 5) * 16 : lane < 26 ? 384 + (lane & 1) * 16 : 448);
                    qc = lb + L_CS + (unsigned)(c * 16 + (lane >> 5) * 8);
                    PIN3(qo, qw, qc);
                }
                PIN2(wd0, wd1); PIN3(h16, h8, so); PIN3(go, xs, sta); PINS(mk);
                GUARD(2000);
                MARK(b_dn);
                if (wave == 4) {
                    unsigned os = h4 + (unsigned)(H4T + (p4 + 4 * (kq & 1)) * 128 + q4 * 16);
                    float sw = *reinterpret_cast<const float*>(smem + L_CNT + 12);
                    PIN2(os, sw);
                    if (layer == 0) {
#pragma unroll 1
                        for (unsigned spins = 0; spins < (kSpinCap << 4); ++spins) if (lds_word(lb + L_CNT + 56)) break;
                    }
                    sched(layer ? X4 : X4A, 0);
                    u32x4 as;
#pragma unroll 1
                    for (unsigned spins = 0; spins < kSpinCap; ++spins) { as = ld16_sc1(ws, os); WAIT1(as, 0); if (!any_empty(um4(as))) break; }
                    float ss = __uint_as_float(as[0]), sm = __uint_as_float(as[1]), rd = __uint_as_float(as[2]);
                    ss += dpp<0x4E>(ss); sm += dpp<0x4E>(sm); rd += dpp<0x4E>(rd);
                    ss += dpp<0xB1>(ss); sm += dpp<0xB1>(sm); rd += dpp<0xB1>(rd);
                    ss = x16_add(ss); sm = x16_add(sm); rd = x16_add(rd);
                    if (LANES(0x1111ull)) *reinterpret_cast<f32x4*>(smem + L_ST + q4 * 16) = (f32x4){ss, sm, rd, sw};
                }
                if (layer == 0 && wave > 4) {
#pragma unroll 1
                    for (unsigned spins = 0; spins < (kSpinCap << 4); ++spins) { if (lds_word(lb + L_CNT + 56)) break; __builtin_amdgcn_s_sleep(1); }
                    sched(X4A, 5);
                    if (wave == 5) empty4(1, rS, lane);
                    else st16m(ws, lane < 40 ? tile35(gj, 15 + (lane >> 3)) + (unsigned)((wave - 6) * 8192 + gi * 128 + (lane & 7) * 16)
                                             : WS_OWN + rS + (unsigned)(gi * 16384 + 4096 + gj * 512 + (wave - 6) * 256 + (lane - 40) * 16), (u32x4){~0u, ~0u, ~0u, ~0u}, 0xffffffffffffffull);
                }
                asm volatile("s_waitcnt lgkmcnt(0)\n\ts_barrier\n\tds_read_b128 v[36:39], %2\n\tds_read_b64 v[40:41], %3\n\t"
                             "s_waitcnt lgkmcnt(1)\n\tv_mfma_f32_16x16x32_bf16 v[32:35], %0, v[36:39], 0\n\ts_waitcnt lgkmcnt(0)\n\tv_mfma_f32_16x16x16_bf16 v[42:45], %1, v[40:41], 0\n\ts_nop 7\n\t"
                             "v_pk_add_f32 v[32:33], v[32:33], v[42:43]\n\tv_pk_add_f32 v[34:35], v[34:35], v[44:45]\n\t"
                             "v_cvt_pk_bf16_f32 v32, v32, v33\n\tv_cvt_pk_bf16_f32 v33, v34, v35\n\t" PUT8("%4", "%5", "%6")
                             :: "v"(wd0), "v"(wd1), "v"(h16), "v"(h8), "v"(so), "s"(ws), "s"(mk)
                             : "memory", "v32", "v33", "v34", "v35", "v36", "v37", "v38", "v39", "v40", "v41", "v42", "v43", "v44", "v45");
                STAMP(13 + 33 * layer);
                if (layer == 0 || gi == 4 * t + (gj & 3)) {
                    const f32x4 sr = *(const LAS f32x4*)(size_t)sta;
                    const f32x2 st = ln_stats(sr[0], sr[1]);
                    float rs = bfr(frcp(1.f + ex2(bfr((sr[2] - st[0] * sr[3]) * st[1]) * -LOG2E))), n1 = bfr(bfr(xe - st[0]) * st[1]);
                    PIN2(rs, n1);
                    if (layer == 0) {
                        sched(X5A, 0);
                        STAMP(15);
                        gather32(go, X5A, [&](float y) { xw = pk2(xe + bfr(n1 + bfr(y * rs)), 0.f); *(LAS u16*)(size_t)xs = (u16)xw; });
                        xe = lo16(xw);
                        STAMP(14);
                    } else {
                        sched(X5B, 0);
                        STAMP(47);
                        gather32(go, X5B, [&](float y) { xw = pk2(xe + bfr(n1 + bfr(y * rs)), 0.f); });
                        st2(out, xs, xw);
                        __builtin_amdgcn_endpgm();
                        MARK(e_dn);
                        STAMP(45);
                    }
                }
            }
            if (layer == 0) {
                BAR();
                if (wave == 3) { stats(sa); STAMP(34); }
                MARK(e_dn);
                if (wave == 3) __builtin_amdgcn_s_setprio(2);
                if (__builtin_expect(wave == 4, 0)) { op_addr(1); qk_addr(false); att_addr(); }
                if (wave >= 6) {
                    const unsigned o = qo;
                    f32x2 cs = -*(const LAS f32x2*)(size_t)qc;
                    float rn = 1.f / DM;
                    unsigned long long m0 = 1ull << 24, m1 = 1ull << 25;
                    PIN2(cs, rn); PINS(m0); PINS(m1);
                    MARK(b_qg);
                    sched(X1, 0);
                    STAMP(51);
                    u32x4 a0, a1, a2, a3; f32x2 u;
                    RETRY({ a0 = ld16_sc1<-3584>(ws, o); a1 = ld16_sc1<-1792>(ws, o); a2 = ld16_sc1<0>(ws, o); a3 = ld16_sc1<1792>(ws, o); },
                          ({ asm volatile("s_waitcnt vmcnt(0)" : "+v"(a0), "+v"(a1), "+v"(a2), "+v"(a3) :: "memory"); max(max(um4(a0), um4(a1)), max(um4(a2), um4(a3))); }),
                          { unsigned sm;
                            asm volatile("s_waitcnt vmcnt(2)\n\tv_pk_add_f32 v[40:41], %2, %4\n\tv_pk_add_f32 v[42:43], %3, %5\n\t"
                                         "s_waitcnt vmcnt(1)\n\tv_pk_add_f32 v[40:41], v[40:41], %6\n\tv_pk_add_f32 v[42:43], v[42:43], %7\n\t"
                                         "s_waitcnt vmcnt(0)\n\tv_pk_add_f32 v[40:41], v[40:41], %8\n\tv_pk_add_f32 v[42:43], v[42:43], %9\n\ts_nop 1\n\t"
                                         "v_permlane32_swap_b32 v40, v42\n\tv_permlane32_swap_b32 v41, v43\n\tv_pk_add_f32 v[40:41], v[40:41], v[42:43]\n\ts_nop 0\n\t"
                                         "v_readlane_b32 %1, v41, 24\n\ts_nop 1\n\tv_mul_f32 v42, %1, %11\n\tv_cvt_pk_bf16_f32 v42, 0, v42\n\tv_pk_fma_f32 %0, %10, v[42:43], v[40:41] op_sel_hi:[1,0,1]"
                                         : "=&v"(u), "=&s"(sm) : "v"(LO2(a0)), "v"(HI2(a0)), "v"(LO2(a1)), "v"(HI2(a1)), "v"(LO2(a2)), "v"(HI2(a2)), "v"(LO2(a3)), "v"(HI2(a3)), "v"(cs), "v"(rn)
                                         : "memory", "v40", "v41", "v42", "v43");
                            unsigned w = pk2v(u);
                            if (LANES(m0)) w = __float_as_uint(u[0]);
                            if (LANES(m1)) w = __float_as_uint(u[1]);
                            *(LAS unsigned*)(size_t)qw = w; __builtin_amdgcn_sched_barrier(0); }, any_nan(u[0], u[1]), X1);
                    MARK(e_qg);
                }
            }
        }
    };
    PINS(ck1); PINS(ck2); PINS(csc);
    chain(IC<0>{});
    STAMP(35);
    att1();
    chain(IC<1>{});
}

__device__ __attribute__((used, noinline)) void mega_code_end() { asm volatile(".Lmega_code_end:"); }

extern "C" void kernel_launch(void* const* d_in, const int* in_sizes, int n_in, void* d_out, int out_size, void* d_ws,
                              size_t ws_size, hipStream_t stream) {
    const u16* x0 = (const u16*)d_in[0];
    const u16* kc = (const u16*)d_in[1];
    const u16* vc = (const u16*)d_in[2];
    const u16* wq = (const u16*)d_in[3];
    const u16* wo = (const u16*)d_in[4];
    const u16* wr = (const u16*)d_in[6];
    const u16* wg = (const u16*)d_in[7];
    const u16* wu = (const u16*)d_in[8];
    const u16* wd = (const u16*)d_in[9];
    (void)hipFuncSetAttribute((const void*)mega_kernel, hipFuncAttributeMaxDynamicSharedMemorySize, L_END);
    mega_kernel<<<256, 512, L_END, stream>>>(x0, kc, vc, wq, wo, wr, wg, wu, wd, (u16*)d_out, (unsigned char*)d_ws
                                             );
}
```

```cpp
#include <hip/hip_runtime.h>
#include <hip/hip_bf16.h>
#include <stdint.h>

typedef unsigned short u16;
typedef unsigned u32x4 __attribute__((ext_vector_type(4)));
typedef unsigned u32x2 __attribute__((ext_vector_type(2)));
typedef unsigned long long u64x8 __attribute__((ext_vector_type(8)));
typedef unsigned long long u64x2 __attribute__((ext_vector_type(2)));
typedef float f32x4 __attribute__((ext_vector_type(4)));
typedef short s16x4 __attribute__((ext_vector_type(4)));
typedef __attribute__((ext_vector_type(4))) __bf16 bf16x4;
typedef __attribute__((ext_vector_type(8))) __bf16 bf16x8;
#define LAS __attribute__((address_space(3)))

constexpr int DM = 640, HQ = 32, SK = 4096, DH = 192, WIN = 1024, FF = 1536;
constexpr int QD = HQ * DH;
constexpr float SCALE = 0.07216878364870322f, LOG2E = 1.44269504088896341f;
constexpr unsigned kSpinCap = 1u << 16;
#define GJ(g) (((g) >> 3) & 7)
#define GI(g) (((g) & 7) * 4 + ((g) >> 6))

#define D4A 111
#define D5A 92
#define D1 84
#define D2 133
#define D3 66
#define D4 76
#define D5B 75
#define PACE 59
#define PINIT 100
#define PMIN 50
#define DM_ 0
constexpr unsigned X4A = D4A + DM_, X5A = X4A + D5A + DM_, X1 = X5A + D1 + DM_, X2 = X1 + D2 + DM_, X3 = X2 + D3 + DM_, X4 = X3 + D4 + DM_, X5B = X4 + D5B + DM_;
#define WOC 2
#define WOFF 6
#define WEARLY 1
#define DG2 12
#define XCOST 10
#define GUC 3
#define XS 0
#define LEAD 12
#define TOFF 12
#define UG 360
#define SLOW 0
#define TLAND 125
#define ROWM 1
#define W3 15
#define ROT gj
#define GROT 1
#define CMODE 1
#define BETA 11
#define WDW 2
#define WOW 3
#define ADOPT 1
constexpr unsigned NUNIT = 384 + XCOST + XS + GUC * (9 + 2 * WEARLY), PBURST = 30;
#define GSTEPS GA = 1, GB = 6, GC = 7, HD = 1
#define OML 2
#define EMODE (OML + 1)
constexpr int GP = 0, GSTEPS;

constexpr unsigned WS_H1 = 0;
constexpr unsigned WS_H4 = 2u << 20;
constexpr unsigned H4C = 20480, H4T = H4C + 3072;
constexpr unsigned WS_H35 = 4u << 20;
constexpr unsigned WS_OWN = WS_H35 + 80 * 16384;
constexpr unsigned WS_CS = WS_OWN + 32 * 16384;
constexpr unsigned WS_MB = WS_CS + 256 * 768;
constexpr unsigned WS_R = WS_MB + 256 * 128;
constexpr unsigned WS_END = WS_R + 128;
constexpr unsigned WS_Q0 = 8u << 20;
static_assert(WS_END <= (6u << 20), "ws map");
__device__ __forceinline__ unsigned tile35(int j, int q) { return WS_H35 + (unsigned)((j * 10 + (q >> 1)) * 16384 + (q & 1) * 4096); }
__device__ __forceinline__ unsigned h4s(int s) { return (unsigned)((s >> 1) * 16384 + (s & 1) * 4096); }

constexpr int L_WQ = 0;
constexpr int L_WO = 33792;
constexpr int L_GU = L_WO + 30720;
constexpr int L_WD = L_GU + 16384;
constexpr int L_X0 = L_WD + 8192;
constexpr int L_WR = L_X0 + 5120;
constexpr int L_XS = L_WR + 2048;
constexpr int L_Q = L_XS + 1024;
constexpr int L_OP = L_Q + 512;
constexpr int L_M = L_OP + 8192;
constexpr int L_H = L_M + 64;
constexpr int L_ST = L_H + 384;
constexpr int L_CS = L_ST + 64;
constexpr int L_CS2 = L_CS + 1024;
constexpr int L_CNT = L_CS2 + 1536;
constexpr int L_P = L_CNT + 96;
constexpr int L_O2 = L_P + 72 * 192;
constexpr int L_DBG = L_O2 + 3328;
constexpr int L_END = L_DBG + 2048;
static_assert(L_END <= 163840 && L_END > 81920, "LDS");

typedef __attribute__((ext_vector_type(2))) __bf16 bf16x2;
typedef float f32x2 __attribute__((ext_vector_type(2)));
__device__ __forceinline__ unsigned pk2(float a, float b) { return __builtin_bit_cast(unsigned, __builtin_convertvector((f32x2){a, b}, bf16x2)); }
__device__ __forceinline__ unsigned fexp(float x) { const unsigned e = (__float_as_uint(x) >> 23) & 255u; return e == 255u ? 0u : e; }
__device__ __forceinline__ u32x2 enc21(float a, float b, float c) {
    const float lim = 1048575.f;
    const unsigned i = (unsigned)(int)__builtin_rintf(__builtin_amdgcn_fmed3f(a, -lim, lim)) & 0x1fffffu, j = (unsigned)(int)__builtin_rintf(__builtin_amdgcn_fmed3f(b, -lim, lim)) & 0x1fffffu;
    const unsigned k = (unsigned)(int)__builtin_rintf(__builtin_amdgcn_fmed3f(c, -lim, lim)) & 0x1fffffu;
    return (u32x2){i | j << 21, j >> 11 | k << 10 | (__builtin_fabsf(a) + __builtin_fabsf(b) + __builtin_fabsf(c) < 1e30f ? 0u : 0x80000000u)};
}
__device__ __forceinline__ float lo16(unsigned w) { return __uint_as_float(w << 16); }
__device__ __forceinline__ float hi16(unsigned w) { return __uint_as_float(w & 0xffff0000u); }
__device__ __forceinline__ float bfr(float f) { return __uint_as_float(pk2(0.f, f)); }
template <int CTRL> __device__ __forceinline__ float dpp(float x) {
    return __builtin_bit_cast(float, __builtin_amdgcn_mov_dpp(__builtin_bit_cast(int, x), CTRL, 0xf, 0xf, true));
}
template <int CTRL> __device__ __forceinline__ unsigned dppu(unsigned x) { return (unsigned)__builtin_amdgcn_mov_dpp((int)x, CTRL, 0xf, 0xf, true); }
__device__ __forceinline__ float row16_sum(float v) {
    v += dpp<0xB1>(v); v += dpp<0x4E>(v); v += dpp<0x141>(v); v += dpp<0x140>(v); return v;
}
__device__ __forceinline__ float fmax2(float a, float b) { return __builtin_elementwise_maximum(a, b); }
__device__ __forceinline__ float fmax3(float a, float b, float c) { return fmax2(fmax2(a, b), c); }
__device__ __forceinline__ float x16_add(float v) { auto s = __builtin_amdgcn_permlane16_swap(__float_as_uint(v), __float_as_uint(v), false, false); return __uint_as_float(s[0]) + __uint_as_float(s[1]); }
__device__ __forceinline__ float x32_add(float v) { auto s = __builtin_amdgcn_permlane32_swap(__float_as_uint(v), __float_as_uint(v), false, false); return __uint_as_float(s[0]) + __uint_as_float(s[1]); }
__device__ __forceinline__ float wave_sum(float v) { return x32_add(x16_add(row16_sum(v))); }
__device__ __forceinline__ float rs32(float a, float b) { auto s = __builtin_amdgcn_permlane32_swap(__float_as_uint(a), __float_as_uint(b), false, false); return __uint_as_float(s[0]) + __uint_as_float(s[1]); }
__device__ __forceinline__ f32x2 sw16(float a, float b) { auto s = __builtin_amdgcn_permlane16_swap(__float_as_uint(a), __float_as_uint(b), false, false); return (f32x2){__uint_as_float(s[0]), __uint_as_float(s[1])}; }

#define GUARD(n) asm volatile("s_branch 1f\n\t.p2align 12,," #n "\n1:")
#define MARK(n) do {} while (0)
#define BAR() asm volatile("s_waitcnt lgkmcnt(0)\n\ts_barrier" ::: "memory")
#define VMW(n) asm volatile("s_waitcnt vmcnt(%0)" :: "i"(n) : "memory")

template <int OFF = 0> __device__ __forceinline__ u32x4 ld16_sc1(const void* sbase, unsigned voff) {
    u32x4 r; asm volatile("global_load_dwordx4 %0, %1, %2 offset:%3 sc1" : "=v"(r) : "v"(voff), "s"(sbase), "i"(OFF) : "memory"); return r;
}
__device__ __forceinline__ unsigned ld4_sc1(const void* sbase, unsigned voff) {
    unsigned r; asm volatile("global_load_dword %0, %1, %2 sc1" : "=v"(r) : "v"(voff), "s"(sbase) : "memory"); return r;
}
template <int OFF = 0> __device__ __forceinline__ void st16_sc1(void* sbase, unsigned voff, u32x4 v) {
    asm volatile("global_store_dwordx4 %0, %1, %2 offset:%3 sc1\n\ts_nop 1" :: "v"(voff), "v"(v), "s"(sbase), "i"(OFF) : "memory");
}
__device__ __forceinline__ void st4_sc1(void* sbase, unsigned voff, unsigned v) {
    asm volatile("global_store_dword %0, %1, %2 sc1" :: "v"(voff), "v"(v), "s"(sbase) : "memory");
}
__device__ __forceinline__ void st8(void* sbase, unsigned voff, u32x2 v) {
    asm volatile("global_store_dwordx2 %0, %1, %2" :: "v"(voff), "v"(v), "s"(sbase) : "memory");
}
__device__ __forceinline__ void st2(void* sbase, unsigned voff, unsigned v) {
    asm volatile("global_store_short %0, %1, %2" :: "v"(voff), "v"(v), "s"(sbase) : "memory");
}
constexpr int RK = 64, RV = 160;
template <int R, int IMM> __device__ __forceinline__ void ld16_nt(const void* sbase, unsigned voff) {
    asm volatile("global_load_dwordx4 v[%c3:%c4], %0, %1 offset:%2 nt" :: "v"(voff), "s"(sbase), "i"(IMM), "i"(R), "i"(R + 3) : "memory");
}
template <int R, int IMM> __device__ __forceinline__ void ld8_nt(const void* sbase, unsigned voff) {
    asm volatile("global_load_dwordx2 v[%c3:%c4], %0, %1 offset:%2 nt" :: "v"(voff), "s"(sbase), "i"(IMM), "i"(R), "i"(R + 1) : "memory");
}
template <int R> __device__ __forceinline__ void smf(f32x4& d, u32x4 a, unsigned idx) {
    asm volatile("v_smfmac_f32_16x16x64_bf16 %0, %1, v[%c3:%c4], %2" : "+v"(d) : "v"(a), "v"(idx), "i"(R), "i"(R + 7));
}
template <int A, int B> __device__ __forceinline__ void zip_in() {
    unsigned t;
    asm volatile("v_perm_b32 %0, v%c2, v%c1, %3\n\tv_perm_b32 v%c2, v%c2, v%c1, %4\n\tv_mov_b32 v%c1, %0" : "=&v"(t) : "i"(A), "i"(B), "s"(0x05040100u), "s"(0x07060302u));
}
template <int A, int B> __device__ __forceinline__ void zip_out(unsigned& lo, unsigned& hi) {
    asm volatile("v_perm_b32 %0, v%c3, v%c2, %4\n\tv_perm_b32 %1, v%c3, v%c2, %5" : "=&v"(lo), "=&v"(hi) : "i"(A), "i"(B), "s"(0x05040100u), "s"(0x07060302u));
}
template <int A, int B, int C, int D> __device__ __forceinline__ void zip_x(unsigned lo, unsigned hi) {
    asm volatile("v_perm_b32 v%c4, v%c3, v%c2, %6\n\tv_perm_b32 v%c5, v%c3, v%c2, %7\n\tv_mov_b32 v%c2, %0\n\tv_mov_b32 v%c3, %1"
                 :: "v"(lo), "v"(hi), "i"(A), "i"(B), "i"(C), "i"(D), "s"(0x05040100u), "s"(0x07060302u));
}
__device__ __forceinline__ void dma16(const void* sbase, unsigned voff, unsigned lds_dst) {
    unsigned keep;
    asm volatile("s_mov_b32 %0, m0\n\ts_mov_b32 m0, %3\n\ts_nop 0\n\tglobal_load_lds_dwordx4 %1, %2\n\ts_mov_b32 m0, %0"
                 : "=&s"(keep) : "v"(voff), "s"(sbase), "s"(lds_dst) : "memory");
}
__device__ __forceinline__ unsigned um4(u32x4 a) { return max(max(a[0], a[1]), max(a[2], a[3])); }
__device__ __forceinline__ bool all_set(unsigned t) { return __builtin_amdgcn_ballot_w64(t == 0u) == 0ull; }
__device__ __forceinline__ bool any_empty(unsigned mx) { return __builtin_amdgcn_ballot_w64(mx == ~0u) != 0ull; }
__device__ __forceinline__ unsigned long long any_nan(float a, float b) { return __builtin_amdgcn_ballot_w64(__builtin_isunordered(a, b)); }
__device__ __forceinline__ f32x4 asf(u32x4 a) { return __builtin_bit_cast(f32x4, a); }
__device__ __forceinline__ u32x4 asu(f32x4 a) { return __builtin_bit_cast(u32x4, a); }
#define LANES(m) __builtin_amdgcn_inverse_ballot_w64(m)

__device__ __forceinline__ bf16x4 tr64(unsigned lds_addr) { return __builtin_amdgcn_ds_read_tr16_b64_v4bf16((LAS bf16x4*)(size_t)lds_addr); }
__device__ __forceinline__ bf16x4 tr64o(unsigned lds_addr, int off) { return __builtin_amdgcn_ds_read_tr16_b64_v4bf16((LAS bf16x4*)((LAS char*)(size_t)lds_addr + off)); }
__device__ __forceinline__ bf16x8 cat8(bf16x4 a, bf16x4 b) { return __builtin_shufflevector(a, b, 0, 1, 2, 3, 4, 5, 6, 7); }
__device__ __forceinline__ bf16x8 lds16(unsigned a) { return *(const LAS bf16x8*)(size_t)a; }
__device__ __forceinline__ bf16x4 lds8(unsigned a) { return *(const LAS bf16x4*)(size_t)a; }
__device__ __forceinline__ f32x4 mfma32(bf16x8 a, bf16x8 b, f32x4 c) { return __builtin_amdgcn_mfma_f32_16x16x32_bf16(a, b, c, 0, 0, 0); }
__device__ __forceinline__ f32x4 mfma32u(u32x4 a, u32x4 b, f32x4 c) { return mfma32(__builtin_bit_cast(bf16x8, a), __builtin_bit_cast(bf16x8, b), c); }
__device__ __forceinline__ f32x4 mfma16(bf16x4 a, bf16x4 b, f32x4 c) {
    return __builtin_amdgcn_mfma_f32_16x16x16bf16_1k(__builtin_bit_cast(s16x4, a), __builtin_bit_cast(s16x4, b), c, 0, 0, 0);
}

#define STAMP(i) do {} while (0)

template <int OFF = 0> __device__ __forceinline__ void st16m(void* sbase, unsigned voff, u32x4 v, unsigned long long mask) {
    asm volatile("s_mov_b64 exec, %4\n\tglobal_store_dwordx4 %0, %1, %2 offset:%3 sc1\n\ts_mov_b64 exec, -1\n\ts_nop 0" :: "v"(voff), "v"(v), "s"(sbase), "i"(OFF), "s"(mask) : "memory");
}
template <int OFF> __device__ __forceinline__ void st16m2(void* sbase, unsigned voff, u32x4 v, unsigned voff2, u32x4 v2, unsigned long long mask) {
    asm volatile("s_mov_b64 exec, %6\n\tglobal_store_dwordx4 %0, %1, %2 sc1\n\tglobal_store_dwordx4 %4, %5, %2 offset:%3 sc1\n\ts_mov_b64 exec, -1\n\ts_nop 0"
                 :: "v"(voff), "v"(v), "s"(sbase), "i"(OFF), "v"(voff2), "v"(v2), "s"(mask) : "memory");
}
#define PINS(a) asm volatile("" : "+s"(a))
#define RETRY(LOAD, EMPTY, COMPUTE, NAN_, X) do { LOAD; COMPUTE; if (__builtin_expect((NAN_) != 0ull, 0)) { \
    _Pragma("unroll 1") for (unsigned spins_ = 0; spins_ < kSpinCap; ++spins_) { slip(X); LOAD; if (!any_empty(EMPTY)) break; } COMPUTE; } } while (0)
__device__ __forceinline__ unsigned now() { return (unsigned)__builtin_amdgcn_s_memrealtime(); }
__device__ __forceinline__ void until(unsigned t) {
    asm volatile("1:\n\ts_memrealtime vcc\n\ts_waitcnt lgkmcnt(0)\n\ts_sub_u32 vcc_lo, %0, vcc_lo\n\ts_cmp_lt_u32 vcc_lo, 0x400\n\ts_cbranch_scc1 1b" :: "s"(t - 1u) : "vcc", "scc", "memory");
}
__device__ __forceinline__ unsigned lds_word(unsigned a) {
    unsigned t_; asm volatile("ds_read_b32 %0, %1\n\ts_waitcnt lgkmcnt(0)\n\ts_nop 0" : "=v"(t_) : "v"(a) : "memory");
    return __builtin_amdgcn_readfirstlane(t_);
}
__device__ __forceinline__ void lds_set(unsigned a, unsigned v) { asm volatile("ds_write_b32 %0, %1" :: "v"(a), "v"(v) : "memory"); }
__device__ __forceinline__ int xrow(int lane) { return (lane >> 5) * 2 + ((lane >> 3) & 1); }
__device__ __forceinline__ int xcol(int lane) { return ((lane >> 4) & 1) * 8 + (lane & 7); }
__device__ __forceinline__ int swz(int r) { return r ^ (((r >> 3) & 1) << 2); }
__device__ __forceinline__ f32x2 unpk(unsigned w) { return (f32x2){lo16(w), hi16(w)}; }
__device__ __forceinline__ unsigned pk2v(f32x2 v) { return __builtin_bit_cast(unsigned, __builtin_convertvector(v, bf16x2)); }
__device__ __forceinline__ float frcp(float x) { return __builtin_amdgcn_rcpf(x); }
__device__ __forceinline__ float ex2(float x) { return __builtin_amdgcn_exp2f(x); }
__device__ __forceinline__ int lane_id() { int l; asm volatile("v_mbcnt_lo_u32_b32 %0, -1, 0\n\tv_mbcnt_hi_u32_b32 %0, -1, %0" : "=v"(l)); return l & 63; }
__device__ __forceinline__ f32x2 ln_stats(float ss, float s) {
    const float mean = s * (1.f / DM);
    const float var = fmax2(__builtin_fmaf(-mean, mean, ss * (1.f / DM)), 0.f);
    return (f32x2){bfr(mean), frcp(bfr(__builtin_amdgcn_sqrtf(bfr(bfr(var) + 1e-5f))))};
}
#define GATHER8(a, o, S) do { a[0] = ld16_sc1<-4 * (S)>(ws, o); a[1] = ld16_sc1<-3 * (S)>(ws, o); a[2] = ld16_sc1<-2 * (S)>(ws, o); a[3] = ld16_sc1<-(S)>(ws, o); \
    a[4] = ld16_sc1<0>(ws, o); a[5] = ld16_sc1<(S)>(ws, o); a[6] = ld16_sc1<2 * (S)>(ws, o); a[7] = ld16_sc1<3 * (S)>(ws, o); } while (0)
template <int OFF> __device__ __forceinline__ void again16(u32x4& r, const void* sbase, unsigned voff) {
    asm volatile("v_cmp_eq_u32 vcc, -1, %1\n\ts_mov_b64 exec, vcc\n\tglobal_load_dwordx4 %0, %2, %3 offset:%4 sc1\n\ts_mov_b64 exec, -1" : "+v"(r) : "v"(um4(r)), "v"(voff), "s"(sbase), "i"(OFF) : "memory", "vcc");
}
#define AGAIN8(a, o, S) do { again16<-4 * (S)>(a[0], ws, o); again16<-3 * (S)>(a[1], ws, o); again16<-2 * (S)>(a[2], ws, o); again16<-(S)>(a[3], ws, o); \
    again16<0>(a[4], ws, o); again16<(S)>(a[5], ws, o); again16<2 * (S)>(a[6], ws, o); again16<3 * (S)>(a[7], ws, o); } while (0)
#define WAIT8(a, N) asm volatile("s_waitcnt vmcnt(%8)" : "+v"((a)[0]), "+v"((a)[1]), "+v"((a)[2]), "+v"((a)[3]), "+v"((a)[4]), "+v"((a)[5]), "+v"((a)[6]), "+v"((a)[7]) : "i"(N) : "memory")
#define UM32(a) max(max(max(um4(a[0]), um4(a[1])), max(um4(a[2]), um4(a[3]))), max(max(um4(a[4]), um4(a[5])), max(um4(a[6]), um4(a[7]))))
#define SUM8(a) (((((((asf(a[0]) + asf(a[1])) + asf(a[2])) + asf(a[3])) + asf(a[4])) + asf(a[5])) + asf(a[6])) + asf(a[7]))
#define WAIT1(x, N) asm volatile("s_waitcnt vmcnt(%1)" : "+v"(x) : "i"(N) : "memory")
#define LO2(x) __builtin_shufflevector(x, x, 0, 1)
#define HI2(x) __builtin_shufflevector(x, x, 2, 3)
#define ACC8(a, s) do { f32x2 l_, h_; \
    asm volatile("s_waitcnt vmcnt(5)\n\tv_pk_add_f32 %0, %2, %4\n\tv_pk_add_f32 %1, %3, %5\n\tv_pk_add_f32 %0, %0, %6\n\tv_pk_add_f32 %1, %1, %7\n\t" \
                 "s_waitcnt vmcnt(3)\n\tv_pk_add_f32 %0, %0, %8\n\tv_pk_add_f32 %1, %1, %9\n\tv_pk_add_f32 %0, %0, %10\n\tv_pk_add_f32 %1, %1, %11\n\t" \
                 "s_waitcnt vmcnt(1)\n\tv_pk_add_f32 %0, %0, %12\n\tv_pk_add_f32 %1, %1, %13\n\tv_pk_add_f32 %0, %0, %14\n\tv_pk_add_f32 %1, %1, %15\n\t" \
                 "s_waitcnt vmcnt(0)\n\tv_pk_add_f32 %0, %0, %16\n\tv_pk_add_f32 %1, %1, %17\n\ts_nop 0" \
                 : "=&v"(l_), "=&v"(h_) : "v"(LO2(a[0])), "v"(HI2(a[0])), "v"(LO2(a[1])), "v"(HI2(a[1])), "v"(LO2(a[2])), "v"(HI2(a[2])), "v"(LO2(a[3])), "v"(HI2(a[3])), \
                   "v"(LO2(a[4])), "v"(HI2(a[4])), "v"(LO2(a[5])), "v"(HI2(a[5])), "v"(LO2(a[6])), "v"(HI2(a[6])), "v"(LO2(a[7])), "v"(HI2(a[7])) : "memory"); \
    s = __builtin_shufflevector(l_, h_, 0, 1, 2, 3); } while (0)
#define RETRY8(a, o, S, s, MID, POST, NAN_, X) do { GATHER8(a, o, S); MID; ACC8(a, s); POST; if (__builtin_expect((NAN_) != 0ull, 0)) { \
    _Pragma("unroll 1") for (unsigned spins_ = 0; spins_ < kSpinCap; ++spins_) { slip(X); AGAIN8(a, o, S); WAIT8(a, 0); if (!any_empty(UM32(a))) break; } s = SUM8(a); POST; } } while (0)
#define PIN1(a) asm volatile("" : "+v"(a))
#define PIN2(a, b) asm volatile("" : "+v"(a), "+v"(b))
#define PIN3(a, b, c) asm volatile("" : "+v"(a), "+v"(b), "+v"(c))
#define LANE_VARS() const int lane = lane_id(); \
    const int kq = lane >> 4, n = lane & 15, q4 = n >> 2, p4 = lane & 3; \
    const int r0s = 8 * kq + q4 + 4 * (kq & 1), r1s = 8 * kq + q4 + 4 * ((kq & 1) ^ 1);     \
    const int r2s = swz(4 * kq + q4);                                                       \
    (void)r0s; (void)r1s; (void)r2s; (void)p4
template <int V> struct IC { static constexpr int value = V; };
struct Frag80 { bf16x8 a0, a1; bf16x4 a2; };

__global__ void __launch_bounds__(512, 2) __attribute__((amdgpu_num_vgpr(32)))
mega_kernel(const u16*, const u16*, const u16*, const u16*, const u16*, const u16*, const u16*, const u16*, const u16*, u16*, unsigned char*
            ) {
    extern __shared__ __attribute__((aligned(16))) unsigned char smem[];
    const unsigned lb = (unsigned)(size_t)smem;
    const int wave = __builtin_amdgcn_readfirstlane(threadIdx.x >> 6);
    u64x8 ar0; u64x2 ar1; unsigned long long ar2, tp, tst64;
    asm volatile("s_load_dwordx16 %0, %5, 0\n\ts_load_dwordx4 %1, %5, 0x40\n\ts_load_dwordx2 %2, %5, 0x50\n\ts_load_dwordx2 %3, %5, %6\n\ts_memrealtime %4"
                 : "=&s"(ar0), "=&s"(ar1), "=&s"(ar2), "=&s"(tp), "=&s"(tst64) : "s"(__builtin_amdgcn_kernarg_segment_ptr()), "s"((0xa9876421u >> (4 * wave) & 15u) * 8u) : "memory");
    const int g = blockIdx.x, gj = GJ(g), gi = GI(g);
    const f32x4 z4 = {0.f, 0.f, 0.f, 0.f};
    asm volatile("" ::: "v255");
    auto sched = [&](unsigned X, unsigned add) __attribute__((always_inline)) {
        u32x2 v;
        asm volatile("ds_read2_b32 %0, %1 offset0:14 offset1:5\n\ts_waitcnt lgkmcnt(0)" : "=v"(v) : "v"(lb + L_CNT) : "memory");
        until(__builtin_amdgcn_readfirstlane(v[0]) + (X * __builtin_amdgcn_readfirstlane(v[1]) >> 12) + add);
    };
    auto slip = [&](unsigned X) __attribute__((always_inline)) {
        if (X) lds_set(lb + L_CNT + 56, now() - (X * lds_word(lb + L_CNT + 20) >> 12));
    };
    float xe = 0.f;

    auto code_pf = [&](int slice, int lane) __attribute__((always_inline)) {
        unsigned long long pc0; unsigned len, back, t;
        asm volatile("s_getpc_b64 %0\n.Lmega_pf%=:\n\ts_mov_b32 %1, .Lmega_code_end-.Lmega_code_begin\n\ts_mov_b32 %2, .Lmega_pf%=-.Lmega_code_begin" : "=s"(pc0), "=s"(len), "=s"(back));
        unsigned off = (unsigned)(slice * 4096 + lane * 64); if (off > len) off = len;
        asm volatile("global_load_dword %0, %1, %2" : "=v"(t) : "v"(off & ~3u), "s"(pc0 - back) : "memory");
        return t;
    };
    asm volatile(".Lmega_code_begin:");
    unsigned pf = 0u, pt1;
    const unsigned wofs = (unsigned)((((gj >> 1) * HQ + gi) * SK + (SK - WIN) + (gj & 1) * 512) * (DH * 2));
    {
        const int lane = threadIdx.x & 63, hi = lane >= 26;
        if (threadIdx.x < 24) lds_set(lb + L_CNT + threadIdx.x * 4, 0u);
        unsigned t2 = (unsigned)(gi * (DH * DM * 2)), t7 = (32u << 20) + (unsigned)(g * 128), to = (unsigned)(gj * 128), w2 = wofs;
        PINS(t2); PINS(t7); PINS(to); PINS(w2);
        to = wave == 7 ? t7 : to; to = wave == 2 ? t2 : to; to = wave < 2 ? w2 : to;
        if (wave == 7) pf = code_pf((g >> 3) & 15, lane);
        unsigned vo = (unsigned)((gj * 80 + swz(2 * wave) + hi) * (QD * 2) + gi * (DH * 2) + (lane - 26 * hi) * 16), d = lb + L_WQ + wave * 832, keep;
        unsigned xo = (0x11043210u >> (4 * wave) & 15u) * 1024 + (unsigned)(lane & (wave < 6 ? 63 : 15)) * 16, xd = lb + L_X0 + min(wave, 6) * 1024;
        PIN2(vo, xo); PINS(d); PINS(xd); PINS(to);
        __builtin_amdgcn_sched_barrier(0);
        asm volatile("s_waitcnt lgkmcnt(0)\n\ts_load_dword %0, %4, %6" : "=&s"(pt1), "+s"(ar0), "+s"(ar1), "+s"(ar2), "+s"(tp), "+s"(tst64) : "s"(to) : "memory");
        dma16((const void*)(wave < 5 ? ar0[0] : ar0[5]), xo, xd);
        asm volatile("s_mov_b32 %0, m0\n\ts_mov_b32 m0, %2\n\ts_mov_b64 exec, %4\n\tglobal_load_lds_dwordx4 %1, %3 nt\n\t"
                     ".rept 4\n\ts_add_u32 m0, m0, 6656\n\tv_add_u32 %1, 0x30000, %1\n\tglobal_load_lds_dwordx4 %1, %3 nt\n\t.endr\n\t"
                     "s_mov_b64 exec, -1\n\ts_mov_b32 m0, %0\n\ts_barrier"
                     : "=&s"(keep), "+v"(vo) : "s"(d), "s"(ar0[3]), "s"(0x0003fffffcffffffull) : "memory", "scc");
        __builtin_amdgcn_sched_barrier(0);
    }
    const u16 *const kc = (const u16*)ar0[1], *const vc = (const u16*)ar0[2], *const wo = (const u16*)ar0[4], *const wg = (const u16*)ar0[6], *const wu = (const u16*)ar0[7], *const wd = (const u16*)ar1[0];
    u16* const out = (u16*)ar1[1];
    unsigned char* const ws = (unsigned char*)((ar2 + (32u << 20) - 1) & ~(unsigned long long)((16u << 20) - 1));
    const unsigned tst = (unsigned)tst64;
    auto fix_k1 = [&](auto IC_, auto NC) __attribute__((always_inline)) {
        constexpr int A = RK + 8 * decltype(IC_)::value, B = A + 4;
        unsigned t0, t1, t2, t3;
        asm volatile("s_waitcnt vmcnt(%12)\n\tv_mov_b32 %0, v%c4\n\tv_mov_b32 %1, v%c5\n\tv_mov_b32 %2, v%c6\n\tv_mov_b32 %3, v%c7\n\t"
                     "v_mov_b32_dpp v%c4, v%c8 row_ror:8 row_mask:0xf bank_mask:0xc\n\tv_mov_b32_dpp v%c5, v%c9 row_ror:8 row_mask:0xf bank_mask:0xc\n\t"
                     "v_mov_b32_dpp v%c6, v%c10 row_ror:8 row_mask:0xf bank_mask:0xc\n\tv_mov_b32_dpp v%c7, v%c11 row_ror:8 row_mask:0xf bank_mask:0xc\n\t"
                     "v_mov_b32_dpp v%c8, %0 row_ror:8 row_mask:0xf bank_mask:0x3\n\tv_mov_b32_dpp v%c9, %1 row_ror:8 row_mask:0xf bank_mask:0x3\n\t"
                     "v_mov_b32_dpp v%c10, %2 row_ror:8 row_mask:0xf bank_mask:0x3\n\tv_mov_b32_dpp v%c11, %3 row_ror:8 row_mask:0xf bank_mask:0x3"
                     : "=&v"(t0), "=&v"(t1), "=&v"(t2), "=&v"(t3)
                     : "i"(A), "i"(A + 1), "i"(A + 2), "i"(A + 3), "i"(B), "i"(B + 1), "i"(B + 2), "i"(B + 3), "i"(decltype(NC)::value) : "memory");
    };
    auto trans_v = [&](auto UC, auto NC) __attribute__((always_inline)) {
        constexpr int R = RV + 4 * decltype(UC)::value, N = decltype(NC)::value;
#define CELL(r, c) R + 16 * (r) + (c), R + 16 * (r) + 8 + (c)
        unsigned a0, a1, b0, b1, c0, c1;
        VMW(N + 6); zip_in<CELL(0, 0)>(); zip_out<CELL(0, 1)>(a0, a1); zip_out<CELL(0, 2)>(b0, b1); zip_out<CELL(0, 3)>(c0, c1);
        VMW(N + 4); zip_x<CELL(1, 0), CELL(0, 1)>(a0, a1); zip_in<CELL(1, 1)>(); zip_out<CELL(1, 2)>(a0, a1);
        VMW(N + 2); zip_x<CELL(2, 0), CELL(0, 2)>(b0, b1); zip_x<CELL(2, 1), CELL(1, 2)>(a0, a1); zip_in<CELL(2, 2)>(); zip_out<CELL(1, 3)>(a0, a1); zip_out<CELL(2, 3)>(b0, b1);
        VMW(N); zip_x<CELL(3, 0), CELL(0, 3)>(c0, c1); zip_x<CELL(3, 1), CELL(1, 3)>(a0, a1); zip_x<CELL(3, 2), CELL(2, 3)>(b0, b1); zip_in<CELL(3, 3)>();
#undef CELL
    };
    auto trans_w = [&](auto UC, auto NC) __attribute__((always_inline)) {
        constexpr int R = RV + 64 + 2 * decltype(UC)::value, N = decltype(NC)::value;
        unsigned a0, a1;
        VMW(N + 2); zip_in<R, R + 8>(); zip_out<R + 1, R + 9>(a0, a1);
        VMW(N); zip_x<R + 16, R + 24, R + 1, R + 9>(a0, a1); zip_in<R + 17, R + 25>();
    };
    auto frag80 = [&](unsigned im, int rs, int r0s, int r1s, int r2s) __attribute__((always_inline)) -> Frag80 {
        return Frag80{cat8(tr64(im + r0s * rs), tr64(im + r1s * rs)), cat8(tr64(im + (r0s + 32) * rs), tr64(im + (r1s + 32) * rs)), tr64(im + (r2s + 64) * rs)};
    };
    auto mm80 = [&](const Frag80& a, const Frag80& b) __attribute__((always_inline)) -> f32x4 {
        return mfma32(a.a1, b.a1, mfma32(a.a0, b.a0, z4)) + mfma16(a.a2, b.a2, z4);
    };
    auto rows80 = [&](unsigned a16, unsigned a8) __attribute__((always_inline)) -> Frag80 { const Frag80 f{lds16(a16), lds16(a16 + 64), lds8(a8)}; __builtin_amdgcn_sched_barrier(0); return f; };
    auto empty5 = [&](int q0, unsigned S, int lane) __attribute__((always_inline)) {
        st16m(ws, tile35(gj, q0 + (lane >> 3)) + S + (unsigned)(gi * 128 + (lane & 7) * 16), (u32x4){~0u, ~0u, ~0u, ~0u}, 0xffffffffffull);
    };
    auto empty4 = [&](int layer, unsigned S, int lane) __attribute__((always_inline)) {
        const unsigned b = WS_H4 + (unsigned)((layer * 32 + gi) * 32768) + (S ^ 8192u);
        st16_sc1(ws, b + (unsigned)((lane >> 5) * 4096 + gj * 512 + (lane & 31) * 16), (u32x4){~0u, ~0u, ~0u, ~0u});
        st16m(ws, b + (unsigned)(lane < 32 ? 16384 + gj * 512 + lane * 16 : H4C + ((lane - 32) >> 3) * 1024 + gj * 128 + (lane & 7) * 16), (u32x4){~0u, ~0u, ~0u, ~0u},
              layer ? 0xff000000ffffffffull : ~0ull);
    };
    int lead = wave == 7 ? LEAD * PACE * 4 : 0;
    auto pace = [&](unsigned n, bool wait = true) __attribute__((always_inline)) {
        unsigned u, w, t0;
        asm volatile("s_mov_b64 exec, 1\n\tds_add_rtn_u32 %0, %3, %4\n\tds_max_u32 %3, %5 offset:12\n\tds_add_rtn_u32 %1, %3, %6 offset:12\n\tds_read_b32 %2, %3 offset:4\n\ts_mov_b64 exec, -1\n\ts_waitcnt lgkmcnt(0)"
                     : "=&v"(u), "=&v"(w), "=&v"(t0) : "v"(lb + L_CNT + 60), "v"(n), "v"((now() - tst) << 6), "v"(n * PMIN * 4u) : "memory");
        int tn = (int)(__builtin_amdgcn_readfirstlane(t0) - tst + PINIT) * 64 + __builtin_amdgcn_readfirstlane(u) * (PACE * 4);
        tn += max((int)__builtin_amdgcn_readfirstlane(u) - UG, 0) * (SLOW * 4);
        const int tw = __builtin_amdgcn_readfirstlane(w) - PBURST * 64;
        if (tw > tn) tn = tw;
        tn -= lead;
        if (wait) {
#pragma unroll 1
            for (int k = 0; k < 2048 && (int)((now() - tst) << 6) < tn; ++k) {}
        }
    };
    auto slot = [&](unsigned u) __attribute__((always_inline)) {
        const unsigned tn = lds_word(lb + L_CNT + 64) + PINIT + (u * PACE + (NUNIT - UG) * SLOW) / 16;
#pragma unroll 1
        for (int k = 0; k < 8192 && (int)(now() - tn) < 0; ++k) __builtin_amdgcn_s_sleep(1);
    };
    auto load_wo = [&](int k, int lane, bool early) __attribute__((always_inline)) {
        const int c = k * 64 + lane, pp = c / 10, cc = c - 10 * pp;
        const unsigned f = (unsigned)(swz(pp) * 1280 + cc * 16);
        const u16* wb = wo + (size_t)gi * DH * DM + gj * 80;
#pragma unroll
        for (int a2 = 0; a2 < 6; ++a2) { if (a2 % 3 == 0) { if (early) pace(GUC); else slot(NUNIT - WOFF + (2 * k + a2 / 3) * WOC); } dma16(wb + 32 * a2 * DM, f, lb + L_WO + (5 * a2 + k) * 1024); }
    };
    auto load_wd = [&](int lane) __attribute__((always_inline)) {
        const u16* db = wd + gi * 48 * DM + gj * 80;
#pragma unroll 1
        for (int k = 0; k < 5; ++k) {
            const int c = k * 64 + lane, pp = c / 10, cc = c - 10 * pp;
            const unsigned fd = (unsigned)(swz(pp) * 1280 + cc * 16);
            if (!(k & 1)) pace(GUC);
            dma16(db, fd, lb + L_WD + k * 1024);
            if (k < 3) dma16(db + 32 * DM, (k < 2 || lane < 32) ? fd : 0u, lb + L_WD + (5 + k) * 1024);
        }
    };
    auto load_gu = [&](int m, int lane) __attribute__((always_inline)) {
#pragma unroll 1
        for (int k0 = 0; k0 < 3; ++k0) {
            const int k = (k0 + (GROT ? g & 7 : 0)) % 3, c = k * 64 + lane, row = c / 6, cc = c - 6 * row;
            const unsigned o = (unsigned)((swz(row) * FF + cc * 8) * 2), d = lb + L_GU + m * 8192 + k * 1024;
            const u16* wb = (m == 0 ? wg : wu) + gj * 80 * FF + gi * 48;
            pace(GUC);
            dma16(wb, o, d); dma16(wb + 32 * FF, o, d + 3072);
            if (k < 2) dma16(wb + 64 * FF, (k == 0 || lane < 32) ? o : 0u, d + 6144);
        }
    };
    auto lds_add = [&](int off, unsigned v) __attribute__((always_inline)) { asm volatile("s_mov_b64 exec, 1\n\tds_add_u32 %0, %1\n\ts_mov_b64 exec, -1" :: "v"(lb + L_CNT + off), "v"(v) : "memory"); };
    auto send_flag = [&](int lane) __attribute__((always_inline)) {
        st4_sc1(ws, WS_MB + (unsigned)((gi * 8 + ((gj == (gi & 7) ? lane : gi) & 7)) * 128 + gj * 4), lds_word(lb + L_CNT + 68));
    };
    auto tiles4 = [&](const Frag80 (&f)[4], const Frag80& b, unsigned long long mk) __attribute__((always_inline)) -> u32x4 {
        f32x4 c[4], d[4];
#pragma unroll
        for (int i = 0; i < 4; ++i) c[i] = mfma32(f[i].a0, b.a0, z4);
#pragma unroll
        for (int i = 0; i < 4; ++i) { c[i] = mfma32(f[i].a1, b.a1, c[i]); d[i] = mfma16(f[i].a2, b.a2, z4); }
        __builtin_amdgcn_sched_barrier(0);
        f32x2 lo, hi;
        asm volatile("s_nop 1\n\ts_mov_b64 exec, %18\n\tv_pk_add_f32 %0, %2, %10\n\tv_pk_add_f32 %1, %3, %11\n\t"
                     "s_lshl_b64 exec, exec, 1\n\tv_pk_add_f32 %0, %4, %12\n\tv_pk_add_f32 %1, %5, %13\n\t"
                     "s_lshl_b64 exec, exec, 1\n\tv_pk_add_f32 %0, %6, %14\n\tv_pk_add_f32 %1, %7, %15\n\t"
                     "s_lshl_b64 exec, exec, 1\n\tv_pk_add_f32 %0, %8, %16\n\tv_pk_add_f32 %1, %9, %17\n\ts_mov_b64 exec, -1"
                     : "=&v"(lo), "=&v"(hi) : "v"(LO2(c[0])), "v"(HI2(c[0])), "v"(LO2(c[1])), "v"(HI2(c[1])), "v"(LO2(c[2])), "v"(HI2(c[2])), "v"(LO2(c[3])), "v"(HI2(c[3])),
                       "v"(LO2(d[0])), "v"(HI2(d[0])), "v"(LO2(d[1])), "v"(HI2(d[1])), "v"(LO2(d[2])), "v"(HI2(d[2])), "v"(LO2(d[3])), "v"(HI2(d[3])), "s"(mk) : "scc");
        return asu(__builtin_shufflevector(lo, hi, 0, 1, 2, 3));
    };
    auto empty1 = [&](int half, unsigned S, int lane) __attribute__((always_inline)) {
        const unsigned o = WS_H1 + S + (unsigned)((gi * 4 + 2 * half) * 16384 + gj * 896 + min(lane, 55) * 16);
        st16_sc1(ws, o, (u32x4){~0u, ~0u, ~0u, ~0u}); st16_sc1(ws, o + 16384, (u32x4){~0u, ~0u, ~0u, ~0u});
    };
    auto report = [&]() __attribute__((always_inline)) { if (gj == (gi & 7)) st4_sc1(ws, WS_R + (unsigned)(gi * 4), lds_word(lb + L_CNT + 88)); };
    auto answer = [&](int lane, unsigned r) __attribute__((always_inline)) {
        int d = min(max((int)(r - (tst << 11)) >> 11, -400), 600) * 512 + (int)((r >> 1) & 15u);
        unsigned k = (r >> 5) & 63u;
        d += (int)dppu<0xB1>((unsigned)d); d += (int)dppu<0x4E>((unsigned)d); d += (int)dppu<0x141>((unsigned)d); d += (int)dppu<0x140>((unsigned)d);
        k = max(k, dppu<0xB1>(k)); k = max(k, dppu<0x4E>(k)); k = max(k, dppu<0x141>(k)); k = max(k, dppu<0x140>(k));
        d = __builtin_amdgcn_readlane(d, 0) + __builtin_amdgcn_readlane(d, 16);
        k = max((unsigned)__builtin_amdgcn_readlane(k, 0), (unsigned)__builtin_amdgcn_readlane(k, 16));
        st4_sc1(ws, WS_MB + (unsigned)(((lane & 31) * 8 + (lane & 7)) * 128 + 32), (tst + (d >> 14) + TOFF) << 8 | k << 2 | ((d & 511) > 128 ? 3u : 1u));
        lds_set(lb + L_CNT + 36, 1u);
    };
    auto adopt = [&](unsigned t) __attribute__((always_inline)) {
        const int dt = (int)(t - (tst << 8)) >> 8;
        if (ADOPT && dt > -256 && dt < 512) lds_set(lb + L_CNT + 64, tst + dt);
        lds_set(lb + L_CNT + 20, 4096u + ((t >> 2) & 63u) * BETA);
        lds_set(lb + L_CNT + 80, lds_word(lb + L_CNT + 92) ^ (t & 2u) << 12);
        lds_set(lb + L_CNT + 84, (((t << 1) ^ lds_word(lb + L_CNT + 68)) & 4u) << 11);
        lds_set(lb + L_CNT + 52, 1u);
    };
    struct StatAddr { unsigned a16, b16, a8, b8, dst; };
    auto stats_addr = [&](int lane, unsigned dbase, int dstride) __attribute__((always_inline)) -> StatAddr {
        const int kq = lane >> 4, q4 = (lane >> 2) & 3, p4 = lane & 3;
        const int x1 = LANES(0x4444444444444444ull) ? L_WR + gj * 160 : L_XS + q4 * 160, xa = LANES(0xaaaaaaaaaaaaaaaaull) ? L_XS + 640 : x1;
        StatAddr r{lb + (unsigned)(xa + kq * 16), lb + (unsigned)(L_XS + p4 * 160 + kq * 16), lb + (unsigned)(xa + 128 + kq * 8), lb + (unsigned)(L_XS + p4 * 160 + 128 + kq * 8), dbase + (unsigned)(kq * dstride)};
        PIN3(r.a16, r.b16, r.dst); PIN2(r.a8, r.b8);
        return r;
    };
    auto stats = [&](const StatAddr& r) __attribute__((always_inline)) {
        const bf16x8 a0 = lds16(r.a16), b0 = lds16(r.b16), a1 = lds16(r.a16 + 64), b1 = lds16(r.b16 + 64);
        const bf16x4 a2 = lds8(r.a8), b2 = lds8(r.b8);
        unsigned long long mk = 0x0008000400020001ull; PINS(mk);
        __builtin_amdgcn_sched_barrier(0);
        st16m(ws, r.dst, asu(mfma32(a1, b1, mfma32(a0, b0, z4)) + mfma16(a2, b2, z4)), mk);
    };
    unsigned qa, pw, ma, idx, qm;
    auto qk_addr = [&](bool L1) __attribute__((always_inline)) {
        LANE_VARS();
        qa = lb + L_Q + (unsigned)(kq * 32 + (lane & 2) * 8);
        pw = lb + L_P + (unsigned)((7 + 8 * wave) * 192 + (kq >> 1) * 64 + (n >> 3) * 32 + (lane & 2) * 8 + (q4 & 1) * 8 + (kq & 1) * 4 + (lane & 1) * 2);
        ma = lb + L_M + (unsigned)(wave * 4);
        idx = (lane & 2) ? 0xeeeeu : 0x4444u;
        qm = lb + L_Q;
        PIN3(qa, pw, ma); PIN2(idx, qm);
        if (L1 && wave < 4) __builtin_amdgcn_s_setprio(2);
    };
    auto att_qk0 = [&]() __attribute__((always_inline)) {
        qk_addr(false);
#pragma unroll 1
        for (unsigned spins = 0; spins < (kSpinCap << 4); ++spins) {
            if (lds_word(lb + L_CNT + 4) != 0u) break;
            __builtin_amdgcn_s_sleep(1);
        }
        STAMP(3);
        const u32x4 q0 = *(const LAS u32x4*)(size_t)qa, q1 = *(const LAS u32x4*)(size_t)(qa + 128), q2 = *(const LAS u32x4*)(size_t)(qa + 256);
        f32x4 c0 = z4, c1 = z4, c2 = z4, c3 = z4;
#define SMF(c, q, r) "v_smfmac_f32_16x16x64_bf16 %" #c ", %" #q ", v[" #r "], %7\n\t"
        asm volatile("s_nop 1\n\t" SMF(0, 4, 64:71) SMF(1, 4, 88:95) SMF(2, 4, 112:119) SMF(3, 4, 136:143) SMF(0, 5, 72:79) SMF(1, 5, 96:103) SMF(2, 5, 120:127) SMF(3, 5, 144:151)
                     SMF(0, 6, 80:87) SMF(1, 6, 104:111) SMF(2, 6, 128:135) SMF(3, 6, 152:159) "s_nop 7"
                     : "+v"(c0), "+v"(c1), "+v"(c2), "+v"(c3) : "v"(q0), "v"(q1), "v"(q2), "v"(idx));
#undef SMF
        const float s0 = c0[0] + c0[2], s1 = c1[0] + c1[2], s2 = c2[0] + c2[2], s3 = c3[0] + c3[2];
        float m = fmax2(fmax3(s0, s1, s2), s3);
        m = fmax2(m, dpp<0xB1>(m)); m = fmax2(m, dpp<0x4E>(m)); m = fmax2(m, dpp<0x124>(m)); m = fmax2(m, dpp<0x128>(m));
        const float s01 = LANES(0xffff0000ffff0000ull) ? s1 : s0, s23 = LANES(0xffff0000ffff0000ull) ? s3 : s2;
        const float sv = LANES(0xffffffff00000000ull) ? s23 : s01;
        m = bfr(bfr(m) * SCALE);
        const float p = ex2((bfr(bfr(sv) * SCALE) - m) * LOG2E);
        *(LAS u16*)(size_t)pw = (u16)pk2(p, p);
        *(LAS float*)(size_t)ma = m;
        __builtin_amdgcn_sched_barrier(0);
    };
    unsigned pr, oa, oc, cma, cxa, cra, cso;
    auto att_addr = [&]() __attribute__((always_inline)) {
        LANE_VARS();
        pr = lb + L_P + (unsigned)((7 + 8 * wave - 2 * q4 - (lane & 1)) * 192 + kq * 32 + (lane & 2) * 8);
        oa = lb + L_OP + (unsigned)(wave * 1024 + (lane & 7) * 64 + kq * 16 + (n >> 3) * 8);
        oc = lb + L_OP + (unsigned)(wave * 1024 + 512 + (kq >> 1) * 256 + (lane & 7) * 32 + (kq & 1) * 16 + (n >> 3) * 8);
        PIN3(pr, oa, oc);
    };
    auto comb_out = [&](float u0, float u1, unsigned so, unsigned long long mk, bool both) __attribute__((always_inline)) {
        const unsigned w = min(pk2(u0, u1), 0xfffffffeu);
        if (CMODE && !both) { st4_sc1(ws, so, w); return; }
        const u32x4 w4 = {both ? dppu<0x00>(w) : w, dppu<0x55>(w), dppu<0xAA>(w), dppu<0xFF>(w)};
        st16m(ws, so, w4, mk);
    };
    auto max8 = [&](f32x4 m0, f32x4 m1) __attribute__((always_inline)) { return fmax3(fmax3(m0[0], m0[1], m0[2]), fmax3(m0[3], m1[0], m1[1]), fmax2(m1[2], m1[3])) * LOG2E; };
    auto comb_o0 = [&](f32x4 m0, f32x4 m1, f32x4 mm, f32x4 o0, f32x4 o1, f32x4 o2, f32x4 o3, unsigned so, unsigned long long mk, bool both) __attribute__((always_inline)) {
        const float M = max8(m0, m1);
        f32x4 f;
#pragma unroll
        for (int k = 0; k < 4; ++k) f[k] = ex2(__builtin_fmaf(mm[k], LOG2E, -M));
        const f32x4 o = f[3] * o3 + (f[2] * o2 + (f[1] * o1 + f[0] * o0));
        comb_out(rs32(o[0], o[2]), rs32(o[1], o[3]), so, mk, both);
    };
    auto comb_o1 = [&](f32x4 m0, f32x4 m1, f32x2 mm, f32x4 o0, f32x4 o1, unsigned so, unsigned long long mk, bool both) __attribute__((always_inline)) {
        const float M = max8(m0, m1);
        const f32x4 o = ex2(__builtin_fmaf(mm[1], LOG2E, -M)) * o1 + ex2(__builtin_fmaf(mm[0], LOG2E, -M)) * o0;
        comb_out(x16_add(rs32(o[0], o[2])), x16_add(rs32(o[1], o[3])), so, mk, both);
    };
    auto ml_addr = [&](int lane, unsigned& xa, unsigned& ra) __attribute__((always_inline)) {
        xa = lb + L_M + (unsigned)(((lane & 15) >> 1) * 4); ra = lb + L_P + (unsigned)((7 + 8 * ((lane & 15) >> 1)) * 192 + (lane & 1) * 64 + (lane >> 4) * 16);
    };
    auto comb_ml = [&](unsigned ma, unsigned xa, unsigned ra, unsigned so, bool both) __attribute__((always_inline)) {
        const u32x4 x4 = *(const LAS u32x4*)(size_t)ra;
        const f32x4 m0 = *(const LAS f32x4*)(size_t)ma, m1 = *(const LAS f32x4*)(size_t)(ma + 16);
        const float mw = *(const LAS float*)(size_t)xa;
        __builtin_amdgcn_sched_barrier(0);
        const f32x4 sp = mfma32u((u32x4){0x3f803f80u, 0x3f803f80u, 0x3f803f80u, 0x3f803f80u}, x4, z4);
        __builtin_amdgcn_sched_barrier(0);
        const float M = max8(m0, m1);
        float L = row16_sum(sp[0] * ex2(__builtin_fmaf(mw, LOG2E, -M)));
        if (both) L = dpp<0x00>(L);
        st16m(ws, so, asu((f32x4){M, L, M, L}), both ? 3ull : 1ull);
    };
    auto att_pv0 = [&]() __attribute__((always_inline)) {
        u32x4 bm[8];
        trans_v(IC<0>{}, IC<24>{}); trans_v(IC<1>{}, IC<16>{});
#pragma unroll
        for (int e = 0; e < 8; ++e) bm[e] = *(const LAS u32x4*)(size_t)(pr + e * 192);
        __builtin_amdgcn_sched_barrier(0);
        f32x4 da = z4, dc = z4;
        PIN2(da, dc);
        asm volatile("s_nop 1");
        __builtin_amdgcn_sched_barrier(0);
        f32x2 r;
        smf<RV>(da, bm[0], idx); smf<RV + 8>(da, bm[1], idx); smf<RV + 16>(da, bm[2], idx); smf<RV + 24>(da, bm[3], idx);
        smf<RV + 32>(da, bm[4], idx); smf<RV + 40>(da, bm[5], idx); smf<RV + 48>(da, bm[6], idx); smf<RV + 56>(da, bm[7], idx);
        asm volatile("s_nop 7\n\tv_pk_add_f32 %0, %1, %2\n\tds_write_b64 %3, %0\n\ts_mov_b64 exec, 1\n\tds_add_u32 %4, %5\n\ts_mov_b64 exec, -1"
                     : "=&v"(r) : "v"(LO2(da)), "v"(HI2(da)), "v"(oa), "v"(lb + L_CNT + 24), "v"(1u) : "memory");
        GUARD(900);
        MARK(b_t5);
        trans_w(IC<0>{}, IC<12>{}); trans_w(IC<1>{}, IC<8>{}); trans_w(IC<2>{}, IC<4>{}); trans_w(IC<3>{}, IC<0>{});
        STAMP(4);
        asm volatile("s_nop 1\n\tv_smfmac_f32_16x16x64_bf16 %0, %1, v[224:231], %5\n\tv_smfmac_f32_16x16x64_bf16 %0, %2, v[232:239], %5\n\t"
                     "v_smfmac_f32_16x16x64_bf16 %0, %3, v[240:247], %5\n\tv_smfmac_f32_16x16x64_bf16 %0, %4, v[248:255], %5\n\ts_nop 6"
                     : "+v"(dc) : "v"(bm[0]), "v"(bm[1]), "v"(bm[2]), "v"(bm[3]), "v"(idx));
        asm volatile("s_nop 0\n\tv_pk_add_f32 %0, %1, %2\n\tds_write_b64 %3, %0" : "=&v"(r) : "v"(LO2(dc)), "v"(HI2(dc)), "v"(oc) : "memory");
        STAMP(20);
    };
    unsigned long long ck1 = 0xffff0000ffff0000ull, ck2 = 0xffffffff00000000ull, csc = 0x3d93cd3a3d93cd3aull;
    auto att1 = [&]() __attribute__((always_inline)) {
        constexpr int layer = 1;
        {
#define SMF(c, q, r) "v_smfmac_f32_16x16x64_bf16 v[" #c "], v[" #q "], v[" #r "], %0\n\t"
#define MAXD(x) "v_max_f32_dpp v41, v41, v41 " x " row_mask:0xf bank_mask:0xf\n\t"
            asm volatile("s_waitcnt lgkmcnt(0)\n\ts_barrier\n\t"
                         "ds_read_b128 v[0:3], %1\n\tds_read2_b32 v[12:13], %2 offset0:96 offset1:100\n\tds_read_b128 v[4:7], %1 offset:128\n\tds_read_b128 v[8:11], %1 offset:256\n\t"
                         "v_mov_b64 v[16:17], 0\n\tv_mov_b64 v[18:19], 0\n\tv_mov_b64 v[20:21], 0\n\tv_mov_b64 v[22:23], 0\n\tv_mov_b64 v[24:25], 0\n\tv_mov_b64 v[26:27], 0\n\t"
                         "v_mov_b64 v[28:29], 0\n\tv_mov_b64 v[30:31], 0\n\tv_mov_b64 v[32:33], 0\n\tv_mov_b64 v[34:35], 0\n\tv_mov_b64 v[36:37], 0\n\tv_mov_b64 v[38:39], 0\n\t"
                         "s_waitcnt lgkmcnt(3)\n\t" SMF(16:19, 0:3, 64:71) SMF(20:23, 0:3, 88:95) "s_waitcnt lgkmcnt(1)\n\tv_mul_f32 v13, 0x3acccccd, v13\n\t"
                         SMF(24:27, 0:3, 112:119) "v_mul_f32 v12, 0x3acccccd, v12\n\t" SMF(28:31, 0:3, 136:143) "v_fma_f32 v12, -v13, v13, v12\n\t"
                         SMF(16:19, 4:7, 72:79) "v_maximum3_f32 v12, v12, 0, 0\n\t" SMF(20:23, 4:7, 96:103) "v_cvt_pk_bf16_f32 v12, 0, v12\n\t"
                         SMF(24:27, 4:7, 120:127) "v_add_f32 v12, 0x3727c5ac, v12\n\t" SMF(28:31, 4:7, 144:151) "v_cvt_pk_bf16_f32 v12, 0, v12\n\ts_waitcnt lgkmcnt(0)\n\t"
                         SMF(16:19, 8:11, 80:87) "v_sqrt_f32 v12, v12\n\t" SMF(20:23, 8:11, 104:111) "v_cvt_pk_bf16_f32 v12, 0, v12\n\t"
                         SMF(24:27, 8:11, 128:135) "v_rcp_f32 v12, v12\n\t" SMF(28:31, 8:11, 152:159)
                         "s_nop 2\n\tv_add_f32 v42, v16, v18\n\ts_nop 0\n\tv_add_f32 v43, v20, v22\n\tv_max_f32 v41, v42, v43\n\tv_add_f32 v44, v24, v26\n\tv_add_f32 v45, v28, v30\n\t"
                         "v_max3_f32 v41, v41, v44, v45\n\tv_cndmask_b32 v42, v42, v43, %8\n\tv_cndmask_b32 v44, v44, v45, %8\n\t"
                         MAXD("quad_perm:[1,0,3,2]") "v_cndmask_b32 v40, v42, v44, %9\n\ts_nop 0\n\t" MAXD("quad_perm:[2,3,0,1]") "s_nop 1\n\t" MAXD("row_ror:4") "s_nop 1\n\t" MAXD("row_ror:8")
                         "v_pk_mul_f32 v[40:41], v[12:13], v[40:41] op_sel_hi:[0,1]\n\tv_cvt_pk_bf16_f32 v40, 0, v40\n\tv_cvt_pk_bf16_f32 v41, 0, v41\n\t"
                         "v_pk_mul_f32 v[40:41], %10, v[40:41]\n\tv_cvt_pk_bf16_f32 v40, 0, v40\n\tv_cvt_pk_bf16_f32 v41, 0, v41\n\t"
                         "v_sub_f32 v40, v40, v41\n\tv_mul_f32 v40, 0x3fb8aa3b, v40\n\tv_exp_f32 v40, v40\n\ts_nop 0\n\tv_cvt_pk_bf16_f32 v40, v40, v40\n\tds_write_b16 %3, v40\n\t"
                         "ds_read_b128 v[0:3], %4\n\tds_read_b128 v[4:7], %4 offset:192\n\tds_read_b128 v[8:11], %4 offset:384\n\tds_read_b128 v[12:15], %4 offset:576\n\t"
                         "ds_read_b128 v[16:19], %4 offset:768\n\tds_read_b128 v[20:23], %4 offset:960\n\tds_read_b128 v[24:27], %4 offset:1152\n\tds_read_b128 v[28:31], %4 offset:1344\n\t"
                         "ds_write_b32 %5, v41\n\t"
                         "s_waitcnt lgkmcnt(8)\n\t" SMF(32:35, 0:3, 160:167) SMF(36:39, 0:3, 224:231) "s_waitcnt lgkmcnt(7)\n\t" SMF(32:35, 4:7, 168:175) SMF(36:39, 4:7, 232:239)
                         "s_waitcnt lgkmcnt(6)\n\t" SMF(32:35, 8:11, 176:183) SMF(36:39, 8:11, 240:247) "s_waitcnt lgkmcnt(4)\n\t" SMF(32:35, 12:15, 184:191) SMF(36:39, 12:15, 248:255)
                         SMF(32:35, 16:19, 192:199) "s_waitcnt lgkmcnt(1)\n\t" SMF(32:35, 20:23, 200:207) SMF(32:35, 24:27, 208:215) SMF(32:35, 28:31, 216:223)
                         "s_nop 2\n\tv_pk_add_f32 v[36:37], v[36:37], v[38:39]\n\tds_write_b64 %7, v[36:37]\n\ts_nop 2\n\tv_pk_add_f32 v[32:33], v[32:33], v[34:35]\n\tds_write_b64 %6, v[32:33]"
                         :: "v"(idx), "v"(qa), "v"(qm), "v"(pw), "v"(pr), "v"(ma), "v"(oa), "v"(oc), "s"(ck1), "s"(ck2), "s"(csc)
                         : "memory", "v0", "v1", "v2", "v3", "v4", "v5", "v6", "v7", "v8", "v9", "v10", "v11", "v12", "v13", "v14", "v15", "v16", "v17", "v18", "v19", "v20", "v21", "v22", "v23",
                           "v24", "v25", "v26", "v27", "v28", "v29", "v30", "v31", "v32", "v33", "v34", "v35", "v36", "v37", "v38", "v39", "v40", "v41", "v42", "v43", "v44", "v45");
#undef SMF
#undef MAXD
            STAMP(20 + 32 * layer);
            BAR();
            if (__builtin_expect(wave == 0, 1)) {
                STAMP(5 + 32 * layer);
                comb_o0(*(const LAS f32x4*)(size_t)cma, *(const LAS f32x4*)(size_t)(cma + 16), *(const LAS f32x4*)(size_t)cxa, *(const LAS f32x4*)(size_t)cra,
                        *(const LAS f32x4*)(size_t)(cra + 1024), *(const LAS f32x4*)(size_t)(cra + 2048), *(const LAS f32x4*)(size_t)(cra + 3072), cso, 0x1111111111111111ull, false);
            }
            __builtin_amdgcn_sched_barrier(0);
            if (__builtin_expect(wave == 1, 1))
                comb_o1(*(const LAS f32x4*)(size_t)cma, *(const LAS f32x4*)(size_t)(cma + 16), *(const LAS f32x2*)(size_t)cxa, *(const LAS f32x4*)(size_t)cra, *(const LAS f32x4*)(size_t)(cra + 1024), cso, 0x0000111100001111ull, false);
            __builtin_amdgcn_sched_barrier(0);
            if (__builtin_expect(wave == 2, 1)) comb_ml(cma, cxa, cra, cso, false);
            MARK(e_q);
            if (wave < 3) STAMP(21 + 32 * layer);
            __builtin_amdgcn_s_setprio(0);
        }
    };

    unsigned nj = 0u, rS = 0u, nS = 0u; int ord;
    {
        const int tid = threadIdx.x, lane = tid & 63;
        asm volatile("ds_max_u32 %0, %1" :: "v"(lb + L_CNT + 64), "v"(tst) : "memory");
        unsigned mb = 0u;
        const u16* kbase = kc + wofs / 2;
        const u16* vbase = vc + wofs / 2;
        unsigned pt0 = 0u;
        if (wave == 7) {
            __builtin_amdgcn_s_setprio(3);
            if (lane < 10) *reinterpret_cast<u32x4*>(smem + L_XS + 640 + lane * 16) = (u32x4){0x3f803f80u, 0x3f803f80u, 0x3f803f80u, 0x3f803f80u};
            if (lane < 16) { *reinterpret_cast<u32x4*>(smem + L_CS + 768 + lane * 16) = (u32x4){0u, 0u, 0u, 0u}; }
        } else {
            if (wave >= 4) {
                if (wave == 4) st16_sc1(ws, WS_OWN + (unsigned)(gi * 16384 + (lane >> 5) * 8192 + gj * 512 + (lane & 31) * 16), (u32x4){~0u, ~0u, ~0u, ~0u});
                else empty5(0, (unsigned)(wave - 5) * 8192u, lane);
                if (wave == 6) {
                    __builtin_amdgcn_s_setprio(3);
                    if (gi + gj == 0) st4_sc1(ws, WS_R + (lane & 31) * 4, 0u);
                    st4_sc1(ws, WS_MB + (unsigned)((gi * 8 + gj) * 128 + (lane & 31) * 4), 0u);
                }
            } else if (wave >= 2) {
                unsigned long long c0, c1;
                asm volatile("s_waitcnt lgkmcnt(0)\n\ts_memtime %0\n\ts_waitcnt lgkmcnt(0)\n\ts_load_dword %2, %3, %4\n\ts_waitcnt lgkmcnt(0)\n\ts_memtime %1\n\ts_waitcnt lgkmcnt(0)"
                             : "=&s"(c0), "=&s"(c1), "=&s"(pt0) : "s"(ws), "s"((6u << 20) + ((((tst + g * 977u) * 2654435761u) >> 11 & 0x1fdf80u) | ((wave ^ gj) & 1) * 8192)) : "memory");
                pt0 = ((wave ^ gj) & 1) ? (unsigned)c1 - (unsigned)c0 : (unsigned)c0 - (unsigned)c1;
            }
            STAMP(36);
            *reinterpret_cast<u32x4*>(smem + L_P + tid * 16) = (u32x4){0u, 0u, 0u, 0u};
            if (tid < 72 * 12 - 448) *reinterpret_cast<u32x4*>(smem + L_P + 7168 + tid * 16) = (u32x4){0u, 0u, 0u, 0u};
            if (wave >= 4) {
                if (wave == 6) VMW(2); else VMW(1);
                asm volatile("ds_max_u32 %0, %1" :: "v"(lb + L_CNT + 64), "v"(now() - TLAND) : "memory");
                STAMP(40);
                lds_add(0, 1u);
            } else if (wave >= 2) { lds_add(20, pt0); VMW(0); lds_add(0, 1u); }
        }
        asm volatile("s_waitcnt lgkmcnt(0)" :: "s"(pt1) : "memory");
        const int rot = ROT;
        const unsigned r0 = now(), c0 = (unsigned)__builtin_amdgcn_s_memtime();
        u32x4 a[4];
        unsigned ko = (unsigned)((wave * 64 + (lane & 7)) * 384 + (((lane >> 3) & 1) * 4 + (lane >> 4)) * 16);
        PIN1(ko);
        auto k_group = [&](auto GC_) __attribute__((always_inline)) {
            constexpr int G = decltype(GC_)::value, R = RK + 24 * (G >> 1) + 4 * (G & 1);
            const unsigned o = ko + (unsigned)((((G >> 1) + rot) & 3) * 6144 + (G & 1) * 3072);
            pace(3);
            ld16_nt<R, 0>(kbase, o); ld16_nt<R + 8, 128>(kbase, o); ld16_nt<R + 16, 256>(kbase, o);
            const int lane = lane_id();
            const unsigned ho = WS_Q0 + (unsigned)((gi * 4 + (gj >> 1)) * 4096 + lane * 16);
            if (wave < 7) {
                asm volatile("; COND_BEGIN");
                if (G == 0 && wave >= 4) { VMW(3); lds_add(32, 1u); }
                if (G == 0) {
                    if (wave < 2) { VMW(3); lds_add(0, 1u); load_gu(wave, lane); }
                    if (wave == WDW) load_wd(lane);
                    if (wave == WOW) {
#pragma unroll 1
                        for (int k = 5 - WEARLY; k < 5; ++k) load_wo(k, lane, true);
                    }
                }
                asm volatile("; COND_END");
            }
            if (wave == 7) {
                asm volatile("; COND_BEGIN");
                if (G == GP) {
                    VMW(3 * (GP + 1));
#pragma unroll 1
                    for (unsigned spins = 0; spins < (kSpinCap << 4); ++spins) {
                        if (lds_word(lb + L_CNT) >= 7u) break;
                        __builtin_amdgcn_s_sleep(1);
                    }
                    STAMP(26);
                    const unsigned char* p = smem + L_X0 + (lane >> 4) * 1280;
                    const float c0 = lo16(*reinterpret_cast<const u16*>(p));
                    const f32x2 c2 = {c0, c0};
                    f32x2 s2 = {0.f, 0.f}, q2 = s2;
#pragma unroll
                    for (int k = 0; k < 5; ++k) {
                        const u32x4 w = *reinterpret_cast<const u32x4*>(p + k * 256 + (lane & 15) * 16);
#pragma unroll
                        for (int e = 0; e < 4; ++e) { const f32x2 d = unpk(w[e]) - c2; s2 += d; q2 += d * d; }
                    }
                    const float s = row16_sum(s2[0] + s2[1]), qq = row16_sum(q2[0] + q2[1]);
                    const float ms = s * (1.f / DM);
                    const unsigned mv = pk2(c0 + ms, fmax2(qq - s * ms, 0.f) * (1.f / DM));
                    const float mb = lo16(mv), rden = frcp(bfr(__builtin_amdgcn_sqrtf(bfr(hi16(mv) + 1e-5f))));
                    const f32x2 mb2 = {mb, mb}, rd2 = {rden, rden};
                    if ((lane & 15) < 10) {
                        u32x4 w = *reinterpret_cast<const u32x4*>(p + gj * 160 + (lane & 15) * 16);
#pragma unroll
                        for (int e = 0; e < 4; ++e) w[e] = pk2v(unpk(pk2v(unpk(w[e]) - mb2)) * rd2);
                        *reinterpret_cast<u32x4*>(smem + L_XS + (lane >> 4) * 160 + (lane & 15) * 16) = w;
                    }
                    STAMP(30);
                }
                if (G == GP || G == GA) {
#pragma unroll 1
                    for (int blk = 0; blk < 3; ++blk) {
                        LANE_VARS();
                        const unsigned xb = lb + L_XS + (unsigned)((G == GP ? q4 : 4) * 160);
                        Frag80 f[4];
#pragma unroll
                        for (int i = 0; i < 4; ++i) f[i] = frag80(lb + L_WQ + (unsigned)(128 * blk + 32 * i + 8 * p4), 416, r0s, r1s, r2s);
                        const Frag80 b = rows80(xb + kq * 16, xb + 128 + kq * 8);
                        const u32x4 c = tiles4(f, b, 0x1111111111111111ull);
                        if (G == GP) *reinterpret_cast<u32x4*>(smem + L_OP + q4 * 768 + 256 * blk + p4 * 64 + kq * 16) = c;
                        else *reinterpret_cast<u32x4*>(smem + L_CS + 256 * blk + kq * 64 + p4 * 16) = c;
                    }
                    if (G == GP) {
                        asm volatile("" ::: "memory");
                        const int ln = lane_id();
                        f32x2 c[6];
                        unsigned m = 20u;
#pragma unroll
                        for (int k = 0; k < 6; ++k) {
                            c[k] = *reinterpret_cast<const f32x2*>(smem + L_OP + (k / 3) * 1536 + ln * 24 + (k % 3) * 8);
                            m = max(m, max(fexp(c[k][0]), fexp(c[k][1])));
                        }
                        m = max(m, dppu<0xB1>(m)); m = max(m, dppu<0x4E>(m)); m = max(m, dppu<0x141>(m)); m = max(m, dppu<0x140>(m));
                        m = max(max((unsigned)__builtin_amdgcn_readlane(m, 0), (unsigned)__builtin_amdgcn_readlane(m, 16)), max((unsigned)__builtin_amdgcn_readlane(m, 32), (unsigned)__builtin_amdgcn_readlane(m, 48)));
                        lds_set(lb + L_CNT + 76, m);
                        const float f = __uint_as_float((273u - m) << 23);
#pragma unroll
                        for (int k = 0; k < 2; ++k) {
                            const u32x2 w0 = enc21(c[3 * k][0] * f, c[3 * k][1] * f, c[3 * k + 1][0] * f), w1 = enc21(c[3 * k + 1][1] * f, c[3 * k + 2][0] * f, c[3 * k + 2][1] * f);
                            st16_sc1(ws, WS_Q0 + (unsigned)((gi * 4 + 2 * k + (ln >> 5)) * 4096 + gj * 512 + (ln & 31) * 16), (u32x4){w0[0], w0[1], w1[0], w1[1]});
                        }
                        STAMP(2);
                        WAIT1(pf, 3 * (GP + 1) + 2);
                    }
                }
                if (G == GA) {
                    const unsigned half = __builtin_amdgcn_s_getreg(2 << 6 | 20);
                    lds_set(lb + L_CNT + 68, lds_word(lb + L_CNT + 64) << 11 | lds_word(lb + L_CNT + 76) << 3 | 1u | (((int)lds_word(lb + L_CNT + 20) > 0) ^ half) << 1 | half << 2);
                    VMW(3 * (GA - GP));
#pragma unroll 1
                    for (unsigned spins = 0; spins < (kSpinCap << 4); ++spins) if (lds_word(lb + L_CNT + 32) >= 3u) break;
                    send_flag(lane_id());
                    lds_set(lb + L_CNT + 8, 1u);
                    if (XS) pace(XS, false);
                    STAMP(17);
                    f32x2 r2 = {0.f, 0.f};
#pragma unroll
                    for (int k = 0; k < 5; ++k) r2 += unpk(*reinterpret_cast<const unsigned*>(smem + L_WR + k * 256 + lane_id() * 4));
                    *reinterpret_cast<float*>(smem + L_CNT + 12) = wave_sum(r2[0] + r2[1]);
                }
                asm volatile("; COND_END");
            }
            if (wave == 6) {
                asm volatile("; COND_BEGIN");
                if (G == GB - 1) mb = ld4_sc1(ws, WS_MB + (unsigned)((gi * 8 + gj) * 128 + (gi & 7) * 4));
                if (G == GB) {
                    WAIT1(mb, 3);
#pragma unroll 1
                    for (unsigned spins = 0; spins < kSpinCap && !all_set(mb); ++spins) {
                        if ((spins & 7u) == 7u && lds_word(lb + L_CNT + 8)) send_flag(lane);
                        mb = ld4_sc1(ws, WS_MB + (unsigned)((gi * 8 + gj) * 128 + (gi & 7) * 4));
                        WAIT1(mb, 0);
                    }
                    mb = ld4_sc1(ws, WS_MB + (unsigned)((gi * 8 + (gi & 7)) * 128 + (lane & 7) * 4));
                }
                if (G == GC) {
                    WAIT1(mb, 3 * (GC - GB));
#pragma unroll 1
                    for (unsigned spins = 0; spins < kSpinCap && !all_set(mb); ++spins) {
                        if ((spins & 7u) == 7u && lds_word(lb + L_CNT + 8)) send_flag(lane);
                        mb = ld4_sc1(ws, WS_MB + (unsigned)((gi * 8 + (gi & 7)) * 128 + (lane & 7) * 4));
                        WAIT1(mb, 0);
                    }
                    lds_set(lb + L_CNT + 92, (__builtin_amdgcn_readlane(mb, gi & 7) & 4u) << 11);
                    int d = min(max((int)(mb - (tst << 11)) >> 11, -400), 600);
                    d += (int)dppu<0xB1>((unsigned)d); d += (int)dppu<0x4E>((unsigned)d); d += (int)dppu<0x141>((unsigned)d);
                    const unsigned tm = tst + (unsigned)(__builtin_amdgcn_readfirstlane(d) >> 3);
                    if (ROWM) lds_set(lb + L_CNT + 64, tm);
                    const float slow = (float)(now() - r0) * 24.f * 256.f * frcp((float)((unsigned)__builtin_amdgcn_s_memtime() - c0)) - 256.f;
                    lds_set(lb + L_CNT + 88, tm << 11 | (unsigned)__builtin_amdgcn_fmed3f(slow, 0.f, 63.f) << 5 | __builtin_popcount((unsigned)__builtin_amdgcn_ballot_w64((mb & 2u) != 0u) & 255u) << 1 | 1u);
                    report();
                    a[0] = ld16_sc1<0>(ws, ho); a[1] = ld16_sc1<1024>(ws, ho); a[2] = ld16_sc1<2048>(ws, ho); a[3] = ld16_sc1<3072>(ws, ho);
                    pace(XCOST, false);
                    STAMP(31);
                }
                asm volatile("; COND_END");
            } else if (G == GC) { for (int k = 0; k < 4; ++k) asm volatile("" : "=v"(a[k])); }
        };
        k_group(IC<0>{}); k_group(IC<1>{}); k_group(IC<2>{}); k_group(IC<3>{}); k_group(IC<4>{}); k_group(IC<5>{}); k_group(IC<6>{}); k_group(IC<7>{});
        {
            const int lane = lane_id();
            const unsigned v0 = (unsigned)((wave * 64 + 4 * (lane >> 4)) * 384 + (lane & 15) * 16);
            const unsigned v1 = v0 + 256 - (unsigned)((lane & 15) * 8);
            auto v_group = [&](auto HC) __attribute__((always_inline)) {
                constexpr int h = decltype(HC)::value, R = h < 8 ? RV + 16 * (h & 3) + 4 * (h >> 2) : RV + 64 + 2 * (h - 8), NV = h < 8 ? 2 * h + 2 : 4 * h - 12;
                const unsigned o = (h < 8 ? v0 : v1) + (unsigned)((((h < 8 ? h >> 1 : h) + rot) & 3) * 6144);
                pace(2);
                if (h < 8) { ld16_nt<R, (h & 1) * 768>(vbase, o); ld16_nt<R + 8, (h & 1) * 768 + 384>(vbase, o); }
                else { ld8_nt<R, 0>(vbase, o); ld8_nt<R + 8, 384>(vbase, o); ld8_nt<R + 16, 768>(vbase, o); ld8_nt<R + 24, 1152>(vbase, o); }
                if (wave == 7 && gi + gj == 0) {
                    asm volatile("; COND_BEGIN");
                    if (h == 3 || h == 6) mb = ld4_sc1(ws, WS_R + (unsigned)((lane & 31) * 4));
                    if (h == 4 || h == 7) {
                        WAIT1(mb, 2);
                        if (all_set(mb) && !lds_word(lb + L_CNT + 36)) answer(lane, mb);
                    }
                    asm volatile("; COND_END");
                }
                if (wave == 6) {
                    asm volatile("; COND_BEGIN");
                if (h == HD) {
                    asm volatile("s_waitcnt vmcnt(%4)" : "+v"(a[0]), "+v"(a[1]), "+v"(a[2]), "+v"(a[3]) : "i"(3 * (7 - GC) + NV) : "memory");
                    float sq[6] = {0.f, 0.f, 0.f, 0.f, 0.f, 0.f};
#pragma unroll
                    for (int k = 0; k < 4; ++k) {
                        const float f = __uint_as_float(((((unsigned)__builtin_amdgcn_ds_bpermute((2 * k + (lane >> 5)) * 4, (int)mb) >> 3) & 255u) - 19u) << 23);
#pragma unroll
                        for (int e = 0; e < 2; ++e) {
                            const unsigned lo = a[k][2 * e], hi = a[k][2 * e + 1];
                            const float fe = (int)hi < 0 ? __builtin_nanf("") : f;
                            sq[3 * e] += (float)((int)(lo << 11) >> 11) * fe;
                            sq[3 * e + 1] += (float)((int)(__builtin_amdgcn_alignbit(hi, lo, 21) << 11) >> 11) * fe;
                            sq[3 * e + 2] += (float)((int)(hi << 1) >> 11) * fe;
                        }
                    }
#pragma unroll
                    for (int e = 0; e < 3; ++e) {
                        const int pi = 3 * (lane & 31) + e;
                        *reinterpret_cast<unsigned*>(smem + L_Q + (pi >> 3) * 32 + (pi & 6) * 2 + (pi & 1) * 16) = pk2(x32_add(sq[2 * e]), x32_add(sq[2 * e + 1]));
                    }
                    asm volatile("s_waitcnt lgkmcnt(0)\n\tds_write_b32 %0, %1" :: "v"(lb + L_CNT + 4), "v"(1u) : "memory");
                    STAMP(27);
                    lead = LEAD * PACE * 4;
                }
                    if (h == 6) mb = ld4_sc1(ws, WS_MB + (unsigned)((gi * 8 + (gi & 7)) * 128 + 32));
                    if (h == 8) {
                        WAIT1(mb, 6);
                        const unsigned r = __builtin_amdgcn_readfirstlane(mb);
                        if (r) { adopt(r); STAMP(29); }
                    }
                    asm volatile("; COND_END");
                }
                if constexpr (h < 6) {
                    constexpr int i = 2 * h;
                    fix_k1(IC<i>{}, IC<20 - 6 * (i / 3) - i % 3 + NV>{});
                    fix_k1(IC<i + 1>{}, IC<20 - 6 * ((i + 1) / 3) - (i + 1) % 3 + NV>{});
                }
                if (h == 5) {
                    STAMP(1);
                    __builtin_amdgcn_s_setprio(0);
                    att_qk0();
                }
            };
            v_group(IC<0>{}); v_group(IC<1>{}); v_group(IC<2>{}); v_group(IC<3>{}); v_group(IC<4>{}); v_group(IC<5>{});
            v_group(IC<6>{}); v_group(IC<7>{}); v_group(IC<8>{}); v_group(IC<9>{}); v_group(IC<10>{}); v_group(IC<11>{});
            lds_add(16, 1u);
            if (wave == WOW) lds_add(44, WEARLY);
        }
    }
    {
        STAMP(28);
        att_addr();
        const int lane = lane_id();
        cma = lb + L_M; cxa = lb + L_M + (unsigned)((lane >> 4) * 8); cra = lb + L_OP + (unsigned)(512 + (lane >> 4) * 2048 + (lane & 15) * 16);
        cso = WS_OWN + (unsigned)(gi * 16384 + gj * 512 + 256 + (lane >> 5) * 64 + (lane & 12) * 4 + (lane & 1) * 8192);
        PIN2(cma, cxa); PIN2(cra, cso);
        att_pv0();
    }
    {
        f32x4 m0, m1, o0, o1; f32x2 mm; unsigned r, taken;
        asm volatile("s_mov_b64 exec, 1\n\tds_add_rtn_u32 %5, %7, %8 offset:%11\n\ts_mov_b64 exec, -1\n\t"
                     "ds_read_b128 %0, %7\n\tds_read_b128 %1, %7 offset:16\n\tds_read_b64 %2, %9\n\tds_read_b128 %3, %10\n\tds_read_b128 %4, %10 offset:1024\n\t"
                     "ds_read_b32 %6, %7 offset:%12\n\ts_waitcnt lgkmcnt(0)"
                     : "=&v"(m0), "=&v"(m1), "=&v"(mm), "=&v"(o0), "=&v"(o1), "=&v"(r), "=&v"(taken)
                     : "v"(cma), "v"(1u), "v"(cxa), "v"(cra), "i"(L_CNT + 28 - L_M), "i"(L_CNT + 40 - L_M) : "memory");
        ord = __builtin_amdgcn_readfirstlane(r);
        if (ord == 7) { STAMP(5); comb_o1(m0, m1, mm, o0, o1, cso, 0x0000333300003333ull, true); lds_set(lb + L_CNT + 48, now() | 1u); STAMP(21); }
        MARK(e_t5);
        if (ord <= OML + 1 || (unsigned)__builtin_amdgcn_readfirstlane(taken) < 5u - WEARLY) {
        const int lane = lane_id();
        auto wait_cnt = [&](int off, unsigned v) __attribute__((always_inline)) {
#pragma unroll 1
            for (unsigned spins = 0; spins < (kSpinCap << 4); ++spins) { if (lds_word(lb + L_CNT + off) >= v) break; __builtin_amdgcn_s_sleep(1); }
        };
        if (ord == OML) {
            wait_cnt(16, 8u);
            unsigned xa, ra; ml_addr(lane, xa, ra);
            comb_ml(cma, xa, ra, WS_OWN + (unsigned)(gi * 16384 + gj * 512 + 384 + (lane & 1) * 8192), true);
            STAMP(24);
        } else if (ord == 0 && !lds_word(lb + L_CNT + 52)) {
            if (gi + gj == 0 && !lds_word(lb + L_CNT + 36)) {
                unsigned r = 0u;
#pragma unroll 1
                for (unsigned spins = 0; spins < kSpinCap; ++spins) {
                    r = ld4_sc1(ws, WS_R + (unsigned)((lane & 31) * 4));
                    WAIT1(r, 0);
                    if (all_set(r)) break;
                    if ((spins & 7u) == 7u) send_flag(lane);
                }
                answer(lane, r);
            }
#pragma unroll 1
            for (unsigned spins = 0; spins < kSpinCap; ++spins) {
                unsigned r = ld4_sc1(ws, WS_MB + (unsigned)((gi * 8 + (gi & 7)) * 128 + 32));
                WAIT1(r, 0);
                r = __builtin_amdgcn_readfirstlane(r);
                if (r) { adopt(r); break; }
                if ((spins & 7u) == 7u) { send_flag(lane); report(); }
                __builtin_amdgcn_s_sleep(16);
            }
            STAMP(29);
        } else if (ord == OML + 1) {
#pragma unroll 1
            for (unsigned spins = 0; spins < (kSpinCap << 6); ++spins) if (lds_word(lb + L_CNT + 24) >= 8u) break;
            const unsigned ra = lb + L_OP + (unsigned)((lane >> 5) * 4096 + (lane & 31) * 16);
            comb_o0(*(const LAS f32x4*)(size_t)cma, *(const LAS f32x4*)(size_t)(cma + 16), *(const LAS f32x4*)(size_t)(cma + (lane >> 5) * 16), *(const LAS f32x4*)(size_t)ra, *(const LAS f32x4*)(size_t)(ra + 1024),
                    *(const LAS f32x4*)(size_t)(ra + 2048), *(const LAS f32x4*)(size_t)(ra + 3072), WS_OWN + (unsigned)(gi * 16384 + gj * 512 + (lane & 60) * 4 + (lane & 1) * 8192), 0x3333333333333333ull, true);
            STAMP(22);
        }
        if (ord == EMODE) {
#pragma unroll 1
            for (unsigned spins = 0; spins < (kSpinCap << 8); ++spins) { if (lds_word(lb + L_CNT + 52)) break; __builtin_amdgcn_s_sleep(1); }
            const unsigned S = lds_word(lb + L_CNT + 80);
            empty4(0, S, lane); empty5(5, 0u, lane); empty5(5, 8192u, lane);
        }
#pragma unroll 1
        for (;;) {
            unsigned j;
            asm volatile("s_mov_b64 exec, 1\n\tds_add_rtn_u32 %0, %1, %2\n\ts_mov_b64 exec, -1\n\ts_waitcnt lgkmcnt(0)" : "=&v"(j) : "v"(lb + L_CNT + 40), "v"(1u) : "memory");
            j = __builtin_amdgcn_readfirstlane(j);
            if (j >= 5u - WEARLY) break;
            load_wo((int)j, lane, false); ++nj;
        }
        }
        STAMP(23);
    }

    unsigned omr, owa, owb;
    auto op_addr = [&](int layer) __attribute__((always_inline)) {
        LANE_VARS();
        const int t = wave - 3;
        cma = WS_OWN + rS + (unsigned)(gi * 16384 + layer * 4096 + (lane >> 3) * 512 + t * 128 + (t == 3 ? 0 : (lane & 7) * 16));
        cxa = lb + L_O2 + (unsigned)((lane >> 3) * 416 + t * 128 + (t == 3 ? 0 : (lane & 7) * 16));
        const int pb = (lane & 4) ? 3 - p4 : p4, jp = 2 * pb + ((lane >> 2) & 1);
        cso = tile35(gj, 10 * layer + t) + (unsigned)((kq & 1) * 8192 + gi * 128 + (kq >> 1) * 64 + p4 * 16);
        cra = lb + L_O2 + (unsigned)(jp * 416 + kq * 16); omr = lb + L_O2 + (unsigned)(jp * 416 + 384);
        owa = lb + L_WO + (unsigned)(r0s * 160 + 32 * t + 8 * p4); owb = lb + L_WO + (unsigned)(r1s * 160 + 32 * t + 8 * p4);
        if (layer) PIN2(cma, cxa);
        PIN2(cso, cra); PIN3(omr, owa, owb);
    };
    auto chain = [&](auto LC) __attribute__((always_inline)) {
        constexpr int layer = decltype(LC)::value;
        unsigned h4 = WS_H4 + (unsigned)((layer * 32 + gi) * 32768) + (layer ? rS ^ 8192u : 0u);
        auto sides = [&]() __attribute__((always_inline)) { rS = lds_word(lb + L_CNT + 80); nS = lds_word(lb + L_CNT + 84); h4 += rS ^ 8192u; };
        if (wave < 3) {
            LANE_VARS();
            if (layer == 0) {
                unsigned lo = WS_OWN + lds_word(lb + L_CNT + 84) + (unsigned)(gi * 16384 + (lane >> 3) * 512), lw = lb + L_O2 + (unsigned)((lane >> 3) * 416);
                unsigned lo0 = lo + (unsigned)((lane & 7) * 16), lw0 = lw + (unsigned)((lane & 7) * 16);
                PIN2(lo0, lo); PIN2(lw0, lw);
                GUARD(800);
                MARK(b_g2);
                if (wave == 2) { if (nj | (ord == EMODE)) VMW(0); }
                else {
                    unsigned tf = 0u;
#pragma unroll 1
                    for (unsigned spins = 0; spins < (kSpinCap << 4); ++spins) { tf = lds_word(lb + L_CNT + 48); if (tf) break; }
                    if (wave == 0) {
                        until(tf + DG2);
#pragma unroll 1
                        for (unsigned spins = 0; spins < kSpinCap; ++spins) {
                            u32x4 o0 = ld16_sc1<256>(ws, lo0);
                            if (nj) { VMW(1); lds_add(44, nj); nj = 0u; }
                            WAIT1(o0, 0);
                            *(LAS u32x4*)(size_t)(lw0 + 256) = o0;
                            if (!any_empty(um4(o0))) break;
                            if ((spins & 15u) == 15u) send_flag(lane);
                        }
                    } else {
#pragma unroll 1
                        for (unsigned spins = 0; spins < kSpinCap; ++spins) {
                            u32x4 o0 = ld16_sc1<0>(ws, lo0), o1 = ld16_sc1<128>(ws, lo0), o2 = ld16_sc1<384>(ws, lo);
                            if (nj) { VMW(3); lds_add(44, nj); nj = 0u; }
                            asm volatile("s_waitcnt vmcnt(0)" : "+v"(o0), "+v"(o1), "+v"(o2) :: "memory");
                            *(LAS u32x4*)(size_t)lw0 = o0; *(LAS u32x4*)(size_t)(lw0 + 128) = o1; *(LAS u32x4*)(size_t)(lw + 384) = o2;
                            if (!any_empty(max(max(um4(o0), um4(o1)), um4(o2)))) break;
                        }
                    }
                }
                if (nj) lds_add(44, nj);
                MARK(e_g2);
                STAMP(25);
                BAR();
                sides();
                if (wave < 2) empty1(wave, rS, lane);
                else {
                    st16m(ws, WS_CS + (unsigned)((gi * 8 + gj) * 768 + lane * 16), *reinterpret_cast<const u32x4*>(smem + L_CS + lane * 16), 0xffffffffffffull);
                    empty5(10, 0u, lane); empty5(10, 8192u, lane);
                }
            } else BAR();
            {
                const unsigned im = lb + L_GU + (unsigned)(32 * wave + 8 * p4);
                Frag80 fg = frag80(im, 96, r0s, r1s, r2s), fu = frag80(im + 8192, 96, r0s, r1s, r2s);
                const unsigned xb = lb + L_XS + (unsigned)((n < 4 ? n : 4) * 160);
                unsigned b16 = xb + kq * 16, b8 = xb + 128 + kq * 8;
                unsigned sg = h4 + (unsigned)(n < 4 ? h4s(wave) + gj * 512 + kq * 64 + n * 16 : H4C + wave * 1024 + gj * 128 + kq * 16), su = sg + (n < 4 ? 256u : 64u);
                unsigned o = h4 + h4s(wave) + (unsigned)(lane * 16);
                unsigned oc = h4 + (unsigned)(H4C + wave * 1024 + ((lane >> 5) * 4 + p4) * 128 + (kq & 1) * 64 + q4 * 16);
                unsigned os = h4 + (unsigned)(H4T + (q4 + 4 * (kq & 1)) * 128 + p4 * 16);
                unsigned ha = lb + L_H + (unsigned)(p4 * 96 + wave * 32 + q4 * 8 + kq * 2);
                f32x2 cs = {0.f, 0.f};
                if (layer != 0) cs = *reinterpret_cast<const f32x2*>(smem + L_CS2 + wave * 512 + lane * 8);
                PIN3(fg.a0, fg.a1, fg.a2); PIN3(fu.a0, fu.a1, fu.a2); PIN3(b16, b8, sg); PIN3(su, o, os); PIN2(ha, cs);
                unsigned long long mk = layer ? 0x000f000f000f000full : 0x001f001f001f001full;
                unsigned ta = WS_MB + (unsigned)((lane >> 1) * 1024 + 64 + (lane & 1) * 16);
                if (layer == 0) PIN2(oc, ta);
                unsigned x4a = layer ? 0u : X4A * lds_word(lb + L_CNT + 20) >> 12;
                PINS(x4a);
                unsigned long long rdm = 0x3acccccd3acccccdull;
                PINS(mk); PINS(rdm);
                if (layer == 0) VMW(0);
                if (layer == 0) GUARD(1200); else GUARD(800);
                MARK(b_gu);
                BAR();
                STAMP(10 + 32 * layer);
                u32x4 tl = {0u, 0u, 0u, 0u};
                if (layer == 0) tl = ld16_sc1(ws, ta);
                const Frag80 b = rows80(b16, b8);
                const f32x4 g0 = mfma32(fg.a0, b.a0, z4), u0 = mfma32(fu.a0, b.a0, z4), g1 = mfma32(fg.a1, b.a1, g0), g2 = mfma16(fg.a2, b.a2, z4), u1 = mfma32(fu.a1, b.a1, u0), u2 = mfma16(fu.a2, b.a2, z4);
                __builtin_amdgcn_sched_barrier(0);
                const f32x4 cg = g1 + g2, cu = u1 + u2;
                st16m2<0>(ws, sg, asu(cg), su, asu(cu), mk);
                STAMP(11 + 32 * layer);
                if (layer == 0) {
                    WAIT1(tl, 2);
#pragma unroll 1
                    for (unsigned spins = 0; spins < kSpinCap && !all_set(min(min(tl[0], tl[1]), min(tl[2], tl[3]))); ++spins) {
                        tl = ld16_sc1(ws, ta);
                        WAIT1(tl, 0);
                    }
                    const unsigned tn = now();
                    unsigned d = min(min(min(tn - tl[0], tn - tl[1]), min(tn - tl[2], tn - tl[3])), 800u);
                    d = min(d, dppu<0xB1>(d)); d = min(d, dppu<0x4E>(d)); d = min(d, dppu<0x124>(d)); d = min(d, dppu<0x128>(d));
                    unsigned tr = tn - min(min((unsigned)__builtin_amdgcn_readlane(d, 0), (unsigned)__builtin_amdgcn_readlane(d, 16)), min((unsigned)__builtin_amdgcn_readlane(d, 32), (unsigned)__builtin_amdgcn_readlane(d, 48)));
                    lds_set(lb + L_CNT + 56, tr | 1u);
                    until(__builtin_amdgcn_readfirstlane(tr | 1u) + x4a);
                } else sched(X4, 0);
                STAMP(16 + 32 * layer);
                float hv;
                u32x4 a0, a1, a2, a3, ac, as;
                auto load = [&]() __attribute__((always_inline)) {
                    as = ld16_sc1(ws, os);
                    if (layer == 0) ac = ld16_sc1(ws, oc);
                    a0 = ld16_sc1<0>(ws, o); a1 = ld16_sc1<1024>(ws, o); a2 = ld16_sc1<2048>(ws, o); a3 = ld16_sc1<3072>(ws, o);
                };
                auto all_in = [&]() __attribute__((always_inline)) {
                    if (layer == 0) asm volatile("s_waitcnt vmcnt(0)" : "+v"(a0), "+v"(a1), "+v"(a2), "+v"(a3), "+v"(ac), "+v"(as) :: "memory");
                    else { asm volatile("s_waitcnt vmcnt(0)" : "+v"(a0), "+v"(a1), "+v"(a2), "+v"(a3), "+v"(as) :: "memory"); ac = as; }
                };
                auto compute = [&]() __attribute__((always_inline)) {
#define ROR(n) " row_ror:" #n " row_mask:0xf bank_mask:0xf\n\t"
#define QD2(p) " quad_perm:" p " row_mask:0xf bank_mask:0xf\n\t"
                    asm volatile("s_waitcnt vmcnt(%16)\n\tv_add_f32_dpp v40, %2, %2" ROR(8) "v_add_f32_dpp v41, %3, %3" ROR(8) "s_nop 0\n\tv_add_f32_dpp v40, v40, v40" ROR(4) "v_add_f32_dpp v41, v41, v41" ROR(4)
                                 "v_mov_b32 v42, v40\n\tv_mov_b32 v43, v41\n\ts_nop 0\n\tv_permlane16_swap_b32 v40, v42\n\tv_permlane16_swap_b32 v41, v43\n\t"
                                 "v_pk_add_f32 v[40:41], v[40:41], v[42:43]\n\tv_pk_mul_f32 v[40:41], v[40:41], %14\n\t"
                                 ".if %17 == 0\n\ts_waitcnt vmcnt(4)\n\tv_mov_b64 v[44:45], %12\n\tv_mov_b64 v[46:47], %13\n\t.endif\n\t"
                                 "v_fma_f32 v40, -v41, v41, v40\n\tv_cvt_pk_bf16_f32 v42, 0, v41\n\tv_maximum3_f32 v40, v40, 0, 0\n\t"
                                 ".if %17 == 0\n\tv_permlane32_swap_b32 v44, v46\n\tv_permlane32_swap_b32 v45, v47\n\tv_pk_add_f32 v[44:45], v[44:45], v[46:47]\n\t.endif\n\t"
                                 "v_cvt_pk_bf16_f32 v40, 0, v40\n\ts_waitcnt vmcnt(2)\n\tv_pk_add_f32 v[32:33], %4, %6\n\tv_add_f32 v40, 0x3727c5ac, v40\n\tv_pk_add_f32 v[34:35], %5, %7\n\t"
                                 ".if %17 == 0\n\tv_permlane16_swap_b32 v44, v45\n\t.endif\n\t"
                                 "v_cvt_pk_bf16_f32 v40, 0, v40\n\ts_waitcnt vmcnt(0)\n\tv_pk_add_f32 v[36:37], %8, %10\n\tv_sqrt_f32 v40, v40\n\tv_pk_add_f32 v[38:39], %9, %11\n\t"
                                 ".if %17 == 0\n\tv_add_f32_dpp v44, v44, v44" QD2("[1,0,3,2]") "v_add_f32_dpp v45, v45, v45" QD2("[1,0,3,2]") ".endif\n\t"
                                 "v_pk_add_f32 v[32:33], v[32:33], v[36:37]\n\tv_pk_add_f32 v[34:35], v[34:35], v[38:39]\n\tv_cvt_pk_bf16_f32 v40, 0, v40\n\t"
                                 ".if %17 == 0\n\tv_add_f32_dpp v44, v44, v44" QD2("[2,3,0,1]") "v_add_f32_dpp v45, v45, v45" QD2("[2,3,0,1]") "v_mov_b64 %1, v[44:45]\n\t.else\n\ts_nop 0\n\t.endif\n\t"
                                 "v_permlane32_swap_b32 v32, v34\n\tv_permlane32_swap_b32 v33, v35\n\tv_rcp_f32 v40, v40\n\tv_pk_add_f32 v[32:33], v[32:33], v[34:35]\n\ts_nop 1\n\t"
                                 "v_permlane16_swap_b32 v32, v33\n\t"
                                 "v_pk_fma_f32 v[32:33], %1, v[42:43], v[32:33] op_sel_hi:[1,0,1] neg_lo:[1,0,0] neg_hi:[1,0,0]\n\tv_pk_mul_f32 v[32:33], v[32:33], v[40:41] op_sel_hi:[1,0]\n\t"
                                 "v_cvt_pk_bf16_f32 v32, 0, v32\n\tv_mul_f32 v34, 0xbfb8aa3b, v32\n\tv_exp_f32 v34, v34\n\tv_cvt_pk_bf16_f32 v33, 0, v33\n\tv_add_f32 v34, 1.0, v34\n\t"
                                 "v_rcp_f32 v34, v34\n\ts_nop 0\n\tv_mul_f32 v32, v34, v32\n\tv_cvt_pk_bf16_f32 v32, 0, v32\n\tv_mul_f32 %0, v33, v32\n\tv_cvt_pk_bf16_f32 v32, %0, %0\n\tds_write_b16 %15, v32"
                                 : "=&v"(hv), "+v"(cs)
                                 : "v"(as[0]), "v"(as[1]), "v"(LO2(a0)), "v"(HI2(a0)), "v"(LO2(a1)), "v"(HI2(a1)), "v"(LO2(a2)), "v"(HI2(a2)), "v"(LO2(a3)), "v"(HI2(a3)),
                                   "v"(LO2(layer ? as : ac)), "v"(HI2(layer ? as : ac)), "s"(rdm), "v"(ha), "i"(layer ? 4 : 5), "i"(layer), "v"(as)
                                 : "memory", "v32", "v33", "v34", "v35", "v36", "v37", "v38", "v39", "v40", "v41", "v42", "v43", "v44", "v45", "v46", "v47");
#undef ROR
#undef QD2
                };
                RETRY(load(), (all_in(), max(max(max(um4(a0), um4(a1)), max(um4(a2), um4(a3))), max(um4(ac), um4(as)))), compute(), any_nan(hv, hv), layer ? X4 : X4A);
                BAR();
                MARK(e_gu);
                STAMP(12 + 32 * layer);
                if (layer == 0) {
                    *reinterpret_cast<f32x2*>(smem + L_CS2 + wave * 512 + lane * 8) = cs;
                }
            }
            if (layer == 0) {
                if (wave == 1) {
                    const unsigned o = WS_CS + (unsigned)(gi * 6144 + 3072 + (lane < 48 ? lane : 0) * 16);
                    u32x4 a[8];
                    GATHER8(a, o, 768);
                    WAIT8(a, 0);
                    if (lane < 48) *reinterpret_cast<f32x4*>(smem + L_CS + lane * 16) = SUM8(a);
                }
                Frag80 f[4];
#pragma unroll
                for (int i = 0; i < 4; ++i) { f[i] = frag80(lb + L_WQ + (unsigned)(128 * wave + 32 * i + 8 * p4), 416, r0s, r1s, r2s); PIN3(f[i].a0, f[i].a1, f[i].a2); }
                unsigned b16 = lb + L_XS + (unsigned)(q4 * 160 + kq * 16), b8 = lb + L_XS + (unsigned)(q4 * 160 + 128 + kq * 8);
                unsigned so = WS_H1 + rS + (unsigned)((gi * 4 + q4) * 16384 + gj * 896 + 256 * wave + kq * 64 + p4 * 16);
                unsigned long long mk = 0x1111111111111111ull;
                PIN3(b16, b8, so); PINS(mk);
                GUARD(3800);
                MARK(b_q);
                BAR();
                STAMP(33);
                const Frag80 b = rows80(b16, b8);
                st16_sc1(ws, so, tiles4(f, b, mk));
                STAMP(34);
                qk_addr(true); att_addr();
                cma = lb + L_M; cxa = lb + L_M + (unsigned)(wave == 0 ? (lane >> 5) * 16 : (lane >> 4) * 8);
                cra = lb + L_OP + (unsigned)(wave == 0 ? (lane >> 5) * 4096 + (lane & 31) * 16 : 512 + (lane >> 4) * 2048 + (lane & 15) * 16);
                if (wave == 2) ml_addr(lane, cxa, cra);
                cso = WS_OWN + rS + (unsigned)(gi * 16384 + 4096 + gj * 512) + (unsigned)(wave == 0 ? lane * 4 : wave == 1 ? 256 + (lane >> 5) * 64 + (lane & 15) * 4 : 384);
                PIN2(cma, cxa); PIN2(cra, cso);
                asm volatile("" : "=v"(omr), "=v"(owa), "=v"(owb));
            }
        } else {
            const int t = wave - 3;
            StatAddr sa;
            u32x4 sel;
            unsigned long long hi = 0xff00ff00ff00ff00ull;
            auto gather32 = [&](unsigned o, unsigned X, auto fin) __attribute__((always_inline)) {
                u32x4 a0 = ld16_sc1<-2048>(ws, o), a1 = ld16_sc1<-1024>(ws, o), a2 = ld16_sc1<0>(ws, o), a3 = ld16_sc1<1024>(ws, o);
                float v;
                auto sum = [&]() __attribute__((always_inline)) {
                    asm volatile("s_waitcnt vmcnt(3)\n\tv_mfma_f32_16x16x32_bf16 v[40:43], %1, %5, 0\n\ts_waitcnt vmcnt(2)\n\tv_mfma_f32_16x16x32_bf16 v[40:43], %2, %5, v[40:43]\n\t"
                                 "s_waitcnt vmcnt(1)\n\tv_mfma_f32_16x16x32_bf16 v[40:43], %3, %5, v[40:43]\n\ts_waitcnt vmcnt(0)\n\tv_mfma_f32_16x16x32_bf16 v[40:43], %4, %5, v[40:43]\n\t"
                                 "s_nop 7\n\tv_cndmask_b32 v40, v40, v41, %6\n\tv_cndmask_b32 v42, v42, v43, %6\n\ts_nop 1\n\tv_permlane32_swap_b32 v40, v42\n\tv_add_f32 v40, v40, v42\n\t"
                                 "v_cvt_pk_bf16_f32 %0, 0, v40"
                                 : "=&v"(v), "+v"(a0), "+v"(a1), "+v"(a2), "+v"(a3) : "v"(sel), "s"(hi) : "memory", "v40", "v41", "v42", "v43");
                    fin(v);
                    __builtin_amdgcn_sched_barrier(0);
                };
                sum();
                if (__builtin_expect(any_nan(v, v) != 0ull, 0)) {
#pragma unroll 1
                    for (unsigned spins = 0; spins < kSpinCap; ++spins) {
                        slip(X);
                        again16<-2048>(a0, ws, o); again16<-1024>(a1, ws, o); again16<0>(a2, ws, o); again16<1024>(a3, ws, o);
                        asm volatile("s_waitcnt vmcnt(0)" : "+v"(a0), "+v"(a1), "+v"(a2), "+v"(a3) :: "memory");
                        if (!any_empty(max(max(um4(a0), um4(a1)), max(um4(a2), um4(a3))))) break;
                    }
                    sum();
                }
            };
#define PUT8(A, B, M) "v_min_u32 v34, -2, v32\n\tv_min_u32 v32, -2, v32\n\tv_min_u32 v35, -2, v33\n\tv_min_u32 v33, -2, v33\n\tv_permlane16_swap_b32 v32, v34\n\ts_nop 0\n\t" \
                      "v_permlane16_swap_b32 v33, v35\n\ts_mov_b64 exec, " M "\n\tglobal_store_dwordx4 " A ", v[32:35], " B " sc1\n\ts_mov_b64 exec, -1\n\ts_nop 0"
            unsigned xw, qo, qw, qc;
            __builtin_amdgcn_s_setprio(0);
            {
                STAMP(6 + 32 * layer);
                bf16x8 af[6];
                unsigned long long mk = 0x000f000f000f000full;
                if (layer == 0) {
                    const int lane = lane_id();
                    xe = lo16(*reinterpret_cast<const u16*>(smem + L_X0 + xrow(lane) * 1280 + gj * 160 + t * 32 + xcol(lane) * 2));
                    op_addr(0);
                    GUARD(1500);
                    MARK(b_op);
                    if (nj | (ord == EMODE)) { VMW(0); lds_add(44, nj); }
#pragma unroll 1
                    for (unsigned spins = 0; spins < (kSpinCap << 4); ++spins) if (lds_word(lb + L_CNT + 44) >= 5u) break;
                    STAMP(8);
                } else MARK(b_op);
                const unsigned lo = cma, lw = cxa, so = cso, br = cra, mr = omr, h3 = tile35(gj, 10 * layer + t);
                unsigned go, xs;
#pragma unroll
                for (int s = 0; s < 6; ++s) af[s] = cat8(tr64o(owa, s * 32 * 160), tr64o(owb, s * 32 * 160));
                PINS(mk); PINS(hi);
                if (layer != 0 && t < 4) {
                    sched(X2, 0);
                    STAMP(49);
                    u32x4 ov = ld16_sc1(ws, lo);
                    WAIT1(ov, 0);
                    *(LAS u32x4*)(size_t)lw = ov;
                    if (__builtin_expect(any_empty(um4(ov)), 0)) {
#pragma unroll 1
                        for (unsigned spins = 0; spins < kSpinCap; ++spins) {
                            slip(X2);
                            ov = ld16_sc1(ws, lo);
                            WAIT1(ov, 0);
                            *(LAS u32x4*)(size_t)lw = ov;
                            if (!any_empty(um4(ov))) break;
                        }
                    }
                }
                {
                    bf16x8 b0, b1, b2, b3, b4, b5;
#define QP " row_half_mirror row_mask:0xf bank_mask:0xf\n\t"
                    asm volatile("s_waitcnt lgkmcnt(0)\n\ts_barrier\n\t"
                                 "ds_read_b128 %0, %12\n\tds_read_b64 v[36:37], %13\n\tds_read_b128 %1, %12 offset:64\n\tds_read_b128 %2, %12 offset:128\n\tds_read_b128 %3, %12 offset:192\n\t"
                                 "ds_read_b128 %4, %12 offset:256\n\tds_read_b128 %5, %12 offset:320\n\t"
                                 "s_waitcnt lgkmcnt(5)\n\tv_mfma_f32_16x16x32_bf16 v[32:35], %6, %0, 0\n\tv_max_f32_dpp v38, v36, v36" QP "v_sub_f32 v38, v36, v38\n\t"
                                 "s_waitcnt lgkmcnt(4)\n\tv_mfma_f32_16x16x32_bf16 v[32:35], %7, %1, v[32:35]\n\tv_exp_f32 v38, v38\n\t"
                                 "s_waitcnt lgkmcnt(3)\n\tv_mfma_f32_16x16x32_bf16 v[32:35], %8, %2, v[32:35]\n\tv_mul_f32 v37, v37, v38\n\t"
                                 "s_waitcnt lgkmcnt(2)\n\tv_mfma_f32_16x16x32_bf16 v[32:35], %9, %3, v[32:35]\n\tv_add_f32_dpp v39, v37, v37" QP
                                 "s_waitcnt lgkmcnt(1)\n\tv_mfma_f32_16x16x32_bf16 v[32:35], %10, %4, v[32:35]\n\tv_rcp_f32 v39, v39\n\t"
                                 "s_waitcnt lgkmcnt(0)\n\tv_mfma_f32_16x16x32_bf16 v[32:35], %11, %5, v[32:35]\n\tv_mul_f32 v38, v38, v39\n\ts_nop 6\n\t"
                                 "v_pk_mul_f32 v[32:33], v[32:33], v[38:39] op_sel_hi:[1,0]\n\tv_pk_mul_f32 v[34:35], v[34:35], v[38:39] op_sel_hi:[1,0]\n\ts_nop 0\n\t"
                                 "v_add_f32_dpp v32, v32, v32" QP "v_add_f32_dpp v33, v33, v33" QP "v_add_f32_dpp v34, v34, v34" QP "v_add_f32_dpp v35, v35, v35" QP
                                 "v_cvt_pk_bf16_f32 v32, v32, v33\n\tv_cvt_pk_bf16_f32 v33, v34, v35\n\t" PUT8("%14", "%15", "%16")
                                 : "=&v"(b0), "=&v"(b1), "=&v"(b2), "=&v"(b3), "=&v"(b4), "=&v"(b5)
                                 : "v"(af[0]), "v"(af[1]), "v"(af[2]), "v"(af[3]), "v"(af[4]), "v"(af[5]), "v"(br), "v"(mr), "v"(so), "s"(ws), "s"(mk)
                                 : "memory", "v32", "v33", "v34", "v35", "v36", "v37", "v38", "v39");
#undef QP
                }
                if (layer == 0 && wave == 7) st4_sc1(ws, WS_MB + (unsigned)(gi * 1024 + 64 + gj * 4), now() | 1u);
                const unsigned tp = layer ? 0u : now();
                if (layer == 0) sides();
                STAMP(7 + 32 * layer);
                {
                    const int ln = lane_id();
#pragma unroll
                    for (int k = 0; k < 4; ++k) sel[k] = ((ln >> 1) & 3) == k ? (ln & 1) ? 0x3f800000u : 0x3f80u : 0u;
                    go = h3 + nS + (unsigned)(2048 + ln * 16); xs = lb + L_XS + (unsigned)(xrow(ln) * 160 + t * 32 + xcol(ln) * 2);
                    PIN3(sel, go, xs);
                    sa = stats_addr(ln, h4 + (unsigned)(H4T + gj * 128), 16);
                }
                if (layer != 0) sched(X3, 0);
                else until(tp + W3);
                STAMP(18 + 32 * layer);
                if (layer != 0) gather32(go, X3, [&](float s) { xw = pk2(xe + s, 0.f); *(LAS u16*)(size_t)xs = (u16)xw; });
                else {
                    unsigned long long m0, m1, m2, m3; unsigned cnt = kSpinCap;
#define CHK(r, m) "v_max3_u32 v44, v" #r ", v[" #r "+1], v[" #r "+2]\n\tv_max_u32 v44, v44, v[" #r "+3]\n\tv_cmp_eq_u32 %" #m ", -1, v44\n\t"
                    asm volatile("s_mov_b64 %1, -1\n\ts_mov_b64 %2, -1\n\ts_mov_b64 %3, -1\n\ts_mov_b64 %4, -1\n"
                                 "1:\n\ts_mov_b64 exec, %1\n\tglobal_load_dwordx4 v[0:3], %6, %7 offset:-2048 sc1\n\ts_mov_b64 exec, %2\n\tglobal_load_dwordx4 v[4:7], %6, %7 offset:-1024 sc1\n\t"
                                 "s_mov_b64 exec, %3\n\tglobal_load_dwordx4 v[8:11], %6, %7 sc1\n\ts_mov_b64 exec, %4\n\tglobal_load_dwordx4 v[12:15], %6, %7 offset:1024 sc1\n\ts_mov_b64 exec, -1\n\t"
                                 "s_sub_u32 %5, %5, 1\n\ts_cselect_b64 vcc, 0, -1\n\ts_waitcnt vmcnt(0)\n\t"
                                 "v_mfma_f32_16x16x32_bf16 v[40:43], v[0:3], %8, 0\n\t" CHK(0, 1) "v_mfma_f32_16x16x32_bf16 v[40:43], v[4:7], %8, v[40:43]\n\t" CHK(4, 2)
                                 "v_mfma_f32_16x16x32_bf16 v[40:43], v[8:11], %8, v[40:43]\n\t" CHK(8, 3) "v_mfma_f32_16x16x32_bf16 v[40:43], v[12:15], %8, v[40:43]\n\t" CHK(12, 4)
                                 "s_or_b64 exec, %1, %2\n\ts_or_b64 exec, exec, %3\n\ts_or_b64 exec, exec, %4\n\ts_and_b64 vcc, exec, vcc\n\ts_cbranch_scc1 1b\n\t"
                                 "s_mov_b64 exec, -1\n\tv_cndmask_b32 v40, v40, v41, %9\n\tv_cndmask_b32 v42, v42, v43, %9\n\ts_nop 1\n\tv_permlane32_swap_b32 v40, v42\n\tv_add_f32 v40, v40, v42\n\t"
                                 "v_cvt_pk_bf16_f32 v40, 0, v40\n\tv_add_f32 v40, %10, v40\n\tv_cvt_pk_bf16_f32 %0, v40, 0\n\tds_write_b16 %11, %0"
                                 : "=&v"(xw), "=&s"(m0), "=&s"(m1), "=&s"(m2), "=&s"(m3), "+s"(cnt) : "v"(go), "s"(ws), "v"(sel), "s"(hi), "v"(xe), "v"(xs)
                                 : "memory", "vcc", "scc", "v0", "v1", "v2", "v3", "v4", "v5", "v6", "v7", "v8", "v9", "v10", "v11", "v12", "v13", "v14", "v15", "v40", "v41", "v42", "v43", "v44");
#undef CHK
                }
                xe = lo16(xw);
                STAMP(9 + 32 * layer);
            }
            BAR();
            if (wave == 3) stats(sa);
            MARK(e_op);
            STAMP(11 + 32 * layer);
            {
                LANE_VARS();
                const unsigned im = lb + L_WD + (unsigned)(32 * t + 8 * p4);
                bf16x8 wd0 = cat8(tr64(im + r0s * 160), tr64(im + r1s * 160)); bf16x4 wd1 = tr64(im + (32 + r2s) * 160);
                const unsigned h5 = tile35(gj, 10 * layer + 5 + t);
                unsigned h16 = lb + L_H + (unsigned)(p4 * 96 + kq * 16), h8 = lb + L_H + (unsigned)(p4 * 96 + 64 + kq * 8);
                unsigned so = h5 + (unsigned)((kq & 1) * 8192 + gi * 128 + (kq >> 1) * 64 + p4 * 16);
                unsigned long long mk = 0x000f000f000f000full;
                unsigned go = h5 + nS + (unsigned)(2048 + lane * 16), xs = (layer ? (unsigned)(xrow(lane) * 1280 + gj * 160) : lb + L_XS + (unsigned)(xrow(lane) * 160)) + (unsigned)(t * 32 + xcol(lane) * 2);
                unsigned sta = lb + L_ST + (unsigned)(xrow(lane) * 16);
                if (layer == 0 && wave == 4) {
                    asm volatile("" : "=v"(sa.a16), "=v"(sa.b16), "=v"(sa.a8), "=v"(sa.b8), "=v"(sa.dst), "=v"(qo), "=v"(qw), "=v"(qc), "=v"(cma), "=v"(cxa), "=v"(cra), "=v"(cso), "=v"(omr), "=v"(owa), "=v"(owb));
                    asm volatile("" : "=v"(qa), "=v"(pw), "=v"(ma), "=v"(idx), "=v"(qm), "=v"(pr), "=v"(oa), "=v"(oc));
                } else if (layer == 0) {
                    sa = stats_addr(lane, WS_H1 + rS + (unsigned)(gi * 65536 + gj * 896 + 768), 16384);
                    op_addr(1); qk_addr(false); att_addr();
                    const int l5 = lane & 31, c = l5 < 24 ? 24 * (wave - 6) + l5 : 48;
                    qo = WS_H1 + rS + (unsigned)((gi * 4 + (gj >> 1)) * 16384 + 3584 + (lane >> 5) * 896 + c * 16);
                    qw = lb + L_Q + (unsigned)(l5 < 24 ? (c >> 4) * 128 + (c & 3) * 32 + (c & 12) + (lane >> 5) * 16 : lane < 26 ? 384 + (lane & 1) * 16 : 448);
                    qc = lb + L_CS + (unsigned)(c * 16 + (lane >> 5) * 8);
                    PIN3(qo, qw, qc);
                }
                PIN2(wd0, wd1); PIN3(h16, h8, so); PIN3(go, xs, sta); PINS(mk);
                GUARD(2000);
                MARK(b_dn);
                if (wave == 4) {
                    unsigned os = h4 + (unsigned)(H4T + (p4 + 4 * (kq & 1)) * 128 + q4 * 16);
                    float sw = *reinterpret_cast<const float*>(smem + L_CNT + 12);
                    PIN2(os, sw);
                    if (layer == 0) {
#pragma unroll 1
                        for (unsigned spins = 0; spins < (kSpinCap << 4); ++spins) if (lds_word(lb + L_CNT + 56)) break;
                    }
                    sched(layer ? X4 : X4A, 0);
                    u32x4 as;
#pragma unroll 1
                    for (unsigned spins = 0; spins < kSpinCap; ++spins) { as = ld16_sc1(ws, os); WAIT1(as, 0); if (!any_empty(um4(as))) break; }
                    float ss = __uint_as_float(as[0]), sm = __uint_as_float(as[1]), rd = __uint_as_float(as[2]);
                    ss += dpp<0x4E>(ss); sm += dpp<0x4E>(sm); rd += dpp<0x4E>(rd);
                    ss += dpp<0xB1>(ss); sm += dpp<0xB1>(sm); rd += dpp<0xB1>(rd);
                    ss = x16_add(ss); sm = x16_add(sm); rd = x16_add(rd);
                    if (LANES(0x1111ull)) *reinterpret_cast<f32x4*>(smem + L_ST + q4 * 16) = (f32x4){ss, sm, rd, sw};
                }
                if (layer == 0 && wave > 4) {
#pragma unroll 1
                    for (unsigned spins = 0; spins < (kSpinCap << 4); ++spins) { if (lds_word(lb + L_CNT + 56)) break; __builtin_amdgcn_s_sleep(1); }
                    sched(X4A, 5);
                    if (wave == 5) empty4(1, rS, lane);
                    else st16m(ws, lane < 40 ? tile35(gj, 15 + (lane >> 3)) + (unsigned)((wave - 6) * 8192 + gi * 128 + (lane & 7) * 16)
                                             : WS_OWN + rS + (unsigned)(gi * 16384 + 4096 + gj * 512 + (wave - 6) * 256 + (lane - 40) * 16), (u32x4){~0u, ~0u, ~0u, ~0u}, 0xffffffffffffffull);
                }
                asm volatile("s_waitcnt lgkmcnt(0)\n\ts_barrier\n\tds_read_b128 v[36:39], %2\n\tds_read_b64 v[40:41], %3\n\t"
                             "s_waitcnt lgkmcnt(1)\n\tv_mfma_f32_16x16x32_bf16 v[32:35], %0, v[36:39], 0\n\ts_waitcnt lgkmcnt(0)\n\tv_mfma_f32_16x16x16_bf16 v[42:45], %1, v[40:41], 0\n\ts_nop 7\n\t"
                             "v_pk_add_f32 v[32:33], v[32:33], v[42:43]\n\tv_pk_add_f32 v[34:35], v[34:35], v[44:45]\n\t"
                             "v_cvt_pk_bf16_f32 v32, v32, v33\n\tv_cvt_pk_bf16_f32 v33, v34, v35\n\t" PUT8("%4", "%5", "%6")
                             :: "v"(wd0), "v"(wd1), "v"(h16), "v"(h8), "v"(so), "s"(ws), "s"(mk)
                             : "memory", "v32", "v33", "v34", "v35", "v36", "v37", "v38", "v39", "v40", "v41", "v42", "v43", "v44", "v45");
                STAMP(13 + 33 * layer);
                if (layer == 0 || gi == 4 * t + (gj & 3)) {
                    const f32x4 sr = *(const LAS f32x4*)(size_t)sta;
                    const f32x2 st = ln_stats(sr[0], sr[1]);
                    float rs = bfr(frcp(1.f + ex2(bfr((sr[2] - st[0] * sr[3]) * st[1]) * -LOG2E))), n1 = bfr(bfr(xe - st[0]) * st[1]);
                    PIN2(rs, n1);
                    if (layer == 0) {
                        sched(X5A, 0);
                        STAMP(15);
                        gather32(go, X5A, [&](float y) { xw = pk2(xe + bfr(n1 + bfr(y * rs)), 0.f); *(LAS u16*)(size_t)xs = (u16)xw; });
                        xe = lo16(xw);
                        STAMP(14);
                    } else {
                        sched(X5B, 0);
                        STAMP(47);
                        gather32(go, X5B, [&](float y) { xw = pk2(xe + bfr(n1 + bfr(y * rs)), 0.f); });
                        st2(out, xs, xw);
                        __builtin_amdgcn_endpgm();
                        MARK(e_dn);
                        STAMP(45);
                    }
                }
            }
            if (layer == 0) {
                BAR();
                if (wave == 3) { stats(sa); STAMP(34); }
                MARK(e_dn);
                if (wave == 3) __builtin_amdgcn_s_setprio(2);
                if (__builtin_expect(wave == 4, 0)) { op_addr(1); qk_addr(false); att_addr(); }
                if (wave >= 6) {
                    const unsigned o = qo;
                    f32x2 cs = -*(const LAS f32x2*)(size_t)qc;
                    float rn = 1.f / DM;
                    unsigned long long m0 = 1ull << 24, m1 = 1ull << 25;
                    PIN2(cs, rn); PINS(m0); PINS(m1);
                    MARK(b_qg);
                    sched(X1, 0);
                    STAMP(51);
                    u32x4 a0, a1, a2, a3; f32x2 u;
                    RETRY({ a0 = ld16_sc1<-3584>(ws, o); a1 = ld16_sc1<-1792>(ws, o); a2 = ld16_sc1<0>(ws, o); a3 = ld16_sc1<1792>(ws, o); },
                          ({ asm volatile("s_waitcnt vmcnt(0)" : "+v"(a0), "+v"(a1), "+v"(a2), "+v"(a3) :: "memory"); max(max(um4(a0), um4(a1)), max(um4(a2), um4(a3))); }),
                          { unsigned sm;
                            asm volatile("s_waitcnt vmcnt(2)\n\tv_pk_add_f32 v[40:41], %2, %4\n\tv_pk_add_f32 v[42:43], %3, %5\n\t"
                                         "s_waitcnt vmcnt(1)\n\tv_pk_add_f32 v[40:41], v[40:41], %6\n\tv_pk_add_f32 v[42:43], v[42:43], %7\n\t"
                                         "s_waitcnt vmcnt(0)\n\tv_pk_add_f32 v[40:41], v[40:41], %8\n\tv_pk_add_f32 v[42:43], v[42:43], %9\n\ts_nop 1\n\t"
                                         "v_permlane32_swap_b32 v40, v42\n\tv_permlane32_swap_b32 v41, v43\n\tv_pk_add_f32 v[40:41], v[40:41], v[42:43]\n\ts_nop 0\n\t"
                                         "v_readlane_b32 %1, v41, 24\n\ts_nop 1\n\tv_mul_f32 v42, %1, %11\n\tv_cvt_pk_bf16_f32 v42, 0, v42\n\tv_pk_fma_f32 %0, %10, v[42:43], v[40:41] op_sel_hi:[1,0,1]"
                                         : "=&v"(u), "=&s"(sm) : "v"(LO2(a0)), "v"(HI2(a0)), "v"(LO2(a1)), "v"(HI2(a1)), "v"(LO2(a2)), "v"(HI2(a2)), "v"(LO2(a3)), "v"(HI2(a3)), "v"(cs), "v"(rn)
                                         : "memory", "v40", "v41", "v42", "v43");
                            unsigned w = pk2v(u);
                            if (LANES(m0)) w = __float_as_uint(u[0]);
                            if (LANES(m1)) w = __float_as_uint(u[1]);
                            *(LAS unsigned*)(size_t)qw = w; __builtin_amdgcn_sched_barrier(0); }, any_nan(u[0], u[1]), X1);
                    MARK(e_qg);
                }
            }
        }
    };
    PINS(ck1); PINS(ck2); PINS(csc);
    chain(IC<0>{});
    STAMP(35);
    att1();
    chain(IC<1>{});
}

__device__ __attribute__((used, noinline)) void mega_code_end() { asm volatile(".Lmega_code_end:"); }

extern "C" void kernel_launch(void* const* d_in, const int* in_sizes, int n_in, void* d_out, int out_size, void* d_ws,
                              size_t ws_size, hipStream_t stream) {
    const u16* x0 = (const u16*)d_in[0];
    const u16* kc = (const u16*)d_in[1];
    const u16* vc = (const u16*)d_in[2];
    const u16* wq = (const u16*)d_in[3];
    const u16* wo = (const u16*)d_in[4];
    const u16* wr = (const u16*)d_in[6];
    const u16* wg = (const u16*)d_in[7];
    const u16* wu = (const u16*)d_in[8];
    const u16* wd = (const u16*)d_in[9];
    (void)hipFuncSetAttribute((const void*)mega_kernel, hipFuncAttributeMaxDynamicSharedMemorySize, L_END);
    mega_kernel<<<256, 512, L_END, stream>>>(x0, kc, vc, wq, wo, wr, wg, wu, wd, (u16*)d_out, (unsigned char*)d_ws
                                             );
}
```

```cpp
#include <hip/hip_runtime.h>
#include <hip/hip_bf16.h>
#include <stdint.h>

typedef unsigned short u16;
typedef unsigned u32x4 __attribute__((ext_vector_type(4)));
typedef unsigned u32x2 __attribute__((ext_vector_type(2)));
typedef unsigned long long u64x8 __attribute__((ext_vector_type(8)));
typedef unsigned long long u64x2 __attribute__((ext_vector_type(2)));
typedef float f32x4 __attribute__((ext_vector_type(4)));
typedef short s16x4 __attribute__((ext_vector_type(4)));
typedef __attribute__((ext_vector_type(4))) __bf16 bf16x4;
typedef __attribute__((ext_vector_type(8))) __bf16 bf16x8;
#define LAS __attribute__((address_space(3)))

constexpr int DM = 640, HQ = 32, SK = 4096, DH = 192, WIN = 1024, FF = 1536;
constexpr int QD = HQ * DH;
constexpr float SCALE = 0.07216878364870322f, LOG2E = 1.44269504088896341f;
constexpr unsigned kSpinCap = 1u << 16;
#define GJ(g) (((g) >> 3) & 7)
#define GI(g) (((g) & 7) * 4 + ((g) >> 6))

#define D4A 111
#define D5A 94
#define D1 84
#define D2 133
#define D3 66
#define D4 78
#define D5B 75
#define PACE 59
#define PINIT 100
#define PMIN 50
#define DM_ 0
constexpr unsigned X4A = D4A + DM_, X5A = X4A + D5A + DM_, X1 = X5A + D1 + DM_, X2 = X1 + D2 + DM_, X3 = X2 + D3 + DM_, X4 = X3 + D4 + DM_, X5B = X4 + D5B + DM_;
#define TLAND 125
#define XCOST 8
#define WOFF 6
#define BETA 11
constexpr unsigned WOC = 2, WEARLY = 1;
constexpr unsigned NUNIT = 384 + XCOST + 3 * (9 + 2 * WEARLY), PBURST = 30;
constexpr int LEAD = 12, TOFF = 4;
constexpr unsigned DG2 = 12, W3 = 15;
constexpr int GP = 0, GA = 1, GB = 6, GC = 7, HD = 1;
#define VORD 0, 1, 8, 2, 3, 9, 4, 5, 6, 7, 10, 11
constexpr int kVord[12] = {VORD};
constexpr int v_upto(int h) { int n = 0; for (int i = 0; i <= h; ++i) n += kVord[i] < 8 ? 2 : 4; return n; }
constexpr int v_after(int p) { int h = 0; while (kVord[h] != p) ++h; return 32 - v_upto(h); }
constexpr int OML = 2;

constexpr unsigned WS_H1 = 0;
constexpr unsigned WS_H4 = 2u << 20;
constexpr unsigned H4C = 20480, H4T = H4C + 3072;
constexpr unsigned WS_H35 = 4u << 20;
constexpr unsigned WS_OWN = WS_H35 + 80 * 16384;
constexpr unsigned WS_CS = WS_OWN + 32 * 16384;
constexpr unsigned WS_MB = WS_CS + 256 * 768;
constexpr unsigned WS_R = WS_MB + 256 * 128;
constexpr unsigned WS_END = WS_R + 128;
constexpr unsigned WS_Q0 = 8u << 20;
static_assert(WS_END <= (6u << 20), "ws map");
__device__ __forceinline__ unsigned tile35(int j, int q) { return WS_H35 + (unsigned)((j * 10 + (q >> 1)) * 16384 + (q & 1) * 4096); }
__device__ __forceinline__ unsigned h4s(int s) { return (unsigned)((s >> 1) * 16384 + (s & 1) * 4096); }

constexpr int L_WQ = 0;
constexpr int L_WO = 33792;
constexpr int L_GU = L_WO + 30720;
constexpr int L_WD = L_GU + 16384;
constexpr int L_X0 = L_WD + 8192;
constexpr int L_WR = L_X0 + 5120;
constexpr int L_XS = L_WR + 2048;
constexpr int L_Q = L_XS + 1024;
constexpr int L_OP = L_Q + 512;
constexpr int L_M = L_OP + 8192;
constexpr int L_H = L_M + 64;
constexpr int L_ST = L_H + 384;
constexpr int L_CS = L_ST + 64;
constexpr int L_CS2 = L_CS + 1024;
constexpr int L_CNT = L_CS2 + 1536;
constexpr int L_P = L_CNT + 96;
constexpr int L_O2 = L_P + 72 * 192;
constexpr int L_DBG = L_O2 + 3328;
constexpr int L_END = L_DBG + 2048;
static_assert(L_END <= 163840 && L_END > 81920, "LDS");

typedef __attribute__((ext_vector_type(2))) __bf16 bf16x2;
typedef float f32x2 __attribute__((ext_vector_type(2)));
__device__ __forceinline__ unsigned pk2(float a, float b) { return __builtin_bit_cast(unsigned, __builtin_convertvector((f32x2){a, b}, bf16x2)); }
__device__ __forceinline__ unsigned fexp(float x) { const unsigned e = (__float_as_uint(x) >> 23) & 255u; return e == 255u ? 0u : e; }
__device__ __forceinline__ u32x2 enc21(float a, float b, float c) {
    const float lim = 1048575.f;
    const unsigned i = (unsigned)(int)__builtin_rintf(__builtin_amdgcn_fmed3f(a, -lim, lim)) & 0x1fffffu, j = (unsigned)(int)__builtin_rintf(__builtin_amdgcn_fmed3f(b, -lim, lim)) & 0x1fffffu;
    const unsigned k = (unsigned)(int)__builtin_rintf(__builtin_amdgcn_fmed3f(c, -lim, lim)) & 0x1fffffu;
    return (u32x2){i | j << 21, j >> 11 | k << 10 | (__builtin_fabsf(a) + __builtin_fabsf(b) + __builtin_fabsf(c) < 1e30f ? 0u : 0x80000000u)};
}
__device__ __forceinline__ float lo16(unsigned w) { return __uint_as_float(w << 16); }
__device__ __forceinline__ float hi16(unsigned w) { return __uint_as_float(w & 0xffff0000u); }
__device__ __forceinline__ float bfr(float f) { return __uint_as_float(pk2(0.f, f)); }
template <int CTRL> __device__ __forceinline__ float dpp(float x) {
    return __builtin_bit_cast(float, __builtin_amdgcn_mov_dpp(__builtin_bit_cast(int, x), CTRL, 0xf, 0xf, true));
}
template <int CTRL> __device__ __forceinline__ unsigned dppu(unsigned x) { return (unsigned)__builtin_amdgcn_mov_dpp((int)x, CTRL, 0xf, 0xf, true); }
__device__ __forceinline__ float row16_sum(float v) {
    v += dpp<0xB1>(v); v += dpp<0x4E>(v); v += dpp<0x141>(v); v += dpp<0x140>(v); return v;
}
__device__ __forceinline__ float fmax2(float a, float b) { return __builtin_elementwise_maximum(a, b); }
__device__ __forceinline__ float fmax3(float a, float b, float c) { return fmax2(fmax2(a, b), c); }
__device__ __forceinline__ float x16_add(float v) { auto s = __builtin_amdgcn_permlane16_swap(__float_as_uint(v), __float_as_uint(v), false, false); return __uint_as_float(s[0]) + __uint_as_float(s[1]); }
__device__ __forceinline__ float x32_add(float v) { auto s = __builtin_amdgcn_permlane32_swap(__float_as_uint(v), __float_as_uint(v), false, false); return __uint_as_float(s[0]) + __uint_as_float(s[1]); }
__device__ __forceinline__ float wave_sum(float v) { return x32_add(x16_add(row16_sum(v))); }
__device__ __forceinline__ float rs32(float a, float b) { auto s = __builtin_amdgcn_permlane32_swap(__float_as_uint(a), __float_as_uint(b), false, false); return __uint_as_float(s[0]) + __uint_as_float(s[1]); }

#define GUARD(n) asm volatile("s_branch 1f\n\t.p2align 12,," #n "\n1:")
#define MARK(n) asm volatile(".Lcrit_" #n "_%=:" ::)
#define BAR() asm volatile("s_waitcnt lgkmcnt(0)\n\ts_barrier" ::: "memory")
#define VMW(n) asm volatile("s_waitcnt vmcnt(%0)" :: "i"(n) : "memory")

template <int OFF = 0> __device__ __forceinline__ u32x4 ld16_sc1(const void* sbase, unsigned voff) {
    u32x4 r; asm volatile("global_load_dwordx4 %0, %1, %2 offset:%3 sc1" : "=v"(r) : "v"(voff), "s"(sbase), "i"(OFF) : "memory"); return r;
}
__device__ __forceinline__ unsigned ld4_sc1(const void* sbase, unsigned voff) {
    unsigned r; asm volatile("global_load_dword %0, %1, %2 sc1" : "=v"(r) : "v"(voff), "s"(sbase) : "memory"); return r;
}
template <int OFF = 0> __device__ __forceinline__ void st16_sc1(void* sbase, unsigned voff, u32x4 v) {
    asm volatile("global_store_dwordx4 %0, %1, %2 offset:%3 sc1\n\ts_nop 1" :: "v"(voff), "v"(v), "s"(sbase), "i"(OFF) : "memory");
}
__device__ __forceinline__ void st4_sc1(void* sbase, unsigned voff, unsigned v) {
    asm volatile("global_store_dword %0, %1, %2 sc1" :: "v"(voff), "v"(v), "s"(sbase) : "memory");
}
__device__ __forceinline__ void st8(void* sbase, unsigned voff, u32x2 v) {
    asm volatile("global_store_dwordx2 %0, %1, %2" :: "v"(voff), "v"(v), "s"(sbase) : "memory");
}
__device__ __forceinline__ void st2(void* sbase, unsigned voff, unsigned v) {
    asm volatile("global_store_short %0, %1, %2" :: "v"(voff), "v"(v), "s"(sbase) : "memory");
}
constexpr int RK = 64, RV = 160;
template <int R, int IMM> __device__ __forceinline__ void ld16_nt(const void* sbase, unsigned voff) {
    asm volatile("global_load_dwordx4 v[%c3:%c4], %0, %1 offset:%2 nt" :: "v"(voff), "s"(sbase), "i"(IMM), "i"(R), "i"(R + 3) : "memory");
}
template <int R, int IMM> __device__ __forceinline__ void ld8_nt(const void* sbase, unsigned voff) {
    asm volatile("global_load_dwordx2 v[%c3:%c4], %0, %1 offset:%2 nt" :: "v"(voff), "s"(sbase), "i"(IMM), "i"(R), "i"(R + 1) : "memory");
}
template <int R> __device__ __forceinline__ void smf(f32x4& d, u32x4 a, unsigned idx) {
    asm volatile("v_smfmac_f32_16x16x64_bf16 %0, %1, v[%c3:%c4], %2" : "+v"(d) : "v"(a), "v"(idx), "i"(R), "i"(R + 7));
}
template <int A, int B> __device__ __forceinline__ void zip_in() {
    unsigned t;
    asm volatile("v_perm_b32 %0, v%c2, v%c1, %3\n\tv_perm_b32 v%c2, v%c2, v%c1, %4\n\tv_mov_b32 v%c1, %0" : "=&v"(t) : "i"(A), "i"(B), "s"(0x05040100u), "s"(0x07060302u));
}
template <int A, int B> __device__ __forceinline__ void zip_out(unsigned& lo, unsigned& hi) {
    asm volatile("v_perm_b32 %0, v%c3, v%c2, %4\n\tv_perm_b32 %1, v%c3, v%c2, %5" : "=&v"(lo), "=&v"(hi) : "i"(A), "i"(B), "s"(0x05040100u), "s"(0x07060302u));
}
template <int A, int B, int C, int D> __device__ __forceinline__ void zip_x(unsigned lo, unsigned hi) {
    asm volatile("v_perm_b32 v%c4, v%c3, v%c2, %6\n\tv_perm_b32 v%c5, v%c3, v%c2, %7\n\tv_mov_b32 v%c2, %0\n\tv_mov_b32 v%c3, %1"
                 :: "v"(lo), "v"(hi), "i"(A), "i"(B), "i"(C), "i"(D), "s"(0x05040100u), "s"(0x07060302u));
}
__device__ __forceinline__ void dma16(const void* sbase, unsigned voff, unsigned lds_dst) {
    unsigned keep;
    asm volatile("s_mov_b32 %0, m0\n\ts_mov_b32 m0, %3\n\ts_nop 0\n\tglobal_load_lds_dwordx4 %1, %2\n\ts_mov_b32 m0, %0"
                 : "=&s"(keep) : "v"(voff), "s"(sbase), "s"(lds_dst) : "memory");
}
__device__ __forceinline__ unsigned um4(u32x4 a) { return max(max(a[0], a[1]), max(a[2], a[3])); }
__device__ __forceinline__ bool all_set(unsigned t) { return __builtin_amdgcn_ballot_w64(t == 0u) == 0ull; }
__device__ __forceinline__ bool any_empty(unsigned mx) { return __builtin_amdgcn_ballot_w64(mx == ~0u) != 0ull; }
__device__ __forceinline__ unsigned long long any_nan(float a, float b) { return __builtin_amdgcn_ballot_w64(__builtin_isunordered(a, b)); }
__device__ __forceinline__ f32x4 asf(u32x4 a) { return __builtin_bit_cast(f32x4, a); }
__device__ __forceinline__ u32x4 asu(f32x4 a) { return __builtin_bit_cast(u32x4, a); }
#define LANES(m) __builtin_amdgcn_inverse_ballot_w64(m)

__device__ __forceinline__ bf16x4 tr64(unsigned lds_addr) { return __builtin_amdgcn_ds_read_tr16_b64_v4bf16((LAS bf16x4*)(size_t)lds_addr); }
__device__ __forceinline__ bf16x4 tr64o(unsigned lds_addr, int off) { return __builtin_amdgcn_ds_read_tr16_b64_v4bf16((LAS bf16x4*)((LAS char*)(size_t)lds_addr + off)); }
__device__ __forceinline__ bf16x8 cat8(bf16x4 a, bf16x4 b) { return __builtin_shufflevector(a, b, 0, 1, 2, 3, 4, 5, 6, 7); }
__device__ __forceinline__ bf16x8 lds16(unsigned a) { return *(const LAS bf16x8*)(size_t)a; }
__device__ __forceinline__ bf16x4 lds8(unsigned a) { return *(const LAS bf16x4*)(size_t)a; }
__device__ __forceinline__ f32x4 mfma32(bf16x8 a, bf16x8 b, f32x4 c) { return __builtin_amdgcn_mfma_f32_16x16x32_bf16(a, b, c, 0, 0, 0); }
__device__ __forceinline__ f32x4 mfma32u(u32x4 a, u32x4 b, f32x4 c) { return mfma32(__builtin_bit_cast(bf16x8, a), __builtin_bit_cast(bf16x8, b), c); }
__device__ __forceinline__ f32x4 mfma16(bf16x4 a, bf16x4 b, f32x4 c) {
    return __builtin_amdgcn_mfma_f32_16x16x16bf16_1k(__builtin_bit_cast(s16x4, a), __builtin_bit_cast(s16x4, b), c, 0, 0, 0);
}

#define STAMP(i) do {} while (0)

template <int OFF = 0> __device__ __forceinline__ void st16m(void* sbase, unsigned voff, u32x4 v, unsigned long long mask) {
    asm volatile("s_mov_b64 exec, %4\n\tglobal_store_dwordx4 %0, %1, %2 offset:%3 sc1\n\ts_mov_b64 exec, -1\n\ts_nop 0" :: "v"(voff), "v"(v), "s"(sbase), "i"(OFF), "s"(mask) : "memory");
}
template <int OFF> __device__ __forceinline__ void st16m2(void* sbase, unsigned voff, u32x4 v, unsigned voff2, u32x4 v2, unsigned long long mask) {
    asm volatile("s_mov_b64 exec, %6\n\tglobal_store_dwordx4 %0, %1, %2 sc1\n\tglobal_store_dwordx4 %4, %5, %2 offset:%3 sc1\n\ts_mov_b64 exec, -1\n\ts_nop 0"
                 :: "v"(voff), "v"(v), "s"(sbase), "i"(OFF), "v"(voff2), "v"(v2), "s"(mask) : "memory");
}
#define PINS(a) asm volatile("" : "+s"(a))
#define RETRY(LOAD, EMPTY, COMPUTE, NAN_, X) do { LOAD; COMPUTE; if (__builtin_expect((NAN_) != 0ull, 0)) { \
    _Pragma("unroll 1") for (unsigned spins_ = 0; spins_ < kSpinCap; ++spins_) { slip(X); LOAD; if (!any_empty(EMPTY)) break; } COMPUTE; } } while (0)
__device__ __forceinline__ unsigned now() { return (unsigned)__builtin_amdgcn_s_memrealtime(); }
__device__ __forceinline__ void until(unsigned t) {
    asm volatile("1:\n\ts_memrealtime vcc\n\ts_waitcnt lgkmcnt(0)\n\ts_sub_u32 vcc_lo, %0, vcc_lo\n\ts_cmp_lt_u32 vcc_lo, 0x400\n\ts_cbranch_scc1 1b" :: "s"(t - 1u) : "vcc", "scc", "memory");
}
__device__ __forceinline__ unsigned lds_word(unsigned a) {
    unsigned t_; asm volatile("ds_read_b32 %0, %1\n\ts_waitcnt lgkmcnt(0)\n\ts_nop 0" : "=v"(t_) : "v"(a) : "memory");
    return __builtin_amdgcn_readfirstlane(t_);
}
__device__ __forceinline__ void lds_set(unsigned a, unsigned v) { asm volatile("ds_write_b32 %0, %1" :: "v"(a), "v"(v) : "memory"); }
__device__ __forceinline__ int xrow(int lane) { return (lane >> 5) * 2 + ((lane >> 3) & 1); }
__device__ __forceinline__ int xcol(int lane) { return ((lane >> 4) & 1) * 8 + (lane & 7); }
__device__ __forceinline__ int swz(int r) { return r ^ (((r >> 3) & 1) << 2); }
__device__ __forceinline__ f32x2 unpk(unsigned w) { return (f32x2){lo16(w), hi16(w)}; }
__device__ __forceinline__ unsigned pk2v(f32x2 v) { return __builtin_bit_cast(unsigned, __builtin_convertvector(v, bf16x2)); }
__device__ __forceinline__ float frcp(float x) { return __builtin_amdgcn_rcpf(x); }
__device__ __forceinline__ float ex2(float x) { return __builtin_amdgcn_exp2f(x); }
__device__ __forceinline__ int lane_id() { int l; asm volatile("v_mbcnt_lo_u32_b32 %0, -1, 0\n\tv_mbcnt_hi_u32_b32 %0, -1, %0" : "=v"(l)); return l & 63; }
__device__ __forceinline__ f32x2 ln_stats(float ss, float s) {
    const float mean = s * (1.f / DM);
    const float var = fmax2(__builtin_fmaf(-mean, mean, ss * (1.f / DM)), 0.f);
    return (f32x2){bfr(mean), frcp(bfr(__builtin_amdgcn_sqrtf(bfr(bfr(var) + 1e-5f))))};
}
#define GATHER8(a, o, S) do { a[0] = ld16_sc1<-4 * (S)>(ws, o); a[1] = ld16_sc1<-3 * (S)>(ws, o); a[2] = ld16_sc1<-2 * (S)>(ws, o); a[3] = ld16_sc1<-(S)>(ws, o); \
    a[4] = ld16_sc1<0>(ws, o); a[5] = ld16_sc1<(S)>(ws, o); a[6] = ld16_sc1<2 * (S)>(ws, o); a[7] = ld16_sc1<3 * (S)>(ws, o); } while (0)
template <int OFF> __device__ __forceinline__ void again16(u32x4& r, const void* sbase, unsigned voff) {
    asm volatile("v_cmp_eq_u32 vcc, -1, %1\n\ts_mov_b64 exec, vcc\n\tglobal_load_dwordx4 %0, %2, %3 offset:%4 sc1\n\ts_mov_b64 exec, -1" : "+v"(r) : "v"(um4(r)), "v"(voff), "s"(sbase), "i"(OFF) : "memory", "vcc");
}
#define WAIT8(a, N) asm volatile("s_waitcnt vmcnt(%8)" : "+v"((a)[0]), "+v"((a)[1]), "+v"((a)[2]), "+v"((a)[3]), "+v"((a)[4]), "+v"((a)[5]), "+v"((a)[6]), "+v"((a)[7]) : "i"(N) : "memory")
#define SUM8(a) (((((((asf(a[0]) + asf(a[1])) + asf(a[2])) + asf(a[3])) + asf(a[4])) + asf(a[5])) + asf(a[6])) + asf(a[7]))
#define WAIT1(x, N) asm volatile("s_waitcnt vmcnt(%1)" : "+v"(x) : "i"(N) : "memory")
#define LO2(x) __builtin_shufflevector(x, x, 0, 1)
#define HI2(x) __builtin_shufflevector(x, x, 2, 3)
#define PIN1(a) asm volatile("" : "+v"(a))
#define PIN2(a, b) asm volatile("" : "+v"(a), "+v"(b))
#define PIN3(a, b, c) asm volatile("" : "+v"(a), "+v"(b), "+v"(c))
#define LANE_VARS() const int lane = lane_id(); \
    const int kq = lane >> 4, n = lane & 15, q4 = n >> 2, p4 = lane & 3; \
    const int r0s = 8 * kq + q4 + 4 * (kq & 1), r1s = 8 * kq + q4 + 4 * ((kq & 1) ^ 1);     \
    const int r2s = swz(4 * kq + q4);                                                       \
    (void)r0s; (void)r1s; (void)r2s; (void)p4
template <int V> struct IC { static constexpr int value = V; };
struct Frag80 { bf16x8 a0, a1; bf16x4 a2; };

__global__ void __launch_bounds__(512, 2) __attribute__((amdgpu_num_vgpr(32)))
mega_kernel(const u16*, const u16*, const u16*, const u16*, const u16*, const u16*, const u16*, const u16*, const u16*, u16*, unsigned char*
            ) {
    extern __shared__ __attribute__((aligned(16))) unsigned char smem[];
    const unsigned lb = (unsigned)(size_t)smem;
    const int wave = __builtin_amdgcn_readfirstlane(threadIdx.x >> 6);
    u64x8 ar0; u64x2 ar1; unsigned long long ar2, tp, tst64;
    asm volatile("s_load_dwordx16 %0, %5, 0\n\ts_load_dwordx4 %1, %5, 0x40\n\ts_load_dwordx2 %2, %5, 0x50\n\ts_load_dwordx2 %3, %5, %6\n\ts_memrealtime %4"
                 : "=&s"(ar0), "=&s"(ar1), "=&s"(ar2), "=&s"(tp), "=&s"(tst64) : "s"(__builtin_amdgcn_kernarg_segment_ptr()), "s"((0xa9876421u >> (4 * wave) & 15u) * 8u) : "memory");
    const int g = blockIdx.x, gj = GJ(g), gi = GI(g);
    const f32x4 z4 = {0.f, 0.f, 0.f, 0.f};
    asm volatile("" ::: "v255");
    auto when = [&](unsigned X) __attribute__((always_inline)) {
        u32x2 v;
        asm volatile("ds_read2_b32 %0, %1 offset0:14 offset1:5\n\ts_waitcnt lgkmcnt(0)" : "=v"(v) : "v"(lb + L_CNT) : "memory");
        return __builtin_amdgcn_readfirstlane(v[0]) + (X * __builtin_amdgcn_readfirstlane(v[1]) >> 12);
    };
    auto sched = [&](unsigned X, unsigned add) __attribute__((always_inline)) { until(when(X) + add); };
    auto slip = [&](unsigned X) __attribute__((always_inline)) {
        if (X) lds_set(lb + L_CNT + 56, now() - (X * lds_word(lb + L_CNT + 20) >> 12));
    };
    float xe = 0.f;

    auto code_pf = [&](int slice, int lane) __attribute__((always_inline)) {
        unsigned long long pc0; unsigned len, back, t;
        asm volatile("s_getpc_b64 %0\n.Lmega_pf%=:\n\ts_mov_b32 %1, .Lmega_code_end-.Lmega_code_begin\n\ts_mov_b32 %2, .Lmega_pf%=-.Lmega_code_begin" : "=s"(pc0), "=s"(len), "=s"(back));
        unsigned off = (unsigned)(slice * 4096 + lane * 64); if (off > len) off = len;
        asm volatile("global_load_dword %0, %1, %2" : "=v"(t) : "v"(off & ~3u), "s"(pc0 - back) : "memory");
        return t;
    };
    asm volatile(".Lmega_code_begin:");
    unsigned pf = 0u, pt1;
    const unsigned wofs = (unsigned)((((gj >> 1) * HQ + gi) * SK + (SK - WIN) + (gj & 1) * 512) * (DH * 2));
    {
        const int lane = threadIdx.x & 63, hi = lane >= 26;
        if (threadIdx.x < 24) lds_set(lb + L_CNT + threadIdx.x * 4, 0u);
        unsigned t2 = (unsigned)(gi * (DH * DM * 2)), t7 = (32u << 20) + (unsigned)(g * 128), to = (unsigned)(gj * 128), w2 = wofs;
        PINS(t2); PINS(t7); PINS(to); PINS(w2);
        to = wave == 7 ? t7 : to; to = wave == 2 ? t2 : to; to = wave < 2 ? w2 : to;
        if (wave == 7) pf = code_pf((g >> 3) & 15, lane);
        unsigned vo = (unsigned)((gj * 80 + swz(2 * wave) + hi) * (QD * 2) + gi * (DH * 2) + (lane - 26 * hi) * 16), d = lb + L_WQ + wave * 832, keep;
        unsigned xo = (0x11043210u >> (4 * wave) & 15u) * 1024 + (unsigned)(lane & (wave < 6 ? 63 : 15)) * 16, xd = lb + L_X0 + min(wave, 6) * 1024;
        PIN2(vo, xo); PINS(d); PINS(xd); PINS(to);
        __builtin_amdgcn_sched_barrier(0);
        asm volatile("s_waitcnt lgkmcnt(0)\n\ts_load_dword %0, %4, %6" : "=&s"(pt1), "+s"(ar0), "+s"(ar1), "+s"(ar2), "+s"(tp), "+s"(tst64) : "s"(to) : "memory");
        dma16((const void*)(wave < 5 ? ar0[0] : ar0[5]), xo, xd);
        asm volatile("s_mov_b32 %0, m0\n\ts_mov_b32 m0, %2\n\ts_mov_b64 exec, %4\n\tglobal_load_lds_dwordx4 %1, %3 nt\n\t"
                     ".rept 4\n\ts_add_u32 m0, m0, 6656\n\tv_add_u32 %1, 0x30000, %1\n\tglobal_load_lds_dwordx4 %1, %3 nt\n\t.endr\n\t"
                     "s_mov_b64 exec, -1\n\ts_mov_b32 m0, %0\n\ts_barrier"
                     : "=&s"(keep), "+v"(vo) : "s"(d), "s"(ar0[3]), "s"(0x0003fffffcffffffull) : "memory", "scc");
        __builtin_amdgcn_sched_barrier(0);
    }
    const u16 *const kc = (const u16*)ar0[1], *const vc = (const u16*)ar0[2], *const wo = (const u16*)ar0[4], *const wg = (const u16*)ar0[6], *const wu = (const u16*)ar0[7], *const wd = (const u16*)ar1[0];
    u16* const out = (u16*)ar1[1];
    unsigned char* const ws = (unsigned char*)((ar2 + (32u << 20) - 1) & ~(unsigned long long)((16u << 20) - 1));
    const unsigned tst = (unsigned)tst64;
    auto fix_k1 = [&](auto IC_, auto NC) __attribute__((always_inline)) {
        constexpr int A = RK + 8 * decltype(IC_)::value, B = A + 4;
        unsigned t0, t1, t2, t3;
        asm volatile("s_waitcnt vmcnt(%12)\n\tv_mov_b32 %0, v%c4\n\tv_mov_b32 %1, v%c5\n\tv_mov_b32 %2, v%c6\n\tv_mov_b32 %3, v%c7\n\t"
                     "v_mov_b32_dpp v%c4, v%c8 row_ror:8 row_mask:0xf bank_mask:0xc\n\tv_mov_b32_dpp v%c5, v%c9 row_ror:8 row_mask:0xf bank_mask:0xc\n\t"
                     "v_mov_b32_dpp v%c6, v%c10 row_ror:8 row_mask:0xf bank_mask:0xc\n\tv_mov_b32_dpp v%c7, v%c11 row_ror:8 row_mask:0xf bank_mask:0xc\n\t"
                     "v_mov_b32_dpp v%c8, %0 row_ror:8 row_mask:0xf bank_mask:0x3\n\tv_mov_b32_dpp v%c9, %1 row_ror:8 row_mask:0xf bank_mask:0x3\n\t"
                     "v_mov_b32_dpp v%c10, %2 row_ror:8 row_mask:0xf bank_mask:0x3\n\tv_mov_b32_dpp v%c11, %3 row_ror:8 row_mask:0xf bank_mask:0x3"
                     : "=&v"(t0), "=&v"(t1), "=&v"(t2), "=&v"(t3)
                     : "i"(A), "i"(A + 1), "i"(A + 2), "i"(A + 3), "i"(B), "i"(B + 1), "i"(B + 2), "i"(B + 3), "i"(decltype(NC)::value) : "memory");
    };
    auto trans_v = [&](auto UC) __attribute__((always_inline)) {
        constexpr int R = RV + 4 * decltype(UC)::value, P = 4 * decltype(UC)::value;
#define CELL(r, c) R + 16 * (r) + (c), R + 16 * (r) + 8 + (c)
        unsigned a0, a1, b0, b1, c0, c1;
        VMW(v_after(P)); zip_in<CELL(0, 0)>(); zip_out<CELL(0, 1)>(a0, a1); zip_out<CELL(0, 2)>(b0, b1); zip_out<CELL(0, 3)>(c0, c1);
        VMW(v_after(P + 1)); zip_x<CELL(1, 0), CELL(0, 1)>(a0, a1); zip_in<CELL(1, 1)>(); zip_out<CELL(1, 2)>(a0, a1);
        VMW(v_after(P + 2)); zip_x<CELL(2, 0), CELL(0, 2)>(b0, b1); zip_x<CELL(2, 1), CELL(1, 2)>(a0, a1); zip_in<CELL(2, 2)>(); zip_out<CELL(1, 3)>(a0, a1); zip_out<CELL(2, 3)>(b0, b1);
        VMW(v_after(P + 3)); zip_x<CELL(3, 0), CELL(0, 3)>(c0, c1); zip_x<CELL(3, 1), CELL(1, 3)>(a0, a1); zip_x<CELL(3, 2), CELL(2, 3)>(b0, b1); zip_in<CELL(3, 3)>();
#undef CELL
    };
    auto trans_w = [&](auto UC) __attribute__((always_inline)) {
        constexpr int R = RV + 64 + 2 * decltype(UC)::value, N = v_after(8 + decltype(UC)::value);
        unsigned a0, a1;
        VMW(N + 2); zip_in<R, R + 8>(); zip_out<R + 1, R + 9>(a0, a1);
        VMW(N); zip_x<R + 16, R + 24, R + 1, R + 9>(a0, a1); zip_in<R + 17, R + 25>();
    };
    auto frag80 = [&](unsigned im, int rs, int r0s, int r1s, int r2s) __attribute__((always_inline)) -> Frag80 {
        return Frag80{cat8(tr64(im + r0s * rs), tr64(im + r1s * rs)), cat8(tr64(im + (r0s + 32) * rs), tr64(im + (r1s + 32) * rs)), tr64(im + (r2s + 64) * rs)};
    };
    auto rows80 = [&](unsigned a16, unsigned a8) __attribute__((always_inline)) -> Frag80 { const Frag80 f{lds16(a16), lds16(a16 + 64), lds8(a8)}; __builtin_amdgcn_sched_barrier(0); return f; };
    auto empty5 = [&](int q0, unsigned S, int lane) __attribute__((always_inline)) {
        st16m(ws, tile35(gj, q0 + (lane >> 3)) + S + (unsigned)(gi * 128 + (lane & 7) * 16), (u32x4){~0u, ~0u, ~0u, ~0u}, 0xffffffffffull);
    };
    auto empty4 = [&](int layer, unsigned S, int lane) __attribute__((always_inline)) {
        const unsigned b = WS_H4 + (unsigned)((layer * 32 + gi) * 32768) + (S ^ 8192u);
        st16_sc1(ws, b + (unsigned)((lane >> 5) * 4096 + gj * 512 + (lane & 31) * 16), (u32x4){~0u, ~0u, ~0u, ~0u});
        st16m(ws, b + (unsigned)(lane < 32 ? 16384 + gj * 512 + lane * 16 : H4C + ((lane - 32) >> 3) * 1024 + gj * 128 + (lane & 7) * 16), (u32x4){~0u, ~0u, ~0u, ~0u},
              layer ? 0xff000000ffffffffull : ~0ull);
    };
    int lead = wave == 7 ? LEAD * PACE * 4 : 0;
    auto pace = [&](unsigned n, bool wait = true) __attribute__((always_inline)) {
        unsigned u, w, t0;
        asm volatile("s_mov_b64 exec, 1\n\tds_add_rtn_u32 %0, %3, %4\n\tds_max_u32 %3, %5 offset:12\n\tds_add_rtn_u32 %1, %3, %6 offset:12\n\tds_read_b32 %2, %3 offset:4\n\ts_mov_b64 exec, -1\n\ts_waitcnt lgkmcnt(0)"
                     : "=&v"(u), "=&v"(w), "=&v"(t0) : "v"(lb + L_CNT + 60), "v"(n), "v"((now() - tst) << 6), "v"(n * PMIN * 4u) : "memory");
        int tn = (int)(__builtin_amdgcn_readfirstlane(t0) - tst + PINIT) * 64 + __builtin_amdgcn_readfirstlane(u) * (PACE * 4);
        const int tw = __builtin_amdgcn_readfirstlane(w) - PBURST * 64;
        if (tw > tn) tn = tw;
        tn -= lead;
        if (wait) {
#pragma unroll 1
            for (int k = 0; k < 2048 && (int)((now() - tst) << 6) < tn; ++k) {}
        }
    };
    auto slot = [&](unsigned u) __attribute__((always_inline)) {
        const unsigned tn = lds_word(lb + L_CNT + 64) + PINIT + u * PACE / 16;
#pragma unroll 1
        for (int k = 0; k < 8192 && (int)(now() - tn) < 0; ++k) __builtin_amdgcn_s_sleep(1);
    };
    auto load_wo = [&](int k, int lane, bool early) __attribute__((always_inline)) {
        const int c = k * 64 + lane, pp = c / 10, cc = c - 10 * pp;
        const unsigned f = (unsigned)(swz(pp) * 1280 + cc * 16);
        const u16* wb = wo + (size_t)gi * DH * DM + gj * 80;
#pragma unroll
        for (int a2 = 0; a2 < 6; ++a2) { if (a2 % 3 == 0) { if (early) pace(3); else slot(NUNIT - WOFF + (2 * k + a2 / 3) * WOC); } dma16(wb + 32 * a2 * DM, f, lb + L_WO + (5 * a2 + k) * 1024); }
    };
    auto load_wd = [&](int lane) __attribute__((always_inline)) {
        const u16* db = wd + gi * 48 * DM + gj * 80;
#pragma unroll 1
        for (int k = 0; k < 5; ++k) {
            const int c = k * 64 + lane, pp = c / 10, cc = c - 10 * pp;
            const unsigned fd = (unsigned)(swz(pp) * 1280 + cc * 16);
            if (!(k & 1)) pace(3);
            dma16(db, fd, lb + L_WD + k * 1024);
            if (k < 3) dma16(db + 32 * DM, (k < 2 || lane < 32) ? fd : 0u, lb + L_WD + (5 + k) * 1024);
        }
    };
    auto load_gu = [&](int m, int lane) __attribute__((always_inline)) {
#pragma unroll 1
        for (int k0 = 0; k0 < 3; ++k0) {
            const int k = (k0 + (g & 7)) % 3, c = k * 64 + lane, row = c / 6, cc = c - 6 * row;
            const unsigned o = (unsigned)((swz(row) * FF + cc * 8) * 2), d = lb + L_GU + m * 8192 + k * 1024;
            const u16* wb = (m == 0 ? wg : wu) + gj * 80 * FF + gi * 48;
            pace(3);
            dma16(wb, o, d); dma16(wb + 32 * FF, o, d + 3072);
            if (k < 2) dma16(wb + 64 * FF, (k == 0 || lane < 32) ? o : 0u, d + 6144);
        }
    };
    auto lds_add = [&](int off, unsigned v) __attribute__((always_inline)) { asm volatile("s_mov_b64 exec, 1\n\tds_add_u32 %0, %1\n\ts_mov_b64 exec, -1" :: "v"(lb + L_CNT + off), "v"(v) : "memory"); };
    auto send_flag = [&](int lane) __attribute__((always_inline)) {
        st4_sc1(ws, WS_MB + (unsigned)((gi * 8 + ((gj == (gi & 7) ? lane : gi) & 7)) * 128 + gj * 4), lds_word(lb + L_CNT + 68));
    };
    auto tiles4 = [&](const Frag80 (&f)[4], const Frag80& b, unsigned long long mk) __attribute__((always_inline)) -> u32x4 {
        f32x4 c[4], d[4];
#pragma unroll
        for (int i = 0; i < 4; ++i) c[i] = mfma32(f[i].a0, b.a0, z4);
#pragma unroll
        for (int i = 0; i < 4; ++i) { c[i] = mfma32(f[i].a1, b.a1, c[i]); d[i] = mfma16(f[i].a2, b.a2, z4); }
        __builtin_amdgcn_sched_barrier(0);
        f32x2 lo, hi;
        asm volatile("s_nop 1\n\ts_mov_b64 exec, %18\n\tv_pk_add_f32 %0, %2, %10\n\tv_pk_add_f32 %1, %3, %11\n\t"
                     "s_lshl_b64 exec, exec, 1\n\tv_pk_add_f32 %0, %4, %12\n\tv_pk_add_f32 %1, %5, %13\n\t"
                     "s_lshl_b64 exec, exec, 1\n\tv_pk_add_f32 %0, %6, %14\n\tv_pk_add_f32 %1, %7, %15\n\t"
                     "s_lshl_b64 exec, exec, 1\n\tv_pk_add_f32 %0, %8, %16\n\tv_pk_add_f32 %1, %9, %17\n\ts_mov_b64 exec, -1"
                     : "=&v"(lo), "=&v"(hi) : "v"(LO2(c[0])), "v"(HI2(c[0])), "v"(LO2(c[1])), "v"(HI2(c[1])), "v"(LO2(c[2])), "v"(HI2(c[2])), "v"(LO2(c[3])), "v"(HI2(c[3])),
                       "v"(LO2(d[0])), "v"(HI2(d[0])), "v"(LO2(d[1])), "v"(HI2(d[1])), "v"(LO2(d[2])), "v"(HI2(d[2])), "v"(LO2(d[3])), "v"(HI2(d[3])), "s"(mk) : "scc");
        return asu(__builtin_shufflevector(lo, hi, 0, 1, 2, 3));
    };
    auto empty1 = [&](int half, unsigned S, int lane) __attribute__((always_inline)) {
        const unsigned o = WS_H1 + S + (unsigned)((gi * 4 + 2 * half) * 16384 + gj * 896 + min(lane, 55) * 16);
        st16_sc1(ws, o, (u32x4){~0u, ~0u, ~0u, ~0u}); st16_sc1(ws, o + 16384, (u32x4){~0u, ~0u, ~0u, ~0u});
    };
    auto report = [&]() __attribute__((always_inline)) { if (gj == (gi & 7)) st4_sc1(ws, WS_R + (unsigned)(gi * 4), lds_word(lb + L_CNT + 88)); };
    auto answer = [&](int lane, unsigned r) __attribute__((always_inline)) {
        int d = min(max((int)(r - (tst << 11)) >> 11, -400), 600) * 512 + (int)((r >> 1) & 15u);
        unsigned k = (r >> 5) & 63u;
        d += (int)dppu<0xB1>((unsigned)d); d += (int)dppu<0x4E>((unsigned)d); d += (int)dppu<0x141>((unsigned)d); d += (int)dppu<0x140>((unsigned)d);
        k = max(k, dppu<0xB1>(k)); k = max(k, dppu<0x4E>(k)); k = max(k, dppu<0x141>(k)); k = max(k, dppu<0x140>(k));
        d = __builtin_amdgcn_readlane(d, 0) + __builtin_amdgcn_readlane(d, 16);
        k = max((unsigned)__builtin_amdgcn_readlane(k, 0), (unsigned)__builtin_amdgcn_readlane(k, 16));
        st4_sc1(ws, WS_MB + (unsigned)(((lane & 31) * 8 + (lane & 7)) * 128 + 32), (tst + (d >> 14) + TOFF) << 8 | k << 2 | ((d & 511) > 128 ? 3u : 1u));
        lds_set(lb + L_CNT + 36, 1u);
    };
    auto adopt = [&](unsigned t) __attribute__((always_inline)) {
        const int dt = (int)(t - (tst << 8)) >> 8;
        if (dt > -256 && dt < 512) lds_set(lb + L_CNT + 64, tst + dt);
        lds_set(lb + L_CNT + 20, 4096u + ((t >> 2) & 63u) * BETA);
        lds_set(lb + L_CNT + 80, lds_word(lb + L_CNT + 92) ^ (t & 2u) << 12);
        lds_set(lb + L_CNT + 84, (((t << 1) ^ lds_word(lb + L_CNT + 68)) & 4u) << 11);
        lds_set(lb + L_CNT + 52, 1u);
    };
    struct StatAddr { unsigned a16, b16, a8, b8, dst; };
    auto stats_addr = [&](int lane, unsigned dbase, int dstride) __attribute__((always_inline)) -> StatAddr {
        const int kq = lane >> 4, q4 = (lane >> 2) & 3, p4 = lane & 3;
        const int x1 = LANES(0x4444444444444444ull) ? L_WR + gj * 160 : L_XS + q4 * 160, xa = LANES(0xaaaaaaaaaaaaaaaaull) ? L_XS + 640 : x1;
        StatAddr r{lb + (unsigned)(xa + kq * 16), lb + (unsigned)(L_XS + p4 * 160 + kq * 16), lb + (unsigned)(xa + 128 + kq * 8), lb + (unsigned)(L_XS + p4 * 160 + 128 + kq * 8), dbase + (unsigned)(kq * dstride)};
        PIN3(r.a16, r.b16, r.dst); PIN2(r.a8, r.b8);
        return r;
    };
    auto stats = [&](const StatAddr& r) __attribute__((always_inline)) {
        const bf16x8 a0 = lds16(r.a16), b0 = lds16(r.b16), a1 = lds16(r.a16 + 64), b1 = lds16(r.b16 + 64);
        const bf16x4 a2 = lds8(r.a8), b2 = lds8(r.b8);
        unsigned long long mk = 0x0008000400020001ull; PINS(mk);
        __builtin_amdgcn_sched_barrier(0);
        st16m(ws, r.dst, asu(mfma32(a1, b1, mfma32(a0, b0, z4)) + mfma16(a2, b2, z4)), mk);
    };
    unsigned qa, pw, ma, idx, qm;
    auto qk_addr = [&](bool L1) __attribute__((always_inline)) {
        LANE_VARS();
        qa = lb + L_Q + (unsigned)(kq * 32 + (lane & 2) * 8);
        pw = lb + L_P + (unsigned)((7 + 8 * wave) * 192 + (kq >> 1) * 64 + (n >> 3) * 32 + (lane & 2) * 8 + (q4 & 1) * 8 + (kq & 1) * 4 + (lane & 1) * 2);
        ma = lb + L_M + (unsigned)(wave * 4);
        idx = (lane & 2) ? 0xeeeeu : 0x4444u;
        qm = lb + L_Q;
        PIN3(qa, pw, ma); PIN2(idx, qm);
        if (L1 && wave < 4) __builtin_amdgcn_s_setprio(2);
    };
    auto att_qk0 = [&]() __attribute__((always_inline)) {
        qk_addr(false);
#pragma unroll 1
        for (unsigned spins = 0; spins < (kSpinCap << 4); ++spins) {
            if (lds_word(lb + L_CNT + 4) != 0u) break;
            __builtin_amdgcn_s_sleep(1);
        }
        STAMP(3);
        const u32x4 q0 = *(const LAS u32x4*)(size_t)qa, q1 = *(const LAS u32x4*)(size_t)(qa + 128), q2 = *(const LAS u32x4*)(size_t)(qa + 256);
        f32x4 c0 = z4, c1 = z4, c2 = z4, c3 = z4;
#define SMF(c, q, r) "v_smfmac_f32_16x16x64_bf16 %" #c ", %" #q ", v[" #r "], %7\n\t"
        asm volatile("s_nop 1\n\t" SMF(0, 4, 64:71) SMF(1, 4, 88:95) SMF(2, 4, 112:119) SMF(3, 4, 136:143) SMF(0, 5, 72:79) SMF(1, 5, 96:103) SMF(2, 5, 120:127) SMF(3, 5, 144:151)
                     SMF(0, 6, 80:87) SMF(1, 6, 104:111) SMF(2, 6, 128:135) SMF(3, 6, 152:159) "s_nop 7"
                     : "+v"(c0), "+v"(c1), "+v"(c2), "+v"(c3) : "v"(q0), "v"(q1), "v"(q2), "v"(idx));
#undef SMF
        const float s0 = c0[0] + c0[2], s1 = c1[0] + c1[2], s2 = c2[0] + c2[2], s3 = c3[0] + c3[2];
        float m = fmax2(fmax3(s0, s1, s2), s3);
        m = fmax2(m, dpp<0xB1>(m)); m = fmax2(m, dpp<0x4E>(m)); m = fmax2(m, dpp<0x124>(m)); m = fmax2(m, dpp<0x128>(m));
        const float s01 = LANES(0xffff0000ffff0000ull) ? s1 : s0, s23 = LANES(0xffff0000ffff0000ull) ? s3 : s2;
        const float sv = LANES(0xffffffff00000000ull) ? s23 : s01;
        m = bfr(bfr(m) * SCALE);
        const float p = ex2((bfr(bfr(sv) * SCALE) - m) * LOG2E);
        *(LAS u16*)(size_t)pw = (u16)pk2(p, p);
        *(LAS float*)(size_t)ma = m;
        __builtin_amdgcn_sched_barrier(0);
    };
    unsigned pr, oa, oc, cma, cxa, cra, cso;
    auto att_addr = [&]() __attribute__((always_inline)) {
        LANE_VARS();
        pr = lb + L_P + (unsigned)((7 + 8 * wave - 2 * q4 - (lane & 1)) * 192 + kq * 32 + (lane & 2) * 8);
        oa = lb + L_OP + (unsigned)(wave * 1024 + (lane & 7) * 64 + kq * 16 + (n >> 3) * 8);
        oc = lb + L_OP + (unsigned)(wave * 1024 + 512 + (kq >> 1) * 256 + (lane & 7) * 32 + (kq & 1) * 16 + (n >> 3) * 8);
        PIN3(pr, oa, oc);
    };
    auto comb_out = [&](float u0, float u1, unsigned so, unsigned long long mk, bool both) __attribute__((always_inline)) {
        const unsigned w = min(pk2(u0, u1), 0xfffffffeu);
        if (!both) { st4_sc1(ws, so, w); return; }
        const u32x4 w4 = {dppu<0x00>(w), dppu<0x55>(w), dppu<0xAA>(w), dppu<0xFF>(w)};
        st16m(ws, so, w4, mk);
    };
    auto max8 = [&](f32x4 m0, f32x4 m1) __attribute__((always_inline)) { return fmax3(fmax3(m0[0], m0[1], m0[2]), fmax3(m0[3], m1[0], m1[1]), fmax2(m1[2], m1[3])) * LOG2E; };
    auto comb_o0 = [&](f32x4 m0, f32x4 m1, f32x4 mm, f32x4 o0, f32x4 o1, f32x4 o2, f32x4 o3, unsigned so, unsigned long long mk, bool both) __attribute__((always_inline)) {
        const float M = max8(m0, m1);
        f32x4 f;
#pragma unroll
        for (int k = 0; k < 4; ++k) f[k] = ex2(__builtin_fmaf(mm[k], LOG2E, -M));
        const f32x4 o = f[3] * o3 + (f[2] * o2 + (f[1] * o1 + f[0] * o0));
        comb_out(rs32(o[0], o[2]), rs32(o[1], o[3]), so, mk, both);
    };
    auto comb_o1 = [&](f32x4 m0, f32x4 m1, f32x2 mm, f32x4 o0, f32x4 o1, unsigned so, unsigned long long mk, bool both) __attribute__((always_inline)) {
        const float M = max8(m0, m1);
        const f32x4 o = ex2(__builtin_fmaf(mm[1], LOG2E, -M)) * o1 + ex2(__builtin_fmaf(mm[0], LOG2E, -M)) * o0;
        comb_out(x16_add(rs32(o[0], o[2])), x16_add(rs32(o[1], o[3])), so, mk, both);
    };
    auto ml_addr = [&](int lane, unsigned& xa, unsigned& ra) __attribute__((always_inline)) {
        xa = lb + L_M + (unsigned)(((lane & 15) >> 1) * 4); ra = lb + L_P + (unsigned)((7 + 8 * ((lane & 15) >> 1)) * 192 + (lane & 1) * 64 + (lane >> 4) * 16);
    };
    auto comb_ml = [&](unsigned ma, unsigned xa, unsigned ra, unsigned so, bool both) __attribute__((always_inline)) {
        const u32x4 x4 = *(const LAS u32x4*)(size_t)ra;
        const f32x4 m0 = *(const LAS f32x4*)(size_t)ma, m1 = *(const LAS f32x4*)(size_t)(ma + 16);
        const float mw = *(const LAS float*)(size_t)xa;
        __builtin_amdgcn_sched_barrier(0);
        const f32x4 sp = mfma32u((u32x4){0x3f803f80u, 0x3f803f80u, 0x3f803f80u, 0x3f803f80u}, x4, z4);
        __builtin_amdgcn_sched_barrier(0);
        const float M = max8(m0, m1);
        float L = row16_sum(sp[0] * ex2(__builtin_fmaf(mw, LOG2E, -M)));
        if (both) L = dpp<0x00>(L);
        st16m(ws, so, asu((f32x4){M, L, M, L}), both ? 3ull : 1ull);
    };
    auto att_pv0 = [&]() __attribute__((always_inline)) {
        u32x4 bm[8];
        trans_v(IC<0>{});
        if (v_after(9) > v_after(7)) { trans_w(IC<0>{}); trans_w(IC<1>{}); }
        trans_v(IC<1>{});
#pragma unroll
        for (int e = 0; e < 8; ++e) bm[e] = *(const LAS u32x4*)(size_t)(pr + e * 192);
        __builtin_amdgcn_sched_barrier(0);
        f32x4 da = z4, dc = z4;
        PIN2(da, dc);
        asm volatile("s_nop 1");
        __builtin_amdgcn_sched_barrier(0);
        f32x2 r;
        smf<RV>(da, bm[0], idx); smf<RV + 8>(da, bm[1], idx); smf<RV + 16>(da, bm[2], idx); smf<RV + 24>(da, bm[3], idx);
        smf<RV + 32>(da, bm[4], idx); smf<RV + 40>(da, bm[5], idx); smf<RV + 48>(da, bm[6], idx); smf<RV + 56>(da, bm[7], idx);
        asm volatile("s_nop 7\n\tv_pk_add_f32 %0, %1, %2\n\tds_write_b64 %3, %0\n\ts_mov_b64 exec, 1\n\tds_add_u32 %4, %5\n\ts_mov_b64 exec, -1"
                     : "=&v"(r) : "v"(LO2(da)), "v"(HI2(da)), "v"(oa), "v"(lb + L_CNT + 24), "v"(1u) : "memory");
        GUARD(900);
        MARK(b_t5);
        if (v_after(9) < v_after(7)) { trans_w(IC<0>{}); trans_w(IC<1>{}); }
        trans_w(IC<2>{}); trans_w(IC<3>{});
        STAMP(4);
        asm volatile("s_nop 1\n\tv_smfmac_f32_16x16x64_bf16 %0, %1, v[224:231], %5\n\tv_smfmac_f32_16x16x64_bf16 %0, %2, v[232:239], %5\n\t"
                     "v_smfmac_f32_16x16x64_bf16 %0, %3, v[240:247], %5\n\tv_smfmac_f32_16x16x64_bf16 %0, %4, v[248:255], %5\n\ts_nop 6"
                     : "+v"(dc) : "v"(bm[0]), "v"(bm[1]), "v"(bm[2]), "v"(bm[3]), "v"(idx));
        asm volatile("s_nop 0\n\tv_pk_add_f32 %0, %1, %2\n\tds_write_b64 %3, %0" : "=&v"(r) : "v"(LO2(dc)), "v"(HI2(dc)), "v"(oc) : "memory");
        STAMP(20);
    };
    unsigned long long ck1 = 0xffff0000ffff0000ull, ck2 = 0xffffffff00000000ull, csc = 0x3d93cd3a3d93cd3aull;
    auto att1 = [&]() __attribute__((always_inline)) {
        constexpr int layer = 1;
        {
#define SMF(c, q, r) "v_smfmac_f32_16x16x64_bf16 v[" #c "], v[" #q "], v[" #r "], %0\n\t"
#define MAXD(x) "v_max_f32_dpp v41, v41, v41 " x " row_mask:0xf bank_mask:0xf\n\t"
            asm volatile("s_waitcnt lgkmcnt(0)\n\ts_barrier\n\t"
                         "ds_read_b128 v[0:3], %1\n\tds_read2_b32 v[12:13], %2 offset0:96 offset1:100\n\tds_read_b128 v[4:7], %1 offset:128\n\tds_read_b128 v[8:11], %1 offset:256\n\t"
                         "v_mov_b64 v[16:17], 0\n\tv_mov_b64 v[18:19], 0\n\tv_mov_b64 v[20:21], 0\n\tv_mov_b64 v[22:23], 0\n\tv_mov_b64 v[24:25], 0\n\tv_mov_b64 v[26:27], 0\n\t"
                         "v_mov_b64 v[28:29], 0\n\tv_mov_b64 v[30:31], 0\n\tv_mov_b64 v[32:33], 0\n\tv_mov_b64 v[34:35], 0\n\tv_mov_b64 v[36:37], 0\n\tv_mov_b64 v[38:39], 0\n\t"
                         "s_waitcnt lgkmcnt(3)\n\t" SMF(16:19, 0:3, 64:71) SMF(20:23, 0:3, 88:95) "s_waitcnt lgkmcnt(1)\n\tv_mul_f32 v13, 0x3acccccd, v13\n\t"
                         SMF(24:27, 0:3, 112:119) "v_mul_f32 v12, 0x3acccccd, v12\n\t" SMF(28:31, 0:3, 136:143) "v_fma_f32 v12, -v13, v13, v12\n\t"
                         SMF(16:19, 4:7, 72:79) "v_maximum3_f32 v12, v12, 0, 0\n\t" SMF(20:23, 4:7, 96:103) "v_cvt_pk_bf16_f32 v12, 0, v12\n\t"
                         SMF(24:27, 4:7, 120:127) "v_add_f32 v12, 0x3727c5ac, v12\n\t" SMF(28:31, 4:7, 144:151) "v_cvt_pk_bf16_f32 v12, 0, v12\n\ts_waitcnt lgkmcnt(0)\n\t"
                         SMF(16:19, 8:11, 80:87) "v_sqrt_f32 v12, v12\n\t" SMF(20:23, 8:11, 104:111) "v_cvt_pk_bf16_f32 v12, 0, v12\n\t"
                         SMF(24:27, 8:11, 128:135) "v_rcp_f32 v12, v12\n\t" SMF(28:31, 8:11, 152:159)
                         "s_nop 2\n\tv_add_f32 v42, v16, v18\n\ts_nop 0\n\tv_add_f32 v43, v20, v22\n\tv_max_f32 v41, v42, v43\n\tv_add_f32 v44, v24, v26\n\tv_add_f32 v45, v28, v30\n\t"
                         "v_max3_f32 v41, v41, v44, v45\n\tv_cndmask_b32 v42, v42, v43, %8\n\tv_cndmask_b32 v44, v44, v45, %8\n\t"
                         MAXD("quad_perm:[1,0,3,2]") "v_cndmask_b32 v40, v42, v44, %9\n\ts_nop 0\n\t" MAXD("quad_perm:[2,3,0,1]") "s_nop 1\n\t" MAXD("row_ror:4") "s_nop 1\n\t" MAXD("row_ror:8")
                         "v_pk_mul_f32 v[40:41], v[12:13], v[40:41] op_sel_hi:[0,1]\n\tv_cvt_pk_bf16_f32 v40, 0, v40\n\tv_cvt_pk_bf16_f32 v41, 0, v41\n\t"
                         "v_pk_mul_f32 v[40:41], %10, v[40:41]\n\tv_cvt_pk_bf16_f32 v40, 0, v40\n\tv_cvt_pk_bf16_f32 v41, 0, v41\n\t"
                         "v_sub_f32 v40, v40, v41\n\tv_mul_f32 v40, 0x3fb8aa3b, v40\n\tv_exp_f32 v40, v40\n\ts_nop 0\n\tv_cvt_pk_bf16_f32 v40, v40, v40\n\tds_write_b16 %3, v40\n\t"
                         "ds_read_b128 v[0:3], %4\n\tds_read_b128 v[4:7], %4 offset:192\n\tds_read_b128 v[8:11], %4 offset:384\n\tds_read_b128 v[12:15], %4 offset:576\n\t"
                         "ds_read_b128 v[16:19], %4 offset:768\n\tds_read_b128 v[20:23], %4 offset:960\n\tds_read_b128 v[24:27], %4 offset:1152\n\tds_read_b128 v[28:31], %4 offset:1344\n\t"
                         "ds_write_b32 %5, v41\n\t"
                         "s_waitcnt lgkmcnt(8)\n\t" SMF(32:35, 0:3, 160:167) SMF(36:39, 0:3, 224:231) "s_waitcnt lgkmcnt(7)\n\t" SMF(32:35, 4:7, 168:175) SMF(36:39, 4:7, 232:239)
                         "s_waitcnt lgkmcnt(6)\n\t" SMF(32:35, 8:11, 176:183) SMF(36:39, 8:11, 240:247) "s_waitcnt lgkmcnt(4)\n\t" SMF(32:35, 12:15, 184:191) SMF(36:39, 12:15, 248:255)
                         SMF(32:35, 16:19, 192:199) "s_waitcnt lgkmcnt(1)\n\t" SMF(32:35, 20:23, 200:207) SMF(32:35, 24:27, 208:215) SMF(32:35, 28:31, 216:223)
                         "s_nop 2\n\tv_pk_add_f32 v[36:37], v[36:37], v[38:39]\n\tds_write_b64 %7, v[36:37]\n\ts_nop 2\n\tv_pk_add_f32 v[32:33], v[32:33], v[34:35]\n\tds_write_b64 %6, v[32:33]"
                         :: "v"(idx), "v"(qa), "v"(qm), "v"(pw), "v"(pr), "v"(ma), "v"(oa), "v"(oc), "s"(ck1), "s"(ck2), "s"(csc)
                         : "memory", "v0", "v1", "v2", "v3", "v4", "v5", "v6", "v7", "v8", "v9", "v10", "v11", "v12", "v13", "v14", "v15", "v16", "v17", "v18", "v19", "v20", "v21", "v22", "v23",
                           "v24", "v25", "v26", "v27", "v28", "v29", "v30", "v31", "v32", "v33", "v34", "v35", "v36", "v37", "v38", "v39", "v40", "v41", "v42", "v43", "v44", "v45");
#undef SMF
#undef MAXD
            STAMP(20 + 32 * layer);
            BAR();
            if (__builtin_expect(wave == 0, 1)) {
                STAMP(5 + 32 * layer);
                comb_o0(*(const LAS f32x4*)(size_t)cma, *(const LAS f32x4*)(size_t)(cma + 16), *(const LAS f32x4*)(size_t)cxa, *(const LAS f32x4*)(size_t)cra,
                        *(const LAS f32x4*)(size_t)(cra + 1024), *(const LAS f32x4*)(size_t)(cra + 2048), *(const LAS f32x4*)(size_t)(cra + 3072), cso, 0x1111111111111111ull, false);
            }
            __builtin_amdgcn_sched_barrier(0);
            if (__builtin_expect(wave == 1, 1))
                comb_o1(*(const LAS f32x4*)(size_t)cma, *(const LAS f32x4*)(size_t)(cma + 16), *(const LAS f32x2*)(size_t)cxa, *(const LAS f32x4*)(size_t)cra, *(const LAS f32x4*)(size_t)(cra + 1024), cso, 0x0000111100001111ull, false);
            __builtin_amdgcn_sched_barrier(0);
            if (__builtin_expect(wave == 2, 1)) comb_ml(cma, cxa, cra, cso, false);
            MARK(e_q);
            if (wave < 3) STAMP(21 + 32 * layer);
            __builtin_amdgcn_s_setprio(0);
        }
    };

    unsigned nj = 0u, rS = 0u, nS = 0u; int ord;
    {
        const int tid = threadIdx.x, lane = tid & 63;
        asm volatile("ds_max_u32 %0, %1" :: "v"(lb + L_CNT + 64), "v"(tst) : "memory");
        unsigned mb = 0u;
        const u16* kbase = kc + wofs / 2;
        const u16* vbase = vc + wofs / 2;
        unsigned pt0 = 0u;
        if (wave == 7) {
            __builtin_amdgcn_s_setprio(3);
            if (lane < 10) *reinterpret_cast<u32x4*>(smem + L_XS + 640 + lane * 16) = (u32x4){0x3f803f80u, 0x3f803f80u, 0x3f803f80u, 0x3f803f80u};
            if (lane < 16) { *reinterpret_cast<u32x4*>(smem + L_CS + 768 + lane * 16) = (u32x4){0u, 0u, 0u, 0u}; }
        } else {
            if (wave >= 4) {
                if (wave == 4) st16_sc1(ws, WS_OWN + (unsigned)(gi * 16384 + (lane >> 5) * 8192 + gj * 512 + (lane & 31) * 16), (u32x4){~0u, ~0u, ~0u, ~0u});
                else empty5(0, (unsigned)(wave - 5) * 8192u, lane);
                if (wave == 6) {
                    __builtin_amdgcn_s_setprio(3);
                    if (gi + gj == 0) st4_sc1(ws, WS_R + (lane & 31) * 4, 0u);
                    st4_sc1(ws, WS_MB + (unsigned)((gi * 8 + gj) * 128 + (lane & 31) * 4), 0u);
                }
            } else if (wave >= 2) {
                unsigned long long c0, c1;
                asm volatile("s_waitcnt lgkmcnt(0)\n\ts_memtime %0\n\ts_waitcnt lgkmcnt(0)\n\ts_load_dword %2, %3, %4\n\ts_waitcnt lgkmcnt(0)\n\ts_memtime %1\n\ts_waitcnt lgkmcnt(0)"
                             : "=&s"(c0), "=&s"(c1), "=&s"(pt0) : "s"(ws), "s"((6u << 20) + ((((tst + g * 977u) * 2654435761u) >> 11 & 0x1fdf80u) | ((wave ^ gj) & 1) * 8192)) : "memory");
                pt0 = ((wave ^ gj) & 1) ? (unsigned)c1 - (unsigned)c0 : (unsigned)c0 - (unsigned)c1;
            }
            STAMP(36);
            *reinterpret_cast<u32x4*>(smem + L_P + tid * 16) = (u32x4){0u, 0u, 0u, 0u};
            if (tid < 72 * 12 - 448) *reinterpret_cast<u32x4*>(smem + L_P + 7168 + tid * 16) = (u32x4){0u, 0u, 0u, 0u};
            if (wave >= 4) {
                if (wave == 6) VMW(2); else VMW(1);
                asm volatile("ds_max_u32 %0, %1" :: "v"(lb + L_CNT + 64), "v"(now() - TLAND) : "memory");
                STAMP(40);
                lds_add(0, 1u);
            } else if (wave >= 2) { lds_add(20, pt0); VMW(0); lds_add(0, 1u); }
        }
        asm volatile("s_waitcnt lgkmcnt(0)" :: "s"(pt1) : "memory");
        const int rot = gj;
        const unsigned r0 = now(), c0 = (unsigned)__builtin_amdgcn_s_memtime();
        u32x4 a[4];
        unsigned ko = (unsigned)((wave * 64 + (lane & 7)) * 384 + (((lane >> 3) & 1) * 4 + (lane >> 4)) * 16);
        PIN1(ko);
        auto k_group = [&](auto GC_) __attribute__((always_inline)) {
            constexpr int G = decltype(GC_)::value, R = RK + 24 * (G >> 1) + 4 * (G & 1);
            const unsigned o = ko + (unsigned)((((G >> 1) + rot) & 3) * 6144 + (G & 1) * 3072);
            pace(3);
            ld16_nt<R, 0>(kbase, o); ld16_nt<R + 8, 128>(kbase, o); ld16_nt<R + 16, 256>(kbase, o);
            const int lane = lane_id();
            const unsigned ho = WS_Q0 + (unsigned)((gi * 4 + (gj >> 1)) * 4096 + lane * 16);
            if (wave < 7) {
                asm volatile("; COND_BEGIN");
                if (G == 0 && wave >= 4) { VMW(3); lds_add(32, 1u); }
                if (G == 0) {
                    if (wave < 2) { VMW(3); lds_add(0, 1u); load_gu(wave, lane); }
                    if (wave == 2) load_wd(lane);
                    if (wave == 3) {
#pragma unroll 1
                        for (int k = 5 - WEARLY; k < 5; ++k) load_wo(k, lane, true);
                    }
                }
                asm volatile("; COND_END");
            }
            if (wave == 7) {
                asm volatile("; COND_BEGIN");
                if (G == GP) {
                    VMW(3 * (GP + 1));
#pragma unroll 1
                    for (unsigned spins = 0; spins < (kSpinCap << 4); ++spins) {
                        if (lds_word(lb + L_CNT) >= 7u) break;
                        __builtin_amdgcn_s_sleep(1);
                    }
                    STAMP(26);
                    const unsigned char* p = smem + L_X0 + (lane >> 4) * 1280;
                    const float c0 = lo16(*reinterpret_cast<const u16*>(p));
                    const f32x2 c2 = {c0, c0};
                    f32x2 s2 = {0.f, 0.f}, q2 = s2;
#pragma unroll
                    for (int k = 0; k < 5; ++k) {
                        const u32x4 w = *reinterpret_cast<const u32x4*>(p + k * 256 + (lane & 15) * 16);
#pragma unroll
                        for (int e = 0; e < 4; ++e) { const f32x2 d = unpk(w[e]) - c2; s2 += d; q2 += d * d; }
                    }
                    const float s = row16_sum(s2[0] + s2[1]), qq = row16_sum(q2[0] + q2[1]);
                    const float ms = s * (1.f / DM);
                    const unsigned mv = pk2(c0 + ms, fmax2(qq - s * ms, 0.f) * (1.f / DM));
                    const float mb = lo16(mv), rden = frcp(bfr(__builtin_amdgcn_sqrtf(bfr(hi16(mv) + 1e-5f))));
                    const f32x2 mb2 = {mb, mb}, rd2 = {rden, rden};
                    if ((lane & 15) < 10) {
                        u32x4 w = *reinterpret_cast<const u32x4*>(p + gj * 160 + (lane & 15) * 16);
#pragma unroll
                        for (int e = 0; e < 4; ++e) w[e] = pk2v(unpk(pk2v(unpk(w[e]) - mb2)) * rd2);
                        *reinterpret_cast<u32x4*>(smem + L_XS + (lane >> 4) * 160 + (lane & 15) * 16) = w;
                    }
                    STAMP(30);
                }
                if (G == GP || G == GA) {
#pragma unroll 1
                    for (int blk = 0; blk < 3; ++blk) {
                        LANE_VARS();
                        const unsigned xb = lb + L_XS + (unsigned)((G == GP ? q4 : 4) * 160);
                        Frag80 f[4];
#pragma unroll
                        for (int i = 0; i < 4; ++i) f[i] = frag80(lb + L_WQ + (unsigned)(128 * blk + 32 * i + 8 * p4), 416, r0s, r1s, r2s);
                        const Frag80 b = rows80(xb + kq * 16, xb + 128 + kq * 8);
                        const u32x4 c = tiles4(f, b, 0x1111111111111111ull);
                        if (G == GP) *reinterpret_cast<u32x4*>(smem + L_OP + q4 * 768 + 256 * blk + p4 * 64 + kq * 16) = c;
                        else *reinterpret_cast<u32x4*>(smem + L_CS + 256 * blk + kq * 64 + p4 * 16) = c;
                    }
                    if (G == GP) {
                        asm volatile("" ::: "memory");
                        const int ln = lane_id();
                        f32x2 c[6];
                        unsigned m = 20u;
#pragma unroll
                        for (int k = 0; k < 6; ++k) {
                            c[k] = *reinterpret_cast<const f32x2*>(smem + L_OP + (k / 3) * 1536 + ln * 24 + (k % 3) * 8);
                            m = max(m, max(fexp(c[k][0]), fexp(c[k][1])));
                        }
                        m = max(m, dppu<0xB1>(m)); m = max(m, dppu<0x4E>(m)); m = max(m, dppu<0x141>(m)); m = max(m, dppu<0x140>(m));
                        m = max(max((unsigned)__builtin_amdgcn_readlane(m, 0), (unsigned)__builtin_amdgcn_readlane(m, 16)), max((unsigned)__builtin_amdgcn_readlane(m, 32), (unsigned)__builtin_amdgcn_readlane(m, 48)));
                        lds_set(lb + L_CNT + 76, m);
                        const float f = __uint_as_float((273u - m) << 23);
#pragma unroll
                        for (int k = 0; k < 2; ++k) {
                            const u32x2 w0 = enc21(c[3 * k][0] * f, c[3 * k][1] * f, c[3 * k + 1][0] * f), w1 = enc21(c[3 * k + 1][1] * f, c[3 * k + 2][0] * f, c[3 * k + 2][1] * f);
                            st16_sc1(ws, WS_Q0 + (unsigned)((gi * 4 + 2 * k + (ln >> 5)) * 4096 + gj * 512 + (ln & 31) * 16), (u32x4){w0[0], w0[1], w1[0], w1[1]});
                        }
                        STAMP(2);
                        WAIT1(pf, 3 * (GP + 1) + 2);
                    }
                }
                if (G == GA) {
                    const unsigned half = __builtin_amdgcn_s_getreg(2 << 6 | 20);
                    lds_set(lb + L_CNT + 68, lds_word(lb + L_CNT + 64) << 11 | lds_word(lb + L_CNT + 76) << 3 | 1u | (((int)lds_word(lb + L_CNT + 20) > 0) ^ half) << 1 | half << 2);
                    VMW(3 * (GA - GP));
#pragma unroll 1
                    for (unsigned spins = 0; spins < (kSpinCap << 4); ++spins) if (lds_word(lb + L_CNT + 32) >= 3u) break;
                    send_flag(lane_id());
                    lds_set(lb + L_CNT + 8, 1u);
                    STAMP(17);
                    f32x2 r2 = {0.f, 0.f};
#pragma unroll
                    for (int k = 0; k < 5; ++k) r2 += unpk(*reinterpret_cast<const unsigned*>(smem + L_WR + k * 256 + lane_id() * 4));
                    *reinterpret_cast<float*>(smem + L_CNT + 12) = wave_sum(r2[0] + r2[1]);
                }
                asm volatile("; COND_END");
            }
            if (wave == 6) {
                asm volatile("; COND_BEGIN");
                if (G == GB - 1) mb = ld4_sc1(ws, WS_MB + (unsigned)((gi * 8 + gj) * 128 + (gi & 7) * 4));
                if (G == GB) {
                    WAIT1(mb, 3);
#pragma unroll 1
                    for (unsigned spins = 0; spins < kSpinCap && !all_set(mb); ++spins) {
                        if ((spins & 7u) == 7u && lds_word(lb + L_CNT + 8)) send_flag(lane);
                        mb = ld4_sc1(ws, WS_MB + (unsigned)((gi * 8 + gj) * 128 + (gi & 7) * 4));
                        WAIT1(mb, 0);
                    }
                    mb = ld4_sc1(ws, WS_MB + (unsigned)((gi * 8 + (gi & 7)) * 128 + (lane & 7) * 4));
                }
                if (G == GC) {
                    WAIT1(mb, 3 * (GC - GB));
#pragma unroll 1
                    for (unsigned spins = 0; spins < kSpinCap && !all_set(mb); ++spins) {
                        if ((spins & 7u) == 7u && lds_word(lb + L_CNT + 8)) send_flag(lane);
                        mb = ld4_sc1(ws, WS_MB + (unsigned)((gi * 8 + (gi & 7)) * 128 + (lane & 7) * 4));
                        WAIT1(mb, 0);
                    }
                    lds_set(lb + L_CNT + 92, (__builtin_amdgcn_readlane(mb, gi & 7) & 4u) << 11);
                    int d = min(max((int)(mb - (tst << 11)) >> 11, -400), 600);
                    d += (int)dppu<0xB1>((unsigned)d); d += (int)dppu<0x4E>((unsigned)d); d += (int)dppu<0x141>((unsigned)d);
                    const unsigned tm = tst + (unsigned)(__builtin_amdgcn_readfirstlane(d) >> 3);
                    lds_set(lb + L_CNT + 64, tm);
                    const float slow = (float)(now() - r0) * 24.f * 256.f * frcp((float)((unsigned)__builtin_amdgcn_s_memtime() - c0)) - 256.f;
                    lds_set(lb + L_CNT + 88, tm << 11 | (unsigned)__builtin_amdgcn_fmed3f(slow, 0.f, 63.f) << 5 | __builtin_popcount((unsigned)__builtin_amdgcn_ballot_w64((mb & 2u) != 0u) & 255u) << 1 | 1u);
                    report();
                    a[0] = ld16_sc1<0>(ws, ho); a[1] = ld16_sc1<1024>(ws, ho); a[2] = ld16_sc1<2048>(ws, ho); a[3] = ld16_sc1<3072>(ws, ho);
                    pace(XCOST, false);
                    STAMP(31);
                }
                asm volatile("; COND_END");
            } else if (G == GC) { for (int k = 0; k < 4; ++k) asm volatile("" : "=v"(a[k])); }
        };
        k_group(IC<0>{}); k_group(IC<1>{}); k_group(IC<2>{}); k_group(IC<3>{}); k_group(IC<4>{}); k_group(IC<5>{}); k_group(IC<6>{}); k_group(IC<7>{});
        {
            const int lane = lane_id();
            const unsigned v0 = (unsigned)((wave * 64 + 4 * (lane >> 4)) * 384 + (lane & 15) * 16);
            const unsigned v1 = v0 + 256 - (unsigned)((lane & 15) * 8);
            auto v_group = [&](auto HC) __attribute__((always_inline)) {
                constexpr int h = decltype(HC)::value, p = kVord[h], R = p < 8 ? RV + 16 * (p & 3) + 4 * (p >> 2) : RV + 64 + 2 * (p - 8), NV = v_upto(h);
                const unsigned o = (p < 8 ? v0 : v1) + (unsigned)((((p < 8 ? p >> 1 : p) + rot) & 3) * 6144);
                pace(2);
                if (p < 8) { ld16_nt<R, (p & 1) * 768>(vbase, o); ld16_nt<R + 8, (p & 1) * 768 + 384>(vbase, o); }
                else { ld8_nt<R, 0>(vbase, o); ld8_nt<R + 8, 384>(vbase, o); ld8_nt<R + 16, 768>(vbase, o); ld8_nt<R + 24, 1152>(vbase, o); }
                if (wave == 7 && gi + gj == 0) {
                    asm volatile("; COND_BEGIN");
                    if (h == 3 || h == 6) mb = ld4_sc1(ws, WS_R + (unsigned)((lane & 31) * 4));
                    if (h == 4 || h == 7) {
                        WAIT1(mb, NV - v_upto(h - 1));
                        if (all_set(mb) && !lds_word(lb + L_CNT + 36)) answer(lane, mb);
                    }
                    asm volatile("; COND_END");
                }
                if (wave == 6) {
                    asm volatile("; COND_BEGIN");
                if (h == HD) {
                    asm volatile("s_waitcnt vmcnt(%4)" : "+v"(a[0]), "+v"(a[1]), "+v"(a[2]), "+v"(a[3]) : "i"(3 * (7 - GC) + NV) : "memory");
                    float sq[6] = {0.f, 0.f, 0.f, 0.f, 0.f, 0.f};
#pragma unroll
                    for (int k = 0; k < 4; ++k) {
                        const float f = __uint_as_float(((((unsigned)__builtin_amdgcn_ds_bpermute((2 * k + (lane >> 5)) * 4, (int)mb) >> 3) & 255u) - 19u) << 23);
#pragma unroll
                        for (int e = 0; e < 2; ++e) {
                            const unsigned lo = a[k][2 * e], hi = a[k][2 * e + 1];
                            const float fe = (int)hi < 0 ? __builtin_nanf("") : f;
                            sq[3 * e] += (float)((int)(lo << 11) >> 11) * fe;
                            sq[3 * e + 1] += (float)((int)(__builtin_amdgcn_alignbit(hi, lo, 21) << 11) >> 11) * fe;
                            sq[3 * e + 2] += (float)((int)(hi << 1) >> 11) * fe;
                        }
                    }
#pragma unroll
                    for (int e = 0; e < 3; ++e) {
                        const int pi = 3 * (lane & 31) + e;
                        *reinterpret_cast<unsigned*>(smem + L_Q + (pi >> 3) * 32 + (pi & 6) * 2 + (pi & 1) * 16) = pk2(x32_add(sq[2 * e]), x32_add(sq[2 * e + 1]));
                    }
                    asm volatile("s_waitcnt lgkmcnt(0)\n\tds_write_b32 %0, %1" :: "v"(lb + L_CNT + 4), "v"(1u) : "memory");
                    STAMP(27);
                    lead = LEAD * PACE * 4;
                }
                    if (h == 6) mb = ld4_sc1(ws, WS_MB + (unsigned)((gi * 8 + (gi & 7)) * 128 + 32));
                    if (h == 8) {
                        WAIT1(mb, NV - v_upto(6));
                        const unsigned r = __builtin_amdgcn_readfirstlane(mb);
                        if (r) { adopt(r); STAMP(29); }
                    }
                    asm volatile("; COND_END");
                }
                if constexpr (h < 6) {
                    constexpr int i = 2 * h;
                    fix_k1(IC<i>{}, IC<20 - 6 * (i / 3) - i % 3 + NV>{});
                    fix_k1(IC<i + 1>{}, IC<20 - 6 * ((i + 1) / 3) - (i + 1) % 3 + NV>{});
                }
                if (h == 5) {
                    STAMP(1);
                    __builtin_amdgcn_s_setprio(0);
                    att_qk0();
                }
            };
            v_group(IC<0>{}); v_group(IC<1>{}); v_group(IC<2>{}); v_group(IC<3>{}); v_group(IC<4>{}); v_group(IC<5>{});
            v_group(IC<6>{}); v_group(IC<7>{}); v_group(IC<8>{}); v_group(IC<9>{}); v_group(IC<10>{}); v_group(IC<11>{});
            lds_add(16, 1u);
            if (wave == 3) lds_add(44, WEARLY);
        }
    }
    {
        STAMP(28);
        att_addr();
        const int lane = lane_id();
        cma = lb + L_M; cxa = lb + L_M + (unsigned)((lane >> 4) * 8); cra = lb + L_OP + (unsigned)(512 + (lane >> 4) * 2048 + (lane & 15) * 16);
        cso = WS_OWN + (unsigned)(gi * 16384 + gj * 512 + 256 + (lane >> 5) * 64 + (lane & 12) * 4 + (lane & 1) * 8192);
        PIN2(cma, cxa); PIN2(cra, cso);
        att_pv0();
    }
    {
        f32x4 m0, m1, o0, o1; f32x2 mm; unsigned r, taken;
        asm volatile("s_mov_b64 exec, 1\n\tds_add_rtn_u32 %5, %7, %8 offset:%11\n\ts_mov_b64 exec, -1\n\t"
                     "ds_read_b128 %0, %7\n\tds_read_b128 %1, %7 offset:16\n\tds_read_b64 %2, %9\n\tds_read_b128 %3, %10\n\tds_read_b128 %4, %10 offset:1024\n\t"
                     "ds_read_b32 %6, %7 offset:%12\n\ts_waitcnt lgkmcnt(0)"
                     : "=&v"(m0), "=&v"(m1), "=&v"(mm), "=&v"(o0), "=&v"(o1), "=&v"(r), "=&v"(taken)
                     : "v"(cma), "v"(1u), "v"(cxa), "v"(cra), "i"(L_CNT + 28 - L_M), "i"(L_CNT + 40 - L_M) : "memory");
        ord = __builtin_amdgcn_readfirstlane(r);
        if (ord == 7) { STAMP(5); comb_o1(m0, m1, mm, o0, o1, cso, 0x0000333300003333ull, true); lds_set(lb + L_CNT + 48, now() | 1u); STAMP(21); }
        MARK(e_t5);
        if (ord <= OML + 1 || (unsigned)__builtin_amdgcn_readfirstlane(taken) < 5u - WEARLY) {
        const int lane = lane_id();
        auto wait_cnt = [&](int off, unsigned v) __attribute__((always_inline)) {
#pragma unroll 1
            for (unsigned spins = 0; spins < (kSpinCap << 4); ++spins) { if (lds_word(lb + L_CNT + off) >= v) break; __builtin_amdgcn_s_sleep(1); }
        };
        if (ord == OML) {
            wait_cnt(16, 8u);
            unsigned xa, ra; ml_addr(lane, xa, ra);
            comb_ml(cma, xa, ra, WS_OWN + (unsigned)(gi * 16384 + gj * 512 + 384 + (lane & 1) * 8192), true);
            STAMP(24);
        } else if (ord == 0 && !lds_word(lb + L_CNT + 52)) {
            if (gi + gj == 0 && !lds_word(lb + L_CNT + 36)) {
                unsigned r = 0u;
#pragma unroll 1
                for (unsigned spins = 0; spins < kSpinCap; ++spins) {
                    r = ld4_sc1(ws, WS_R + (unsigned)((lane & 31) * 4));
                    WAIT1(r, 0);
                    if (all_set(r)) break;
                    if ((spins & 7u) == 7u) send_flag(lane);
                }
                answer(lane, r);
            }
#pragma unroll 1
            for (unsigned spins = 0; spins < kSpinCap; ++spins) {
                unsigned r = ld4_sc1(ws, WS_MB + (unsigned)((gi * 8 + (gi & 7)) * 128 + 32));
                WAIT1(r, 0);
                r = __builtin_amdgcn_readfirstlane(r);
                if (r) { adopt(r); break; }
                if ((spins & 7u) == 7u) { send_flag(lane); report(); }
                __builtin_amdgcn_s_sleep(16);
            }
            STAMP(29);
        } else if (ord == OML + 1) {
#pragma unroll 1
            for (unsigned spins = 0; spins < (kSpinCap << 6); ++spins) if (lds_word(lb + L_CNT + 24) >= 8u) break;
            const unsigned ra = lb + L_OP + (unsigned)((lane >> 5) * 4096 + (lane & 31) * 16);
            comb_o0(*(const LAS f32x4*)(size_t)cma, *(const LAS f32x4*)(size_t)(cma + 16), *(const LAS f32x4*)(size_t)(cma + (lane >> 5) * 16), *(const LAS f32x4*)(size_t)ra, *(const LAS f32x4*)(size_t)(ra + 1024),
                    *(const LAS f32x4*)(size_t)(ra + 2048), *(const LAS f32x4*)(size_t)(ra + 3072), WS_OWN + (unsigned)(gi * 16384 + gj * 512 + (lane & 60) * 4 + (lane & 1) * 8192), 0x3333333333333333ull, true);
            STAMP(22);
        }
        if (ord == OML) {
#pragma unroll 1
            for (unsigned spins = 0; spins < (kSpinCap << 8); ++spins) { if (lds_word(lb + L_CNT + 52)) break; __builtin_amdgcn_s_sleep(1); }
            const unsigned S = lds_word(lb + L_CNT + 80);
            empty4(0, S, lane); empty5(5, 0u, lane); empty5(5, 8192u, lane);
        }
#pragma unroll 1
        for (;;) {
            unsigned j;
            asm volatile("s_mov_b64 exec, 1\n\tds_add_rtn_u32 %0, %1, %2\n\ts_mov_b64 exec, -1\n\ts_waitcnt lgkmcnt(0)" : "=&v"(j) : "v"(lb + L_CNT + 40), "v"(1u) : "memory");
            j = __builtin_amdgcn_readfirstlane(j);
            if (j >= 5u - WEARLY) break;
            load_wo((int)j, lane, false); ++nj;
        }
        }
        STAMP(23);
    }

    unsigned omr, owa, owb;
    auto op_addr = [&](int layer) __attribute__((always_inline)) {
        LANE_VARS();
        const int t = wave - 3;
        cma = WS_OWN + rS + (unsigned)(gi * 16384 + layer * 4096 + (lane >> 3) * 512 + t * 128 + (t == 3 ? 0 : (lane & 7) * 16));
        cxa = lb + L_O2 + (unsigned)((lane >> 3) * 416 + t * 128 + (t == 3 ? 0 : (lane & 7) * 16));
        const int pb = (lane & 4) ? 3 - p4 : p4, jp = 2 * pb + ((lane >> 2) & 1);
        cso = tile35(gj, 10 * layer + t) + (unsigned)((kq & 1) * 8192 + gi * 128 + (kq >> 1) * 64 + p4 * 16);
        cra = lb + L_O2 + (unsigned)(jp * 416 + kq * 16); omr = lb + L_O2 + (unsigned)(jp * 416 + 384);
        owa = lb + L_WO + (unsigned)(r0s * 160 + 32 * t + 8 * p4); owb = lb + L_WO + (unsigned)(r1s * 160 + 32 * t + 8 * p4);
        if (layer) PIN2(cma, cxa);
        PIN2(cso, cra); PIN3(omr, owa, owb);
    };
    auto chain = [&](auto LC) __attribute__((always_inline)) {
        constexpr int layer = decltype(LC)::value;
        unsigned h4 = WS_H4 + (unsigned)((layer * 32 + gi) * 32768) + (layer ? rS ^ 8192u : 0u);
        auto sides = [&]() __attribute__((always_inline)) { rS = lds_word(lb + L_CNT + 80); nS = lds_word(lb + L_CNT + 84); h4 += rS ^ 8192u; };
        if (wave < 3) {
            LANE_VARS();
            if (layer == 0) {
                unsigned lo = WS_OWN + lds_word(lb + L_CNT + 84) + (unsigned)(gi * 16384 + (lane >> 3) * 512), lw = lb + L_O2 + (unsigned)((lane >> 3) * 416);
                unsigned lo0 = lo + (unsigned)((lane & 7) * 16), lw0 = lw + (unsigned)((lane & 7) * 16);
                PIN2(lo0, lo); PIN2(lw0, lw);
                GUARD(400);
                MARK(b_g2);
                if (__builtin_expect(wave == 2, 0)) { if (nj | (ord == OML)) { VMW(0); lds_add(44, nj); } }
                else if (__builtin_expect(wave == 1, 0)) {
#pragma unroll 1
                    for (unsigned spins = 0; spins < (kSpinCap << 4); ++spins) if (lds_word(lb + L_CNT + 48)) break;
#pragma unroll 1
                    for (unsigned spins = 0; spins < kSpinCap; ++spins) {
                        u32x4 o0 = ld16_sc1<0>(ws, lo0), o1 = ld16_sc1<128>(ws, lo0), o2 = ld16_sc1<384>(ws, lo);
                        if (nj) { VMW(3); lds_add(44, nj); nj = 0u; }
                        asm volatile("s_waitcnt vmcnt(0)" : "+v"(o0), "+v"(o1), "+v"(o2) :: "memory");
                        *(LAS u32x4*)(size_t)lw0 = o0; *(LAS u32x4*)(size_t)(lw0 + 128) = o1; *(LAS u32x4*)(size_t)(lw + 384) = o2;
                        if (!any_empty(max(max(um4(o0), um4(o1)), um4(o2)))) break;
                    }
                } else {
                    unsigned long long miss; unsigned cnt = kSpinCap << 4, tf, nv = nj;
                    asm volatile("1:\n\tds_read_b32 v4, %4 offset:48\n\ts_sub_u32 %1, %1, 1\n\ts_cbranch_scc1 5f\n\ts_waitcnt lgkmcnt(0)\n\tv_readfirstlane_b32 %2, v4\n\ts_cmp_eq_u32 %2, 0\n\ts_cbranch_scc1 1b\n\t"
                                 "s_add_u32 %2, %2, %8\n"
                                 "2:\n\ts_memrealtime vcc\n\ts_waitcnt lgkmcnt(0)\n\ts_sub_u32 vcc_lo, %2, vcc_lo\n\ts_cmp_lt_u32 vcc_lo, 0x400\n\ts_cbranch_scc1 2b\n"
                                 "5:\n\ts_mov_b32 %1, 15\n"
                                 "3:\n\tglobal_load_dwordx4 v[0:3], %5, %6 offset:256 sc1\n\ts_waitcnt vmcnt(1)\n\ts_mov_b64 exec, 1\n\tds_add_u32 %4, %3 offset:44\n\ts_mov_b64 exec, -1\n\tv_mov_b32 %3, 0\n\t"
                                 "s_sub_u32 %1, %1, 1\n\ts_cselect_b64 vcc, 0, -1\n\ts_waitcnt vmcnt(0)\n\tds_write_b128 %7, v[0:3] offset:256\n\tv_max3_u32 v4, v0, v1, v2\n\tv_max_u32 v4, v4, v3\n\t"
                                 "v_cmp_eq_u32 %0, -1, v4\n\ts_and_b64 vcc, vcc, %0\n\ts_cbranch_scc1 3b"
                                 : "=&s"(miss), "+s"(cnt), "=&s"(tf), "+v"(nv) : "v"(lb + L_CNT), "v"(lo0), "s"(ws), "v"(lw0), "i"(DG2 - 1) : "memory", "vcc", "scc", "v0", "v1", "v2", "v3", "v4");
                    if (__builtin_expect(miss != 0ull, 0)) {
#pragma unroll 1
                        for (unsigned spins = 0; spins < kSpinCap; ++spins) {
                            if ((spins & 15u) == 0u) send_flag(lane);
                            u32x4 o0 = ld16_sc1<256>(ws, lo0);
                            WAIT1(o0, 0);
                            *(LAS u32x4*)(size_t)(lw0 + 256) = o0;
                            if (!any_empty(um4(o0))) break;
                        }
                    }
                }
                MARK(e_g2);
                STAMP(25);
                BAR();
                sides();
                if (wave < 2) empty1(wave, rS, lane);
                else {
                    st16m(ws, WS_CS + (unsigned)((gi * 8 + gj) * 768 + lane * 16), *reinterpret_cast<const u32x4*>(smem + L_CS + lane * 16), 0xffffffffffffull);
                    empty5(10, 0u, lane); empty5(10, 8192u, lane);
                }
            } else BAR();
            {
                const unsigned im = lb + L_GU + (unsigned)(32 * wave + 8 * p4);
                Frag80 fg = frag80(im, 96, r0s, r1s, r2s), fu = frag80(im + 8192, 96, r0s, r1s, r2s);
                const unsigned xb = lb + L_XS + (unsigned)((n < 4 ? n : 4) * 160);
                unsigned b16 = xb + kq * 16, b8 = xb + 128 + kq * 8;
                unsigned sg = h4 + (unsigned)(n < 4 ? h4s(wave) + gj * 512 + kq * 64 + n * 16 : H4C + wave * 1024 + gj * 128 + kq * 16), su = sg + (n < 4 ? 256u : 64u);
                unsigned o = h4 + h4s(wave) + (unsigned)(lane * 16);
                unsigned oc = h4 + (unsigned)(H4C + wave * 1024 + ((lane >> 5) * 4 + p4) * 128 + (kq & 1) * 64 + q4 * 16);
                unsigned os = h4 + (unsigned)(H4T + (q4 + 4 * (kq & 1)) * 128 + p4 * 16);
                unsigned ha = lb + L_H + (unsigned)(p4 * 96 + wave * 32 + q4 * 8 + kq * 2);
                f32x2 cs = {0.f, 0.f};
                if (layer != 0) cs = *reinterpret_cast<const f32x2*>(smem + L_CS2 + wave * 512 + lane * 8);
                PIN3(fg.a0, fg.a1, fg.a2); PIN3(fu.a0, fu.a1, fu.a2); PIN3(b16, b8, sg); PIN3(su, o, os); PIN2(ha, cs);
                unsigned long long mk = layer ? 0x000f000f000f000full : 0x001f001f001f001full;
                unsigned ta = WS_MB + (unsigned)((lane >> 1) * 1024 + 64 + (lane & 1) * 16);
                if (layer == 0) PIN2(oc, ta);
                unsigned x4a = layer ? 0u : X4A * lds_word(lb + L_CNT + 20) >> 12;
                PINS(x4a);
                unsigned long long rdm = 0x3acccccd3acccccdull;
                PINS(mk); PINS(rdm);
                if (layer == 0) VMW(0);
                if (layer == 0) GUARD(1200); else GUARD(800);
                MARK(b_gu);
                BAR();
                STAMP(10 + 32 * layer);
                u32x4 tl = {0u, 0u, 0u, 0u};
                if (layer == 0) tl = ld16_sc1(ws, ta);
                const Frag80 b = rows80(b16, b8);
                const f32x4 g0 = mfma32(fg.a0, b.a0, z4), u0 = mfma32(fu.a0, b.a0, z4), g1 = mfma32(fg.a1, b.a1, g0), g2 = mfma16(fg.a2, b.a2, z4), u1 = mfma32(fu.a1, b.a1, u0), u2 = mfma16(fu.a2, b.a2, z4);
                __builtin_amdgcn_sched_barrier(0);
                const f32x4 cg = g1 + g2, cu = u1 + u2;
                st16m2<0>(ws, sg, asu(cg), su, asu(cu), mk);
                STAMP(11 + 32 * layer);
                if (layer == 0) {
                    WAIT1(tl, 2);
#pragma unroll 1
                    for (unsigned spins = 0; spins < kSpinCap && !all_set(min(min(tl[0], tl[1]), min(tl[2], tl[3]))); ++spins) {
                        tl = ld16_sc1(ws, ta);
                        WAIT1(tl, 0);
                    }
                    const unsigned tn = now();
                    unsigned d = min(min(min(tn - tl[0], tn - tl[1]), min(tn - tl[2], tn - tl[3])), 800u);
                    d = min(d, dppu<0xB1>(d)); d = min(d, dppu<0x4E>(d)); d = min(d, dppu<0x124>(d)); d = min(d, dppu<0x128>(d));
                    unsigned tr = tn - min(min((unsigned)__builtin_amdgcn_readlane(d, 0), (unsigned)__builtin_amdgcn_readlane(d, 16)), min((unsigned)__builtin_amdgcn_readlane(d, 32), (unsigned)__builtin_amdgcn_readlane(d, 48)));
                    lds_set(lb + L_CNT + 56, tr | 1u);
                    until(__builtin_amdgcn_readfirstlane(tr | 1u) + x4a);
                } else sched(X4, 0);
                STAMP(16 + 32 * layer);
                float hv;
                u32x4 a0, a1, a2, a3, ac, as;
                auto load = [&]() __attribute__((always_inline)) {
                    as = ld16_sc1(ws, os);
                    if (layer == 0) ac = ld16_sc1(ws, oc);
                    a0 = ld16_sc1<0>(ws, o); a1 = ld16_sc1<1024>(ws, o); a2 = ld16_sc1<2048>(ws, o); a3 = ld16_sc1<3072>(ws, o);
                };
                auto all_in = [&]() __attribute__((always_inline)) {
                    if (layer == 0) asm volatile("s_waitcnt vmcnt(0)" : "+v"(a0), "+v"(a1), "+v"(a2), "+v"(a3), "+v"(ac), "+v"(as) :: "memory");
                    else { asm volatile("s_waitcnt vmcnt(0)" : "+v"(a0), "+v"(a1), "+v"(a2), "+v"(a3), "+v"(as) :: "memory"); ac = as; }
                };
                auto compute = [&]() __attribute__((always_inline)) {
#define ROR(n) " row_ror:" #n " row_mask:0xf bank_mask:0xf\n\t"
#define QD2(p) " quad_perm:" p " row_mask:0xf bank_mask:0xf\n\t"
                    asm volatile("s_waitcnt vmcnt(%16)\n\tv_add_f32_dpp v40, %2, %2" ROR(8) "v_add_f32_dpp v41, %3, %3" ROR(8) "s_nop 0\n\tv_add_f32_dpp v40, v40, v40" ROR(4) "v_add_f32_dpp v41, v41, v41" ROR(4)
                                 "v_mov_b32 v42, v40\n\tv_mov_b32 v43, v41\n\ts_nop 0\n\tv_permlane16_swap_b32 v40, v42\n\tv_permlane16_swap_b32 v41, v43\n\t"
                                 "v_pk_add_f32 v[40:41], v[40:41], v[42:43]\n\tv_pk_mul_f32 v[40:41], v[40:41], %14\n\t"
                                 ".if %17 == 0\n\ts_waitcnt vmcnt(4)\n\tv_mov_b64 v[44:45], %12\n\tv_mov_b64 v[46:47], %13\n\t.endif\n\t"
                                 "v_fma_f32 v40, -v41, v41, v40\n\tv_cvt_pk_bf16_f32 v42, 0, v41\n\tv_maximum3_f32 v40, v40, 0, 0\n\t"
                                 ".if %17 == 0\n\tv_permlane32_swap_b32 v44, v46\n\tv_permlane32_swap_b32 v45, v47\n\tv_pk_add_f32 v[44:45], v[44:45], v[46:47]\n\t.endif\n\t"
                                 "v_cvt_pk_bf16_f32 v40, 0, v40\n\ts_waitcnt vmcnt(2)\n\tv_pk_add_f32 v[32:33], %4, %6\n\tv_add_f32 v40, 0x3727c5ac, v40\n\tv_pk_add_f32 v[34:35], %5, %7\n\t"
                                 ".if %17 == 0\n\tv_permlane16_swap_b32 v44, v45\n\t.endif\n\t"
                                 "v_cvt_pk_bf16_f32 v40, 0, v40\n\ts_waitcnt vmcnt(0)\n\tv_pk_add_f32 v[36:37], %8, %10\n\tv_sqrt_f32 v40, v40\n\tv_pk_add_f32 v[38:39], %9, %11\n\t"
                                 ".if %17 == 0\n\tv_add_f32_dpp v44, v44, v44" QD2("[1,0,3,2]") "v_add_f32_dpp v45, v45, v45" QD2("[1,0,3,2]") ".endif\n\t"
                                 "v_pk_add_f32 v[32:33], v[32:33], v[36:37]\n\tv_pk_add_f32 v[34:35], v[34:35], v[38:39]\n\tv_cvt_pk_bf16_f32 v40, 0, v40\n\t"
                                 ".if %17 == 0\n\tv_add_f32_dpp v44, v44, v44" QD2("[2,3,0,1]") "v_add_f32_dpp v45, v45, v45" QD2("[2,3,0,1]") "v_mov_b64 %1, v[44:45]\n\t.else\n\ts_nop 0\n\t.endif\n\t"
                                 "v_permlane32_swap_b32 v32, v34\n\tv_permlane32_swap_b32 v33, v35\n\tv_rcp_f32 v40, v40\n\tv_pk_add_f32 v[32:33], v[32:33], v[34:35]\n\ts_nop 1\n\t"
                                 "v_permlane16_swap_b32 v32, v33\n\t"
                                 "v_pk_fma_f32 v[32:33], %1, v[42:43], v[32:33] op_sel_hi:[1,0,1] neg_lo:[1,0,0] neg_hi:[1,0,0]\n\tv_pk_mul_f32 v[32:33], v[32:33], v[40:41] op_sel_hi:[1,0]\n\t"
                                 "v_cvt_pk_bf16_f32 v32, 0, v32\n\tv_mul_f32 v34, 0xbfb8aa3b, v32\n\tv_exp_f32 v34, v34\n\tv_cvt_pk_bf16_f32 v33, 0, v33\n\tv_add_f32 v34, 1.0, v34\n\t"
                                 "v_rcp_f32 v34, v34\n\ts_nop 0\n\tv_mul_f32 v32, v34, v32\n\tv_cvt_pk_bf16_f32 v32, 0, v32\n\tv_mul_f32 %0, v33, v32\n\tv_cvt_pk_bf16_f32 v32, %0, %0\n\tds_write_b16 %15, v32"
                                 : "=&v"(hv), "+v"(cs)
                                 : "v"(as[0]), "v"(as[1]), "v"(LO2(a0)), "v"(HI2(a0)), "v"(LO2(a1)), "v"(HI2(a1)), "v"(LO2(a2)), "v"(HI2(a2)), "v"(LO2(a3)), "v"(HI2(a3)),
                                   "v"(LO2(layer ? as : ac)), "v"(HI2(layer ? as : ac)), "s"(rdm), "v"(ha), "i"(layer ? 4 : 5), "i"(layer), "v"(as)
                                 : "memory", "v32", "v33", "v34", "v35", "v36", "v37", "v38", "v39", "v40", "v41", "v42", "v43", "v44", "v45", "v46", "v47");
#undef ROR
#undef QD2
                };
                RETRY(load(), (all_in(), max(max(max(um4(a0), um4(a1)), max(um4(a2), um4(a3))), max(um4(ac), um4(as)))), compute(), any_nan(hv, hv), layer ? X4 : X4A);
                BAR();
                MARK(e_gu);
                STAMP(12 + 32 * layer);
                if (layer == 0) {
                    *reinterpret_cast<f32x2*>(smem + L_CS2 + wave * 512 + lane * 8) = cs;
                }
            }
            if (layer == 0) {
                if (wave == 1) {
                    const unsigned o = WS_CS + (unsigned)(gi * 6144 + 3072 + (lane < 48 ? lane : 0) * 16);
                    u32x4 a[8];
                    GATHER8(a, o, 768);
                    WAIT8(a, 0);
                    if (lane < 48) *reinterpret_cast<f32x4*>(smem + L_CS + lane * 16) = SUM8(a);
                }
                Frag80 f[4];
#pragma unroll
                for (int i = 0; i < 4; ++i) { f[i] = frag80(lb + L_WQ + (unsigned)(128 * wave + 32 * i + 8 * p4), 416, r0s, r1s, r2s); PIN3(f[i].a0, f[i].a1, f[i].a2); }
                unsigned b16 = lb + L_XS + (unsigned)(q4 * 160 + kq * 16), b8 = lb + L_XS + (unsigned)(q4 * 160 + 128 + kq * 8);
                unsigned so = WS_H1 + rS + (unsigned)((gi * 4 + q4) * 16384 + gj * 896 + 256 * wave + kq * 64 + p4 * 16);
                unsigned long long mk = 0x1111111111111111ull;
                PIN3(b16, b8, so); PINS(mk);
                GUARD(3800);
                MARK(b_q);
                BAR();
                STAMP(33);
                const Frag80 b = rows80(b16, b8);
                st16_sc1(ws, so, tiles4(f, b, mk));
                STAMP(34);
                qk_addr(true); att_addr();
                unsigned x0a = lb + L_M + (unsigned)((lane >> 5) * 16), x1a = lb + L_M + (unsigned)((lane >> 4) * 8), x2a, r2a;
                unsigned r0a = lb + L_OP + (unsigned)((lane >> 5) * 4096 + (lane & 31) * 16), r1a = lb + L_OP + (unsigned)(512 + (lane >> 4) * 2048 + (lane & 15) * 16);
                unsigned s0a = (unsigned)(lane * 4), s1a = (unsigned)(256 + (lane >> 5) * 64 + (lane & 15) * 4);
                ml_addr(lane, x2a, r2a);
                PIN3(x0a, x1a, x2a); PIN3(r0a, r1a, r2a); PIN2(s0a, s1a);
                cma = lb + L_M; cxa = wave == 0 ? x0a : wave == 1 ? x1a : x2a; cra = wave == 0 ? r0a : wave == 1 ? r1a : r2a;
                cso = WS_OWN + rS + (unsigned)(gi * 16384 + 4096 + gj * 512) + (wave == 0 ? s0a : wave == 1 ? s1a : 384u);
                PIN2(cma, cxa); PIN2(cra, cso);
                asm volatile("" : "=v"(omr), "=v"(owa), "=v"(owb));
            }
        } else {
            const int t = wave - 3;
            StatAddr sa;
            u32x4 sel;
            unsigned long long hi = 0xff00ff00ff00ff00ull;
            auto gather32 = [&](unsigned o, unsigned X, auto fin) __attribute__((always_inline)) {
                u32x4 a0 = ld16_sc1<-2048>(ws, o), a1 = ld16_sc1<-1024>(ws, o), a2 = ld16_sc1<0>(ws, o), a3 = ld16_sc1<1024>(ws, o);
                float v;
                auto sum = [&]() __attribute__((always_inline)) {
                    asm volatile("s_waitcnt vmcnt(3)\n\tv_mfma_f32_16x16x32_bf16 v[40:43], %1, %5, 0\n\ts_waitcnt vmcnt(2)\n\tv_mfma_f32_16x16x32_bf16 v[40:43], %2, %5, v[40:43]\n\t"
                                 "s_waitcnt vmcnt(1)\n\tv_mfma_f32_16x16x32_bf16 v[40:43], %3, %5, v[40:43]\n\ts_waitcnt vmcnt(0)\n\tv_mfma_f32_16x16x32_bf16 v[40:43], %4, %5, v[40:43]\n\t"
                                 "s_nop 7\n\tv_cndmask_b32 v40, v40, v41, %6\n\tv_cndmask_b32 v42, v42, v43, %6\n\ts_nop 1\n\tv_permlane32_swap_b32 v40, v42\n\tv_add_f32 v40, v40, v42\n\t"
                                 "v_cvt_pk_bf16_f32 %0, 0, v40"
                                 : "=&v"(v), "+v"(a0), "+v"(a1), "+v"(a2), "+v"(a3) : "v"(sel), "s"(hi) : "memory", "v40", "v41", "v42", "v43");
                    fin(v);
                    __builtin_amdgcn_sched_barrier(0);
                };
                sum();
                if (__builtin_expect(any_nan(v, v) != 0ull, 0)) {
#pragma unroll 1
                    for (unsigned spins = 0; spins < kSpinCap; ++spins) {
                        slip(X);
                        again16<-2048>(a0, ws, o); again16<-1024>(a1, ws, o); again16<0>(a2, ws, o); again16<1024>(a3, ws, o);
                        asm volatile("s_waitcnt vmcnt(0)" : "+v"(a0), "+v"(a1), "+v"(a2), "+v"(a3) :: "memory");
                        if (!any_empty(max(max(um4(a0), um4(a1)), max(um4(a2), um4(a3))))) break;
                    }
                    sum();
                }
            };
            auto poll32 = [&](unsigned o, unsigned t0) __attribute__((always_inline)) {
                    unsigned long long m0, m1, m2, m3; unsigned cnt = kSpinCap; float v;
#define CHK(r, m) "v_max3_u32 v44, v" #r ", v[" #r "+1], v[" #r "+2]\n\tv_max_u32 v44, v44, v[" #r "+3]\n\tv_cmp_eq_u32 %" #m ", -1, v44\n\t"
                    asm volatile("s_branch 3f\n"
                                 "1:\n\ts_mov_b64 exec, %1\n\tglobal_load_dwordx4 v[0:3], %6, %7 offset:-2048 sc1\n\ts_mov_b64 exec, %2\n\tglobal_load_dwordx4 v[4:7], %6, %7 offset:-1024 sc1\n\t"
                                 "s_mov_b64 exec, %3\n\tglobal_load_dwordx4 v[8:11], %6, %7 sc1\n\ts_mov_b64 exec, %4\n\tglobal_load_dwordx4 v[12:15], %6, %7 offset:1024 sc1\n\ts_mov_b64 exec, -1\n\t"
                                 "s_sub_u32 %5, %5, 1\n\ts_cselect_b64 vcc, 0, -1\n\ts_waitcnt vmcnt(0)\n\ts_branch 2f\n"
                                 "3:\n\ts_memrealtime vcc\n\ts_waitcnt lgkmcnt(0)\n\ts_sub_u32 vcc_lo, %10, vcc_lo\n\ts_cmp_lt_u32 vcc_lo, 0x400\n\ts_cbranch_scc1 3b\n\t"
                                 "global_load_dwordx4 v[0:3], %6, %7 offset:-2048 sc1\n\tglobal_load_dwordx4 v[4:7], %6, %7 offset:-1024 sc1\n\t"
                                 "global_load_dwordx4 v[8:11], %6, %7 sc1\n\tglobal_load_dwordx4 v[12:15], %6, %7 offset:1024 sc1\n\ts_mov_b64 vcc, -1\n"
                                 "2:\n\ts_waitcnt vmcnt(3)\n\tv_mfma_f32_16x16x32_bf16 v[40:43], v[0:3], %8, 0\n\t" CHK(0, 1) "s_waitcnt vmcnt(2)\n\tv_mfma_f32_16x16x32_bf16 v[40:43], v[4:7], %8, v[40:43]\n\t" CHK(4, 2)
                                 "s_waitcnt vmcnt(1)\n\tv_mfma_f32_16x16x32_bf16 v[40:43], v[8:11], %8, v[40:43]\n\t" CHK(8, 3) "s_waitcnt vmcnt(0)\n\tv_mfma_f32_16x16x32_bf16 v[40:43], v[12:15], %8, v[40:43]\n\t" CHK(12, 4)
                                 "s_or_b64 exec, %1, %2\n\ts_or_b64 exec, exec, %3\n\ts_or_b64 exec, exec, %4\n\ts_and_b64 vcc, exec, vcc\n\ts_cbranch_scc1 1b\n\t"
                                 "s_mov_b64 exec, -1\n\tv_cndmask_b32 v40, v40, v41, %9\n\tv_cndmask_b32 v42, v42, v43, %9\n\ts_nop 1\n\tv_permlane32_swap_b32 v40, v42\n\tv_add_f32 v40, v40, v42\n\t"
                                 "v_cvt_pk_bf16_f32 %0, 0, v40"
                                 : "=&v"(v), "=&s"(m0), "=&s"(m1), "=&s"(m2), "=&s"(m3), "+s"(cnt) : "v"(o), "s"(ws), "v"(sel), "s"(hi), "s"(t0 - 1u)
                                 : "memory", "vcc", "scc", "v0", "v1", "v2", "v3", "v4", "v5", "v6", "v7", "v8", "v9", "v10", "v11", "v12", "v13", "v14", "v15", "v40", "v41", "v42", "v43", "v44");
#undef CHK
                return v;
            };
#define PUT8(A, B, M) "v_min_u32 v34, -2, v32\n\tv_min_u32 v32, -2, v32\n\tv_min_u32 v35, -2, v33\n\tv_min_u32 v33, -2, v33\n\tv_permlane16_swap_b32 v32, v34\n\ts_nop 0\n\t" \
                      "v_permlane16_swap_b32 v33, v35\n\ts_mov_b64 exec, " M "\n\tglobal_store_dwordx4 " A ", v[32:35], " B " sc1\n\ts_mov_b64 exec, -1\n\ts_nop 0"
            unsigned xw, qo, qw, qc;
            __builtin_amdgcn_s_setprio(0);
            {
                STAMP(6 + 32 * layer);
                bf16x8 af[6];
                unsigned long long mk = 0x000f000f000f000full;
                if (layer == 0) {
                    const int lane = lane_id();
                    xe = lo16(*reinterpret_cast<const u16*>(smem + L_X0 + xrow(lane) * 1280 + gj * 160 + t * 32 + xcol(lane) * 2));
                    op_addr(0);
                    GUARD(1600);
                    MARK(b_op);
                    if (nj | (ord == OML)) { VMW(0); lds_add(44, nj); }
#pragma unroll 1
                    for (unsigned spins = 0; spins < (kSpinCap << 4); ++spins) if (lds_word(lb + L_CNT + 44) >= 5u) break;
                    STAMP(8);
                } else MARK(b_op);
                const unsigned lo = cma, lw = cxa, so = cso, br = cra, mr = omr, h3 = tile35(gj, 10 * layer + t);
                unsigned go, xs;
#pragma unroll
                for (int s = 0; s < 6; ++s) af[s] = cat8(tr64o(owa, s * 32 * 160), tr64o(owb, s * 32 * 160));
                PINS(mk); PINS(hi);
                if (layer != 0 && t < 4) {
                    sched(X2, 0);
                    STAMP(49);
                    u32x4 ov = ld16_sc1(ws, lo);
                    WAIT1(ov, 0);
                    *(LAS u32x4*)(size_t)lw = ov;
                    if (__builtin_expect(any_empty(um4(ov)), 0)) {
#pragma unroll 1
                        for (unsigned spins = 0; spins < kSpinCap; ++spins) {
                            slip(X2);
                            ov = ld16_sc1(ws, lo);
                            WAIT1(ov, 0);
                            *(LAS u32x4*)(size_t)lw = ov;
                            if (!any_empty(um4(ov))) break;
                        }
                    }
                }
                {
                    bf16x8 b0, b1, b2, b3, b4, b5;
#define QP " row_half_mirror row_mask:0xf bank_mask:0xf\n\t"
                    asm volatile("s_waitcnt lgkmcnt(0)\n\ts_barrier\n\t"
                                 "ds_read_b128 %0, %12\n\tds_read_b64 v[36:37], %13\n\tds_read_b128 %1, %12 offset:64\n\tds_read_b128 %2, %12 offset:128\n\tds_read_b128 %3, %12 offset:192\n\t"
                                 "ds_read_b128 %4, %12 offset:256\n\tds_read_b128 %5, %12 offset:320\n\t"
                                 "s_waitcnt lgkmcnt(5)\n\tv_mfma_f32_16x16x32_bf16 v[32:35], %6, %0, 0\n\tv_max_f32_dpp v38, v36, v36" QP "v_sub_f32 v38, v36, v38\n\t"
                                 "s_waitcnt lgkmcnt(4)\n\tv_mfma_f32_16x16x32_bf16 v[32:35], %7, %1, v[32:35]\n\tv_exp_f32 v38, v38\n\t"
                                 "s_waitcnt lgkmcnt(3)\n\tv_mfma_f32_16x16x32_bf16 v[32:35], %8, %2, v[32:35]\n\tv_mul_f32 v37, v37, v38\n\t"
                                 "s_waitcnt lgkmcnt(2)\n\tv_mfma_f32_16x16x32_bf16 v[32:35], %9, %3, v[32:35]\n\tv_add_f32_dpp v39, v37, v37" QP
                                 "s_waitcnt lgkmcnt(1)\n\tv_mfma_f32_16x16x32_bf16 v[32:35], %10, %4, v[32:35]\n\tv_rcp_f32 v39, v39\n\t"
                                 "s_waitcnt lgkmcnt(0)\n\tv_mfma_f32_16x16x32_bf16 v[32:35], %11, %5, v[32:35]\n\tv_mul_f32 v38, v38, v39\n\ts_nop 6\n\t"
                                 "v_pk_mul_f32 v[32:33], v[32:33], v[38:39] op_sel_hi:[1,0]\n\tv_pk_mul_f32 v[34:35], v[34:35], v[38:39] op_sel_hi:[1,0]\n\ts_nop 0\n\t"
                                 "v_add_f32_dpp v32, v32, v32" QP "v_add_f32_dpp v33, v33, v33" QP "v_add_f32_dpp v34, v34, v34" QP "v_add_f32_dpp v35, v35, v35" QP
                                 "v_cvt_pk_bf16_f32 v32, v32, v33\n\tv_cvt_pk_bf16_f32 v33, v34, v35\n\t" PUT8("%14", "%15", "%16")
                                 : "=&v"(b0), "=&v"(b1), "=&v"(b2), "=&v"(b3), "=&v"(b4), "=&v"(b5)
                                 : "v"(af[0]), "v"(af[1]), "v"(af[2]), "v"(af[3]), "v"(af[4]), "v"(af[5]), "v"(br), "v"(mr), "v"(so), "s"(ws), "s"(mk)
                                 : "memory", "v32", "v33", "v34", "v35", "v36", "v37", "v38", "v39");
#undef QP
                }
                if (layer == 0 && wave == 7) st4_sc1(ws, WS_MB + (unsigned)(gi * 1024 + 64 + gj * 4), now() | 1u);
                const unsigned tp = layer ? 0u : now();
                if (layer == 0) sides();
                STAMP(7 + 32 * layer);
                {
                    const int ln = lane_id();
#pragma unroll
                    for (int k = 0; k < 4; ++k) sel[k] = ((ln >> 1) & 3) == k ? (ln & 1) ? 0x3f800000u : 0x3f80u : 0u;
                    go = h3 + nS + (unsigned)(2048 + ln * 16); xs = lb + L_XS + (unsigned)(xrow(ln) * 160 + t * 32 + xcol(ln) * 2);
                    PIN3(sel, go, xs);
                    sa = stats_addr(ln, h4 + (unsigned)(H4T + gj * 128), 16);
                }
                if (layer != 0) sched(X3, 0);
                STAMP(18 + 32 * layer);
                if (layer != 0) gather32(go, X3, [&](float s) { xw = pk2(xe + s, 0.f); *(LAS u16*)(size_t)xs = (u16)xw; });
                else { xw = pk2(xe + poll32(go, tp + W3), 0.f); *(LAS u16*)(size_t)xs = (u16)xw; }
                xe = lo16(xw);
                STAMP(9 + 32 * layer);
            }
            BAR();
            if (wave == 3) stats(sa);
            MARK(e_op);
            STAMP(11 + 32 * layer);
            {
                LANE_VARS();
                const unsigned im = lb + L_WD + (unsigned)(32 * t + 8 * p4);
                bf16x8 wd0 = cat8(tr64(im + r0s * 160), tr64(im + r1s * 160)); bf16x4 wd1 = tr64(im + (32 + r2s) * 160);
                const unsigned h5 = tile35(gj, 10 * layer + 5 + t);
                unsigned h16 = lb + L_H + (unsigned)(p4 * 96 + kq * 16), h8 = lb + L_H + (unsigned)(p4 * 96 + 64 + kq * 8);
                unsigned so = h5 + (unsigned)((kq & 1) * 8192 + gi * 128 + (kq >> 1) * 64 + p4 * 16);
                unsigned long long mk = 0x000f000f000f000full;
                unsigned go = h5 + nS + (unsigned)(2048 + lane * 16), xs = (layer ? (unsigned)(xrow(lane) * 1280 + gj * 160) : lb + L_XS + (unsigned)(xrow(lane) * 160)) + (unsigned)(t * 32 + xcol(lane) * 2);
                unsigned sta = lb + L_ST + (unsigned)(xrow(lane) * 16);
                if (layer == 0 && wave == 4) {
                    asm volatile("" : "=v"(sa.a16), "=v"(sa.b16), "=v"(sa.a8), "=v"(sa.b8), "=v"(sa.dst), "=v"(qo), "=v"(qw), "=v"(qc), "=v"(cma), "=v"(cxa), "=v"(cra), "=v"(cso), "=v"(omr), "=v"(owa), "=v"(owb));
                    asm volatile("" : "=v"(qa), "=v"(pw), "=v"(ma), "=v"(idx), "=v"(qm), "=v"(pr), "=v"(oa), "=v"(oc));
                } else if (layer == 0) {
                    sa = stats_addr(lane, WS_H1 + rS + (unsigned)(gi * 65536 + gj * 896 + 768), 16384);
                    op_addr(1); qk_addr(false); att_addr();
                    const int l5 = lane & 31, c = l5 < 24 ? 24 * (wave - 6) + l5 : 48;
                    qo = WS_H1 + rS + (unsigned)((gi * 4 + (gj >> 1)) * 16384 + 3584 + (lane >> 5) * 896 + c * 16);
                    qw = lb + L_Q + (unsigned)(l5 < 24 ? (c >> 4) * 128 + (c & 3) * 32 + (c & 12) + (lane >> 5) * 16 : lane < 26 ? 384 + (lane & 1) * 16 : 448);
                    qc = lb + L_CS + (unsigned)(c * 16 + (lane >> 5) * 8);
                    PIN3(qo, qw, qc);
                }
                PIN2(wd0, wd1); PIN3(h16, h8, so); PIN3(go, xs, sta); PINS(mk);
                GUARD(2000);
                MARK(b_dn);
                if (wave == 4) {
                    unsigned os = h4 + (unsigned)(H4T + (p4 + 4 * (kq & 1)) * 128 + q4 * 16);
                    float sw = *reinterpret_cast<const float*>(smem + L_CNT + 12);
                    PIN2(os, sw);
                    if (layer == 0) {
#pragma unroll 1
                        for (unsigned spins = 0; spins < (kSpinCap << 4); ++spins) if (lds_word(lb + L_CNT + 56)) break;
                    }
                    sched(layer ? X4 : X4A, 0);
                    u32x4 as;
#pragma unroll 1
                    for (unsigned spins = 0; spins < kSpinCap; ++spins) { as = ld16_sc1(ws, os); WAIT1(as, 0); if (!any_empty(um4(as))) break; }
                    float ss = __uint_as_float(as[0]), sm = __uint_as_float(as[1]), rd = __uint_as_float(as[2]);
                    ss += dpp<0x4E>(ss); sm += dpp<0x4E>(sm); rd += dpp<0x4E>(rd);
                    ss += dpp<0xB1>(ss); sm += dpp<0xB1>(sm); rd += dpp<0xB1>(rd);
                    ss = x16_add(ss); sm = x16_add(sm); rd = x16_add(rd);
                    if (LANES(0x1111ull)) *reinterpret_cast<f32x4*>(smem + L_ST + q4 * 16) = (f32x4){ss, sm, rd, sw};
                }
                if (layer == 0 && wave > 4) {
#pragma unroll 1
                    for (unsigned spins = 0; spins < (kSpinCap << 4); ++spins) { if (lds_word(lb + L_CNT + 56)) break; __builtin_amdgcn_s_sleep(1); }
                    sched(X4A, 5);
                    if (wave == 5) empty4(1, rS, lane);
                    else st16m(ws, lane < 40 ? tile35(gj, 15 + (lane >> 3)) + (unsigned)((wave - 6) * 8192 + gi * 128 + (lane & 7) * 16)
                                             : WS_OWN + rS + (unsigned)(gi * 16384 + 4096 + gj * 512 + (wave - 6) * 256 + (lane - 40) * 16), (u32x4){~0u, ~0u, ~0u, ~0u}, 0xffffffffffffffull);
                }
                asm volatile("s_waitcnt lgkmcnt(0)\n\ts_barrier\n\tds_read_b128 v[36:39], %2\n\tds_read_b64 v[40:41], %3\n\t"
                             "s_waitcnt lgkmcnt(1)\n\tv_mfma_f32_16x16x32_bf16 v[32:35], %0, v[36:39], 0\n\ts_waitcnt lgkmcnt(0)\n\tv_mfma_f32_16x16x16_bf16 v[42:45], %1, v[40:41], 0\n\ts_nop 7\n\t"
                             "v_pk_add_f32 v[32:33], v[32:33], v[42:43]\n\tv_pk_add_f32 v[34:35], v[34:35], v[44:45]\n\t"
                             "v_cvt_pk_bf16_f32 v32, v32, v33\n\tv_cvt_pk_bf16_f32 v33, v34, v35\n\t" PUT8("%4", "%5", "%6")
                             :: "v"(wd0), "v"(wd1), "v"(h16), "v"(h8), "v"(so), "s"(ws), "s"(mk)
                             : "memory", "v32", "v33", "v34", "v35", "v36", "v37", "v38", "v39", "v40", "v41", "v42", "v43", "v44", "v45");
                STAMP(13 + 33 * layer);
                if (layer == 0 || __builtin_expect(gi == 20 + 2 * t + (gj & 1), 1)) {
                    const f32x4 sr = *(const LAS f32x4*)(size_t)sta;
                    const f32x2 st = ln_stats(sr[0], sr[1]);
                    float rs = bfr(frcp(1.f + ex2(bfr((sr[2] - st[0] * sr[3]) * st[1]) * -LOG2E))), n1 = bfr(bfr(xe - st[0]) * st[1]);
                    PIN2(rs, n1);
                    if (layer == 0) {
                        sched(X5A, 0);
                        STAMP(15);
                        gather32(go, X5A, [&](float y) { xw = pk2(xe + bfr(n1 + bfr(y * rs)), 0.f); *(LAS u16*)(size_t)xs = (u16)xw; });
                        xe = lo16(xw);
                        STAMP(14);
                    } else {
                        st2(out, xs, pk2(xe + bfr(n1 + bfr(poll32(go, when(X5B)) * rs)), 0.f));
                        asm volatile("s_endpgm");
                        MARK(e_dn);
                        STAMP(45);
                    }
                }
            }
            if (layer == 0) {
                BAR();
                if (wave == 3) { stats(sa); STAMP(34); }
                MARK(e_dn);
                if (wave == 3) __builtin_amdgcn_s_setprio(2);
                if (__builtin_expect(wave == 4, 0)) { op_addr(1); qk_addr(false); att_addr(); }
                if (wave >= 6) {
                    const unsigned o = qo;
                    f32x2 cs = -*(const LAS f32x2*)(size_t)qc;
                    float rn = 1.f / DM;
                    unsigned long long m0 = 1ull << 24, m1 = 1ull << 25;
                    PIN2(cs, rn); PINS(m0); PINS(m1);
                    MARK(b_qg);
                    sched(X1, 0);
                    STAMP(51);
                    u32x4 a0, a1, a2, a3; f32x2 u;
                    RETRY({ a0 = ld16_sc1<-3584>(ws, o); a1 = ld16_sc1<-1792>(ws, o); a2 = ld16_sc1<0>(ws, o); a3 = ld16_sc1<1792>(ws, o); },
                          ({ asm volatile("s_waitcnt vmcnt(0)" : "+v"(a0), "+v"(a1), "+v"(a2), "+v"(a3) :: "memory"); max(max(um4(a0), um4(a1)), max(um4(a2), um4(a3))); }),
                          { unsigned sm;
                            asm volatile("s_waitcnt vmcnt(2)\n\tv_pk_add_f32 v[40:41], %2, %4\n\tv_pk_add_f32 v[42:43], %3, %5\n\t"
                                         "s_waitcnt vmcnt(1)\n\tv_pk_add_f32 v[40:41], v[40:41], %6\n\tv_pk_add_f32 v[42:43], v[42:43], %7\n\t"
                                         "s_waitcnt vmcnt(0)\n\tv_pk_add_f32 v[40:41], v[40:41], %8\n\tv_pk_add_f32 v[42:43], v[42:43], %9\n\ts_nop 1\n\t"
                                         "v_permlane32_swap_b32 v40, v42\n\tv_permlane32_swap_b32 v41, v43\n\tv_pk_add_f32 v[40:41], v[40:41], v[42:43]\n\ts_nop 0\n\t"
                                         "v_readlane_b32 %1, v41, 24\n\ts_nop 1\n\tv_mul_f32 v42, %1, %11\n\tv_cvt_pk_bf16_f32 v42, 0, v42\n\tv_pk_fma_f32 %0, %10, v[42:43], v[40:41] op_sel_hi:[1,0,1]"
                                         : "=&v"(u), "=&s"(sm) : "v"(LO2(a0)), "v"(HI2(a0)), "v"(LO2(a1)), "v"(HI2(a1)), "v"(LO2(a2)), "v"(HI2(a2)), "v"(LO2(a3)), "v"(HI2(a3)), "v"(cs), "v"(rn)
                                         : "memory", "v40", "v41", "v42", "v43");
                            unsigned w = pk2v(u);
                            if (LANES(m0)) w = __float_as_uint(u[0]);
                            if (LANES(m1)) w = __float_as_uint(u[1]);
                            *(LAS unsigned*)(size_t)qw = w; __builtin_amdgcn_sched_barrier(0); }, any_nan(u[0], u[1]), X1);
                    MARK(e_qg);
                }
            }
        }
    };
    PINS(ck1); PINS(ck2); PINS(csc);
    chain(IC<0>{});
    STAMP(35);
    att1();
    chain(IC<1>{});
}

__device__ __attribute__((used, noinline)) void mega_code_end() { asm volatile(".Lmega_code_end:"); }

extern "C" void kernel_launch(void* const* d_in, const int* in_sizes, int n_in, void* d_out, int out_size, void* d_ws,
                              size_t ws_size, hipStream_t stream) {
    const u16* x0 = (const u16*)d_in[0];
    const u16* kc = (const u16*)d_in[1];
    const u16* vc = (const u16*)d_in[2];
    const u16* wq = (const u16*)d_in[3];
    const u16* wo = (const u16*)d_in[4];
    const u16* wr = (const u16*)d_in[6];
    const u16* wg = (const u16*)d_in[7];
    const u16* wu = (const u16*)d_in[8];
    const u16* wd = (const u16*)d_in[9];
    (void)hipFuncSetAttribute((const void*)mega_kernel, hipFuncAttributeMaxDynamicSharedMemorySize, L_END);
    mega_kernel<<<256, 512, L_END, stream>>>(x0, kc, vc, wq, wo, wr, wg, wu, wd, (u16*)d_out, (unsigned char*)d_ws
                                             );
}
```

```cpp
#define DM_ 1
#include <hip/hip_runtime.h>
#include <hip/hip_bf16.h>
#include <stdint.h>

typedef unsigned short u16;
typedef unsigned u32x4 __attribute__((ext_vector_type(4)));
typedef unsigned u32x2 __attribute__((ext_vector_type(2)));
typedef unsigned long long u64x8 __attribute__((ext_vector_type(8)));
typedef unsigned long long u64x2 __attribute__((ext_vector_type(2)));
typedef float f32x4 __attribute__((ext_vector_type(4)));
typedef short s16x4 __attribute__((ext_vector_type(4)));
typedef __attribute__((ext_vector_type(4))) __bf16 bf16x4;
typedef __attribute__((ext_vector_type(8))) __bf16 bf16x8;
#define LAS __attribute__((address_space(3)))

constexpr int DM = 640, HQ = 32, SK = 4096, DH = 192, WIN = 1024, FF = 1536;
constexpr int QD = HQ * DH;
constexpr float SCALE = 0.07216878364870322f, LOG2E = 1.44269504088896341f;
constexpr unsigned kSpinCap = 1u << 16;
#define GJ(g) (((g) >> 3) & 7)
#define GI(g) (((g) & 7) * 4 + ((g) >> 6))

#define D4A 104
#define D5A 90
#define D1 84
#define D2 132
#define D3 64
#define D4 76
#define D5B 76
#define PACE 59
#define PINIT 100
#define PMIN 50
constexpr unsigned X4A = D4A + DM_, X5A = X4A + D5A + DM_, X1 = X5A + D1 + DM_, X2 = X1 + D2 + DM_, X3 = X2 + D3 + DM_, X4 = X3 + D4 + DM_, X5B = X4 + D5B + DM_;
#define TLAND 132
#define XCOST 8
#define WOFF 6
#define BETA 11
constexpr unsigned WOC = 2, WEARLY = 1;
constexpr unsigned NUNIT = 384 + XCOST + 3 * (9 + 2 * WEARLY), PBURST = 30;
constexpr int LEAD = 12, TOFF = 4;
constexpr unsigned DG2 = 12, W3 = 15;
constexpr int GP = 0, GA = 1, GB = 6, GC = 7, HD = 1;
#define VORD 0, 1, 8, 2, 3, 9, 4, 5, 6, 7, 10, 11
constexpr int kVord[12] = {VORD};
constexpr int v_upto(int h) { int n = 0; for (int i = 0; i <= h; ++i) n += kVord[i] < 8 ? 2 : 4; return n; }
constexpr int v_after(int p) { int h = 0; while (kVord[h] != p) ++h; return 32 - v_upto(h); }
constexpr int OML = 2;

constexpr unsigned WS_H1 = 0;
constexpr unsigned WS_H4 = 2u << 20;
constexpr unsigned H4C = 20480, H4T = H4C + 3072;
constexpr unsigned WS_H35 = 4u << 20;
constexpr unsigned WS_OWN = WS_H35 + 80 * 16384;
constexpr unsigned WS_CS = WS_OWN + 32 * 16384;
constexpr unsigned WS_MB = WS_CS + 256 * 768;
constexpr unsigned WS_R = WS_MB + 256 * 128;
constexpr unsigned WS_END = WS_R + 128;
constexpr unsigned WS_Q0 = 6u << 20;
static_assert(WS_END <= (6u << 20), "ws map");
__device__ __forceinline__ unsigned tile35(int j, int q) { return WS_H35 + (unsigned)((j * 10 + (q >> 1)) * 16384 + (q & 1) * 4096); }
__device__ __forceinline__ unsigned h4s(int s) { return (unsigned)((s >> 1) * 16384 + (s & 1) * 4096); }

constexpr int L_WQ = 0;
constexpr int L_WO = 33792;
constexpr int L_GU = L_WO + 30720;
constexpr int L_WD = L_GU + 16384;
constexpr int L_X0 = L_WD + 8192;
constexpr int L_WR = L_X0 + 5120;
constexpr int L_XS = L_WR + 2048;
constexpr int L_Q = L_XS + 1024;
constexpr int L_OP = L_Q + 512;
constexpr int L_M = L_OP + 8192;
constexpr int L_H = L_M + 64;
constexpr int L_ST = L_H + 384;
constexpr int L_CS = L_ST + 64;
constexpr int L_CS2 = L_CS + 1024;
constexpr int L_CNT = L_CS2 + 1536;
constexpr int L_P = L_CNT + 96;
constexpr int L_O2 = L_P + 72 * 192;
constexpr int L_DBG = L_O2 + 3328;
constexpr int L_END = L_DBG + 2048;
static_assert(L_END <= 163840 && L_END > 81920, "LDS");

typedef __attribute__((ext_vector_type(2))) __bf16 bf16x2;
typedef float f32x2 __attribute__((ext_vector_type(2)));
__device__ __forceinline__ unsigned pk2(float a, float b) { return __builtin_bit_cast(unsigned, __builtin_convertvector((f32x2){a, b}, bf16x2)); }
__device__ __forceinline__ unsigned fexp(float x) { const unsigned e = (__float_as_uint(x) >> 23) & 255u; return e == 255u ? 0u : e; }
__device__ __forceinline__ u32x2 enc21(float a, float b, float c) {
    const float lim = 1048575.f;
    const unsigned i = (unsigned)(int)__builtin_rintf(__builtin_amdgcn_fmed3f(a, -lim, lim)) & 0x1fffffu, j = (unsigned)(int)__builtin_rintf(__builtin_amdgcn_fmed3f(b, -lim, lim)) & 0x1fffffu;
    const unsigned k = (unsigned)(int)__builtin_rintf(__builtin_amdgcn_fmed3f(c, -lim, lim)) & 0x1fffffu;
    return (u32x2){i | j << 21, j >> 11 | k << 10 | (__builtin_fabsf(a) + __builtin_fabsf(b) + __builtin_fabsf(c) < 1e30f ? 0u : 0x80000000u)};
}
__device__ __forceinline__ float lo16(unsigned w) { return __uint_as_float(w << 16); }
__device__ __forceinline__ float hi16(unsigned w) { return __uint_as_float(w & 0xffff0000u); }
__device__ __forceinline__ float bfr(float f) { return __uint_as_float(pk2(0.f, f)); }
template <int CTRL> __device__ __forceinline__ float dpp(float x) {
    return __builtin_bit_cast(float, __builtin_amdgcn_mov_dpp(__builtin_bit_cast(int, x), CTRL, 0xf, 0xf, true));
}
template <int CTRL> __device__ __forceinline__ unsigned dppu(unsigned x) { return (unsigned)__builtin_amdgcn_mov_dpp((int)x, CTRL, 0xf, 0xf, true); }
__device__ __forceinline__ float row16_sum(float v) {
    v += dpp<0xB1>(v); v += dpp<0x4E>(v); v += dpp<0x141>(v); v += dpp<0x140>(v); return v;
}
__device__ __forceinline__ float fmax2(float a, float b) { return __builtin_elementwise_maximum(a, b); }
__device__ __forceinline__ float fmax3(float a, float b, float c) { return fmax2(fmax2(a, b), c); }
__device__ __forceinline__ float x16_add(float v) { auto s = __builtin_amdgcn_permlane16_swap(__float_as_uint(v), __float_as_uint(v), false, false); return __uint_as_float(s[0]) + __uint_as_float(s[1]); }
__device__ __forceinline__ float x32_add(float v) { auto s = __builtin_amdgcn_permlane32_swap(__float_as_uint(v), __float_as_uint(v), false, false); return __uint_as_float(s[0]) + __uint_as_float(s[1]); }
__device__ __forceinline__ float wave_sum(float v) { return x32_add(x16_add(row16_sum(v))); }
__device__ __forceinline__ float rs32(float a, float b) { auto s = __builtin_amdgcn_permlane32_swap(__float_as_uint(a), __float_as_uint(b), false, false); return __uint_as_float(s[0]) + __uint_as_float(s[1]); }

#define GUARD(n) asm volatile("s_branch 1f\n\t.p2align 12,," #n "\n1:")
#define MARK(n) asm volatile(".Lcrit_" #n "_%=:" ::)
#define BAR() asm volatile("s_waitcnt lgkmcnt(0)\n\ts_barrier" ::: "memory")
#define VMW(n) asm volatile("s_waitcnt vmcnt(%0)" :: "i"(n) : "memory")

template <int OFF = 0> __device__ __forceinline__ u32x4 ld16_sc1(const void* sbase, unsigned voff) {
    u32x4 r; asm volatile("global_load_dwordx4 %0, %1, %2 offset:%3 sc1" : "=v"(r) : "v"(voff), "s"(sbase), "i"(OFF) : "memory"); return r;
}
__device__ __forceinline__ unsigned ld4_sc1(const void* sbase, unsigned voff) {
    unsigned r; asm volatile("global_load_dword %0, %1, %2 sc1" : "=v"(r) : "v"(voff), "s"(sbase) : "memory"); return r;
}
template <int OFF = 0> __device__ __forceinline__ void st16_sc1(void* sbase, unsigned voff, u32x4 v) {
    asm volatile("global_store_dwordx4 %0, %1, %2 offset:%3 sc1\n\ts_nop 1" :: "v"(voff), "v"(v), "s"(sbase), "i"(OFF) : "memory");
}
__device__ __forceinline__ void st4_sc1(void* sbase, unsigned voff, unsigned v) {
    asm volatile("global_store_dword %0, %1, %2 sc1" :: "v"(voff), "v"(v), "s"(sbase) : "memory");
}
__device__ __forceinline__ void st8(void* sbase, unsigned voff, u32x2 v) {
    asm volatile("global_store_dwordx2 %0, %1, %2" :: "v"(voff), "v"(v), "s"(sbase) : "memory");
}
__device__ __forceinline__ void st2(void* sbase, unsigned voff, unsigned v) {
    asm volatile("global_store_short %0, %1, %2" :: "v"(voff), "v"(v), "s"(sbase) : "memory");
}
constexpr int RK = 64, RV = 160;
template <int R, int IMM> __device__ __forceinline__ void ld16_nt(const void* sbase, unsigned voff) {
    asm volatile("global_load_dwordx4 v[%c3:%c4], %0, %1 offset:%2 nt" :: "v"(voff), "s"(sbase), "i"(IMM), "i"(R), "i"(R + 3) : "memory");
}
template <int R, int IMM> __device__ __forceinline__ void ld8_nt(const void* sbase, unsigned voff) {
    asm volatile("global_load_dwordx2 v[%c3:%c4], %0, %1 offset:%2 nt" :: "v"(voff), "s"(sbase), "i"(IMM), "i"(R), "i"(R + 1) : "memory");
}
template <int R> __device__ __forceinline__ void smf(f32x4& d, u32x4 a, unsigned idx) {
    asm volatile("v_smfmac_f32_16x16x64_bf16 %0, %1, v[%c3:%c4], %2" : "+v"(d) : "v"(a), "v"(idx), "i"(R), "i"(R + 7));
}
template <int A, int B> __device__ __forceinline__ void zip_in() {
    unsigned t;
    asm volatile("v_perm_b32 %0, v%c2, v%c1, %3\n\tv_perm_b32 v%c2, v%c2, v%c1, %4\n\tv_mov_b32 v%c1, %0" : "=&v"(t) : "i"(A), "i"(B), "s"(0x05040100u), "s"(0x07060302u));
}
template <int A, int B> __device__ __forceinline__ void zip_out(unsigned& lo, unsigned& hi) {
    asm volatile("v_perm_b32 %0, v%c3, v%c2, %4\n\tv_perm_b32 %1, v%c3, v%c2, %5" : "=&v"(lo), "=&v"(hi) : "i"(A), "i"(B), "s"(0x05040100u), "s"(0x07060302u));
}
template <int A, int B, int C, int D> __device__ __forceinline__ void zip_x(unsigned lo, unsigned hi) {
    asm volatile("v_perm_b32 v%c4, v%c3, v%c2, %6\n\tv_perm_b32 v%c5, v%c3, v%c2, %7\n\tv_mov_b32 v%c2, %0\n\tv_mov_b32 v%c3, %1"
                 :: "v"(lo), "v"(hi), "i"(A), "i"(B), "i"(C), "i"(D), "s"(0x05040100u), "s"(0x07060302u));
}
__device__ __forceinline__ void dma16(const void* sbase, unsigned voff, unsigned lds_dst) {
    unsigned keep;
    asm volatile("s_mov_b32 %0, m0\n\ts_mov_b32 m0, %3\n\ts_nop 0\n\tglobal_load_lds_dwordx4 %1, %2\n\ts_mov_b32 m0, %0"
                 : "=&s"(keep) : "v"(voff), "s"(sbase), "s"(lds_dst) : "memory");
}
__device__ __forceinline__ unsigned um4(u32x4 a) { return max(max(a[0], a[1]), max(a[2], a[3])); }
__device__ __forceinline__ bool all_set(unsigned t) { return __builtin_amdgcn_ballot_w64(t == 0u) == 0ull; }
__device__ __forceinline__ bool any_empty(unsigned mx) { return __builtin_amdgcn_ballot_w64(mx == ~0u) != 0ull; }
__device__ __forceinline__ unsigned long long any_nan(float a, float b) { return __builtin_amdgcn_ballot_w64(__builtin_isunordered(a, b)); }
__device__ __forceinline__ f32x4 asf(u32x4 a) { return __builtin_bit_cast(f32x4, a); }
__device__ __forceinline__ u32x4 asu(f32x4 a) { return __builtin_bit_cast(u32x4, a); }
#define LANES(m) __builtin_amdgcn_inverse_ballot_w64(m)

__device__ __forceinline__ bf16x4 tr64(unsigned lds_addr) { return __builtin_amdgcn_ds_read_tr16_b64_v4bf16((LAS bf16x4*)(size_t)lds_addr); }
__device__ __forceinline__ bf16x4 tr64o(unsigned lds_addr, int off) { return __builtin_amdgcn_ds_read_tr16_b64_v4bf16((LAS bf16x4*)((LAS char*)(size_t)lds_addr + off)); }
__device__ __forceinline__ bf16x8 cat8(bf16x4 a, bf16x4 b) { return __builtin_shufflevector(a, b, 0, 1, 2, 3, 4, 5, 6, 7); }
__device__ __forceinline__ bf16x8 lds16(unsigned a) { return *(const LAS bf16x8*)(size_t)a; }
__device__ __forceinline__ bf16x4 lds8(unsigned a) { return *(const LAS bf16x4*)(size_t)a; }
__device__ __forceinline__ f32x4 mfma32(bf16x8 a, bf16x8 b, f32x4 c) { return __builtin_amdgcn_mfma_f32_16x16x32_bf16(a, b, c, 0, 0, 0); }
__device__ __forceinline__ f32x4 mfma32u(u32x4 a, u32x4 b, f32x4 c) { return mfma32(__builtin_bit_cast(bf16x8, a), __builtin_bit_cast(bf16x8, b), c); }
__device__ __forceinline__ f32x4 mfma16(bf16x4 a, bf16x4 b, f32x4 c) {
    return __builtin_amdgcn_mfma_f32_16x16x16bf16_1k(__builtin_bit_cast(s16x4, a), __builtin_bit_cast(s16x4, b), c, 0, 0, 0);
}

#define STAMP(i) do {} while (0)

template <int OFF = 0> __device__ __forceinline__ void st16m(void* sbase, unsigned voff, u32x4 v, unsigned long long mask) {
    asm volatile("s_mov_b64 exec, %4\n\tglobal_store_dwordx4 %0, %1, %2 offset:%3 sc1\n\ts_mov_b64 exec, -1\n\ts_nop 0" :: "v"(voff), "v"(v), "s"(sbase), "i"(OFF), "s"(mask) : "memory");
}
template <int OFF> __device__ __forceinline__ void st16m2(void* sbase, unsigned voff, u32x4 v, unsigned voff2, u32x4 v2, unsigned long long mask) {
    asm volatile("s_mov_b64 exec, %6\n\tglobal_store_dwordx4 %0, %1, %2 sc1\n\tglobal_store_dwordx4 %4, %5, %2 offset:%3 sc1\n\ts_mov_b64 exec, -1\n\ts_nop 0"
                 :: "v"(voff), "v"(v), "s"(sbase), "i"(OFF), "v"(voff2), "v"(v2), "s"(mask) : "memory");
}
#define PINS(a) asm volatile("" : "+s"(a))
#define RETRY(LOAD, EMPTY, COMPUTE, NAN_, X) do { LOAD; COMPUTE; if (__builtin_expect((NAN_) != 0ull, 0)) { \
    _Pragma("unroll 1") for (unsigned spins_ = 0; spins_ < kSpinCap; ++spins_) { slip(X); LOAD; if (!any_empty(EMPTY)) break; } COMPUTE; } } while (0)
__device__ __forceinline__ unsigned now() { return (unsigned)__builtin_amdgcn_s_memrealtime(); }
__device__ __forceinline__ void until(unsigned t) {
    asm volatile("1:\n\ts_memrealtime vcc\n\ts_waitcnt lgkmcnt(0)\n\ts_sub_u32 vcc_lo, %0, vcc_lo\n\ts_cmp_lt_u32 vcc_lo, 0x400\n\ts_cbranch_scc1 1b" :: "s"(t - 1u) : "vcc", "scc", "memory");
}
__device__ __forceinline__ unsigned lds_word(unsigned a) {
    unsigned t_; asm volatile("ds_read_b32 %0, %1\n\ts_waitcnt lgkmcnt(0)\n\ts_nop 0" : "=v"(t_) : "v"(a) : "memory");
    return __builtin_amdgcn_readfirstlane(t_);
}
__device__ __forceinline__ void lds_set(unsigned a, unsigned v) { asm volatile("ds_write_b32 %0, %1" :: "v"(a), "v"(v) : "memory"); }
__device__ __forceinline__ int xrow(int lane) { return (lane >> 5) * 2 + ((lane >> 3) & 1); }
__device__ __forceinline__ int xcol(int lane) { return ((lane >> 4) & 1) * 8 + (lane & 7); }
__device__ __forceinline__ int swz(int r) { return r ^ (((r >> 3) & 1) << 2); }
__device__ __forceinline__ f32x2 unpk(unsigned w) { return (f32x2){lo16(w), hi16(w)}; }
__device__ __forceinline__ unsigned pk2v(f32x2 v) { return __builtin_bit_cast(unsigned, __builtin_convertvector(v, bf16x2)); }
__device__ __forceinline__ float frcp(float x) { return __builtin_amdgcn_rcpf(x); }
__device__ __forceinline__ float ex2(float x) { return __builtin_amdgcn_exp2f(x); }
__device__ __forceinline__ int lane_id() { int l; asm volatile("v_mbcnt_lo_u32_b32 %0, -1, 0\n\tv_mbcnt_hi_u32_b32 %0, -1, %0" : "=v"(l)); return l & 63; }
__device__ __forceinline__ f32x2 ln_stats(float ss, float s) {
    const float mean = s * (1.f / DM);
    const float var = fmax2(__builtin_fmaf(-mean, mean, ss * (1.f / DM)), 0.f);
    return (f32x2){bfr(mean), frcp(bfr(__builtin_amdgcn_sqrtf(bfr(bfr(var) + 1e-5f))))};
}
#define GATHER8(a, o, S) do { a[0] = ld16_sc1<-4 * (S)>(ws, o); a[1] = ld16_sc1<-3 * (S)>(ws, o); a[2] = ld16_sc1<-2 * (S)>(ws, o); a[3] = ld16_sc1<-(S)>(ws, o); \
    a[4] = ld16_sc1<0>(ws, o); a[5] = ld16_sc1<(S)>(ws, o); a[6] = ld16_sc1<2 * (S)>(ws, o); a[7] = ld16_sc1<3 * (S)>(ws, o); } while (0)
template <int OFF> __device__ __forceinline__ void again16(u32x4& r, const void* sbase, unsigned voff) {
    asm volatile("v_cmp_eq_u32 vcc, -1, %1\n\ts_mov_b64 exec, vcc\n\tglobal_load_dwordx4 %0, %2, %3 offset:%4 sc1\n\ts_mov_b64 exec, -1" : "+v"(r) : "v"(um4(r)), "v"(voff), "s"(sbase), "i"(OFF) : "memory", "vcc");
}
#define WAIT8(a, N) asm volatile("s_waitcnt vmcnt(%8)" : "+v"((a)[0]), "+v"((a)[1]), "+v"((a)[2]), "+v"((a)[3]), "+v"((a)[4]), "+v"((a)[5]), "+v"((a)[6]), "+v"((a)[7]) : "i"(N) : "memory")
#define SUM8(a) (((((((asf(a[0]) + asf(a[1])) + asf(a[2])) + asf(a[3])) + asf(a[4])) + asf(a[5])) + asf(a[6])) + asf(a[7]))
#define WAIT1(x, N) asm volatile("s_waitcnt vmcnt(%1)" : "+v"(x) : "i"(N) : "memory")
#define LO2(x) __builtin_shufflevector(x, x, 0, 1)
#define HI2(x) __builtin_shufflevector(x, x, 2, 3)
#define PIN1(a) asm volatile("" : "+v"(a))
#define PIN2(a, b) asm volatile("" : "+v"(a), "+v"(b))
#define PIN3(a, b, c) asm volatile("" : "+v"(a), "+v"(b), "+v"(c))
#define LANE_VARS() const int lane = lane_id(); \
    const int kq = lane >> 4, n = lane & 15, q4 = n >> 2, p4 = lane & 3; \
    const int r0s = 8 * kq + q4 + 4 * (kq & 1), r1s = 8 * kq + q4 + 4 * ((kq & 1) ^ 1);     \
    const int r2s = swz(4 * kq + q4);                                                       \
    (void)r0s; (void)r1s; (void)r2s; (void)p4
template <int V> struct IC { static constexpr int value = V; };
struct Frag80 { bf16x8 a0, a1; bf16x4 a2; };

__global__ void __launch_bounds__(512, 2) __attribute__((amdgpu_num_vgpr(32)))
mega_kernel(const u16*, const u16*, const u16*, const u16*, const u16*, const u16*, const u16*, const u16*, const u16*, u16*, unsigned char*
            ) {
    extern __shared__ __attribute__((aligned(16))) unsigned char smem[];
    const unsigned lb = (unsigned)(size_t)smem;
    const int wave = __builtin_amdgcn_readfirstlane(threadIdx.x >> 6);
    u64x8 ar0; u64x2 ar1; unsigned long long ar2, tp, tst64;
    asm volatile("s_load_dwordx16 %0, %5, 0\n\ts_load_dwordx4 %1, %5, 0x40\n\ts_load_dwordx2 %2, %5, 0x50\n\ts_load_dwordx2 %3, %5, %6\n\ts_memrealtime %4"
                 : "=&s"(ar0), "=&s"(ar1), "=&s"(ar2), "=&s"(tp), "=&s"(tst64) : "s"(__builtin_amdgcn_kernarg_segment_ptr()), "s"((0xa9876421u >> (4 * wave) & 15u) * 8u) : "memory");
    const int g = blockIdx.x, gj = GJ(g), gi = GI(g);
    const f32x4 z4 = {0.f, 0.f, 0.f, 0.f};
    asm volatile("" ::: "v255");
    auto when = [&](unsigned X) __attribute__((always_inline)) {
        u32x2 v;
        asm volatile("ds_read2_b32 %0, %1 offset0:14 offset1:5\n\ts_waitcnt lgkmcnt(0)" : "=v"(v) : "v"(lb + L_CNT) : "memory");
        return __builtin_amdgcn_readfirstlane(v[0]) + (X * __builtin_amdgcn_readfirstlane(v[1]) >> 12);
    };
    auto sched = [&](unsigned X, unsigned add) __attribute__((always_inline)) { until(when(X) + add); };
    auto slip = [&](unsigned X) __attribute__((always_inline)) {
        if (X) lds_set(lb + L_CNT + 56, now() - (X * lds_word(lb + L_CNT + 20) >> 12));
    };
    float xe = 0.f;

    auto code_pf = [&](int slice, int lane) __attribute__((always_inline)) {
        unsigned long long pc0; unsigned len, back, t;
        asm volatile("s_getpc_b64 %0\n.Lmega_pf%=:\n\ts_mov_b32 %1, .Lmega_code_end-.Lmega_code_begin\n\ts_mov_b32 %2, .Lmega_pf%=-.Lmega_code_begin" : "=s"(pc0), "=s"(len), "=s"(back));
        unsigned off = (unsigned)(slice * 4096 + lane * 64); if (off > len) off = len;
        asm volatile("global_load_dword %0, %1, %2" : "=v"(t) : "v"(off & ~3u), "s"(pc0 - back) : "memory");
        return t;
    };
    asm volatile(".Lmega_code_begin:");
    unsigned pf = 0u, pt1;
    const unsigned wofs = (unsigned)((((gj >> 1) * HQ + gi) * SK + (SK - WIN) + (gj & 1) * 512) * (DH * 2));
    {
        const int lane = threadIdx.x & 63, hi = lane >= 26;
        if (threadIdx.x < 24) lds_set(lb + L_CNT + threadIdx.x * 4, 0u);
        unsigned t2 = (unsigned)(gi * (DH * DM * 2)), t7 = (32u << 20) + (unsigned)(g * 128), to = (unsigned)(gj * 128), w2 = wofs;
        PINS(t2); PINS(t7); PINS(to); PINS(w2);
        to = wave == 7 ? t7 : to; to = wave == 2 ? t2 : to; to = wave < 2 ? w2 : to;
        if (wave == 7) pf = code_pf((g >> 3) & 15, lane);
        unsigned vo = (unsigned)((gj * 80 + swz(2 * wave) + hi) * (QD * 2) + gi * (DH * 2) + (lane - 26 * hi) * 16), d = lb + L_WQ + wave * 832, keep;
        unsigned xo = (0x11043210u >> (4 * wave) & 15u) * 1024 + (unsigned)(lane & (wave < 6 ? 63 : 15)) * 16, xd = lb + L_X0 + min(wave, 6) * 1024;
        PIN2(vo, xo); PINS(d); PINS(xd); PINS(to);
        __builtin_amdgcn_sched_barrier(0);
        asm volatile("s_waitcnt lgkmcnt(0)\n\ts_load_dword %0, %4, %6" : "=&s"(pt1), "+s"(ar0), "+s"(ar1), "+s"(ar2), "+s"(tp), "+s"(tst64) : "s"(to) : "memory");
        dma16((const void*)(wave < 5 ? ar0[0] : ar0[5]), xo, xd);
        asm volatile("s_mov_b32 %0, m0\n\ts_mov_b32 m0, %2\n\ts_mov_b64 exec, %4\n\tglobal_load_lds_dwordx4 %1, %3 nt\n\t"
                     ".rept 4\n\ts_add_u32 m0, m0, 6656\n\tv_add_u32 %1, 0x30000, %1\n\tglobal_load_lds_dwordx4 %1, %3 nt\n\t.endr\n\t"
                     "s_mov_b64 exec, -1\n\ts_mov_b32 m0, %0\n\ts_barrier"
                     : "=&s"(keep), "+v"(vo) : "s"(d), "s"(ar0[3]), "s"(0x0003fffffcffffffull) : "memory", "scc");
        __builtin_amdgcn_sched_barrier(0);
    }
    const u16 *const kc = (const u16*)ar0[1], *const vc = (const u16*)ar0[2], *const wo = (const u16*)ar0[4], *const wg = (const u16*)ar0[6], *const wu = (const u16*)ar0[7], *const wd = (const u16*)ar1[0];
    u16* const out = (u16*)ar1[1];
    unsigned char* const ws = (unsigned char*)((ar2 + (32u << 20) - 1) & ~(unsigned long long)((16u << 20) - 1));
    const unsigned tst = (unsigned)tst64;
    auto fix_k1 = [&](auto IC_, auto NC) __attribute__((always_inline)) {
        constexpr int A = RK + 8 * decltype(IC_)::value, B = A + 4;
        unsigned t0, t1, t2, t3;
        asm volatile("s_waitcnt vmcnt(%12)\n\tv_mov_b32 %0, v%c4\n\tv_mov_b32 %1, v%c5\n\tv_mov_b32 %2, v%c6\n\tv_mov_b32 %3, v%c7\n\t"
                     "v_mov_b32_dpp v%c4, v%c8 row_ror:8 row_mask:0xf bank_mask:0xc\n\tv_mov_b32_dpp v%c5, v%c9 row_ror:8 row_mask:0xf bank_mask:0xc\n\t"
                     "v_mov_b32_dpp v%c6, v%c10 row_ror:8 row_mask:0xf bank_mask:0xc\n\tv_mov_b32_dpp v%c7, v%c11 row_ror:8 row_mask:0xf bank_mask:0xc\n\t"
                     "v_mov_b32_dpp v%c8, %0 row_ror:8 row_mask:0xf bank_mask:0x3\n\tv_mov_b32_dpp v%c9, %1 row_ror:8 row_mask:0xf bank_mask:0x3\n\t"
                     "v_mov_b32_dpp v%c10, %2 row_ror:8 row_mask:0xf bank_mask:0x3\n\tv_mov_b32_dpp v%c11, %3 row_ror:8 row_mask:0xf bank_mask:0x3"
                     : "=&v"(t0), "=&v"(t1), "=&v"(t2), "=&v"(t3)
                     : "i"(A), "i"(A + 1), "i"(A + 2), "i"(A + 3), "i"(B), "i"(B + 1), "i"(B + 2), "i"(B + 3), "i"(decltype(NC)::value) : "memory");
    };
    auto trans_v = [&](auto UC) __attribute__((always_inline)) {
        constexpr int R = RV + 4 * decltype(UC)::value, P = 4 * decltype(UC)::value;
#define CELL(r, c) R + 16 * (r) + (c), R + 16 * (r) + 8 + (c)
        unsigned a0, a1, b0, b1, c0, c1;
        VMW(v_after(P)); zip_in<CELL(0, 0)>(); zip_out<CELL(0, 1)>(a0, a1); zip_out<CELL(0, 2)>(b0, b1); zip_out<CELL(0, 3)>(c0, c1);
        VMW(v_after(P + 1)); zip_x<CELL(1, 0), CELL(0, 1)>(a0, a1); zip_in<CELL(1, 1)>(); zip_out<CELL(1, 2)>(a0, a1);
        VMW(v_after(P + 2)); zip_x<CELL(2, 0), CELL(0, 2)>(b0, b1); zip_x<CELL(2, 1), CELL(1, 2)>(a0, a1); zip_in<CELL(2, 2)>(); zip_out<CELL(1, 3)>(a0, a1); zip_out<CELL(2, 3)>(b0, b1);
        VMW(v_after(P + 3)); zip_x<CELL(3, 0), CELL(0, 3)>(c0, c1); zip_x<CELL(3, 1), CELL(1, 3)>(a0, a1); zip_x<CELL(3, 2), CELL(2, 3)>(b0, b1); zip_in<CELL(3, 3)>();
#undef CELL
    };
    auto trans_w = [&](auto UC) __attribute__((always_inline)) {
        constexpr int R = RV + 64 + 2 * decltype(UC)::value, N = v_after(8 + decltype(UC)::value);
        unsigned a0, a1;
        VMW(N + 2); zip_in<R, R + 8>(); zip_out<R + 1, R + 9>(a0, a1);
        VMW(N); zip_x<R + 16, R + 24, R + 1, R + 9>(a0, a1); zip_in<R + 17, R + 25>();
    };
    auto frag80 = [&](unsigned im, int rs, int r0s, int r1s, int r2s) __attribute__((always_inline)) -> Frag80 {
        return Frag80{cat8(tr64(im + r0s * rs), tr64(im + r1s * rs)), cat8(tr64(im + (r0s + 32) * rs), tr64(im + (r1s + 32) * rs)), tr64(im + (r2s + 64) * rs)};
    };
    auto rows80 = [&](unsigned a16, unsigned a8) __attribute__((always_inline)) -> Frag80 { const Frag80 f{lds16(a16), lds16(a16 + 64), lds8(a8)}; __builtin_amdgcn_sched_barrier(0); return f; };
    auto empty5 = [&](int q0, unsigned S, int lane) __attribute__((always_inline)) {
        st16m(ws, tile35(gj, q0 + (lane >> 3)) + S + (unsigned)(gi * 128 + (lane & 7) * 16), (u32x4){~0u, ~0u, ~0u, ~0u}, 0xffffffffffull);
    };
    auto empty4 = [&](int layer, unsigned S, int lane) __attribute__((always_inline)) {
        const unsigned b = WS_H4 + (unsigned)((layer * 32 + gi) * 32768) + S;
        st16_sc1(ws, b + (unsigned)((lane >> 5) * 4096 + gj * 512 + (lane & 31) * 16), (u32x4){~0u, ~0u, ~0u, ~0u});
        st16m(ws, b + (unsigned)(lane < 32 ? 16384 + gj * 512 + lane * 16 : H4C + ((lane - 32) >> 3) * 1024 + gj * 128 + (lane & 7) * 16), (u32x4){~0u, ~0u, ~0u, ~0u},
              layer ? 0xff000000ffffffffull : ~0ull);
    };
    int lead = wave == 7 ? LEAD * PACE * 4 : 0;
    auto pace = [&](unsigned n, bool wait = true) __attribute__((always_inline)) {
        unsigned u, w, t0;
        asm volatile("s_mov_b64 exec, 1\n\tds_add_rtn_u32 %0, %3, %4\n\tds_max_u32 %3, %5 offset:12\n\tds_add_rtn_u32 %1, %3, %6 offset:12\n\tds_read_b32 %2, %3 offset:4\n\ts_mov_b64 exec, -1\n\ts_waitcnt lgkmcnt(0)"
                     : "=&v"(u), "=&v"(w), "=&v"(t0) : "v"(lb + L_CNT + 60), "v"(n), "v"((now() - tst) << 6), "v"(n * PMIN * 4u) : "memory");
        int tn = (int)(__builtin_amdgcn_readfirstlane(t0) - tst + PINIT) * 64 + __builtin_amdgcn_readfirstlane(u) * (PACE * 4);
        const int tw = __builtin_amdgcn_readfirstlane(w) - PBURST * 64;
        if (tw > tn) tn = tw;
        tn -= lead;
        if (wait) {
#pragma unroll 1
            for (int k = 0; k < 2048 && (int)((now() - tst) << 6) < tn; ++k) {}
        }
    };
    auto slot = [&](unsigned u) __attribute__((always_inline)) {
        const unsigned tn = lds_word(lb + L_CNT + 64) + PINIT + u * PACE / 16;
#pragma unroll 1
        for (int k = 0; k < 8192 && (int)(now() - tn) < 0; ++k) __builtin_amdgcn_s_sleep(1);
    };
    auto load_wo = [&](int k, int lane, bool early) __attribute__((always_inline)) {
        const int c = k * 64 + lane, pp = c / 10, cc = c - 10 * pp;
        const unsigned f = (unsigned)(swz(pp) * 1280 + cc * 16);
        const u16* wb = wo + (size_t)gi * DH * DM + gj * 80;
#pragma unroll
        for (int a2 = 0; a2 < 6; ++a2) { if (a2 % 3 == 0) { if (early) pace(3); else slot(NUNIT - WOFF + (2 * k + a2 / 3) * WOC); } dma16(wb + 32 * a2 * DM, f, lb + L_WO + (5 * a2 + k) * 1024); }
    };
    auto load_wd = [&](int lane) __attribute__((always_inline)) {
        const u16* db = wd + gi * 48 * DM + gj * 80;
#pragma unroll 1
        for (int k = 0; k < 5; ++k) {
            const int c = k * 64 + lane, pp = c / 10, cc = c - 10 * pp;
            const unsigned fd = (unsigned)(swz(pp) * 1280 + cc * 16);
            if (!(k & 1)) pace(3);
            dma16(db, fd, lb + L_WD + k * 1024);
            if (k < 3) dma16(db + 32 * DM, (k < 2 || lane < 32) ? fd : 0u, lb + L_WD + (5 + k) * 1024);
        }
    };
    auto load_gu = [&](int m, int lane) __attribute__((always_inline)) {
#pragma unroll 1
        for (int k0 = 0; k0 < 3; ++k0) {
            const int k = (k0 + (g & 7)) % 3, c = k * 64 + lane, row = c / 6, cc = c - 6 * row;
            const unsigned o = (unsigned)((swz(row) * FF + cc * 8) * 2), d = lb + L_GU + m * 8192 + k * 1024;
            const u16* wb = (m == 0 ? wg : wu) + gj * 80 * FF + gi * 48;
            pace(3);
            dma16(wb, o, d); dma16(wb + 32 * FF, o, d + 3072);
            if (k < 2) dma16(wb + 64 * FF, (k == 0 || lane < 32) ? o : 0u, d + 6144);
        }
    };
    auto lds_add = [&](int off, unsigned v) __attribute__((always_inline)) { asm volatile("s_mov_b64 exec, 1\n\tds_add_u32 %0, %1\n\ts_mov_b64 exec, -1" :: "v"(lb + L_CNT + off), "v"(v) : "memory"); };
    auto send_flag = [&](int lane) __attribute__((always_inline)) {
        st4_sc1(ws, WS_MB + (unsigned)((gi * 8 + ((gj == (gi & 7) ? lane : gi) & 7)) * 128 + gj * 4), lds_word(lb + L_CNT + 68));
    };
    auto tiles4 = [&](const Frag80 (&f)[4], const Frag80& b, unsigned long long mk) __attribute__((always_inline)) -> u32x4 {
        f32x4 c[4], d[4];
#pragma unroll
        for (int i = 0; i < 4; ++i) c[i] = mfma32(f[i].a0, b.a0, z4);
#pragma unroll
        for (int i = 0; i < 4; ++i) { c[i] = mfma32(f[i].a1, b.a1, c[i]); d[i] = mfma16(f[i].a2, b.a2, z4); }
        __builtin_amdgcn_sched_barrier(0);
        f32x2 lo, hi;
        asm volatile("s_nop 1\n\ts_mov_b64 exec, %18\n\tv_pk_add_f32 %0, %2, %10\n\tv_pk_add_f32 %1, %3, %11\n\t"
                     "s_lshl_b64 exec, exec, 1\n\tv_pk_add_f32 %0, %4, %12\n\tv_pk_add_f32 %1, %5, %13\n\t"
                     "s_lshl_b64 exec, exec, 1\n\tv_pk_add_f32 %0, %6, %14\n\tv_pk_add_f32 %1, %7, %15\n\t"
                     "s_lshl_b64 exec, exec, 1\n\tv_pk_add_f32 %0, %8, %16\n\tv_pk_add_f32 %1, %9, %17\n\ts_mov_b64 exec, -1"
                     : "=&v"(lo), "=&v"(hi) : "v"(LO2(c[0])), "v"(HI2(c[0])), "v"(LO2(c[1])), "v"(HI2(c[1])), "v"(LO2(c[2])), "v"(HI2(c[2])), "v"(LO2(c[3])), "v"(HI2(c[3])),
                       "v"(LO2(d[0])), "v"(HI2(d[0])), "v"(LO2(d[1])), "v"(HI2(d[1])), "v"(LO2(d[2])), "v"(HI2(d[2])), "v"(LO2(d[3])), "v"(HI2(d[3])), "s"(mk) : "scc");
        return asu(__builtin_shufflevector(lo, hi, 0, 1, 2, 3));
    };
    auto empty1 = [&](int half, unsigned S, int lane) __attribute__((always_inline)) {
        const unsigned o = WS_H1 + S + (unsigned)((gi * 4 + 2 * half) * 16384 + gj * 896 + min(lane, 55) * 16);
        st16_sc1(ws, o, (u32x4){~0u, ~0u, ~0u, ~0u}); st16_sc1(ws, o + 16384, (u32x4){~0u, ~0u, ~0u, ~0u});
    };
    auto report = [&]() __attribute__((always_inline)) { if (gj == (gi & 7)) st4_sc1(ws, WS_R + (unsigned)(gi * 4), lds_word(lb + L_CNT + 88)); };
    auto answer = [&](int lane, unsigned r) __attribute__((always_inline)) {
        int d = min(max((int)(r - (tst << 11)) >> 11, -400), 600);
        unsigned k = (r >> 5) & 63u, v = (r >> 1) & 15u;
        v += dppu<0x124>(v); v += dppu<0x128>(v);
#define VOTES(l) (__builtin_amdgcn_readlane(v, l) + __builtin_amdgcn_readlane(v, 16 + l))
        const unsigned c = (VOTES(0) > 32 ? 2u : 0u) | (VOTES(1) > 32 ? 4u : 0u) | (VOTES(2) + VOTES(3) > 64 ? 8u : 0u);
        d += (int)dppu<0xB1>((unsigned)d); d += (int)dppu<0x4E>((unsigned)d); d += (int)dppu<0x141>((unsigned)d); d += (int)dppu<0x140>((unsigned)d);
        k = max(k, dppu<0xB1>(k)); k = max(k, dppu<0x4E>(k)); k = max(k, dppu<0x141>(k)); k = max(k, dppu<0x140>(k));
        d = __builtin_amdgcn_readlane(d, 0) + __builtin_amdgcn_readlane(d, 16);
        k = max((unsigned)__builtin_amdgcn_readlane(k, 0), (unsigned)__builtin_amdgcn_readlane(k, 16));
        st4_sc1(ws, WS_MB + (unsigned)(((lane & 31) * 8 + (lane & 7)) * 128 + 32), (tst + (d >> 5) + TOFF) << 10 | k << 4 | c | 1u);
        lds_set(lb + L_CNT + 36, 1u);
    };
    auto adopt = [&](unsigned t) __attribute__((always_inline)) {
        const int dt = (int)(t - (tst << 10)) >> 10;
        if (dt > -256 && dt < 512) lds_set(lb + L_CNT + 64, tst + dt);
        lds_set(lb + L_CNT + 20, 4096u + ((t >> 4) & 63u) * BETA);
        lds_set(lb + L_CNT + 80, lds_word(lb + L_CNT + 92) ^ t);
        lds_set(lb + L_CNT + 84, (((t >> 1) ^ lds_word(lb + L_CNT + 68)) & 4u) << 11);
        lds_set(lb + L_CNT + 52, 1u);
    };
    struct StatAddr { unsigned a16, b16, a8, b8, dst; };
    auto stats_addr = [&](int lane, unsigned dbase, int dstride) __attribute__((always_inline)) -> StatAddr {
        const int kq = lane >> 4, q4 = (lane >> 2) & 3, p4 = lane & 3;
        const int x1 = LANES(0x4444444444444444ull) ? L_WR + gj * 160 : L_XS + q4 * 160, xa = LANES(0xaaaaaaaaaaaaaaaaull) ? L_XS + 640 : x1;
        StatAddr r{lb + (unsigned)(xa + kq * 16), lb + (unsigned)(L_XS + p4 * 160 + kq * 16), lb + (unsigned)(xa + 128 + kq * 8), lb + (unsigned)(L_XS + p4 * 160 + 128 + kq * 8), dbase + (unsigned)(kq * dstride)};
        PIN3(r.a16, r.b16, r.dst); PIN2(r.a8, r.b8);
        return r;
    };
    auto stats = [&](const StatAddr& r) __attribute__((always_inline)) {
        const bf16x8 a0 = lds16(r.a16), b0 = lds16(r.b16), a1 = lds16(r.a16 + 64), b1 = lds16(r.b16 + 64);
        const bf16x4 a2 = lds8(r.a8), b2 = lds8(r.b8);
        unsigned long long mk = 0x0008000400020001ull; PINS(mk);
        __builtin_amdgcn_sched_barrier(0);
        st16m(ws, r.dst, asu(mfma32(a1, b1, mfma32(a0, b0, z4)) + mfma16(a2, b2, z4)), mk);
    };
    unsigned qa, pw, ma, idx, qm;
    auto qk_addr = [&](bool L1) __attribute__((always_inline)) {
        LANE_VARS();
        qa = lb + L_Q + (unsigned)(kq * 32 + (lane & 2) * 8);
        pw = lb + L_P + (unsigned)((7 + 8 * wave) * 192 + (kq >> 1) * 64 + (n >> 3) * 32 + (lane & 2) * 8 + (q4 & 1) * 8 + (kq & 1) * 4 + (lane & 1) * 2);
        ma = lb + L_M + (unsigned)(wave * 4);
        idx = (lane & 2) ? 0xeeeeu : 0x4444u;
        qm = lb + L_Q;
        PIN3(qa, pw, ma); PIN2(idx, qm);
        if (L1 && wave < 4) __builtin_amdgcn_s_setprio(2);
    };
    auto att_qk0 = [&]() __attribute__((always_inline)) {
        qk_addr(false);
#pragma unroll 1
        for (unsigned spins = 0; spins < (kSpinCap << 4); ++spins) {
            if (lds_word(lb + L_CNT + 4) != 0u) break;
            __builtin_amdgcn_s_sleep(1);
        }
        STAMP(3);
        const u32x4 q0 = *(const LAS u32x4*)(size_t)qa, q1 = *(const LAS u32x4*)(size_t)(qa + 128), q2 = *(const LAS u32x4*)(size_t)(qa + 256);
        f32x4 c0 = z4, c1 = z4, c2 = z4, c3 = z4;
#define SMF(c, q, r) "v_smfmac_f32_16x16x64_bf16 %" #c ", %" #q ", v[" #r "], %7\n\t"
        asm volatile("s_nop 1\n\t" SMF(0, 4, 64:71) SMF(1, 4, 88:95) SMF(2, 4, 112:119) SMF(3, 4, 136:143) SMF(0, 5, 72:79) SMF(1, 5, 96:103) SMF(2, 5, 120:127) SMF(3, 5, 144:151)
                     SMF(0, 6, 80:87) SMF(1, 6, 104:111) SMF(2, 6, 128:135) SMF(3, 6, 152:159) "s_nop 7"
                     : "+v"(c0), "+v"(c1), "+v"(c2), "+v"(c3) : "v"(q0), "v"(q1), "v"(q2), "v"(idx));
#undef SMF
        const float s0 = c0[0] + c0[2], s1 = c1[0] + c1[2], s2 = c2[0] + c2[2], s3 = c3[0] + c3[2];
        float m = fmax2(fmax3(s0, s1, s2), s3);
        m = fmax2(m, dpp<0xB1>(m)); m = fmax2(m, dpp<0x4E>(m)); m = fmax2(m, dpp<0x124>(m)); m = fmax2(m, dpp<0x128>(m));
        const float s01 = LANES(0xffff0000ffff0000ull) ? s1 : s0, s23 = LANES(0xffff0000ffff0000ull) ? s3 : s2;
        const float sv = LANES(0xffffffff00000000ull) ? s23 : s01;
        m = bfr(bfr(m) * SCALE);
        const float p = ex2((bfr(bfr(sv) * SCALE) - m) * LOG2E);
        *(LAS u16*)(size_t)pw = (u16)pk2(p, p);
        *(LAS float*)(size_t)ma = m;
        __builtin_amdgcn_sched_barrier(0);
    };
    unsigned pr, oa, oc, cma, cxa, cra, cso;
    auto att_addr = [&]() __attribute__((always_inline)) {
        LANE_VARS();
        pr = lb + L_P + (unsigned)((7 + 8 * wave - 2 * q4 - (lane & 1)) * 192 + kq * 32 + (lane & 2) * 8);
        oa = lb + L_OP + (unsigned)(wave * 1024 + (lane & 7) * 64 + kq * 16 + (n >> 3) * 8);
        oc = lb + L_OP + (unsigned)(wave * 1024 + 512 + (kq >> 1) * 256 + (lane & 7) * 32 + (kq & 1) * 16 + (n >> 3) * 8);
        PIN3(pr, oa, oc);
    };
    auto comb_out = [&](float u0, float u1, unsigned so, unsigned long long mk, bool both) __attribute__((always_inline)) {
        const unsigned w = min(pk2(u0, u1), 0xfffffffeu);
        if (!both) { st4_sc1(ws, so, w); return; }
        const u32x4 w4 = {dppu<0x00>(w), dppu<0x55>(w), dppu<0xAA>(w), dppu<0xFF>(w)};
        st16m(ws, so, w4, mk);
    };
    auto max8 = [&](f32x4 m0, f32x4 m1) __attribute__((always_inline)) { return fmax3(fmax3(m0[0], m0[1], m0[2]), fmax3(m0[3], m1[0], m1[1]), fmax2(m1[2], m1[3])) * LOG2E; };
    auto comb_o0 = [&](f32x4 m0, f32x4 m1, f32x4 mm, f32x4 o0, f32x4 o1, f32x4 o2, f32x4 o3, unsigned so, unsigned long long mk, bool both) __attribute__((always_inline)) {
        const float M = max8(m0, m1);
        f32x4 f;
#pragma unroll
        for (int k = 0; k < 4; ++k) f[k] = ex2(__builtin_fmaf(mm[k], LOG2E, -M));
        const f32x4 o = f[3] * o3 + (f[2] * o2 + (f[1] * o1 + f[0] * o0));
        comb_out(rs32(o[0], o[2]), rs32(o[1], o[3]), so, mk, both);
    };
    auto comb_o1 = [&](f32x4 m0, f32x4 m1, f32x2 mm, f32x4 o0, f32x4 o1, unsigned so, unsigned long long mk, bool both) __attribute__((always_inline)) {
        const float M = max8(m0, m1);
        const f32x4 o = ex2(__builtin_fmaf(mm[1], LOG2E, -M)) * o1 + ex2(__builtin_fmaf(mm[0], LOG2E, -M)) * o0;
        comb_out(x16_add(rs32(o[0], o[2])), x16_add(rs32(o[1], o[3])), so, mk, both);
    };
    auto ml_addr = [&](int lane, unsigned& xa, unsigned& ra) __attribute__((always_inline)) {
        xa = lb + L_M + (unsigned)(((lane & 15) >> 1) * 4); ra = lb + L_P + (unsigned)((7 + 8 * ((lane & 15) >> 1)) * 192 + (lane & 1) * 64 + (lane >> 4) * 16);
    };
    auto comb_ml = [&](unsigned ma, unsigned xa, unsigned ra, unsigned so, bool both) __attribute__((always_inline)) {
        const u32x4 x4 = *(const LAS u32x4*)(size_t)ra;
        const f32x4 m0 = *(const LAS f32x4*)(size_t)ma, m1 = *(const LAS f32x4*)(size_t)(ma + 16);
        const float mw = *(const LAS float*)(size_t)xa;
        __builtin_amdgcn_sched_barrier(0);
        const f32x4 sp = mfma32u((u32x4){0x3f803f80u, 0x3f803f80u, 0x3f803f80u, 0x3f803f80u}, x4, z4);
        __builtin_amdgcn_sched_barrier(0);
        const float M = max8(m0, m1);
        float L = row16_sum(sp[0] * ex2(__builtin_fmaf(mw, LOG2E, -M)));
        if (both) L = dpp<0x00>(L);
        st16m(ws, so, asu((f32x4){M, L, M, L}), both ? 3ull : 1ull);
    };
    auto att_pv0 = [&]() __attribute__((always_inline)) {
        u32x4 bm[8];
        trans_v(IC<0>{});
        if (v_after(9) > v_after(7)) { trans_w(IC<0>{}); trans_w(IC<1>{}); }
        trans_v(IC<1>{});
#pragma unroll
        for (int e = 0; e < 8; ++e) bm[e] = *(const LAS u32x4*)(size_t)(pr + e * 192);
        __builtin_amdgcn_sched_barrier(0);
        f32x4 da = z4, dc = z4;
        PIN2(da, dc);
        asm volatile("s_nop 1");
        __builtin_amdgcn_sched_barrier(0);
        f32x2 r;
        smf<RV>(da, bm[0], idx); smf<RV + 8>(da, bm[1], idx); smf<RV + 16>(da, bm[2], idx); smf<RV + 24>(da, bm[3], idx);
        smf<RV + 32>(da, bm[4], idx); smf<RV + 40>(da, bm[5], idx); smf<RV + 48>(da, bm[6], idx); smf<RV + 56>(da, bm[7], idx);
        asm volatile("s_nop 7\n\tv_pk_add_f32 %0, %1, %2\n\tds_write_b64 %3, %0\n\ts_mov_b64 exec, 1\n\tds_add_u32 %4, %5\n\ts_mov_b64 exec, -1"
                     : "=&v"(r) : "v"(LO2(da)), "v"(HI2(da)), "v"(oa), "v"(lb + L_CNT + 24), "v"(1u) : "memory");
        GUARD(900);
        MARK(b_t5);
        if (v_after(9) < v_after(7)) { trans_w(IC<0>{}); trans_w(IC<1>{}); }
        trans_w(IC<2>{}); trans_w(IC<3>{});
        STAMP(4);
        asm volatile("s_nop 1\n\tv_smfmac_f32_16x16x64_bf16 %0, %1, v[224:231], %5\n\tv_smfmac_f32_16x16x64_bf16 %0, %2, v[232:239], %5\n\t"
                     "v_smfmac_f32_16x16x64_bf16 %0, %3, v[240:247], %5\n\tv_smfmac_f32_16x16x64_bf16 %0, %4, v[248:255], %5\n\ts_nop 6"
                     : "+v"(dc) : "v"(bm[0]), "v"(bm[1]), "v"(bm[2]), "v"(bm[3]), "v"(idx));
        asm volatile("s_nop 0\n\tv_pk_add_f32 %0, %1, %2\n\tds_write_b64 %3, %0" : "=&v"(r) : "v"(LO2(dc)), "v"(HI2(dc)), "v"(oc) : "memory");
        STAMP(20);
    };
    unsigned long long ck1 = 0xffff0000ffff0000ull, ck2 = 0xffffffff00000000ull, csc = 0x3d93cd3a3d93cd3aull;
    auto att1 = [&]() __attribute__((always_inline)) {
        constexpr int layer = 1;
        {
#define SMF(c, q, r) "v_smfmac_f32_16x16x64_bf16 v[" #c "], v[" #q "], v[" #r "], %0\n\t"
#define MAXD(x) "v_max_f32_dpp v41, v41, v41 " x " row_mask:0xf bank_mask:0xf\n\t"
            asm volatile("s_waitcnt lgkmcnt(0)\n\ts_barrier\n\t"
                         "ds_read_b128 v[0:3], %1\n\tds_read2_b32 v[12:13], %2 offset0:96 offset1:100\n\tds_read_b128 v[4:7], %1 offset:128\n\tds_read_b128 v[8:11], %1 offset:256\n\t"
                         "v_mov_b64 v[16:17], 0\n\tv_mov_b64 v[18:19], 0\n\tv_mov_b64 v[20:21], 0\n\tv_mov_b64 v[22:23], 0\n\tv_mov_b64 v[24:25], 0\n\tv_mov_b64 v[26:27], 0\n\t"
                         "v_mov_b64 v[28:29], 0\n\tv_mov_b64 v[30:31], 0\n\tv_mov_b64 v[32:33], 0\n\tv_mov_b64 v[34:35], 0\n\tv_mov_b64 v[36:37], 0\n\tv_mov_b64 v[38:39], 0\n\t"
                         "s_waitcnt lgkmcnt(3)\n\t" SMF(16:19, 0:3, 64:71) SMF(20:23, 0:3, 88:95) "s_waitcnt lgkmcnt(1)\n\tv_mul_f32 v13, 0x3acccccd, v13\n\t"
                         SMF(24:27, 0:3, 112:119) "v_mul_f32 v12, 0x3acccccd, v12\n\t" SMF(28:31, 0:3, 136:143) "v_fma_f32 v12, -v13, v13, v12\n\t"
                         SMF(16:19, 4:7, 72:79) "v_maximum3_f32 v12, v12, 0, 0\n\t" SMF(20:23, 4:7, 96:103) "v_cvt_pk_bf16_f32 v12, 0, v12\n\t"
                         SMF(24:27, 4:7, 120:127) "v_add_f32 v12, 0x3727c5ac, v12\n\t" SMF(28:31, 4:7, 144:151) "v_cvt_pk_bf16_f32 v12, 0, v12\n\ts_waitcnt lgkmcnt(0)\n\t"
                         SMF(16:19, 8:11, 80:87) "v_sqrt_f32 v12, v12\n\t" SMF(20:23, 8:11, 104:111) "v_cvt_pk_bf16_f32 v12, 0, v12\n\t"
                         SMF(24:27, 8:11, 128:135) "v_rcp_f32 v12, v12\n\t" SMF(28:31, 8:11, 152:159)
                         "s_nop 2\n\tv_add_f32 v42, v16, v18\n\ts_nop 0\n\tv_add_f32 v43, v20, v22\n\tv_max_f32 v41, v42, v43\n\tv_add_f32 v44, v24, v26\n\tv_add_f32 v45, v28, v30\n\t"
                         "v_max3_f32 v41, v41, v44, v45\n\tv_cndmask_b32 v42, v42, v43, %8\n\tv_cndmask_b32 v44, v44, v45, %8\n\t"
                         MAXD("quad_perm:[1,0,3,2]") "v_cndmask_b32 v40, v42, v44, %9\n\ts_nop 0\n\t" MAXD("quad_perm:[2,3,0,1]") "s_nop 1\n\t" MAXD("row_ror:4") "s_nop 1\n\t" MAXD("row_ror:8")
                         "v_pk_mul_f32 v[40:41], v[12:13], v[40:41] op_sel_hi:[0,1]\n\tv_cvt_pk_bf16_f32 v40, 0, v40\n\tv_cvt_pk_bf16_f32 v41, 0, v41\n\t"
                         "v_pk_mul_f32 v[40:41], %10, v[40:41]\n\tv_cvt_pk_bf16_f32 v40, 0, v40\n\tv_cvt_pk_bf16_f32 v41, 0, v41\n\t"
                         "v_sub_f32 v40, v40, v41\n\tv_mul_f32 v40, 0x3fb8aa3b, v40\n\tv_exp_f32 v40, v40\n\ts_nop 0\n\tv_cvt_pk_bf16_f32 v40, v40, v40\n\tds_write_b16 %3, v40\n\t"
                         "ds_read_b128 v[0:3], %4\n\tds_read_b128 v[4:7], %4 offset:192\n\tds_read_b128 v[8:11], %4 offset:384\n\tds_read_b128 v[12:15], %4 offset:576\n\t"
                         "ds_read_b128 v[16:19], %4 offset:768\n\tds_read_b128 v[20:23], %4 offset:960\n\tds_read_b128 v[24:27], %4 offset:1152\n\tds_read_b128 v[28:31], %4 offset:1344\n\t"
                         "ds_write_b32 %5, v41\n\t"
                         "s_waitcnt lgkmcnt(8)\n\t" SMF(32:35, 0:3, 160:167) SMF(36:39, 0:3, 224:231) "s_waitcnt lgkmcnt(7)\n\t" SMF(32:35, 4:7, 168:175) SMF(36:39, 4:7, 232:239)
                         "s_waitcnt lgkmcnt(6)\n\t" SMF(32:35, 8:11, 176:183) SMF(36:39, 8:11, 240:247) "s_waitcnt lgkmcnt(4)\n\t" SMF(32:35, 12:15, 184:191) SMF(36:39, 12:15, 248:255)
                         SMF(32:35, 16:19, 192:199) "s_waitcnt lgkmcnt(1)\n\t" SMF(32:35, 20:23, 200:207) SMF(32:35, 24:27, 208:215) SMF(32:35, 28:31, 216:223)
                         "s_nop 2\n\tv_pk_add_f32 v[36:37], v[36:37], v[38:39]\n\tds_write_b64 %7, v[36:37]\n\ts_nop 2\n\tv_pk_add_f32 v[32:33], v[32:33], v[34:35]\n\tds_write_b64 %6, v[32:33]"
                         :: "v"(idx), "v"(qa), "v"(qm), "v"(pw), "v"(pr), "v"(ma), "v"(oa), "v"(oc), "s"(ck1), "s"(ck2), "s"(csc)
                         : "memory", "v0", "v1", "v2", "v3", "v4", "v5", "v6", "v7", "v8", "v9", "v10", "v11", "v12", "v13", "v14", "v15", "v16", "v17", "v18", "v19", "v20", "v21", "v22", "v23",
                           "v24", "v25", "v26", "v27", "v28", "v29", "v30", "v31", "v32", "v33", "v34", "v35", "v36", "v37", "v38", "v39", "v40", "v41", "v42", "v43", "v44", "v45");
#undef SMF
#undef MAXD
            STAMP(20 + 32 * layer);
            BAR();
            if (__builtin_expect(wave == 0, 1)) {
                STAMP(5 + 32 * layer);
                comb_o0(*(const LAS f32x4*)(size_t)cma, *(const LAS f32x4*)(size_t)(cma + 16), *(const LAS f32x4*)(size_t)cxa, *(const LAS f32x4*)(size_t)cra,
                        *(const LAS f32x4*)(size_t)(cra + 1024), *(const LAS f32x4*)(size_t)(cra + 2048), *(const LAS f32x4*)(size_t)(cra + 3072), cso, 0x1111111111111111ull, false);
            }
            __builtin_amdgcn_sched_barrier(0);
            if (__builtin_expect(wave == 1, 1))
                comb_o1(*(const LAS f32x4*)(size_t)cma, *(const LAS f32x4*)(size_t)(cma + 16), *(const LAS f32x2*)(size_t)cxa, *(const LAS f32x4*)(size_t)cra, *(const LAS f32x4*)(size_t)(cra + 1024), cso, 0x0000111100001111ull, false);
            __builtin_amdgcn_sched_barrier(0);
            if (__builtin_expect(wave == 2, 1)) comb_ml(cma, cxa, cra, cso, false);
            MARK(e_q);
            if (wave < 3) STAMP(21 + 32 * layer);
            __builtin_amdgcn_s_setprio(0);
        }
    };

    unsigned nj = 0u, qS = 0u, fS = 0u, rS = 0u, nS = 0u; int ord;
    {
        const int tid = threadIdx.x, lane = tid & 63;
        asm volatile("ds_max_u32 %0, %1" :: "v"(lb + L_CNT + 64), "v"(tst) : "memory");
        unsigned mb = 0u;
        const u16* kbase = kc + wofs / 2;
        const u16* vbase = vc + wofs / 2;
        unsigned pt0 = 0u;
        if (wave == 7) {
            __builtin_amdgcn_s_setprio(3);
            if (lane < 10) *reinterpret_cast<u32x4*>(smem + L_XS + 640 + lane * 16) = (u32x4){0x3f803f80u, 0x3f803f80u, 0x3f803f80u, 0x3f803f80u};
            if (lane < 16) { *reinterpret_cast<u32x4*>(smem + L_CS + 768 + lane * 16) = (u32x4){0u, 0u, 0u, 0u}; }
        } else {
            if (wave >= 4) {
                if (wave == 4) st16_sc1(ws, WS_OWN + (unsigned)(gi * 16384 + (lane >> 5) * 8192 + gj * 512 + (lane & 31) * 16), (u32x4){~0u, ~0u, ~0u, ~0u});
                else empty5(0, (unsigned)(wave - 5) * 8192u, lane);
                if (wave == 6) {
                    __builtin_amdgcn_s_setprio(3);
                    if (gi + gj == 0) st4_sc1(ws, WS_R + (lane & 31) * 4, 0u);
                    st4_sc1(ws, WS_MB + (unsigned)((gi * 8 + gj) * 128 + (lane & 31) * 4), 0u);
                }
            } else if (wave >= 2) {
                unsigned long long c0, c1;
                asm volatile("s_waitcnt lgkmcnt(0)\n\ts_memtime %0\n\ts_waitcnt lgkmcnt(0)\n\ts_load_dword %2, %3, %4\n\ts_waitcnt lgkmcnt(0)\n\ts_memtime %1\n\ts_waitcnt lgkmcnt(0)"
                             : "=&s"(c0), "=&s"(c1), "=&s"(pt0) : "s"(ws), "s"((unsigned)(min(gi & 3, 2) << 21) + ((((tst + g * 977u) * 2654435761u) >> 11 & 0x1fdf80u) | ((wave ^ gj) & 1) * 8192)) : "memory");
                pt0 = ((wave ^ gj) & 1) ? (unsigned)c0 - (unsigned)c1 : (unsigned)c1 - (unsigned)c0;
            }
            STAMP(36);
            *reinterpret_cast<u32x4*>(smem + L_P + tid * 16) = (u32x4){0u, 0u, 0u, 0u};
            if (tid < 72 * 12 - 448) *reinterpret_cast<u32x4*>(smem + L_P + 7168 + tid * 16) = (u32x4){0u, 0u, 0u, 0u};
            if (wave >= 4) {
                if (wave == 6) VMW(2); else VMW(1);
                asm volatile("ds_max_u32 %0, %1" :: "v"(lb + L_CNT + 64), "v"(now() - TLAND) : "memory");
                STAMP(40);
                lds_add(0, 1u);
            } else if (wave >= 2) { lds_add(20, pt0); VMW(0); lds_add(0, 1u); }
        }
        asm volatile("s_waitcnt lgkmcnt(0)" :: "s"(pt1) : "memory");
        const int rot = gj + wave;
        const unsigned r0 = now(), c0 = (unsigned)__builtin_amdgcn_s_memtime();
        u32x4 a[4];
        unsigned ko = (unsigned)((wave * 64 + (lane & 7)) * 384 + (((lane >> 3) & 1) * 4 + (lane >> 4)) * 16);
        PIN1(ko);
        auto k_group = [&](auto GC_) __attribute__((always_inline)) {
            constexpr int G = decltype(GC_)::value, R = RK + 24 * (G >> 1) + 4 * (G & 1);
            const unsigned o = ko + (unsigned)((((G >> 1) + rot) & 3) * 6144 + (G & 1) * 3072);
            pace(3);
            ld16_nt<R, 0>(kbase, o); ld16_nt<R + 8, 128>(kbase, o); ld16_nt<R + 16, 256>(kbase, o);
            const int lane = lane_id();
            const unsigned ho = WS_Q0 + (unsigned)((gi * 4 + (gj >> 1)) * 4096 + lane * 16);
            if (wave < 7) {
                asm volatile("; COND_BEGIN");
                if (G == 0 && wave >= 4) { VMW(3); lds_add(32, 1u); }
                if (G == 0) {
                    if (wave < 2) { VMW(3); lds_add(0, 1u); load_gu(wave, lane); }
                    if (wave == 2) load_wd(lane);
                    if (wave == 3) {
#pragma unroll 1
                        for (int k = 5 - WEARLY; k < 5; ++k) load_wo(k, lane, true);
                    }
                }
                asm volatile("; COND_END");
            }
            if (wave == 7) {
                asm volatile("; COND_BEGIN");
                if (G == GP) {
                    VMW(3 * (GP + 1));
#pragma unroll 1
                    for (unsigned spins = 0; spins < (kSpinCap << 4); ++spins) {
                        if (lds_word(lb + L_CNT) >= 7u) break;
                        __builtin_amdgcn_s_sleep(1);
                    }
                    STAMP(26);
                    const unsigned char* p = smem + L_X0 + (lane >> 4) * 1280;
                    const float c0 = lo16(*reinterpret_cast<const u16*>(p));
                    const f32x2 c2 = {c0, c0};
                    f32x2 s2 = {0.f, 0.f}, q2 = s2;
#pragma unroll
                    for (int k = 0; k < 5; ++k) {
                        const u32x4 w = *reinterpret_cast<const u32x4*>(p + k * 256 + (lane & 15) * 16);
#pragma unroll
                        for (int e = 0; e < 4; ++e) { const f32x2 d = unpk(w[e]) - c2; s2 += d; q2 += d * d; }
                    }
                    const float s = row16_sum(s2[0] + s2[1]), qq = row16_sum(q2[0] + q2[1]);
                    const float ms = s * (1.f / DM);
                    const unsigned mv = pk2(c0 + ms, fmax2(qq - s * ms, 0.f) * (1.f / DM));
                    const float mb = lo16(mv), rden = frcp(bfr(__builtin_amdgcn_sqrtf(bfr(hi16(mv) + 1e-5f))));
                    const f32x2 mb2 = {mb, mb}, rd2 = {rden, rden};
                    if ((lane & 15) < 10) {
                        u32x4 w = *reinterpret_cast<const u32x4*>(p + gj * 160 + (lane & 15) * 16);
#pragma unroll
                        for (int e = 0; e < 4; ++e) w[e] = pk2v(unpk(pk2v(unpk(w[e]) - mb2)) * rd2);
                        *reinterpret_cast<u32x4*>(smem + L_XS + (lane >> 4) * 160 + (lane & 15) * 16) = w;
                    }
                    STAMP(30);
                }
                if (G == GP || G == GA) {
#pragma unroll 1
                    for (int blk = 0; blk < 3; ++blk) {
                        LANE_VARS();
                        const unsigned xb = lb + L_XS + (unsigned)((G == GP ? q4 : 4) * 160);
                        Frag80 f[4];
#pragma unroll
                        for (int i = 0; i < 4; ++i) f[i] = frag80(lb + L_WQ + (unsigned)(128 * blk + 32 * i + 8 * p4), 416, r0s, r1s, r2s);
                        const Frag80 b = rows80(xb + kq * 16, xb + 128 + kq * 8);
                        const u32x4 c = tiles4(f, b, 0x1111111111111111ull);
                        if (G == GP) *reinterpret_cast<u32x4*>(smem + L_OP + q4 * 768 + 256 * blk + p4 * 64 + kq * 16) = c;
                        else *reinterpret_cast<u32x4*>(smem + L_CS + 256 * blk + kq * 64 + p4 * 16) = c;
                    }
                    if (G == GP) {
                        asm volatile("" ::: "memory");
                        const int ln = lane_id();
                        f32x2 c[6];
                        unsigned m = 20u;
#pragma unroll
                        for (int k = 0; k < 6; ++k) {
                            c[k] = *reinterpret_cast<const f32x2*>(smem + L_OP + (k / 3) * 1536 + ln * 24 + (k % 3) * 8);
                            m = max(m, max(fexp(c[k][0]), fexp(c[k][1])));
                        }
                        m = max(m, dppu<0xB1>(m)); m = max(m, dppu<0x4E>(m)); m = max(m, dppu<0x141>(m)); m = max(m, dppu<0x140>(m));
                        m = max(max((unsigned)__builtin_amdgcn_readlane(m, 0), (unsigned)__builtin_amdgcn_readlane(m, 16)), max((unsigned)__builtin_amdgcn_readlane(m, 32), (unsigned)__builtin_amdgcn_readlane(m, 48)));
                        lds_set(lb + L_CNT + 76, m);
                        const float f = __uint_as_float((273u - m) << 23);
#pragma unroll
                        for (int k = 0; k < 2; ++k) {
                            const u32x2 w0 = enc21(c[3 * k][0] * f, c[3 * k][1] * f, c[3 * k + 1][0] * f), w1 = enc21(c[3 * k + 1][1] * f, c[3 * k + 2][0] * f, c[3 * k + 2][1] * f);
                            st16_sc1(ws, WS_Q0 + (unsigned)((gi * 4 + 2 * k + (ln >> 5)) * 4096 + gj * 512 + (ln & 31) * 16), (u32x4){w0[0], w0[1], w1[0], w1[1]});
                        }
                        STAMP(2);
                        WAIT1(pf, 3 * (GP + 1) + 2);
                    }
                }
                if (G == GA) {
                    const unsigned half = __builtin_amdgcn_s_getreg(2 << 6 | 20);
                    lds_set(lb + L_CNT + 68, lds_word(lb + L_CNT + 64) << 11 | lds_word(lb + L_CNT + 76) << 3 | 1u | (((int)lds_word(lb + L_CNT + 20) > 0) ^ half) << 1 | half << 2);
                    VMW(3 * (GA - GP));
#pragma unroll 1
                    for (unsigned spins = 0; spins < (kSpinCap << 4); ++spins) if (lds_word(lb + L_CNT + 32) >= 3u) break;
                    send_flag(lane_id());
                    lds_set(lb + L_CNT + 8, 1u);
                    STAMP(17);
                    f32x2 r2 = {0.f, 0.f};
#pragma unroll
                    for (int k = 0; k < 5; ++k) r2 += unpk(*reinterpret_cast<const unsigned*>(smem + L_WR + k * 256 + lane_id() * 4));
                    *reinterpret_cast<float*>(smem + L_CNT + 12) = wave_sum(r2[0] + r2[1]);
                }
                asm volatile("; COND_END");
            }
            if (wave == 6) {
                asm volatile("; COND_BEGIN");
                if (G == GB - 1) mb = ld4_sc1(ws, WS_MB + (unsigned)((gi * 8 + gj) * 128 + (gi & 7) * 4));
                if (G == GB) {
                    WAIT1(mb, 3);
#pragma unroll 1
                    for (unsigned spins = 0; spins < kSpinCap && !all_set(mb); ++spins) {
                        if ((spins & 7u) == 7u && lds_word(lb + L_CNT + 8)) send_flag(lane);
                        mb = ld4_sc1(ws, WS_MB + (unsigned)((gi * 8 + gj) * 128 + (gi & 7) * 4));
                        WAIT1(mb, 0);
                    }
                    mb = ld4_sc1(ws, WS_MB + (unsigned)((gi * 8 + (gi & 7)) * 128 + (lane & 7) * 4));
                }
                if (G == GC) {
                    WAIT1(mb, 3 * (GC - GB));
#pragma unroll 1
                    for (unsigned spins = 0; spins < kSpinCap && !all_set(mb); ++spins) {
                        if ((spins & 7u) == 7u && lds_word(lb + L_CNT + 8)) send_flag(lane);
                        mb = ld4_sc1(ws, WS_MB + (unsigned)((gi * 8 + (gi & 7)) * 128 + (lane & 7) * 4));
                        WAIT1(mb, 0);
                    }
                    lds_set(lb + L_CNT + 92, (__builtin_amdgcn_readlane(mb, gi & 7) & 4u) ? 14u : 0u);
                    int d = min(max((int)(mb - (tst << 11)) >> 11, -400), 600);
                    d += (int)dppu<0xB1>((unsigned)d); d += (int)dppu<0x4E>((unsigned)d); d += (int)dppu<0x141>((unsigned)d);
                    const unsigned tm = tst + (unsigned)(__builtin_amdgcn_readfirstlane(d) >> 3);
                    lds_set(lb + L_CNT + 64, tm);
                    const float slow = (float)(now() - r0) * 24.f * 256.f * frcp((float)((unsigned)__builtin_amdgcn_s_memtime() - c0)) - 256.f;
                    lds_set(lb + L_CNT + 88, tm << 11 | (unsigned)__builtin_amdgcn_fmed3f(slow, 0.f, 63.f) << 5 | __builtin_popcount((unsigned)__builtin_amdgcn_ballot_w64((mb & 2u) != 0u) & 255u) << 1 | 1u);
                    report();
                    a[0] = ld16_sc1<0>(ws, ho); a[1] = ld16_sc1<1024>(ws, ho); a[2] = ld16_sc1<2048>(ws, ho); a[3] = ld16_sc1<3072>(ws, ho);
                    pace(XCOST, false);
                    STAMP(31);
                }
                asm volatile("; COND_END");
            } else if (G == GC) { for (int k = 0; k < 4; ++k) asm volatile("" : "=v"(a[k])); }
        };
        k_group(IC<0>{}); k_group(IC<1>{}); k_group(IC<2>{}); k_group(IC<3>{}); k_group(IC<4>{}); k_group(IC<5>{}); k_group(IC<6>{}); k_group(IC<7>{});
        {
            const int lane = lane_id();
            const unsigned v0 = (unsigned)((wave * 64 + 4 * (lane >> 4)) * 384 + (lane & 15) * 16);
            const unsigned v1 = v0 + 256 - (unsigned)((lane & 15) * 8);
            auto v_group = [&](auto HC) __attribute__((always_inline)) {
                constexpr int h = decltype(HC)::value, p = kVord[h], R = p < 8 ? RV + 16 * (p & 3) + 4 * (p >> 2) : RV + 64 + 2 * (p - 8), NV = v_upto(h);
                const unsigned o = (p < 8 ? v0 : v1) + (unsigned)((((p < 8 ? p >> 1 : p) + rot) & 3) * 6144);
                pace(2);
                if (p < 8) { ld16_nt<R, (p & 1) * 768>(vbase, o); ld16_nt<R + 8, (p & 1) * 768 + 384>(vbase, o); }
                else { ld8_nt<R, 0>(vbase, o); ld8_nt<R + 8, 384>(vbase, o); ld8_nt<R + 16, 768>(vbase, o); ld8_nt<R + 24, 1152>(vbase, o); }
                if (wave == 7 && gi + gj == 0) {
                    asm volatile("; COND_BEGIN");
                    if (h == 3 || h == 6) mb = ld4_sc1(ws, WS_R + (unsigned)((lane & 31) * 4));
                    if (h == 4 || h == 7) {
                        WAIT1(mb, NV - v_upto(h - 1));
                        if (all_set(mb) && !lds_word(lb + L_CNT + 36)) answer(lane, mb);
                    }
                    asm volatile("; COND_END");
                }
                if (wave == 6) {
                    asm volatile("; COND_BEGIN");
                if (h == HD) {
                    asm volatile("s_waitcnt vmcnt(%4)" : "+v"(a[0]), "+v"(a[1]), "+v"(a[2]), "+v"(a[3]) : "i"(3 * (7 - GC) + NV) : "memory");
                    float sq[6] = {0.f, 0.f, 0.f, 0.f, 0.f, 0.f};
#pragma unroll
                    for (int k = 0; k < 4; ++k) {
                        const float f = __uint_as_float(((((unsigned)__builtin_amdgcn_ds_bpermute((2 * k + (lane >> 5)) * 4, (int)mb) >> 3) & 255u) - 19u) << 23);
#pragma unroll
                        for (int e = 0; e < 2; ++e) {
                            const unsigned lo = a[k][2 * e], hi = a[k][2 * e + 1];
                            const float fe = (int)hi < 0 ? __builtin_nanf("") : f;
                            sq[3 * e] += (float)((int)(lo << 11) >> 11) * fe;
                            sq[3 * e + 1] += (float)((int)(__builtin_amdgcn_alignbit(hi, lo, 21) << 11) >> 11) * fe;
                            sq[3 * e + 2] += (float)((int)(hi << 1) >> 11) * fe;
                        }
                    }
#pragma unroll
                    for (int e = 0; e < 3; ++e) {
                        const int pi = 3 * (lane & 31) + e;
                        *reinterpret_cast<unsigned*>(smem + L_Q + (pi >> 3) * 32 + (pi & 6) * 2 + (pi & 1) * 16) = pk2(x32_add(sq[2 * e]), x32_add(sq[2 * e + 1]));
                    }
                    asm volatile("s_waitcnt lgkmcnt(0)\n\tds_write_b32 %0, %1" :: "v"(lb + L_CNT + 4), "v"(1u) : "memory");
                    STAMP(27);
                    lead = LEAD * PACE * 4;
                }
                    if (h == 6) mb = ld4_sc1(ws, WS_MB + (unsigned)((gi * 8 + (gi & 7)) * 128 + 32));
                    if (h == 8) {
                        WAIT1(mb, NV - v_upto(6));
                        const unsigned r = __builtin_amdgcn_readfirstlane(mb);
                        if (r) { adopt(r); STAMP(29); }
                    }
                    asm volatile("; COND_END");
                }
                if constexpr (h < 6) {
                    constexpr int i = 2 * h;
                    fix_k1(IC<i>{}, IC<20 - 6 * (i / 3) - i % 3 + NV>{});
                    fix_k1(IC<i + 1>{}, IC<20 - 6 * ((i + 1) / 3) - (i + 1) % 3 + NV>{});
                }
                if (h == 5) {
                    STAMP(1);
                    __builtin_amdgcn_s_setprio(0);
                    att_qk0();
                }
            };
            v_group(IC<0>{}); v_group(IC<1>{}); v_group(IC<2>{}); v_group(IC<3>{}); v_group(IC<4>{}); v_group(IC<5>{});
            v_group(IC<6>{}); v_group(IC<7>{}); v_group(IC<8>{}); v_group(IC<9>{}); v_group(IC<10>{}); v_group(IC<11>{});
            lds_add(16, 1u);
            if (wave == 3) lds_add(44, WEARLY);
        }
    }
    {
        STAMP(28);
        att_addr();
        const int lane = lane_id();
        cma = lb + L_M; cxa = lb + L_M + (unsigned)((lane >> 4) * 8); cra = lb + L_OP + (unsigned)(512 + (lane >> 4) * 2048 + (lane & 15) * 16);
        cso = WS_OWN + (unsigned)(gi * 16384 + gj * 512 + 256 + (lane >> 5) * 64 + (lane & 12) * 4 + (lane & 1) * 8192);
        PIN2(cma, cxa); PIN2(cra, cso);
        att_pv0();
    }
    {
        f32x4 m0, m1, o0, o1; f32x2 mm; unsigned r, taken;
        asm volatile("s_mov_b64 exec, 1\n\tds_add_rtn_u32 %5, %7, %8 offset:%11\n\ts_mov_b64 exec, -1\n\t"
                     "ds_read_b128 %0, %7\n\tds_read_b128 %1, %7 offset:16\n\tds_read_b64 %2, %9\n\tds_read_b128 %3, %10\n\tds_read_b128 %4, %10 offset:1024\n\t"
                     "ds_read_b32 %6, %7 offset:%12\n\ts_waitcnt lgkmcnt(0)"
                     : "=&v"(m0), "=&v"(m1), "=&v"(mm), "=&v"(o0), "=&v"(o1), "=&v"(r), "=&v"(taken)
                     : "v"(cma), "v"(1u), "v"(cxa), "v"(cra), "i"(L_CNT + 28 - L_M), "i"(L_CNT + 40 - L_M) : "memory");
        ord = __builtin_amdgcn_readfirstlane(r);
        if (ord == 7) { STAMP(5); comb_o1(m0, m1, mm, o0, o1, cso, 0x0000333300003333ull, true); lds_set(lb + L_CNT + 48, now() | 1u); STAMP(21); }
        MARK(e_t5);
        if (ord <= OML + 1 || (unsigned)__builtin_amdgcn_readfirstlane(taken) < 5u - WEARLY) {
        const int lane = lane_id();
        auto wait_cnt = [&](int off, unsigned v) __attribute__((always_inline)) {
#pragma unroll 1
            for (unsigned spins = 0; spins < (kSpinCap << 4); ++spins) { if (lds_word(lb + L_CNT + off) >= v) break; __builtin_amdgcn_s_sleep(1); }
        };
        if (ord == OML) {
            wait_cnt(16, 8u);
            unsigned xa, ra; ml_addr(lane, xa, ra);
            comb_ml(cma, xa, ra, WS_OWN + (unsigned)(gi * 16384 + gj * 512 + 384 + (lane & 1) * 8192), true);
            STAMP(24);
        } else if (ord == 0 && !lds_word(lb + L_CNT + 52)) {
            if (gi + gj == 0 && !lds_word(lb + L_CNT + 36)) {
                unsigned r = 0u;
#pragma unroll 1
                for (unsigned spins = 0; spins < kSpinCap; ++spins) {
                    r = ld4_sc1(ws, WS_R + (unsigned)((lane & 31) * 4));
                    WAIT1(r, 0);
                    if (all_set(r)) break;
                    if ((spins & 7u) == 7u) send_flag(lane);
                }
                answer(lane, r);
            }
#pragma unroll 1
            for (unsigned spins = 0; spins < kSpinCap; ++spins) {
                unsigned r = ld4_sc1(ws, WS_MB + (unsigned)((gi * 8 + (gi & 7)) * 128 + 32));
                WAIT1(r, 0);
                r = __builtin_amdgcn_readfirstlane(r);
                if (r) { adopt(r); break; }
                if ((spins & 7u) == 7u) { send_flag(lane); report(); }
                __builtin_amdgcn_s_sleep(16);
            }
            STAMP(29);
        } else if (ord == OML + 1) {
#pragma unroll 1
            for (unsigned spins = 0; spins < (kSpinCap << 6); ++spins) if (lds_word(lb + L_CNT + 24) >= 8u) break;
            const unsigned ra = lb + L_OP + (unsigned)((lane >> 5) * 4096 + (lane & 31) * 16);
            comb_o0(*(const LAS f32x4*)(size_t)cma, *(const LAS f32x4*)(size_t)(cma + 16), *(const LAS f32x4*)(size_t)(cma + (lane >> 5) * 16), *(const LAS f32x4*)(size_t)ra, *(const LAS f32x4*)(size_t)(ra + 1024),
                    *(const LAS f32x4*)(size_t)(ra + 2048), *(const LAS f32x4*)(size_t)(ra + 3072), WS_OWN + (unsigned)(gi * 16384 + gj * 512 + (lane & 60) * 4 + (lane & 1) * 8192), 0x3333333333333333ull, true);
            STAMP(22);
        }
        if (ord == OML) {
#pragma unroll 1
            for (unsigned spins = 0; spins < (kSpinCap << 8); ++spins) { if (lds_word(lb + L_CNT + 52)) break; __builtin_amdgcn_s_sleep(1); }
            const unsigned S = (lds_word(lb + L_CNT + 80) & 4u) << 11;
            empty4(0, S, lane); empty5(5, 0u, lane); empty5(5, 8192u, lane);
        }
#pragma unroll 1
        for (;;) {
            unsigned j;
            asm volatile("s_mov_b64 exec, 1\n\tds_add_rtn_u32 %0, %1, %2\n\ts_mov_b64 exec, -1\n\ts_waitcnt lgkmcnt(0)" : "=&v"(j) : "v"(lb + L_CNT + 40), "v"(1u) : "memory");
            j = __builtin_amdgcn_readfirstlane(j);
            if (j >= 5u - WEARLY) break;
            load_wo((int)j, lane, false); ++nj;
        }
        }
        STAMP(23);
    }

    unsigned omr, owa, owb;
    auto op_addr = [&](int layer) __attribute__((always_inline)) {
        LANE_VARS();
        const int t = wave - 3;
        cma = WS_OWN + rS + (unsigned)(gi * 16384 + layer * 4096 + (lane >> 3) * 512 + t * 128 + (t == 3 ? 0 : (lane & 7) * 16));
        cxa = lb + L_O2 + (unsigned)((lane >> 3) * 416 + t * 128 + (t == 3 ? 0 : (lane & 7) * 16));
        const int pb = (lane & 4) ? 3 - p4 : p4, jp = 2 * pb + ((lane >> 2) & 1);
        cso = tile35(gj, 10 * layer + t) + (unsigned)((kq & 1) * 8192 + gi * 128 + (kq >> 1) * 64 + p4 * 16);
        cra = lb + L_O2 + (unsigned)(jp * 416 + kq * 16); omr = lb + L_O2 + (unsigned)(jp * 416 + 384);
        owa = lb + L_WO + (unsigned)(r0s * 160 + 32 * t + 8 * p4); owb = lb + L_WO + (unsigned)(r1s * 160 + 32 * t + 8 * p4);
        if (layer) PIN2(cma, cxa);
        PIN2(cso, cra); PIN3(omr, owa, owb);
    };
    auto chain = [&](auto LC) __attribute__((always_inline)) {
        constexpr int layer = decltype(LC)::value;
        unsigned h4 = WS_H4 + (unsigned)((layer * 32 + gi) * 32768) + (layer ? fS : 0u);
        auto sides = [&]() __attribute__((always_inline)) {
            const unsigned m = lds_word(lb + L_CNT + 80);
            qS = (m & 2u) << 12; fS = (m & 4u) << 11; rS = (m & 8u) << 10; nS = lds_word(lb + L_CNT + 84); h4 += fS;
        };
        if (wave < 3) {
            LANE_VARS();
            if (layer == 0) {
                unsigned lo = WS_OWN + lds_word(lb + L_CNT + 84) + (unsigned)(gi * 16384 + (lane >> 3) * 512), lw = lb + L_O2 + (unsigned)((lane >> 3) * 416);
                unsigned lo0 = lo + (unsigned)((lane & 7) * 16), lw0 = lw + (unsigned)((lane & 7) * 16);
                PIN2(lo0, lo); PIN2(lw0, lw);
                GUARD(400);
                MARK(b_g2);
                if (__builtin_expect(wave == 2, 0)) { if (nj | (ord == OML)) { VMW(0); lds_add(44, nj); } }
                else if (__builtin_expect(wave == 1, 0)) {
#pragma unroll 1
                    for (unsigned spins = 0; spins < (kSpinCap << 4); ++spins) if (lds_word(lb + L_CNT + 48)) break;
#pragma unroll 1
                    for (unsigned spins = 0; spins < kSpinCap; ++spins) {
                        u32x4 o0 = ld16_sc1<0>(ws, lo0), o1 = ld16_sc1<128>(ws, lo0), o2 = ld16_sc1<384>(ws, lo);
                        if (nj) { VMW(3); lds_add(44, nj); nj = 0u; }
                        asm volatile("s_waitcnt vmcnt(0)" : "+v"(o0), "+v"(o1), "+v"(o2) :: "memory");
                        *(LAS u32x4*)(size_t)lw0 = o0; *(LAS u32x4*)(size_t)(lw0 + 128) = o1; *(LAS u32x4*)(size_t)(lw + 384) = o2;
                        if (!any_empty(max(max(um4(o0), um4(o1)), um4(o2)))) break;
                    }
                } else {
                    unsigned long long miss; unsigned cnt = kSpinCap << 4, tf, nv = nj;
                    asm volatile("1:\n\tds_read_b32 v4, %4 offset:48\n\ts_sub_u32 %1, %1, 1\n\ts_cbranch_scc1 5f\n\ts_waitcnt lgkmcnt(0)\n\tv_readfirstlane_b32 %2, v4\n\ts_cmp_eq_u32 %2, 0\n\ts_cbranch_scc1 1b\n\t"
                                 "s_add_u32 %2, %2, %8\n"
                                 "2:\n\ts_memrealtime vcc\n\ts_waitcnt lgkmcnt(0)\n\ts_sub_u32 vcc_lo, %2, vcc_lo\n\ts_cmp_lt_u32 vcc_lo, 0x400\n\ts_cbranch_scc1 2b\n"
                                 "5:\n\ts_mov_b32 %1, 15\n"
                                 "3:\n\tglobal_load_dwordx4 v[0:3], %5, %6 offset:256 sc1\n\ts_waitcnt vmcnt(1)\n\ts_mov_b64 exec, 1\n\tds_add_u32 %4, %3 offset:44\n\ts_mov_b64 exec, -1\n\tv_mov_b32 %3, 0\n\t"
                                 "s_sub_u32 %1, %1, 1\n\ts_cselect_b64 vcc, 0, -1\n\ts_waitcnt vmcnt(0)\n\tds_write_b128 %7, v[0:3] offset:256\n\tv_max3_u32 v4, v0, v1, v2\n\tv_max_u32 v4, v4, v3\n\t"
                                 "v_cmp_eq_u32 %0, -1, v4\n\ts_and_b64 vcc, vcc, %0\n\ts_cbranch_scc1 3b"
                                 : "=&s"(miss), "+s"(cnt), "=&s"(tf), "+v"(nv) : "v"(lb + L_CNT), "v"(lo0), "s"(ws), "v"(lw0), "i"(DG2 - 1) : "memory", "vcc", "scc", "v0", "v1", "v2", "v3", "v4");
                    if (__builtin_expect(miss != 0ull, 0)) {
#pragma unroll 1
                        for (unsigned spins = 0; spins < kSpinCap; ++spins) {
                            if ((spins & 15u) == 0u) send_flag(lane);
                            u32x4 o0 = ld16_sc1<256>(ws, lo0);
                            WAIT1(o0, 0);
                            *(LAS u32x4*)(size_t)(lw0 + 256) = o0;
                            if (!any_empty(um4(o0))) break;
                        }
                    }
                }
                MARK(e_g2);
                STAMP(25);
                BAR();
                sides();
                if (wave < 2) empty1(wave, qS, lane);
                else {
                    st16m(ws, WS_CS + (unsigned)((gi * 8 + gj) * 768 + lane * 16), *reinterpret_cast<const u32x4*>(smem + L_CS + lane * 16), 0xffffffffffffull);
                    empty5(10, 0u, lane); empty5(10, 8192u, lane);
                }
            } else BAR();
            {
                const unsigned im = lb + L_GU + (unsigned)(32 * wave + 8 * p4);
                Frag80 fg = frag80(im, 96, r0s, r1s, r2s), fu = frag80(im + 8192, 96, r0s, r1s, r2s);
                const unsigned xb = lb + L_XS + (unsigned)((n < 4 ? n : 4) * 160);
                unsigned b16 = xb + kq * 16, b8 = xb + 128 + kq * 8;
                unsigned sg = h4 + (unsigned)(n < 4 ? h4s(wave) + gj * 512 + kq * 64 + n * 16 : H4C + wave * 1024 + gj * 128 + kq * 16), su = sg + (n < 4 ? 256u : 64u);
                unsigned o = h4 + h4s(wave) + (unsigned)(lane * 16);
                unsigned oc = h4 + (unsigned)(H4C + wave * 1024 + ((lane >> 5) * 4 + p4) * 128 + (kq & 1) * 64 + q4 * 16);
                unsigned os = h4 + (unsigned)(H4T + (q4 + 4 * (kq & 1)) * 128 + p4 * 16);
                unsigned ha = lb + L_H + (unsigned)(p4 * 96 + wave * 32 + q4 * 8 + kq * 2);
                f32x2 cs = {0.f, 0.f};
                if (layer != 0) cs = *reinterpret_cast<const f32x2*>(smem + L_CS2 + wave * 512 + lane * 8);
                PIN3(fg.a0, fg.a1, fg.a2); PIN3(fu.a0, fu.a1, fu.a2); PIN3(b16, b8, sg); PIN3(su, o, os); PIN2(ha, cs);
                unsigned long long mk = layer ? 0x000f000f000f000full : 0x001f001f001f001full;
                unsigned ta = WS_MB + (unsigned)((lane >> 1) * 1024 + 64 + (lane & 1) * 16);
                if (layer == 0) PIN2(oc, ta);
                unsigned x4a = layer ? 0u : X4A * lds_word(lb + L_CNT + 20) >> 12;
                unsigned long long w0 = __builtin_amdgcn_ballot_w64(wave == 0);
                PINS(x4a); PINS(w0);
                unsigned long long rdm = 0x3acccccd3acccccdull;
                PINS(mk); PINS(rdm);
                if (layer == 0) VMW(0);
                if (layer == 0) GUARD(1200); else GUARD(800);
                MARK(b_gu);
                BAR();
                STAMP(10 + 32 * layer);
                u32x4 tl = {0u, 0u, 0u, 0u};
                if (layer == 0) asm volatile("s_mov_b64 exec, %3\n\tglobal_load_dwordx4 %0, %1, %2 sc1\n\ts_mov_b64 exec, -1" : "+v"(tl) : "v"(ta), "s"(ws), "s"(w0) : "memory");
                const Frag80 b = rows80(b16, b8);
                const f32x4 g0 = mfma32(fg.a0, b.a0, z4), u0 = mfma32(fu.a0, b.a0, z4), g1 = mfma32(fg.a1, b.a1, g0), g2 = mfma16(fg.a2, b.a2, z4), u1 = mfma32(fu.a1, b.a1, u0), u2 = mfma16(fu.a2, b.a2, z4);
                __builtin_amdgcn_sched_barrier(0);
                const f32x4 cg = g1 + g2, cu = u1 + u2;
                st16m2<0>(ws, sg, asu(cg), su, asu(cu), mk);
                STAMP(11 + 32 * layer);
                if (layer == 0 && wave != 0) {
                    unsigned tr = 0u;
#pragma unroll 1
                    for (unsigned spins = 0; spins < (kSpinCap << 6); ++spins) { tr = lds_word(lb + L_CNT + 56); if (tr) break; }
                    until(tr + x4a);
                } else if (layer == 0) {
                    WAIT1(tl, 2);
#pragma unroll 1
                    for (unsigned spins = 0; spins < kSpinCap; ++spins) {
                        const unsigned long long miss = __builtin_amdgcn_ballot_w64(min(min(tl[0], tl[1]), min(tl[2], tl[3])) == 0u);
                        if (!miss) break;
                        asm volatile("s_mov_b64 exec, %3\n\tglobal_load_dwordx4 %0, %1, %2 sc1\n\ts_mov_b64 exec, -1\n\ts_waitcnt vmcnt(0)" : "+v"(tl) : "v"(ta), "s"(ws), "s"(miss) : "memory");
                    }
                    const unsigned tn = now();
                    unsigned d = min(min(min(tn - tl[0], tn - tl[1]), min(tn - tl[2], tn - tl[3])), 800u);
                    d = min(d, dppu<0xB1>(d)); d = min(d, dppu<0x4E>(d)); d = min(d, dppu<0x124>(d)); d = min(d, dppu<0x128>(d));
                    unsigned tr = tn - min(min((unsigned)__builtin_amdgcn_readlane(d, 0), (unsigned)__builtin_amdgcn_readlane(d, 16)), min((unsigned)__builtin_amdgcn_readlane(d, 32), (unsigned)__builtin_amdgcn_readlane(d, 48)));
                    lds_set(lb + L_CNT + 56, tr | 1u);
                    until(__builtin_amdgcn_readfirstlane(tr | 1u) + x4a);
                } else sched(X4, 0);
                STAMP(16 + 32 * layer);
                float hv;
                u32x4 a0, a1, a2, a3, ac, as;
                auto load = [&]() __attribute__((always_inline)) {
                    as = ld16_sc1(ws, os);
                    if (layer == 0) ac = ld16_sc1(ws, oc);
                    a0 = ld16_sc1<0>(ws, o); a1 = ld16_sc1<1024>(ws, o); a2 = ld16_sc1<2048>(ws, o); a3 = ld16_sc1<3072>(ws, o);
                    if (layer == 0) lds_add(32, 1u);
                };
                auto all_in = [&]() __attribute__((always_inline)) {
                    if (layer == 0) asm volatile("s_waitcnt vmcnt(0)" : "+v"(a0), "+v"(a1), "+v"(a2), "+v"(a3), "+v"(ac), "+v"(as) :: "memory");
                    else { asm volatile("s_waitcnt vmcnt(0)" : "+v"(a0), "+v"(a1), "+v"(a2), "+v"(a3), "+v"(as) :: "memory"); ac = as; }
                };
                auto compute = [&]() __attribute__((always_inline)) {
#define ROR(n) " row_ror:" #n " row_mask:0xf bank_mask:0xf\n\t"
#define QD2(p) " quad_perm:" p " row_mask:0xf bank_mask:0xf\n\t"
                    asm volatile("s_waitcnt vmcnt(%16)\n\tv_add_f32_dpp v40, %2, %2" ROR(8) "v_add_f32_dpp v41, %3, %3" ROR(8) "s_nop 0\n\tv_add_f32_dpp v40, v40, v40" ROR(4) "v_add_f32_dpp v41, v41, v41" ROR(4)
                                 "v_mov_b32 v42, v40\n\tv_mov_b32 v43, v41\n\ts_nop 0\n\tv_permlane16_swap_b32 v40, v42\n\tv_permlane16_swap_b32 v41, v43\n\t"
                                 "v_pk_add_f32 v[40:41], v[40:41], v[42:43]\n\tv_pk_mul_f32 v[40:41], v[40:41], %14\n\t"
                                 ".if %17 == 0\n\ts_waitcnt vmcnt(4)\n\tv_mov_b64 v[44:45], %12\n\tv_mov_b64 v[46:47], %13\n\t.endif\n\t"
                                 "v_fma_f32 v40, -v41, v41, v40\n\tv_cvt_pk_bf16_f32 v42, 0, v41\n\tv_maximum3_f32 v40, v40, 0, 0\n\t"
                                 ".if %17 == 0\n\tv_permlane32_swap_b32 v44, v46\n\tv_permlane32_swap_b32 v45, v47\n\tv_pk_add_f32 v[44:45], v[44:45], v[46:47]\n\t.endif\n\t"
                                 "v_cvt_pk_bf16_f32 v40, 0, v40\n\ts_waitcnt vmcnt(2)\n\tv_pk_add_f32 v[32:33], %4, %6\n\tv_add_f32 v40, 0x3727c5ac, v40\n\tv_pk_add_f32 v[34:35], %5, %7\n\t"
                                 ".if %17 == 0\n\tv_permlane16_swap_b32 v44, v45\n\t.endif\n\t"
                                 "v_cvt_pk_bf16_f32 v40, 0, v40\n\ts_waitcnt vmcnt(0)\n\tv_pk_add_f32 v[36:37], %8, %10\n\tv_sqrt_f32 v40, v40\n\tv_pk_add_f32 v[38:39], %9, %11\n\t"
                                 ".if %17 == 0\n\tv_add_f32_dpp v44, v44, v44" QD2("[1,0,3,2]") "v_add_f32_dpp v45, v45, v45" QD2("[1,0,3,2]") ".endif\n\t"
                                 "v_pk_add_f32 v[32:33], v[32:33], v[36:37]\n\tv_pk_add_f32 v[34:35], v[34:35], v[38:39]\n\tv_cvt_pk_bf16_f32 v40, 0, v40\n\t"
                                 ".if %17 == 0\n\tv_add_f32_dpp v44, v44, v44" QD2("[2,3,0,1]") "v_add_f32_dpp v45, v45, v45" QD2("[2,3,0,1]") "v_mov_b64 %1, v[44:45]\n\t.else\n\ts_nop 0\n\t.endif\n\t"
                                 "v_permlane32_swap_b32 v32, v34\n\tv_permlane32_swap_b32 v33, v35\n\tv_rcp_f32 v40, v40\n\tv_pk_add_f32 v[32:33], v[32:33], v[34:35]\n\ts_nop 1\n\t"
                                 "v_permlane16_swap_b32 v32, v33\n\t"
                                 "v_pk_fma_f32 v[32:33], %1, v[42:43], v[32:33] op_sel_hi:[1,0,1] neg_lo:[1,0,0] neg_hi:[1,0,0]\n\tv_pk_mul_f32 v[32:33], v[32:33], v[40:41] op_sel_hi:[1,0]\n\t"
                                 "v_cvt_pk_bf16_f32 v32, 0, v32\n\tv_mul_f32 v34, 0xbfb8aa3b, v32\n\tv_exp_f32 v34, v34\n\tv_cvt_pk_bf16_f32 v33, 0, v33\n\tv_add_f32 v34, 1.0, v34\n\t"
                                 "v_rcp_f32 v34, v34\n\ts_nop 0\n\tv_mul_f32 v32, v34, v32\n\tv_cvt_pk_bf16_f32 v32, 0, v32\n\tv_mul_f32 %0, v33, v32\n\tv_cvt_pk_bf16_f32 v32, %0, %0\n\tds_write_b16 %15, v32"
                                 : "=&v"(hv), "+v"(cs)
                                 : "v"(as[0]), "v"(as[1]), "v"(LO2(a0)), "v"(HI2(a0)), "v"(LO2(a1)), "v"(HI2(a1)), "v"(LO2(a2)), "v"(HI2(a2)), "v"(LO2(a3)), "v"(HI2(a3)),
                                   "v"(LO2(layer ? as : ac)), "v"(HI2(layer ? as : ac)), "s"(rdm), "v"(ha), "i"(layer ? 4 : 5), "i"(layer), "v"(as)
                                 : "memory", "v32", "v33", "v34", "v35", "v36", "v37", "v38", "v39", "v40", "v41", "v42", "v43", "v44", "v45", "v46", "v47");
#undef ROR
#undef QD2
                };
                RETRY(load(), (all_in(), max(max(max(um4(a0), um4(a1)), max(um4(a2), um4(a3))), max(um4(ac), um4(as)))), compute(), any_nan(hv, hv), layer ? X4 : X4A);
                BAR();
                MARK(e_gu);
                STAMP(12 + 32 * layer);
                if (layer == 0) {
                    *reinterpret_cast<f32x2*>(smem + L_CS2 + wave * 512 + lane * 8) = cs;
                }
            }
            if (layer == 0) {
                if (wave == 1) {
                    const unsigned o = WS_CS + (unsigned)(gi * 6144 + 3072 + (lane < 48 ? lane : 0) * 16);
                    u32x4 a[8];
                    GATHER8(a, o, 768);
                    WAIT8(a, 0);
                    if (lane < 48) *reinterpret_cast<f32x4*>(smem + L_CS + lane * 16) = SUM8(a);
                }
                Frag80 f[4];
#pragma unroll
                for (int i = 0; i < 4; ++i) { f[i] = frag80(lb + L_WQ + (unsigned)(128 * wave + 32 * i + 8 * p4), 416, r0s, r1s, r2s); PIN3(f[i].a0, f[i].a1, f[i].a2); }
                unsigned b16 = lb + L_XS + (unsigned)(q4 * 160 + kq * 16), b8 = lb + L_XS + (unsigned)(q4 * 160 + 128 + kq * 8);
                unsigned so = WS_H1 + qS + (unsigned)((gi * 4 + q4) * 16384 + gj * 896 + 256 * wave + kq * 64 + p4 * 16);
                unsigned long long mk = 0x1111111111111111ull;
                PIN3(b16, b8, so); PINS(mk);
                GUARD(3800);
                MARK(b_q);
                BAR();
                STAMP(33);
                const Frag80 b = rows80(b16, b8);
                st16_sc1(ws, so, tiles4(f, b, mk));
                STAMP(34);
                qk_addr(true); att_addr();
                unsigned x0a = lb + L_M + (unsigned)((lane >> 5) * 16), x1a = lb + L_M + (unsigned)((lane >> 4) * 8), x2a, r2a;
                unsigned r0a = lb + L_OP + (unsigned)((lane >> 5) * 4096 + (lane & 31) * 16), r1a = lb + L_OP + (unsigned)(512 + (lane >> 4) * 2048 + (lane & 15) * 16);
                unsigned s0a = (unsigned)(lane * 4), s1a = (unsigned)(256 + (lane >> 5) * 64 + (lane & 15) * 4);
                ml_addr(lane, x2a, r2a);
                PIN3(x0a, x1a, x2a); PIN3(r0a, r1a, r2a); PIN2(s0a, s1a);
                cma = lb + L_M; cxa = wave == 0 ? x0a : wave == 1 ? x1a : x2a; cra = wave == 0 ? r0a : wave == 1 ? r1a : r2a;
                cso = WS_OWN + rS + (unsigned)(gi * 16384 + 4096 + gj * 512) + (wave == 0 ? s0a : wave == 1 ? s1a : 384u);
                PIN2(cma, cxa); PIN2(cra, cso);
                asm volatile("" : "=v"(omr), "=v"(owa), "=v"(owb));
            }
        } else {
            const int t = wave - 3;
            StatAddr sa;
            u32x4 sel;
            unsigned long long hi = 0xff00ff00ff00ff00ull;
            auto gather32 = [&](unsigned o, unsigned X, auto fin) __attribute__((always_inline)) {
                u32x4 a0 = ld16_sc1<-2048>(ws, o), a1 = ld16_sc1<-1024>(ws, o), a2 = ld16_sc1<0>(ws, o), a3 = ld16_sc1<1024>(ws, o);
                float v;
                auto sum = [&]() __attribute__((always_inline)) {
                    asm volatile("s_waitcnt vmcnt(3)\n\tv_mfma_f32_16x16x32_bf16 v[40:43], %1, %5, 0\n\ts_waitcnt vmcnt(2)\n\tv_mfma_f32_16x16x32_bf16 v[40:43], %2, %5, v[40:43]\n\t"
                                 "s_waitcnt vmcnt(1)\n\tv_mfma_f32_16x16x32_bf16 v[40:43], %3, %5, v[40:43]\n\ts_waitcnt vmcnt(0)\n\tv_mfma_f32_16x16x32_bf16 v[40:43], %4, %5, v[40:43]\n\t"
                                 "s_nop 7\n\tv_cndmask_b32 v40, v40, v41, %6\n\tv_cndmask_b32 v42, v42, v43, %6\n\ts_nop 1\n\tv_permlane32_swap_b32 v40, v42\n\tv_add_f32 v40, v40, v42\n\t"
                                 "v_cvt_pk_bf16_f32 %0, 0, v40"
                                 : "=&v"(v), "+v"(a0), "+v"(a1), "+v"(a2), "+v"(a3) : "v"(sel), "s"(hi) : "memory", "v40", "v41", "v42", "v43");
                    fin(v);
                    __builtin_amdgcn_sched_barrier(0);
                };
                sum();
                if (__builtin_expect(any_nan(v, v) != 0ull, 0)) {
#pragma unroll 1
                    for (unsigned spins = 0; spins < kSpinCap; ++spins) {
                        slip(X);
                        again16<-2048>(a0, ws, o); again16<-1024>(a1, ws, o); again16<0>(a2, ws, o); again16<1024>(a3, ws, o);
                        asm volatile("s_waitcnt vmcnt(0)" : "+v"(a0), "+v"(a1), "+v"(a2), "+v"(a3) :: "memory");
                        if (!any_empty(max(max(um4(a0), um4(a1)), max(um4(a2), um4(a3))))) break;
                    }
                    sum();
                }
            };
            auto poll32 = [&](unsigned o, unsigned t0) __attribute__((always_inline)) {
                    unsigned long long m0, m1, m2, m3; unsigned cnt = kSpinCap; float v;
#define CHK(r, m) "v_max3_u32 v44, v" #r ", v[" #r "+1], v[" #r "+2]\n\tv_max_u32 v44, v44, v[" #r "+3]\n\tv_cmp_eq_u32 %" #m ", -1, v44\n\t"
                    asm volatile("s_branch 3f\n"
                                 "1:\n\ts_mov_b64 exec, %1\n\tglobal_load_dwordx4 v[0:3], %6, %7 offset:-2048 sc1\n\ts_mov_b64 exec, %2\n\tglobal_load_dwordx4 v[4:7], %6, %7 offset:-1024 sc1\n\t"
                                 "s_mov_b64 exec, %3\n\tglobal_load_dwordx4 v[8:11], %6, %7 sc1\n\ts_mov_b64 exec, %4\n\tglobal_load_dwordx4 v[12:15], %6, %7 offset:1024 sc1\n\ts_mov_b64 exec, -1\n\t"
                                 "s_sub_u32 %5, %5, 1\n\ts_cselect_b64 vcc, 0, -1\n\ts_waitcnt vmcnt(0)\n\ts_branch 2f\n"
                                 "3:\n\ts_memrealtime vcc\n\ts_waitcnt lgkmcnt(0)\n\ts_sub_u32 vcc_lo, %10, vcc_lo\n\ts_cmp_lt_u32 vcc_lo, 0x400\n\ts_cbranch_scc1 3b\n\t"
                                 "global_load_dwordx4 v[0:3], %6, %7 offset:-2048 sc1\n\tglobal_load_dwordx4 v[4:7], %6, %7 offset:-1024 sc1\n\t"
                                 "global_load_dwordx4 v[8:11], %6, %7 sc1\n\tglobal_load_dwordx4 v[12:15], %6, %7 offset:1024 sc1\n\ts_mov_b64 vcc, -1\n"
                                 "2:\n\ts_waitcnt vmcnt(3)\n\tv_mfma_f32_16x16x32_bf16 v[40:43], v[0:3], %8, 0\n\t" CHK(0, 1) "s_waitcnt vmcnt(2)\n\tv_mfma_f32_16x16x32_bf16 v[40:43], v[4:7], %8, v[40:43]\n\t" CHK(4, 2)
                                 "s_waitcnt vmcnt(1)\n\tv_mfma_f32_16x16x32_bf16 v[40:43], v[8:11], %8, v[40:43]\n\t" CHK(8, 3) "s_waitcnt vmcnt(0)\n\tv_mfma_f32_16x16x32_bf16 v[40:43], v[12:15], %8, v[40:43]\n\t" CHK(12, 4)
                                 "s_or_b64 exec, %1, %2\n\ts_or_b64 exec, exec, %3\n\ts_or_b64 exec, exec, %4\n\ts_and_b64 vcc, exec, vcc\n\ts_cbranch_scc1 1b\n\t"
                                 "s_mov_b64 exec, -1\n\tv_cndmask_b32 v40, v40, v41, %9\n\tv_cndmask_b32 v42, v42, v43, %9\n\ts_nop 1\n\tv_permlane32_swap_b32 v40, v42\n\tv_add_f32 v40, v40, v42\n\t"
                                 "v_cvt_pk_bf16_f32 %0, 0, v40"
                                 : "=&v"(v), "=&s"(m0), "=&s"(m1), "=&s"(m2), "=&s"(m3), "+s"(cnt) : "v"(o), "s"(ws), "v"(sel), "s"(hi), "s"(t0 - 1u)
                                 : "memory", "vcc", "scc", "v0", "v1", "v2", "v3", "v4", "v5", "v6", "v7", "v8", "v9", "v10", "v11", "v12", "v13", "v14", "v15", "v40", "v41", "v42", "v43", "v44");
#undef CHK
                return v;
            };
#define PUT8(A, B, M) "v_min_u32 v34, -2, v32\n\tv_min_u32 v32, -2, v32\n\tv_min_u32 v35, -2, v33\n\tv_min_u32 v33, -2, v33\n\tv_permlane16_swap_b32 v32, v34\n\ts_nop 0\n\t" \
                      "v_permlane16_swap_b32 v33, v35\n\ts_mov_b64 exec, " M "\n\tglobal_store_dwordx4 " A ", v[32:35], " B " sc1\n\ts_mov_b64 exec, -1\n\ts_nop 0"
            unsigned xw, qo, qw, qc;
            __builtin_amdgcn_s_setprio(0);
            {
                STAMP(6 + 32 * layer);
                bf16x8 af[6];
                unsigned long long mk = 0x000f000f000f000full;
                if (layer == 0) {
                    const int lane = lane_id();
                    xe = lo16(*reinterpret_cast<const u16*>(smem + L_X0 + xrow(lane) * 1280 + gj * 160 + t * 32 + xcol(lane) * 2));
                    op_addr(0);
                    GUARD(1600);
                    MARK(b_op);
                    if (nj | (ord == OML)) { VMW(0); lds_add(44, nj); }
#pragma unroll 1
                    for (unsigned spins = 0; spins < (kSpinCap << 4); ++spins) if (lds_word(lb + L_CNT + 44) >= 5u) break;
                    STAMP(8);
                } else MARK(b_op);
                const unsigned lo = cma, lw = cxa, so = cso, br = cra, mr = omr, h3 = tile35(gj, 10 * layer + t);
                unsigned go, xs;
#pragma unroll
                for (int s = 0; s < 6; ++s) af[s] = cat8(tr64o(owa, s * 32 * 160), tr64o(owb, s * 32 * 160));
                PINS(mk); PINS(hi);
                if (layer != 0 && t < 4) {
                    sched(X2, 0);
                    STAMP(49);
                    u32x4 ov = ld16_sc1(ws, lo);
                    WAIT1(ov, 0);
                    *(LAS u32x4*)(size_t)lw = ov;
                    if (__builtin_expect(any_empty(um4(ov)), 0)) {
#pragma unroll 1
                        for (unsigned spins = 0; spins < kSpinCap; ++spins) {
                            slip(X2);
                            ov = ld16_sc1(ws, lo);
                            WAIT1(ov, 0);
                            *(LAS u32x4*)(size_t)lw = ov;
                            if (!any_empty(um4(ov))) break;
                        }
                    }
                }
                {
                    bf16x8 b0, b1, b2, b3, b4, b5;
#define QP " row_half_mirror row_mask:0xf bank_mask:0xf\n\t"
                    asm volatile("s_waitcnt lgkmcnt(0)\n\ts_barrier\n\t"
                                 "ds_read_b128 %0, %12\n\tds_read_b64 v[36:37], %13\n\tds_read_b128 %1, %12 offset:64\n\tds_read_b128 %2, %12 offset:128\n\tds_read_b128 %3, %12 offset:192\n\t"
                                 "ds_read_b128 %4, %12 offset:256\n\tds_read_b128 %5, %12 offset:320\n\t"
                                 "s_waitcnt lgkmcnt(5)\n\tv_mfma_f32_16x16x32_bf16 v[32:35], %6, %0, 0\n\tv_max_f32_dpp v38, v36, v36" QP "v_sub_f32 v38, v36, v38\n\t"
                                 "s_waitcnt lgkmcnt(4)\n\tv_mfma_f32_16x16x32_bf16 v[32:35], %7, %1, v[32:35]\n\tv_exp_f32 v38, v38\n\t"
                                 "s_waitcnt lgkmcnt(3)\n\tv_mfma_f32_16x16x32_bf16 v[32:35], %8, %2, v[32:35]\n\tv_mul_f32 v37, v37, v38\n\t"
                                 "s_waitcnt lgkmcnt(2)\n\tv_mfma_f32_16x16x32_bf16 v[32:35], %9, %3, v[32:35]\n\tv_add_f32_dpp v39, v37, v37" QP
                                 "s_waitcnt lgkmcnt(1)\n\tv_mfma_f32_16x16x32_bf16 v[32:35], %10, %4, v[32:35]\n\tv_rcp_f32 v39, v39\n\t"
                                 "s_waitcnt lgkmcnt(0)\n\tv_mfma_f32_16x16x32_bf16 v[32:35], %11, %5, v[32:35]\n\tv_mul_f32 v38, v38, v39\n\ts_nop 6\n\t"
                                 "v_pk_mul_f32 v[32:33], v[32:33], v[38:39] op_sel_hi:[1,0]\n\tv_pk_mul_f32 v[34:35], v[34:35], v[38:39] op_sel_hi:[1,0]\n\ts_nop 0\n\t"
                                 "v_add_f32_dpp v32, v32, v32" QP "v_add_f32_dpp v33, v33, v33" QP "v_add_f32_dpp v34, v34, v34" QP "v_add_f32_dpp v35, v35, v35" QP
                                 "v_cvt_pk_bf16_f32 v32, v32, v33\n\tv_cvt_pk_bf16_f32 v33, v34, v35\n\t" PUT8("%14", "%15", "%16")
                                 : "=&v"(b0), "=&v"(b1), "=&v"(b2), "=&v"(b3), "=&v"(b4), "=&v"(b5)
                                 : "v"(af[0]), "v"(af[1]), "v"(af[2]), "v"(af[3]), "v"(af[4]), "v"(af[5]), "v"(br), "v"(mr), "v"(so), "s"(ws), "s"(mk)
                                 : "memory", "v32", "v33", "v34", "v35", "v36", "v37", "v38", "v39");
#undef QP
                }
                if (layer == 0 && wave == 7) st4_sc1(ws, WS_MB + (unsigned)(gi * 1024 + 64 + gj * 4), now() | 1u);
                const unsigned tp = layer ? 0u : now();
                if (layer == 0) sides();
                STAMP(7 + 32 * layer);
                {
                    const int ln = lane_id();
#pragma unroll
                    for (int k = 0; k < 4; ++k) sel[k] = ((ln >> 1) & 3) == k ? (ln & 1) ? 0x3f800000u : 0x3f80u : 0u;
                    go = h3 + nS + (unsigned)(2048 + ln * 16); xs = lb + L_XS + (unsigned)(xrow(ln) * 160 + t * 32 + xcol(ln) * 2);
                    PIN3(sel, go, xs);
                    sa = stats_addr(ln, h4 + (unsigned)(H4T + gj * 128), 16);
                }
                if (layer != 0) sched(X3, 0);
                STAMP(18 + 32 * layer);
                if (layer != 0) gather32(go, X3, [&](float s) { xw = pk2(xe + s, 0.f); *(LAS u16*)(size_t)xs = (u16)xw; });
                else { xw = pk2(xe + poll32(go, tp + W3), 0.f); *(LAS u16*)(size_t)xs = (u16)xw; }
                xe = lo16(xw);
                STAMP(9 + 32 * layer);
            }
            BAR();
            if (wave == 3) stats(sa);
            MARK(e_op);
            STAMP(11 + 32 * layer);
            {
                LANE_VARS();
                const unsigned im = lb + L_WD + (unsigned)(32 * t + 8 * p4);
                bf16x8 wd0 = cat8(tr64(im + r0s * 160), tr64(im + r1s * 160)); bf16x4 wd1 = tr64(im + (32 + r2s) * 160);
                const unsigned h5 = tile35(gj, 10 * layer + 5 + t);
                unsigned h16 = lb + L_H + (unsigned)(p4 * 96 + kq * 16), h8 = lb + L_H + (unsigned)(p4 * 96 + 64 + kq * 8);
                unsigned so = h5 + (unsigned)((kq & 1) * 8192 + gi * 128 + (kq >> 1) * 64 + p4 * 16);
                unsigned long long mk = 0x000f000f000f000full;
                unsigned go = h5 + nS + (unsigned)(2048 + lane * 16), xs = (layer ? (unsigned)(xrow(lane) * 1280 + gj * 160) : lb + L_XS + (unsigned)(xrow(lane) * 160)) + (unsigned)(t * 32 + xcol(lane) * 2);
                unsigned sta = lb + L_ST + (unsigned)(xrow(lane) * 16);
                if (layer == 0 && wave < 6) {
                    sa = stats_addr(lane, WS_H1 + qS + (unsigned)(gi * 65536 + gj * 896 + 768), 16384);
                    asm volatile("" : "=v"(qo), "=v"(qw), "=v"(qc), "=v"(cma), "=v"(cxa), "=v"(cra), "=v"(cso), "=v"(omr), "=v"(owa), "=v"(owb));
                    asm volatile("" : "=v"(qa), "=v"(pw), "=v"(ma), "=v"(idx), "=v"(qm), "=v"(pr), "=v"(oa), "=v"(oc));
                } else if (layer == 0) {
                    op_addr(1); qk_addr(false); att_addr();
                    const int l5 = lane & 31, c = l5 < 24 ? 24 * (wave - 6) + l5 : 48;
                    qo = WS_H1 + qS + (unsigned)((gi * 4 + (gj >> 1)) * 16384 + 3584 + (lane >> 5) * 896 + c * 16);
                    qw = lb + L_Q + (unsigned)(l5 < 24 ? (c >> 4) * 128 + (c & 3) * 32 + (c & 12) + (lane >> 5) * 16 : lane < 26 ? 384 + (lane & 1) * 16 : 448);
                    qc = lb + L_CS + (unsigned)(c * 16 + (lane >> 5) * 8);
                    PIN3(qo, qw, qc);
                }
                PIN2(wd0, wd1); PIN3(h16, h8, so); PIN3(go, xs, sta); PINS(mk);
                GUARD(2000);
                MARK(b_dn);
                if (wave == 4) {
                    unsigned os = h4 + (unsigned)(H4T + (p4 + 4 * (kq & 1)) * 128 + q4 * 16);
                    float sw = *reinterpret_cast<const float*>(smem + L_CNT + 12);
                    PIN2(os, sw);
                    if (layer == 0) {
#pragma unroll 1
                        for (unsigned spins = 0; spins < (kSpinCap << 6); ++spins) if (lds_word(lb + L_CNT + 56)) break;
                    }
                    sched(layer ? X4 : X4A, 0);
                    u32x4 as;
#pragma unroll 1
                    for (unsigned spins = 0; spins < kSpinCap; ++spins) { as = ld16_sc1(ws, os); WAIT1(as, 0); if (!any_empty(um4(as))) break; }
                    float ss = __uint_as_float(as[0]), sm = __uint_as_float(as[1]), rd = __uint_as_float(as[2]);
                    ss += dpp<0x4E>(ss); sm += dpp<0x4E>(sm); rd += dpp<0x4E>(rd);
                    ss += dpp<0xB1>(ss); sm += dpp<0xB1>(sm); rd += dpp<0xB1>(rd);
                    ss = x16_add(ss); sm = x16_add(sm); rd = x16_add(rd);
                    if (LANES(0x1111ull)) *reinterpret_cast<f32x4*>(smem + L_ST + q4 * 16) = (f32x4){ss, sm, rd, sw};
                }
                if (layer == 0 && (wave == 3 || wave == 5)) {
                    unsigned ea = lane < 40 ? tile35(gj, 15 + (lane >> 3)) + (unsigned)(gi * 128 + (lane & 7) * 16) : WS_OWN + rS + (unsigned)(gi * 16384 + 4096 + gj * 512 + (lane - 40) * 16);
                    unsigned eb = ea + (lane < 40 ? 8192u : 256u);
                    PIN2(ea, eb);
#pragma unroll 1
                    for (unsigned spins = 0; spins < (kSpinCap << 6); ++spins) { if (lds_word(lb + L_CNT + 32) >= 6u) break; __builtin_amdgcn_s_sleep(1); }
                    if (wave == 5) empty4(1, fS, lane);
                    else { st16m(ws, ea, (u32x4){~0u, ~0u, ~0u, ~0u}, 0xffffffffffffffull); st16m(ws, eb, (u32x4){~0u, ~0u, ~0u, ~0u}, 0xffffffffffffffull); }
                }
                asm volatile("s_waitcnt lgkmcnt(0)\n\ts_barrier\n\tds_read_b128 v[36:39], %2\n\tds_read_b64 v[40:41], %3\n\t"
                             "s_waitcnt lgkmcnt(1)\n\tv_mfma_f32_16x16x32_bf16 v[32:35], %0, v[36:39], 0\n\ts_waitcnt lgkmcnt(0)\n\tv_mfma_f32_16x16x16_bf16 v[42:45], %1, v[40:41], 0\n\ts_nop 7\n\t"
                             "v_pk_add_f32 v[32:33], v[32:33], v[42:43]\n\tv_pk_add_f32 v[34:35], v[34:35], v[44:45]\n\t"
                             "v_cvt_pk_bf16_f32 v32, v32, v33\n\tv_cvt_pk_bf16_f32 v33, v34, v35\n\t" PUT8("%4", "%5", "%6")
                             :: "v"(wd0), "v"(wd1), "v"(h16), "v"(h8), "v"(so), "s"(ws), "s"(mk)
                             : "memory", "v32", "v33", "v34", "v35", "v36", "v37", "v38", "v39", "v40", "v41", "v42", "v43", "v44", "v45");
                STAMP(13 + 33 * layer);
                if (layer == 0 || __builtin_expect(gi == 20 + 2 * t + (gj & 1), 1)) {
                    const f32x4 sr = *(const LAS f32x4*)(size_t)sta;
                    const f32x2 st = ln_stats(sr[0], sr[1]);
                    float rs = bfr(frcp(1.f + ex2(bfr((sr[2] - st[0] * sr[3]) * st[1]) * -LOG2E))), n1 = bfr(bfr(xe - st[0]) * st[1]);
                    PIN2(rs, n1);
                    if (layer == 0) {
                        sched(X5A, 0);
                        STAMP(15);
                        gather32(go, X5A, [&](float y) { xw = pk2(xe + bfr(n1 + bfr(y * rs)), 0.f); *(LAS u16*)(size_t)xs = (u16)xw; });
                        xe = lo16(xw);
                        STAMP(14);
                    } else {
                        st2(out, xs, pk2(xe + bfr(n1 + bfr(poll32(go, when(X5B)) * rs)), 0.f));
                        asm volatile("s_endpgm");
                        MARK(e_dn);
                        STAMP(45);
                    }
                }
            }
            if (layer == 0) {
                BAR();
                if (wave == 3) { stats(sa); STAMP(34); }
                MARK(e_dn);
                if (wave == 3) __builtin_amdgcn_s_setprio(2);
                if (__builtin_expect(wave < 6, 0)) { op_addr(1); qk_addr(false); att_addr(); }
                if (wave >= 6) {
                    const unsigned o = qo;
                    f32x2 cs = -*(const LAS f32x2*)(size_t)qc;
                    float rn = 1.f / DM;
                    unsigned long long m0 = 1ull << 24, m1 = 1ull << 25;
                    PIN2(cs, rn); PINS(m0); PINS(m1);
                    MARK(b_qg);
                    sched(X1, 0);
                    STAMP(51);
                    u32x4 a0, a1, a2, a3; f32x2 u;
                    RETRY({ a0 = ld16_sc1<-3584>(ws, o); a1 = ld16_sc1<-1792>(ws, o); a2 = ld16_sc1<0>(ws, o); a3 = ld16_sc1<1792>(ws, o); },
                          ({ asm volatile("s_waitcnt vmcnt(0)" : "+v"(a0), "+v"(a1), "+v"(a2), "+v"(a3) :: "memory"); max(max(um4(a0), um4(a1)), max(um4(a2), um4(a3))); }),
                          { unsigned sm;
                            asm volatile("s_waitcnt vmcnt(2)\n\tv_pk_add_f32 v[40:41], %2, %4\n\tv_pk_add_f32 v[42:43], %3, %5\n\t"
                                         "s_waitcnt vmcnt(1)\n\tv_pk_add_f32 v[40:41], v[40:41], %6\n\tv_pk_add_f32 v[42:43], v[42:43], %7\n\t"
                                         "s_waitcnt vmcnt(0)\n\tv_pk_add_f32 v[40:41], v[40:41], %8\n\tv_pk_add_f32 v[42:43], v[42:43], %9\n\ts_nop 1\n\t"
                                         "v_permlane32_swap_b32 v40, v42\n\tv_permlane32_swap_b32 v41, v43\n\tv_pk_add_f32 v[40:41], v[40:41], v[42:43]\n\ts_nop 0\n\t"
                                         "v_readlane_b32 %1, v41, 24\n\ts_nop 1\n\tv_mul_f32 v42, %1, %11\n\tv_cvt_pk_bf16_f32 v42, 0, v42\n\tv_pk_fma_f32 %0, %10, v[42:43], v[40:41] op_sel_hi:[1,0,1]"
                                         : "=&v"(u), "=&s"(sm) : "v"(LO2(a0)), "v"(HI2(a0)), "v"(LO2(a1)), "v"(HI2(a1)), "v"(LO2(a2)), "v"(HI2(a2)), "v"(LO2(a3)), "v"(HI2(a3)), "v"(cs), "v"(rn)
                                         : "memory", "v40", "v41", "v42", "v43");
                            unsigned w = pk2v(u);
                            if (LANES(m0)) w = __float_as_uint(u[0]);
                            if (LANES(m1)) w = __float_as_uint(u[1]);
                            *(LAS unsigned*)(size_t)qw = w; __builtin_amdgcn_sched_barrier(0); }, any_nan(u[0], u[1]), X1);
                    MARK(e_qg);
                }
            }
        }
    };
    PINS(ck1); PINS(ck2); PINS(csc);
    chain(IC<0>{});
    STAMP(35);
    att1();
    chain(IC<1>{});
}

__device__ __attribute__((used, noinline)) void mega_code_end() { asm volatile(".Lmega_code_end:"); }

extern "C" void kernel_launch(void* const* d_in, const int* in_sizes, int n_in, void* d_out, int out_size, void* d_ws,
                              size_t ws_size, hipStream_t stream) {
    const u16* x0 = (const u16*)d_in[0];
    const u16* kc = (const u16*)d_in[1];
    const u16* vc = (const u16*)d_in[2];
    const u16* wq = (const u16*)d_in[3];
    const u16* wo = (const u16*)d_in[4];
    const u16* wr = (const u16*)d_in[6];
    const u16* wg = (const u16*)d_in[7];
    const u16* wu = (const u16*)d_in[8];
    const u16* wd = (const u16*)d_in[9];
    (void)hipFuncSetAttribute((const void*)mega_kernel, hipFuncAttributeMaxDynamicSharedMemorySize, L_END);
    mega_kernel<<<256, 512, L_END, stream>>>(x0, kc, vc, wq, wo, wr, wg, wu, wd, (u16*)d_out, (unsigned char*)d_ws
                                             );
}
```

```cpp
#include <hip/hip_runtime.h>
#include <hip/hip_bf16.h>
#include <stdint.h>

typedef unsigned short u16;
typedef unsigned u32x4 __attribute__((ext_vector_type(4)));
typedef unsigned u32x2 __attribute__((ext_vector_type(2)));
typedef unsigned long long u64x8 __attribute__((ext_vector_type(8)));
typedef unsigned long long u64x2 __attribute__((ext_vector_type(2)));
typedef float f32x4 __attribute__((ext_vector_type(4)));
typedef short s16x4 __attribute__((ext_vector_type(4)));
typedef __attribute__((ext_vector_type(4))) __bf16 bf16x4;
typedef __attribute__((ext_vector_type(8))) __bf16 bf16x8;
#define LAS __attribute__((address_space(3)))

constexpr int DM = 640, HQ = 32, SK = 4096, DH = 192, WIN = 1024, FF = 1536;
constexpr int QD = HQ * DH;
constexpr float SCALE = 0.07216878364870322f, LOG2E = 1.44269504088896341f;
constexpr unsigned kSpinCap = 1u << 16;
#define GJ(g) (((g) >> 3) & 7)
#define GI(g) (((g) & 7) * 4 + ((g) >> 6))

#define D4A 105
#define D5A 91
#define D1 84
#define D2 133
#define D3 66
#define D4 78
#define D5B 76
#define PACE 59
#define PINIT 100
#define PMIN 50
#define DM_ 0
constexpr unsigned X4A = D4A + DM_, X5A = X4A + D5A + DM_, X1 = X5A + D1 + DM_, X2 = X1 + D2 + DM_, X3 = X2 + D3 + DM_, X4 = X3 + D4 + DM_, X5B = X4 + D5B + DM_;
#define TLAND 132
#define XCOST 8
#define WOFF 6
#define BETA 11
constexpr unsigned WOC = 2, WEARLY = 1;
constexpr unsigned NUNIT = 384 + XCOST + 3 * (9 + 2 * WEARLY), PBURST = 30;
constexpr int LEAD = 12, TOFF = 4;
constexpr unsigned DG2 = 12, W3 = 15;
constexpr int GP = 0, GA = 1, GB = 6, GC = 7, HD = 1;
#define VORD 0, 1, 8, 2, 3, 9, 4, 5, 6, 7, 10, 11
constexpr int kVord[12] = {VORD};
constexpr int v_upto(int h) { int n = 0; for (int i = 0; i <= h; ++i) n += kVord[i] < 8 ? 2 : 4; return n; }
constexpr int v_after(int p) { int h = 0; while (kVord[h] != p) ++h; return 32 - v_upto(h); }
constexpr int OML = 2;

constexpr unsigned WS_H1 = 0;
constexpr unsigned WS_H4 = 2u << 20;
constexpr unsigned H4C = 20480, H4T = H4C + 3072;
constexpr unsigned WS_H35 = 4u << 20;
constexpr unsigned WS_OWN = WS_H35 + 80 * 16384;
constexpr unsigned WS_CS = WS_OWN + 32 * 16384;
constexpr unsigned WS_MB = WS_CS + 256 * 768;
constexpr unsigned WS_R = WS_MB + 256 * 128;
constexpr unsigned WS_END = WS_R + 128;
constexpr unsigned WS_Q0 = 6u << 20;
static_assert(WS_END <= (6u << 20), "ws map");
__device__ __forceinline__ unsigned tile35(int j, int q) { return WS_H35 + (unsigned)((j * 10 + (q >> 1)) * 16384 + (q & 1) * 4096); }
__device__ __forceinline__ unsigned h4s(int s) { return (unsigned)((s >> 1) * 16384 + (s & 1) * 4096); }

constexpr int L_WQ = 0;
constexpr int L_WO = 33792;
constexpr int L_GU = L_WO + 30720;
constexpr int L_WD = L_GU + 16384;
constexpr int L_X0 = L_WD + 8192;
constexpr int L_WR = L_X0 + 5120;
constexpr int L_XS = L_WR + 2048;
constexpr int L_Q = L_XS + 1024;
constexpr int L_OP = L_Q + 512;
constexpr int L_M = L_OP + 8192;
constexpr int L_H = L_M + 64;
constexpr int L_ST = L_H + 384;
constexpr int L_CS = L_ST + 64;
constexpr int L_CS2 = L_CS + 1024;
constexpr int L_CNT = L_CS2 + 1536;
constexpr int L_P = L_CNT + 96;
constexpr int L_O2 = L_P + 72 * 192;
constexpr int L_DBG = L_O2 + 3328;
constexpr int L_END = L_DBG + 2048;
static_assert(L_END <= 163840 && L_END > 81920, "LDS");

typedef __attribute__((ext_vector_type(2))) __bf16 bf16x2;
typedef float f32x2 __attribute__((ext_vector_type(2)));
__device__ __forceinline__ unsigned pk2(float a, float b) { return __builtin_bit_cast(unsigned, __builtin_convertvector((f32x2){a, b}, bf16x2)); }
__device__ __forceinline__ unsigned fexp(float x) { const unsigned e = (__float_as_uint(x) >> 23) & 255u; return e == 255u ? 0u : e; }
__device__ __forceinline__ u32x2 enc21(float a, float b, float c) {
    const float lim = 1048575.f;
    const unsigned i = (unsigned)(int)__builtin_rintf(__builtin_amdgcn_fmed3f(a, -lim, lim)) & 0x1fffffu, j = (unsigned)(int)__builtin_rintf(__builtin_amdgcn_fmed3f(b, -lim, lim)) & 0x1fffffu;
    const unsigned k = (unsigned)(int)__builtin_rintf(__builtin_amdgcn_fmed3f(c, -lim, lim)) & 0x1fffffu;
    return (u32x2){i | j << 21, j >> 11 | k << 10 | (__builtin_fabsf(a) + __builtin_fabsf(b) + __builtin_fabsf(c) < 1e30f ? 0u : 0x80000000u)};
}
__device__ __forceinline__ float lo16(unsigned w) { return __uint_as_float(w << 16); }
__device__ __forceinline__ float hi16(unsigned w) { return __uint_as_float(w & 0xffff0000u); }
__device__ __forceinline__ float bfr(float f) { return __uint_as_float(pk2(0.f, f)); }
template <int CTRL> __device__ __forceinline__ float dpp(float x) {
    return __builtin_bit_cast(float, __builtin_amdgcn_mov_dpp(__builtin_bit_cast(int, x), CTRL, 0xf, 0xf, true));
}
template <int CTRL> __device__ __forceinline__ unsigned dppu(unsigned x) { return (unsigned)__builtin_amdgcn_mov_dpp((int)x, CTRL, 0xf, 0xf, true); }
__device__ __forceinline__ float row16_sum(float v) {
    v += dpp<0xB1>(v); v += dpp<0x4E>(v); v += dpp<0x141>(v); v += dpp<0x140>(v); return v;
}
__device__ __forceinline__ float fmax2(float a, float b) { return __builtin_elementwise_maximum(a, b); }
__device__ __forceinline__ float fmax3(float a, float b, float c) { return fmax2(fmax2(a, b), c); }
__device__ __forceinline__ float x16_add(float v) { auto s = __builtin_amdgcn_permlane16_swap(__float_as_uint(v), __float_as_uint(v), false, false); return __uint_as_float(s[0]) + __uint_as_float(s[1]); }
__device__ __forceinline__ float x32_add(float v) { auto s = __builtin_amdgcn_permlane32_swap(__float_as_uint(v), __float_as_uint(v), false, false); return __uint_as_float(s[0]) + __uint_as_float(s[1]); }
__device__ __forceinline__ float wave_sum(float v) { return x32_add(x16_add(row16_sum(v))); }
__device__ __forceinline__ float rs32(float a, float b) { auto s = __builtin_amdgcn_permlane32_swap(__float_as_uint(a), __float_as_uint(b), false, false); return __uint_as_float(s[0]) + __uint_as_float(s[1]); }

#define GUARD(n) asm volatile("s_branch 1f\n\t.p2align 12,," #n "\n1:")
#define MARK(n) asm volatile(".Lcrit_" #n "_%=:" ::)
#define BAR() asm volatile("s_waitcnt lgkmcnt(0)\n\ts_barrier" ::: "memory")
#define VMW(n) asm volatile("s_waitcnt vmcnt(%0)" :: "i"(n) : "memory")

template <int OFF = 0> __device__ __forceinline__ u32x4 ld16_sc1(const void* sbase, unsigned voff) {
    u32x4 r; asm volatile("global_load_dwordx4 %0, %1, %2 offset:%3 sc1" : "=v"(r) : "v"(voff), "s"(sbase), "i"(OFF) : "memory"); return r;
}
__device__ __forceinline__ unsigned ld4_sc1(const void* sbase, unsigned voff) {
    unsigned r; asm volatile("global_load_dword %0, %1, %2 sc1" : "=v"(r) : "v"(voff), "s"(sbase) : "memory"); return r;
}
template <int OFF = 0> __device__ __forceinline__ void st16_sc1(void* sbase, unsigned voff, u32x4 v) {
    asm volatile("global_store_dwordx4 %0, %1, %2 offset:%3 sc1\n\ts_nop 1" :: "v"(voff), "v"(v), "s"(sbase), "i"(OFF) : "memory");
}
__device__ __forceinline__ void st4_sc1(void* sbase, unsigned voff, unsigned v) {
    asm volatile("global_store_dword %0, %1, %2 sc1" :: "v"(voff), "v"(v), "s"(sbase) : "memory");
}
__device__ __forceinline__ void st8(void* sbase, unsigned voff, u32x2 v) {
    asm volatile("global_store_dwordx2 %0, %1, %2" :: "v"(voff), "v"(v), "s"(sbase) : "memory");
}
__device__ __forceinline__ void st2(void* sbase, unsigned voff, unsigned v) {
    asm volatile("global_store_short %0, %1, %2" :: "v"(voff), "v"(v), "s"(sbase) : "memory");
}
constexpr int RK = 64, RV = 160;
template <int R, int IMM> __device__ __forceinline__ void ld16_nt(const void* sbase, unsigned voff) {
    asm volatile("global_load_dwordx4 v[%c3:%c4], %0, %1 offset:%2 nt" :: "v"(voff), "s"(sbase), "i"(IMM), "i"(R), "i"(R + 3) : "memory");
}
template <int R, int IMM> __device__ __forceinline__ void ld8_nt(const void* sbase, unsigned voff) {
    asm volatile("global_load_dwordx2 v[%c3:%c4], %0, %1 offset:%2 nt" :: "v"(voff), "s"(sbase), "i"(IMM), "i"(R), "i"(R + 1) : "memory");
}
template <int R> __device__ __forceinline__ void smf(f32x4& d, u32x4 a, unsigned idx) {
    asm volatile("v_smfmac_f32_16x16x64_bf16 %0, %1, v[%c3:%c4], %2" : "+v"(d) : "v"(a), "v"(idx), "i"(R), "i"(R + 7));
}
template <int A, int B> __device__ __forceinline__ void zip_in() {
    unsigned t;
    asm volatile("v_perm_b32 %0, v%c2, v%c1, %3\n\tv_perm_b32 v%c2, v%c2, v%c1, %4\n\tv_mov_b32 v%c1, %0" : "=&v"(t) : "i"(A), "i"(B), "s"(0x05040100u), "s"(0x07060302u));
}
template <int A, int B> __device__ __forceinline__ void zip_out(unsigned& lo, unsigned& hi) {
    asm volatile("v_perm_b32 %0, v%c3, v%c2, %4\n\tv_perm_b32 %1, v%c3, v%c2, %5" : "=&v"(lo), "=&v"(hi) : "i"(A), "i"(B), "s"(0x05040100u), "s"(0x07060302u));
}
template <int A, int B, int C, int D> __device__ __forceinline__ void zip_x(unsigned lo, unsigned hi) {
    asm volatile("v_perm_b32 v%c4, v%c3, v%c2, %6\n\tv_perm_b32 v%c5, v%c3, v%c2, %7\n\tv_mov_b32 v%c2, %0\n\tv_mov_b32 v%c3, %1"
                 :: "v"(lo), "v"(hi), "i"(A), "i"(B), "i"(C), "i"(D), "s"(0x05040100u), "s"(0x07060302u));
}
__device__ __forceinline__ void dma16(const void* sbase, unsigned voff, unsigned lds_dst) {
    unsigned keep;
    asm volatile("s_mov_b32 %0, m0\n\ts_mov_b32 m0, %3\n\ts_nop 0\n\tglobal_load_lds_dwordx4 %1, %2\n\ts_mov_b32 m0, %0"
                 : "=&s"(keep) : "v"(voff), "s"(sbase), "s"(lds_dst) : "memory");
}
__device__ __forceinline__ unsigned um4(u32x4 a) { return max(max(a[0], a[1]), max(a[2], a[3])); }
__device__ __forceinline__ bool all_set(unsigned t) { return __builtin_amdgcn_ballot_w64(t == 0u) == 0ull; }
__device__ __forceinline__ bool any_empty(unsigned mx) { return __builtin_amdgcn_ballot_w64(mx == ~0u) != 0ull; }
__device__ __forceinline__ unsigned long long any_nan(float a, float b) { return __builtin_amdgcn_ballot_w64(__builtin_isunordered(a, b)); }
__device__ __forceinline__ f32x4 asf(u32x4 a) { return __builtin_bit_cast(f32x4, a); }
__device__ __forceinline__ u32x4 asu(f32x4 a) { return __builtin_bit_cast(u32x4, a); }
#define LANES(m) __builtin_amdgcn_inverse_ballot_w64(m)

__device__ __forceinline__ bf16x4 tr64(unsigned lds_addr) { return __builtin_amdgcn_ds_read_tr16_b64_v4bf16((LAS bf16x4*)(size_t)lds_addr); }
__device__ __forceinline__ bf16x4 tr64o(unsigned lds_addr, int off) { return __builtin_amdgcn_ds_read_tr16_b64_v4bf16((LAS bf16x4*)((LAS char*)(size_t)lds_addr + off)); }
__device__ __forceinline__ bf16x8 cat8(bf16x4 a, bf16x4 b) { return __builtin_shufflevector(a, b, 0, 1, 2, 3, 4, 5, 6, 7); }
__device__ __forceinline__ bf16x8 lds16(unsigned a) { return *(const LAS bf16x8*)(size_t)a; }
__device__ __forceinline__ bf16x4 lds8(unsigned a) { return *(const LAS bf16x4*)(size_t)a; }
__device__ __forceinline__ f32x4 mfma32(bf16x8 a, bf16x8 b, f32x4 c) { return __builtin_amdgcn_mfma_f32_16x16x32_bf16(a, b, c, 0, 0, 0); }
__device__ __forceinline__ f32x4 mfma32u(u32x4 a, u32x4 b, f32x4 c) { return mfma32(__builtin_bit_cast(bf16x8, a), __builtin_bit_cast(bf16x8, b), c); }
__device__ __forceinline__ f32x4 mfma16(bf16x4 a, bf16x4 b, f32x4 c) {
    return __builtin_amdgcn_mfma_f32_16x16x16bf16_1k(__builtin_bit_cast(s16x4, a), __builtin_bit_cast(s16x4, b), c, 0, 0, 0);
}

#define STAMP(i) do {} while (0)

template <int OFF = 0> __device__ __forceinline__ void st16m(void* sbase, unsigned voff, u32x4 v, unsigned long long mask) {
    asm volatile("s_mov_b64 exec, %4\n\tglobal_store_dwordx4 %0, %1, %2 offset:%3 sc1\n\ts_mov_b64 exec, -1\n\ts_nop 0" :: "v"(voff), "v"(v), "s"(sbase), "i"(OFF), "s"(mask) : "memory");
}
template <int OFF> __device__ __forceinline__ void st16m2(void* sbase, unsigned voff, u32x4 v, unsigned voff2, u32x4 v2, unsigned long long mask) {
    asm volatile("s_mov_b64 exec, %6\n\tglobal_store_dwordx4 %0, %1, %2 sc1\n\tglobal_store_dwordx4 %4, %5, %2 offset:%3 sc1\n\ts_mov_b64 exec, -1\n\ts_nop 0"
                 :: "v"(voff), "v"(v), "s"(sbase), "i"(OFF), "v"(voff2), "v"(v2), "s"(mask) : "memory");
}
#define PINS(a) asm volatile("" : "+s"(a))
#define RETRY(LOAD, EMPTY, COMPUTE, NAN_, X) do { LOAD; COMPUTE; if (__builtin_expect((NAN_) != 0ull, 0)) { \
    _Pragma("unroll 1") for (unsigned spins_ = 0; spins_ < kSpinCap; ++spins_) { slip(X); LOAD; if (!any_empty(EMPTY)) break; } COMPUTE; } } while (0)
__device__ __forceinline__ unsigned now() { return (unsigned)__builtin_amdgcn_s_memrealtime(); }
__device__ __forceinline__ void until(unsigned t) {
    asm volatile("1:\n\ts_memrealtime vcc\n\ts_waitcnt lgkmcnt(0)\n\ts_sub_u32 vcc_lo, %0, vcc_lo\n\ts_cmp_lt_u32 vcc_lo, 0x400\n\ts_cbranch_scc1 1b" :: "s"(t - 1u) : "vcc", "scc", "memory");
}
__device__ __forceinline__ unsigned lds_word(unsigned a) {
    unsigned t_; asm volatile("ds_read_b32 %0, %1\n\ts_waitcnt lgkmcnt(0)\n\ts_nop 0" : "=v"(t_) : "v"(a) : "memory");
    return __builtin_amdgcn_readfirstlane(t_);
}
__device__ __forceinline__ void lds_set(unsigned a, unsigned v) { asm volatile("ds_write_b32 %0, %1" :: "v"(a), "v"(v) : "memory"); }
__device__ __forceinline__ int xrow(int lane) { return (lane >> 5) * 2 + ((lane >> 3) & 1); }
__device__ __forceinline__ int xcol(int lane) { return ((lane >> 4) & 1) * 8 + (lane & 7); }
__device__ __forceinline__ int swz(int r) { return r ^ (((r >> 3) & 1) << 2); }
__device__ __forceinline__ f32x2 unpk(unsigned w) { return (f32x2){lo16(w), hi16(w)}; }
__device__ __forceinline__ unsigned pk2v(f32x2 v) { return __builtin_bit_cast(unsigned, __builtin_convertvector(v, bf16x2)); }
__device__ __forceinline__ float frcp(float x) { return __builtin_amdgcn_rcpf(x); }
__device__ __forceinline__ float ex2(float x) { return __builtin_amdgcn_exp2f(x); }
__device__ __forceinline__ int lane_id() { int l; asm volatile("v_mbcnt_lo_u32_b32 %0, -1, 0\n\tv_mbcnt_hi_u32_b32 %0, -1, %0" : "=v"(l)); return l & 63; }
__device__ __forceinline__ f32x2 ln_stats(float ss, float s) {
    const float mean = s * (1.f / DM);
    const float var = fmax2(__builtin_fmaf(-mean, mean, ss * (1.f / DM)), 0.f);
    return (f32x2){bfr(mean), frcp(bfr(__builtin_amdgcn_sqrtf(bfr(bfr(var) + 1e-5f))))};
}
#define GATHER8(a, o, S) do { a[0] = ld16_sc1<-4 * (S)>(ws, o); a[1] = ld16_sc1<-3 * (S)>(ws, o); a[2] = ld16_sc1<-2 * (S)>(ws, o); a[3] = ld16_sc1<-(S)>(ws, o); \
    a[4] = ld16_sc1<0>(ws, o); a[5] = ld16_sc1<(S)>(ws, o); a[6] = ld16_sc1<2 * (S)>(ws, o); a[7] = ld16_sc1<3 * (S)>(ws, o); } while (0)
template <int OFF> __device__ __forceinline__ void again16(u32x4& r, const void* sbase, unsigned voff) {
    asm volatile("v_cmp_eq_u32 vcc, -1, %1\n\ts_mov_b64 exec, vcc\n\tglobal_load_dwordx4 %0, %2, %3 offset:%4 sc1\n\ts_mov_b64 exec, -1" : "+v"(r) : "v"(um4(r)), "v"(voff), "s"(sbase), "i"(OFF) : "memory", "vcc");
}
#define WAIT8(a, N) asm volatile("s_waitcnt vmcnt(%8)" : "+v"((a)[0]), "+v"((a)[1]), "+v"((a)[2]), "+v"((a)[3]), "+v"((a)[4]), "+v"((a)[5]), "+v"((a)[6]), "+v"((a)[7]) : "i"(N) : "memory")
#define SUM8(a) (((((((asf(a[0]) + asf(a[1])) + asf(a[2])) + asf(a[3])) + asf(a[4])) + asf(a[5])) + asf(a[6])) + asf(a[7]))
#define WAIT1(x, N) asm volatile("s_waitcnt vmcnt(%1)" : "+v"(x) : "i"(N) : "memory")
#define LO2(x) __builtin_shufflevector(x, x, 0, 1)
#define HI2(x) __builtin_shufflevector(x, x, 2, 3)
#define PIN1(a) asm volatile("" : "+v"(a))
#define PIN2(a, b) asm volatile("" : "+v"(a), "+v"(b))
#define PIN3(a, b, c) asm volatile("" : "+v"(a), "+v"(b), "+v"(c))
#define LANE_VARS() const int lane = lane_id(); \
    const int kq = lane >> 4, n = lane & 15, q4 = n >> 2, p4 = lane & 3; \
    const int r0s = 8 * kq + q4 + 4 * (kq & 1), r1s = 8 * kq + q4 + 4 * ((kq & 1) ^ 1);     \
    const int r2s = swz(4 * kq + q4);                                                       \
    (void)r0s; (void)r1s; (void)r2s; (void)p4
template <int V> struct IC { static constexpr int value = V; };
struct Frag80 { bf16x8 a0, a1; bf16x4 a2; };

__global__ void __launch_bounds__(512, 2) __attribute__((amdgpu_num_vgpr(32)))
mega_kernel(const u16*, const u16*, const u16*, const u16*, const u16*, const u16*, const u16*, const u16*, const u16*, u16*, unsigned char*
            ) {
    extern __shared__ __attribute__((aligned(16))) unsigned char smem[];
    const unsigned lb = (unsigned)(size_t)smem;
    const int wave = __builtin_amdgcn_readfirstlane(threadIdx.x >> 6);
    u64x8 ar0; u64x2 ar1; unsigned long long ar2, tp, tst64;
    asm volatile("s_load_dwordx16 %0, %5, 0\n\ts_load_dwordx4 %1, %5, 0x40\n\ts_load_dwordx2 %2, %5, 0x50\n\ts_load_dwordx2 %3, %5, %6\n\ts_memrealtime %4"
                 : "=&s"(ar0), "=&s"(ar1), "=&s"(ar2), "=&s"(tp), "=&s"(tst64) : "s"(__builtin_amdgcn_kernarg_segment_ptr()), "s"((0xa9876421u >> (4 * wave) & 15u) * 8u) : "memory");
    const int g = blockIdx.x, gj = GJ(g), gi = GI(g);
    const f32x4 z4 = {0.f, 0.f, 0.f, 0.f};
    asm volatile("" ::: "v255");
    auto when = [&](unsigned X) __attribute__((always_inline)) {
        u32x2 v;
        asm volatile("ds_read2_b32 %0, %1 offset0:14 offset1:5\n\ts_waitcnt lgkmcnt(0)" : "=v"(v) : "v"(lb + L_CNT) : "memory");
        return __builtin_amdgcn_readfirstlane(v[0]) + (X * __builtin_amdgcn_readfirstlane(v[1]) >> 12);
    };
    auto sched = [&](unsigned X, unsigned add) __attribute__((always_inline)) { until(when(X) + add); };
    auto slip = [&](unsigned X) __attribute__((always_inline)) {
        if (X) lds_set(lb + L_CNT + 56, now() - (X * lds_word(lb + L_CNT + 20) >> 12));
    };
    float xe = 0.f;

    auto code_pf = [&](int slice, int lane) __attribute__((always_inline)) {
        unsigned long long pc0; unsigned len, back, t;
        asm volatile("s_getpc_b64 %0\n.Lmega_pf%=:\n\ts_mov_b32 %1, .Lmega_code_end-.Lmega_code_begin\n\ts_mov_b32 %2, .Lmega_pf%=-.Lmega_code_begin" : "=s"(pc0), "=s"(len), "=s"(back));
        unsigned off = (unsigned)(slice * 4096 + lane * 64); if (off > len) off = len;
        asm volatile("global_load_dword %0, %1, %2" : "=v"(t) : "v"(off & ~3u), "s"(pc0 - back) : "memory");
        return t;
    };
    asm volatile(".Lmega_code_begin:");
    unsigned pf = 0u, pt1;
    const unsigned wofs = (unsigned)((((gj >> 1) * HQ + gi) * SK + (SK - WIN) + (gj & 1) * 512) * (DH * 2));
    {
        const int lane = threadIdx.x & 63, hi = lane >= 26;
        if (threadIdx.x < 24) lds_set(lb + L_CNT + threadIdx.x * 4, 0u);
        unsigned t2 = (unsigned)(gi * (DH * DM * 2)), t7 = (32u << 20) + (unsigned)(g * 128), to = (unsigned)(gj * 128), w2 = wofs;
        PINS(t2); PINS(t7); PINS(to); PINS(w2);
        to = wave == 7 ? t7 : to; to = wave == 2 ? t2 : to; to = wave < 2 ? w2 : to;
        if (wave == 7) pf = code_pf((g >> 3) & 15, lane);
        unsigned vo = (unsigned)((gj * 80 + swz(2 * wave) + hi) * (QD * 2) + gi * (DH * 2) + (lane - 26 * hi) * 16), d = lb + L_WQ + wave * 832, keep;
        unsigned xo = (0x11043210u >> (4 * wave) & 15u) * 1024 + (unsigned)(lane & (wave < 6 ? 63 : 15)) * 16, xd = lb + L_X0 + min(wave, 6) * 1024;
        PIN2(vo, xo); PINS(d); PINS(xd); PINS(to);
        __builtin_amdgcn_sched_barrier(0);
        asm volatile("s_waitcnt lgkmcnt(0)\n\ts_load_dword %0, %4, %6" : "=&s"(pt1), "+s"(ar0), "+s"(ar1), "+s"(ar2), "+s"(tp), "+s"(tst64) : "s"(to) : "memory");
        dma16((const void*)(wave < 5 ? ar0[0] : ar0[5]), xo, xd);
        asm volatile("s_mov_b32 %0, m0\n\ts_mov_b32 m0, %2\n\ts_mov_b64 exec, %4\n\tglobal_load_lds_dwordx4 %1, %3 nt\n\t"
                     ".rept 4\n\ts_add_u32 m0, m0, 6656\n\tv_add_u32 %1, 0x30000, %1\n\tglobal_load_lds_dwordx4 %1, %3 nt\n\t.endr\n\t"
                     "s_mov_b64 exec, -1\n\ts_mov_b32 m0, %0\n\ts_barrier"
                     : "=&s"(keep), "+v"(vo) : "s"(d), "s"(ar0[3]), "s"(0x0003fffffcffffffull) : "memory", "scc");
        __builtin_amdgcn_sched_barrier(0);
    }
    const u16 *const kc = (const u16*)ar0[1], *const vc = (const u16*)ar0[2], *const wo = (const u16*)ar0[4], *const wg = (const u16*)ar0[6], *const wu = (const u16*)ar0[7], *const wd = (const u16*)ar1[0];
    u16* const out = (u16*)ar1[1];
    unsigned char* const ws = (unsigned char*)((ar2 + (32u << 20) - 1) & ~(unsigned long long)((16u << 20) - 1));
    const unsigned tst = (unsigned)tst64;
    auto fix_k1 = [&](auto IC_, auto NC) __attribute__((always_inline)) {
        constexpr int A = RK + 8 * decltype(IC_)::value, B = A + 4;
        unsigned t0, t1, t2, t3;
        asm volatile("s_waitcnt vmcnt(%12)\n\tv_mov_b32 %0, v%c4\n\tv_mov_b32 %1, v%c5\n\tv_mov_b32 %2, v%c6\n\tv_mov_b32 %3, v%c7\n\t"
                     "v_mov_b32_dpp v%c4, v%c8 row_ror:8 row_mask:0xf bank_mask:0xc\n\tv_mov_b32_dpp v%c5, v%c9 row_ror:8 row_mask:0xf bank_mask:0xc\n\t"
                     "v_mov_b32_dpp v%c6, v%c10 row_ror:8 row_mask:0xf bank_mask:0xc\n\tv_mov_b32_dpp v%c7, v%c11 row_ror:8 row_mask:0xf bank_mask:0xc\n\t"
                     "v_mov_b32_dpp v%c8, %0 row_ror:8 row_mask:0xf bank_mask:0x3\n\tv_mov_b32_dpp v%c9, %1 row_ror:8 row_mask:0xf bank_mask:0x3\n\t"
                     "v_mov_b32_dpp v%c10, %2 row_ror:8 row_mask:0xf bank_mask:0x3\n\tv_mov_b32_dpp v%c11, %3 row_ror:8 row_mask:0xf bank_mask:0x3"
                     : "=&v"(t0), "=&v"(t1), "=&v"(t2), "=&v"(t3)
                     : "i"(A), "i"(A + 1), "i"(A + 2), "i"(A + 3), "i"(B), "i"(B + 1), "i"(B + 2), "i"(B + 3), "i"(decltype(NC)::value) : "memory");
    };
    auto trans_v = [&](auto UC) __attribute__((always_inline)) {
        constexpr int R = RV + 4 * decltype(UC)::value, P = 4 * decltype(UC)::value;
#define CELL(r, c) R + 16 * (r) + (c), R + 16 * (r) + 8 + (c)
        unsigned a0, a1, b0, b1, c0, c1;
        VMW(v_after(P)); zip_in<CELL(0, 0)>(); zip_out<CELL(0, 1)>(a0, a1); zip_out<CELL(0, 2)>(b0, b1); zip_out<CELL(0, 3)>(c0, c1);
        VMW(v_after(P + 1)); zip_x<CELL(1, 0), CELL(0, 1)>(a0, a1); zip_in<CELL(1, 1)>(); zip_out<CELL(1, 2)>(a0, a1);
        VMW(v_after(P + 2)); zip_x<CELL(2, 0), CELL(0, 2)>(b0, b1); zip_x<CELL(2, 1), CELL(1, 2)>(a0, a1); zip_in<CELL(2, 2)>(); zip_out<CELL(1, 3)>(a0, a1); zip_out<CELL(2, 3)>(b0, b1);
        VMW(v_after(P + 3)); zip_x<CELL(3, 0), CELL(0, 3)>(c0, c1); zip_x<CELL(3, 1), CELL(1, 3)>(a0, a1); zip_x<CELL(3, 2), CELL(2, 3)>(b0, b1); zip_in<CELL(3, 3)>();
#undef CELL
    };
    auto trans_w = [&](auto UC) __attribute__((always_inline)) {
        constexpr int R = RV + 64 + 2 * decltype(UC)::value, N = v_after(8 + decltype(UC)::value);
        unsigned a0, a1;
        VMW(N + 2); zip_in<R, R + 8>(); zip_out<R + 1, R + 9>(a0, a1);
        VMW(N); zip_x<R + 16, R + 24, R + 1, R + 9>(a0, a1); zip_in<R + 17, R + 25>();
    };
    auto frag80 = [&](unsigned im, int rs, int r0s, int r1s, int r2s) __attribute__((always_inline)) -> Frag80 {
        return Frag80{cat8(tr64(im + r0s * rs), tr64(im + r1s * rs)), cat8(tr64(im + (r0s + 32) * rs), tr64(im + (r1s + 32) * rs)), tr64(im + (r2s + 64) * rs)};
    };
    auto rows80 = [&](unsigned a16, unsigned a8) __attribute__((always_inline)) -> Frag80 { const Frag80 f{lds16(a16), lds16(a16 + 64), lds8(a8)}; __builtin_amdgcn_sched_barrier(0); return f; };
    auto empty5 = [&](int q0, unsigned S, int lane) __attribute__((always_inline)) {
        st16m(ws, tile35(gj, q0 + (lane >> 3)) + S + (unsigned)(gi * 128 + (lane & 7) * 16), (u32x4){~0u, ~0u, ~0u, ~0u}, 0xffffffffffull);
    };
    auto empty4 = [&](int layer, unsigned S, int lane) __attribute__((always_inline)) {
        const unsigned b = WS_H4 + (unsigned)((layer * 32 + gi) * 32768) + S;
        st16_sc1(ws, b + (unsigned)((lane >> 5) * 4096 + gj * 512 + (lane & 31) * 16), (u32x4){~0u, ~0u, ~0u, ~0u});
        st16m(ws, b + (unsigned)(lane < 32 ? 16384 + gj * 512 + lane * 16 : H4C + ((lane - 32) >> 3) * 1024 + gj * 128 + (lane & 7) * 16), (u32x4){~0u, ~0u, ~0u, ~0u},
              layer ? 0xff000000ffffffffull : ~0ull);
    };
    int lead = wave == 7 ? LEAD * PACE * 4 : 0;
    auto pace = [&](unsigned n, bool wait = true) __attribute__((always_inline)) {
        unsigned u, w, t0;
        asm volatile("s_mov_b64 exec, 1\n\tds_add_rtn_u32 %0, %3, %4\n\tds_max_u32 %3, %5 offset:12\n\tds_add_rtn_u32 %1, %3, %6 offset:12\n\tds_read_b32 %2, %3 offset:4\n\ts_mov_b64 exec, -1\n\ts_waitcnt lgkmcnt(0)"
                     : "=&v"(u), "=&v"(w), "=&v"(t0) : "v"(lb + L_CNT + 60), "v"(n), "v"((now() - tst) << 6), "v"(n * PMIN * 4u) : "memory");
        int tn = (int)(__builtin_amdgcn_readfirstlane(t0) - tst + PINIT) * 64 + __builtin_amdgcn_readfirstlane(u) * (PACE * 4);
        const int tw = __builtin_amdgcn_readfirstlane(w) - PBURST * 64;
        if (tw > tn) tn = tw;
        tn -= lead;
        if (wait) {
#pragma unroll 1
            for (int k = 0; k < 2048 && (int)((now() - tst) << 6) < tn; ++k) {}
        }
    };
    auto slot = [&](unsigned u) __attribute__((always_inline)) {
        const unsigned tn = lds_word(lb + L_CNT + 64) + PINIT + u * PACE / 16;
#pragma unroll 1
        for (int k = 0; k < 8192 && (int)(now() - tn) < 0; ++k) __builtin_amdgcn_s_sleep(1);
    };
    auto load_wo = [&](int k, int lane, bool early) __attribute__((always_inline)) {
        const int c = k * 64 + lane, pp = c / 10, cc = c - 10 * pp;
        const unsigned f = (unsigned)(swz(pp) * 1280 + cc * 16);
        const u16* wb = wo + (size_t)gi * DH * DM + gj * 80;
#pragma unroll
        for (int a2 = 0; a2 < 6; ++a2) { if (a2 % 3 == 0) { if (early) pace(3); else slot(NUNIT - WOFF + (2 * k + a2 / 3) * WOC); } dma16(wb + 32 * a2 * DM, f, lb + L_WO + (5 * a2 + k) * 1024); }
    };
    auto load_wd = [&](int lane) __attribute__((always_inline)) {
        const u16* db = wd + gi * 48 * DM + gj * 80;
#pragma unroll 1
        for (int k = 0; k < 5; ++k) {
            const int c = k * 64 + lane, pp = c / 10, cc = c - 10 * pp;
            const unsigned fd = (unsigned)(swz(pp) * 1280 + cc * 16);
            if (!(k & 1)) pace(3);
            dma16(db, fd, lb + L_WD + k * 1024);
            if (k < 3) dma16(db + 32 * DM, (k < 2 || lane < 32) ? fd : 0u, lb + L_WD + (5 + k) * 1024);
        }
    };
    auto load_gu = [&](int m, int lane) __attribute__((always_inline)) {
#pragma unroll 1
        for (int k0 = 0; k0 < 3; ++k0) {
            const int k = (k0 + (g & 7)) % 3, c = k * 64 + lane, row = c / 6, cc = c - 6 * row;
            const unsigned o = (unsigned)((swz(row) * FF + cc * 8) * 2), d = lb + L_GU + m * 8192 + k * 1024;
            const u16* wb = (m == 0 ? wg : wu) + gj * 80 * FF + gi * 48;
            pace(3);
            dma16(wb, o, d); dma16(wb + 32 * FF, o, d + 3072);
            if (k < 2) dma16(wb + 64 * FF, (k == 0 || lane < 32) ? o : 0u, d + 6144);
        }
    };
    auto lds_add = [&](int off, unsigned v) __attribute__((always_inline)) { asm volatile("s_mov_b64 exec, 1\n\tds_add_u32 %0, %1\n\ts_mov_b64 exec, -1" :: "v"(lb + L_CNT + off), "v"(v) : "memory"); };
    auto send_flag = [&](int lane) __attribute__((always_inline)) {
        st4_sc1(ws, WS_MB + (unsigned)((gi * 8 + ((gj == (gi & 7) ? lane : gi) & 7)) * 128 + gj * 4), lds_word(lb + L_CNT + 68));
    };
    auto tiles4 = [&](const Frag80 (&f)[4], const Frag80& b, unsigned long long mk) __attribute__((always_inline)) -> u32x4 {
        f32x4 c[4], d[4];
#pragma unroll
        for (int i = 0; i < 4; ++i) c[i] = mfma32(f[i].a0, b.a0, z4);
#pragma unroll
        for (int i = 0; i < 4; ++i) { c[i] = mfma32(f[i].a1, b.a1, c[i]); d[i] = mfma16(f[i].a2, b.a2, z4); }
        __builtin_amdgcn_sched_barrier(0);
        f32x2 lo, hi;
        asm volatile("s_nop 1\n\ts_mov_b64 exec, %18\n\tv_pk_add_f32 %0, %2, %10\n\tv_pk_add_f32 %1, %3, %11\n\t"
                     "s_lshl_b64 exec, exec, 1\n\tv_pk_add_f32 %0, %4, %12\n\tv_pk_add_f32 %1, %5, %13\n\t"
                     "s_lshl_b64 exec, exec, 1\n\tv_pk_add_f32 %0, %6, %14\n\tv_pk_add_f32 %1, %7, %15\n\t"
                     "s_lshl_b64 exec, exec, 1\n\tv_pk_add_f32 %0, %8, %16\n\tv_pk_add_f32 %1, %9, %17\n\ts_mov_b64 exec, -1"
                     : "=&v"(lo), "=&v"(hi) : "v"(LO2(c[0])), "v"(HI2(c[0])), "v"(LO2(c[1])), "v"(HI2(c[1])), "v"(LO2(c[2])), "v"(HI2(c[2])), "v"(LO2(c[3])), "v"(HI2(c[3])),
                       "v"(LO2(d[0])), "v"(HI2(d[0])), "v"(LO2(d[1])), "v"(HI2(d[1])), "v"(LO2(d[2])), "v"(HI2(d[2])), "v"(LO2(d[3])), "v"(HI2(d[3])), "s"(mk) : "scc");
        return asu(__builtin_shufflevector(lo, hi, 0, 1, 2, 3));
    };
    auto empty1 = [&](int half, unsigned S, int lane) __attribute__((always_inline)) {
        const unsigned o = WS_H1 + S + (unsigned)((gi * 4 + 2 * half) * 16384 + gj * 896 + min(lane, 55) * 16);
        st16_sc1(ws, o, (u32x4){~0u, ~0u, ~0u, ~0u}); st16_sc1(ws, o + 16384, (u32x4){~0u, ~0u, ~0u, ~0u});
    };
    auto report = [&]() __attribute__((always_inline)) { if (gj == (gi & 7)) st4_sc1(ws, WS_R + (unsigned)(gi * 4), lds_word(lb + L_CNT + 88)); };
    auto answer = [&](int lane, unsigned r) __attribute__((always_inline)) {
        int d = min(max((int)(r - (tst << 11)) >> 11, -400), 600);
        unsigned k = (r >> 5) & 63u, v = (r >> 1) & 15u;
        v += dppu<0x124>(v); v += dppu<0x128>(v);
#define VOTES(l) (__builtin_amdgcn_readlane(v, l) + __builtin_amdgcn_readlane(v, 16 + l))
        const unsigned c = (VOTES(0) > 32 ? 2u : 0u) | (VOTES(1) > 32 ? 4u : 0u) | (VOTES(2) + VOTES(3) > 64 ? 8u : 0u);
        d += (int)dppu<0xB1>((unsigned)d); d += (int)dppu<0x4E>((unsigned)d); d += (int)dppu<0x141>((unsigned)d); d += (int)dppu<0x140>((unsigned)d);
        k = max(k, dppu<0xB1>(k)); k = max(k, dppu<0x4E>(k)); k = max(k, dppu<0x141>(k)); k = max(k, dppu<0x140>(k));
        d = __builtin_amdgcn_readlane(d, 0) + __builtin_amdgcn_readlane(d, 16);
        k = max((unsigned)__builtin_amdgcn_readlane(k, 0), (unsigned)__builtin_amdgcn_readlane(k, 16));
        st4_sc1(ws, WS_MB + (unsigned)(((lane & 31) * 8 + (lane & 7)) * 128 + 32), (tst + (d >> 5) + TOFF) << 10 | k << 4 | c | 1u);
        lds_set(lb + L_CNT + 36, 1u);
    };
    auto adopt = [&](unsigned t) __attribute__((always_inline)) {
        const int dt = (int)(t - (tst << 10)) >> 10;
        if (dt > -256 && dt < 512) lds_set(lb + L_CNT + 64, tst + dt);
        lds_set(lb + L_CNT + 20, 4096u + ((t >> 4) & 63u) * BETA);
        lds_set(lb + L_CNT + 80, lds_word(lb + L_CNT + 92) ^ t);
        lds_set(lb + L_CNT + 84, (((t >> 1) ^ lds_word(lb + L_CNT + 68)) & 4u) << 11);
        lds_set(lb + L_CNT + 52, 1u);
    };
    struct StatAddr { unsigned a16, b16, a8, b8, dst; };
    auto stats_addr = [&](int lane, unsigned dbase, int dstride) __attribute__((always_inline)) -> StatAddr {
        const int kq = lane >> 4, q4 = (lane >> 2) & 3, p4 = lane & 3;
        const int x1 = LANES(0x4444444444444444ull) ? L_WR + gj * 160 : L_XS + q4 * 160, xa = LANES(0xaaaaaaaaaaaaaaaaull) ? L_XS + 640 : x1;
        StatAddr r{lb + (unsigned)(xa + kq * 16), lb + (unsigned)(L_XS + p4 * 160 + kq * 16), lb + (unsigned)(xa + 128 + kq * 8), lb + (unsigned)(L_XS + p4 * 160 + 128 + kq * 8), dbase + (unsigned)(kq * dstride)};
        PIN3(r.a16, r.b16, r.dst); PIN2(r.a8, r.b8);
        return r;
    };
    auto stats = [&](const StatAddr& r) __attribute__((always_inline)) {
        const bf16x8 a0 = lds16(r.a16), b0 = lds16(r.b16), a1 = lds16(r.a16 + 64), b1 = lds16(r.b16 + 64);
        const bf16x4 a2 = lds8(r.a8), b2 = lds8(r.b8);
        unsigned long long mk = 0x0008000400020001ull; PINS(mk);
        __builtin_amdgcn_sched_barrier(0);
        st16m(ws, r.dst, asu(mfma32(a1, b1, mfma32(a0, b0, z4)) + mfma16(a2, b2, z4)), mk);
    };
    unsigned qa, pw, ma, idx, qm;
    auto qk_addr = [&](bool L1) __attribute__((always_inline)) {
        LANE_VARS();
        qa = lb + L_Q + (unsigned)(kq * 32 + (lane & 2) * 8);
        pw = lb + L_P + (unsigned)((7 + 8 * wave) * 192 + (kq >> 1) * 64 + (n >> 3) * 32 + (lane & 2) * 8 + (q4 & 1) * 8 + (kq & 1) * 4 + (lane & 1) * 2);
        ma = lb + L_M + (unsigned)(wave * 4);
        idx = (lane & 2) ? 0xeeeeu : 0x4444u;
        qm = lb + L_Q;
        PIN3(qa, pw, ma); PIN2(idx, qm);
        if (L1 && wave < 4) __builtin_amdgcn_s_setprio(2);
    };
    auto att_qk0 = [&]() __attribute__((always_inline)) {
        qk_addr(false);
#pragma unroll 1
        for (unsigned spins = 0; spins < (kSpinCap << 4); ++spins) {
            if (lds_word(lb + L_CNT + 4) != 0u) break;
            __builtin_amdgcn_s_sleep(1);
        }
        STAMP(3);
        const u32x4 q0 = *(const LAS u32x4*)(size_t)qa, q1 = *(const LAS u32x4*)(size_t)(qa + 128), q2 = *(const LAS u32x4*)(size_t)(qa + 256);
        f32x4 c0 = z4, c1 = z4, c2 = z4, c3 = z4;
#define SMF(c, q, r) "v_smfmac_f32_16x16x64_bf16 %" #c ", %" #q ", v[" #r "], %7\n\t"
        asm volatile("s_nop 1\n\t" SMF(0, 4, 64:71) SMF(1, 4, 88:95) SMF(2, 4, 112:119) SMF(3, 4, 136:143) SMF(0, 5, 72:79) SMF(1, 5, 96:103) SMF(2, 5, 120:127) SMF(3, 5, 144:151)
                     SMF(0, 6, 80:87) SMF(1, 6, 104:111) SMF(2, 6, 128:135) SMF(3, 6, 152:159) "s_nop 7"
                     : "+v"(c0), "+v"(c1), "+v"(c2), "+v"(c3) : "v"(q0), "v"(q1), "v"(q2), "v"(idx));
#undef SMF
        const float s0 = c0[0] + c0[2], s1 = c1[0] + c1[2], s2 = c2[0] + c2[2], s3 = c3[0] + c3[2];
        float m = fmax2(fmax3(s0, s1, s2), s3);
        m = fmax2(m, dpp<0xB1>(m)); m = fmax2(m, dpp<0x4E>(m)); m = fmax2(m, dpp<0x124>(m)); m = fmax2(m, dpp<0x128>(m));
        const float s01 = LANES(0xffff0000ffff0000ull) ? s1 : s0, s23 = LANES(0xffff0000ffff0000ull) ? s3 : s2;
        const float sv = LANES(0xffffffff00000000ull) ? s23 : s01;
        m = bfr(bfr(m) * SCALE);
        const float p = ex2((bfr(bfr(sv) * SCALE) - m) * LOG2E);
        *(LAS u16*)(size_t)pw = (u16)pk2(p, p);
        *(LAS float*)(size_t)ma = m;
        __builtin_amdgcn_sched_barrier(0);
    };
    unsigned pr, oa, oc, cma, cxa, cra, cso;
    auto att_addr = [&]() __attribute__((always_inline)) {
        LANE_VARS();
        pr = lb + L_P + (unsigned)((7 + 8 * wave - 2 * q4 - (lane & 1)) * 192 + kq * 32 + (lane & 2) * 8);
        oa = lb + L_OP + (unsigned)(wave * 1024 + (lane & 7) * 64 + kq * 16 + (n >> 3) * 8);
        oc = lb + L_OP + (unsigned)(wave * 1024 + 512 + (kq >> 1) * 256 + (lane & 7) * 32 + (kq & 1) * 16 + (n >> 3) * 8);
        PIN3(pr, oa, oc);
    };
    auto comb_out = [&](float u0, float u1, unsigned so, unsigned long long mk, bool both) __attribute__((always_inline)) {
        const unsigned w = min(pk2(u0, u1), 0xfffffffeu);
        if (!both) { st4_sc1(ws, so, w); return; }
        const u32x4 w4 = {dppu<0x00>(w), dppu<0x55>(w), dppu<0xAA>(w), dppu<0xFF>(w)};
        st16m(ws, so, w4, mk);
    };
    auto max8 = [&](f32x4 m0, f32x4 m1) __attribute__((always_inline)) { return fmax3(fmax3(m0[0], m0[1], m0[2]), fmax3(m0[3], m1[0], m1[1]), fmax2(m1[2], m1[3])) * LOG2E; };
    auto comb_o0 = [&](f32x4 m0, f32x4 m1, f32x4 mm, f32x4 o0, f32x4 o1, f32x4 o2, f32x4 o3, unsigned so, unsigned long long mk, bool both) __attribute__((always_inline)) {
        const float M = max8(m0, m1);
        f32x4 f;
#pragma unroll
        for (int k = 0; k < 4; ++k) f[k] = ex2(__builtin_fmaf(mm[k], LOG2E, -M));
        const f32x4 o = f[3] * o3 + (f[2] * o2 + (f[1] * o1 + f[0] * o0));
        comb_out(rs32(o[0], o[2]), rs32(o[1], o[3]), so, mk, both);
    };
    auto comb_o1 = [&](f32x4 m0, f32x4 m1, f32x2 mm, f32x4 o0, f32x4 o1, unsigned so, unsigned long long mk, bool both) __attribute__((always_inline)) {
        const float M = max8(m0, m1);
        const f32x4 o = ex2(__builtin_fmaf(mm[1], LOG2E, -M)) * o1 + ex2(__builtin_fmaf(mm[0], LOG2E, -M)) * o0;
        comb_out(x16_add(rs32(o[0], o[2])), x16_add(rs32(o[1], o[3])), so, mk, both);
    };
    auto ml_addr = [&](int lane, unsigned& xa, unsigned& ra) __attribute__((always_inline)) {
        xa = lb + L_M + (unsigned)(((lane & 15) >> 1) * 4); ra = lb + L_P + (unsigned)((7 + 8 * ((lane & 15) >> 1)) * 192 + (lane & 1) * 64 + (lane >> 4) * 16);
    };
    auto comb_ml = [&](unsigned ma, unsigned xa, unsigned ra, unsigned so, bool both) __attribute__((always_inline)) {
        const u32x4 x4 = *(const LAS u32x4*)(size_t)ra;
        const f32x4 m0 = *(const LAS f32x4*)(size_t)ma, m1 = *(const LAS f32x4*)(size_t)(ma + 16);
        const float mw = *(const LAS float*)(size_t)xa;
        __builtin_amdgcn_sched_barrier(0);
        const f32x4 sp = mfma32u((u32x4){0x3f803f80u, 0x3f803f80u, 0x3f803f80u, 0x3f803f80u}, x4, z4);
        __builtin_amdgcn_sched_barrier(0);
        const float M = max8(m0, m1);
        float L = row16_sum(sp[0] * ex2(__builtin_fmaf(mw, LOG2E, -M)));
        if (both) L = dpp<0x00>(L);
        st16m(ws, so, asu((f32x4){M, L, M, L}), both ? 3ull : 1ull);
    };
    auto att_pv0 = [&]() __attribute__((always_inline)) {
        u32x4 bm[8];
        trans_v(IC<0>{});
        if (v_after(9) > v_after(7)) { trans_w(IC<0>{}); trans_w(IC<1>{}); }
        trans_v(IC<1>{});
#pragma unroll
        for (int e = 0; e < 8; ++e) bm[e] = *(const LAS u32x4*)(size_t)(pr + e * 192);
        __builtin_amdgcn_sched_barrier(0);
        f32x4 da = z4, dc = z4;
        PIN2(da, dc);
        asm volatile("s_nop 1");
        __builtin_amdgcn_sched_barrier(0);
        f32x2 r;
        smf<RV>(da, bm[0], idx); smf<RV + 8>(da, bm[1], idx); smf<RV + 16>(da, bm[2], idx); smf<RV + 24>(da, bm[3], idx);
        smf<RV + 32>(da, bm[4], idx); smf<RV + 40>(da, bm[5], idx); smf<RV + 48>(da, bm[6], idx); smf<RV + 56>(da, bm[7], idx);
        asm volatile("s_nop 7\n\tv_pk_add_f32 %0, %1, %2\n\tds_write_b64 %3, %0\n\ts_mov_b64 exec, 1\n\tds_add_u32 %4, %5\n\ts_mov_b64 exec, -1"
                     : "=&v"(r) : "v"(LO2(da)), "v"(HI2(da)), "v"(oa), "v"(lb + L_CNT + 24), "v"(1u) : "memory");
        GUARD(900);
        MARK(b_t5);
        if (v_after(9) < v_after(7)) { trans_w(IC<0>{}); trans_w(IC<1>{}); }
        trans_w(IC<2>{}); trans_w(IC<3>{});
        STAMP(4);
        asm volatile("s_nop 1\n\tv_smfmac_f32_16x16x64_bf16 %0, %1, v[224:231], %5\n\tv_smfmac_f32_16x16x64_bf16 %0, %2, v[232:239], %5\n\t"
                     "v_smfmac_f32_16x16x64_bf16 %0, %3, v[240:247], %5\n\tv_smfmac_f32_16x16x64_bf16 %0, %4, v[248:255], %5\n\ts_nop 6"
                     : "+v"(dc) : "v"(bm[0]), "v"(bm[1]), "v"(bm[2]), "v"(bm[3]), "v"(idx));
        asm volatile("s_nop 0\n\tv_pk_add_f32 %0, %1, %2\n\tds_write_b64 %3, %0" : "=&v"(r) : "v"(LO2(dc)), "v"(HI2(dc)), "v"(oc) : "memory");
        STAMP(20);
    };
    unsigned long long ck1 = 0xffff0000ffff0000ull, ck2 = 0xffffffff00000000ull, csc = 0x3d93cd3a3d93cd3aull;
    auto att1 = [&]() __attribute__((always_inline)) {
        constexpr int layer = 1;
        {
#define SMF(c, q, r) "v_smfmac_f32_16x16x64_bf16 v[" #c "], v[" #q "], v[" #r "], %0\n\t"
#define MAXD(x) "v_max_f32_dpp v41, v41, v41 " x " row_mask:0xf bank_mask:0xf\n\t"
            asm volatile("s_waitcnt lgkmcnt(0)\n\ts_barrier\n\t"
                         "ds_read_b128 v[0:3], %1\n\tds_read2_b32 v[12:13], %2 offset0:96 offset1:100\n\tds_read_b128 v[4:7], %1 offset:128\n\tds_read_b128 v[8:11], %1 offset:256\n\t"
                         "v_mov_b64 v[16:17], 0\n\tv_mov_b64 v[18:19], 0\n\tv_mov_b64 v[20:21], 0\n\tv_mov_b64 v[22:23], 0\n\tv_mov_b64 v[24:25], 0\n\tv_mov_b64 v[26:27], 0\n\t"
                         "v_mov_b64 v[28:29], 0\n\tv_mov_b64 v[30:31], 0\n\tv_mov_b64 v[32:33], 0\n\tv_mov_b64 v[34:35], 0\n\tv_mov_b64 v[36:37], 0\n\tv_mov_b64 v[38:39], 0\n\t"
                         "s_waitcnt lgkmcnt(3)\n\t" SMF(16:19, 0:3, 64:71) SMF(20:23, 0:3, 88:95) "s_waitcnt lgkmcnt(1)\n\tv_mul_f32 v13, 0x3acccccd, v13\n\t"
                         SMF(24:27, 0:3, 112:119) "v_mul_f32 v12, 0x3acccccd, v12\n\t" SMF(28:31, 0:3, 136:143) "v_fma_f32 v12, -v13, v13, v12\n\t"
                         SMF(16:19, 4:7, 72:79) "v_maximum3_f32 v12, v12, 0, 0\n\t" SMF(20:23, 4:7, 96:103) "v_cvt_pk_bf16_f32 v12, 0, v12\n\t"
                         SMF(24:27, 4:7, 120:127) "v_add_f32 v12, 0x3727c5ac, v12\n\t" SMF(28:31, 4:7, 144:151) "v_cvt_pk_bf16_f32 v12, 0, v12\n\ts_waitcnt lgkmcnt(0)\n\t"
                         SMF(16:19, 8:11, 80:87) "v_sqrt_f32 v12, v12\n\t" SMF(20:23, 8:11, 104:111) "v_cvt_pk_bf16_f32 v12, 0, v12\n\t"
                         SMF(24:27, 8:11, 128:135) "v_rcp_f32 v12, v12\n\t" SMF(28:31, 8:11, 152:159)
                         "s_nop 2\n\tv_add_f32 v42, v16, v18\n\ts_nop 0\n\tv_add_f32 v43, v20, v22\n\tv_max_f32 v41, v42, v43\n\tv_add_f32 v44, v24, v26\n\tv_add_f32 v45, v28, v30\n\t"
                         "v_max3_f32 v41, v41, v44, v45\n\tv_cndmask_b32 v42, v42, v43, %8\n\tv_cndmask_b32 v44, v44, v45, %8\n\t"
                         MAXD("quad_perm:[1,0,3,2]") "v_cndmask_b32 v40, v42, v44, %9\n\ts_nop 0\n\t" MAXD("quad_perm:[2,3,0,1]") "s_nop 1\n\t" MAXD("row_ror:4") "s_nop 1\n\t" MAXD("row_ror:8")
                         "v_pk_mul_f32 v[40:41], v[12:13], v[40:41] op_sel_hi:[0,1]\n\tv_cvt_pk_bf16_f32 v40, 0, v40\n\tv_cvt_pk_bf16_f32 v41, 0, v41\n\t"
                         "v_pk_mul_f32 v[40:41], %10, v[40:41]\n\tv_cvt_pk_bf16_f32 v40, 0, v40\n\tv_cvt_pk_bf16_f32 v41, 0, v41\n\t"
                         "v_sub_f32 v40, v40, v41\n\tv_mul_f32 v40, 0x3fb8aa3b, v40\n\tv_exp_f32 v40, v40\n\ts_nop 0\n\tv_cvt_pk_bf16_f32 v40, v40, v40\n\tds_write_b16 %3, v40\n\t"
                         "ds_read_b128 v[0:3], %4\n\tds_read_b128 v[4:7], %4 offset:192\n\tds_read_b128 v[8:11], %4 offset:384\n\tds_read_b128 v[12:15], %4 offset:576\n\t"
                         "ds_read_b128 v[16:19], %4 offset:768\n\tds_read_b128 v[20:23], %4 offset:960\n\tds_read_b128 v[24:27], %4 offset:1152\n\tds_read_b128 v[28:31], %4 offset:1344\n\t"
                         "ds_write_b32 %5, v41\n\t"
                         "s_waitcnt lgkmcnt(8)\n\t" SMF(32:35, 0:3, 160:167) SMF(36:39, 0:3, 224:231) "s_waitcnt lgkmcnt(7)\n\t" SMF(32:35, 4:7, 168:175) SMF(36:39, 4:7, 232:239)
                         "s_waitcnt lgkmcnt(6)\n\t" SMF(32:35, 8:11, 176:183) SMF(36:39, 8:11, 240:247) "s_waitcnt lgkmcnt(4)\n\t" SMF(32:35, 12:15, 184:191) SMF(36:39, 12:15, 248:255)
                         SMF(32:35, 16:19, 192:199) "s_waitcnt lgkmcnt(1)\n\t" SMF(32:35, 20:23, 200:207) SMF(32:35, 24:27, 208:215) SMF(32:35, 28:31, 216:223)
                         "s_nop 2\n\tv_pk_add_f32 v[36:37], v[36:37], v[38:39]\n\tds_write_b64 %7, v[36:37]\n\ts_nop 2\n\tv_pk_add_f32 v[32:33], v[32:33], v[34:35]\n\tds_write_b64 %6, v[32:33]"
                         :: "v"(idx), "v"(qa), "v"(qm), "v"(pw), "v"(pr), "v"(ma), "v"(oa), "v"(oc), "s"(ck1), "s"(ck2), "s"(csc)
                         : "memory", "v0", "v1", "v2", "v3", "v4", "v5", "v6", "v7", "v8", "v9", "v10", "v11", "v12", "v13", "v14", "v15", "v16", "v17", "v18", "v19", "v20", "v21", "v22", "v23",
                           "v24", "v25", "v26", "v27", "v28", "v29", "v30", "v31", "v32", "v33", "v34", "v35", "v36", "v37", "v38", "v39", "v40", "v41", "v42", "v43", "v44", "v45");
#undef SMF
#undef MAXD
            STAMP(20 + 32 * layer);
            BAR();
            if (__builtin_expect(wave == 0, 1)) {
                STAMP(5 + 32 * layer);
                comb_o0(*(const LAS f32x4*)(size_t)cma, *(const LAS f32x4*)(size_t)(cma + 16), *(const LAS f32x4*)(size_t)cxa, *(const LAS f32x4*)(size_t)cra,
                        *(const LAS f32x4*)(size_t)(cra + 1024), *(const LAS f32x4*)(size_t)(cra + 2048), *(const LAS f32x4*)(size_t)(cra + 3072), cso, 0x1111111111111111ull, false);
            }
            __builtin_amdgcn_sched_barrier(0);
            if (__builtin_expect(wave == 1, 1))
                comb_o1(*(const LAS f32x4*)(size_t)cma, *(const LAS f32x4*)(size_t)(cma + 16), *(const LAS f32x2*)(size_t)cxa, *(const LAS f32x4*)(size_t)cra, *(const LAS f32x4*)(size_t)(cra + 1024), cso, 0x0000111100001111ull, false);
            __builtin_amdgcn_sched_barrier(0);
            if (__builtin_expect(wave == 2, 1)) comb_ml(cma, cxa, cra, cso, false);
            MARK(e_q);
            if (wave < 3) STAMP(21 + 32 * layer);
            __builtin_amdgcn_s_setprio(0);
        }
    };

    unsigned nj = 0u, qS = 0u, fS = 0u, rS = 0u, nS = 0u; int ord;
    {
        const int tid = threadIdx.x, lane = tid & 63;
        asm volatile("ds_max_u32 %0, %1" :: "v"(lb + L_CNT + 64), "v"(tst) : "memory");
        unsigned mb = 0u;
        const u16* kbase = kc + wofs / 2;
        const u16* vbase = vc + wofs / 2;
        unsigned pt0 = 0u;
        if (wave == 7) {
            __builtin_amdgcn_s_setprio(3);
            if (lane < 10) *reinterpret_cast<u32x4*>(smem + L_XS + 640 + lane * 16) = (u32x4){0x3f803f80u, 0x3f803f80u, 0x3f803f80u, 0x3f803f80u};
            if (lane < 16) { *reinterpret_cast<u32x4*>(smem + L_CS + 768 + lane * 16) = (u32x4){0u, 0u, 0u, 0u}; }
        } else {
            if (wave >= 4) {
                if (wave == 4) st16_sc1(ws, WS_OWN + (unsigned)(gi * 16384 + (lane >> 5) * 8192 + gj * 512 + (lane & 31) * 16), (u32x4){~0u, ~0u, ~0u, ~0u});
                else empty5(0, (unsigned)(wave - 5) * 8192u, lane);
                if (wave == 6) {
                    __builtin_amdgcn_s_setprio(3);
                    if (gi + gj == 0) st4_sc1(ws, WS_R + (lane & 31) * 4, 0u);
                    st4_sc1(ws, WS_MB + (unsigned)((gi * 8 + gj) * 128 + (lane & 31) * 4), 0u);
                }
            } else if (wave >= 2) {
                unsigned long long c0, c1;
                asm volatile("s_waitcnt lgkmcnt(0)\n\ts_memtime %0\n\ts_waitcnt lgkmcnt(0)\n\ts_load_dword %2, %3, %4\n\ts_waitcnt lgkmcnt(0)\n\ts_memtime %1\n\ts_waitcnt lgkmcnt(0)"
                             : "=&s"(c0), "=&s"(c1), "=&s"(pt0) : "s"(ws), "s"((unsigned)(min(gi & 3, 2) << 21) + ((((tst + g * 977u) * 2654435761u) >> 11 & 0x1fdf80u) | ((wave ^ gj) & 1) * 8192)) : "memory");
                pt0 = ((wave ^ gj) & 1) ? (unsigned)c0 - (unsigned)c1 : (unsigned)c1 - (unsigned)c0;
            }
            STAMP(36);
            *reinterpret_cast<u32x4*>(smem + L_P + tid * 16) = (u32x4){0u, 0u, 0u, 0u};
            if (tid < 72 * 12 - 448) *reinterpret_cast<u32x4*>(smem + L_P + 7168 + tid * 16) = (u32x4){0u, 0u, 0u, 0u};
            if (wave >= 4) {
                if (wave == 6) VMW(2); else VMW(1);
                asm volatile("ds_max_u32 %0, %1" :: "v"(lb + L_CNT + 64), "v"(now() - TLAND) : "memory");
                STAMP(40);
                lds_add(0, 1u);
            } else if (wave >= 2) { lds_add(20, pt0); VMW(0); lds_add(0, 1u); }
        }
        asm volatile("s_waitcnt lgkmcnt(0)" :: "s"(pt1) : "memory");
        const int rot = gj + wave;
        const unsigned r0 = now(), c0 = (unsigned)__builtin_amdgcn_s_memtime();
        u32x4 a[4];
        unsigned ko = (unsigned)((wave * 64 + (lane & 7)) * 384 + (((lane >> 3) & 1) * 4 + (lane >> 4)) * 16);
        PIN1(ko);
        auto k_group = [&](auto GC_) __attribute__((always_inline)) {
            constexpr int G = decltype(GC_)::value, R = RK + 24 * (G >> 1) + 4 * (G & 1);
            const unsigned o = ko + (unsigned)((((G >> 1) + rot) & 3) * 6144 + (G & 1) * 3072);
            pace(3);
            ld16_nt<R, 0>(kbase, o); ld16_nt<R + 8, 128>(kbase, o); ld16_nt<R + 16, 256>(kbase, o);
            const int lane = lane_id();
            const unsigned ho = WS_Q0 + (unsigned)((gi * 4 + (gj >> 1)) * 4096 + lane * 16);
            if (wave < 7) {
                asm volatile("; COND_BEGIN");
                if (G == 0 && wave >= 4) { VMW(3); lds_add(32, 1u); }
                if (G == 0) {
                    if (wave < 2) { VMW(3); lds_add(0, 1u); load_gu(wave, lane); }
                    if (wave == 2) load_wd(lane);
                    if (wave == 3) {
#pragma unroll 1
                        for (int k = 5 - WEARLY; k < 5; ++k) load_wo(k, lane, true);
                    }
                }
                asm volatile("; COND_END");
            }
            if (wave == 7) {
                asm volatile("; COND_BEGIN");
                if (G == GP) {
                    VMW(3 * (GP + 1));
#pragma unroll 1
                    for (unsigned spins = 0; spins < (kSpinCap << 4); ++spins) {
                        if (lds_word(lb + L_CNT) >= 7u) break;
                        __builtin_amdgcn_s_sleep(1);
                    }
                    STAMP(26);
                    const unsigned char* p = smem + L_X0 + (lane >> 4) * 1280;
                    const float c0 = lo16(*reinterpret_cast<const u16*>(p));
                    const f32x2 c2 = {c0, c0};
                    f32x2 s2 = {0.f, 0.f}, q2 = s2;
#pragma unroll
                    for (int k = 0; k < 5; ++k) {
                        const u32x4 w = *reinterpret_cast<const u32x4*>(p + k * 256 + (lane & 15) * 16);
#pragma unroll
                        for (int e = 0; e < 4; ++e) { const f32x2 d = unpk(w[e]) - c2; s2 += d; q2 += d * d; }
                    }
                    const float s = row16_sum(s2[0] + s2[1]), qq = row16_sum(q2[0] + q2[1]);
                    const float ms = s * (1.f / DM);
                    const unsigned mv = pk2(c0 + ms, fmax2(qq - s * ms, 0.f) * (1.f / DM));
                    const float mb = lo16(mv), rden = frcp(bfr(__builtin_amdgcn_sqrtf(bfr(hi16(mv) + 1e-5f))));
                    const f32x2 mb2 = {mb, mb}, rd2 = {rden, rden};
                    if ((lane & 15) < 10) {
                        u32x4 w = *reinterpret_cast<const u32x4*>(p + gj * 160 + (lane & 15) * 16);
#pragma unroll
                        for (int e = 0; e < 4; ++e) w[e] = pk2v(unpk(pk2v(unpk(w[e]) - mb2)) * rd2);
                        *reinterpret_cast<u32x4*>(smem + L_XS + (lane >> 4) * 160 + (lane & 15) * 16) = w;
                    }
                    STAMP(30);
                }
                if (G == GP || G == GA) {
#pragma unroll 1
                    for (int blk = 0; blk < 3; ++blk) {
                        LANE_VARS();
                        const unsigned xb = lb + L_XS + (unsigned)((G == GP ? q4 : 4) * 160);
                        Frag80 f[4];
#pragma unroll
                        for (int i = 0; i < 4; ++i) f[i] = frag80(lb + L_WQ + (unsigned)(128 * blk + 32 * i + 8 * p4), 416, r0s, r1s, r2s);
                        const Frag80 b = rows80(xb + kq * 16, xb + 128 + kq * 8);
                        const u32x4 c = tiles4(f, b, 0x1111111111111111ull);
                        if (G == GP) *reinterpret_cast<u32x4*>(smem + L_OP + q4 * 768 + 256 * blk + p4 * 64 + kq * 16) = c;
                        else *reinterpret_cast<u32x4*>(smem + L_CS + 256 * blk + kq * 64 + p4 * 16) = c;
                    }
                    if (G == GP) {
                        asm volatile("" ::: "memory");
                        const int ln = lane_id();
                        f32x2 c[6];
                        unsigned m = 20u;
#pragma unroll
                        for (int k = 0; k < 6; ++k) {
                            c[k] = *reinterpret_cast<const f32x2*>(smem + L_OP + (k / 3) * 1536 + ln * 24 + (k % 3) * 8);
                            m = max(m, max(fexp(c[k][0]), fexp(c[k][1])));
                        }
                        m = max(m, dppu<0xB1>(m)); m = max(m, dppu<0x4E>(m)); m = max(m, dppu<0x141>(m)); m = max(m, dppu<0x140>(m));
                        m = max(max((unsigned)__builtin_amdgcn_readlane(m, 0), (unsigned)__builtin_amdgcn_readlane(m, 16)), max((unsigned)__builtin_amdgcn_readlane(m, 32), (unsigned)__builtin_amdgcn_readlane(m, 48)));
                        lds_set(lb + L_CNT + 76, m);
                        const float f = __uint_as_float((273u - m) << 23);
#pragma unroll
                        for (int k = 0; k < 2; ++k) {
                            const u32x2 w0 = enc21(c[3 * k][0] * f, c[3 * k][1] * f, c[3 * k + 1][0] * f), w1 = enc21(c[3 * k + 1][1] * f, c[3 * k + 2][0] * f, c[3 * k + 2][1] * f);
                            st16_sc1(ws, WS_Q0 + (unsigned)((gi * 4 + 2 * k + (ln >> 5)) * 4096 + gj * 512 + (ln & 31) * 16), (u32x4){w0[0], w0[1], w1[0], w1[1]});
                        }
                        STAMP(2);
                        WAIT1(pf, 3 * (GP + 1) + 2);
                    }
                }
                if (G == GA) {
                    const unsigned half = __builtin_amdgcn_s_getreg(2 << 6 | 20);
                    lds_set(lb + L_CNT + 68, lds_word(lb + L_CNT + 64) << 11 | lds_word(lb + L_CNT + 76) << 3 | 1u | (((int)lds_word(lb + L_CNT + 20) > 0) ^ half) << 1 | half << 2);
                    VMW(3 * (GA - GP));
#pragma unroll 1
                    for (unsigned spins = 0; spins < (kSpinCap << 4); ++spins) if (lds_word(lb + L_CNT + 32) >= 3u) break;
                    send_flag(lane_id());
                    lds_set(lb + L_CNT + 8, 1u);
                    STAMP(17);
                    f32x2 r2 = {0.f, 0.f};
#pragma unroll
                    for (int k = 0; k < 5; ++k) r2 += unpk(*reinterpret_cast<const unsigned*>(smem + L_WR + k * 256 + lane_id() * 4));
                    *reinterpret_cast<float*>(smem + L_CNT + 12) = wave_sum(r2[0] + r2[1]);
                }
                asm volatile("; COND_END");
            }
            if (wave == 6) {
                asm volatile("; COND_BEGIN");
                if (G == GB - 1) mb = ld4_sc1(ws, WS_MB + (unsigned)((gi * 8 + gj) * 128 + (gi & 7) * 4));
                if (G == GB) {
                    WAIT1(mb, 3);
#pragma unroll 1
                    for (unsigned spins = 0; spins < kSpinCap && !all_set(mb); ++spins) {
                        if ((spins & 7u) == 7u && lds_word(lb + L_CNT + 8)) send_flag(lane);
                        mb = ld4_sc1(ws, WS_MB + (unsigned)((gi * 8 + gj) * 128 + (gi & 7) * 4));
                        WAIT1(mb, 0);
                    }
                    mb = ld4_sc1(ws, WS_MB + (unsigned)((gi * 8 + (gi & 7)) * 128 + (lane & 7) * 4));
                }
                if (G == GC) {
                    WAIT1(mb, 3 * (GC - GB));
#pragma unroll 1
                    for (unsigned spins = 0; spins < kSpinCap && !all_set(mb); ++spins) {
                        if ((spins & 7u) == 7u && lds_word(lb + L_CNT + 8)) send_flag(lane);
                        mb = ld4_sc1(ws, WS_MB + (unsigned)((gi * 8 + (gi & 7)) * 128 + (lane & 7) * 4));
                        WAIT1(mb, 0);
                    }
                    lds_set(lb + L_CNT + 92, (__builtin_amdgcn_readlane(mb, gi & 7) & 4u) ? 14u : 0u);
                    int d = min(max((int)(mb - (tst << 11)) >> 11, -400), 600);
                    d += (int)dppu<0xB1>((unsigned)d); d += (int)dppu<0x4E>((unsigned)d); d += (int)dppu<0x141>((unsigned)d);
                    const unsigned tm = tst + (unsigned)(__builtin_amdgcn_readfirstlane(d) >> 3);
                    lds_set(lb + L_CNT + 64, tm);
                    const float slow = (float)(now() - r0) * 24.f * 256.f * frcp((float)((unsigned)__builtin_amdgcn_s_memtime() - c0)) - 256.f;
                    lds_set(lb + L_CNT + 88, tm << 11 | (unsigned)__builtin_amdgcn_fmed3f(slow, 0.f, 63.f) << 5 | __builtin_popcount((unsigned)__builtin_amdgcn_ballot_w64((mb & 2u) != 0u) & 255u) << 1 | 1u);
                    report();
                    a[0] = ld16_sc1<0>(ws, ho); a[1] = ld16_sc1<1024>(ws, ho); a[2] = ld16_sc1<2048>(ws, ho); a[3] = ld16_sc1<3072>(ws, ho);
                    pace(XCOST, false);
                    STAMP(31);
                }
                asm volatile("; COND_END");
            } else if (G == GC) { for (int k = 0; k < 4; ++k) asm volatile("" : "=v"(a[k])); }
        };
        k_group(IC<0>{}); k_group(IC<1>{}); k_group(IC<2>{}); k_group(IC<3>{}); k_group(IC<4>{}); k_group(IC<5>{}); k_group(IC<6>{}); k_group(IC<7>{});
        {
            const int lane = lane_id();
            const unsigned v0 = (unsigned)((wave * 64 + 4 * (lane >> 4)) * 384 + (lane & 15) * 16);
            const unsigned v1 = v0 + 256 - (unsigned)((lane & 15) * 8);
            auto v_group = [&](auto HC) __attribute__((always_inline)) {
                constexpr int h = decltype(HC)::value, p = kVord[h], R = p < 8 ? RV + 16 * (p & 3) + 4 * (p >> 2) : RV + 64 + 2 * (p - 8), NV = v_upto(h);
                const unsigned o = (p < 8 ? v0 : v1) + (unsigned)((((p < 8 ? p >> 1 : p) + rot) & 3) * 6144);
                pace(2);
                if (p < 8) { ld16_nt<R, (p & 1) * 768>(vbase, o); ld16_nt<R + 8, (p & 1) * 768 + 384>(vbase, o); }
                else { ld8_nt<R, 0>(vbase, o); ld8_nt<R + 8, 384>(vbase, o); ld8_nt<R + 16, 768>(vbase, o); ld8_nt<R + 24, 1152>(vbase, o); }
                if (wave == 7 && gi + gj == 0) {
                    asm volatile("; COND_BEGIN");
                    if (h == 3 || h == 6) mb = ld4_sc1(ws, WS_R + (unsigned)((lane & 31) * 4));
                    if (h == 4 || h == 7) {
                        WAIT1(mb, NV - v_upto(h - 1));
                        if (all_set(mb) && !lds_word(lb + L_CNT + 36)) answer(lane, mb);
                    }
                    asm volatile("; COND_END");
                }
                if (wave == 6) {
                    asm volatile("; COND_BEGIN");
                if (h == HD) {
                    asm volatile("s_waitcnt vmcnt(%4)" : "+v"(a[0]), "+v"(a[1]), "+v"(a[2]), "+v"(a[3]) : "i"(3 * (7 - GC) + NV) : "memory");
                    float sq[6] = {0.f, 0.f, 0.f, 0.f, 0.f, 0.f};
#pragma unroll
                    for (int k = 0; k < 4; ++k) {
                        const float f = __uint_as_float(((((unsigned)__builtin_amdgcn_ds_bpermute((2 * k + (lane >> 5)) * 4, (int)mb) >> 3) & 255u) - 19u) << 23);
#pragma unroll
                        for (int e = 0; e < 2; ++e) {
                            const unsigned lo = a[k][2 * e], hi = a[k][2 * e + 1];
                            const float fe = (int)hi < 0 ? __builtin_nanf("") : f;
                            sq[3 * e] += (float)((int)(lo << 11) >> 11) * fe;
                            sq[3 * e + 1] += (float)((int)(__builtin_amdgcn_alignbit(hi, lo, 21) << 11) >> 11) * fe;
                            sq[3 * e + 2] += (float)((int)(hi << 1) >> 11) * fe;
                        }
                    }
#pragma unroll
                    for (int e = 0; e < 3; ++e) {
                        const int pi = 3 * (lane & 31) + e;
                        *reinterpret_cast<unsigned*>(smem + L_Q + (pi >> 3) * 32 + (pi & 6) * 2 + (pi & 1) * 16) = pk2(x32_add(sq[2 * e]), x32_add(sq[2 * e + 1]));
                    }
                    asm volatile("s_waitcnt lgkmcnt(0)\n\tds_write_b32 %0, %1" :: "v"(lb + L_CNT + 4), "v"(1u) : "memory");
                    STAMP(27);
                    lead = LEAD * PACE * 4;
                }
                    if (h == 6) mb = ld4_sc1(ws, WS_MB + (unsigned)((gi * 8 + (gi & 7)) * 128 + 32));
                    if (h == 8) {
                        WAIT1(mb, NV - v_upto(6));
                        const unsigned r = __builtin_amdgcn_readfirstlane(mb);
                        if (r) { adopt(r); STAMP(29); }
                    }
                    asm volatile("; COND_END");
                }
                if constexpr (h < 6) {
                    constexpr int i = 2 * h;
                    fix_k1(IC<i>{}, IC<20 - 6 * (i / 3) - i % 3 + NV>{});
                    fix_k1(IC<i + 1>{}, IC<20 - 6 * ((i + 1) / 3) - (i + 1) % 3 + NV>{});
                }
                if (h == 5) {
                    STAMP(1);
                    __builtin_amdgcn_s_setprio(0);
                    att_qk0();
                }
            };
            v_group(IC<0>{}); v_group(IC<1>{}); v_group(IC<2>{}); v_group(IC<3>{}); v_group(IC<4>{}); v_group(IC<5>{});
            v_group(IC<6>{}); v_group(IC<7>{}); v_group(IC<8>{}); v_group(IC<9>{}); v_group(IC<10>{}); v_group(IC<11>{});
            lds_add(16, 1u);
            if (wave == 3) lds_add(44, WEARLY);
        }
    }
    {
        STAMP(28);
        att_addr();
        const int lane = lane_id();
        cma = lb + L_M; cxa = lb + L_M + (unsigned)((lane >> 4) * 8); cra = lb + L_OP + (unsigned)(512 + (lane >> 4) * 2048 + (lane & 15) * 16);
        cso = WS_OWN + (unsigned)(gi * 16384 + gj * 512 + 256 + (lane >> 5) * 64 + (lane & 12) * 4 + (lane & 1) * 8192);
        PIN2(cma, cxa); PIN2(cra, cso);
        att_pv0();
    }
    {
        f32x4 m0, m1, o0, o1; f32x2 mm; unsigned r, taken;
        asm volatile("s_mov_b64 exec, 1\n\tds_add_rtn_u32 %5, %7, %8 offset:%11\n\ts_mov_b64 exec, -1\n\t"
                     "ds_read_b128 %0, %7\n\tds_read_b128 %1, %7 offset:16\n\tds_read_b64 %2, %9\n\tds_read_b128 %3, %10\n\tds_read_b128 %4, %10 offset:1024\n\t"
                     "ds_read_b32 %6, %7 offset:%12\n\ts_waitcnt lgkmcnt(0)"
                     : "=&v"(m0), "=&v"(m1), "=&v"(mm), "=&v"(o0), "=&v"(o1), "=&v"(r), "=&v"(taken)
                     : "v"(cma), "v"(1u), "v"(cxa), "v"(cra), "i"(L_CNT + 28 - L_M), "i"(L_CNT + 40 - L_M) : "memory");
        ord = __builtin_amdgcn_readfirstlane(r);
        if (ord == 7) { STAMP(5); comb_o1(m0, m1, mm, o0, o1, cso, 0x0000333300003333ull, true); lds_set(lb + L_CNT + 48, now() | 1u); STAMP(21); }
        MARK(e_t5);
        if (ord <= OML + 1 || (unsigned)__builtin_amdgcn_readfirstlane(taken) < 5u - WEARLY) {
        const int lane = lane_id();
        auto wait_cnt = [&](int off, unsigned v) __attribute__((always_inline)) {
#pragma unroll 1
            for (unsigned spins = 0; spins < (kSpinCap << 4); ++spins) { if (lds_word(lb + L_CNT + off) >= v) break; __builtin_amdgcn_s_sleep(1); }
        };
        if (ord == OML) {
            wait_cnt(16, 8u);
            unsigned xa, ra; ml_addr(lane, xa, ra);
            comb_ml(cma, xa, ra, WS_OWN + (unsigned)(gi * 16384 + gj * 512 + 384 + (lane & 1) * 8192), true);
            STAMP(24);
        } else if (ord == 0 && !lds_word(lb + L_CNT + 52)) {
            if (gi + gj == 0 && !lds_word(lb + L_CNT + 36)) {
                unsigned r = 0u;
#pragma unroll 1
                for (unsigned spins = 0; spins < kSpinCap; ++spins) {
                    r = ld4_sc1(ws, WS_R + (unsigned)((lane & 31) * 4));
                    WAIT1(r, 0);
                    if (all_set(r)) break;
                    if ((spins & 7u) == 7u) send_flag(lane);
                }
                answer(lane, r);
            }
#pragma unroll 1
            for (unsigned spins = 0; spins < kSpinCap; ++spins) {
                unsigned r = ld4_sc1(ws, WS_MB + (unsigned)((gi * 8 + (gi & 7)) * 128 + 32));
                WAIT1(r, 0);
                r = __builtin_amdgcn_readfirstlane(r);
                if (r) { adopt(r); break; }
                if ((spins & 7u) == 7u) { send_flag(lane); report(); }
                __builtin_amdgcn_s_sleep(16);
            }
            STAMP(29);
        } else if (ord == OML + 1) {
#pragma unroll 1
            for (unsigned spins = 0; spins < (kSpinCap << 6); ++spins) if (lds_word(lb + L_CNT + 24) >= 8u) break;
            const unsigned ra = lb + L_OP + (unsigned)((lane >> 5) * 4096 + (lane & 31) * 16);
            comb_o0(*(const LAS f32x4*)(size_t)cma, *(const LAS f32x4*)(size_t)(cma + 16), *(const LAS f32x4*)(size_t)(cma + (lane >> 5) * 16), *(const LAS f32x4*)(size_t)ra, *(const LAS f32x4*)(size_t)(ra + 1024),
                    *(const LAS f32x4*)(size_t)(ra + 2048), *(const LAS f32x4*)(size_t)(ra + 3072), WS_OWN + (unsigned)(gi * 16384 + gj * 512 + (lane & 60) * 4 + (lane & 1) * 8192), 0x3333333333333333ull, true);
            STAMP(22);
        }
        if (ord == OML) {
#pragma unroll 1
            for (unsigned spins = 0; spins < (kSpinCap << 8); ++spins) { if (lds_word(lb + L_CNT + 52)) break; __builtin_amdgcn_s_sleep(1); }
            const unsigned S = (lds_word(lb + L_CNT + 80) & 4u) << 11;
            empty4(0, S, lane); empty5(5, 0u, lane); empty5(5, 8192u, lane);
        }
#pragma unroll 1
        for (;;) {
            unsigned j;
            asm volatile("s_mov_b64 exec, 1\n\tds_add_rtn_u32 %0, %1, %2\n\ts_mov_b64 exec, -1\n\ts_waitcnt lgkmcnt(0)" : "=&v"(j) : "v"(lb + L_CNT + 40), "v"(1u) : "memory");
            j = __builtin_amdgcn_readfirstlane(j);
            if (j >= 5u - WEARLY) break;
            load_wo((int)j, lane, false); ++nj;
        }
        }
        STAMP(23);
    }

    unsigned omr, owa, owb;
    auto op_addr = [&](int layer) __attribute__((always_inline)) {
        LANE_VARS();
        const int t = wave - 3;
        cma = WS_OWN + rS + (unsigned)(gi * 16384 + layer * 4096 + (lane >> 3) * 512 + t * 128 + (t == 3 ? 0 : (lane & 7) * 16));
        cxa = lb + L_O2 + (unsigned)((lane >> 3) * 416 + t * 128 + (t == 3 ? 0 : (lane & 7) * 16));
        const int pb = (lane & 4) ? 3 - p4 : p4, jp = 2 * pb + ((lane >> 2) & 1);
        cso = tile35(gj, 10 * layer + t) + (unsigned)((kq & 1) * 8192 + gi * 128 + (kq >> 1) * 64 + p4 * 16);
        cra = lb + L_O2 + (unsigned)(jp * 416 + kq * 16); omr = lb + L_O2 + (unsigned)(jp * 416 + 384);
        owa = lb + L_WO + (unsigned)(r0s * 160 + 32 * t + 8 * p4); owb = lb + L_WO + (unsigned)(r1s * 160 + 32 * t + 8 * p4);
        if (layer) PIN2(cma, cxa);
        PIN2(cso, cra); PIN3(omr, owa, owb);
    };
    auto chain = [&](auto LC) __attribute__((always_inline)) {
        constexpr int layer = decltype(LC)::value;
        unsigned h4 = WS_H4 + (unsigned)((layer * 32 + gi) * 32768) + (layer ? fS : 0u);
        auto sides = [&]() __attribute__((always_inline)) {
            const unsigned m = lds_word(lb + L_CNT + 80);
            qS = (m & 2u) << 12; fS = (m & 4u) << 11; rS = (m & 8u) << 10; nS = lds_word(lb + L_CNT + 84); h4 += fS;
        };
        if (wave < 3) {
            LANE_VARS();
            if (layer == 0) {
                unsigned lo = WS_OWN + lds_word(lb + L_CNT + 84) + (unsigned)(gi * 16384 + (lane >> 3) * 512), lw = lb + L_O2 + (unsigned)((lane >> 3) * 416);
                unsigned lo0 = lo + (unsigned)((lane & 7) * 16), lw0 = lw + (unsigned)((lane & 7) * 16);
                PIN2(lo0, lo); PIN2(lw0, lw);
                GUARD(400);
                MARK(b_g2);
                if (__builtin_expect(wave == 2, 0)) { if (nj | (ord == OML)) { VMW(0); lds_add(44, nj); } }
                else if (__builtin_expect(wave == 1, 0)) {
#pragma unroll 1
                    for (unsigned spins = 0; spins < (kSpinCap << 4); ++spins) if (lds_word(lb + L_CNT + 48)) break;
#pragma unroll 1
                    for (unsigned spins = 0; spins < kSpinCap; ++spins) {
                        u32x4 o0 = ld16_sc1<0>(ws, lo0), o1 = ld16_sc1<128>(ws, lo0), o2 = ld16_sc1<384>(ws, lo);
                        if (nj) { VMW(3); lds_add(44, nj); nj = 0u; }
                        asm volatile("s_waitcnt vmcnt(0)" : "+v"(o0), "+v"(o1), "+v"(o2) :: "memory");
                        *(LAS u32x4*)(size_t)lw0 = o0; *(LAS u32x4*)(size_t)(lw0 + 128) = o1; *(LAS u32x4*)(size_t)(lw + 384) = o2;
                        if (!any_empty(max(max(um4(o0), um4(o1)), um4(o2)))) break;
                    }
                } else {
                    unsigned long long miss; unsigned cnt = kSpinCap << 4, tf, nv = nj;
                    asm volatile("1:\n\tds_read_b32 v4, %4 offset:48\n\ts_sub_u32 %1, %1, 1\n\ts_cbranch_scc1 5f\n\ts_waitcnt lgkmcnt(0)\n\tv_readfirstlane_b32 %2, v4\n\ts_cmp_eq_u32 %2, 0\n\ts_cbranch_scc1 1b\n\t"
                                 "s_add_u32 %2, %2, %8\n"
                                 "2:\n\ts_memrealtime vcc\n\ts_waitcnt lgkmcnt(0)\n\ts_sub_u32 vcc_lo, %2, vcc_lo\n\ts_cmp_lt_u32 vcc_lo, 0x400\n\ts_cbranch_scc1 2b\n"
                                 "5:\n\ts_mov_b32 %1, 15\n"
                                 "3:\n\tglobal_load_dwordx4 v[0:3], %5, %6 offset:256 sc1\n\ts_waitcnt vmcnt(1)\n\ts_mov_b64 exec, 1\n\tds_add_u32 %4, %3 offset:44\n\ts_mov_b64 exec, -1\n\tv_mov_b32 %3, 0\n\t"
                                 "s_sub_u32 %1, %1, 1\n\ts_cselect_b64 vcc, 0, -1\n\ts_waitcnt vmcnt(0)\n\tds_write_b128 %7, v[0:3] offset:256\n\tv_max3_u32 v4, v0, v1, v2\n\tv_max_u32 v4, v4, v3\n\t"
                                 "v_cmp_eq_u32 %0, -1, v4\n\ts_and_b64 vcc, vcc, %0\n\ts_cbranch_scc1 3b"
                                 : "=&s"(miss), "+s"(cnt), "=&s"(tf), "+v"(nv) : "v"(lb + L_CNT), "v"(lo0), "s"(ws), "v"(lw0), "i"(DG2 - 1) : "memory", "vcc", "scc", "v0", "v1", "v2", "v3", "v4");
                    if (__builtin_expect(miss != 0ull, 0)) {
#pragma unroll 1
                        for (unsigned spins = 0; spins < kSpinCap; ++spins) {
                            if ((spins & 15u) == 0u) send_flag(lane);
                            u32x4 o0 = ld16_sc1<256>(ws, lo0);
                            WAIT1(o0, 0);
                            *(LAS u32x4*)(size_t)(lw0 + 256) = o0;
                            if (!any_empty(um4(o0))) break;
                        }
                    }
                }
                MARK(e_g2);
                STAMP(25);
                BAR();
                sides();
                if (wave < 2) empty1(wave, qS, lane);
                else {
                    st16m(ws, WS_CS + (unsigned)((gi * 8 + gj) * 768 + lane * 16), *reinterpret_cast<const u32x4*>(smem + L_CS + lane * 16), 0xffffffffffffull);
                    empty5(10, 0u, lane); empty5(10, 8192u, lane);
                }
            } else BAR();
            {
                const unsigned im = lb + L_GU + (unsigned)(32 * wave + 8 * p4);
                Frag80 fg = frag80(im, 96, r0s, r1s, r2s), fu = frag80(im + 8192, 96, r0s, r1s, r2s);
                const unsigned xb = lb + L_XS + (unsigned)((n < 4 ? n : 4) * 160);
                unsigned b16 = xb + kq * 16, b8 = xb + 128 + kq * 8;
                unsigned sg = h4 + (unsigned)(n < 4 ? h4s(wave) + gj * 512 + kq * 64 + n * 16 : H4C + wave * 1024 + gj * 128 + kq * 16), su = sg + (n < 4 ? 256u : 64u);
                unsigned o = h4 + h4s(wave) + (unsigned)(lane * 16);
                unsigned oc = h4 + (unsigned)(H4C + wave * 1024 + ((lane >> 5) * 4 + p4) * 128 + (kq & 1) * 64 + q4 * 16);
                unsigned os = h4 + (unsigned)(H4T + (q4 + 4 * (kq & 1)) * 128 + p4 * 16);
                unsigned ha = lb + L_H + (unsigned)(p4 * 96 + wave * 32 + q4 * 8 + kq * 2);
                f32x2 cs = {0.f, 0.f};
                if (layer != 0) cs = *reinterpret_cast<const f32x2*>(smem + L_CS2 + wave * 512 + lane * 8);
                PIN3(fg.a0, fg.a1, fg.a2); PIN3(fu.a0, fu.a1, fu.a2); PIN3(b16, b8, sg); PIN3(su, o, os); PIN2(ha, cs);
                unsigned long long mk = layer ? 0x000f000f000f000full : 0x001f001f001f001full;
                unsigned ta = WS_MB + (unsigned)((lane >> 1) * 1024 + 64 + (lane & 1) * 16);
                if (layer == 0) PIN2(oc, ta);
                unsigned x4a = layer ? 0u : X4A * lds_word(lb + L_CNT + 20) >> 12;
                unsigned long long w0 = __builtin_amdgcn_ballot_w64(wave == 0);
                PINS(x4a); PINS(w0);
                unsigned long long rdm = 0x3acccccd3acccccdull;
                PINS(mk); PINS(rdm);
                if (layer == 0) VMW(0);
                if (layer == 0) GUARD(1200); else GUARD(800);
                MARK(b_gu);
                BAR();
                STAMP(10 + 32 * layer);
                u32x4 tl = {0u, 0u, 0u, 0u};
                if (layer == 0) asm volatile("s_mov_b64 exec, %3\n\tglobal_load_dwordx4 %0, %1, %2 sc1\n\ts_mov_b64 exec, -1" : "+v"(tl) : "v"(ta), "s"(ws), "s"(w0) : "memory");
                const Frag80 b = rows80(b16, b8);
                const f32x4 g0 = mfma32(fg.a0, b.a0, z4), u0 = mfma32(fu.a0, b.a0, z4), g1 = mfma32(fg.a1, b.a1, g0), g2 = mfma16(fg.a2, b.a2, z4), u1 = mfma32(fu.a1, b.a1, u0), u2 = mfma16(fu.a2, b.a2, z4);
                __builtin_amdgcn_sched_barrier(0);
                const f32x4 cg = g1 + g2, cu = u1 + u2;
                st16m2<0>(ws, sg, asu(cg), su, asu(cu), mk);
                STAMP(11 + 32 * layer);
                if (layer == 0 && wave != 0) {
                    unsigned tr = 0u;
#pragma unroll 1
                    for (unsigned spins = 0; spins < (kSpinCap << 6); ++spins) { tr = lds_word(lb + L_CNT + 56); if (tr) break; }
                    until(tr + x4a);
                } else if (layer == 0) {
                    WAIT1(tl, 2);
#pragma unroll 1
                    for (unsigned spins = 0; spins < kSpinCap; ++spins) {
                        const unsigned long long miss = __builtin_amdgcn_ballot_w64(min(min(tl[0], tl[1]), min(tl[2], tl[3])) == 0u);
                        if (!miss) break;
                        asm volatile("s_mov_b64 exec, %3\n\tglobal_load_dwordx4 %0, %1, %2 sc1\n\ts_mov_b64 exec, -1\n\ts_waitcnt vmcnt(0)" : "+v"(tl) : "v"(ta), "s"(ws), "s"(miss) : "memory");
                    }
                    const unsigned tn = now();
                    unsigned d = min(min(min(tn - tl[0], tn - tl[1]), min(tn - tl[2], tn - tl[3])), 800u);
                    d = min(d, dppu<0xB1>(d)); d = min(d, dppu<0x4E>(d)); d = min(d, dppu<0x124>(d)); d = min(d, dppu<0x128>(d));
                    unsigned tr = tn - min(min((unsigned)__builtin_amdgcn_readlane(d, 0), (unsigned)__builtin_amdgcn_readlane(d, 16)), min((unsigned)__builtin_amdgcn_readlane(d, 32), (unsigned)__builtin_amdgcn_readlane(d, 48)));
                    lds_set(lb + L_CNT + 56, tr | 1u);
                    until(__builtin_amdgcn_readfirstlane(tr | 1u) + x4a);
                } else sched(X4, 0);
                STAMP(16 + 32 * layer);
                float hv;
                u32x4 a0, a1, a2, a3, ac, as;
                auto load = [&]() __attribute__((always_inline)) {
                    as = ld16_sc1(ws, os);
                    if (layer == 0) ac = ld16_sc1(ws, oc);
                    a0 = ld16_sc1<0>(ws, o); a1 = ld16_sc1<1024>(ws, o); a2 = ld16_sc1<2048>(ws, o); a3 = ld16_sc1<3072>(ws, o);
                    if (layer == 0) lds_add(32, 1u);
                };
                auto all_in = [&]() __attribute__((always_inline)) {
                    if (layer == 0) asm volatile("s_waitcnt vmcnt(0)" : "+v"(a0), "+v"(a1), "+v"(a2), "+v"(a3), "+v"(ac), "+v"(as) :: "memory");
                    else { asm volatile("s_waitcnt vmcnt(0)" : "+v"(a0), "+v"(a1), "+v"(a2), "+v"(a3), "+v"(as) :: "memory"); ac = as; }
                };
                auto compute = [&]() __attribute__((always_inline)) {
#define ROR(n) " row_ror:" #n " row_mask:0xf bank_mask:0xf\n\t"
#define QD2(p) " quad_perm:" p " row_mask:0xf bank_mask:0xf\n\t"
                    asm volatile("s_waitcnt vmcnt(%16)\n\tv_add_f32_dpp v40, %2, %2" ROR(8) "v_add_f32_dpp v41, %3, %3" ROR(8) "s_nop 0\n\tv_add_f32_dpp v40, v40, v40" ROR(4) "v_add_f32_dpp v41, v41, v41" ROR(4)
                                 "v_mov_b32 v42, v40\n\tv_mov_b32 v43, v41\n\ts_nop 0\n\tv_permlane16_swap_b32 v40, v42\n\tv_permlane16_swap_b32 v41, v43\n\t"
                                 "v_pk_add_f32 v[40:41], v[40:41], v[42:43]\n\tv_pk_mul_f32 v[40:41], v[40:41], %14\n\t"
                                 ".if %17 == 0\n\ts_waitcnt vmcnt(4)\n\tv_mov_b64 v[44:45], %12\n\tv_mov_b64 v[46:47], %13\n\t.endif\n\t"
                                 "v_fma_f32 v40, -v41, v41, v40\n\tv_cvt_pk_bf16_f32 v42, 0, v41\n\tv_maximum3_f32 v40, v40, 0, 0\n\t"
                                 ".if %17 == 0\n\tv_permlane32_swap_b32 v44, v46\n\tv_permlane32_swap_b32 v45, v47\n\tv_pk_add_f32 v[44:45], v[44:45], v[46:47]\n\t.endif\n\t"
                                 "v_cvt_pk_bf16_f32 v40, 0, v40\n\ts_waitcnt vmcnt(2)\n\tv_pk_add_f32 v[32:33], %4, %6\n\tv_add_f32 v40, 0x3727c5ac, v40\n\tv_pk_add_f32 v[34:35], %5, %7\n\t"
                                 ".if %17 == 0\n\tv_permlane16_swap_b32 v44, v45\n\t.endif\n\t"
                                 "v_cvt_pk_bf16_f32 v40, 0, v40\n\ts_waitcnt vmcnt(0)\n\tv_pk_add_f32 v[36:37], %8, %10\n\tv_sqrt_f32 v40, v40\n\tv_pk_add_f32 v[38:39], %9, %11\n\t"
                                 ".if %17 == 0\n\tv_add_f32_dpp v44, v44, v44" QD2("[1,0,3,2]") "v_add_f32_dpp v45, v45, v45" QD2("[1,0,3,2]") ".endif\n\t"
                                 "v_pk_add_f32 v[32:33], v[32:33], v[36:37]\n\tv_pk_add_f32 v[34:35], v[34:35], v[38:39]\n\tv_cvt_pk_bf16_f32 v40, 0, v40\n\t"
                                 ".if %17 == 0\n\tv_add_f32_dpp v44, v44, v44" QD2("[2,3,0,1]") "v_add_f32_dpp v45, v45, v45" QD2("[2,3,0,1]") "v_mov_b64 %1, v[44:45]\n\t.else\n\ts_nop 0\n\t.endif\n\t"
                                 "v_permlane32_swap_b32 v32, v34\n\tv_permlane32_swap_b32 v33, v35\n\tv_rcp_f32 v40, v40\n\tv_pk_add_f32 v[32:33], v[32:33], v[34:35]\n\ts_nop 1\n\t"
                                 "v_permlane16_swap_b32 v32, v33\n\t"
                                 "v_pk_fma_f32 v[32:33], %1, v[42:43], v[32:33] op_sel_hi:[1,0,1] neg_lo:[1,0,0] neg_hi:[1,0,0]\n\tv_pk_mul_f32 v[32:33], v[32:33], v[40:41] op_sel_hi:[1,0]\n\t"
                                 "v_cvt_pk_bf16_f32 v32, 0, v32\n\tv_mul_f32 v34, 0xbfb8aa3b, v32\n\tv_exp_f32 v34, v34\n\tv_cvt_pk_bf16_f32 v33, 0, v33\n\tv_add_f32 v34, 1.0, v34\n\t"
                                 "v_rcp_f32 v34, v34\n\ts_nop 0\n\tv_mul_f32 v32, v34, v32\n\tv_cvt_pk_bf16_f32 v32, 0, v32\n\tv_mul_f32 %0, v33, v32\n\tv_cvt_pk_bf16_f32 v32, %0, %0\n\tds_write_b16 %15, v32"
                                 : "=&v"(hv), "+v"(cs)
                                 : "v"(as[0]), "v"(as[1]), "v"(LO2(a0)), "v"(HI2(a0)), "v"(LO2(a1)), "v"(HI2(a1)), "v"(LO2(a2)), "v"(HI2(a2)), "v"(LO2(a3)), "v"(HI2(a3)),
                                   "v"(LO2(layer ? as : ac)), "v"(HI2(layer ? as : ac)), "s"(rdm), "v"(ha), "i"(layer ? 4 : 5), "i"(layer), "v"(as)
                                 : "memory", "v32", "v33", "v34", "v35", "v36", "v37", "v38", "v39", "v40", "v41", "v42", "v43", "v44", "v45", "v46", "v47");
#undef ROR
#undef QD2
                };
                RETRY(load(), (all_in(), max(max(max(um4(a0), um4(a1)), max(um4(a2), um4(a3))), max(um4(ac), um4(as)))), compute(), any_nan(hv, hv), layer ? X4 : X4A);
                BAR();
                MARK(e_gu);
                STAMP(12 + 32 * layer);
                if (layer == 0) {
                    *reinterpret_cast<f32x2*>(smem + L_CS2 + wave * 512 + lane * 8) = cs;
                }
            }
            if (layer == 0) {
                if (wave == 1) {
                    const unsigned o = WS_CS + (unsigned)(gi * 6144 + 3072 + (lane < 48 ? lane : 0) * 16);
                    u32x4 a[8];
                    GATHER8(a, o, 768);
                    WAIT8(a, 0);
                    if (lane < 48) *reinterpret_cast<f32x4*>(smem + L_CS + lane * 16) = SUM8(a);
                }
                Frag80 f[4];
#pragma unroll
                for (int i = 0; i < 4; ++i) { f[i] = frag80(lb + L_WQ + (unsigned)(128 * wave + 32 * i + 8 * p4), 416, r0s, r1s, r2s); PIN3(f[i].a0, f[i].a1, f[i].a2); }
                unsigned b16 = lb + L_XS + (unsigned)(q4 * 160 + kq * 16), b8 = lb + L_XS + (unsigned)(q4 * 160 + 128 + kq * 8);
                unsigned so = WS_H1 + qS + (unsigned)((gi * 4 + q4) * 16384 + gj * 896 + 256 * wave + kq * 64 + p4 * 16);
                unsigned long long mk = 0x1111111111111111ull;
                PIN3(b16, b8, so); PINS(mk);
                GUARD(3800);
                MARK(b_q);
                BAR();
                STAMP(33);
                const Frag80 b = rows80(b16, b8);
                st16_sc1(ws, so, tiles4(f, b, mk));
                STAMP(34);
                qk_addr(true); att_addr();
                unsigned x0a = lb + L_M + (unsigned)((lane >> 5) * 16), x1a = lb + L_M + (unsigned)((lane >> 4) * 8), x2a, r2a;
                unsigned r0a = lb + L_OP + (unsigned)((lane >> 5) * 4096 + (lane & 31) * 16), r1a = lb + L_OP + (unsigned)(512 + (lane >> 4) * 2048 + (lane & 15) * 16);
                unsigned s0a = (unsigned)(lane * 4), s1a = (unsigned)(256 + (lane >> 5) * 64 + (lane & 15) * 4);
                ml_addr(lane, x2a, r2a);
                PIN3(x0a, x1a, x2a); PIN3(r0a, r1a, r2a); PIN2(s0a, s1a);
                cma = lb + L_M; cxa = wave == 0 ? x0a : wave == 1 ? x1a : x2a; cra = wave == 0 ? r0a : wave == 1 ? r1a : r2a;
                cso = WS_OWN + rS + (unsigned)(gi * 16384 + 4096 + gj * 512) + (wave == 0 ? s0a : wave == 1 ? s1a : 384u);
                PIN2(cma, cxa); PIN2(cra, cso);
                asm volatile("" : "=v"(omr), "=v"(owa), "=v"(owb));
            }
        } else {
            const int t = wave - 3;
            StatAddr sa;
            u32x4 sel;
            unsigned long long hi = 0xff00ff00ff00ff00ull;
            auto gather32 = [&](unsigned o, unsigned X, auto fin) __attribute__((always_inline)) {
                u32x4 a0 = ld16_sc1<-2048>(ws, o), a1 = ld16_sc1<-1024>(ws, o), a2 = ld16_sc1<0>(ws, o), a3 = ld16_sc1<1024>(ws, o);
                float v;
                auto sum = [&]() __attribute__((always_inline)) {
                    asm volatile("s_waitcnt vmcnt(3)\n\tv_mfma_f32_16x16x32_bf16 v[40:43], %1, %5, 0\n\ts_waitcnt vmcnt(2)\n\tv_mfma_f32_16x16x32_bf16 v[40:43], %2, %5, v[40:43]\n\t"
                                 "s_waitcnt vmcnt(1)\n\tv_mfma_f32_16x16x32_bf16 v[40:43], %3, %5, v[40:43]\n\ts_waitcnt vmcnt(0)\n\tv_mfma_f32_16x16x32_bf16 v[40:43], %4, %5, v[40:43]\n\t"
                                 "s_nop 7\n\tv_cndmask_b32 v40, v40, v41, %6\n\tv_cndmask_b32 v42, v42, v43, %6\n\ts_nop 1\n\tv_permlane32_swap_b32 v40, v42\n\tv_add_f32 v40, v40, v42\n\t"
                                 "v_cvt_pk_bf16_f32 %0, 0, v40"
                                 : "=&v"(v), "+v"(a0), "+v"(a1), "+v"(a2), "+v"(a3) : "v"(sel), "s"(hi) : "memory", "v40", "v41", "v42", "v43");
                    fin(v);
                    __builtin_amdgcn_sched_barrier(0);
                };
                sum();
                if (__builtin_expect(any_nan(v, v) != 0ull, 0)) {
#pragma unroll 1
                    for (unsigned spins = 0; spins < kSpinCap; ++spins) {
                        slip(X);
                        again16<-2048>(a0, ws, o); again16<-1024>(a1, ws, o); again16<0>(a2, ws, o); again16<1024>(a3, ws, o);
                        asm volatile("s_waitcnt vmcnt(0)" : "+v"(a0), "+v"(a1), "+v"(a2), "+v"(a3) :: "memory");
                        if (!any_empty(max(max(um4(a0), um4(a1)), max(um4(a2), um4(a3))))) break;
                    }
                    sum();
                }
            };
            auto poll32 = [&](unsigned o, unsigned t0) __attribute__((always_inline)) {
                    unsigned long long m0, m1, m2, m3; unsigned cnt = kSpinCap; float v;
#define CHK(r, m) "v_max3_u32 v44, v" #r ", v[" #r "+1], v[" #r "+2]\n\tv_max_u32 v44, v44, v[" #r "+3]\n\tv_cmp_eq_u32 %" #m ", -1, v44\n\t"
                    asm volatile("s_branch 3f\n"
                                 "1:\n\ts_mov_b64 exec, %1\n\tglobal_load_dwordx4 v[0:3], %6, %7 offset:-2048 sc1\n\ts_mov_b64 exec, %2\n\tglobal_load_dwordx4 v[4:7], %6, %7 offset:-1024 sc1\n\t"
                                 "s_mov_b64 exec, %3\n\tglobal_load_dwordx4 v[8:11], %6, %7 sc1\n\ts_mov_b64 exec, %4\n\tglobal_load_dwordx4 v[12:15], %6, %7 offset:1024 sc1\n\ts_mov_b64 exec, -1\n\t"
                                 "s_sub_u32 %5, %5, 1\n\ts_cselect_b64 vcc, 0, -1\n\ts_waitcnt vmcnt(0)\n\ts_branch 2f\n"
                                 "3:\n\ts_memrealtime vcc\n\ts_waitcnt lgkmcnt(0)\n\ts_sub_u32 vcc_lo, %10, vcc_lo\n\ts_cmp_lt_u32 vcc_lo, 0x400\n\ts_cbranch_scc1 3b\n\t"
                                 "global_load_dwordx4 v[0:3], %6, %7 offset:-2048 sc1\n\tglobal_load_dwordx4 v[4:7], %6, %7 offset:-1024 sc1\n\t"
                                 "global_load_dwordx4 v[8:11], %6, %7 sc1\n\tglobal_load_dwordx4 v[12:15], %6, %7 offset:1024 sc1\n\ts_mov_b64 vcc, -1\n"
                                 "2:\n\ts_waitcnt vmcnt(3)\n\tv_mfma_f32_16x16x32_bf16 v[40:43], v[0:3], %8, 0\n\t" CHK(0, 1) "s_waitcnt vmcnt(2)\n\tv_mfma_f32_16x16x32_bf16 v[40:43], v[4:7], %8, v[40:43]\n\t" CHK(4, 2)
                                 "s_waitcnt vmcnt(1)\n\tv_mfma_f32_16x16x32_bf16 v[40:43], v[8:11], %8, v[40:43]\n\t" CHK(8, 3) "s_waitcnt vmcnt(0)\n\tv_mfma_f32_16x16x32_bf16 v[40:43], v[12:15], %8, v[40:43]\n\t" CHK(12, 4)
                                 "s_or_b64 exec, %1, %2\n\ts_or_b64 exec, exec, %3\n\ts_or_b64 exec, exec, %4\n\ts_and_b64 vcc, exec, vcc\n\ts_cbranch_scc1 1b\n\t"
                                 "s_mov_b64 exec, -1\n\tv_cndmask_b32 v40, v40, v41, %9\n\tv_cndmask_b32 v42, v42, v43, %9\n\ts_nop 1\n\tv_permlane32_swap_b32 v40, v42\n\tv_add_f32 v40, v40, v42\n\t"
                                 "v_cvt_pk_bf16_f32 %0, 0, v40"
                                 : "=&v"(v), "=&s"(m0), "=&s"(m1), "=&s"(m2), "=&s"(m3), "+s"(cnt) : "v"(o), "s"(ws), "v"(sel), "s"(hi), "s"(t0 - 1u)
                                 : "memory", "vcc", "scc", "v0", "v1", "v2", "v3", "v4", "v5", "v6", "v7", "v8", "v9", "v10", "v11", "v12", "v13", "v14", "v15", "v40", "v41", "v42", "v43", "v44");
#undef CHK
                return v;
            };
#define PUT8(A, B, M) "v_min_u32 v34, -2, v32\n\tv_min_u32 v32, -2, v32\n\tv_min_u32 v35, -2, v33\n\tv_min_u32 v33, -2, v33\n\tv_permlane16_swap_b32 v32, v34\n\ts_nop 0\n\t" \
                      "v_permlane16_swap_b32 v33, v35\n\ts_mov_b64 exec, " M "\n\tglobal_store_dwordx4 " A ", v[32:35], " B " sc1\n\ts_mov_b64 exec, -1\n\ts_nop 0"
            unsigned xw, qo, qw, qc;
            __builtin_amdgcn_s_setprio(0);
            {
                STAMP(6 + 32 * layer);
                bf16x8 af[6];
                unsigned long long mk = 0x000f000f000f000full;
                if (layer == 0) {
                    const int lane = lane_id();
                    xe = lo16(*reinterpret_cast<const u16*>(smem + L_X0 + xrow(lane) * 1280 + gj * 160 + t * 32 + xcol(lane) * 2));
                    op_addr(0);
                    GUARD(1600);
                    MARK(b_op);
                    if (nj | (ord == OML)) { VMW(0); lds_add(44, nj); }
#pragma unroll 1
                    for (unsigned spins = 0; spins < (kSpinCap << 4); ++spins) if (lds_word(lb + L_CNT + 44) >= 5u) break;
                    STAMP(8);
                } else MARK(b_op);
                const unsigned lo = cma, lw = cxa, so = cso, br = cra, mr = omr, h3 = tile35(gj, 10 * layer + t);
                unsigned go, xs;
#pragma unroll
                for (int s = 0; s < 6; ++s) af[s] = cat8(tr64o(owa, s * 32 * 160), tr64o(owb, s * 32 * 160));
                PINS(mk); PINS(hi);
                if (layer != 0 && t < 4) {
                    sched(X2, 0);
                    STAMP(49);
                    u32x4 ov = ld16_sc1(ws, lo);
                    WAIT1(ov, 0);
                    *(LAS u32x4*)(size_t)lw = ov;
                    if (__builtin_expect(any_empty(um4(ov)), 0)) {
#pragma unroll 1
                        for (unsigned spins = 0; spins < kSpinCap; ++spins) {
                            slip(X2);
                            ov = ld16_sc1(ws, lo);
                            WAIT1(ov, 0);
                            *(LAS u32x4*)(size_t)lw = ov;
                            if (!any_empty(um4(ov))) break;
                        }
                    }
                }
                {
                    bf16x8 b0, b1, b2, b3, b4, b5;
#define QP " row_half_mirror row_mask:0xf bank_mask:0xf\n\t"
                    asm volatile("s_waitcnt lgkmcnt(0)\n\ts_barrier\n\t"
                                 "ds_read_b128 %0, %12\n\tds_read_b64 v[36:37], %13\n\tds_read_b128 %1, %12 offset:64\n\tds_read_b128 %2, %12 offset:128\n\tds_read_b128 %3, %12 offset:192\n\t"
                                 "ds_read_b128 %4, %12 offset:256\n\tds_read_b128 %5, %12 offset:320\n\t"
                                 "s_waitcnt lgkmcnt(5)\n\tv_mfma_f32_16x16x32_bf16 v[32:35], %6, %0, 0\n\tv_max_f32_dpp v38, v36, v36" QP "v_sub_f32 v38, v36, v38\n\t"
                                 "s_waitcnt lgkmcnt(4)\n\tv_mfma_f32_16x16x32_bf16 v[32:35], %7, %1, v[32:35]\n\tv_exp_f32 v38, v38\n\t"
                                 "s_waitcnt lgkmcnt(3)\n\tv_mfma_f32_16x16x32_bf16 v[32:35], %8, %2, v[32:35]\n\tv_mul_f32 v37, v37, v38\n\t"
                                 "s_waitcnt lgkmcnt(2)\n\tv_mfma_f32_16x16x32_bf16 v[32:35], %9, %3, v[32:35]\n\tv_add_f32_dpp v39, v37, v37" QP
                                 "s_waitcnt lgkmcnt(1)\n\tv_mfma_f32_16x16x32_bf16 v[32:35], %10, %4, v[32:35]\n\tv_rcp_f32 v39, v39\n\t"
                                 "s_waitcnt lgkmcnt(0)\n\tv_mfma_f32_16x16x32_bf16 v[32:35], %11, %5, v[32:35]\n\tv_mul_f32 v38, v38, v39\n\ts_nop 6\n\t"
                                 "v_pk_mul_f32 v[32:33], v[32:33], v[38:39] op_sel_hi:[1,0]\n\tv_pk_mul_f32 v[34:35], v[34:35], v[38:39] op_sel_hi:[1,0]\n\ts_nop 0\n\t"
                                 "v_add_f32_dpp v32, v32, v32" QP "v_add_f32_dpp v33, v33, v33" QP "v_add_f32_dpp v34, v34, v34" QP "v_add_f32_dpp v35, v35, v35" QP
                                 "v_cvt_pk_bf16_f32 v32, v32, v33\n\tv_cvt_pk_bf16_f32 v33, v34, v35\n\t" PUT8("%14", "%15", "%16")
                                 : "=&v"(b0), "=&v"(b1), "=&v"(b2), "=&v"(b3), "=&v"(b4), "=&v"(b5)
                                 : "v"(af[0]), "v"(af[1]), "v"(af[2]), "v"(af[3]), "v"(af[4]), "v"(af[5]), "v"(br), "v"(mr), "v"(so), "s"(ws), "s"(mk)
                                 : "memory", "v32", "v33", "v34", "v35", "v36", "v37", "v38", "v39");
#undef QP
                }
                if (layer == 0 && wave == 7) st4_sc1(ws, WS_MB + (unsigned)(gi * 1024 + 64 + gj * 4), now() | 1u);
                const unsigned tp = layer ? 0u : now();
                if (layer == 0) sides();
                STAMP(7 + 32 * layer);
                {
                    const int ln = lane_id();
#pragma unroll
                    for (int k = 0; k < 4; ++k) sel[k] = ((ln >> 1) & 3) == k ? (ln & 1) ? 0x3f800000u : 0x3f80u : 0u;
                    go = h3 + nS + (unsigned)(2048 + ln * 16); xs = lb + L_XS + (unsigned)(xrow(ln) * 160 + t * 32 + xcol(ln) * 2);
                    PIN3(sel, go, xs);
                    sa = stats_addr(ln, h4 + (unsigned)(H4T + gj * 128), 16);
                }
                if (layer != 0) sched(X3, 0);
                STAMP(18 + 32 * layer);
                if (layer != 0) gather32(go, X3, [&](float s) { xw = pk2(xe + s, 0.f); *(LAS u16*)(size_t)xs = (u16)xw; });
                else { xw = pk2(xe + poll32(go, tp + W3), 0.f); *(LAS u16*)(size_t)xs = (u16)xw; }
                xe = lo16(xw);
                STAMP(9 + 32 * layer);
            }
            BAR();
            if (wave == 3) stats(sa);
            MARK(e_op);
            STAMP(11 + 32 * layer);
            {
                LANE_VARS();
                const unsigned im = lb + L_WD + (unsigned)(32 * t + 8 * p4);
                bf16x8 wd0 = cat8(tr64(im + r0s * 160), tr64(im + r1s * 160)); bf16x4 wd1 = tr64(im + (32 + r2s) * 160);
                const unsigned h5 = tile35(gj, 10 * layer + 5 + t);
                unsigned h16 = lb + L_H + (unsigned)(p4 * 96 + kq * 16), h8 = lb + L_H + (unsigned)(p4 * 96 + 64 + kq * 8);
                unsigned so = h5 + (unsigned)((kq & 1) * 8192 + gi * 128 + (kq >> 1) * 64 + p4 * 16);
                unsigned long long mk = 0x000f000f000f000full;
                unsigned go = h5 + nS + (unsigned)(2048 + lane * 16), xs = (layer ? (unsigned)(xrow(lane) * 1280 + gj * 160) : lb + L_XS + (unsigned)(xrow(lane) * 160)) + (unsigned)(t * 32 + xcol(lane) * 2);
                unsigned sta = lb + L_ST + (unsigned)(xrow(lane) * 16);
                if (layer == 0 && wave < 6) {
                    sa = stats_addr(lane, WS_H1 + qS + (unsigned)(gi * 65536 + gj * 896 + 768), 16384);
                    asm volatile("" : "=v"(qo), "=v"(qw), "=v"(qc), "=v"(cma), "=v"(cxa), "=v"(cra), "=v"(cso), "=v"(omr), "=v"(owa), "=v"(owb));
                    asm volatile("" : "=v"(qa), "=v"(pw), "=v"(ma), "=v"(idx), "=v"(qm), "=v"(pr), "=v"(oa), "=v"(oc));
                } else if (layer == 0) {
                    op_addr(1); qk_addr(false); att_addr();
                    const int l5 = lane & 31, c = l5 < 24 ? 24 * (wave - 6) + l5 : 48;
                    qo = WS_H1 + qS + (unsigned)((gi * 4 + (gj >> 1)) * 16384 + 3584 + (lane >> 5) * 896 + c * 16);
                    qw = lb + L_Q + (unsigned)(l5 < 24 ? (c >> 4) * 128 + (c & 3) * 32 + (c & 12) + (lane >> 5) * 16 : lane < 26 ? 384 + (lane & 1) * 16 : 448);
                    qc = lb + L_CS + (unsigned)(c * 16 + (lane >> 5) * 8);
                    PIN3(qo, qw, qc);
                }
                PIN2(wd0, wd1); PIN3(h16, h8, so); PIN3(go, xs, sta); PINS(mk);
                GUARD(2000);
                MARK(b_dn);
                if (wave == 4) {
                    unsigned os = h4 + (unsigned)(H4T + (p4 + 4 * (kq & 1)) * 128 + q4 * 16);
                    float sw = *reinterpret_cast<const float*>(smem + L_CNT + 12);
                    PIN2(os, sw);
                    if (layer == 0) {
#pragma unroll 1
                        for (unsigned spins = 0; spins < (kSpinCap << 6); ++spins) if (lds_word(lb + L_CNT + 56)) break;
                    }
                    sched(layer ? X4 : X4A, 0);
                    u32x4 as;
#pragma unroll 1
                    for (unsigned spins = 0; spins < kSpinCap; ++spins) { as = ld16_sc1(ws, os); WAIT1(as, 0); if (!any_empty(um4(as))) break; }
                    float ss = __uint_as_float(as[0]), sm = __uint_as_float(as[1]), rd = __uint_as_float(as[2]);
                    ss += dpp<0x4E>(ss); sm += dpp<0x4E>(sm); rd += dpp<0x4E>(rd);
                    ss += dpp<0xB1>(ss); sm += dpp<0xB1>(sm); rd += dpp<0xB1>(rd);
                    ss = x16_add(ss); sm = x16_add(sm); rd = x16_add(rd);
                    if (LANES(0x1111ull)) *reinterpret_cast<f32x4*>(smem + L_ST + q4 * 16) = (f32x4){ss, sm, rd, sw};
                }
                if (layer == 0 && (wave == 3 || wave == 5)) {
                    unsigned ea = lane < 40 ? tile35(gj, 15 + (lane >> 3)) + (unsigned)(gi * 128 + (lane & 7) * 16) : WS_OWN + rS + (unsigned)(gi * 16384 + 4096 + gj * 512 + (lane - 40) * 16);
                    unsigned eb = ea + (lane < 40 ? 8192u : 256u);
                    PIN2(ea, eb);
#pragma unroll 1
                    for (unsigned spins = 0; spins < (kSpinCap << 6); ++spins) { if (lds_word(lb + L_CNT + 32) >= 6u) break; __builtin_amdgcn_s_sleep(1); }
                    if (wave == 5) empty4(1, fS, lane);
                    else { st16m(ws, ea, (u32x4){~0u, ~0u, ~0u, ~0u}, 0xffffffffffffffull); st16m(ws, eb, (u32x4){~0u, ~0u, ~0u, ~0u}, 0xffffffffffffffull); }
                }
                asm volatile("s_waitcnt lgkmcnt(0)\n\ts_barrier\n\tds_read_b128 v[36:39], %2\n\tds_read_b64 v[40:41], %3\n\t"
                             "s_waitcnt lgkmcnt(1)\n\tv_mfma_f32_16x16x32_bf16 v[32:35], %0, v[36:39], 0\n\ts_waitcnt lgkmcnt(0)\n\tv_mfma_f32_16x16x16_bf16 v[42:45], %1, v[40:41], 0\n\ts_nop 7\n\t"
                             "v_pk_add_f32 v[32:33], v[32:33], v[42:43]\n\tv_pk_add_f32 v[34:35], v[34:35], v[44:45]\n\t"
                             "v_cvt_pk_bf16_f32 v32, v32, v33\n\tv_cvt_pk_bf16_f32 v33, v34, v35\n\t" PUT8("%4", "%5", "%6")
                             :: "v"(wd0), "v"(wd1), "v"(h16), "v"(h8), "v"(so), "s"(ws), "s"(mk)
                             : "memory", "v32", "v33", "v34", "v35", "v36", "v37", "v38", "v39", "v40", "v41", "v42", "v43", "v44", "v45");
                STAMP(13 + 33 * layer);
                if (layer == 0 || __builtin_expect(gi == 20 + 2 * t + (gj & 1), 1)) {
                    const f32x4 sr = *(const LAS f32x4*)(size_t)sta;
                    const f32x2 st = ln_stats(sr[0], sr[1]);
                    float rs = bfr(frcp(1.f + ex2(bfr((sr[2] - st[0] * sr[3]) * st[1]) * -LOG2E))), n1 = bfr(bfr(xe - st[0]) * st[1]);
                    PIN2(rs, n1);
                    if (layer == 0) {
                        sched(X5A, 0);
                        STAMP(15);
                        gather32(go, X5A, [&](float y) { xw = pk2(xe + bfr(n1 + bfr(y * rs)), 0.f); *(LAS u16*)(size_t)xs = (u16)xw; });
                        xe = lo16(xw);
                        STAMP(14);
                    } else {
                        st2(out, xs, pk2(xe + bfr(n1 + bfr(poll32(go, when(X5B)) * rs)), 0.f));
                        asm volatile("s_endpgm");
                        MARK(e_dn);
                        STAMP(45);
                    }
                }
            }
            if (layer == 0) {
                BAR();
                if (wave == 3) { stats(sa); STAMP(34); }
                MARK(e_dn);
                if (wave == 3) __builtin_amdgcn_s_setprio(2);
                if (__builtin_expect(wave < 6, 0)) { op_addr(1); qk_addr(false); att_addr(); }
                if (wave >= 6) {
                    const unsigned o = qo;
                    f32x2 cs = -*(const LAS f32x2*)(size_t)qc;
                    float rn = 1.f / DM;
                    unsigned long long m0 = 1ull << 24, m1 = 1ull << 25;
                    PIN2(cs, rn); PINS(m0); PINS(m1);
                    MARK(b_qg);
                    sched(X1, 0);
                    STAMP(51);
                    u32x4 a0, a1, a2, a3; f32x2 u;
                    RETRY({ a0 = ld16_sc1<-3584>(ws, o); a1 = ld16_sc1<-1792>(ws, o); a2 = ld16_sc1<0>(ws, o); a3 = ld16_sc1<1792>(ws, o); },
                          ({ asm volatile("s_waitcnt vmcnt(0)" : "+v"(a0), "+v"(a1), "+v"(a2), "+v"(a3) :: "memory"); max(max(um4(a0), um4(a1)), max(um4(a2), um4(a3))); }),
                          { unsigned sm;
                            asm volatile("s_waitcnt vmcnt(2)\n\tv_pk_add_f32 v[40:41], %2, %4\n\tv_pk_add_f32 v[42:43], %3, %5\n\t"
                                         "s_waitcnt vmcnt(1)\n\tv_pk_add_f32 v[40:41], v[40:41], %6\n\tv_pk_add_f32 v[42:43], v[42:43], %7\n\t"
                                         "s_waitcnt vmcnt(0)\n\tv_pk_add_f32 v[40:41], v[40:41], %8\n\tv_pk_add_f32 v[42:43], v[42:43], %9\n\ts_nop 1\n\t"
                                         "v_permlane32_swap_b32 v40, v42\n\tv_permlane32_swap_b32 v41, v43\n\tv_pk_add_f32 v[40:41], v[40:41], v[42:43]\n\ts_nop 0\n\t"
                                         "v_readlane_b32 %1, v41, 24\n\ts_nop 1\n\tv_mul_f32 v42, %1, %11\n\tv_cvt_pk_bf16_f32 v42, 0, v42\n\tv_pk_fma_f32 %0, %10, v[42:43], v[40:41] op_sel_hi:[1,0,1]"
                                         : "=&v"(u), "=&s"(sm) : "v"(LO2(a0)), "v"(HI2(a0)), "v"(LO2(a1)), "v"(HI2(a1)), "v"(LO2(a2)), "v"(HI2(a2)), "v"(LO2(a3)), "v"(HI2(a3)), "v"(cs), "v"(rn)
                                         : "memory", "v40", "v41", "v42", "v43");
                            unsigned w = pk2v(u);
                            if (LANES(m0)) w = __float_as_uint(u[0]);
                            if (LANES(m1)) w = __float_as_uint(u[1]);
                            *(LAS unsigned*)(size_t)qw = w; __builtin_amdgcn_sched_barrier(0); }, any_nan(u[0], u[1]), X1);
                    MARK(e_qg);
                }
            }
        }
    };
    PINS(ck1); PINS(ck2); PINS(csc);
    chain(IC<0>{});
    STAMP(35);
    att1();
    chain(IC<1>{});
}

__device__ __attribute__((used, noinline)) void mega_code_end() { asm volatile(".Lmega_code_end:"); }

extern "C" void kernel_launch(void* const* d_in, const int* in_sizes, int n_in, void* d_out, int out_size, void* d_ws,
                              size_t ws_size, hipStream_t stream) {
    const u16* x0 = (const u16*)d_in[0];
    const u16* kc = (const u16*)d_in[1];
    const u16* vc = (const u16*)d_in[2];
    const u16* wq = (const u16*)d_in[3];
    const u16* wo = (const u16*)d_in[4];
    const u16* wr = (const u16*)d_in[6];
    const u16* wg = (const u16*)d_in[7];
    const u16* wu = (const u16*)d_in[8];
    const u16* wd = (const u16*)d_in[9];
    (void)hipFuncSetAttribute((const void*)mega_kernel, hipFuncAttributeMaxDynamicSharedMemorySize, L_END);
    mega_kernel<<<256, 512, L_END, stream>>>(x0, kc, vc, wq, wo, wr, wg, wu, wd, (u16*)d_out, (unsigned char*)d_ws
                                             );
}
```

```cpp
#include <hip/hip_runtime.h>
#include <hip/hip_bf16.h>
#include <stdint.h>

typedef unsigned short u16;
typedef unsigned u32x4 __attribute__((ext_vector_type(4)));
typedef unsigned u32x2 __attribute__((ext_vector_type(2)));
typedef unsigned long long u64x8 __attribute__((ext_vector_type(8)));
typedef unsigned long long u64x2 __attribute__((ext_vector_type(2)));
typedef float f32x4 __attribute__((ext_vector_type(4)));
typedef short s16x4 __attribute__((ext_vector_type(4)));
typedef __attribute__((ext_vector_type(4))) __bf16 bf16x4;
typedef __attribute__((ext_vector_type(8))) __bf16 bf16x8;
#define LAS __attribute__((address_space(3)))

constexpr int DM = 640, HQ = 32, SK = 4096, DH = 192, WIN = 1024, FF = 1536;
constexpr int QD = HQ * DH;
constexpr float SCALE = 0.07216878364870322f, LOG2E = 1.44269504088896341f;
constexpr unsigned kSpinCap = 1u << 16;
#define GJ(g) (((g) >> 3) & 7)
#define GI(g) (((g) & 7) * 4 + ((g) >> 6))

#define D4A 105
#define D5A 86
#define D1 84
#define D2 133
#define D3 66
#define D4 78
#define D5B 74
#define PACE 59
#define PINIT 100
#define PMIN 50
#define DM_ 0
constexpr unsigned X4A = D4A + DM_, X5A = X4A + D5A + DM_, X1 = X5A + D1 + DM_, X2 = X1 + D2 + DM_, X3 = X2 + D3 + DM_, X4 = X3 + D4 + DM_, X5B = X4 + D5B + DM_;
#define TLAND 132
#define XCOST 8
#define WOFF 6
#define BETA 11
constexpr unsigned WOC = 2, WEARLY = 1;
constexpr unsigned NUNIT = 384 + XCOST + 3 * (9 + 2 * WEARLY), PBURST = 30;
constexpr int LEAD = 12, TOFF = 4;
constexpr unsigned DG2 = 12, W3 = 15;
constexpr int GP = 0, GA = 1, GB = 6, GC = 7, HD = 1;
#define VORD 0, 1, 8, 2, 3, 9, 4, 5, 6, 7, 10, 11
constexpr int kVord[12] = {VORD};
constexpr int v_upto(int h) { int n = 0; for (int i = 0; i <= h; ++i) n += kVord[i] < 8 ? 2 : 4; return n; }
constexpr int v_after(int p) { int h = 0; while (kVord[h] != p) ++h; return 32 - v_upto(h); }
constexpr int OML = 2;

constexpr unsigned WS_H1 = 0;
constexpr unsigned WS_H4 = 2u << 20;
constexpr unsigned H4C = 20480, H4T = H4C + 3072;
constexpr unsigned WS_H35 = 4u << 20;
constexpr unsigned WS_OWN = WS_H35 + 80 * 16384;
constexpr unsigned WS_CS = WS_OWN + 32 * 16384;
constexpr unsigned WS_MB = WS_CS + 256 * 768;
constexpr unsigned WS_R = WS_MB + 256 * 128;
constexpr unsigned WS_END = WS_R + 128;
constexpr unsigned WS_Q0 = 6u << 20;
static_assert(WS_END <= (6u << 20), "ws map");
__device__ __forceinline__ unsigned tile35(int j, int q) { return WS_H35 + (unsigned)((j * 10 + (q >> 1)) * 16384 + (q & 1) * 4096); }
__device__ __forceinline__ unsigned h4s(int s) { return (unsigned)((s >> 1) * 16384 + (s & 1) * 4096); }

constexpr int L_WQ = 0;
constexpr int L_WO = 33792;
constexpr int L_GU = L_WO + 30720;
constexpr int L_WD = L_GU + 16384;
constexpr int L_X0 = L_WD + 8192;
constexpr int L_WR = L_X0 + 5120;
constexpr int L_XS = L_WR + 2048;
constexpr int L_Q = L_XS + 1024;
constexpr int L_OP = L_Q + 512;
constexpr int L_M = L_OP + 8192;
constexpr int L_H = L_M + 64;
constexpr int L_ST = L_H + 384;
constexpr int L_CS = L_ST + 64;
constexpr int L_CS2 = L_CS + 1024;
constexpr int L_CNT = L_CS2 + 1536;
constexpr int L_P = L_CNT + 96;
constexpr int L_O2 = L_P + 72 * 192;
constexpr int L_DBG = L_O2 + 3328;
constexpr int L_END = L_DBG + 2048;
static_assert(L_END <= 163840 && L_END > 81920, "LDS");

typedef __attribute__((ext_vector_type(2))) __bf16 bf16x2;
typedef float f32x2 __attribute__((ext_vector_type(2)));
__device__ __forceinline__ unsigned pk2(float a, float b) { return __builtin_bit_cast(unsigned, __builtin_convertvector((f32x2){a, b}, bf16x2)); }
__device__ __forceinline__ unsigned fexp(float x) { const unsigned e = (__float_as_uint(x) >> 23) & 255u; return e == 255u ? 0u : e; }
__device__ __forceinline__ u32x2 enc21(float a, float b, float c) {
    const float lim = 1048575.f;
    const unsigned i = (unsigned)(int)__builtin_rintf(__builtin_amdgcn_fmed3f(a, -lim, lim)) & 0x1fffffu, j = (unsigned)(int)__builtin_rintf(__builtin_amdgcn_fmed3f(b, -lim, lim)) & 0x1fffffu;
    const unsigned k = (unsigned)(int)__builtin_rintf(__builtin_amdgcn_fmed3f(c, -lim, lim)) & 0x1fffffu;
    return (u32x2){i | j << 21, j >> 11 | k << 10 | (__builtin_fabsf(a) + __builtin_fabsf(b) + __builtin_fabsf(c) < 1e30f ? 0u : 0x80000000u)};
}
__device__ __forceinline__ float lo16(unsigned w) { return __uint_as_float(w << 16); }
__device__ __forceinline__ float hi16(unsigned w) { return __uint_as_float(w & 0xffff0000u); }
__device__ __forceinline__ float bfr(float f) { return __uint_as_float(pk2(0.f, f)); }
template <int CTRL> __device__ __forceinline__ float dpp(float x) {
    return __builtin_bit_cast(float, __builtin_amdgcn_mov_dpp(__builtin_bit_cast(int, x), CTRL, 0xf, 0xf, true));
}
template <int CTRL> __device__ __forceinline__ unsigned dppu(unsigned x) { return (unsigned)__builtin_amdgcn_mov_dpp((int)x, CTRL, 0xf, 0xf, true); }
__device__ __forceinline__ float row16_sum(float v) {
    v += dpp<0xB1>(v); v += dpp<0x4E>(v); v += dpp<0x141>(v); v += dpp<0x140>(v); return v;
}
__device__ __forceinline__ float fmax2(float a, float b) { return __builtin_elementwise_maximum(a, b); }
__device__ __forceinline__ float fmax3(float a, float b, float c) { return fmax2(fmax2(a, b), c); }
__device__ __forceinline__ float x16_add(float v) { auto s = __builtin_amdgcn_permlane16_swap(__float_as_uint(v), __float_as_uint(v), false, false); return __uint_as_float(s[0]) + __uint_as_float(s[1]); }
__device__ __forceinline__ float x32_add(float v) { auto s = __builtin_amdgcn_permlane32_swap(__float_as_uint(v), __float_as_uint(v), false, false); return __uint_as_float(s[0]) + __uint_as_float(s[1]); }
__device__ __forceinline__ float wave_sum(float v) { return x32_add(x16_add(row16_sum(v))); }
__device__ __forceinline__ float rs32(float a, float b) { auto s = __builtin_amdgcn_permlane32_swap(__float_as_uint(a), __float_as_uint(b), false, false); return __uint_as_float(s[0]) + __uint_as_float(s[1]); }

#define GUARD(n) asm volatile("s_branch 1f\n\t.p2align 12,," #n "\n1:")
#define MARK(n) asm volatile(".Lcrit_" #n "_%=:" ::)
#define BAR() asm volatile("s_waitcnt lgkmcnt(0)\n\ts_barrier" ::: "memory")
#define VMW(n) asm volatile("s_waitcnt vmcnt(%0)" :: "i"(n) : "memory")

template <int OFF = 0> __device__ __forceinline__ u32x4 ld16_sc1(const void* sbase, unsigned voff) {
    u32x4 r; asm volatile("global_load_dwordx4 %0, %1, %2 offset:%3 sc1" : "=v"(r) : "v"(voff), "s"(sbase), "i"(OFF) : "memory"); return r;
}
__device__ __forceinline__ unsigned ld4_sc1(const void* sbase, unsigned voff) {
    unsigned r; asm volatile("global_load_dword %0, %1, %2 sc1" : "=v"(r) : "v"(voff), "s"(sbase) : "memory"); return r;
}
template <int OFF = 0> __device__ __forceinline__ void st16_sc1(void* sbase, unsigned voff, u32x4 v) {
    asm volatile("global_store_dwordx4 %0, %1, %2 offset:%3 sc1\n\ts_nop 1" :: "v"(voff), "v"(v), "s"(sbase), "i"(OFF) : "memory");
}
__device__ __forceinline__ void st4_sc1(void* sbase, unsigned voff, unsigned v) {
    asm volatile("global_store_dword %0, %1, %2 sc1" :: "v"(voff), "v"(v), "s"(sbase) : "memory");
}
__device__ __forceinline__ void st8(void* sbase, unsigned voff, u32x2 v) {
    asm volatile("global_store_dwordx2 %0, %1, %2" :: "v"(voff), "v"(v), "s"(sbase) : "memory");
}
__device__ __forceinline__ void st2(void* sbase, unsigned voff, unsigned v) {
    asm volatile("global_store_short %0, %1, %2" :: "v"(voff), "v"(v), "s"(sbase) : "memory");
}
constexpr int RK = 64, RV = 160;
template <int R, int IMM> __device__ __forceinline__ void ld16_nt(const void* sbase, unsigned voff) {
    asm volatile("global_load_dwordx4 v[%c3:%c4], %0, %1 offset:%2 nt" :: "v"(voff), "s"(sbase), "i"(IMM), "i"(R), "i"(R + 3) : "memory");
}
template <int R, int IMM> __device__ __forceinline__ void ld8_nt(const void* sbase, unsigned voff) {
    asm volatile("global_load_dwordx2 v[%c3:%c4], %0, %1 offset:%2 nt" :: "v"(voff), "s"(sbase), "i"(IMM), "i"(R), "i"(R + 1) : "memory");
}
template <int R> __device__ __forceinline__ void smf(f32x4& d, u32x4 a, unsigned idx) {
    asm volatile("v_smfmac_f32_16x16x64_bf16 %0, %1, v[%c3:%c4], %2" : "+v"(d) : "v"(a), "v"(idx), "i"(R), "i"(R + 7));
}
template <int A, int B> __device__ __forceinline__ void zip_in() {
    unsigned t;
    asm volatile("v_perm_b32 %0, v%c2, v%c1, %3\n\tv_perm_b32 v%c2, v%c2, v%c1, %4\n\tv_mov_b32 v%c1, %0" : "=&v"(t) : "i"(A), "i"(B), "s"(0x05040100u), "s"(0x07060302u));
}
template <int A, int B> __device__ __forceinline__ void zip_out(unsigned& lo, unsigned& hi) {
    asm volatile("v_perm_b32 %0, v%c3, v%c2, %4\n\tv_perm_b32 %1, v%c3, v%c2, %5" : "=&v"(lo), "=&v"(hi) : "i"(A), "i"(B), "s"(0x05040100u), "s"(0x07060302u));
}
template <int A, int B, int C, int D> __device__ __forceinline__ void zip_x(unsigned lo, unsigned hi) {
    asm volatile("v_perm_b32 v%c4, v%c3, v%c2, %6\n\tv_perm_b32 v%c5, v%c3, v%c2, %7\n\tv_mov_b32 v%c2, %0\n\tv_mov_b32 v%c3, %1"
                 :: "v"(lo), "v"(hi), "i"(A), "i"(B), "i"(C), "i"(D), "s"(0x05040100u), "s"(0x07060302u));
}
__device__ __forceinline__ void dma16(const void* sbase, unsigned voff, unsigned lds_dst) {
    unsigned keep;
    asm volatile("s_mov_b32 %0, m0\n\ts_mov_b32 m0, %3\n\ts_nop 0\n\tglobal_load_lds_dwordx4 %1, %2\n\ts_mov_b32 m0, %0"
                 : "=&s"(keep) : "v"(voff), "s"(sbase), "s"(lds_dst) : "memory");
}
__device__ __forceinline__ unsigned um4(u32x4 a) { return max(max(a[0], a[1]), max(a[2], a[3])); }
__device__ __forceinline__ bool all_set(unsigned t) { return __builtin_amdgcn_ballot_w64(t == 0u) == 0ull; }
__device__ __forceinline__ bool any_empty(unsigned mx) { return __builtin_amdgcn_ballot_w64(mx == ~0u) != 0ull; }
__device__ __forceinline__ unsigned long long any_nan(float a, float b) { return __builtin_amdgcn_ballot_w64(__builtin_isunordered(a, b)); }
__device__ __forceinline__ f32x4 asf(u32x4 a) { return __builtin_bit_cast(f32x4, a); }
__device__ __forceinline__ u32x4 asu(f32x4 a) { return __builtin_bit_cast(u32x4, a); }
#define LANES(m) __builtin_amdgcn_inverse_ballot_w64(m)

__device__ __forceinline__ bf16x4 tr64(unsigned lds_addr) { return __builtin_amdgcn_ds_read_tr16_b64_v4bf16((LAS bf16x4*)(size_t)lds_addr); }
__device__ __forceinline__ bf16x4 tr64o(unsigned lds_addr, int off) { return __builtin_amdgcn_ds_read_tr16_b64_v4bf16((LAS bf16x4*)((LAS char*)(size_t)lds_addr + off)); }
__device__ __forceinline__ bf16x8 cat8(bf16x4 a, bf16x4 b) { return __builtin_shufflevector(a, b, 0, 1, 2, 3, 4, 5, 6, 7); }
__device__ __forceinline__ bf16x8 lds16(unsigned a) { return *(const LAS bf16x8*)(size_t)a; }
__device__ __forceinline__ bf16x4 lds8(unsigned a) { return *(const LAS bf16x4*)(size_t)a; }
__device__ __forceinline__ f32x4 mfma32(bf16x8 a, bf16x8 b, f32x4 c) { return __builtin_amdgcn_mfma_f32_16x16x32_bf16(a, b, c, 0, 0, 0); }
__device__ __forceinline__ f32x4 mfma32u(u32x4 a, u32x4 b, f32x4 c) { return mfma32(__builtin_bit_cast(bf16x8, a), __builtin_bit_cast(bf16x8, b), c); }
__device__ __forceinline__ f32x4 mfma16(bf16x4 a, bf16x4 b, f32x4 c) {
    return __builtin_amdgcn_mfma_f32_16x16x16bf16_1k(__builtin_bit_cast(s16x4, a), __builtin_bit_cast(s16x4, b), c, 0, 0, 0);
}

#define STAMP(i) do {} while (0)

template <int OFF = 0> __device__ __forceinline__ void st16m(void* sbase, unsigned voff, u32x4 v, unsigned long long mask) {
    asm volatile("s_mov_b64 exec, %4\n\tglobal_store_dwordx4 %0, %1, %2 offset:%3 sc1\n\ts_mov_b64 exec, -1\n\ts_nop 0" :: "v"(voff), "v"(v), "s"(sbase), "i"(OFF), "s"(mask) : "memory");
}
template <int OFF> __device__ __forceinline__ void st16m2(void* sbase, unsigned voff, u32x4 v, unsigned voff2, u32x4 v2, unsigned long long mask) {
    asm volatile("s_mov_b64 exec, %6\n\tglobal_store_dwordx4 %0, %1, %2 sc1\n\tglobal_store_dwordx4 %4, %5, %2 offset:%3 sc1\n\ts_mov_b64 exec, -1\n\ts_nop 0"
                 :: "v"(voff), "v"(v), "s"(sbase), "i"(OFF), "v"(voff2), "v"(v2), "s"(mask) : "memory");
}
#define PINS(a) asm volatile("" : "+s"(a))
#define RETRY(LOAD, EMPTY, COMPUTE, NAN_, X) do { LOAD; COMPUTE; if (__builtin_expect((NAN_) != 0ull, 0)) { \
    _Pragma("unroll 1") for (unsigned spins_ = 0; spins_ < kSpinCap; ++spins_) { slip(X); LOAD; if (!any_empty(EMPTY)) break; } COMPUTE; } } while (0)
__device__ __forceinline__ unsigned now() { return (unsigned)__builtin_amdgcn_s_memrealtime(); }
__device__ __forceinline__ void until(unsigned t) {
    asm volatile("1:\n\ts_memrealtime vcc\n\ts_waitcnt lgkmcnt(0)\n\ts_sub_u32 vcc_lo, %0, vcc_lo\n\ts_cmp_lt_u32 vcc_lo, 0x400\n\ts_cbranch_scc1 1b" :: "s"(t - 1u) : "vcc", "scc", "memory");
}
__device__ __forceinline__ unsigned lds_word(unsigned a) {
    unsigned t_; asm volatile("ds_read_b32 %0, %1\n\ts_waitcnt lgkmcnt(0)\n\ts_nop 0" : "=v"(t_) : "v"(a) : "memory");
    return __builtin_amdgcn_readfirstlane(t_);
}
__device__ __forceinline__ void lds_set(unsigned a, unsigned v) { asm volatile("ds_write_b32 %0, %1" :: "v"(a), "v"(v) : "memory"); }
__device__ __forceinline__ int xrow(int lane) { return (lane >> 5) * 2 + ((lane >> 3) & 1); }
__device__ __forceinline__ int xcol(int lane) { return ((lane >> 4) & 1) * 8 + (lane & 7); }
__device__ __forceinline__ int swz(int r) { return r ^ (((r >> 3) & 1) << 2); }
__device__ __forceinline__ f32x2 unpk(unsigned w) { return (f32x2){lo16(w), hi16(w)}; }
__device__ __forceinline__ unsigned pk2v(f32x2 v) { return __builtin_bit_cast(unsigned, __builtin_convertvector(v, bf16x2)); }
__device__ __forceinline__ float frcp(float x) { return __builtin_amdgcn_rcpf(x); }
__device__ __forceinline__ float ex2(float x) { return __builtin_amdgcn_exp2f(x); }
__device__ __forceinline__ int lane_id() { int l; asm volatile("v_mbcnt_lo_u32_b32 %0, -1, 0\n\tv_mbcnt_hi_u32_b32 %0, -1, %0" : "=v"(l)); return l & 63; }
__device__ __forceinline__ f32x2 ln_stats(float ss, float s) {
    const float mean = s * (1.f / DM);
    const float var = fmax2(__builtin_fmaf(-mean, mean, ss * (1.f / DM)), 0.f);
    return (f32x2){bfr(mean), frcp(bfr(__builtin_amdgcn_sqrtf(bfr(bfr(var) + 1e-5f))))};
}
#define GATHER8(a, o, S) do { a[0] = ld16_sc1<-4 * (S)>(ws, o); a[1] = ld16_sc1<-3 * (S)>(ws, o); a[2] = ld16_sc1<-2 * (S)>(ws, o); a[3] = ld16_sc1<-(S)>(ws, o); \
    a[4] = ld16_sc1<0>(ws, o); a[5] = ld16_sc1<(S)>(ws, o); a[6] = ld16_sc1<2 * (S)>(ws, o); a[7] = ld16_sc1<3 * (S)>(ws, o); } while (0)
template <int OFF> __device__ __forceinline__ void again16(u32x4& r, const void* sbase, unsigned voff) {
    asm volatile("v_cmp_eq_u32 vcc, -1, %1\n\ts_mov_b64 exec, vcc\n\tglobal_load_dwordx4 %0, %2, %3 offset:%4 sc1\n\ts_mov_b64 exec, -1" : "+v"(r) : "v"(um4(r)), "v"(voff), "s"(sbase), "i"(OFF) : "memory", "vcc");
}
#define WAIT8(a, N) asm volatile("s_waitcnt vmcnt(%8)" : "+v"((a)[0]), "+v"((a)[1]), "+v"((a)[2]), "+v"((a)[3]), "+v"((a)[4]), "+v"((a)[5]), "+v"((a)[6]), "+v"((a)[7]) : "i"(N) : "memory")
#define SUM8(a) (((((((asf(a[0]) + asf(a[1])) + asf(a[2])) + asf(a[3])) + asf(a[4])) + asf(a[5])) + asf(a[6])) + asf(a[7]))
#define WAIT1(x, N) asm volatile("s_waitcnt vmcnt(%1)" : "+v"(x) : "i"(N) : "memory")
#define LO2(x) __builtin_shufflevector(x, x, 0, 1)
#define HI2(x) __builtin_shufflevector(x, x, 2, 3)
#define PIN1(a) asm volatile("" : "+v"(a))
#define PIN2(a, b) asm volatile("" : "+v"(a), "+v"(b))
#define PIN3(a, b, c) asm volatile("" : "+v"(a), "+v"(b), "+v"(c))
#define LANE_VARS() const int lane = lane_id(); \
    const int kq = lane >> 4, n = lane & 15, q4 = n >> 2, p4 = lane & 3; \
    const int r0s = 8 * kq + q4 + 4 * (kq & 1), r1s = 8 * kq + q4 + 4 * ((kq & 1) ^ 1);     \
    const int r2s = swz(4 * kq + q4);                                                       \
    (void)r0s; (void)r1s; (void)r2s; (void)p4
template <int V> struct IC { static constexpr int value = V; };
struct Frag80 { bf16x8 a0, a1; bf16x4 a2; };

__global__ void __launch_bounds__(512, 2) __attribute__((amdgpu_num_vgpr(32)))
mega_kernel(const u16*, const u16*, const u16*, const u16*, const u16*, const u16*, const u16*, const u16*, const u16*, u16*, unsigned char*
            ) {
    extern __shared__ __attribute__((aligned(16))) unsigned char smem[];
    const unsigned lb = (unsigned)(size_t)smem;
    const int wave = __builtin_amdgcn_readfirstlane(threadIdx.x >> 6);
    u64x8 ar0; u64x2 ar1; unsigned long long ar2, tp, tst64;
    asm volatile("s_load_dwordx16 %0, %5, 0\n\ts_load_dwordx4 %1, %5, 0x40\n\ts_load_dwordx2 %2, %5, 0x50\n\ts_load_dwordx2 %3, %5, %6\n\ts_memrealtime %4"
                 : "=&s"(ar0), "=&s"(ar1), "=&s"(ar2), "=&s"(tp), "=&s"(tst64) : "s"(__builtin_amdgcn_kernarg_segment_ptr()), "s"((0xa9876421u >> (4 * wave) & 15u) * 8u) : "memory");
    const int g = blockIdx.x, gj = GJ(g), gi = GI(g);
    const f32x4 z4 = {0.f, 0.f, 0.f, 0.f};
    asm volatile("" ::: "v255");
    auto when = [&](unsigned X) __attribute__((always_inline)) {
        u32x2 v;
        asm volatile("ds_read2_b32 %0, %1 offset0:14 offset1:5\n\ts_waitcnt lgkmcnt(0)" : "=v"(v) : "v"(lb + L_CNT) : "memory");
        return __builtin_amdgcn_readfirstlane(v[0]) + (X * __builtin_amdgcn_readfirstlane(v[1]) >> 12);
    };
    auto sched = [&](unsigned X, unsigned add) __attribute__((always_inline)) { until(when(X) + add); };
    auto slip = [&](unsigned X) __attribute__((always_inline)) {
        if (X) lds_set(lb + L_CNT + 56, now() - (X * lds_word(lb + L_CNT + 20) >> 12));
    };
    float xe = 0.f;

    auto code_pf = [&](int slice, int lane) __attribute__((always_inline)) {
        unsigned long long pc0; unsigned len, back, t;
        asm volatile("s_getpc_b64 %0\n.Lmega_pf%=:\n\ts_mov_b32 %1, .Lmega_code_end-.Lmega_code_begin\n\ts_mov_b32 %2, .Lmega_pf%=-.Lmega_code_begin" : "=s"(pc0), "=s"(len), "=s"(back));
        unsigned off = (unsigned)(slice * 4096 + lane * 64); if (off > len) off = len;
        asm volatile("global_load_dword %0, %1, %2" : "=v"(t) : "v"(off & ~3u), "s"(pc0 - back) : "memory");
        return t;
    };
    asm volatile(".Lmega_code_begin:");
    unsigned pf = 0u, pt1;
    const unsigned wofs = (unsigned)((((gj >> 1) * HQ + gi) * SK + (SK - WIN) + (gj & 1) * 512) * (DH * 2));
    {
        const int lane = threadIdx.x & 63, hi = lane >= 26;
        if (threadIdx.x < 24) lds_set(lb + L_CNT + threadIdx.x * 4, 0u);
        unsigned t2 = (unsigned)(gi * (DH * DM * 2)), t7 = (32u << 20) + (unsigned)(g * 128), to = (unsigned)(gj * 128), w2 = wofs;
        PINS(t2); PINS(t7); PINS(to); PINS(w2);
        to = wave == 7 ? t7 : to; to = wave == 2 ? t2 : to; to = wave < 2 ? w2 : to;
        if (wave == 7) pf = code_pf((g >> 3) & 15, lane);
        unsigned vo = (unsigned)((gj * 80 + swz(2 * wave) + hi) * (QD * 2) + gi * (DH * 2) + (lane - 26 * hi) * 16), d = lb + L_WQ + wave * 832, keep;
        unsigned xo = (0x11043210u >> (4 * wave) & 15u) * 1024 + (unsigned)(lane & (wave < 6 ? 63 : 15)) * 16, xd = lb + L_X0 + min(wave, 6) * 1024;
        PIN2(vo, xo); PINS(d); PINS(xd); PINS(to);
        __builtin_amdgcn_sched_barrier(0);
        asm volatile("s_waitcnt lgkmcnt(0)\n\ts_load_dword %0, %4, %6" : "=&s"(pt1), "+s"(ar0), "+s"(ar1), "+s"(ar2), "+s"(tp), "+s"(tst64) : "s"(to) : "memory");
        dma16((const void*)(wave < 5 ? ar0[0] : ar0[5]), xo, xd);
        asm volatile("s_mov_b32 %0, m0\n\ts_mov_b32 m0, %2\n\ts_mov_b64 exec, %4\n\tglobal_load_lds_dwordx4 %1, %3 nt\n\t"
                     ".rept 4\n\ts_add_u32 m0, m0, 6656\n\tv_add_u32 %1, 0x30000, %1\n\tglobal_load_lds_dwordx4 %1, %3 nt\n\t.endr\n\t"
                     "s_mov_b64 exec, -1\n\ts_mov_b32 m0, %0\n\ts_barrier"
                     : "=&s"(keep), "+v"(vo) : "s"(d), "s"(ar0[3]), "s"(0x0003fffffcffffffull) : "memory", "scc");
        __builtin_amdgcn_sched_barrier(0);
    }
    const u16 *const kc = (const u16*)ar0[1], *const vc = (const u16*)ar0[2], *const wo = (const u16*)ar0[4], *const wg = (const u16*)ar0[6], *const wu = (const u16*)ar0[7], *const wd = (const u16*)ar1[0];
    u16* const out = (u16*)ar1[1];
    unsigned char* const ws = (unsigned char*)((ar2 + (32u << 20) - 1) & ~(unsigned long long)((16u << 20) - 1));
    const unsigned tst = (unsigned)tst64;
    auto fix_k1 = [&](auto IC_, auto NC) __attribute__((always_inline)) {
        constexpr int A = RK + 8 * decltype(IC_)::value, B = A + 4;
        unsigned t0, t1, t2, t3;
        asm volatile("s_waitcnt vmcnt(%12)\n\tv_mov_b32 %0, v%c4\n\tv_mov_b32 %1, v%c5\n\tv_mov_b32 %2, v%c6\n\tv_mov_b32 %3, v%c7\n\t"
                     "v_mov_b32_dpp v%c4, v%c8 row_ror:8 row_mask:0xf bank_mask:0xc\n\tv_mov_b32_dpp v%c5, v%c9 row_ror:8 row_mask:0xf bank_mask:0xc\n\t"
                     "v_mov_b32_dpp v%c6, v%c10 row_ror:8 row_mask:0xf bank_mask:0xc\n\tv_mov_b32_dpp v%c7, v%c11 row_ror:8 row_mask:0xf bank_mask:0xc\n\t"
                     "v_mov_b32_dpp v%c8, %0 row_ror:8 row_mask:0xf bank_mask:0x3\n\tv_mov_b32_dpp v%c9, %1 row_ror:8 row_mask:0xf bank_mask:0x3\n\t"
                     "v_mov_b32_dpp v%c10, %2 row_ror:8 row_mask:0xf bank_mask:0x3\n\tv_mov_b32_dpp v%c11, %3 row_ror:8 row_mask:0xf bank_mask:0x3"
                     : "=&v"(t0), "=&v"(t1), "=&v"(t2), "=&v"(t3)
                     : "i"(A), "i"(A + 1), "i"(A + 2), "i"(A + 3), "i"(B), "i"(B + 1), "i"(B + 2), "i"(B + 3), "i"(decltype(NC)::value) : "memory");
    };
    auto trans_v = [&](auto UC) __attribute__((always_inline)) {
        constexpr int R = RV + 4 * decltype(UC)::value, P = 4 * decltype(UC)::value;
#define CELL(r, c) R + 16 * (r) + (c), R + 16 * (r) + 8 + (c)
        unsigned a0, a1, b0, b1, c0, c1;
        VMW(v_after(P)); zip_in<CELL(0, 0)>(); zip_out<CELL(0, 1)>(a0, a1); zip_out<CELL(0, 2)>(b0, b1); zip_out<CELL(0, 3)>(c0, c1);
        VMW(v_after(P + 1)); zip_x<CELL(1, 0), CELL(0, 1)>(a0, a1); zip_in<CELL(1, 1)>(); zip_out<CELL(1, 2)>(a0, a1);
        VMW(v_after(P + 2)); zip_x<CELL(2, 0), CELL(0, 2)>(b0, b1); zip_x<CELL(2, 1), CELL(1, 2)>(a0, a1); zip_in<CELL(2, 2)>(); zip_out<CELL(1, 3)>(a0, a1); zip_out<CELL(2, 3)>(b0, b1);
        VMW(v_after(P + 3)); zip_x<CELL(3, 0), CELL(0, 3)>(c0, c1); zip_x<CELL(3, 1), CELL(1, 3)>(a0, a1); zip_x<CELL(3, 2), CELL(2, 3)>(b0, b1); zip_in<CELL(3, 3)>();
#undef CELL
    };
    auto trans_w = [&](auto UC) __attribute__((always_inline)) {
        constexpr int R = RV + 64 + 2 * decltype(UC)::value, N = v_after(8 + decltype(UC)::value);
        unsigned a0, a1;
        VMW(N + 2); zip_in<R, R + 8>(); zip_out<R + 1, R + 9>(a0, a1);
        VMW(N); zip_x<R + 16, R + 24, R + 1, R + 9>(a0, a1); zip_in<R + 17, R + 25>();
    };
    auto frag80 = [&](unsigned im, int rs, int r0s, int r1s, int r2s) __attribute__((always_inline)) -> Frag80 {
        return Frag80{cat8(tr64(im + r0s * rs), tr64(im + r1s * rs)), cat8(tr64(im + (r0s + 32) * rs), tr64(im + (r1s + 32) * rs)), tr64(im + (r2s + 64) * rs)};
    };
    auto rows80 = [&](unsigned a16, unsigned a8) __attribute__((always_inline)) -> Frag80 { const Frag80 f{lds16(a16), lds16(a16 + 64), lds8(a8)}; __builtin_amdgcn_sched_barrier(0); return f; };
    auto empty5 = [&](int q0, unsigned S, int lane) __attribute__((always_inline)) {
        st16m(ws, tile35(gj, q0 + (lane >> 3)) + S + (unsigned)(gi * 128 + (lane & 7) * 16), (u32x4){~0u, ~0u, ~0u, ~0u}, 0xffffffffffull);
    };
    auto empty4 = [&](int layer, unsigned S, int lane) __attribute__((always_inline)) {
        const unsigned b = WS_H4 + (unsigned)((layer * 32 + gi) * 32768) + S;
        st16_sc1(ws, b + (unsigned)((lane >> 5) * 4096 + gj * 512 + (lane & 31) * 16), (u32x4){~0u, ~0u, ~0u, ~0u});
        st16m(ws, b + (unsigned)(lane < 32 ? 16384 + gj * 512 + lane * 16 : H4C + ((lane - 32) >> 3) * 1024 + gj * 128 + (lane & 7) * 16), (u32x4){~0u, ~0u, ~0u, ~0u},
              layer ? 0xff000000ffffffffull : ~0ull);
    };
    int lead = (int)((0x00003411u | LEAD << 28) >> (4 * wave) & 15u) * PACE * 4;
    auto pace = [&](unsigned n, bool wait = true) __attribute__((always_inline)) {
        unsigned u, w, t0;
        asm volatile("s_mov_b64 exec, 1\n\tds_add_rtn_u32 %0, %3, %4\n\tds_max_u32 %3, %5 offset:12\n\tds_add_rtn_u32 %1, %3, %6 offset:12\n\tds_read_b32 %2, %3 offset:4\n\ts_mov_b64 exec, -1\n\ts_waitcnt lgkmcnt(0)"
                     : "=&v"(u), "=&v"(w), "=&v"(t0) : "v"(lb + L_CNT + 60), "v"(n), "v"((now() - tst) << 6), "v"(n * PMIN * 4u) : "memory");
        int tn = (int)(__builtin_amdgcn_readfirstlane(t0) - tst + PINIT) * 64 + __builtin_amdgcn_readfirstlane(u) * (PACE * 4);
        const int tw = __builtin_amdgcn_readfirstlane(w) - PBURST * 64;
        if (tw > tn) tn = tw;
        tn -= lead;
        if (wait) {
#pragma unroll 1
            for (int k = 0; k < 2048 && (int)((now() - tst) << 6) < tn; ++k) {}
        }
    };
    auto slot = [&](unsigned u) __attribute__((always_inline)) {
        const unsigned tn = lds_word(lb + L_CNT + 64) + PINIT + u * PACE / 16;
#pragma unroll 1
        for (int k = 0; k < 8192 && (int)(now() - tn) < 0; ++k) __builtin_amdgcn_s_sleep(1);
    };
    auto load_wo = [&](int k, int lane, bool early) __attribute__((always_inline)) {
        const int c = k * 64 + lane, pp = c / 10, cc = c - 10 * pp;
        const unsigned f = (unsigned)(swz(pp) * 1280 + cc * 16);
        const u16* wb = wo + (size_t)gi * DH * DM + gj * 80;
#pragma unroll
        for (int a2 = 0; a2 < 6; ++a2) { if (a2 % 3 == 0) { if (early) pace(3); else slot(NUNIT - WOFF + (2 * k + a2 / 3) * WOC); } dma16(wb + 32 * a2 * DM, f, lb + L_WO + (5 * a2 + k) * 1024); }
    };
    auto load_wd = [&](int lane) __attribute__((always_inline)) {
        const u16* db = wd + gi * 48 * DM + gj * 80;
#pragma unroll 1
        for (int k = 0; k < 5; ++k) {
            const int c = k * 64 + lane, pp = c / 10, cc = c - 10 * pp;
            const unsigned fd = (unsigned)(swz(pp) * 1280 + cc * 16);
            if (!(k & 1)) pace(3);
            dma16(db, fd, lb + L_WD + k * 1024);
            if (k < 3) dma16(db + 32 * DM, (k < 2 || lane < 32) ? fd : 0u, lb + L_WD + (5 + k) * 1024);
        }
    };
    auto load_gu = [&](int m, int lane) __attribute__((always_inline)) {
#pragma unroll 1
        for (int k0 = 0; k0 < 3; ++k0) {
            const int k = (k0 + (g & 7)) % 3, c = k * 64 + lane, row = c / 6, cc = c - 6 * row;
            const unsigned o = (unsigned)((swz(row) * FF + cc * 8) * 2), d = lb + L_GU + m * 8192 + k * 1024;
            const u16* wb = (m == 0 ? wg : wu) + gj * 80 * FF + gi * 48;
            pace(3);
            dma16(wb, o, d); dma16(wb + 32 * FF, o, d + 3072);
            if (k < 2) dma16(wb + 64 * FF, (k == 0 || lane < 32) ? o : 0u, d + 6144);
        }
    };
    auto lds_add = [&](int off, unsigned v) __attribute__((always_inline)) { asm volatile("s_mov_b64 exec, 1\n\tds_add_u32 %0, %1\n\ts_mov_b64 exec, -1" :: "v"(lb + L_CNT + off), "v"(v) : "memory"); };
    auto send_flag = [&](int lane) __attribute__((always_inline)) {
        st4_sc1(ws, WS_MB + (unsigned)((gi * 8 + ((gj == (gi & 7) ? lane : gi) & 7)) * 128 + gj * 4), lds_word(lb + L_CNT + 68));
    };
    auto tiles4 = [&](const Frag80 (&f)[4], const Frag80& b, unsigned long long mk) __attribute__((always_inline)) -> u32x4 {
        f32x4 c[4], d[4];
#pragma unroll
        for (int i = 0; i < 4; ++i) c[i] = mfma32(f[i].a0, b.a0, z4);
#pragma unroll
        for (int i = 0; i < 4; ++i) { c[i] = mfma32(f[i].a1, b.a1, c[i]); d[i] = mfma16(f[i].a2, b.a2, z4); }
        __builtin_amdgcn_sched_barrier(0);
        f32x2 lo, hi;
        asm volatile("s_nop 1\n\ts_mov_b64 exec, %18\n\tv_pk_add_f32 %0, %2, %10\n\tv_pk_add_f32 %1, %3, %11\n\t"
                     "s_lshl_b64 exec, exec, 1\n\tv_pk_add_f32 %0, %4, %12\n\tv_pk_add_f32 %1, %5, %13\n\t"
                     "s_lshl_b64 exec, exec, 1\n\tv_pk_add_f32 %0, %6, %14\n\tv_pk_add_f32 %1, %7, %15\n\t"
                     "s_lshl_b64 exec, exec, 1\n\tv_pk_add_f32 %0, %8, %16\n\tv_pk_add_f32 %1, %9, %17\n\ts_mov_b64 exec, -1"
                     : "=&v"(lo), "=&v"(hi) : "v"(LO2(c[0])), "v"(HI2(c[0])), "v"(LO2(c[1])), "v"(HI2(c[1])), "v"(LO2(c[2])), "v"(HI2(c[2])), "v"(LO2(c[3])), "v"(HI2(c[3])),
                       "v"(LO2(d[0])), "v"(HI2(d[0])), "v"(LO2(d[1])), "v"(HI2(d[1])), "v"(LO2(d[2])), "v"(HI2(d[2])), "v"(LO2(d[3])), "v"(HI2(d[3])), "s"(mk) : "scc");
        return asu(__builtin_shufflevector(lo, hi, 0, 1, 2, 3));
    };
    auto empty1 = [&](int half, unsigned S, int lane) __attribute__((always_inline)) {
        const unsigned o = WS_H1 + S + (unsigned)((gi * 4 + 2 * half) * 16384 + gj * 896 + min(lane, 55) * 16);
        st16_sc1(ws, o, (u32x4){~0u, ~0u, ~0u, ~0u}); st16_sc1(ws, o + 16384, (u32x4){~0u, ~0u, ~0u, ~0u});
    };
    auto report = [&]() __attribute__((always_inline)) { if (gj == (gi & 7)) st4_sc1(ws, WS_R + (unsigned)(gi * 4), lds_word(lb + L_CNT + 88)); };
    auto answer = [&](int lane, unsigned r) __attribute__((always_inline)) {
        int d = min(max((int)(r - (tst << 11)) >> 11, -400), 600);
        unsigned k = (r >> 5) & 63u, v = (r >> 1) & 15u;
        v += dppu<0x124>(v); v += dppu<0x128>(v);
#define VOTES(l) (__builtin_amdgcn_readlane(v, l) + __builtin_amdgcn_readlane(v, 16 + l))
        const unsigned c = (VOTES(0) > 32 ? 2u : 0u) | (VOTES(1) > 32 ? 4u : 0u) | (VOTES(2) + VOTES(3) > 64 ? 8u : 0u);
        d += (int)dppu<0xB1>((unsigned)d); d += (int)dppu<0x4E>((unsigned)d); d += (int)dppu<0x141>((unsigned)d); d += (int)dppu<0x140>((unsigned)d);
        k = max(k, dppu<0xB1>(k)); k = max(k, dppu<0x4E>(k)); k = max(k, dppu<0x141>(k)); k = max(k, dppu<0x140>(k));
        d = __builtin_amdgcn_readlane(d, 0) + __builtin_amdgcn_readlane(d, 16);
        k = max((unsigned)__builtin_amdgcn_readlane(k, 0), (unsigned)__builtin_amdgcn_readlane(k, 16));
        st4_sc1(ws, WS_MB + (unsigned)(((lane & 31) * 8 + (lane & 7)) * 128 + 32), (tst + (d >> 5) + TOFF) << 10 | k << 4 | c | 1u);
        lds_set(lb + L_CNT + 36, 1u);
    };
    auto adopt = [&](unsigned t) __attribute__((always_inline)) {
        const int dt = (int)(t - (tst << 10)) >> 10;
        if (dt > -256 && dt < 512) lds_set(lb + L_CNT + 64, tst + dt);
        lds_set(lb + L_CNT + 20, 4096u + ((t >> 4) & 63u) * BETA);
        lds_set(lb + L_CNT + 80, lds_word(lb + L_CNT + 92) ^ t);
        lds_set(lb + L_CNT + 84, (((t >> 1) ^ lds_word(lb + L_CNT + 68)) & 4u) << 11);
        lds_set(lb + L_CNT + 52, 1u);
    };
    struct StatAddr { unsigned a16, b16, a8, b8, dst; };
    auto stats_addr = [&](int lane, unsigned dbase, int dstride) __attribute__((always_inline)) -> StatAddr {
        const int kq = lane >> 4, q4 = (lane >> 2) & 3, p4 = lane & 3;
        const int x1 = LANES(0x4444444444444444ull) ? L_WR + gj * 160 : L_XS + q4 * 160, xa = LANES(0xaaaaaaaaaaaaaaaaull) ? L_XS + 640 : x1;
        StatAddr r{lb + (unsigned)(xa + kq * 16), lb + (unsigned)(L_XS + p4 * 160 + kq * 16), lb + (unsigned)(xa + 128 + kq * 8), lb + (unsigned)(L_XS + p4 * 160 + 128 + kq * 8), dbase + (unsigned)(kq * dstride)};
        PIN3(r.a16, r.b16, r.dst); PIN2(r.a8, r.b8);
        return r;
    };
    auto stats = [&](const StatAddr& r) __attribute__((always_inline)) {
        const bf16x8 a0 = lds16(r.a16), b0 = lds16(r.b16), a1 = lds16(r.a16 + 64), b1 = lds16(r.b16 + 64);
        const bf16x4 a2 = lds8(r.a8), b2 = lds8(r.b8);
        unsigned long long mk = 0x0008000400020001ull; PINS(mk);
        __builtin_amdgcn_sched_barrier(0);
        st16m(ws, r.dst, asu(mfma32(a1, b1, mfma32(a0, b0, z4)) + mfma16(a2, b2, z4)), mk);
    };
    unsigned qa, pw, ma, idx, qm;
    auto qk_addr = [&](bool L1) __attribute__((always_inline)) {
        LANE_VARS();
        qa = lb + L_Q + (unsigned)(kq * 32 + (lane & 2) * 8);
        pw = lb + L_P + (unsigned)((7 + 8 * wave) * 192 + (kq >> 1) * 64 + (n >> 3) * 32 + (lane & 2) * 8 + (q4 & 1) * 8 + (kq & 1) * 4 + (lane & 1) * 2);
        ma = lb + L_M + (unsigned)(wave * 4);
        idx = (lane & 2) ? 0xeeeeu : 0x4444u;
        qm = lb + L_Q;
        PIN3(qa, pw, ma); PIN2(idx, qm);
        if (L1 && wave < 4) __builtin_amdgcn_s_setprio(2);
    };
    auto att_qk0 = [&]() __attribute__((always_inline)) {
        qk_addr(false);
#pragma unroll 1
        for (unsigned spins = 0; spins < (kSpinCap << 4); ++spins) {
            if (lds_word(lb + L_CNT + 4) != 0u) break;
            __builtin_amdgcn_s_sleep(1);
        }
        STAMP(3);
        const u32x4 q0 = *(const LAS u32x4*)(size_t)qa, q1 = *(const LAS u32x4*)(size_t)(qa + 128), q2 = *(const LAS u32x4*)(size_t)(qa + 256);
        f32x4 c0 = z4, c1 = z4, c2 = z4, c3 = z4;
#define SMF(c, q, r) "v_smfmac_f32_16x16x64_bf16 %" #c ", %" #q ", v[" #r "], %7\n\t"
        asm volatile("s_nop 1\n\t" SMF(0, 4, 64:71) SMF(1, 4, 88:95) SMF(2, 4, 112:119) SMF(3, 4, 136:143) SMF(0, 5, 72:79) SMF(1, 5, 96:103) SMF(2, 5, 120:127) SMF(3, 5, 144:151)
                     SMF(0, 6, 80:87) SMF(1, 6, 104:111) SMF(2, 6, 128:135) SMF(3, 6, 152:159) "s_nop 7"
                     : "+v"(c0), "+v"(c1), "+v"(c2), "+v"(c3) : "v"(q0), "v"(q1), "v"(q2), "v"(idx));
#undef SMF
        const float s0 = c0[0] + c0[2], s1 = c1[0] + c1[2], s2 = c2[0] + c2[2], s3 = c3[0] + c3[2];
        float m = fmax2(fmax3(s0, s1, s2), s3);
        m = fmax2(m, dpp<0xB1>(m)); m = fmax2(m, dpp<0x4E>(m)); m = fmax2(m, dpp<0x124>(m)); m = fmax2(m, dpp<0x128>(m));
        const float s01 = LANES(0xffff0000ffff0000ull) ? s1 : s0, s23 = LANES(0xffff0000ffff0000ull) ? s3 : s2;
        const float sv = LANES(0xffffffff00000000ull) ? s23 : s01;
        m = bfr(bfr(m) * SCALE);
        const float p = ex2((bfr(bfr(sv) * SCALE) - m) * LOG2E);
        *(LAS u16*)(size_t)pw = (u16)pk2(p, p);
        *(LAS float*)(size_t)ma = m;
        __builtin_amdgcn_sched_barrier(0);
    };
    unsigned pr, oa, oc, cma, cxa, cra, cso;
    auto att_addr = [&]() __attribute__((always_inline)) {
        LANE_VARS();
        pr = lb + L_P + (unsigned)((7 + 8 * wave - 2 * q4 - (lane & 1)) * 192 + kq * 32 + (lane & 2) * 8);
        oa = lb + L_OP + (unsigned)(wave * 1024 + (lane & 7) * 64 + kq * 16 + (n >> 3) * 8);
        oc = lb + L_OP + (unsigned)(wave * 1024 + 512 + (kq >> 1) * 256 + (lane & 7) * 32 + (kq & 1) * 16 + (n >> 3) * 8);
        PIN3(pr, oa, oc);
    };
    auto comb_out = [&](float u0, float u1, unsigned so, unsigned long long mk, bool both) __attribute__((always_inline)) {
        const unsigned w = min(pk2(u0, u1), 0xfffffffeu);
        if (!both) { st4_sc1(ws, so, w); return; }
        const u32x4 w4 = {dppu<0x00>(w), dppu<0x55>(w), dppu<0xAA>(w), dppu<0xFF>(w)};
        st16m(ws, so, w4, mk);
    };
    auto max8 = [&](f32x4 m0, f32x4 m1) __attribute__((always_inline)) { return fmax3(fmax3(m0[0], m0[1], m0[2]), fmax3(m0[3], m1[0], m1[1]), fmax2(m1[2], m1[3])) * LOG2E; };
    auto comb_o0 = [&](f32x4 m0, f32x4 m1, f32x4 mm, f32x4 o0, f32x4 o1, f32x4 o2, f32x4 o3, unsigned so, unsigned long long mk, bool both) __attribute__((always_inline)) {
        const float M = max8(m0, m1);
        f32x4 f;
#pragma unroll
        for (int k = 0; k < 4; ++k) f[k] = ex2(__builtin_fmaf(mm[k], LOG2E, -M));
        const f32x4 o = f[3] * o3 + (f[2] * o2 + (f[1] * o1 + f[0] * o0));
        comb_out(rs32(o[0], o[2]), rs32(o[1], o[3]), so, mk, both);
    };
    auto comb_o1 = [&](f32x4 m0, f32x4 m1, f32x2 mm, f32x4 o0, f32x4 o1, unsigned so, unsigned long long mk, bool both) __attribute__((always_inline)) {
        const float M = max8(m0, m1);
        const f32x4 o = ex2(__builtin_fmaf(mm[1], LOG2E, -M)) * o1 + ex2(__builtin_fmaf(mm[0], LOG2E, -M)) * o0;
        comb_out(x16_add(rs32(o[0], o[2])), x16_add(rs32(o[1], o[3])), so, mk, both);
    };
    auto ml_addr = [&](int lane, unsigned& xa, unsigned& ra) __attribute__((always_inline)) {
        xa = lb + L_M + (unsigned)(((lane & 15) >> 1) * 4); ra = lb + L_P + (unsigned)((7 + 8 * ((lane & 15) >> 1)) * 192 + (lane & 1) * 64 + (lane >> 4) * 16);
    };
    auto comb_ml = [&](unsigned ma, unsigned xa, unsigned ra, unsigned so, bool both) __attribute__((always_inline)) {
        const u32x4 x4 = *(const LAS u32x4*)(size_t)ra;
        const f32x4 m0 = *(const LAS f32x4*)(size_t)ma, m1 = *(const LAS f32x4*)(size_t)(ma + 16);
        const float mw = *(const LAS float*)(size_t)xa;
        __builtin_amdgcn_sched_barrier(0);
        const f32x4 sp = mfma32u((u32x4){0x3f803f80u, 0x3f803f80u, 0x3f803f80u, 0x3f803f80u}, x4, z4);
        __builtin_amdgcn_sched_barrier(0);
        const float M = max8(m0, m1);
        float L = row16_sum(sp[0] * ex2(__builtin_fmaf(mw, LOG2E, -M)));
        if (both) L = dpp<0x00>(L);
        st16m(ws, so, asu((f32x4){M, L, M, L}), both ? 3ull : 1ull);
    };
    auto att_pv0 = [&]() __attribute__((always_inline)) {
        u32x4 bm[8];
        trans_v(IC<0>{});
        if (v_after(9) > v_after(7)) { trans_w(IC<0>{}); trans_w(IC<1>{}); }
        trans_v(IC<1>{});
#pragma unroll
        for (int e = 0; e < 8; ++e) bm[e] = *(const LAS u32x4*)(size_t)(pr + e * 192);
        __builtin_amdgcn_sched_barrier(0);
        f32x4 da = z4, dc = z4;
        PIN2(da, dc);
        asm volatile("s_nop 1");
        __builtin_amdgcn_sched_barrier(0);
        f32x2 r;
        smf<RV>(da, bm[0], idx); smf<RV + 8>(da, bm[1], idx); smf<RV + 16>(da, bm[2], idx); smf<RV + 24>(da, bm[3], idx);
        smf<RV + 32>(da, bm[4], idx); smf<RV + 40>(da, bm[5], idx); smf<RV + 48>(da, bm[6], idx); smf<RV + 56>(da, bm[7], idx);
        asm volatile("s_nop 7\n\tv_pk_add_f32 %0, %1, %2\n\tds_write_b64 %3, %0\n\ts_mov_b64 exec, 1\n\tds_add_u32 %4, %5\n\ts_mov_b64 exec, -1"
                     : "=&v"(r) : "v"(LO2(da)), "v"(HI2(da)), "v"(oa), "v"(lb + L_CNT + 24), "v"(1u) : "memory");
        GUARD(900);
        MARK(b_t5);
        if (v_after(9) < v_after(7)) { trans_w(IC<0>{}); trans_w(IC<1>{}); }
        trans_w(IC<2>{}); trans_w(IC<3>{});
        STAMP(4);
        asm volatile("s_nop 1\n\tv_smfmac_f32_16x16x64_bf16 %0, %1, v[224:231], %5\n\tv_smfmac_f32_16x16x64_bf16 %0, %2, v[232:239], %5\n\t"
                     "v_smfmac_f32_16x16x64_bf16 %0, %3, v[240:247], %5\n\tv_smfmac_f32_16x16x64_bf16 %0, %4, v[248:255], %5\n\ts_nop 6"
                     : "+v"(dc) : "v"(bm[0]), "v"(bm[1]), "v"(bm[2]), "v"(bm[3]), "v"(idx));
        asm volatile("s_nop 0\n\tv_pk_add_f32 %0, %1, %2\n\tds_write_b64 %3, %0" : "=&v"(r) : "v"(LO2(dc)), "v"(HI2(dc)), "v"(oc) : "memory");
        STAMP(20);
    };
    unsigned long long ck1 = 0xffff0000ffff0000ull, ck2 = 0xffffffff00000000ull, csc = 0x3d93cd3a3d93cd3aull;
    auto att1 = [&]() __attribute__((always_inline)) {
        constexpr int layer = 1;
        {
#define SMF(c, q, r) "v_smfmac_f32_16x16x64_bf16 v[" #c "], v[" #q "], v[" #r "], %0\n\t"
#define MAXD(x) "v_max_f32_dpp v41, v41, v41 " x " row_mask:0xf bank_mask:0xf\n\t"
            asm volatile("s_waitcnt lgkmcnt(0)\n\ts_barrier\n\t"
                         "ds_read_b128 v[0:3], %1\n\tds_read2_b32 v[12:13], %2 offset0:96 offset1:100\n\tds_read_b128 v[4:7], %1 offset:128\n\tds_read_b128 v[8:11], %1 offset:256\n\t"
                         "v_mov_b64 v[16:17], 0\n\tv_mov_b64 v[18:19], 0\n\tv_mov_b64 v[20:21], 0\n\tv_mov_b64 v[22:23], 0\n\tv_mov_b64 v[24:25], 0\n\tv_mov_b64 v[26:27], 0\n\t"
                         "v_mov_b64 v[28:29], 0\n\tv_mov_b64 v[30:31], 0\n\tv_mov_b64 v[32:33], 0\n\tv_mov_b64 v[34:35], 0\n\tv_mov_b64 v[36:37], 0\n\tv_mov_b64 v[38:39], 0\n\t"
                         "s_waitcnt lgkmcnt(3)\n\t" SMF(16:19, 0:3, 64:71) SMF(20:23, 0:3, 88:95) "s_waitcnt lgkmcnt(1)\n\tv_mul_f32 v13, 0x3acccccd, v13\n\t"
                         SMF(24:27, 0:3, 112:119) "v_mul_f32 v12, 0x3acccccd, v12\n\t" SMF(28:31, 0:3, 136:143) "v_fma_f32 v12, -v13, v13, v12\n\t"
                         SMF(16:19, 4:7, 72:79) "v_maximum3_f32 v12, v12, 0, 0\n\t" SMF(20:23, 4:7, 96:103) "v_cvt_pk_bf16_f32 v12, 0, v12\n\t"
                         SMF(24:27, 4:7, 120:127) "v_add_f32 v12, 0x3727c5ac, v12\n\t" SMF(28:31, 4:7, 144:151) "v_cvt_pk_bf16_f32 v12, 0, v12\n\ts_waitcnt lgkmcnt(0)\n\t"
                         SMF(16:19, 8:11, 80:87) "v_sqrt_f32 v12, v12\n\t" SMF(20:23, 8:11, 104:111) "v_cvt_pk_bf16_f32 v12, 0, v12\n\t"
                         SMF(24:27, 8:11, 128:135) "v_rcp_f32 v12, v12\n\t" SMF(28:31, 8:11, 152:159)
                         "s_nop 2\n\tv_add_f32 v42, v16, v18\n\ts_nop 0\n\tv_add_f32 v43, v20, v22\n\tv_max_f32 v41, v42, v43\n\tv_add_f32 v44, v24, v26\n\tv_add_f32 v45, v28, v30\n\t"
                         "v_max3_f32 v41, v41, v44, v45\n\tv_cndmask_b32 v42, v42, v43, %8\n\tv_cndmask_b32 v44, v44, v45, %8\n\t"
                         MAXD("quad_perm:[1,0,3,2]") "v_cndmask_b32 v40, v42, v44, %9\n\ts_nop 0\n\t" MAXD("quad_perm:[2,3,0,1]") "s_nop 1\n\t" MAXD("row_ror:4") "s_nop 1\n\t" MAXD("row_ror:8")
                         "v_pk_mul_f32 v[40:41], v[12:13], v[40:41] op_sel_hi:[0,1]\n\tv_cvt_pk_bf16_f32 v40, 0, v40\n\tv_cvt_pk_bf16_f32 v41, 0, v41\n\t"
                         "v_pk_mul_f32 v[40:41], %10, v[40:41]\n\tv_cvt_pk_bf16_f32 v40, 0, v40\n\tv_cvt_pk_bf16_f32 v41, 0, v41\n\t"
                         "v_sub_f32 v40, v40, v41\n\tv_mul_f32 v40, 0x3fb8aa3b, v40\n\tv_exp_f32 v40, v40\n\ts_nop 0\n\tv_cvt_pk_bf16_f32 v40, v40, v40\n\tds_write_b16 %3, v40\n\t"
                         "ds_read_b128 v[0:3], %4\n\tds_read_b128 v[4:7], %4 offset:192\n\tds_read_b128 v[8:11], %4 offset:384\n\tds_read_b128 v[12:15], %4 offset:576\n\t"
                         "ds_read_b128 v[16:19], %4 offset:768\n\tds_read_b128 v[20:23], %4 offset:960\n\tds_read_b128 v[24:27], %4 offset:1152\n\tds_read_b128 v[28:31], %4 offset:1344\n\t"
                         "ds_write_b32 %5, v41\n\t"
                         "s_waitcnt lgkmcnt(8)\n\t" SMF(32:35, 0:3, 160:167) SMF(36:39, 0:3, 224:231) "s_waitcnt lgkmcnt(7)\n\t" SMF(32:35, 4:7, 168:175) SMF(36:39, 4:7, 232:239)
                         "s_waitcnt lgkmcnt(6)\n\t" SMF(32:35, 8:11, 176:183) SMF(36:39, 8:11, 240:247) "s_waitcnt lgkmcnt(4)\n\t" SMF(32:35, 12:15, 184:191) SMF(36:39, 12:15, 248:255)
                         SMF(32:35, 16:19, 192:199) "s_waitcnt lgkmcnt(1)\n\t" SMF(32:35, 20:23, 200:207) SMF(32:35, 24:27, 208:215) SMF(32:35, 28:31, 216:223)
                         "s_nop 2\n\tv_pk_add_f32 v[36:37], v[36:37], v[38:39]\n\tds_write_b64 %7, v[36:37]\n\ts_nop 2\n\tv_pk_add_f32 v[32:33], v[32:33], v[34:35]\n\tds_write_b64 %6, v[32:33]"
                         :: "v"(idx), "v"(qa), "v"(qm), "v"(pw), "v"(pr), "v"(ma), "v"(oa), "v"(oc), "s"(ck1), "s"(ck2), "s"(csc)
                         : "memory", "v0", "v1", "v2", "v3", "v4", "v5", "v6", "v7", "v8", "v9", "v10", "v11", "v12", "v13", "v14", "v15", "v16", "v17", "v18", "v19", "v20", "v21", "v22", "v23",
                           "v24", "v25", "v26", "v27", "v28", "v29", "v30", "v31", "v32", "v33", "v34", "v35", "v36", "v37", "v38", "v39", "v40", "v41", "v42", "v43", "v44", "v45");
#undef SMF
#undef MAXD
            STAMP(20 + 32 * layer);
            BAR();
            if (__builtin_expect(wave == 0, 1)) {
                STAMP(5 + 32 * layer);
                comb_o0(*(const LAS f32x4*)(size_t)cma, *(const LAS f32x4*)(size_t)(cma + 16), *(const LAS f32x4*)(size_t)cxa, *(const LAS f32x4*)(size_t)cra,
                        *(const LAS f32x4*)(size_t)(cra + 1024), *(const LAS f32x4*)(size_t)(cra + 2048), *(const LAS f32x4*)(size_t)(cra + 3072), cso, 0x1111111111111111ull, false);
            }
            __builtin_amdgcn_sched_barrier(0);
            if (__builtin_expect(wave == 1, 1))
                comb_o1(*(const LAS f32x4*)(size_t)cma, *(const LAS f32x4*)(size_t)(cma + 16), *(const LAS f32x2*)(size_t)cxa, *(const LAS f32x4*)(size_t)cra, *(const LAS f32x4*)(size_t)(cra + 1024), cso, 0x0000111100001111ull, false);
            __builtin_amdgcn_sched_barrier(0);
            if (__builtin_expect(wave == 2, 1)) comb_ml(cma, cxa, cra, cso, false);
            MARK(e_q);
            if (wave < 3) STAMP(21 + 32 * layer);
            __builtin_amdgcn_s_setprio(0);
        }
    };

    unsigned nj = 0u, qS = 0u, fS = 0u, rS = 0u, nS = 0u; int ord;
    {
        const int tid = threadIdx.x, lane = tid & 63;
        asm volatile("ds_max_u32 %0, %1" :: "v"(lb + L_CNT + 64), "v"(tst) : "memory");
        unsigned mb = 0u;
        const u16* kbase = kc + wofs / 2;
        const u16* vbase = vc + wofs / 2;
        unsigned pt0 = 0u;
        if (wave == 7) {
            __builtin_amdgcn_s_setprio(3);
            if (lane < 10) *reinterpret_cast<u32x4*>(smem + L_XS + 640 + lane * 16) = (u32x4){0x3f803f80u, 0x3f803f80u, 0x3f803f80u, 0x3f803f80u};
            if (lane < 16) { *reinterpret_cast<u32x4*>(smem + L_CS + 768 + lane * 16) = (u32x4){0u, 0u, 0u, 0u}; }
        } else {
            if (wave >= 4) {
                if (wave == 4) st16_sc1(ws, WS_OWN + (unsigned)(gi * 16384 + (lane >> 5) * 8192 + gj * 512 + (lane & 31) * 16), (u32x4){~0u, ~0u, ~0u, ~0u});
                else empty5(0, (unsigned)(wave - 5) * 8192u, lane);
                if (wave == 6) {
                    __builtin_amdgcn_s_setprio(3);
                    if (gi + gj == 0) st4_sc1(ws, WS_R + (lane & 31) * 4, 0u);
                    st4_sc1(ws, WS_MB + (unsigned)((gi * 8 + gj) * 128 + (lane & 31) * 4), 0u);
                }
            } else if (wave >= 2) {
                unsigned long long c0, c1;
                asm volatile("s_waitcnt lgkmcnt(0)\n\ts_memtime %0\n\ts_waitcnt lgkmcnt(0)\n\ts_load_dword %2, %3, %4\n\ts_waitcnt lgkmcnt(0)\n\ts_memtime %1\n\ts_waitcnt lgkmcnt(0)"
                             : "=&s"(c0), "=&s"(c1), "=&s"(pt0) : "s"(ws), "s"((unsigned)(min(gi & 3, 2) << 21) + ((((tst + g * 977u) * 2654435761u) >> 11 & 0x1fdf80u) | ((wave ^ gj) & 1) * 8192)) : "memory");
                pt0 = ((wave ^ gj) & 1) ? (unsigned)c0 - (unsigned)c1 : (unsigned)c1 - (unsigned)c0;
            }
            STAMP(36);
            *reinterpret_cast<u32x4*>(smem + L_P + tid * 16) = (u32x4){0u, 0u, 0u, 0u};
            if (tid < 72 * 12 - 448) *reinterpret_cast<u32x4*>(smem + L_P + 7168 + tid * 16) = (u32x4){0u, 0u, 0u, 0u};
            if (wave >= 4) {
                if (wave == 6) VMW(2); else VMW(1);
                asm volatile("ds_max_u32 %0, %1" :: "v"(lb + L_CNT + 64), "v"(now() - TLAND) : "memory");
                STAMP(40);
                lds_add(0, 1u);
            } else if (wave >= 2) { lds_add(20, pt0); VMW(0); lds_add(0, 1u); }
        }
        asm volatile("s_waitcnt lgkmcnt(0)" :: "s"(pt1) : "memory");
        const int rot = gj + wave;
        const unsigned r0 = now(), c0 = (unsigned)__builtin_amdgcn_s_memtime();
        u32x4 a[4];
        unsigned ko = (unsigned)((wave * 64 + (lane & 7)) * 384 + (((lane >> 3) & 1) * 4 + (lane >> 4)) * 16);
        PIN1(ko);
        auto k_group = [&](auto GC_) __attribute__((always_inline)) {
            constexpr int G = decltype(GC_)::value, R = RK + 24 * (G >> 1) + 4 * (G & 1);
            const unsigned o = ko + (unsigned)((((G >> 1) + rot) & 3) * 6144 + (G & 1) * 3072);
            pace(3);
            ld16_nt<R, 0>(kbase, o); ld16_nt<R + 8, 128>(kbase, o); ld16_nt<R + 16, 256>(kbase, o);
            const int lane = lane_id();
            const unsigned ho = WS_Q0 + (unsigned)((gi * 4 + (gj >> 1)) * 4096 + lane * 16);
            if (wave < 7) {
                asm volatile("; COND_BEGIN");
                if (G == 0 && wave >= 4) { VMW(3); lds_add(32, 1u); }
                if (G == 0) {
                    if (wave < 2) { VMW(3); lds_add(0, 1u); load_gu(wave, lane); }
                    if (wave == 2) load_wd(lane);
                    if (wave == 3) {
#pragma unroll 1
                        for (int k = 5 - WEARLY; k < 5; ++k) load_wo(k, lane, true);
                    }
                }
                asm volatile("; COND_END");
            }
            if (wave == 7) {
                asm volatile("; COND_BEGIN");
                if (G == GP) {
                    VMW(3 * (GP + 1));
#pragma unroll 1
                    for (unsigned spins = 0; spins < (kSpinCap << 4); ++spins) {
                        if (lds_word(lb + L_CNT) >= 7u) break;
                        __builtin_amdgcn_s_sleep(1);
                    }
                    STAMP(26);
                    const unsigned char* p = smem + L_X0 + (lane >> 4) * 1280;
                    const float c0 = lo16(*reinterpret_cast<const u16*>(p));
                    const f32x2 c2 = {c0, c0};
                    f32x2 s2 = {0.f, 0.f}, q2 = s2;
#pragma unroll
                    for (int k = 0; k < 5; ++k) {
                        const u32x4 w = *reinterpret_cast<const u32x4*>(p + k * 256 + (lane & 15) * 16);
#pragma unroll
                        for (int e = 0; e < 4; ++e) { const f32x2 d = unpk(w[e]) - c2; s2 += d; q2 += d * d; }
                    }
                    const float s = row16_sum(s2[0] + s2[1]), qq = row16_sum(q2[0] + q2[1]);
                    const float ms = s * (1.f / DM);
                    const unsigned mv = pk2(c0 + ms, fmax2(qq - s * ms, 0.f) * (1.f / DM));
                    const float mb = lo16(mv), rden = frcp(bfr(__builtin_amdgcn_sqrtf(bfr(hi16(mv) + 1e-5f))));
                    const f32x2 mb2 = {mb, mb}, rd2 = {rden, rden};
                    if ((lane & 15) < 10) {
                        u32x4 w = *reinterpret_cast<const u32x4*>(p + gj * 160 + (lane & 15) * 16);
#pragma unroll
                        for (int e = 0; e < 4; ++e) w[e] = pk2v(unpk(pk2v(unpk(w[e]) - mb2)) * rd2);
                        *reinterpret_cast<u32x4*>(smem + L_XS + (lane >> 4) * 160 + (lane & 15) * 16) = w;
                    }
                    STAMP(30);
                }
                if (G == GP || G == GA) {
#pragma unroll 1
                    for (int blk = 0; blk < 3; ++blk) {
                        LANE_VARS();
                        const unsigned xb = lb + L_XS + (unsigned)((G == GP ? q4 : 4) * 160);
                        Frag80 f[4];
#pragma unroll
                        for (int i = 0; i < 4; ++i) f[i] = frag80(lb + L_WQ + (unsigned)(128 * blk + 32 * i + 8 * p4), 416, r0s, r1s, r2s);
                        const Frag80 b = rows80(xb + kq * 16, xb + 128 + kq * 8);
                        const u32x4 c = tiles4(f, b, 0x1111111111111111ull);
                        if (G == GP) *reinterpret_cast<u32x4*>(smem + L_OP + q4 * 768 + 256 * blk + p4 * 64 + kq * 16) = c;
                        else *reinterpret_cast<u32x4*>(smem + L_CS + 256 * blk + kq * 64 + p4 * 16) = c;
                    }
                    if (G == GP) {
                        asm volatile("" ::: "memory");
                        const int ln = lane_id();
                        f32x2 c[6];
                        unsigned m = 20u;
#pragma unroll
                        for (int k = 0; k < 6; ++k) {
                            c[k] = *reinterpret_cast<const f32x2*>(smem + L_OP + (k / 3) * 1536 + ln * 24 + (k % 3) * 8);
                            m = max(m, max(fexp(c[k][0]), fexp(c[k][1])));
                        }
                        m = max(m, dppu<0xB1>(m)); m = max(m, dppu<0x4E>(m)); m = max(m, dppu<0x141>(m)); m = max(m, dppu<0x140>(m));
                        m = max(max((unsigned)__builtin_amdgcn_readlane(m, 0), (unsigned)__builtin_amdgcn_readlane(m, 16)), max((unsigned)__builtin_amdgcn_readlane(m, 32), (unsigned)__builtin_amdgcn_readlane(m, 48)));
                        lds_set(lb + L_CNT + 76, m);
                        const float f = __uint_as_float((273u - m) << 23);
#pragma unroll
                        for (int k = 0; k < 2; ++k) {
                            const u32x2 w0 = enc21(c[3 * k][0] * f, c[3 * k][1] * f, c[3 * k + 1][0] * f), w1 = enc21(c[3 * k + 1][1] * f, c[3 * k + 2][0] * f, c[3 * k + 2][1] * f);
                            st16_sc1(ws, WS_Q0 + (unsigned)((gi * 4 + 2 * k + (ln >> 5)) * 4096 + gj * 512 + (ln & 31) * 16), (u32x4){w0[0], w0[1], w1[0], w1[1]});
                        }
                        STAMP(2);
                        WAIT1(pf, 3 * (GP + 1) + 2);
                    }
                }
                if (G == GA) {
                    const unsigned half = __builtin_amdgcn_s_getreg(2 << 6 | 20);
                    lds_set(lb + L_CNT + 68, lds_word(lb + L_CNT + 64) << 11 | lds_word(lb + L_CNT + 76) << 3 | 1u | (((int)lds_word(lb + L_CNT + 20) > 0) ^ half) << 1 | half << 2);
                    VMW(3 * (GA - GP));
#pragma unroll 1
                    for (unsigned spins = 0; spins < (kSpinCap << 4); ++spins) if (lds_word(lb + L_CNT + 32) >= 3u) break;
                    send_flag(lane_id());
                    lds_set(lb + L_CNT + 8, 1u);
                    STAMP(17);
                    f32x2 r2 = {0.f, 0.f};
#pragma unroll
                    for (int k = 0; k < 5; ++k) r2 += unpk(*reinterpret_cast<const unsigned*>(smem + L_WR + k * 256 + lane_id() * 4));
                    *reinterpret_cast<float*>(smem + L_CNT + 12) = wave_sum(r2[0] + r2[1]);
                }
                asm volatile("; COND_END");
            }
            if (wave == 6) {
                asm volatile("; COND_BEGIN");
                if (G == GB - 1) mb = ld4_sc1(ws, WS_MB + (unsigned)((gi * 8 + gj) * 128 + (gi & 7) * 4));
                if (G == GB) {
                    WAIT1(mb, 3);
#pragma unroll 1
                    for (unsigned spins = 0; spins < kSpinCap && !all_set(mb); ++spins) {
                        if ((spins & 7u) == 7u && lds_word(lb + L_CNT + 8)) send_flag(lane);
                        mb = ld4_sc1(ws, WS_MB + (unsigned)((gi * 8 + gj) * 128 + (gi & 7) * 4));
                        WAIT1(mb, 0);
                    }
                    mb = ld4_sc1(ws, WS_MB + (unsigned)((gi * 8 + (gi & 7)) * 128 + (lane & 7) * 4));
                }
                if (G == GC) {
                    WAIT1(mb, 3 * (GC - GB));
#pragma unroll 1
                    for (unsigned spins = 0; spins < kSpinCap && !all_set(mb); ++spins) {
                        if ((spins & 7u) == 7u && lds_word(lb + L_CNT + 8)) send_flag(lane);
                        mb = ld4_sc1(ws, WS_MB + (unsigned)((gi * 8 + (gi & 7)) * 128 + (lane & 7) * 4));
                        WAIT1(mb, 0);
                    }
                    lds_set(lb + L_CNT + 92, (__builtin_amdgcn_readlane(mb, gi & 7) & 4u) ? 14u : 0u);
                    int d = min(max((int)(mb - (tst << 11)) >> 11, -400), 600);
                    d += (int)dppu<0xB1>((unsigned)d); d += (int)dppu<0x4E>((unsigned)d); d += (int)dppu<0x141>((unsigned)d);
                    const unsigned tm = tst + (unsigned)(__builtin_amdgcn_readfirstlane(d) >> 3);
                    lds_set(lb + L_CNT + 64, tm);
                    const float slow = (float)(now() - r0) * 24.f * 256.f * frcp((float)((unsigned)__builtin_amdgcn_s_memtime() - c0)) - 256.f;
                    lds_set(lb + L_CNT + 88, tm << 11 | (unsigned)__builtin_amdgcn_fmed3f(slow, 0.f, 63.f) << 5 | __builtin_popcount((unsigned)__builtin_amdgcn_ballot_w64((mb & 2u) != 0u) & 255u) << 1 | 1u);
                    report();
                    a[0] = ld16_sc1<0>(ws, ho); a[1] = ld16_sc1<1024>(ws, ho); a[2] = ld16_sc1<2048>(ws, ho); a[3] = ld16_sc1<3072>(ws, ho);
                    pace(XCOST, false);
                    STAMP(31);
                }
                asm volatile("; COND_END");
            } else if (G == GC) { for (int k = 0; k < 4; ++k) asm volatile("" : "=v"(a[k])); }
        };
        k_group(IC<0>{}); k_group(IC<1>{}); k_group(IC<2>{}); k_group(IC<3>{}); k_group(IC<4>{}); k_group(IC<5>{}); k_group(IC<6>{}); k_group(IC<7>{});
        {
            const int lane = lane_id();
            const unsigned v0 = (unsigned)((wave * 64 + 4 * (lane >> 4)) * 384 + (lane & 15) * 16);
            const unsigned v1 = v0 + 256 - (unsigned)((lane & 15) * 8);
            auto v_group = [&](auto HC) __attribute__((always_inline)) {
                constexpr int h = decltype(HC)::value, p = kVord[h], R = p < 8 ? RV + 16 * (p & 3) + 4 * (p >> 2) : RV + 64 + 2 * (p - 8), NV = v_upto(h);
                const unsigned o = (p < 8 ? v0 : v1) + (unsigned)((((p < 8 ? p >> 1 : p) + rot) & 3) * 6144);
                pace(2);
                if (p < 8) { ld16_nt<R, (p & 1) * 768>(vbase, o); ld16_nt<R + 8, (p & 1) * 768 + 384>(vbase, o); }
                else { ld8_nt<R, 0>(vbase, o); ld8_nt<R + 8, 384>(vbase, o); ld8_nt<R + 16, 768>(vbase, o); ld8_nt<R + 24, 1152>(vbase, o); }
                if (wave == 7 && gi + gj == 0) {
                    asm volatile("; COND_BEGIN");
                    if (h == 3 || h == 6) mb = ld4_sc1(ws, WS_R + (unsigned)((lane & 31) * 4));
                    if (h == 4 || h == 7) {
                        WAIT1(mb, NV - v_upto(h - 1));
                        if (all_set(mb) && !lds_word(lb + L_CNT + 36)) answer(lane, mb);
                    }
                    asm volatile("; COND_END");
                }
                if (wave == 6) {
                    asm volatile("; COND_BEGIN");
                if (h == HD) {
                    asm volatile("s_waitcnt vmcnt(%4)" : "+v"(a[0]), "+v"(a[1]), "+v"(a[2]), "+v"(a[3]) : "i"(3 * (7 - GC) + NV) : "memory");
                    float sq[6] = {0.f, 0.f, 0.f, 0.f, 0.f, 0.f};
#pragma unroll
                    for (int k = 0; k < 4; ++k) {
                        const float f = __uint_as_float(((((unsigned)__builtin_amdgcn_ds_bpermute((2 * k + (lane >> 5)) * 4, (int)mb) >> 3) & 255u) - 19u) << 23);
#pragma unroll
                        for (int e = 0; e < 2; ++e) {
                            const unsigned lo = a[k][2 * e], hi = a[k][2 * e + 1];
                            const float fe = (int)hi < 0 ? __builtin_nanf("") : f;
                            sq[3 * e] += (float)((int)(lo << 11) >> 11) * fe;
                            sq[3 * e + 1] += (float)((int)(__builtin_amdgcn_alignbit(hi, lo, 21) << 11) >> 11) * fe;
                            sq[3 * e + 2] += (float)((int)(hi << 1) >> 11) * fe;
                        }
                    }
#pragma unroll
                    for (int e = 0; e < 3; ++e) {
                        const int pi = 3 * (lane & 31) + e;
                        *reinterpret_cast<unsigned*>(smem + L_Q + (pi >> 3) * 32 + (pi & 6) * 2 + (pi & 1) * 16) = pk2(x32_add(sq[2 * e]), x32_add(sq[2 * e + 1]));
                    }
                    asm volatile("s_waitcnt lgkmcnt(0)\n\tds_write_b32 %0, %1" :: "v"(lb + L_CNT + 4), "v"(1u) : "memory");
                    STAMP(27);
                    lead = LEAD * PACE * 4;
                }
                    if (h == 6) mb = ld4_sc1(ws, WS_MB + (unsigned)((gi * 8 + (gi & 7)) * 128 + 32));
                    if (h == 8) {
                        WAIT1(mb, NV - v_upto(6));
                        const unsigned r = __builtin_amdgcn_readfirstlane(mb);
                        if (r) { adopt(r); STAMP(29); }
                    }
                    asm volatile("; COND_END");
                }
                if constexpr (h < 6) {
                    constexpr int i = 2 * h;
                    fix_k1(IC<i>{}, IC<20 - 6 * (i / 3) - i % 3 + NV>{});
                    fix_k1(IC<i + 1>{}, IC<20 - 6 * ((i + 1) / 3) - (i + 1) % 3 + NV>{});
                }
                if (h == 5) {
                    STAMP(1);
                    __builtin_amdgcn_s_setprio(0);
                    att_qk0();
                }
            };
            v_group(IC<0>{}); v_group(IC<1>{}); v_group(IC<2>{}); v_group(IC<3>{}); v_group(IC<4>{}); v_group(IC<5>{});
            v_group(IC<6>{}); v_group(IC<7>{}); v_group(IC<8>{}); v_group(IC<9>{}); v_group(IC<10>{}); v_group(IC<11>{});
            lds_add(16, 1u);
            if (wave == 3) lds_add(44, WEARLY);
        }
    }
    {
        STAMP(28);
        att_addr();
        const int lane = lane_id();
        cma = lb + L_M; cxa = lb + L_M + (unsigned)((lane >> 4) * 8); cra = lb + L_OP + (unsigned)(512 + (lane >> 4) * 2048 + (lane & 15) * 16);
        cso = WS_OWN + (unsigned)(gi * 16384 + gj * 512 + 256 + (lane >> 5) * 64 + (lane & 12) * 4 + (lane & 1) * 8192);
        PIN2(cma, cxa); PIN2(cra, cso);
        att_pv0();
    }
    {
        f32x4 m0, m1, o0, o1; f32x2 mm; unsigned r, taken;
        asm volatile("s_mov_b64 exec, 1\n\tds_add_rtn_u32 %5, %7, %8 offset:%11\n\ts_mov_b64 exec, -1\n\t"
                     "ds_read_b128 %0, %7\n\tds_read_b128 %1, %7 offset:16\n\tds_read_b64 %2, %9\n\tds_read_b128 %3, %10\n\tds_read_b128 %4, %10 offset:1024\n\t"
                     "ds_read_b32 %6, %7 offset:%12\n\ts_waitcnt lgkmcnt(0)"
                     : "=&v"(m0), "=&v"(m1), "=&v"(mm), "=&v"(o0), "=&v"(o1), "=&v"(r), "=&v"(taken)
                     : "v"(cma), "v"(1u), "v"(cxa), "v"(cra), "i"(L_CNT + 28 - L_M), "i"(L_CNT + 40 - L_M) : "memory");
        ord = __builtin_amdgcn_readfirstlane(r);
        if (ord == 7) { STAMP(5); comb_o1(m0, m1, mm, o0, o1, cso, 0x0000333300003333ull, true); lds_set(lb + L_CNT + 48, now() | 1u); STAMP(21); }
        MARK(e_t5);
        if (ord <= OML + 1 || (unsigned)__builtin_amdgcn_readfirstlane(taken) < 5u - WEARLY) {
        const int lane = lane_id();
        auto wait_cnt = [&](int off, unsigned v) __attribute__((always_inline)) {
#pragma unroll 1
            for (unsigned spins = 0; spins < (kSpinCap << 4); ++spins) { if (lds_word(lb + L_CNT + off) >= v) break; __builtin_amdgcn_s_sleep(1); }
        };
        if (ord == OML) {
            wait_cnt(16, 8u);
            unsigned xa, ra; ml_addr(lane, xa, ra);
            comb_ml(cma, xa, ra, WS_OWN + (unsigned)(gi * 16384 + gj * 512 + 384 + (lane & 1) * 8192), true);
            STAMP(24);
        } else if (ord == 0 && !lds_word(lb + L_CNT + 52)) {
            if (gi + gj == 0 && !lds_word(lb + L_CNT + 36)) {
                unsigned r = 0u;
#pragma unroll 1
                for (unsigned spins = 0; spins < kSpinCap; ++spins) {
                    r = ld4_sc1(ws, WS_R + (unsigned)((lane & 31) * 4));
                    WAIT1(r, 0);
                    if (all_set(r)) break;
                    if ((spins & 7u) == 7u) send_flag(lane);
                }
                answer(lane, r);
            }
#pragma unroll 1
            for (unsigned spins = 0; spins < kSpinCap; ++spins) {
                unsigned r = ld4_sc1(ws, WS_MB + (unsigned)((gi * 8 + (gi & 7)) * 128 + 32));
                WAIT1(r, 0);
                r = __builtin_amdgcn_readfirstlane(r);
                if (r) { adopt(r); break; }
                if ((spins & 7u) == 7u) { send_flag(lane); report(); }
                __builtin_amdgcn_s_sleep(16);
            }
            STAMP(29);
        } else if (ord == OML + 1) {
#pragma unroll 1
            for (unsigned spins = 0; spins < (kSpinCap << 6); ++spins) if (lds_word(lb + L_CNT + 24) >= 8u) break;
            const unsigned ra = lb + L_OP + (unsigned)((lane >> 5) * 4096 + (lane & 31) * 16);
            comb_o0(*(const LAS f32x4*)(size_t)cma, *(const LAS f32x4*)(size_t)(cma + 16), *(const LAS f32x4*)(size_t)(cma + (lane >> 5) * 16), *(const LAS f32x4*)(size_t)ra, *(const LAS f32x4*)(size_t)(ra + 1024),
                    *(const LAS f32x4*)(size_t)(ra + 2048), *(const LAS f32x4*)(size_t)(ra + 3072), WS_OWN + (unsigned)(gi * 16384 + gj * 512 + (lane & 60) * 4 + (lane & 1) * 8192), 0x3333333333333333ull, true);
            STAMP(22);
        }
        if (ord == OML) {
#pragma unroll 1
            for (unsigned spins = 0; spins < (kSpinCap << 8); ++spins) { if (lds_word(lb + L_CNT + 52)) break; __builtin_amdgcn_s_sleep(1); }
            const unsigned S = (lds_word(lb + L_CNT + 80) & 4u) << 11;
            empty4(0, S, lane); empty5(5, 0u, lane); empty5(5, 8192u, lane);
        }
#pragma unroll 1
        for (;;) {
            unsigned j;
            asm volatile("s_mov_b64 exec, 1\n\tds_add_rtn_u32 %0, %1, %2\n\ts_mov_b64 exec, -1\n\ts_waitcnt lgkmcnt(0)" : "=&v"(j) : "v"(lb + L_CNT + 40), "v"(1u) : "memory");
            j = __builtin_amdgcn_readfirstlane(j);
            if (j >= 5u - WEARLY) break;
            load_wo((int)j, lane, false); ++nj;
        }
        }
        STAMP(23);
    }

    unsigned omr, owa, owb;
    auto op_addr = [&](int layer) __attribute__((always_inline)) {
        LANE_VARS();
        const int t = wave - 3;
        cma = WS_OWN + rS + (unsigned)(gi * 16384 + layer * 4096 + (lane >> 3) * 512 + t * 128 + (t == 3 ? 0 : (lane & 7) * 16));
        cxa = lb + L_O2 + (unsigned)((lane >> 3) * 416 + t * 128 + (t == 3 ? 0 : (lane & 7) * 16));
        const int pb = (lane & 4) ? 3 - p4 : p4, jp = 2 * pb + ((lane >> 2) & 1);
        cso = tile35(gj, 10 * layer + t) + (unsigned)((kq & 1) * 8192 + gi * 128 + (kq >> 1) * 64 + p4 * 16);
        cra = lb + L_O2 + (unsigned)(jp * 416 + kq * 16); omr = lb + L_O2 + (unsigned)(jp * 416 + 384);
        owa = lb + L_WO + (unsigned)(r0s * 160 + 32 * t + 8 * p4); owb = lb + L_WO + (unsigned)(r1s * 160 + 32 * t + 8 * p4);
        if (layer) PIN2(cma, cxa);
        PIN2(cso, cra); PIN3(omr, owa, owb);
    };
    auto chain = [&](auto LC) __attribute__((always_inline)) {
        constexpr int layer = decltype(LC)::value;
        unsigned h4 = WS_H4 + (unsigned)((layer * 32 + gi) * 32768) + (layer ? fS : 0u);
        auto sides = [&]() __attribute__((always_inline)) {
            const unsigned m = lds_word(lb + L_CNT + 80);
            qS = (m & 2u) << 12; fS = (m & 4u) << 11; rS = (m & 8u) << 10; nS = lds_word(lb + L_CNT + 84); h4 += fS;
        };
        if (wave < 3) {
            LANE_VARS();
            if (layer == 0) {
                unsigned lo = WS_OWN + lds_word(lb + L_CNT + 84) + (unsigned)(gi * 16384 + (lane >> 3) * 512), lw = lb + L_O2 + (unsigned)((lane >> 3) * 416);
                unsigned lo0 = lo + (unsigned)((lane & 7) * 16), lw0 = lw + (unsigned)((lane & 7) * 16);
                PIN2(lo0, lo); PIN2(lw0, lw);
                GUARD(400);
                MARK(b_g2);
                if (__builtin_expect(wave == 2, 0)) { if (nj | (ord == OML)) { VMW(0); lds_add(44, nj); } }
                else if (__builtin_expect(wave == 1, 0)) {
#pragma unroll 1
                    for (unsigned spins = 0; spins < (kSpinCap << 4); ++spins) if (lds_word(lb + L_CNT + 48)) break;
#pragma unroll 1
                    for (unsigned spins = 0; spins < kSpinCap; ++spins) {
                        u32x4 o0 = ld16_sc1<0>(ws, lo0), o1 = ld16_sc1<128>(ws, lo0), o2 = ld16_sc1<384>(ws, lo);
                        if (nj) { VMW(3); lds_add(44, nj); nj = 0u; }
                        asm volatile("s_waitcnt vmcnt(0)" : "+v"(o0), "+v"(o1), "+v"(o2) :: "memory");
                        *(LAS u32x4*)(size_t)lw0 = o0; *(LAS u32x4*)(size_t)(lw0 + 128) = o1; *(LAS u32x4*)(size_t)(lw + 384) = o2;
                        if (!any_empty(max(max(um4(o0), um4(o1)), um4(o2)))) break;
                    }
                } else {
                    unsigned long long miss; unsigned cnt = kSpinCap << 4, tf, nv = nj;
                    asm volatile("1:\n\tds_read_b32 v4, %4 offset:48\n\ts_sub_u32 %1, %1, 1\n\ts_cbranch_scc1 5f\n\ts_waitcnt lgkmcnt(0)\n\tv_readfirstlane_b32 %2, v4\n\ts_cmp_eq_u32 %2, 0\n\ts_cbranch_scc1 1b\n\t"
                                 "s_add_u32 %2, %2, %8\n"
                                 "2:\n\ts_memrealtime vcc\n\ts_waitcnt lgkmcnt(0)\n\ts_sub_u32 vcc_lo, %2, vcc_lo\n\ts_cmp_lt_u32 vcc_lo, 0x400\n\ts_cbranch_scc1 2b\n"
                                 "5:\n\ts_mov_b32 %1, 15\n"
                                 "3:\n\tglobal_load_dwordx4 v[0:3], %5, %6 offset:256 sc1\n\ts_waitcnt vmcnt(1)\n\ts_mov_b64 exec, 1\n\tds_add_u32 %4, %3 offset:44\n\ts_mov_b64 exec, -1\n\tv_mov_b32 %3, 0\n\t"
                                 "s_sub_u32 %1, %1, 1\n\ts_cselect_b64 vcc, 0, -1\n\ts_waitcnt vmcnt(0)\n\tds_write_b128 %7, v[0:3] offset:256\n\tv_max3_u32 v4, v0, v1, v2\n\tv_max_u32 v4, v4, v3\n\t"
                                 "v_cmp_eq_u32 %0, -1, v4\n\ts_and_b64 vcc, vcc, %0\n\ts_cbranch_scc1 3b"
                                 : "=&s"(miss), "+s"(cnt), "=&s"(tf), "+v"(nv) : "v"(lb + L_CNT), "v"(lo0), "s"(ws), "v"(lw0), "i"(DG2 - 1) : "memory", "vcc", "scc", "v0", "v1", "v2", "v3", "v4");
                    if (__builtin_expect(miss != 0ull, 0)) {
#pragma unroll 1
                        for (unsigned spins = 0; spins < kSpinCap; ++spins) {
                            if ((spins & 15u) == 0u) send_flag(lane);
                            u32x4 o0 = ld16_sc1<256>(ws, lo0);
                            WAIT1(o0, 0);
                            *(LAS u32x4*)(size_t)(lw0 + 256) = o0;
                            if (!any_empty(um4(o0))) break;
                        }
                    }
                }
                MARK(e_g2);
                STAMP(25);
                BAR();
                sides();
                if (wave < 2) empty1(wave, qS, lane);
                else {
                    st16m(ws, WS_CS + (unsigned)((gi * 8 + gj) * 768 + lane * 16), *reinterpret_cast<const u32x4*>(smem + L_CS + lane * 16), 0xffffffffffffull);
                    empty5(10, 0u, lane); empty5(10, 8192u, lane);
                }
            } else BAR();
            {
                const unsigned im = lb + L_GU + (unsigned)(32 * wave + 8 * p4);
                Frag80 fg = frag80(im, 96, r0s, r1s, r2s), fu = frag80(im + 8192, 96, r0s, r1s, r2s);
                const unsigned xb = lb + L_XS + (unsigned)((n < 4 ? n : 4) * 160);
                unsigned b16 = xb + kq * 16, b8 = xb + 128 + kq * 8;
                unsigned sg = h4 + (unsigned)(n < 4 ? h4s(wave) + gj * 512 + kq * 64 + n * 16 : H4C + wave * 1024 + gj * 128 + kq * 16), su = sg + (n < 4 ? 256u : 64u);
                unsigned o = h4 + h4s(wave) + (unsigned)(lane * 16);
                unsigned oc = h4 + (unsigned)(H4C + wave * 1024 + lane * 16), pc = (unsigned)((kq * 16 + q4) * 4);
                unsigned os = h4 + (unsigned)(H4T + (q4 + 4 * (kq & 1)) * 128 + p4 * 16);
                unsigned ha = lb + L_H + (unsigned)(p4 * 96 + wave * 32 + q4 * 8 + kq * 2);
                f32x2 cs = {0.f, 0.f};
                if (layer != 0) cs = *reinterpret_cast<const f32x2*>(smem + L_CS2 + wave * 512 + lane * 8);
                PIN3(fg.a0, fg.a1, fg.a2); PIN3(fu.a0, fu.a1, fu.a2); PIN3(b16, b8, sg); PIN3(su, o, os); PIN2(ha, cs);
                unsigned long long mk = layer ? 0x000f000f000f000full : 0x001f001f001f001full;
                unsigned ta = WS_MB + (unsigned)((lane >> 1) * 1024 + 64 + (lane & 1) * 16);
                if (layer == 0) PIN3(oc, ta, pc);
                unsigned x4a = layer ? 0u : X4A * lds_word(lb + L_CNT + 20) >> 12;
                unsigned long long w0 = __builtin_amdgcn_ballot_w64(wave == 0);
                PINS(x4a); PINS(w0);
                unsigned long long rdm = 0x3acccccd3acccccdull;
                PINS(mk); PINS(rdm);
                if (layer == 0) VMW(0);
                if (layer == 0) GUARD(1200); else GUARD(800);
                MARK(b_gu);
                BAR();
                STAMP(10 + 32 * layer);
                u32x4 tl = {0u, 0u, 0u, 0u};
                if (layer == 0) asm volatile("s_mov_b64 exec, %3\n\tglobal_load_dwordx4 %0, %1, %2 sc1\n\ts_mov_b64 exec, -1" : "+v"(tl) : "v"(ta), "s"(ws), "s"(w0) : "memory");
                const Frag80 b = rows80(b16, b8);
                const f32x4 g0 = mfma32(fg.a0, b.a0, z4), u0 = mfma32(fu.a0, b.a0, z4), g1 = mfma32(fg.a1, b.a1, g0), g2 = mfma16(fg.a2, b.a2, z4), u1 = mfma32(fu.a1, b.a1, u0), u2 = mfma16(fu.a2, b.a2, z4);
                __builtin_amdgcn_sched_barrier(0);
                const f32x4 cg = g1 + g2, cu = u1 + u2;
                st16m2<0>(ws, sg, asu(cg), su, asu(cu), mk);
                STAMP(11 + 32 * layer);
                if (layer == 0 && wave != 0) {
                    unsigned tr = 0u;
#pragma unroll 1
                    for (unsigned spins = 0; spins < (kSpinCap << 6); ++spins) { tr = lds_word(lb + L_CNT + 56); if (tr) break; }
                    until(tr + x4a);
                } else if (layer == 0) {
                    WAIT1(tl, 2);
#pragma unroll 1
                    for (unsigned spins = 0; spins < kSpinCap; ++spins) {
                        const unsigned long long miss = __builtin_amdgcn_ballot_w64(min(min(tl[0], tl[1]), min(tl[2], tl[3])) == 0u);
                        if (!miss) break;
                        asm volatile("s_mov_b64 exec, %3\n\tglobal_load_dwordx4 %0, %1, %2 sc1\n\ts_mov_b64 exec, -1\n\ts_waitcnt vmcnt(0)" : "+v"(tl) : "v"(ta), "s"(ws), "s"(miss) : "memory");
                    }
                    const unsigned tn = now();
                    unsigned d = min(min(min(tn - tl[0], tn - tl[1]), min(tn - tl[2], tn - tl[3])), 800u);
                    d = min(d, dppu<0xB1>(d)); d = min(d, dppu<0x4E>(d)); d = min(d, dppu<0x124>(d)); d = min(d, dppu<0x128>(d));
                    unsigned tr = tn - min(min((unsigned)__builtin_amdgcn_readlane(d, 0), (unsigned)__builtin_amdgcn_readlane(d, 16)), min((unsigned)__builtin_amdgcn_readlane(d, 32), (unsigned)__builtin_amdgcn_readlane(d, 48)));
                    lds_set(lb + L_CNT + 56, tr | 1u);
                    until(__builtin_amdgcn_readfirstlane(tr | 1u) + x4a);
                } else sched(X4, 0);
                STAMP(16 + 32 * layer);
                float hv;
                u32x4 a0, a1, a2, a3, ac, as;
                auto load = [&]() __attribute__((always_inline)) {
                    as = ld16_sc1(ws, os);
                    if (layer == 0) ac = ld16_sc1(ws, oc);
                    a0 = ld16_sc1<0>(ws, o); a1 = ld16_sc1<1024>(ws, o); a2 = ld16_sc1<2048>(ws, o); a3 = ld16_sc1<3072>(ws, o);
                    if (layer == 0) lds_add(32, 1u);
                };
                auto all_in = [&]() __attribute__((always_inline)) {
                    if (layer == 0) asm volatile("s_waitcnt vmcnt(0)" : "+v"(a0), "+v"(a1), "+v"(a2), "+v"(a3), "+v"(ac), "+v"(as) :: "memory");
                    else { asm volatile("s_waitcnt vmcnt(0)" : "+v"(a0), "+v"(a1), "+v"(a2), "+v"(a3), "+v"(as) :: "memory"); ac = as; }
                };
                auto compute = [&]() __attribute__((always_inline)) {
#define ROR(n) " row_ror:" #n " row_mask:0xf bank_mask:0xf\n\t"
                    asm volatile("s_waitcnt vmcnt(%16)\n\tv_add_f32_dpp v40, %2, %2" ROR(8) "v_add_f32_dpp v41, %3, %3" ROR(8) "s_nop 0\n\tv_add_f32_dpp v40, v40, v40" ROR(4) "v_add_f32_dpp v41, v41, v41" ROR(4)
                                 "v_mov_b32 v42, v40\n\tv_mov_b32 v43, v41\n\ts_nop 0\n\tv_permlane16_swap_b32 v40, v42\n\tv_permlane16_swap_b32 v41, v43\n\t"
                                 "v_pk_add_f32 v[40:41], v[40:41], v[42:43]\n\tv_pk_mul_f32 v[40:41], v[40:41], %14\n\t"
                                 ".if %17 == 0\n\ts_waitcnt vmcnt(4)\n\tv_mov_b64 v[44:45], %12\n\tv_mov_b64 v[46:47], %13\n\t.endif\n\t"
                                 "v_fma_f32 v40, -v41, v41, v40\n\tv_cvt_pk_bf16_f32 v42, 0, v41\n\tv_maximum3_f32 v40, v40, 0, 0\n\t"
                                 ".if %17 == 0\n\tv_permlane32_swap_b32 v44, v46\n\tv_permlane32_swap_b32 v45, v47\n\tv_pk_add_f32 v[44:45], v[44:45], v[46:47]\n\t.endif\n\t"
                                 "v_cvt_pk_bf16_f32 v40, 0, v40\n\ts_waitcnt vmcnt(2)\n\tv_pk_add_f32 v[32:33], %4, %6\n\tv_add_f32 v40, 0x3727c5ac, v40\n\tv_pk_add_f32 v[34:35], %5, %7\n\t"
                                 ".if %17 == 0\n\tv_permlane16_swap_b32 v44, v45\n\tv_add_f32 v44, v44, v45\n\t.endif\n\t"
                                 "v_cvt_pk_bf16_f32 v40, 0, v40\n\ts_waitcnt vmcnt(0)\n\tv_pk_add_f32 v[36:37], %8, %10\n\tv_sqrt_f32 v40, v40\n\tv_pk_add_f32 v[38:39], %9, %11\n\t"
                                 ".if %17 == 0\n\tv_add_f32_dpp v44, v44, v44" ROR(8) "ds_bpermute_b32 v46, %19, v44\n\tds_bpermute_b32 v47, %19, v44 offset:16\n\t.endif\n\t"
                                 "v_pk_add_f32 v[32:33], v[32:33], v[36:37]\n\tv_pk_add_f32 v[34:35], v[34:35], v[38:39]\n\tv_cvt_pk_bf16_f32 v40, 0, v40\n\t"
                                 "s_nop 0\n\t"
                                 "v_permlane32_swap_b32 v32, v34\n\tv_permlane32_swap_b32 v33, v35\n\tv_rcp_f32 v40, v40\n\tv_pk_add_f32 v[32:33], v[32:33], v[34:35]\n\ts_nop 1\n\t"
                                 "v_permlane16_swap_b32 v32, v33\n\t.if %17 == 0\n\ts_waitcnt lgkmcnt(0)\n\tv_mov_b64 %1, v[46:47]\n\t.endif\n\t"
                                 "v_pk_fma_f32 v[32:33], %1, v[42:43], v[32:33] op_sel_hi:[1,0,1] neg_lo:[1,0,0] neg_hi:[1,0,0]\n\tv_pk_mul_f32 v[32:33], v[32:33], v[40:41] op_sel_hi:[1,0]\n\t"
                                 "v_cvt_pk_bf16_f32 v32, 0, v32\n\tv_mul_f32 v34, 0xbfb8aa3b, v32\n\tv_exp_f32 v34, v34\n\tv_cvt_pk_bf16_f32 v33, 0, v33\n\tv_add_f32 v34, 1.0, v34\n\t"
                                 "v_rcp_f32 v34, v34\n\ts_nop 0\n\tv_mul_f32 v32, v34, v32\n\tv_cvt_pk_bf16_f32 v32, 0, v32\n\tv_mul_f32 %0, v33, v32\n\tv_cvt_pk_bf16_f32 v32, %0, %0\n\tds_write_b16 %15, v32"
                                 : "=&v"(hv), "+v"(cs)
                                 : "v"(as[0]), "v"(as[1]), "v"(LO2(a0)), "v"(HI2(a0)), "v"(LO2(a1)), "v"(HI2(a1)), "v"(LO2(a2)), "v"(HI2(a2)), "v"(LO2(a3)), "v"(HI2(a3)),
                                   "v"(LO2(layer ? as : ac)), "v"(HI2(layer ? as : ac)), "s"(rdm), "v"(ha), "i"(layer ? 4 : 5), "i"(layer), "v"(as), "v"(layer ? ha : pc)
                                 : "memory", "v32", "v33", "v34", "v35", "v36", "v37", "v38", "v39", "v40", "v41", "v42", "v43", "v44", "v45", "v46", "v47");
#undef ROR
                };
                RETRY(load(), (all_in(), max(max(max(um4(a0), um4(a1)), max(um4(a2), um4(a3))), max(um4(ac), um4(as)))), compute(), any_nan(hv, hv), layer ? X4 : X4A);
                BAR();
                MARK(e_gu);
                STAMP(12 + 32 * layer);
                if (layer == 0) {
                    *reinterpret_cast<f32x2*>(smem + L_CS2 + wave * 512 + lane * 8) = cs;
                }
            }
            if (layer == 0) {
                if (wave == 1) {
                    const unsigned o = WS_CS + (unsigned)(gi * 6144 + 3072 + (lane < 48 ? lane : 0) * 16);
                    u32x4 a[8];
                    GATHER8(a, o, 768);
                    WAIT8(a, 0);
                    if (lane < 48) *reinterpret_cast<f32x4*>(smem + L_CS + lane * 16) = SUM8(a);
                }
                Frag80 f[4];
#pragma unroll
                for (int i = 0; i < 4; ++i) { f[i] = frag80(lb + L_WQ + (unsigned)(128 * wave + 32 * i + 8 * p4), 416, r0s, r1s, r2s); PIN3(f[i].a0, f[i].a1, f[i].a2); }
                unsigned b16 = lb + L_XS + (unsigned)(q4 * 160 + kq * 16), b8 = lb + L_XS + (unsigned)(q4 * 160 + 128 + kq * 8);
                unsigned so = WS_H1 + qS + (unsigned)((gi * 4 + q4) * 16384 + gj * 896 + 256 * wave + kq * 64 + p4 * 16);
                unsigned long long mk = 0x1111111111111111ull;
                PIN3(b16, b8, so); PINS(mk);
                GUARD(3800);
                MARK(b_q);
                BAR();
                STAMP(33);
                const Frag80 b = rows80(b16, b8);
                st16_sc1(ws, so, tiles4(f, b, mk));
                STAMP(34);
                qk_addr(true); att_addr();
                unsigned x0a = lb + L_M + (unsigned)((lane >> 5) * 16), x1a = lb + L_M + (unsigned)((lane >> 4) * 8), x2a, r2a;
                unsigned r0a = lb + L_OP + (unsigned)((lane >> 5) * 4096 + (lane & 31) * 16), r1a = lb + L_OP + (unsigned)(512 + (lane >> 4) * 2048 + (lane & 15) * 16);
                unsigned s0a = (unsigned)(lane * 4), s1a = (unsigned)(256 + (lane >> 5) * 64 + (lane & 15) * 4);
                ml_addr(lane, x2a, r2a);
                PIN3(x0a, x1a, x2a); PIN3(r0a, r1a, r2a); PIN2(s0a, s1a);
                cma = lb + L_M; cxa = wave == 0 ? x0a : wave == 1 ? x1a : x2a; cra = wave == 0 ? r0a : wave == 1 ? r1a : r2a;
                cso = WS_OWN + rS + (unsigned)(gi * 16384 + 4096 + gj * 512) + (wave == 0 ? s0a : wave == 1 ? s1a : 384u);
                PIN2(cma, cxa); PIN2(cra, cso);
                asm volatile("" : "=v"(omr), "=v"(owa), "=v"(owb));
            }
        } else {
            const int t = wave - 3;
            StatAddr sa;
            u32x4 sel;
            unsigned long long hi = 0xff00ff00ff00ff00ull;
            auto gather32 = [&](unsigned o, unsigned X, auto fin) __attribute__((always_inline)) {
                u32x4 a0 = ld16_sc1<-2048>(ws, o), a1 = ld16_sc1<-1024>(ws, o), a2 = ld16_sc1<0>(ws, o), a3 = ld16_sc1<1024>(ws, o);
                float v;
                auto sum = [&]() __attribute__((always_inline)) {
                    asm volatile("s_waitcnt vmcnt(3)\n\tv_mfma_f32_16x16x32_bf16 v[40:43], %1, %5, 0\n\ts_waitcnt vmcnt(2)\n\tv_mfma_f32_16x16x32_bf16 v[40:43], %2, %5, v[40:43]\n\t"
                                 "s_waitcnt vmcnt(1)\n\tv_mfma_f32_16x16x32_bf16 v[40:43], %3, %5, v[40:43]\n\ts_waitcnt vmcnt(0)\n\tv_mfma_f32_16x16x32_bf16 v[40:43], %4, %5, v[40:43]\n\t"
                                 "s_nop 7\n\tv_cndmask_b32 v40, v40, v41, %6\n\tv_cndmask_b32 v42, v42, v43, %6\n\ts_nop 1\n\tv_permlane32_swap_b32 v40, v42\n\tv_add_f32 v40, v40, v42\n\t"
                                 "v_cvt_pk_bf16_f32 %0, 0, v40"
                                 : "=&v"(v), "+v"(a0), "+v"(a1), "+v"(a2), "+v"(a3) : "v"(sel), "s"(hi) : "memory", "v40", "v41", "v42", "v43");
                    fin(v);
                    __builtin_amdgcn_sched_barrier(0);
                };
                sum();
                if (__builtin_expect(any_nan(v, v) != 0ull, 0)) {
#pragma unroll 1
                    for (unsigned spins = 0; spins < kSpinCap; ++spins) {
                        slip(X);
                        again16<-2048>(a0, ws, o); again16<-1024>(a1, ws, o); again16<0>(a2, ws, o); again16<1024>(a3, ws, o);
                        asm volatile("s_waitcnt vmcnt(0)" : "+v"(a0), "+v"(a1), "+v"(a2), "+v"(a3) :: "memory");
                        if (!any_empty(max(max(um4(a0), um4(a1)), max(um4(a2), um4(a3))))) break;
                    }
                    sum();
                }
            };
            auto poll32 = [&](unsigned o, unsigned t0) __attribute__((always_inline)) {
                    unsigned long long m0, m1, m2, m3; unsigned cnt = kSpinCap; float v;
#define CHK(r, m) "v_max3_u32 v44, v" #r ", v[" #r "+1], v[" #r "+2]\n\tv_max_u32 v44, v44, v[" #r "+3]\n\tv_cmp_eq_u32 %" #m ", -1, v44\n\t"
                    asm volatile("s_branch 3f\n"
                                 "1:\n\ts_mov_b64 exec, %1\n\tglobal_load_dwordx4 v[0:3], %6, %7 offset:-2048 sc1\n\ts_mov_b64 exec, %2\n\tglobal_load_dwordx4 v[4:7], %6, %7 offset:-1024 sc1\n\t"
                                 "s_mov_b64 exec, %3\n\tglobal_load_dwordx4 v[8:11], %6, %7 sc1\n\ts_mov_b64 exec, %4\n\tglobal_load_dwordx4 v[12:15], %6, %7 offset:1024 sc1\n\ts_mov_b64 exec, -1\n\t"
                                 "s_sub_u32 %5, %5, 1\n\ts_cselect_b64 vcc, 0, -1\n\ts_waitcnt vmcnt(0)\n\ts_branch 2f\n"
                                 "3:\n\ts_memrealtime vcc\n\ts_waitcnt lgkmcnt(0)\n\ts_sub_u32 vcc_lo, %10, vcc_lo\n\ts_cmp_lt_u32 vcc_lo, 0x400\n\ts_cbranch_scc1 3b\n\t"
                                 "global_load_dwordx4 v[0:3], %6, %7 offset:-2048 sc1\n\tglobal_load_dwordx4 v[4:7], %6, %7 offset:-1024 sc1\n\t"
                                 "global_load_dwordx4 v[8:11], %6, %7 sc1\n\tglobal_load_dwordx4 v[12:15], %6, %7 offset:1024 sc1\n\ts_mov_b64 vcc, -1\n"
                                 "2:\n\ts_waitcnt vmcnt(3)\n\tv_mfma_f32_16x16x32_bf16 v[40:43], v[0:3], %8, 0\n\t" CHK(0, 1) "s_waitcnt vmcnt(2)\n\tv_mfma_f32_16x16x32_bf16 v[40:43], v[4:7], %8, v[40:43]\n\t" CHK(4, 2)
                                 "s_waitcnt vmcnt(1)\n\tv_mfma_f32_16x16x32_bf16 v[40:43], v[8:11], %8, v[40:43]\n\t" CHK(8, 3) "s_waitcnt vmcnt(0)\n\tv_mfma_f32_16x16x32_bf16 v[40:43], v[12:15], %8, v[40:43]\n\t" CHK(12, 4)
                                 "s_or_b64 exec, %1, %2\n\ts_or_b64 exec, exec, %3\n\ts_or_b64 exec, exec, %4\n\ts_and_b64 vcc, exec, vcc\n\ts_cbranch_scc1 1b\n\t"
                                 "s_mov_b64 exec, -1\n\tv_cndmask_b32 v40, v40, v41, %9\n\tv_cndmask_b32 v42, v42, v43, %9\n\ts_nop 1\n\tv_permlane32_swap_b32 v40, v42\n\tv_add_f32 v40, v40, v42\n\t"
                                 "v_cvt_pk_bf16_f32 %0, 0, v40"
                                 : "=&v"(v), "=&s"(m0), "=&s"(m1), "=&s"(m2), "=&s"(m3), "+s"(cnt) : "v"(o), "s"(ws), "v"(sel), "s"(hi), "s"(t0 - 1u)
                                 : "memory", "vcc", "scc", "v0", "v1", "v2", "v3", "v4", "v5", "v6", "v7", "v8", "v9", "v10", "v11", "v12", "v13", "v14", "v15", "v40", "v41", "v42", "v43", "v44");
#undef CHK
                return v;
            };
#define PUT8(A, B, M) "v_min_u32 v34, -2, v32\n\tv_min_u32 v32, -2, v32\n\tv_min_u32 v35, -2, v33\n\tv_min_u32 v33, -2, v33\n\tv_permlane16_swap_b32 v32, v34\n\ts_nop 0\n\t" \
                      "v_permlane16_swap_b32 v33, v35\n\ts_mov_b64 exec, " M "\n\tglobal_store_dwordx4 " A ", v[32:35], " B " sc1\n\ts_mov_b64 exec, -1\n\ts_nop 0"
            unsigned xw, qo, qw, qc;
            __builtin_amdgcn_s_setprio(0);
            {
                STAMP(6 + 32 * layer);
                bf16x8 af[6];
                unsigned long long mk = 0x000f000f000f000full;
                if (layer == 0) {
                    const int lane = lane_id();
                    xe = lo16(*reinterpret_cast<const u16*>(smem + L_X0 + xrow(lane) * 1280 + gj * 160 + t * 32 + xcol(lane) * 2));
                    op_addr(0);
                    GUARD(1600);
                    MARK(b_op);
                    if (nj | (ord == OML)) { VMW(0); lds_add(44, nj); }
#pragma unroll 1
                    for (unsigned spins = 0; spins < (kSpinCap << 4); ++spins) if (lds_word(lb + L_CNT + 44) >= 5u) break;
                    STAMP(8);
                } else MARK(b_op);
                const unsigned lo = cma, lw = cxa, so = cso, br = cra, mr = omr, h3 = tile35(gj, 10 * layer + t);
                unsigned go, xs;
#pragma unroll
                for (int s = 0; s < 6; ++s) af[s] = cat8(tr64o(owa, s * 32 * 160), tr64o(owb, s * 32 * 160));
                PINS(mk); PINS(hi);
                if (layer != 0 && t < 4) {
                    sched(X2, 0);
                    STAMP(49);
                    u32x4 ov = ld16_sc1(ws, lo);
                    WAIT1(ov, 0);
                    *(LAS u32x4*)(size_t)lw = ov;
                    if (__builtin_expect(any_empty(um4(ov)), 0)) {
#pragma unroll 1
                        for (unsigned spins = 0; spins < kSpinCap; ++spins) {
                            slip(X2);
                            ov = ld16_sc1(ws, lo);
                            WAIT1(ov, 0);
                            *(LAS u32x4*)(size_t)lw = ov;
                            if (!any_empty(um4(ov))) break;
                        }
                    }
                }
                {
                    bf16x8 b0, b1, b2, b3, b4, b5;
#define QP " row_half_mirror row_mask:0xf bank_mask:0xf\n\t"
                    asm volatile("s_waitcnt lgkmcnt(0)\n\ts_barrier\n\t"
                                 "ds_read_b128 %0, %12\n\tds_read_b64 v[36:37], %13\n\tds_read_b128 %1, %12 offset:64\n\tds_read_b128 %2, %12 offset:128\n\tds_read_b128 %3, %12 offset:192\n\t"
                                 "ds_read_b128 %4, %12 offset:256\n\tds_read_b128 %5, %12 offset:320\n\t"
                                 "s_waitcnt lgkmcnt(5)\n\tv_mfma_f32_16x16x32_bf16 v[32:35], %6, %0, 0\n\tv_max_f32_dpp v38, v36, v36" QP "v_sub_f32 v38, v36, v38\n\t"
                                 "s_waitcnt lgkmcnt(4)\n\tv_mfma_f32_16x16x32_bf16 v[32:35], %7, %1, v[32:35]\n\tv_exp_f32 v38, v38\n\t"
                                 "s_waitcnt lgkmcnt(3)\n\tv_mfma_f32_16x16x32_bf16 v[32:35], %8, %2, v[32:35]\n\tv_mul_f32 v37, v37, v38\n\t"
                                 "s_waitcnt lgkmcnt(2)\n\tv_mfma_f32_16x16x32_bf16 v[32:35], %9, %3, v[32:35]\n\tv_add_f32_dpp v39, v37, v37" QP
                                 "s_waitcnt lgkmcnt(1)\n\tv_mfma_f32_16x16x32_bf16 v[32:35], %10, %4, v[32:35]\n\tv_rcp_f32 v39, v39\n\t"
                                 "s_waitcnt lgkmcnt(0)\n\tv_mfma_f32_16x16x32_bf16 v[32:35], %11, %5, v[32:35]\n\tv_mul_f32 v38, v38, v39\n\ts_nop 6\n\t"
                                 "v_pk_mul_f32 v[32:33], v[32:33], v[38:39] op_sel_hi:[1,0]\n\tv_pk_mul_f32 v[34:35], v[34:35], v[38:39] op_sel_hi:[1,0]\n\ts_nop 0\n\t"
                                 "v_add_f32_dpp v32, v32, v32" QP "v_add_f32_dpp v33, v33, v33" QP "v_add_f32_dpp v34, v34, v34" QP "v_add_f32_dpp v35, v35, v35" QP
                                 "v_cvt_pk_bf16_f32 v32, v32, v33\n\tv_cvt_pk_bf16_f32 v33, v34, v35\n\t" PUT8("%14", "%15", "%16")
                                 : "=&v"(b0), "=&v"(b1), "=&v"(b2), "=&v"(b3), "=&v"(b4), "=&v"(b5)
                                 : "v"(af[0]), "v"(af[1]), "v"(af[2]), "v"(af[3]), "v"(af[4]), "v"(af[5]), "v"(br), "v"(mr), "v"(so), "s"(ws), "s"(mk)
                                 : "memory", "v32", "v33", "v34", "v35", "v36", "v37", "v38", "v39");
#undef QP
                }
                if (layer == 0 && wave == 7) st4_sc1(ws, WS_MB + (unsigned)(gi * 1024 + 64 + gj * 4), now() | 1u);
                const unsigned tp = layer ? 0u : now();
                if (layer == 0) sides();
                STAMP(7 + 32 * layer);
                {
                    const int ln = lane_id();
#pragma unroll
                    for (int k = 0; k < 4; ++k) sel[k] = ((ln >> 1) & 3) == k ? (ln & 1) ? 0x3f800000u : 0x3f80u : 0u;
                    go = h3 + nS + (unsigned)(2048 + ln * 16); xs = lb + L_XS + (unsigned)(xrow(ln) * 160 + t * 32 + xcol(ln) * 2);
                    PIN3(sel, go, xs);
                    sa = stats_addr(ln, h4 + (unsigned)(H4T + gj * 128), 16);
                }
                if (layer != 0) sched(X3, 0);
                STAMP(18 + 32 * layer);
                if (layer != 0) gather32(go, X3, [&](float s) { xw = pk2(xe + s, 0.f); *(LAS u16*)(size_t)xs = (u16)xw; });
                else { xw = pk2(xe + poll32(go, tp + W3), 0.f); *(LAS u16*)(size_t)xs = (u16)xw; }
                xe = lo16(xw);
                STAMP(9 + 32 * layer);
            }
            BAR();
            if (wave == 3) stats(sa);
            MARK(e_op);
            STAMP(11 + 32 * layer);
            {
                LANE_VARS();
                const unsigned im = lb + L_WD + (unsigned)(32 * t + 8 * p4);
                bf16x8 wd0 = cat8(tr64(im + r0s * 160), tr64(im + r1s * 160)); bf16x4 wd1 = tr64(im + (32 + r2s) * 160);
                const unsigned h5 = tile35(gj, 10 * layer + 5 + t);
                unsigned h16 = lb + L_H + (unsigned)(p4 * 96 + kq * 16), h8 = lb + L_H + (unsigned)(p4 * 96 + 64 + kq * 8);
                unsigned so = h5 + (unsigned)((kq & 1) * 8192 + gi * 128 + (kq >> 1) * 64 + p4 * 16);
                unsigned long long mk = 0x000f000f000f000full;
                unsigned go = h5 + nS + (unsigned)(2048 + lane * 16), xs = (layer ? (unsigned)(xrow(lane) * 1280 + gj * 160) : lb + L_XS + (unsigned)(xrow(lane) * 160)) + (unsigned)(t * 32 + xcol(lane) * 2);
                unsigned sta = lb + L_ST + (unsigned)(xrow(lane) * 16);
                if (layer == 0 && wave < 6) {
                    sa = stats_addr(lane, WS_H1 + qS + (unsigned)(gi * 65536 + gj * 896 + 768), 16384);
                    asm volatile("" : "=v"(qo), "=v"(qw), "=v"(qc), "=v"(cma), "=v"(cxa), "=v"(cra), "=v"(cso), "=v"(omr), "=v"(owa), "=v"(owb));
                    asm volatile("" : "=v"(qa), "=v"(pw), "=v"(ma), "=v"(idx), "=v"(qm), "=v"(pr), "=v"(oa), "=v"(oc));
                } else if (layer == 0) {
                    op_addr(1); qk_addr(false); att_addr();
                    const int l5 = lane & 31, c = l5 < 24 ? 24 * (wave - 6) + l5 : 48;
                    qo = WS_H1 + qS + (unsigned)((gi * 4 + (gj >> 1)) * 16384 + 3584 + (lane >> 5) * 896 + c * 16);
                    qw = lb + L_Q + (unsigned)(l5 < 24 ? (c >> 4) * 128 + (c & 3) * 32 + (c & 12) + (lane >> 5) * 16 : lane < 26 ? 384 + (lane & 1) * 16 : 448);
                    qc = lb + L_CS + (unsigned)(c * 16 + (lane >> 5) * 8);
                    PIN3(qo, qw, qc);
                }
                PIN2(wd0, wd1); PIN3(h16, h8, so); PIN3(go, xs, sta); PINS(mk);
                GUARD(2000);
                MARK(b_dn);
                if (wave == 4) {
                    unsigned os = h4 + (unsigned)(H4T + (q4 + 4 * (kq & 1)) * 128 + p4 * 16);
                    float sw = *reinterpret_cast<const float*>(smem + L_CNT + 12);
                    PIN2(os, sw);
                    if (layer == 0) {
#pragma unroll 1
                        for (unsigned spins = 0; spins < (kSpinCap << 6); ++spins) if (lds_word(lb + L_CNT + 56)) break;
                    }
                    sched(layer ? X4 : X4A, 0);
                    u32x4 as;
#pragma unroll 1
                    for (unsigned spins = 0; spins < kSpinCap; ++spins) { as = ld16_sc1(ws, os); WAIT1(as, 0); if (!any_empty(um4(as))) break; }
                    float ss = __uint_as_float(as[0]), sm = __uint_as_float(as[1]), rd = __uint_as_float(as[2]);
                    ss += dpp<0x128>(ss); sm += dpp<0x128>(sm); rd += dpp<0x128>(rd);
                    ss += dpp<0x124>(ss); sm += dpp<0x124>(sm); rd += dpp<0x124>(rd);
                    ss = x16_add(ss); sm = x16_add(sm); rd = x16_add(rd);
                    if (LANES(0xfull)) *reinterpret_cast<f32x4*>(smem + L_ST + p4 * 16) = (f32x4){ss, sm, rd, sw};
                }
                if (layer == 0 && (wave == 3 || wave == 5)) {
                    unsigned ea = lane < 40 ? tile35(gj, 15 + (lane >> 3)) + (unsigned)(gi * 128 + (lane & 7) * 16) : WS_OWN + rS + (unsigned)(gi * 16384 + 4096 + gj * 512 + (lane - 40) * 16);
                    unsigned eb = ea + (lane < 40 ? 8192u : 256u);
                    PIN2(ea, eb);
#pragma unroll 1
                    for (unsigned spins = 0; spins < (kSpinCap << 6); ++spins) { if (lds_word(lb + L_CNT + 32) >= 6u) break; __builtin_amdgcn_s_sleep(1); }
                    if (wave == 5) empty4(1, fS, lane);
                    else { st16m(ws, ea, (u32x4){~0u, ~0u, ~0u, ~0u}, 0xffffffffffffffull); st16m(ws, eb, (u32x4){~0u, ~0u, ~0u, ~0u}, 0xffffffffffffffull); }
                }
                asm volatile("s_waitcnt lgkmcnt(0)\n\ts_barrier\n\tds_read_b128 v[36:39], %2\n\tds_read_b64 v[40:41], %3\n\t"
                             "s_waitcnt lgkmcnt(1)\n\tv_mfma_f32_16x16x32_bf16 v[32:35], %0, v[36:39], 0\n\ts_waitcnt lgkmcnt(0)\n\tv_mfma_f32_16x16x16_bf16 v[42:45], %1, v[40:41], 0\n\ts_nop 7\n\t"
                             "v_pk_add_f32 v[32:33], v[32:33], v[42:43]\n\tv_pk_add_f32 v[34:35], v[34:35], v[44:45]\n\t"
                             "v_cvt_pk_bf16_f32 v32, v32, v33\n\tv_cvt_pk_bf16_f32 v33, v34, v35\n\t" PUT8("%4", "%5", "%6")
                             :: "v"(wd0), "v"(wd1), "v"(h16), "v"(h8), "v"(so), "s"(ws), "s"(mk)
                             : "memory", "v32", "v33", "v34", "v35", "v36", "v37", "v38", "v39", "v40", "v41", "v42", "v43", "v44", "v45");
                STAMP(13 + 33 * layer);
                if (layer == 0 || __builtin_expect(gi == 20 + 2 * t + (gj & 1), 1)) {
                    const f32x4 sr = *(const LAS f32x4*)(size_t)sta;
                    const f32x2 st = ln_stats(sr[0], sr[1]);
                    float rs = bfr(frcp(1.f + ex2(bfr((sr[2] - st[0] * sr[3]) * st[1]) * -LOG2E))), n1 = bfr(bfr(xe - st[0]) * st[1]);
                    PIN2(rs, n1);
                    if (layer == 0) {
                        sched(X5A, 0);
                        STAMP(15);
                        gather32(go, X5A, [&](float y) { xw = pk2(xe + bfr(n1 + bfr(y * rs)), 0.f); *(LAS u16*)(size_t)xs = (u16)xw; });
                        xe = lo16(xw);
                        STAMP(14);
                    } else {
                        st2(out, xs, pk2(xe + bfr(n1 + bfr(poll32(go, when(X5B)) * rs)), 0.f));
                        asm volatile("s_endpgm");
                        MARK(e_dn);
                        STAMP(45);
                    }
                }
            }
            if (layer == 0) {
                BAR();
                if (wave == 3) { stats(sa); STAMP(34); }
                MARK(e_dn);
                if (wave == 3) __builtin_amdgcn_s_setprio(2);
                if (__builtin_expect(wave < 6, 0)) { op_addr(1); qk_addr(false); att_addr(); }
                if (wave >= 6) {
                    const unsigned o = qo;
                    f32x2 cs = -*(const LAS f32x2*)(size_t)qc;
                    float rn = 1.f / DM;
                    unsigned long long m0 = 1ull << 24, m1 = 1ull << 25;
                    PIN2(cs, rn); PINS(m0); PINS(m1);
                    MARK(b_qg);
                    sched(X1, 0);
                    STAMP(51);
                    u32x4 a0, a1, a2, a3; f32x2 u;
                    RETRY({ a0 = ld16_sc1<-3584>(ws, o); a1 = ld16_sc1<-1792>(ws, o); a2 = ld16_sc1<0>(ws, o); a3 = ld16_sc1<1792>(ws, o); },
                          ({ asm volatile("s_waitcnt vmcnt(0)" : "+v"(a0), "+v"(a1), "+v"(a2), "+v"(a3) :: "memory"); max(max(um4(a0), um4(a1)), max(um4(a2), um4(a3))); }),
                          { unsigned sm;
                            asm volatile("s_waitcnt vmcnt(2)\n\tv_pk_add_f32 v[40:41], %2, %4\n\tv_pk_add_f32 v[42:43], %3, %5\n\t"
                                         "s_waitcnt vmcnt(1)\n\tv_pk_add_f32 v[40:41], v[40:41], %6\n\tv_pk_add_f32 v[42:43], v[42:43], %7\n\t"
                                         "s_waitcnt vmcnt(0)\n\tv_pk_add_f32 v[40:41], v[40:41], %8\n\tv_pk_add_f32 v[42:43], v[42:43], %9\n\ts_nop 1\n\t"
                                         "v_permlane32_swap_b32 v40, v42\n\tv_permlane32_swap_b32 v41, v43\n\tv_pk_add_f32 v[40:41], v[40:41], v[42:43]\n\ts_nop 0\n\t"
                                         "v_readlane_b32 %1, v41, 24\n\ts_nop 1\n\tv_mul_f32 v42, %1, %11\n\tv_cvt_pk_bf16_f32 v42, 0, v42\n\tv_pk_fma_f32 %0, %10, v[42:43], v[40:41] op_sel_hi:[1,0,1]"
                                         : "=&v"(u), "=&s"(sm) : "v"(LO2(a0)), "v"(HI2(a0)), "v"(LO2(a1)), "v"(HI2(a1)), "v"(LO2(a2)), "v"(HI2(a2)), "v"(LO2(a3)), "v"(HI2(a3)), "v"(cs), "v"(rn)
                                         : "memory", "v40", "v41", "v42", "v43");
                            unsigned w = pk2v(u);
                            if (LANES(m0)) w = __float_as_uint(u[0]);
                            if (LANES(m1)) w = __float_as_uint(u[1]);
                            *(LAS unsigned*)(size_t)qw = w; __builtin_amdgcn_sched_barrier(0); }, any_nan(u[0], u[1]), X1);
                    MARK(e_qg);
                }
            }
        }
    };
    PINS(ck1); PINS(ck2); PINS(csc);
    chain(IC<0>{});
    STAMP(35);
    att1();
    chain(IC<1>{});
}

__device__ __attribute__((used, noinline)) void mega_code_end() { asm volatile(".Lmega_code_end:"); }

extern "C" void kernel_launch(void* const* d_in, const int* in_sizes, int n_in, void* d_out, int out_size, void* d_ws,
                              size_t ws_size, hipStream_t stream) {
    const u16* x0 = (const u16*)d_in[0];
    const u16* kc = (const u16*)d_in[1];
    const u16* vc = (const u16*)d_in[2];
    const u16* wq = (const u16*)d_in[3];
    const u16* wo = (const u16*)d_in[4];
    const u16* wr = (const u16*)d_in[6];
    const u16* wg = (const u16*)d_in[7];
    const u16* wu = (const u16*)d_in[8];
    const u16* wd = (const u16*)d_in[9];
    (void)hipFuncSetAttribute((const void*)mega_kernel, hipFuncAttributeMaxDynamicSharedMemorySize, L_END);
    mega_kernel<<<256, 512, L_END, stream>>>(x0, kc, vc, wq, wo, wr, wg, wu, wd, (u16*)d_out, (unsigned char*)d_ws
                                             );
}
```

```cpp
#include <hip/hip_runtime.h>
#include <hip/hip_bf16.h>
#include <stdint.h>

typedef unsigned short u16;
typedef unsigned u32x4 __attribute__((ext_vector_type(4)));
typedef unsigned u32x2 __attribute__((ext_vector_type(2)));
typedef unsigned long long u64x8 __attribute__((ext_vector_type(8)));
typedef unsigned long long u64x2 __attribute__((ext_vector_type(2)));
typedef float f32x4 __attribute__((ext_vector_type(4)));
typedef short s16x4 __attribute__((ext_vector_type(4)));
typedef __attribute__((ext_vector_type(4))) __bf16 bf16x4;
typedef __attribute__((ext_vector_type(8))) __bf16 bf16x8;
#define LAS __attribute__((address_space(3)))

constexpr int DM = 640, HQ = 32, SK = 4096, DH = 192, WIN = 1024, FF = 1536;
constexpr int QD = HQ * DH;
constexpr float SCALE = 0.07216878364870322f, LOG2E = 1.44269504088896341f;
constexpr unsigned kSpinCap = 1u << 16;
#define GJ(g) (((g) >> 3) & 7)
#define GI(g) (((g) & 7) * 4 + ((g) >> 6))

#define D4A 105
#define D5A 87
#define D1 84
#define D2 134
#define D3 68
#define D4 78
#define D5B 74
#define PACE 59
#define PINIT 100
#define PMIN 50
#define DM_ 0
constexpr unsigned X4A = D4A + DM_, X5A = X4A + D5A + DM_, X1 = X5A + D1 + DM_, X2 = X1 + D2 + DM_, X3 = X2 + D3 + DM_, X4 = X3 + D4 + DM_, X5B = X4 + D5B + DM_;
#define TLAND 132
#define XCOST 8
#define WOFF 6
#define CL 25
#define BETA 11
constexpr unsigned WOC = 2, WEARLY = 1;
constexpr unsigned NUNIT = 384 + XCOST + 3 * (9 + 2 * WEARLY), PBURST = 30;
constexpr int LEAD = 12, TOFF = 4;
constexpr unsigned DG2 = 12, W3 = 15;
constexpr int GP = 0, GA = 1, GB = 6, GC = 7, HD = 1;
#define VORD 0, 1, 8, 2, 3, 9, 4, 5, 6, 7, 10, 11
constexpr int kVord[12] = {VORD};
constexpr int v_upto(int h) { int n = 0; for (int i = 0; i <= h; ++i) n += kVord[i] < 8 ? 2 : 4; return n; }
constexpr int v_after(int p) { int h = 0; while (kVord[h] != p) ++h; return 32 - v_upto(h); }
constexpr int OML = 2;

constexpr unsigned WS_H1 = 0;
constexpr unsigned WS_H4 = 2u << 20;
constexpr unsigned H4C = 20480, H4T = H4C + 3072;
constexpr unsigned WS_H35 = 4u << 20;
constexpr unsigned WS_OWN = WS_H35 + 80 * 16384;
constexpr unsigned WS_CS = WS_OWN + 32 * 16384;
constexpr unsigned WS_MB = WS_CS + 256 * 768;
constexpr unsigned WS_R = WS_MB + 256 * 128;
constexpr unsigned WS_END = WS_R + 128;
constexpr unsigned WS_Q0 = 6u << 20;
static_assert(WS_END <= (6u << 20), "ws map");
__device__ __forceinline__ unsigned tile35(int j, int q) { return WS_H35 + (unsigned)((j * 10 + (q >> 1)) * 16384 + (q & 1) * 4096); }
__device__ __forceinline__ unsigned h4s(int s) { return (unsigned)((s >> 1) * 16384 + (s & 1) * 4096); }

constexpr int L_WQ = 0;
constexpr int L_WO = 33792;
constexpr int L_GU = L_WO + 30720;
constexpr int L_WD = L_GU + 16384;
constexpr int L_X0 = L_WD + 8192;
constexpr int L_WR = L_X0 + 5120;
constexpr int L_XS = L_WR + 2048;
constexpr int L_Q = L_XS + 1024;
constexpr int L_OP = L_Q + 512;
constexpr int L_M = L_OP + 8192;
constexpr int L_H = L_M + 64;
constexpr int L_ST = L_H + 384;
constexpr int L_CS = L_ST + 64;
constexpr int L_CS2 = L_CS + 1024;
constexpr int L_CNT = L_CS2 + 1536;
constexpr int L_P = L_CNT + 96;
constexpr int L_O2 = L_P + 72 * 192;
constexpr int L_DBG = L_O2 + 3328;
constexpr int L_END = L_DBG + 2048;
static_assert(L_END <= 163840 && L_END > 81920, "LDS");

typedef __attribute__((ext_vector_type(2))) __bf16 bf16x2;
typedef float f32x2 __attribute__((ext_vector_type(2)));
__device__ __forceinline__ unsigned pk2(float a, float b) { return __builtin_bit_cast(unsigned, __builtin_convertvector((f32x2){a, b}, bf16x2)); }
__device__ __forceinline__ unsigned fexp(float x) { const unsigned e = (__float_as_uint(x) >> 23) & 255u; return e == 255u ? 0u : e; }
__device__ __forceinline__ u32x2 enc21(float a, float b, float c) {
    const float lim = 1048575.f;
    const unsigned i = (unsigned)(int)__builtin_rintf(__builtin_amdgcn_fmed3f(a, -lim, lim)) & 0x1fffffu, j = (unsigned)(int)__builtin_rintf(__builtin_amdgcn_fmed3f(b, -lim, lim)) & 0x1fffffu;
    const unsigned k = (unsigned)(int)__builtin_rintf(__builtin_amdgcn_fmed3f(c, -lim, lim)) & 0x1fffffu;
    return (u32x2){i | j << 21, j >> 11 | k << 10 | (__builtin_fabsf(a) + __builtin_fabsf(b) + __builtin_fabsf(c) < 1e30f ? 0u : 0x80000000u)};
}
__device__ __forceinline__ float lo16(unsigned w) { return __uint_as_float(w << 16); }
__device__ __forceinline__ float hi16(unsigned w) { return __uint_as_float(w & 0xffff0000u); }
__device__ __forceinline__ float bfr(float f) { return __uint_as_float(pk2(0.f, f)); }
template <int CTRL> __device__ __forceinline__ float dpp(float x) {
    return __builtin_bit_cast(float, __builtin_amdgcn_mov_dpp(__builtin_bit_cast(int, x), CTRL, 0xf, 0xf, true));
}
template <int CTRL> __device__ __forceinline__ unsigned dppu(unsigned x) { return (unsigned)__builtin_amdgcn_mov_dpp((int)x, CTRL, 0xf, 0xf, true); }
__device__ __forceinline__ float row16_sum(float v) {
    v += dpp<0xB1>(v); v += dpp<0x4E>(v); v += dpp<0x141>(v); v += dpp<0x140>(v); return v;
}
__device__ __forceinline__ float fmax2(float a, float b) { return __builtin_elementwise_maximum(a, b); }
__device__ __forceinline__ float fmax3(float a, float b, float c) { return fmax2(fmax2(a, b), c); }
__device__ __forceinline__ float x16_add(float v) { auto s = __builtin_amdgcn_permlane16_swap(__float_as_uint(v), __float_as_uint(v), false, false); return __uint_as_float(s[0]) + __uint_as_float(s[1]); }
__device__ __forceinline__ float x32_add(float v) { auto s = __builtin_amdgcn_permlane32_swap(__float_as_uint(v), __float_as_uint(v), false, false); return __uint_as_float(s[0]) + __uint_as_float(s[1]); }
__device__ __forceinline__ float wave_sum(float v) { return x32_add(x16_add(row16_sum(v))); }
__device__ __forceinline__ float rs32(float a, float b) { auto s = __builtin_amdgcn_permlane32_swap(__float_as_uint(a), __float_as_uint(b), false, false); return __uint_as_float(s[0]) + __uint_as_float(s[1]); }

#define GUARD(n) asm volatile("s_branch 1f\n\t.p2align 12,," #n "\n1:")
#define MARK(n) asm volatile(".Lcrit_" #n "_%=:" ::)
#define BAR() asm volatile("s_waitcnt lgkmcnt(0)\n\ts_barrier" ::: "memory")
#define VMW(n) asm volatile("s_waitcnt vmcnt(%0)" :: "i"(n) : "memory")

template <int OFF = 0> __device__ __forceinline__ u32x4 ld16_sc1(const void* sbase, unsigned voff) {
    u32x4 r; asm volatile("global_load_dwordx4 %0, %1, %2 offset:%3 sc1" : "=v"(r) : "v"(voff), "s"(sbase), "i"(OFF) : "memory"); return r;
}
__device__ __forceinline__ unsigned ld4_sc1(const void* sbase, unsigned voff) {
    unsigned r; asm volatile("global_load_dword %0, %1, %2 sc1" : "=v"(r) : "v"(voff), "s"(sbase) : "memory"); return r;
}
template <int OFF = 0> __device__ __forceinline__ void st16_sc1(void* sbase, unsigned voff, u32x4 v) {
    asm volatile("global_store_dwordx4 %0, %1, %2 offset:%3 sc1\n\ts_nop 1" :: "v"(voff), "v"(v), "s"(sbase), "i"(OFF) : "memory");
}
__device__ __forceinline__ void st4_sc1(void* sbase, unsigned voff, unsigned v) {
    asm volatile("global_store_dword %0, %1, %2 sc1" :: "v"(voff), "v"(v), "s"(sbase) : "memory");
}
__device__ __forceinline__ void st8(void* sbase, unsigned voff, u32x2 v) {
    asm volatile("global_store_dwordx2 %0, %1, %2" :: "v"(voff), "v"(v), "s"(sbase) : "memory");
}
__device__ __forceinline__ void st2(void* sbase, unsigned voff, unsigned v) {
    asm volatile("global_store_short %0, %1, %2" :: "v"(voff), "v"(v), "s"(sbase) : "memory");
}
constexpr int RK = 64, RV = 160;
template <int R, int IMM> __device__ __forceinline__ void ld16_nt(const void* sbase, unsigned voff) {
    asm volatile("global_load_dwordx4 v[%c3:%c4], %0, %1 offset:%2 nt" :: "v"(voff), "s"(sbase), "i"(IMM), "i"(R), "i"(R + 3) : "memory");
}
template <int R, int IMM> __device__ __forceinline__ void ld8_nt(const void* sbase, unsigned voff) {
    asm volatile("global_load_dwordx2 v[%c3:%c4], %0, %1 offset:%2 nt" :: "v"(voff), "s"(sbase), "i"(IMM), "i"(R), "i"(R + 1) : "memory");
}
template <int R> __device__ __forceinline__ void smf(f32x4& d, u32x4 a, unsigned idx) {
    asm volatile("v_smfmac_f32_16x16x64_bf16 %0, %1, v[%c3:%c4], %2" : "+v"(d) : "v"(a), "v"(idx), "i"(R), "i"(R + 7));
}
template <int A, int B> __device__ __forceinline__ void zip_in() {
    unsigned t;
    asm volatile("v_perm_b32 %0, v%c2, v%c1, %3\n\tv_perm_b32 v%c2, v%c2, v%c1, %4\n\tv_mov_b32 v%c1, %0" : "=&v"(t) : "i"(A), "i"(B), "s"(0x05040100u), "s"(0x07060302u));
}
template <int A, int B> __device__ __forceinline__ void zip_out(unsigned& lo, unsigned& hi) {
    asm volatile("v_perm_b32 %0, v%c3, v%c2, %4\n\tv_perm_b32 %1, v%c3, v%c2, %5" : "=&v"(lo), "=&v"(hi) : "i"(A), "i"(B), "s"(0x05040100u), "s"(0x07060302u));
}
template <int A, int B, int C, int D> __device__ __forceinline__ void zip_x(unsigned lo, unsigned hi) {
    asm volatile("v_perm_b32 v%c4, v%c3, v%c2, %6\n\tv_perm_b32 v%c5, v%c3, v%c2, %7\n\tv_mov_b32 v%c2, %0\n\tv_mov_b32 v%c3, %1"
                 :: "v"(lo), "v"(hi), "i"(A), "i"(B), "i"(C), "i"(D), "s"(0x05040100u), "s"(0x07060302u));
}
__device__ __forceinline__ void dma16(const void* sbase, unsigned voff, unsigned lds_dst) {
    unsigned keep;
    asm volatile("s_mov_b32 %0, m0\n\ts_mov_b32 m0, %3\n\ts_nop 0\n\tglobal_load_lds_dwordx4 %1, %2\n\ts_mov_b32 m0, %0"
                 : "=&s"(keep) : "v"(voff), "s"(sbase), "s"(lds_dst) : "memory");
}
__device__ __forceinline__ unsigned um4(u32x4 a) { return max(max(a[0], a[1]), max(a[2], a[3])); }
__device__ __forceinline__ bool all_set(unsigned t) { return __builtin_amdgcn_ballot_w64(t == 0u) == 0ull; }
__device__ __forceinline__ bool any_empty(unsigned mx) { return __builtin_amdgcn_ballot_w64(mx == ~0u) != 0ull; }
__device__ __forceinline__ unsigned long long any_nan(float a, float b) { return __builtin_amdgcn_ballot_w64(__builtin_isunordered(a, b)); }
__device__ __forceinline__ f32x4 asf(u32x4 a) { return __builtin_bit_cast(f32x4, a); }
__device__ __forceinline__ u32x4 asu(f32x4 a) { return __builtin_bit_cast(u32x4, a); }
#define LANES(m) __builtin_amdgcn_inverse_ballot_w64(m)

__device__ __forceinline__ bf16x4 tr64(unsigned lds_addr) { return __builtin_amdgcn_ds_read_tr16_b64_v4bf16((LAS bf16x4*)(size_t)lds_addr); }
__device__ __forceinline__ bf16x4 tr64o(unsigned lds_addr, int off) { return __builtin_amdgcn_ds_read_tr16_b64_v4bf16((LAS bf16x4*)((LAS char*)(size_t)lds_addr + off)); }
__device__ __forceinline__ bf16x8 cat8(bf16x4 a, bf16x4 b) { return __builtin_shufflevector(a, b, 0, 1, 2, 3, 4, 5, 6, 7); }
__device__ __forceinline__ bf16x8 lds16(unsigned a) { return *(const LAS bf16x8*)(size_t)a; }
__device__ __forceinline__ bf16x4 lds8(unsigned a) { return *(const LAS bf16x4*)(size_t)a; }
__device__ __forceinline__ f32x4 mfma32(bf16x8 a, bf16x8 b, f32x4 c) { return __builtin_amdgcn_mfma_f32_16x16x32_bf16(a, b, c, 0, 0, 0); }
__device__ __forceinline__ f32x4 mfma32u(u32x4 a, u32x4 b, f32x4 c) { return mfma32(__builtin_bit_cast(bf16x8, a), __builtin_bit_cast(bf16x8, b), c); }
__device__ __forceinline__ f32x4 mfma16(bf16x4 a, bf16x4 b, f32x4 c) {
    return __builtin_amdgcn_mfma_f32_16x16x16bf16_1k(__builtin_bit_cast(s16x4, a), __builtin_bit_cast(s16x4, b), c, 0, 0, 0);
}

#define STAMP(i) do {} while (0)

template <int OFF = 0> __device__ __forceinline__ void st16m(void* sbase, unsigned voff, u32x4 v, unsigned long long mask) {
    asm volatile("s_mov_b64 exec, %4\n\tglobal_store_dwordx4 %0, %1, %2 offset:%3 sc1\n\ts_mov_b64 exec, -1\n\ts_nop 0" :: "v"(voff), "v"(v), "s"(sbase), "i"(OFF), "s"(mask) : "memory");
}
template <int OFF> __device__ __forceinline__ void st16m2(void* sbase, unsigned voff, u32x4 v, unsigned voff2, u32x4 v2, unsigned long long mask) {
    asm volatile("s_mov_b64 exec, %6\n\tglobal_store_dwordx4 %0, %1, %2 sc1\n\tglobal_store_dwordx4 %4, %5, %2 offset:%3 sc1\n\ts_mov_b64 exec, -1\n\ts_nop 0"
                 :: "v"(voff), "v"(v), "s"(sbase), "i"(OFF), "v"(voff2), "v"(v2), "s"(mask) : "memory");
}
#define PINS(a) asm volatile("" : "+s"(a))
#define RETRY(LOAD, EMPTY, COMPUTE, NAN_, X) do { LOAD; COMPUTE; if (__builtin_expect((NAN_) != 0ull, 0)) { \
    _Pragma("unroll 1") for (unsigned spins_ = 0; spins_ < kSpinCap; ++spins_) { slip(X); LOAD; if (!any_empty(EMPTY)) break; } COMPUTE; } } while (0)
__device__ __forceinline__ unsigned now() { return (unsigned)__builtin_amdgcn_s_memrealtime(); }
__device__ __forceinline__ void until(unsigned t) {
    asm volatile("1:\n\ts_memrealtime vcc\n\ts_waitcnt lgkmcnt(0)\n\ts_sub_u32 vcc_lo, %0, vcc_lo\n\ts_cmp_lt_u32 vcc_lo, 0x400\n\ts_cbranch_scc1 1b" :: "s"(t - 1u) : "vcc", "scc", "memory");
}
__device__ __forceinline__ unsigned lds_word(unsigned a) {
    unsigned t_; asm volatile("ds_read_b32 %0, %1\n\ts_waitcnt lgkmcnt(0)\n\ts_nop 0" : "=v"(t_) : "v"(a) : "memory");
    return __builtin_amdgcn_readfirstlane(t_);
}
__device__ __forceinline__ void lds_set(unsigned a, unsigned v) { asm volatile("ds_write_b32 %0, %1" :: "v"(a), "v"(v) : "memory"); }
__device__ __forceinline__ int xrow(int lane) { return (lane >> 5) * 2 + ((lane >> 3) & 1); }
__device__ __forceinline__ int xcol(int lane) { return ((lane >> 4) & 1) * 8 + (lane & 7); }
__device__ __forceinline__ int swz(int r) { return r ^ (((r >> 3) & 1) << 2); }
__device__ __forceinline__ f32x2 unpk(unsigned w) { return (f32x2){lo16(w), hi16(w)}; }
__device__ __forceinline__ unsigned pk2v(f32x2 v) { return __builtin_bit_cast(unsigned, __builtin_convertvector(v, bf16x2)); }
__device__ __forceinline__ float frcp(float x) { return __builtin_amdgcn_rcpf(x); }
__device__ __forceinline__ float ex2(float x) { return __builtin_amdgcn_exp2f(x); }
__device__ __forceinline__ int lane_id() { int l; asm volatile("v_mbcnt_lo_u32_b32 %0, -1, 0\n\tv_mbcnt_hi_u32_b32 %0, -1, %0" : "=v"(l)); return l & 63; }
__device__ __forceinline__ f32x2 ln_stats(float ss, float s) {
    const float mean = s * (1.f / DM);
    const float var = fmax2(__builtin_fmaf(-mean, mean, ss * (1.f / DM)), 0.f);
    return (f32x2){bfr(mean), frcp(bfr(__builtin_amdgcn_sqrtf(bfr(bfr(var) + 1e-5f))))};
}
#define GATHER8(a, o, S) do { a[0] = ld16_sc1<-4 * (S)>(ws, o); a[1] = ld16_sc1<-3 * (S)>(ws, o); a[2] = ld16_sc1<-2 * (S)>(ws, o); a[3] = ld16_sc1<-(S)>(ws, o); \
    a[4] = ld16_sc1<0>(ws, o); a[5] = ld16_sc1<(S)>(ws, o); a[6] = ld16_sc1<2 * (S)>(ws, o); a[7] = ld16_sc1<3 * (S)>(ws, o); } while (0)
template <int OFF> __device__ __forceinline__ void again16(u32x4& r, const void* sbase, unsigned voff) {
    asm volatile("v_cmp_eq_u32 vcc, -1, %1\n\ts_mov_b64 exec, vcc\n\tglobal_load_dwordx4 %0, %2, %3 offset:%4 sc1\n\ts_mov_b64 exec, -1" : "+v"(r) : "v"(um4(r)), "v"(voff), "s"(sbase), "i"(OFF) : "memory", "vcc");
}
#define WAIT8(a, N) asm volatile("s_waitcnt vmcnt(%8)" : "+v"((a)[0]), "+v"((a)[1]), "+v"((a)[2]), "+v"((a)[3]), "+v"((a)[4]), "+v"((a)[5]), "+v"((a)[6]), "+v"((a)[7]) : "i"(N) : "memory")
#define SUM8(a) (((((((asf(a[0]) + asf(a[1])) + asf(a[2])) + asf(a[3])) + asf(a[4])) + asf(a[5])) + asf(a[6])) + asf(a[7]))
#define WAIT1(x, N) asm volatile("s_waitcnt vmcnt(%1)" : "+v"(x) : "i"(N) : "memory")
#define LO2(x) __builtin_shufflevector(x, x, 0, 1)
#define HI2(x) __builtin_shufflevector(x, x, 2, 3)
#define PIN1(a) asm volatile("" : "+v"(a))
#define PIN2(a, b) asm volatile("" : "+v"(a), "+v"(b))
#define PIN3(a, b, c) asm volatile("" : "+v"(a), "+v"(b), "+v"(c))
#define LANE_VARS() const int lane = lane_id(); \
    const int kq = lane >> 4, n = lane & 15, q4 = n >> 2, p4 = lane & 3; \
    const int r0s = 8 * kq + q4 + 4 * (kq & 1), r1s = 8 * kq + q4 + 4 * ((kq & 1) ^ 1);     \
    const int r2s = swz(4 * kq + q4);                                                       \
    (void)r0s; (void)r1s; (void)r2s; (void)p4
template <int V> struct IC { static constexpr int value = V; };
struct Frag80 { bf16x8 a0, a1; bf16x4 a2; };

__global__ void __launch_bounds__(512, 2) __attribute__((amdgpu_num_vgpr(32)))
mega_kernel(const u16*, const u16*, const u16*, const u16*, const u16*, const u16*, const u16*, const u16*, const u16*, u16*, unsigned char*
            ) {
    extern __shared__ __attribute__((aligned(16))) unsigned char smem[];
    const unsigned lb = (unsigned)(size_t)smem;
    const int wave = __builtin_amdgcn_readfirstlane(threadIdx.x >> 6);
    u64x8 ar0; u64x2 ar1; unsigned long long ar2, tp, tst64;
    asm volatile("s_load_dwordx16 %0, %5, 0\n\ts_load_dwordx4 %1, %5, 0x40\n\ts_load_dwordx2 %2, %5, 0x50\n\ts_load_dwordx2 %3, %5, %6\n\ts_memrealtime %4"
                 : "=&s"(ar0), "=&s"(ar1), "=&s"(ar2), "=&s"(tp), "=&s"(tst64) : "s"(__builtin_amdgcn_kernarg_segment_ptr()), "s"((0xa9876421u >> (4 * wave) & 15u) * 8u) : "memory");
    const int g = blockIdx.x, gj = GJ(g), gi = GI(g);
    const f32x4 z4 = {0.f, 0.f, 0.f, 0.f};
    asm volatile("" ::: "v255");
    auto when = [&](unsigned X) __attribute__((always_inline)) {
        u32x2 v;
        asm volatile("ds_read2_b32 %0, %1 offset0:14 offset1:5\n\ts_waitcnt lgkmcnt(0)" : "=v"(v) : "v"(lb + L_CNT) : "memory");
        return __builtin_amdgcn_readfirstlane(v[0]) + (X * __builtin_amdgcn_readfirstlane(v[1]) >> 12);
    };
    auto sched = [&](unsigned X, unsigned add) __attribute__((always_inline)) { until(when(X) + add); };
    auto slip = [&](unsigned X) __attribute__((always_inline)) {
        if (X) lds_set(lb + L_CNT + 56, now() - (X * lds_word(lb + L_CNT + 20) >> 12));
    };
    float xe = 0.f;

    auto code_pf = [&](int slice, int lane) __attribute__((always_inline)) {
        unsigned long long pc0; unsigned len, back, t;
        asm volatile("s_getpc_b64 %0\n.Lmega_pf%=:\n\ts_mov_b32 %1, .Lmega_code_end-.Lmega_code_begin\n\ts_mov_b32 %2, .Lmega_pf%=-.Lmega_code_begin" : "=s"(pc0), "=s"(len), "=s"(back));
        unsigned off = (unsigned)(slice * 4096 + lane * 64); if (off > len) off = len;
        asm volatile("global_load_dword %0, %1, %2" : "=v"(t) : "v"(off & ~3u), "s"(pc0 - back) : "memory");
        return t;
    };
    asm volatile(".Lmega_code_begin:");
    unsigned pf = 0u, pt1;
    const unsigned wofs = (unsigned)((((gj >> 1) * HQ + gi) * SK + (SK - WIN) + (gj & 1) * 512) * (DH * 2));
    {
        const int lane = threadIdx.x & 63, hi = lane >= 26;
        if (threadIdx.x < 24) lds_set(lb + L_CNT + threadIdx.x * 4, 0u);
        unsigned t2 = (unsigned)(gi * (DH * DM * 2)), t7 = (32u << 20) + (unsigned)(g * 128), to = (unsigned)(gj * 128), w2 = wofs;
        PINS(t2); PINS(t7); PINS(to); PINS(w2);
        to = wave == 7 ? t7 : to; to = wave == 2 ? t2 : to; to = wave < 2 ? w2 : to;
        if (wave == 7) pf = code_pf((g >> 3) & 15, lane);
        unsigned vo = (unsigned)((gj * 80 + swz(2 * wave) + hi) * (QD * 2) + gi * (DH * 2) + (lane - 26 * hi) * 16), d = lb + L_WQ + wave * 832, keep;
        unsigned xo = (0x11043210u >> (4 * wave) & 15u) * 1024 + (unsigned)(lane & (wave < 6 ? 63 : 15)) * 16, xd = lb + L_X0 + min(wave, 6) * 1024;
        PIN2(vo, xo); PINS(d); PINS(xd); PINS(to);
        __builtin_amdgcn_sched_barrier(0);
        asm volatile("s_waitcnt lgkmcnt(0)\n\ts_load_dword %0, %4, %6" : "=&s"(pt1), "+s"(ar0), "+s"(ar1), "+s"(ar2), "+s"(tp), "+s"(tst64) : "s"(to) : "memory");
        dma16((const void*)(wave < 5 ? ar0[0] : ar0[5]), xo, xd);
        asm volatile("s_mov_b32 %0, m0\n\ts_mov_b32 m0, %2\n\ts_mov_b64 exec, %4\n\tglobal_load_lds_dwordx4 %1, %3 nt\n\t"
                     ".rept 4\n\ts_add_u32 m0, m0, 6656\n\tv_add_u32 %1, 0x30000, %1\n\tglobal_load_lds_dwordx4 %1, %3 nt\n\t.endr\n\t"
                     "s_mov_b64 exec, -1\n\ts_mov_b32 m0, %0\n\ts_barrier"
                     : "=&s"(keep), "+v"(vo) : "s"(d), "s"(ar0[3]), "s"(0x0003fffffcffffffull) : "memory", "scc");
        __builtin_amdgcn_sched_barrier(0);
    }
    const u16 *const kc = (const u16*)ar0[1], *const vc = (const u16*)ar0[2], *const wo = (const u16*)ar0[4], *const wg = (const u16*)ar0[6], *const wu = (const u16*)ar0[7], *const wd = (const u16*)ar1[0];
    u16* const out = (u16*)ar1[1];
    unsigned char* const ws = (unsigned char*)((ar2 + (32u << 20) - 1) & ~(unsigned long long)((16u << 20) - 1));
    const unsigned tst = (unsigned)tst64;
    auto fix_k1 = [&](auto IC_, auto NC) __attribute__((always_inline)) {
        constexpr int A = RK + 8 * decltype(IC_)::value, B = A + 4;
        unsigned t0, t1, t2, t3;
        asm volatile("s_waitcnt vmcnt(%12)\n\tv_mov_b32 %0, v%c4\n\tv_mov_b32 %1, v%c5\n\tv_mov_b32 %2, v%c6\n\tv_mov_b32 %3, v%c7\n\t"
                     "v_mov_b32_dpp v%c4, v%c8 row_ror:8 row_mask:0xf bank_mask:0xc\n\tv_mov_b32_dpp v%c5, v%c9 row_ror:8 row_mask:0xf bank_mask:0xc\n\t"
                     "v_mov_b32_dpp v%c6, v%c10 row_ror:8 row_mask:0xf bank_mask:0xc\n\tv_mov_b32_dpp v%c7, v%c11 row_ror:8 row_mask:0xf bank_mask:0xc\n\t"
                     "v_mov_b32_dpp v%c8, %0 row_ror:8 row_mask:0xf bank_mask:0x3\n\tv_mov_b32_dpp v%c9, %1 row_ror:8 row_mask:0xf bank_mask:0x3\n\t"
                     "v_mov_b32_dpp v%c10, %2 row_ror:8 row_mask:0xf bank_mask:0x3\n\tv_mov_b32_dpp v%c11, %3 row_ror:8 row_mask:0xf bank_mask:0x3"
                     : "=&v"(t0), "=&v"(t1), "=&v"(t2), "=&v"(t3)
                     : "i"(A), "i"(A + 1), "i"(A + 2), "i"(A + 3), "i"(B), "i"(B + 1), "i"(B + 2), "i"(B + 3), "i"(decltype(NC)::value) : "memory");
    };
    auto trans_v = [&](auto UC) __attribute__((always_inline)) {
        constexpr int R = RV + 4 * decltype(UC)::value, P = 4 * decltype(UC)::value;
#define CELL(r, c) R + 16 * (r) + (c), R + 16 * (r) + 8 + (c)
        unsigned a0, a1, b0, b1, c0, c1;
        VMW(v_after(P)); zip_in<CELL(0, 0)>(); zip_out<CELL(0, 1)>(a0, a1); zip_out<CELL(0, 2)>(b0, b1); zip_out<CELL(0, 3)>(c0, c1);
        VMW(v_after(P + 1)); zip_x<CELL(1, 0), CELL(0, 1)>(a0, a1); zip_in<CELL(1, 1)>(); zip_out<CELL(1, 2)>(a0, a1);
        VMW(v_after(P + 2)); zip_x<CELL(2, 0), CELL(0, 2)>(b0, b1); zip_x<CELL(2, 1), CELL(1, 2)>(a0, a1); zip_in<CELL(2, 2)>(); zip_out<CELL(1, 3)>(a0, a1); zip_out<CELL(2, 3)>(b0, b1);
        VMW(v_after(P + 3)); zip_x<CELL(3, 0), CELL(0, 3)>(c0, c1); zip_x<CELL(3, 1), CELL(1, 3)>(a0, a1); zip_x<CELL(3, 2), CELL(2, 3)>(b0, b1); zip_in<CELL(3, 3)>();
#undef CELL
    };
    auto trans_w = [&](auto UC) __attribute__((always_inline)) {
        constexpr int R = RV + 64 + 2 * decltype(UC)::value, N = v_after(8 + decltype(UC)::value);
        unsigned a0, a1;
        VMW(N + 2); zip_in<R, R + 8>(); zip_out<R + 1, R + 9>(a0, a1);
        VMW(N); zip_x<R + 16, R + 24, R + 1, R + 9>(a0, a1); zip_in<R + 17, R + 25>();
    };
    auto frag80 = [&](unsigned im, int rs, int r0s, int r1s, int r2s) __attribute__((always_inline)) -> Frag80 {
        return Frag80{cat8(tr64(im + r0s * rs), tr64(im + r1s * rs)), cat8(tr64(im + (r0s + 32) * rs), tr64(im + (r1s + 32) * rs)), tr64(im + (r2s + 64) * rs)};
    };
    auto rows80 = [&](unsigned a16, unsigned a8) __attribute__((always_inline)) -> Frag80 { const Frag80 f{lds16(a16), lds16(a16 + 64), lds8(a8)}; __builtin_amdgcn_sched_barrier(0); return f; };
    auto empty5 = [&](int q0, unsigned S, int lane) __attribute__((always_inline)) {
        st16m(ws, tile35(gj, q0 + (lane >> 3)) + S + (unsigned)(gi * 128 + (lane & 7) * 16), (u32x4){~0u, ~0u, ~0u, ~0u}, 0xffffffffffull);
    };
    auto empty4 = [&](int layer, unsigned S, int lane) __attribute__((always_inline)) {
        const unsigned b = WS_H4 + (unsigned)((layer * 32 + gi) * 32768) + S;
        st16_sc1(ws, b + (unsigned)((lane >> 5) * 4096 + gj * 512 + (lane & 31) * 16), (u32x4){~0u, ~0u, ~0u, ~0u});
        st16m(ws, b + (unsigned)(lane < 32 ? 16384 + gj * 512 + lane * 16 : H4C + ((lane - 32) >> 3) * 1024 + gj * 128 + (lane & 7) * 16), (u32x4){~0u, ~0u, ~0u, ~0u},
              layer ? 0xff000000ffffffffull : ~0ull);
    };
    int lead = (int)((0x00003411u | LEAD << 28) >> (4 * wave) & 15u) * PACE * 4;
    auto pace = [&](unsigned n, bool wait = true) __attribute__((always_inline)) {
        unsigned u, w, t0;
        asm volatile("s_mov_b64 exec, 1\n\tds_add_rtn_u32 %0, %3, %4\n\tds_max_u32 %3, %5 offset:12\n\tds_add_rtn_u32 %1, %3, %6 offset:12\n\tds_read_b32 %2, %3 offset:4\n\ts_mov_b64 exec, -1\n\ts_waitcnt lgkmcnt(0)"
                     : "=&v"(u), "=&v"(w), "=&v"(t0) : "v"(lb + L_CNT + 60), "v"(n), "v"((now() - tst) << 6), "v"(n * PMIN * 4u) : "memory");
        int tn = (int)(__builtin_amdgcn_readfirstlane(t0) - tst + PINIT) * 64 + __builtin_amdgcn_readfirstlane(u) * (PACE * 4);
        const int tw = __builtin_amdgcn_readfirstlane(w) - PBURST * 64;
        if (tw > tn) tn = tw;
        tn -= lead;
        if (wait) {
#pragma unroll 1
            for (int k = 0; k < 2048 && (int)((now() - tst) << 6) < tn; ++k) {}
        }
    };
    auto slot = [&](unsigned u) __attribute__((always_inline)) {
        const unsigned tn = lds_word(lb + L_CNT + 64) + PINIT + u * PACE / 16;
#pragma unroll 1
        for (int k = 0; k < 8192 && (int)(now() - tn) < 0; ++k) __builtin_amdgcn_s_sleep(1);
    };
    auto load_wo = [&](int k, int lane, bool early) __attribute__((always_inline)) {
        const int c = k * 64 + lane, pp = c / 10, cc = c - 10 * pp;
        const unsigned f = (unsigned)(swz(pp) * 1280 + cc * 16);
        const u16* wb = wo + (size_t)gi * DH * DM + gj * 80;
#pragma unroll
        for (int a2 = 0; a2 < 6; ++a2) { if (a2 % 3 == 0) { if (early) pace(3); else slot(NUNIT - WOFF + (2 * k + a2 / 3) * WOC); } dma16(wb + 32 * a2 * DM, f, lb + L_WO + (5 * a2 + k) * 1024); }
    };
    auto load_wd = [&](int lane) __attribute__((always_inline)) {
        const u16* db = wd + gi * 48 * DM + gj * 80;
#pragma unroll 1
        for (int k = 0; k < 5; ++k) {
            const int c = k * 64 + lane, pp = c / 10, cc = c - 10 * pp;
            const unsigned fd = (unsigned)(swz(pp) * 1280 + cc * 16);
            if (!(k & 1)) pace(3);
            dma16(db, fd, lb + L_WD + k * 1024);
            if (k < 3) dma16(db + 32 * DM, (k < 2 || lane < 32) ? fd : 0u, lb + L_WD + (5 + k) * 1024);
        }
    };
    auto load_gu = [&](int m, int lane) __attribute__((always_inline)) {
#pragma unroll 1
        for (int k0 = 0; k0 < 3; ++k0) {
            const int k = (k0 + (g & 7)) % 3, c = k * 64 + lane, row = c / 6, cc = c - 6 * row;
            const unsigned o = (unsigned)((swz(row) * FF + cc * 8) * 2), d = lb + L_GU + m * 8192 + k * 1024;
            const u16* wb = (m == 0 ? wg : wu) + gj * 80 * FF + gi * 48;
            pace(3);
            dma16(wb, o, d); dma16(wb + 32 * FF, o, d + 3072);
            if (k < 2) dma16(wb + 64 * FF, (k == 0 || lane < 32) ? o : 0u, d + 6144);
        }
    };
    auto lds_add = [&](int off, unsigned v) __attribute__((always_inline)) { asm volatile("s_mov_b64 exec, 1\n\tds_add_u32 %0, %1\n\ts_mov_b64 exec, -1" :: "v"(lb + L_CNT + off), "v"(v) : "memory"); };
    auto send_flag = [&](int lane) __attribute__((always_inline)) {
        st4_sc1(ws, WS_MB + (unsigned)((gi * 8 + ((gj == (gi & 7) ? lane : gi) & 7)) * 128 + gj * 4), lds_word(lb + L_CNT + 68));
    };
    auto tiles4 = [&](const Frag80 (&f)[4], const Frag80& b, unsigned long long mk) __attribute__((always_inline)) -> u32x4 {
        f32x4 c[4], d[4];
#pragma unroll
        for (int i = 0; i < 4; ++i) c[i] = mfma32(f[i].a0, b.a0, z4);
#pragma unroll
        for (int i = 0; i < 4; ++i) { c[i] = mfma32(f[i].a1, b.a1, c[i]); d[i] = mfma16(f[i].a2, b.a2, z4); }
        __builtin_amdgcn_sched_barrier(0);
        f32x2 lo, hi;
        asm volatile("s_nop 1\n\ts_mov_b64 exec, %18\n\tv_pk_add_f32 %0, %2, %10\n\tv_pk_add_f32 %1, %3, %11\n\t"
                     "s_lshl_b64 exec, exec, 1\n\tv_pk_add_f32 %0, %4, %12\n\tv_pk_add_f32 %1, %5, %13\n\t"
                     "s_lshl_b64 exec, exec, 1\n\tv_pk_add_f32 %0, %6, %14\n\tv_pk_add_f32 %1, %7, %15\n\t"
                     "s_lshl_b64 exec, exec, 1\n\tv_pk_add_f32 %0, %8, %16\n\tv_pk_add_f32 %1, %9, %17\n\ts_mov_b64 exec, -1"
                     : "=&v"(lo), "=&v"(hi) : "v"(LO2(c[0])), "v"(HI2(c[0])), "v"(LO2(c[1])), "v"(HI2(c[1])), "v"(LO2(c[2])), "v"(HI2(c[2])), "v"(LO2(c[3])), "v"(HI2(c[3])),
                       "v"(LO2(d[0])), "v"(HI2(d[0])), "v"(LO2(d[1])), "v"(HI2(d[1])), "v"(LO2(d[2])), "v"(HI2(d[2])), "v"(LO2(d[3])), "v"(HI2(d[3])), "s"(mk) : "scc");
        return asu(__builtin_shufflevector(lo, hi, 0, 1, 2, 3));
    };
    auto empty1 = [&](int half, unsigned S, int lane) __attribute__((always_inline)) {
        const unsigned o = WS_H1 + S + (unsigned)((gi * 4 + 2 * half) * 16384 + gj * 896 + min(lane, 55) * 16);
        st16_sc1(ws, o, (u32x4){~0u, ~0u, ~0u, ~0u}); st16_sc1(ws, o + 16384, (u32x4){~0u, ~0u, ~0u, ~0u});
    };
    auto report = [&]() __attribute__((always_inline)) { if (gj == (gi & 7)) st4_sc1(ws, WS_R + (unsigned)(gi * 4), lds_word(lb + L_CNT + 88)); };
    auto answer = [&](int lane, unsigned r) __attribute__((always_inline)) {
        int d = min(max((int)(r - (tst << 11)) >> 11, -400), 600);
        unsigned k = (r >> 5) & 63u, v = (r >> 1) & 15u;
        v += dppu<0x124>(v); v += dppu<0x128>(v);
#define VOTES(l) (__builtin_amdgcn_readlane(v, l) + __builtin_amdgcn_readlane(v, 16 + l))
        const unsigned c = (VOTES(0) > 32 ? 2u : 0u) | (VOTES(1) > 32 ? 4u : 0u) | (VOTES(2) + VOTES(3) > 64 ? 8u : 0u);
        d += (int)dppu<0xB1>((unsigned)d); d += (int)dppu<0x4E>((unsigned)d); d += (int)dppu<0x141>((unsigned)d); d += (int)dppu<0x140>((unsigned)d);
        k = max(k, dppu<0xB1>(k)); k = max(k, dppu<0x4E>(k)); k = max(k, dppu<0x141>(k)); k = max(k, dppu<0x140>(k));
        d = __builtin_amdgcn_readlane(d, 0) + __builtin_amdgcn_readlane(d, 16);
        k = max((unsigned)__builtin_amdgcn_readlane(k, 0), (unsigned)__builtin_amdgcn_readlane(k, 16));
        st4_sc1(ws, WS_MB + (unsigned)(((lane & 31) * 8 + (lane & 7)) * 128 + 32), (tst + (d >> 5) + TOFF) << 10 | k << 4 | c | 1u);
        lds_set(lb + L_CNT + 36, 1u);
    };
    auto adopt = [&](unsigned t) __attribute__((always_inline)) {
        const int dt = (int)(t - (tst << 10)) >> 10;
        if (dt > -256 && dt < 512) lds_set(lb + L_CNT + 64, tst + max(dt, (int)(lds_word(lb + L_CNT + 88) - (tst << 11) ) / 2048 - CL));
        lds_set(lb + L_CNT + 20, 4096u + ((t >> 4) & 63u) * BETA);
        lds_set(lb + L_CNT + 80, lds_word(lb + L_CNT + 92) ^ t);
        lds_set(lb + L_CNT + 84, (((t >> 1) ^ lds_word(lb + L_CNT + 68)) & 4u) << 11);
        lds_set(lb + L_CNT + 52, 1u);
    };
    struct StatAddr { unsigned a16, b16, a8, b8, dst; };
    auto stats_addr = [&](int lane, unsigned dbase, int dstride) __attribute__((always_inline)) -> StatAddr {
        const int kq = lane >> 4, q4 = (lane >> 2) & 3, p4 = lane & 3;
        const int x1 = LANES(0x4444444444444444ull) ? L_WR + gj * 160 : L_XS + q4 * 160, xa = LANES(0xaaaaaaaaaaaaaaaaull) ? L_XS + 640 : x1;
        StatAddr r{lb + (unsigned)(xa + kq * 16), lb + (unsigned)(L_XS + p4 * 160 + kq * 16), lb + (unsigned)(xa + 128 + kq * 8), lb + (unsigned)(L_XS + p4 * 160 + 128 + kq * 8), dbase + (unsigned)(kq * dstride)};
        PIN3(r.a16, r.b16, r.dst); PIN2(r.a8, r.b8);
        return r;
    };
    auto stats = [&](const StatAddr& r) __attribute__((always_inline)) {
        const bf16x8 a0 = lds16(r.a16), b0 = lds16(r.b16), a1 = lds16(r.a16 + 64), b1 = lds16(r.b16 + 64);
        const bf16x4 a2 = lds8(r.a8), b2 = lds8(r.b8);
        unsigned long long mk = 0x0008000400020001ull; PINS(mk);
        __builtin_amdgcn_sched_barrier(0);
        st16m(ws, r.dst, asu(mfma32(a1, b1, mfma32(a0, b0, z4)) + mfma16(a2, b2, z4)), mk);
    };
    unsigned qa, pw, ma, idx, qm;
    auto qk_addr = [&](bool L1) __attribute__((always_inline)) {
        LANE_VARS();
        qa = lb + L_Q + (unsigned)(kq * 32 + (lane & 2) * 8);
        pw = lb + L_P + (unsigned)((7 + 8 * wave) * 192 + (kq >> 1) * 64 + (n >> 3) * 32 + (lane & 2) * 8 + (q4 & 1) * 8 + (kq & 1) * 4 + (lane & 1) * 2);
        ma = lb + L_M + (unsigned)(wave * 4);
        idx = (lane & 2) ? 0xeeeeu : 0x4444u;
        qm = lb + L_Q;
        PIN3(qa, pw, ma); PIN2(idx, qm);
        if (L1 && wave < 4) __builtin_amdgcn_s_setprio(2);
    };
    auto att_qk0 = [&]() __attribute__((always_inline)) {
        qk_addr(false);
#pragma unroll 1
        for (unsigned spins = 0; spins < (kSpinCap << 4); ++spins) {
            if (lds_word(lb + L_CNT + 4) != 0u) break;
            __builtin_amdgcn_s_sleep(1);
        }
        STAMP(3);
        const u32x4 q0 = *(const LAS u32x4*)(size_t)qa, q1 = *(const LAS u32x4*)(size_t)(qa + 128), q2 = *(const LAS u32x4*)(size_t)(qa + 256);
        f32x4 c0 = z4, c1 = z4, c2 = z4, c3 = z4;
#define SMF(c, q, r) "v_smfmac_f32_16x16x64_bf16 %" #c ", %" #q ", v[" #r "], %7\n\t"
        asm volatile("s_nop 1\n\t" SMF(0, 4, 64:71) SMF(1, 4, 88:95) SMF(2, 4, 112:119) SMF(3, 4, 136:143) SMF(0, 5, 72:79) SMF(1, 5, 96:103) SMF(2, 5, 120:127) SMF(3, 5, 144:151)
                     SMF(0, 6, 80:87) SMF(1, 6, 104:111) SMF(2, 6, 128:135) SMF(3, 6, 152:159) "s_nop 7"
                     : "+v"(c0), "+v"(c1), "+v"(c2), "+v"(c3) : "v"(q0), "v"(q1), "v"(q2), "v"(idx));
#undef SMF
        const float s0 = c0[0] + c0[2], s1 = c1[0] + c1[2], s2 = c2[0] + c2[2], s3 = c3[0] + c3[2];
        float m = fmax2(fmax3(s0, s1, s2), s3);
        m = fmax2(m, dpp<0xB1>(m)); m = fmax2(m, dpp<0x4E>(m)); m = fmax2(m, dpp<0x124>(m)); m = fmax2(m, dpp<0x128>(m));
        const float s01 = LANES(0xffff0000ffff0000ull) ? s1 : s0, s23 = LANES(0xffff0000ffff0000ull) ? s3 : s2;
        const float sv = LANES(0xffffffff00000000ull) ? s23 : s01;
        m = bfr(bfr(m) * SCALE);
        const float p = ex2((bfr(bfr(sv) * SCALE) - m) * LOG2E);
        *(LAS u16*)(size_t)pw = (u16)pk2(p, p);
        *(LAS float*)(size_t)ma = m;
        __builtin_amdgcn_sched_barrier(0);
    };
    unsigned pr, oa, oc, cma, cxa, cra, cso;
    auto att_addr = [&]() __attribute__((always_inline)) {
        LANE_VARS();
        pr = lb + L_P + (unsigned)((7 + 8 * wave - 2 * q4 - (lane & 1)) * 192 + kq * 32 + (lane & 2) * 8);
        oa = lb + L_OP + (unsigned)(wave * 1024 + (lane & 7) * 64 + kq * 16 + (n >> 3) * 8);
        oc = lb + L_OP + (unsigned)(wave * 1024 + 512 + (kq >> 1) * 256 + (lane & 7) * 32 + (kq & 1) * 16 + (n >> 3) * 8);
        PIN3(pr, oa, oc);
    };
    auto comb_out = [&](float u0, float u1, unsigned so, unsigned long long mk, bool both) __attribute__((always_inline)) {
        const unsigned w = min(pk2(u0, u1), 0xfffffffeu);
        if (!both) { st4_sc1(ws, so, w); return; }
        const u32x4 w4 = {dppu<0x00>(w), dppu<0x55>(w), dppu<0xAA>(w), dppu<0xFF>(w)};
        st16m(ws, so, w4, mk);
    };
    auto max8 = [&](f32x4 m0, f32x4 m1) __attribute__((always_inline)) { return fmax3(fmax3(m0[0], m0[1], m0[2]), fmax3(m0[3], m1[0], m1[1]), fmax2(m1[2], m1[3])) * LOG2E; };
    auto comb_o0 = [&](f32x4 m0, f32x4 m1, f32x4 mm, f32x4 o0, f32x4 o1, f32x4 o2, f32x4 o3, unsigned so, unsigned long long mk, bool both) __attribute__((always_inline)) {
        const float M = max8(m0, m1);
        f32x4 f;
#pragma unroll
        for (int k = 0; k < 4; ++k) f[k] = ex2(__builtin_fmaf(mm[k], LOG2E, -M));
        const f32x4 o = f[3] * o3 + (f[2] * o2 + (f[1] * o1 + f[0] * o0));
        comb_out(rs32(o[0], o[2]), rs32(o[1], o[3]), so, mk, both);
    };
    auto comb_o1 = [&](f32x4 m0, f32x4 m1, f32x2 mm, f32x4 o0, f32x4 o1, unsigned so, unsigned long long mk, bool both) __attribute__((always_inline)) {
        const float M = max8(m0, m1);
        const f32x4 o = ex2(__builtin_fmaf(mm[1], LOG2E, -M)) * o1 + ex2(__builtin_fmaf(mm[0], LOG2E, -M)) * o0;
        comb_out(x16_add(rs32(o[0], o[2])), x16_add(rs32(o[1], o[3])), so, mk, both);
    };
    auto ml_addr = [&](int lane, unsigned& xa, unsigned& ra) __attribute__((always_inline)) {
        xa = lb + L_M + (unsigned)(((lane & 15) >> 1) * 4); ra = lb + L_P + (unsigned)((7 + 8 * ((lane & 15) >> 1)) * 192 + (lane & 1) * 64 + (lane >> 4) * 16);
    };
    auto comb_ml = [&](unsigned ma, unsigned xa, unsigned ra, unsigned so, bool both) __attribute__((always_inline)) {
        const u32x4 x4 = *(const LAS u32x4*)(size_t)ra;
        const f32x4 m0 = *(const LAS f32x4*)(size_t)ma, m1 = *(const LAS f32x4*)(size_t)(ma + 16);
        const float mw = *(const LAS float*)(size_t)xa;
        __builtin_amdgcn_sched_barrier(0);
        const f32x4 sp = mfma32u((u32x4){0x3f803f80u, 0x3f803f80u, 0x3f803f80u, 0x3f803f80u}, x4, z4);
        __builtin_amdgcn_sched_barrier(0);
        const float M = max8(m0, m1);
        float L = row16_sum(sp[0] * ex2(__builtin_fmaf(mw, LOG2E, -M)));
        if (both) L = dpp<0x00>(L);
        st16m(ws, so, asu((f32x4){M, L, M, L}), both ? 3ull : 1ull);
    };
    auto att_pv0 = [&]() __attribute__((always_inline)) {
        u32x4 bm[8];
        trans_v(IC<0>{});
        if (v_after(9) > v_after(7)) { trans_w(IC<0>{}); trans_w(IC<1>{}); }
        trans_v(IC<1>{});
#pragma unroll
        for (int e = 0; e < 8; ++e) bm[e] = *(const LAS u32x4*)(size_t)(pr + e * 192);
        __builtin_amdgcn_sched_barrier(0);
        f32x4 da = z4, dc = z4;
        PIN2(da, dc);
        asm volatile("s_nop 1");
        __builtin_amdgcn_sched_barrier(0);
        f32x2 r;
        smf<RV>(da, bm[0], idx); smf<RV + 8>(da, bm[1], idx); smf<RV + 16>(da, bm[2], idx); smf<RV + 24>(da, bm[3], idx);
        smf<RV + 32>(da, bm[4], idx); smf<RV + 40>(da, bm[5], idx); smf<RV + 48>(da, bm[6], idx); smf<RV + 56>(da, bm[7], idx);
        asm volatile("s_nop 7\n\tv_pk_add_f32 %0, %1, %2\n\tds_write_b64 %3, %0\n\ts_mov_b64 exec, 1\n\tds_add_u32 %4, %5\n\ts_mov_b64 exec, -1"
                     : "=&v"(r) : "v"(LO2(da)), "v"(HI2(da)), "v"(oa), "v"(lb + L_CNT + 24), "v"(1u) : "memory");
        GUARD(900);
        MARK(b_t5);
        if (v_after(9) < v_after(7)) { trans_w(IC<0>{}); trans_w(IC<1>{}); }
        trans_w(IC<2>{}); trans_w(IC<3>{});
        STAMP(4);
        asm volatile("s_nop 1\n\tv_smfmac_f32_16x16x64_bf16 %0, %1, v[224:231], %5\n\tv_smfmac_f32_16x16x64_bf16 %0, %2, v[232:239], %5\n\t"
                     "v_smfmac_f32_16x16x64_bf16 %0, %3, v[240:247], %5\n\tv_smfmac_f32_16x16x64_bf16 %0, %4, v[248:255], %5\n\ts_nop 6"
                     : "+v"(dc) : "v"(bm[0]), "v"(bm[1]), "v"(bm[2]), "v"(bm[3]), "v"(idx));
        asm volatile("s_nop 0\n\tv_pk_add_f32 %0, %1, %2\n\tds_write_b64 %3, %0" : "=&v"(r) : "v"(LO2(dc)), "v"(HI2(dc)), "v"(oc) : "memory");
        STAMP(20);
    };
    unsigned long long ck1 = 0xffff0000ffff0000ull, ck2 = 0xffffffff00000000ull, csc = 0x3d93cd3a3d93cd3aull;
    auto att1 = [&]() __attribute__((always_inline)) {
        constexpr int layer = 1;
        {
#define SMF(c, q, r) "v_smfmac_f32_16x16x64_bf16 v[" #c "], v[" #q "], v[" #r "], %0\n\t"
#define MAXD(x) "v_max_f32_dpp v41, v41, v41 " x " row_mask:0xf bank_mask:0xf\n\t"
            asm volatile("s_waitcnt lgkmcnt(0)\n\ts_barrier\n\t"
                         "ds_read_b128 v[0:3], %1\n\tds_read2_b32 v[12:13], %2 offset0:96 offset1:100\n\tds_read_b128 v[4:7], %1 offset:128\n\tds_read_b128 v[8:11], %1 offset:256\n\t"
                         "v_mov_b64 v[16:17], 0\n\tv_mov_b64 v[18:19], 0\n\tv_mov_b64 v[20:21], 0\n\tv_mov_b64 v[22:23], 0\n\tv_mov_b64 v[24:25], 0\n\tv_mov_b64 v[26:27], 0\n\t"
                         "v_mov_b64 v[28:29], 0\n\tv_mov_b64 v[30:31], 0\n\tv_mov_b64 v[32:33], 0\n\tv_mov_b64 v[34:35], 0\n\tv_mov_b64 v[36:37], 0\n\tv_mov_b64 v[38:39], 0\n\t"
                         "s_waitcnt lgkmcnt(3)\n\t" SMF(16:19, 0:3, 64:71) SMF(20:23, 0:3, 88:95) "s_waitcnt lgkmcnt(1)\n\tv_mul_f32 v13, 0x3acccccd, v13\n\t"
                         SMF(24:27, 0:3, 112:119) "v_mul_f32 v12, 0x3acccccd, v12\n\t" SMF(28:31, 0:3, 136:143) "v_fma_f32 v12, -v13, v13, v12\n\t"
                         SMF(16:19, 4:7, 72:79) "v_maximum3_f32 v12, v12, 0, 0\n\t" SMF(20:23, 4:7, 96:103) "v_cvt_pk_bf16_f32 v12, 0, v12\n\t"
                         SMF(24:27, 4:7, 120:127) "v_add_f32 v12, 0x3727c5ac, v12\n\t" SMF(28:31, 4:7, 144:151) "v_cvt_pk_bf16_f32 v12, 0, v12\n\ts_waitcnt lgkmcnt(0)\n\t"
                         SMF(16:19, 8:11, 80:87) "v_sqrt_f32 v12, v12\n\t" SMF(20:23, 8:11, 104:111) "v_cvt_pk_bf16_f32 v12, 0, v12\n\t"
                         SMF(24:27, 8:11, 128:135) "v_rcp_f32 v12, v12\n\t" SMF(28:31, 8:11, 152:159)
                         "s_nop 2\n\tv_add_f32 v42, v16, v18\n\ts_nop 0\n\tv_add_f32 v43, v20, v22\n\tv_max_f32 v41, v42, v43\n\tv_add_f32 v44, v24, v26\n\tv_add_f32 v45, v28, v30\n\t"
                         "v_max3_f32 v41, v41, v44, v45\n\tv_cndmask_b32 v42, v42, v43, %8\n\tv_cndmask_b32 v44, v44, v45, %8\n\t"
                         MAXD("quad_perm:[1,0,3,2]") "v_cndmask_b32 v40, v42, v44, %9\n\ts_nop 0\n\t" MAXD("quad_perm:[2,3,0,1]") "s_nop 1\n\t" MAXD("row_ror:4") "s_nop 1\n\t" MAXD("row_ror:8")
                         "v_pk_mul_f32 v[40:41], v[12:13], v[40:41] op_sel_hi:[0,1]\n\tv_cvt_pk_bf16_f32 v40, 0, v40\n\tv_cvt_pk_bf16_f32 v41, 0, v41\n\t"
                         "v_pk_mul_f32 v[40:41], %10, v[40:41]\n\tv_cvt_pk_bf16_f32 v40, 0, v40\n\tv_cvt_pk_bf16_f32 v41, 0, v41\n\t"
                         "v_sub_f32 v40, v40, v41\n\tv_mul_f32 v40, 0x3fb8aa3b, v40\n\tv_exp_f32 v40, v40\n\ts_nop 0\n\tv_cvt_pk_bf16_f32 v40, v40, v40\n\tds_write_b16 %3, v40\n\t"
                         "ds_read_b128 v[0:3], %4\n\tds_read_b128 v[4:7], %4 offset:192\n\tds_read_b128 v[8:11], %4 offset:384\n\tds_read_b128 v[12:15], %4 offset:576\n\t"
                         "ds_read_b128 v[16:19], %4 offset:768\n\tds_read_b128 v[20:23], %4 offset:960\n\tds_read_b128 v[24:27], %4 offset:1152\n\tds_read_b128 v[28:31], %4 offset:1344\n\t"
                         "ds_write_b32 %5, v41\n\t"
                         "s_waitcnt lgkmcnt(8)\n\t" SMF(32:35, 0:3, 160:167) SMF(36:39, 0:3, 224:231) "s_waitcnt lgkmcnt(7)\n\t" SMF(32:35, 4:7, 168:175) SMF(36:39, 4:7, 232:239)
                         "s_waitcnt lgkmcnt(6)\n\t" SMF(32:35, 8:11, 176:183) SMF(36:39, 8:11, 240:247) "s_waitcnt lgkmcnt(4)\n\t" SMF(32:35, 12:15, 184:191) SMF(36:39, 12:15, 248:255)
                         SMF(32:35, 16:19, 192:199) "s_waitcnt lgkmcnt(1)\n\t" SMF(32:35, 20:23, 200:207) SMF(32:35, 24:27, 208:215) SMF(32:35, 28:31, 216:223)
                         "s_nop 2\n\tv_pk_add_f32 v[36:37], v[36:37], v[38:39]\n\tds_write_b64 %7, v[36:37]\n\ts_nop 2\n\tv_pk_add_f32 v[32:33], v[32:33], v[34:35]\n\tds_write_b64 %6, v[32:33]"
                         :: "v"(idx), "v"(qa), "v"(qm), "v"(pw), "v"(pr), "v"(ma), "v"(oa), "v"(oc), "s"(ck1), "s"(ck2), "s"(csc)
                         : "memory", "v0", "v1", "v2", "v3", "v4", "v5", "v6", "v7", "v8", "v9", "v10", "v11", "v12", "v13", "v14", "v15", "v16", "v17", "v18", "v19", "v20", "v21", "v22", "v23",
                           "v24", "v25", "v26", "v27", "v28", "v29", "v30", "v31", "v32", "v33", "v34", "v35", "v36", "v37", "v38", "v39", "v40", "v41", "v42", "v43", "v44", "v45");
#undef SMF
#undef MAXD
            STAMP(20 + 32 * layer);
            BAR();
            if (__builtin_expect(wave == 0, 1)) {
                STAMP(5 + 32 * layer);
                comb_o0(*(const LAS f32x4*)(size_t)cma, *(const LAS f32x4*)(size_t)(cma + 16), *(const LAS f32x4*)(size_t)cxa, *(const LAS f32x4*)(size_t)cra,
                        *(const LAS f32x4*)(size_t)(cra + 1024), *(const LAS f32x4*)(size_t)(cra + 2048), *(const LAS f32x4*)(size_t)(cra + 3072), cso, 0x1111111111111111ull, false);
            }
            __builtin_amdgcn_sched_barrier(0);
            if (__builtin_expect(wave == 1, 1))
                comb_o1(*(const LAS f32x4*)(size_t)cma, *(const LAS f32x4*)(size_t)(cma + 16), *(const LAS f32x2*)(size_t)cxa, *(const LAS f32x4*)(size_t)cra, *(const LAS f32x4*)(size_t)(cra + 1024), cso, 0x0000111100001111ull, false);
            __builtin_amdgcn_sched_barrier(0);
            if (__builtin_expect(wave == 2, 1)) comb_ml(cma, cxa, cra, cso, false);
            MARK(e_q);
            if (wave < 3) STAMP(21 + 32 * layer);
            __builtin_amdgcn_s_setprio(0);
        }
    };

    unsigned nj = 0u, qS = 0u, fS = 0u, rS = 0u, nS = 0u; int ord;
    {
        const int tid = threadIdx.x, lane = tid & 63;
        asm volatile("ds_max_u32 %0, %1" :: "v"(lb + L_CNT + 64), "v"(tst) : "memory");
        unsigned mb = 0u;
        const u16* kbase = kc + wofs / 2;
        const u16* vbase = vc + wofs / 2;
        unsigned pt0 = 0u;
        if (wave == 7) {
            __builtin_amdgcn_s_setprio(3);
            if (lane < 10) *reinterpret_cast<u32x4*>(smem + L_XS + 640 + lane * 16) = (u32x4){0x3f803f80u, 0x3f803f80u, 0x3f803f80u, 0x3f803f80u};
            if (lane < 16) { *reinterpret_cast<u32x4*>(smem + L_CS + 768 + lane * 16) = (u32x4){0u, 0u, 0u, 0u}; }
        } else {
            if (wave >= 4) {
                if (wave == 4) st16_sc1(ws, WS_OWN + (unsigned)(gi * 16384 + (lane >> 5) * 8192 + gj * 512 + (lane & 31) * 16), (u32x4){~0u, ~0u, ~0u, ~0u});
                else empty5(0, (unsigned)(wave - 5) * 8192u, lane);
                if (wave == 6) {
                    __builtin_amdgcn_s_setprio(3);
                    if (gi + gj == 0) st4_sc1(ws, WS_R + (lane & 31) * 4, 0u);
                    st4_sc1(ws, WS_MB + (unsigned)((gi * 8 + gj) * 128 + (lane & 31) * 4), 0u);
                }
            } else if (wave >= 2) {
                unsigned long long c0, c1;
                asm volatile("s_waitcnt lgkmcnt(0)\n\ts_memtime %0\n\ts_waitcnt lgkmcnt(0)\n\ts_load_dword %2, %3, %4\n\ts_waitcnt lgkmcnt(0)\n\ts_memtime %1\n\ts_waitcnt lgkmcnt(0)"
                             : "=&s"(c0), "=&s"(c1), "=&s"(pt0) : "s"(ws), "s"((unsigned)(min(gi & 3, 2) << 21) + ((((tst + g * 977u) * 2654435761u) >> 11 & 0x1fdf80u) | ((wave ^ gj) & 1) * 8192)) : "memory");
                pt0 = ((wave ^ gj) & 1) ? (unsigned)c0 - (unsigned)c1 : (unsigned)c1 - (unsigned)c0;
            }
            STAMP(36);
            *reinterpret_cast<u32x4*>(smem + L_P + tid * 16) = (u32x4){0u, 0u, 0u, 0u};
            if (tid < 72 * 12 - 448) *reinterpret_cast<u32x4*>(smem + L_P + 7168 + tid * 16) = (u32x4){0u, 0u, 0u, 0u};
            if (wave >= 4) {
                if (wave == 6) VMW(2); else VMW(1);
                asm volatile("ds_max_u32 %0, %1" :: "v"(lb + L_CNT + 64), "v"(now() - TLAND) : "memory");
                STAMP(40);
                lds_add(0, 1u);
            } else if (wave >= 2) { lds_add(20, pt0); VMW(0); lds_add(0, 1u); }
        }
        asm volatile("s_waitcnt lgkmcnt(0)" :: "s"(pt1) : "memory");
        const int rot = gj + wave;
        const unsigned r0 = now(), c0 = (unsigned)__builtin_amdgcn_s_memtime();
        u32x4 a[4];
        unsigned ko = (unsigned)((wave * 64 + (lane & 7)) * 384 + (((lane >> 3) & 1) * 4 + (lane >> 4)) * 16);
        PIN1(ko);
        auto k_group = [&](auto GC_) __attribute__((always_inline)) {
            constexpr int G = decltype(GC_)::value, R = RK + 24 * (G >> 1) + 4 * (G & 1);
            const unsigned o = ko + (unsigned)((((G >> 1) + rot) & 3) * 6144 + (G & 1) * 3072);
            pace(3);
            ld16_nt<R, 0>(kbase, o); ld16_nt<R + 8, 128>(kbase, o); ld16_nt<R + 16, 256>(kbase, o);
            const int lane = lane_id();
            const unsigned ho = WS_Q0 + (unsigned)((gi * 4 + (gj >> 1)) * 4096 + lane * 16);
            if (wave < 7) {
                asm volatile("; COND_BEGIN");
                if (G == 0 && wave >= 4) { VMW(3); lds_add(32, 1u); }
                if (G == 0) {
                    if (wave < 2) { VMW(3); lds_add(0, 1u); load_gu(wave, lane); }
                    if (wave == 2) load_wd(lane);
                    if (wave == 3) {
#pragma unroll 1
                        for (int k = 5 - WEARLY; k < 5; ++k) load_wo(k, lane, true);
                    }
                }
                asm volatile("; COND_END");
            }
            if (wave == 7) {
                asm volatile("; COND_BEGIN");
                if (G == GP) {
                    VMW(3 * (GP + 1));
#pragma unroll 1
                    for (unsigned spins = 0; spins < (kSpinCap << 4); ++spins) {
                        if (lds_word(lb + L_CNT) >= 7u) break;
                        __builtin_amdgcn_s_sleep(1);
                    }
                    STAMP(26);
                    const unsigned char* p = smem + L_X0 + (lane >> 4) * 1280;
                    const float c0 = lo16(*reinterpret_cast<const u16*>(p));
                    const f32x2 c2 = {c0, c0};
                    f32x2 s2 = {0.f, 0.f}, q2 = s2;
#pragma unroll
                    for (int k = 0; k < 5; ++k) {
                        const u32x4 w = *reinterpret_cast<const u32x4*>(p + k * 256 + (lane & 15) * 16);
#pragma unroll
                        for (int e = 0; e < 4; ++e) { const f32x2 d = unpk(w[e]) - c2; s2 += d; q2 += d * d; }
                    }
                    const float s = row16_sum(s2[0] + s2[1]), qq = row16_sum(q2[0] + q2[1]);
                    const float ms = s * (1.f / DM);
                    const unsigned mv = pk2(c0 + ms, fmax2(qq - s * ms, 0.f) * (1.f / DM));
                    const float mb = lo16(mv), rden = frcp(bfr(__builtin_amdgcn_sqrtf(bfr(hi16(mv) + 1e-5f))));
                    const f32x2 mb2 = {mb, mb}, rd2 = {rden, rden};
                    if ((lane & 15) < 10) {
                        u32x4 w = *reinterpret_cast<const u32x4*>(p + gj * 160 + (lane & 15) * 16);
#pragma unroll
                        for (int e = 0; e < 4; ++e) w[e] = pk2v(unpk(pk2v(unpk(w[e]) - mb2)) * rd2);
                        *reinterpret_cast<u32x4*>(smem + L_XS + (lane >> 4) * 160 + (lane & 15) * 16) = w;
                    }
                    STAMP(30);
                }
                if (G == GP || G == GA) {
#pragma unroll 1
                    for (int blk = 0; blk < 3; ++blk) {
                        LANE_VARS();
                        const unsigned xb = lb + L_XS + (unsigned)((G == GP ? q4 : 4) * 160);
                        Frag80 f[4];
#pragma unroll
                        for (int i = 0; i < 4; ++i) f[i] = frag80(lb + L_WQ + (unsigned)(128 * blk + 32 * i + 8 * p4), 416, r0s, r1s, r2s);
                        const Frag80 b = rows80(xb + kq * 16, xb + 128 + kq * 8);
                        const u32x4 c = tiles4(f, b, 0x1111111111111111ull);
                        if (G == GP) *reinterpret_cast<u32x4*>(smem + L_OP + q4 * 768 + 256 * blk + p4 * 64 + kq * 16) = c;
                        else *reinterpret_cast<u32x4*>(smem + L_CS + 256 * blk + kq * 64 + p4 * 16) = c;
                    }
                    if (G == GP) {
                        asm volatile("" ::: "memory");
                        const int ln = lane_id();
                        f32x2 c[6];
                        unsigned m = 20u;
#pragma unroll
                        for (int k = 0; k < 6; ++k) {
                            c[k] = *reinterpret_cast<const f32x2*>(smem + L_OP + (k / 3) * 1536 + ln * 24 + (k % 3) * 8);
                            m = max(m, max(fexp(c[k][0]), fexp(c[k][1])));
                        }
                        m = max(m, dppu<0xB1>(m)); m = max(m, dppu<0x4E>(m)); m = max(m, dppu<0x141>(m)); m = max(m, dppu<0x140>(m));
                        m = max(max((unsigned)__builtin_amdgcn_readlane(m, 0), (unsigned)__builtin_amdgcn_readlane(m, 16)), max((unsigned)__builtin_amdgcn_readlane(m, 32), (unsigned)__builtin_amdgcn_readlane(m, 48)));
                        lds_set(lb + L_CNT + 76, m);
                        const float f = __uint_as_float((273u - m) << 23);
#pragma unroll
                        for (int k = 0; k < 2; ++k) {
                            const u32x2 w0 = enc21(c[3 * k][0] * f, c[3 * k][1] * f, c[3 * k + 1][0] * f), w1 = enc21(c[3 * k + 1][1] * f, c[3 * k + 2][0] * f, c[3 * k + 2][1] * f);
                            st16_sc1(ws, WS_Q0 + (unsigned)((gi * 4 + 2 * k + (ln >> 5)) * 4096 + gj * 512 + (ln & 31) * 16), (u32x4){w0[0], w0[1], w1[0], w1[1]});
                        }
                        STAMP(2);
                        WAIT1(pf, 3 * (GP + 1) + 2);
                    }
                }
                if (G == GA) {
                    const unsigned half = __builtin_amdgcn_s_getreg(2 << 6 | 20);
                    lds_set(lb + L_CNT + 68, lds_word(lb + L_CNT + 64) << 11 | lds_word(lb + L_CNT + 76) << 3 | 1u | (((int)lds_word(lb + L_CNT + 20) > 0) ^ half) << 1 | half << 2);
                    VMW(3 * (GA - GP));
#pragma unroll 1
                    for (unsigned spins = 0; spins < (kSpinCap << 4); ++spins) if (lds_word(lb + L_CNT + 32) >= 3u) break;
                    send_flag(lane_id());
                    lds_set(lb + L_CNT + 8, 1u);
                    STAMP(17);
                    f32x2 r2 = {0.f, 0.f};
#pragma unroll
                    for (int k = 0; k < 5; ++k) r2 += unpk(*reinterpret_cast<const unsigned*>(smem + L_WR + k * 256 + lane_id() * 4));
                    *reinterpret_cast<float*>(smem + L_CNT + 12) = wave_sum(r2[0] + r2[1]);
                }
                asm volatile("; COND_END");
            }
            if (wave == 6) {
                asm volatile("; COND_BEGIN");
                if (G == GB - 1) mb = ld4_sc1(ws, WS_MB + (unsigned)((gi * 8 + gj) * 128 + (gi & 7) * 4));
                if (G == GB) {
                    WAIT1(mb, 3);
#pragma unroll 1
                    for (unsigned spins = 0; spins < kSpinCap && !all_set(mb); ++spins) {
                        if ((spins & 7u) == 7u && lds_word(lb + L_CNT + 8)) send_flag(lane);
                        mb = ld4_sc1(ws, WS_MB + (unsigned)((gi * 8 + gj) * 128 + (gi & 7) * 4));
                        WAIT1(mb, 0);
                    }
                    mb = ld4_sc1(ws, WS_MB + (unsigned)((gi * 8 + (gi & 7)) * 128 + (lane & 7) * 4));
                }
                if (G == GC) {
                    WAIT1(mb, 3 * (GC - GB));
#pragma unroll 1
                    for (unsigned spins = 0; spins < kSpinCap && !all_set(mb); ++spins) {
                        if ((spins & 7u) == 7u && lds_word(lb + L_CNT + 8)) send_flag(lane);
                        mb = ld4_sc1(ws, WS_MB + (unsigned)((gi * 8 + (gi & 7)) * 128 + (lane & 7) * 4));
                        WAIT1(mb, 0);
                    }
                    lds_set(lb + L_CNT + 92, (__builtin_amdgcn_readlane(mb, gi & 7) & 4u) ? 14u : 0u);
                    int d = min(max((int)(mb - (tst << 11)) >> 11, -400), 600);
                    d += (int)dppu<0xB1>((unsigned)d); d += (int)dppu<0x4E>((unsigned)d); d += (int)dppu<0x141>((unsigned)d);
                    const unsigned tm = tst + (unsigned)(__builtin_amdgcn_readfirstlane(d) >> 3);
                    lds_set(lb + L_CNT + 64, tm);
                    const float slow = (float)(now() - r0) * 24.f * 256.f * frcp((float)((unsigned)__builtin_amdgcn_s_memtime() - c0)) - 256.f;
                    lds_set(lb + L_CNT + 88, tm << 11 | (unsigned)__builtin_amdgcn_fmed3f(slow, 0.f, 63.f) << 5 | __builtin_popcount((unsigned)__builtin_amdgcn_ballot_w64((mb & 2u) != 0u) & 255u) << 1 | 1u);
                    report();
                    a[0] = ld16_sc1<0>(ws, ho); a[1] = ld16_sc1<1024>(ws, ho); a[2] = ld16_sc1<2048>(ws, ho); a[3] = ld16_sc1<3072>(ws, ho);
                    pace(XCOST, false);
                    STAMP(31);
                }
                asm volatile("; COND_END");
            } else if (G == GC) { for (int k = 0; k < 4; ++k) asm volatile("" : "=v"(a[k])); }
        };
        k_group(IC<0>{}); k_group(IC<1>{}); k_group(IC<2>{}); k_group(IC<3>{}); k_group(IC<4>{}); k_group(IC<5>{}); k_group(IC<6>{}); k_group(IC<7>{});
        {
            const int lane = lane_id();
            const unsigned v0 = (unsigned)((wave * 64 + 4 * (lane >> 4)) * 384 + (lane & 15) * 16);
            const unsigned v1 = v0 + 256 - (unsigned)((lane & 15) * 8);
            auto v_group = [&](auto HC) __attribute__((always_inline)) {
                constexpr int h = decltype(HC)::value, p = kVord[h], R = p < 8 ? RV + 16 * (p & 3) + 4 * (p >> 2) : RV + 64 + 2 * (p - 8), NV = v_upto(h);
                const unsigned o = (p < 8 ? v0 : v1) + (unsigned)((((p < 8 ? p >> 1 : p) + rot) & 3) * 6144);
                pace(2);
                if (p < 8) { ld16_nt<R, (p & 1) * 768>(vbase, o); ld16_nt<R + 8, (p & 1) * 768 + 384>(vbase, o); }
                else { ld8_nt<R, 0>(vbase, o); ld8_nt<R + 8, 384>(vbase, o); ld8_nt<R + 16, 768>(vbase, o); ld8_nt<R + 24, 1152>(vbase, o); }
                if (wave == 7 && gi + gj == 0) {
                    asm volatile("; COND_BEGIN");
                    if (h == 3 || h == 6) mb = ld4_sc1(ws, WS_R + (unsigned)((lane & 31) * 4));
                    if (h == 4 || h == 7) {
                        WAIT1(mb, NV - v_upto(h - 1));
                        if (all_set(mb) && !lds_word(lb + L_CNT + 36)) answer(lane, mb);
                    }
                    asm volatile("; COND_END");
                }
                if (wave == 6) {
                    asm volatile("; COND_BEGIN");
                if (h == HD) {
                    asm volatile("s_waitcnt vmcnt(%4)" : "+v"(a[0]), "+v"(a[1]), "+v"(a[2]), "+v"(a[3]) : "i"(3 * (7 - GC) + NV) : "memory");
                    float sq[6] = {0.f, 0.f, 0.f, 0.f, 0.f, 0.f};
#pragma unroll
                    for (int k = 0; k < 4; ++k) {
                        const float f = __uint_as_float(((((unsigned)__builtin_amdgcn_ds_bpermute((2 * k + (lane >> 5)) * 4, (int)mb) >> 3) & 255u) - 19u) << 23);
#pragma unroll
                        for (int e = 0; e < 2; ++e) {
                            const unsigned lo = a[k][2 * e], hi = a[k][2 * e + 1];
                            const float fe = (int)hi < 0 ? __builtin_nanf("") : f;
                            sq[3 * e] += (float)((int)(lo << 11) >> 11) * fe;
                            sq[3 * e + 1] += (float)((int)(__builtin_amdgcn_alignbit(hi, lo, 21) << 11) >> 11) * fe;
                            sq[3 * e + 2] += (float)((int)(hi << 1) >> 11) * fe;
                        }
                    }
#pragma unroll
                    for (int e = 0; e < 3; ++e) {
                        const int pi = 3 * (lane & 31) + e;
                        *reinterpret_cast<unsigned*>(smem + L_Q + (pi >> 3) * 32 + (pi & 6) * 2 + (pi & 1) * 16) = pk2(x32_add(sq[2 * e]), x32_add(sq[2 * e + 1]));
                    }
                    asm volatile("s_waitcnt lgkmcnt(0)\n\tds_write_b32 %0, %1" :: "v"(lb + L_CNT + 4), "v"(1u) : "memory");
                    STAMP(27);
                    lead = LEAD * PACE * 4;
                }
                    if (h == 6) mb = ld4_sc1(ws, WS_MB + (unsigned)((gi * 8 + (gi & 7)) * 128 + 32));
                    if (h == 8) {
                        WAIT1(mb, NV - v_upto(6));
                        const unsigned r = __builtin_amdgcn_readfirstlane(mb);
                        if (r) { adopt(r); STAMP(29); }
                    }
                    asm volatile("; COND_END");
                }
                if constexpr (h < 6) {
                    constexpr int i = 2 * h;
                    fix_k1(IC<i>{}, IC<20 - 6 * (i / 3) - i % 3 + NV>{});
                    fix_k1(IC<i + 1>{}, IC<20 - 6 * ((i + 1) / 3) - (i + 1) % 3 + NV>{});
                }
                if (h == 5) {
                    STAMP(1);
                    __builtin_amdgcn_s_setprio(0);
                    att_qk0();
                }
            };
            v_group(IC<0>{}); v_group(IC<1>{}); v_group(IC<2>{}); v_group(IC<3>{}); v_group(IC<4>{}); v_group(IC<5>{});
            v_group(IC<6>{}); v_group(IC<7>{}); v_group(IC<8>{}); v_group(IC<9>{}); v_group(IC<10>{}); v_group(IC<11>{});
            lds_add(16, 1u);
            if (wave == 3) lds_add(44, WEARLY);
        }
    }
    {
        STAMP(28);
        att_addr();
        const int lane = lane_id();
        cma = lb + L_M; cxa = lb + L_M + (unsigned)((lane >> 4) * 8); cra = lb + L_OP + (unsigned)(512 + (lane >> 4) * 2048 + (lane & 15) * 16);
        cso = WS_OWN + (unsigned)(gi * 16384 + gj * 512 + 256 + (lane >> 5) * 64 + (lane & 12) * 4 + (lane & 1) * 8192);
        PIN2(cma, cxa); PIN2(cra, cso);
        att_pv0();
    }
    {
        f32x4 m0, m1, o0, o1; f32x2 mm; unsigned r, taken;
        asm volatile("s_mov_b64 exec, 1\n\tds_add_rtn_u32 %5, %7, %8 offset:%11\n\ts_mov_b64 exec, -1\n\t"
                     "ds_read_b128 %0, %7\n\tds_read_b128 %1, %7 offset:16\n\tds_read_b64 %2, %9\n\tds_read_b128 %3, %10\n\tds_read_b128 %4, %10 offset:1024\n\t"
                     "ds_read_b32 %6, %7 offset:%12\n\ts_waitcnt lgkmcnt(0)"
                     : "=&v"(m0), "=&v"(m1), "=&v"(mm), "=&v"(o0), "=&v"(o1), "=&v"(r), "=&v"(taken)
                     : "v"(cma), "v"(1u), "v"(cxa), "v"(cra), "i"(L_CNT + 28 - L_M), "i"(L_CNT + 40 - L_M) : "memory");
        ord = __builtin_amdgcn_readfirstlane(r);
        if (ord == 7) { STAMP(5); comb_o1(m0, m1, mm, o0, o1, cso, 0x0000333300003333ull, true); lds_set(lb + L_CNT + 48, now() | 1u); STAMP(21); }
        MARK(e_t5);
        if (ord <= OML + 1 || (unsigned)__builtin_amdgcn_readfirstlane(taken) < 5u - WEARLY) {
        const int lane = lane_id();
        auto wait_cnt = [&](int off, unsigned v) __attribute__((always_inline)) {
#pragma unroll 1
            for (unsigned spins = 0; spins < (kSpinCap << 4); ++spins) { if (lds_word(lb + L_CNT + off) >= v) break; __builtin_amdgcn_s_sleep(1); }
        };
        if (ord == OML) {
            wait_cnt(16, 8u);
            unsigned xa, ra; ml_addr(lane, xa, ra);
            comb_ml(cma, xa, ra, WS_OWN + (unsigned)(gi * 16384 + gj * 512 + 384 + (lane & 1) * 8192), true);
            STAMP(24);
        } else if (ord == 0 && !lds_word(lb + L_CNT + 52)) {
            if (gi + gj == 0 && !lds_word(lb + L_CNT + 36)) {
                unsigned r = 0u;
#pragma unroll 1
                for (unsigned spins = 0; spins < kSpinCap; ++spins) {
                    r = ld4_sc1(ws, WS_R + (unsigned)((lane & 31) * 4));
                    WAIT1(r, 0);
                    if (all_set(r)) break;
                    if ((spins & 7u) == 7u) send_flag(lane);
                }
                answer(lane, r);
            }
#pragma unroll 1
            for (unsigned spins = 0; spins < kSpinCap; ++spins) {
                unsigned r = ld4_sc1(ws, WS_MB + (unsigned)((gi * 8 + (gi & 7)) * 128 + 32));
                WAIT1(r, 0);
                r = __builtin_amdgcn_readfirstlane(r);
                if (r) { adopt(r); break; }
                if ((spins & 7u) == 7u) { send_flag(lane); report(); }
                __builtin_amdgcn_s_sleep(16);
            }
            STAMP(29);
        } else if (ord == OML + 1) {
#pragma unroll 1
            for (unsigned spins = 0; spins < (kSpinCap << 6); ++spins) if (lds_word(lb + L_CNT + 24) >= 8u) break;
            const unsigned ra = lb + L_OP + (unsigned)((lane >> 5) * 4096 + (lane & 31) * 16);
            comb_o0(*(const LAS f32x4*)(size_t)cma, *(const LAS f32x4*)(size_t)(cma + 16), *(const LAS f32x4*)(size_t)(cma + (lane >> 5) * 16), *(const LAS f32x4*)(size_t)ra, *(const LAS f32x4*)(size_t)(ra + 1024),
                    *(const LAS f32x4*)(size_t)(ra + 2048), *(const LAS f32x4*)(size_t)(ra + 3072), WS_OWN + (unsigned)(gi * 16384 + gj * 512 + (lane & 60) * 4 + (lane & 1) * 8192), 0x3333333333333333ull, true);
            STAMP(22);
        }
        if (ord == OML) {
#pragma unroll 1
            for (unsigned spins = 0; spins < (kSpinCap << 8); ++spins) { if (lds_word(lb + L_CNT + 52)) break; __builtin_amdgcn_s_sleep(1); }
            const unsigned S = (lds_word(lb + L_CNT + 80) & 4u) << 11;
            empty4(0, S, lane); empty5(5, 0u, lane); empty5(5, 8192u, lane);
        }
#pragma unroll 1
        for (;;) {
            unsigned j;
            asm volatile("s_mov_b64 exec, 1\n\tds_add_rtn_u32 %0, %1, %2\n\ts_mov_b64 exec, -1\n\ts_waitcnt lgkmcnt(0)" : "=&v"(j) : "v"(lb + L_CNT + 40), "v"(1u) : "memory");
            j = __builtin_amdgcn_readfirstlane(j);
            if (j >= 5u - WEARLY) break;
            load_wo((int)j, lane, false); ++nj;
        }
        }
        STAMP(23);
    }

    unsigned omr, owa, owb;
    auto op_addr = [&](int layer) __attribute__((always_inline)) {
        LANE_VARS();
        const int t = wave - 3;
        cma = WS_OWN + rS + (unsigned)(gi * 16384 + layer * 4096 + (lane >> 3) * 512 + t * 128 + (t == 3 ? 0 : (lane & 7) * 16));
        cxa = lb + L_O2 + (unsigned)((lane >> 3) * 416 + t * 128 + (t == 3 ? 0 : (lane & 7) * 16));
        const int pb = (lane & 4) ? 3 - p4 : p4, jp = 2 * pb + ((lane >> 2) & 1);
        cso = tile35(gj, 10 * layer + t) + (unsigned)((kq & 1) * 8192 + gi * 128 + (kq >> 1) * 64 + p4 * 16);
        cra = lb + L_O2 + (unsigned)(jp * 416 + kq * 16); omr = lb + L_O2 + (unsigned)(jp * 416 + 384);
        owa = lb + L_WO + (unsigned)(r0s * 160 + 32 * t + 8 * p4); owb = lb + L_WO + (unsigned)(r1s * 160 + 32 * t + 8 * p4);
        if (layer) PIN2(cma, cxa);
        PIN2(cso, cra); PIN3(omr, owa, owb);
    };
    auto chain = [&](auto LC) __attribute__((always_inline)) {
        constexpr int layer = decltype(LC)::value;
        unsigned h4 = WS_H4 + (unsigned)((layer * 32 + gi) * 32768) + (layer ? fS : 0u);
        auto sides = [&]() __attribute__((always_inline)) {
            const unsigned m = lds_word(lb + L_CNT + 80);
            qS = (m & 2u) << 12; fS = (m & 4u) << 11; rS = (m & 8u) << 10; nS = lds_word(lb + L_CNT + 84); h4 += fS;
        };
        if (wave < 3) {
            LANE_VARS();
            if (layer == 0) {
                unsigned lo = WS_OWN + lds_word(lb + L_CNT + 84) + (unsigned)(gi * 16384 + (lane >> 3) * 512), lw = lb + L_O2 + (unsigned)((lane >> 3) * 416);
                unsigned lo0 = lo + (unsigned)((lane & 7) * 16), lw0 = lw + (unsigned)((lane & 7) * 16);
                PIN2(lo0, lo); PIN2(lw0, lw);
                GUARD(400);
                MARK(b_g2);
                if (__builtin_expect(wave == 2, 0)) { if (nj | (ord == OML)) { VMW(0); lds_add(44, nj); } }
                else if (__builtin_expect(wave == 1, 0)) {
#pragma unroll 1
                    for (unsigned spins = 0; spins < (kSpinCap << 4); ++spins) if (lds_word(lb + L_CNT + 48)) break;
#pragma unroll 1
                    for (unsigned spins = 0; spins < kSpinCap; ++spins) {
                        u32x4 o0 = ld16_sc1<0>(ws, lo0), o1 = ld16_sc1<128>(ws, lo0), o2 = ld16_sc1<384>(ws, lo);
                        if (nj) { VMW(3); lds_add(44, nj); nj = 0u; }
                        asm volatile("s_waitcnt vmcnt(0)" : "+v"(o0), "+v"(o1), "+v"(o2) :: "memory");
                        *(LAS u32x4*)(size_t)lw0 = o0; *(LAS u32x4*)(size_t)(lw0 + 128) = o1; *(LAS u32x4*)(size_t)(lw + 384) = o2;
                        if (!any_empty(max(max(um4(o0), um4(o1)), um4(o2)))) break;
                    }
                } else {
                    unsigned long long miss; unsigned cnt = kSpinCap << 4, tf, nv = nj;
                    asm volatile("1:\n\tds_read_b32 v4, %4 offset:48\n\ts_sub_u32 %1, %1, 1\n\ts_cbranch_scc1 5f\n\ts_waitcnt lgkmcnt(0)\n\tv_readfirstlane_b32 %2, v4\n\ts_cmp_eq_u32 %2, 0\n\ts_cbranch_scc1 1b\n\t"
                                 "s_add_u32 %2, %2, %8\n"
                                 "2:\n\ts_memrealtime vcc\n\ts_waitcnt lgkmcnt(0)\n\ts_sub_u32 vcc_lo, %2, vcc_lo\n\ts_cmp_lt_u32 vcc_lo, 0x400\n\ts_cbranch_scc1 2b\n"
                                 "5:\n\ts_mov_b32 %1, 15\n"
                                 "3:\n\tglobal_load_dwordx4 v[0:3], %5, %6 offset:256 sc1\n\ts_waitcnt vmcnt(1)\n\ts_mov_b64 exec, 1\n\tds_add_u32 %4, %3 offset:44\n\ts_mov_b64 exec, -1\n\tv_mov_b32 %3, 0\n\t"
                                 "s_sub_u32 %1, %1, 1\n\ts_cselect_b64 vcc, 0, -1\n\ts_waitcnt vmcnt(0)\n\tds_write_b128 %7, v[0:3] offset:256\n\tv_max3_u32 v4, v0, v1, v2\n\tv_max_u32 v4, v4, v3\n\t"
                                 "v_cmp_eq_u32 %0, -1, v4\n\ts_and_b64 vcc, vcc, %0\n\ts_cbranch_scc1 3b"
                                 : "=&s"(miss), "+s"(cnt), "=&s"(tf), "+v"(nv) : "v"(lb + L_CNT), "v"(lo0), "s"(ws), "v"(lw0), "i"(DG2 - 1) : "memory", "vcc", "scc", "v0", "v1", "v2", "v3", "v4");
                    if (__builtin_expect(miss != 0ull, 0)) {
#pragma unroll 1
                        for (unsigned spins = 0; spins < kSpinCap; ++spins) {
                            if ((spins & 15u) == 0u) send_flag(lane);
                            u32x4 o0 = ld16_sc1<256>(ws, lo0);
                            WAIT1(o0, 0);
                            *(LAS u32x4*)(size_t)(lw0 + 256) = o0;
                            if (!any_empty(um4(o0))) break;
                        }
                    }
                }
                MARK(e_g2);
                STAMP(25);
                BAR();
                sides();
                if (wave < 2) empty1(wave, qS, lane);
                else {
                    st16m(ws, WS_CS + (unsigned)((gi * 8 + gj) * 768 + lane * 16), *reinterpret_cast<const u32x4*>(smem + L_CS + lane * 16), 0xffffffffffffull);
                    empty5(10, 0u, lane); empty5(10, 8192u, lane);
                }
            } else BAR();
            {
                const unsigned im = lb + L_GU + (unsigned)(32 * wave + 8 * p4);
                Frag80 fg = frag80(im, 96, r0s, r1s, r2s), fu = frag80(im + 8192, 96, r0s, r1s, r2s);
                const unsigned xb = lb + L_XS + (unsigned)((n < 4 ? n : 4) * 160);
                unsigned b16 = xb + kq * 16, b8 = xb + 128 + kq * 8;
                unsigned sg = h4 + (unsigned)(n < 4 ? h4s(wave) + gj * 512 + kq * 64 + n * 16 : H4C + wave * 1024 + gj * 128 + kq * 16), su = sg + (n < 4 ? 256u : 64u);
                unsigned o = h4 + h4s(wave) + (unsigned)(lane * 16);
                unsigned oc = h4 + (unsigned)(H4C + wave * 1024 + lane * 16), pc = (unsigned)((kq * 16 + q4) * 4);
                unsigned os = h4 + (unsigned)(H4T + (q4 + 4 * (kq & 1)) * 128 + p4 * 16);
                unsigned ha = lb + L_H + (unsigned)(p4 * 96 + wave * 32 + q4 * 8 + kq * 2);
                f32x2 cs = {0.f, 0.f};
                if (layer != 0) cs = *reinterpret_cast<const f32x2*>(smem + L_CS2 + wave * 512 + lane * 8);
                PIN3(fg.a0, fg.a1, fg.a2); PIN3(fu.a0, fu.a1, fu.a2); PIN3(b16, b8, sg); PIN3(su, o, os); PIN2(ha, cs);
                unsigned long long mk = layer ? 0x000f000f000f000full : 0x001f001f001f001full;
                unsigned ta = WS_MB + (unsigned)((lane >> 1) * 1024 + 64 + (lane & 1) * 16);
                if (layer == 0) PIN3(oc, ta, pc);
                unsigned x4a = layer ? 0u : X4A * lds_word(lb + L_CNT + 20) >> 12;
                unsigned long long w0 = __builtin_amdgcn_ballot_w64(wave == 0);
                PINS(x4a); PINS(w0);
                unsigned long long rdm = 0x3acccccd3acccccdull;
                PINS(mk); PINS(rdm);
                if (layer == 0) VMW(0);
                if (layer == 0) GUARD(1200); else GUARD(800);
                MARK(b_gu);
                BAR();
                STAMP(10 + 32 * layer);
                u32x4 tl = {0u, 0u, 0u, 0u};
                if (layer == 0) asm volatile("s_mov_b64 exec, %3\n\tglobal_load_dwordx4 %0, %1, %2 sc1\n\ts_mov_b64 exec, -1" : "+v"(tl) : "v"(ta), "s"(ws), "s"(w0) : "memory");
                const Frag80 b = rows80(b16, b8);
                const f32x4 g0 = mfma32(fg.a0, b.a0, z4), u0 = mfma32(fu.a0, b.a0, z4), g1 = mfma32(fg.a1, b.a1, g0), g2 = mfma16(fg.a2, b.a2, z4), u1 = mfma32(fu.a1, b.a1, u0), u2 = mfma16(fu.a2, b.a2, z4);
                __builtin_amdgcn_sched_barrier(0);
                const f32x4 cg = g1 + g2, cu = u1 + u2;
                st16m2<0>(ws, sg, asu(cg), su, asu(cu), mk);
                STAMP(11 + 32 * layer);
                if (layer == 0 && wave != 0) {
                    unsigned tr = 0u;
#pragma unroll 1
                    for (unsigned spins = 0; spins < (kSpinCap << 6); ++spins) { tr = lds_word(lb + L_CNT + 56); if (tr) break; }
                    until(tr + x4a);
                } else if (layer == 0) {
                    WAIT1(tl, 2);
#pragma unroll 1
                    for (unsigned spins = 0; spins < kSpinCap; ++spins) {
                        const unsigned long long miss = __builtin_amdgcn_ballot_w64(min(min(tl[0], tl[1]), min(tl[2], tl[3])) == 0u);
                        if (!miss) break;
                        asm volatile("s_mov_b64 exec, %3\n\tglobal_load_dwordx4 %0, %1, %2 sc1\n\ts_mov_b64 exec, -1\n\ts_waitcnt vmcnt(0)" : "+v"(tl) : "v"(ta), "s"(ws), "s"(miss) : "memory");
                    }
                    const unsigned tn = now();
                    unsigned d = min(min(min(tn - tl[0], tn - tl[1]), min(tn - tl[2], tn - tl[3])), 800u);
                    d = min(d, dppu<0xB1>(d)); d = min(d, dppu<0x4E>(d)); d = min(d, dppu<0x124>(d)); d = min(d, dppu<0x128>(d));
                    unsigned tr = tn - min(min((unsigned)__builtin_amdgcn_readlane(d, 0), (unsigned)__builtin_amdgcn_readlane(d, 16)), min((unsigned)__builtin_amdgcn_readlane(d, 32), (unsigned)__builtin_amdgcn_readlane(d, 48)));
                    lds_set(lb + L_CNT + 56, tr | 1u);
                    until(__builtin_amdgcn_readfirstlane(tr | 1u) + x4a);
                } else sched(X4, 0);
                STAMP(16 + 32 * layer);
                float hv;
                u32x4 a0, a1, a2, a3, ac, as;
                auto load = [&]() __attribute__((always_inline)) {
                    as = ld16_sc1(ws, os);
                    if (layer == 0) ac = ld16_sc1(ws, oc);
                    a0 = ld16_sc1<0>(ws, o); a1 = ld16_sc1<1024>(ws, o); a2 = ld16_sc1<2048>(ws, o); a3 = ld16_sc1<3072>(ws, o);
                    if (layer == 0) lds_add(32, 1u);
                };
                auto all_in = [&]() __attribute__((always_inline)) {
                    if (layer == 0) asm volatile("s_waitcnt vmcnt(0)" : "+v"(a0), "+v"(a1), "+v"(a2), "+v"(a3), "+v"(ac), "+v"(as) :: "memory");
                    else { asm volatile("s_waitcnt vmcnt(0)" : "+v"(a0), "+v"(a1), "+v"(a2), "+v"(a3), "+v"(as) :: "memory"); ac = as; }
                };
                auto compute = [&]() __attribute__((always_inline)) {
#define ROR(n) " row_ror:" #n " row_mask:0xf bank_mask:0xf\n\t"
                    asm volatile("s_waitcnt vmcnt(%16)\n\tv_add_f32_dpp v40, %2, %2" ROR(8) "v_add_f32_dpp v41, %3, %3" ROR(8) "s_nop 0\n\tv_add_f32_dpp v40, v40, v40" ROR(4) "v_add_f32_dpp v41, v41, v41" ROR(4)
                                 "v_mov_b32 v42, v40\n\tv_mov_b32 v43, v41\n\ts_nop 0\n\tv_permlane16_swap_b32 v40, v42\n\tv_permlane16_swap_b32 v41, v43\n\t"
                                 "v_pk_add_f32 v[40:41], v[40:41], v[42:43]\n\tv_pk_mul_f32 v[40:41], v[40:41], %14\n\t"
                                 ".if %17 == 0\n\ts_waitcnt vmcnt(4)\n\tv_mov_b64 v[44:45], %12\n\tv_mov_b64 v[46:47], %13\n\t.endif\n\t"
                                 "v_fma_f32 v40, -v41, v41, v40\n\tv_cvt_pk_bf16_f32 v42, 0, v41\n\tv_maximum3_f32 v40, v40, 0, 0\n\t"
                                 ".if %17 == 0\n\tv_permlane32_swap_b32 v44, v46\n\tv_permlane32_swap_b32 v45, v47\n\tv_pk_add_f32 v[44:45], v[44:45], v[46:47]\n\t.endif\n\t"
                                 "v_cvt_pk_bf16_f32 v40, 0, v40\n\ts_waitcnt vmcnt(2)\n\tv_pk_add_f32 v[32:33], %4, %6\n\tv_add_f32 v40, 0x3727c5ac, v40\n\tv_pk_add_f32 v[34:35], %5, %7\n\t"
                                 ".if %17 == 0\n\tv_permlane16_swap_b32 v44, v45\n\tv_add_f32 v44, v44, v45\n\t.endif\n\t"
                                 "v_cvt_pk_bf16_f32 v40, 0, v40\n\ts_waitcnt vmcnt(0)\n\tv_pk_add_f32 v[36:37], %8, %10\n\tv_sqrt_f32 v40, v40\n\tv_pk_add_f32 v[38:39], %9, %11\n\t"
                                 ".if %17 == 0\n\tv_add_f32_dpp v44, v44, v44" ROR(8) "ds_bpermute_b32 v46, %19, v44\n\tds_bpermute_b32 v47, %19, v44 offset:16\n\t.endif\n\t"
                                 "v_pk_add_f32 v[32:33], v[32:33], v[36:37]\n\tv_pk_add_f32 v[34:35], v[34:35], v[38:39]\n\tv_cvt_pk_bf16_f32 v40, 0, v40\n\t"
                                 "s_nop 0\n\t"
                                 "v_permlane32_swap_b32 v32, v34\n\tv_permlane32_swap_b32 v33, v35\n\tv_rcp_f32 v40, v40\n\tv_pk_add_f32 v[32:33], v[32:33], v[34:35]\n\ts_nop 1\n\t"
                                 "v_permlane16_swap_b32 v32, v33\n\t.if %17 == 0\n\ts_waitcnt lgkmcnt(0)\n\tv_mov_b64 %1, v[46:47]\n\t.endif\n\t"
                                 "v_pk_fma_f32 v[32:33], %1, v[42:43], v[32:33] op_sel_hi:[1,0,1] neg_lo:[1,0,0] neg_hi:[1,0,0]\n\tv_pk_mul_f32 v[32:33], v[32:33], v[40:41] op_sel_hi:[1,0]\n\t"
                                 "v_cvt_pk_bf16_f32 v32, 0, v32\n\tv_mul_f32 v34, 0xbfb8aa3b, v32\n\tv_exp_f32 v34, v34\n\tv_cvt_pk_bf16_f32 v33, 0, v33\n\tv_add_f32 v34, 1.0, v34\n\t"
                                 "v_rcp_f32 v34, v34\n\ts_nop 0\n\tv_mul_f32 v32, v34, v32\n\tv_cvt_pk_bf16_f32 v32, 0, v32\n\tv_mul_f32 %0, v33, v32\n\tv_cvt_pk_bf16_f32 v32, %0, %0\n\tds_write_b16 %15, v32"
                                 : "=&v"(hv), "+v"(cs)
                                 : "v"(as[0]), "v"(as[1]), "v"(LO2(a0)), "v"(HI2(a0)), "v"(LO2(a1)), "v"(HI2(a1)), "v"(LO2(a2)), "v"(HI2(a2)), "v"(LO2(a3)), "v"(HI2(a3)),
                                   "v"(LO2(layer ? as : ac)), "v"(HI2(layer ? as : ac)), "s"(rdm), "v"(ha), "i"(layer ? 4 : 5), "i"(layer), "v"(as), "v"(layer ? ha : pc)
                                 : "memory", "v32", "v33", "v34", "v35", "v36", "v37", "v38", "v39", "v40", "v41", "v42", "v43", "v44", "v45", "v46", "v47");
#undef ROR
                };
                RETRY(load(), (all_in(), max(max(max(um4(a0), um4(a1)), max(um4(a2), um4(a3))), max(um4(ac), um4(as)))), compute(), any_nan(hv, hv), layer ? X4 : X4A);
                BAR();
                MARK(e_gu);
                STAMP(12 + 32 * layer);
                if (layer == 0) {
                    *reinterpret_cast<f32x2*>(smem + L_CS2 + wave * 512 + lane * 8) = cs;
                }
            }
            if (layer == 0) {
                if (wave == 1) {
                    const unsigned o = WS_CS + (unsigned)(gi * 6144 + 3072 + (lane < 48 ? lane : 0) * 16);
                    u32x4 a[8];
                    GATHER8(a, o, 768);
                    WAIT8(a, 0);
                    if (lane < 48) *reinterpret_cast<f32x4*>(smem + L_CS + lane * 16) = SUM8(a);
                }
                Frag80 f[4];
#pragma unroll
                for (int i = 0; i < 4; ++i) { f[i] = frag80(lb + L_WQ + (unsigned)(128 * wave + 32 * i + 8 * p4), 416, r0s, r1s, r2s); PIN3(f[i].a0, f[i].a1, f[i].a2); }
                unsigned b16 = lb + L_XS + (unsigned)(q4 * 160 + kq * 16), b8 = lb + L_XS + (unsigned)(q4 * 160 + 128 + kq * 8);
                unsigned so = WS_H1 + qS + (unsigned)((gi * 4 + q4) * 16384 + gj * 896 + 256 * wave + kq * 64 + p4 * 16);
                unsigned long long mk = 0x1111111111111111ull;
                PIN3(b16, b8, so); PINS(mk);
                GUARD(3800);
                MARK(b_q);
                BAR();
                STAMP(33);
                const Frag80 b = rows80(b16, b8);
                st16_sc1(ws, so, tiles4(f, b, mk));
                STAMP(34);
                qk_addr(true); att_addr();
                unsigned x0a = lb + L_M + (unsigned)((lane >> 5) * 16), x1a = lb + L_M + (unsigned)((lane >> 4) * 8), x2a, r2a;
                unsigned r0a = lb + L_OP + (unsigned)((lane >> 5) * 4096 + (lane & 31) * 16), r1a = lb + L_OP + (unsigned)(512 + (lane >> 4) * 2048 + (lane & 15) * 16);
                unsigned s0a = (unsigned)(lane * 4), s1a = (unsigned)(256 + (lane >> 5) * 64 + (lane & 15) * 4);
                ml_addr(lane, x2a, r2a);
                PIN3(x0a, x1a, x2a); PIN3(r0a, r1a, r2a); PIN2(s0a, s1a);
                cma = lb + L_M; cxa = wave == 0 ? x0a : wave == 1 ? x1a : x2a; cra = wave == 0 ? r0a : wave == 1 ? r1a : r2a;
                cso = WS_OWN + rS + (unsigned)(gi * 16384 + 4096 + gj * 512) + (wave == 0 ? s0a : wave == 1 ? s1a : 384u);
                PIN2(cma, cxa); PIN2(cra, cso);
                asm volatile("" : "=v"(omr), "=v"(owa), "=v"(owb));
            }
        } else {
            const int t = wave - 3;
            StatAddr sa;
            u32x4 sel;
            unsigned long long hi = 0xff00ff00ff00ff00ull;
            auto gather32 = [&](unsigned o, unsigned X, auto fin) __attribute__((always_inline)) {
                u32x4 a0 = ld16_sc1<-2048>(ws, o), a1 = ld16_sc1<-1024>(ws, o), a2 = ld16_sc1<0>(ws, o), a3 = ld16_sc1<1024>(ws, o);
                float v;
                auto sum = [&]() __attribute__((always_inline)) {
                    asm volatile("s_waitcnt vmcnt(3)\n\tv_mfma_f32_16x16x32_bf16 v[40:43], %1, %5, 0\n\ts_waitcnt vmcnt(2)\n\tv_mfma_f32_16x16x32_bf16 v[40:43], %2, %5, v[40:43]\n\t"
                                 "s_waitcnt vmcnt(1)\n\tv_mfma_f32_16x16x32_bf16 v[40:43], %3, %5, v[40:43]\n\ts_waitcnt vmcnt(0)\n\tv_mfma_f32_16x16x32_bf16 v[40:43], %4, %5, v[40:43]\n\t"
                                 "s_nop 7\n\tv_cndmask_b32 v40, v40, v41, %6\n\tv_cndmask_b32 v42, v42, v43, %6\n\ts_nop 1\n\tv_permlane32_swap_b32 v40, v42\n\tv_add_f32 v40, v40, v42\n\t"
                                 "v_cvt_pk_bf16_f32 %0, 0, v40"
                                 : "=&v"(v), "+v"(a0), "+v"(a1), "+v"(a2), "+v"(a3) : "v"(sel), "s"(hi) : "memory", "v40", "v41", "v42", "v43");
                    fin(v);
                    __builtin_amdgcn_sched_barrier(0);
                };
                sum();
                if (__builtin_expect(any_nan(v, v) != 0ull, 0)) {
#pragma unroll 1
                    for (unsigned spins = 0; spins < kSpinCap; ++spins) {
                        slip(X);
                        again16<-2048>(a0, ws, o); again16<-1024>(a1, ws, o); again16<0>(a2, ws, o); again16<1024>(a3, ws, o);
                        asm volatile("s_waitcnt vmcnt(0)" : "+v"(a0), "+v"(a1), "+v"(a2), "+v"(a3) :: "memory");
                        if (!any_empty(max(max(um4(a0), um4(a1)), max(um4(a2), um4(a3))))) break;
                    }
                    sum();
                }
            };
            auto poll32 = [&](unsigned o, unsigned t0) __attribute__((always_inline)) {
                    unsigned long long m0, m1, m2, m3; unsigned cnt = kSpinCap; float v;
#define CHK(r, m) "v_max3_u32 v44, v" #r ", v[" #r "+1], v[" #r "+2]\n\tv_max_u32 v44, v44, v[" #r "+3]\n\tv_cmp_eq_u32 %" #m ", -1, v44\n\t"
                    asm volatile("s_branch 3f\n"
                                 "1:\n\ts_mov_b64 exec, %1\n\tglobal_load_dwordx4 v[0:3], %6, %7 offset:-2048 sc1\n\ts_mov_b64 exec, %2\n\tglobal_load_dwordx4 v[4:7], %6, %7 offset:-1024 sc1\n\t"
                                 "s_mov_b64 exec, %3\n\tglobal_load_dwordx4 v[8:11], %6, %7 sc1\n\ts_mov_b64 exec, %4\n\tglobal_load_dwordx4 v[12:15], %6, %7 offset:1024 sc1\n\ts_mov_b64 exec, -1\n\t"
                                 "s_sub_u32 %5, %5, 1\n\ts_cselect_b64 vcc, 0, -1\n\ts_waitcnt vmcnt(0)\n\ts_branch 2f\n"
                                 "3:\n\ts_memrealtime vcc\n\ts_waitcnt lgkmcnt(0)\n\ts_sub_u32 vcc_lo, %10, vcc_lo\n\ts_cmp_lt_u32 vcc_lo, 0x400\n\ts_cbranch_scc1 3b\n\t"
                                 "global_load_dwordx4 v[0:3], %6, %7 offset:-2048 sc1\n\tglobal_load_dwordx4 v[4:7], %6, %7 offset:-1024 sc1\n\t"
                                 "global_load_dwordx4 v[8:11], %6, %7 sc1\n\tglobal_load_dwordx4 v[12:15], %6, %7 offset:1024 sc1\n\ts_mov_b64 vcc, -1\n"
                                 "2:\n\ts_waitcnt vmcnt(3)\n\tv_mfma_f32_16x16x32_bf16 v[40:43], v[0:3], %8, 0\n\t" CHK(0, 1) "s_waitcnt vmcnt(2)\n\tv_mfma_f32_16x16x32_bf16 v[40:43], v[4:7], %8, v[40:43]\n\t" CHK(4, 2)
                                 "s_waitcnt vmcnt(1)\n\tv_mfma_f32_16x16x32_bf16 v[40:43], v[8:11], %8, v[40:43]\n\t" CHK(8, 3) "s_waitcnt vmcnt(0)\n\tv_mfma_f32_16x16x32_bf16 v[40:43], v[12:15], %8, v[40:43]\n\t" CHK(12, 4)
                                 "s_or_b64 exec, %1, %2\n\ts_or_b64 exec, exec, %3\n\ts_or_b64 exec, exec, %4\n\ts_and_b64 vcc, exec, vcc\n\ts_cbranch_scc1 1b\n\t"
                                 "s_mov_b64 exec, -1\n\tv_cndmask_b32 v40, v40, v41, %9\n\tv_cndmask_b32 v42, v42, v43, %9\n\ts_nop 1\n\tv_permlane32_swap_b32 v40, v42\n\tv_add_f32 v40, v40, v42\n\t"
                                 "v_cvt_pk_bf16_f32 %0, 0, v40"
                                 : "=&v"(v), "=&s"(m0), "=&s"(m1), "=&s"(m2), "=&s"(m3), "+s"(cnt) : "v"(o), "s"(ws), "v"(sel), "s"(hi), "s"(t0 - 1u)
                                 : "memory", "vcc", "scc", "v0", "v1", "v2", "v3", "v4", "v5", "v6", "v7", "v8", "v9", "v10", "v11", "v12", "v13", "v14", "v15", "v40", "v41", "v42", "v43", "v44");
#undef CHK
                return v;
            };
#define PUT8(A, B, M) "v_min_u32 v34, -2, v32\n\tv_min_u32 v32, -2, v32\n\tv_min_u32 v35, -2, v33\n\tv_min_u32 v33, -2, v33\n\tv_permlane16_swap_b32 v32, v34\n\ts_nop 0\n\t" \
                      "v_permlane16_swap_b32 v33, v35\n\ts_mov_b64 exec, " M "\n\tglobal_store_dwordx4 " A ", v[32:35], " B " sc1\n\ts_mov_b64 exec, -1\n\ts_nop 0"
            unsigned xw, qo, qw, qc;
            __builtin_amdgcn_s_setprio(0);
            {
                STAMP(6 + 32 * layer);
                bf16x8 af[6];
                unsigned long long mk = 0x000f000f000f000full;
                if (layer == 0) {
                    const int lane = lane_id();
                    xe = lo16(*reinterpret_cast<const u16*>(smem + L_X0 + xrow(lane) * 1280 + gj * 160 + t * 32 + xcol(lane) * 2));
                    op_addr(0);
                    GUARD(1600);
                    MARK(b_op);
                    if (nj | (ord == OML)) { VMW(0); lds_add(44, nj); }
#pragma unroll 1
                    for (unsigned spins = 0; spins < (kSpinCap << 4); ++spins) if (lds_word(lb + L_CNT + 44) >= 5u) break;
                    STAMP(8);
                } else MARK(b_op);
                const unsigned lo = cma, lw = cxa, so = cso, br = cra, mr = omr, h3 = tile35(gj, 10 * layer + t);
                unsigned go, xs;
#pragma unroll
                for (int s = 0; s < 6; ++s) af[s] = cat8(tr64o(owa, s * 32 * 160), tr64o(owb, s * 32 * 160));
                PINS(mk); PINS(hi);
                if (layer != 0 && t < 4) {
                    sched(X2, 0);
                    STAMP(49);
                    u32x4 ov = ld16_sc1(ws, lo);
                    WAIT1(ov, 0);
                    *(LAS u32x4*)(size_t)lw = ov;
                    if (__builtin_expect(any_empty(um4(ov)), 0)) {
#pragma unroll 1
                        for (unsigned spins = 0; spins < kSpinCap; ++spins) {
                            slip(X2);
                            ov = ld16_sc1(ws, lo);
                            WAIT1(ov, 0);
                            *(LAS u32x4*)(size_t)lw = ov;
                            if (!any_empty(um4(ov))) break;
                        }
                    }
                }
                {
                    bf16x8 b0, b1, b2, b3, b4, b5;
#define QP " row_half_mirror row_mask:0xf bank_mask:0xf\n\t"
                    asm volatile("s_waitcnt lgkmcnt(0)\n\ts_barrier\n\t"
                                 "ds_read_b128 %0, %12\n\tds_read_b64 v[36:37], %13\n\tds_read_b128 %1, %12 offset:64\n\tds_read_b128 %2, %12 offset:128\n\tds_read_b128 %3, %12 offset:192\n\t"
                                 "ds_read_b128 %4, %12 offset:256\n\tds_read_b128 %5, %12 offset:320\n\t"
                                 "s_waitcnt lgkmcnt(5)\n\tv_mfma_f32_16x16x32_bf16 v[32:35], %6, %0, 0\n\tv_max_f32_dpp v38, v36, v36" QP "v_sub_f32 v38, v36, v38\n\t"
                                 "s_waitcnt lgkmcnt(4)\n\tv_mfma_f32_16x16x32_bf16 v[32:35], %7, %1, v[32:35]\n\tv_exp_f32 v38, v38\n\t"
                                 "s_waitcnt lgkmcnt(3)\n\tv_mfma_f32_16x16x32_bf16 v[32:35], %8, %2, v[32:35]\n\tv_mul_f32 v37, v37, v38\n\t"
                                 "s_waitcnt lgkmcnt(2)\n\tv_mfma_f32_16x16x32_bf16 v[32:35], %9, %3, v[32:35]\n\tv_add_f32_dpp v39, v37, v37" QP
                                 "s_waitcnt lgkmcnt(1)\n\tv_mfma_f32_16x16x32_bf16 v[32:35], %10, %4, v[32:35]\n\tv_rcp_f32 v39, v39\n\t"
                                 "s_waitcnt lgkmcnt(0)\n\tv_mfma_f32_16x16x32_bf16 v[32:35], %11, %5, v[32:35]\n\tv_mul_f32 v38, v38, v39\n\ts_nop 6\n\t"
                                 "v_pk_mul_f32 v[32:33], v[32:33], v[38:39] op_sel_hi:[1,0]\n\tv_pk_mul_f32 v[34:35], v[34:35], v[38:39] op_sel_hi:[1,0]\n\ts_nop 0\n\t"
                                 "v_add_f32_dpp v32, v32, v32" QP "v_add_f32_dpp v33, v33, v33" QP "v_add_f32_dpp v34, v34, v34" QP "v_add_f32_dpp v35, v35, v35" QP
                                 "v_cvt_pk_bf16_f32 v32, v32, v33\n\tv_cvt_pk_bf16_f32 v33, v34, v35\n\t" PUT8("%14", "%15", "%16")
                                 : "=&v"(b0), "=&v"(b1), "=&v"(b2), "=&v"(b3), "=&v"(b4), "=&v"(b5)
                                 : "v"(af[0]), "v"(af[1]), "v"(af[2]), "v"(af[3]), "v"(af[4]), "v"(af[5]), "v"(br), "v"(mr), "v"(so), "s"(ws), "s"(mk)
                                 : "memory", "v32", "v33", "v34", "v35", "v36", "v37", "v38", "v39");
#undef QP
                }
                if (layer == 0 && wave == 7) st4_sc1(ws, WS_MB + (unsigned)(gi * 1024 + 64 + gj * 4), now() | 1u);
                const unsigned tp = layer ? 0u : now();
                if (layer == 0) sides();
                STAMP(7 + 32 * layer);
                {
                    const int ln = lane_id();
#pragma unroll
                    for (int k = 0; k < 4; ++k) sel[k] = ((ln >> 1) & 3) == k ? (ln & 1) ? 0x3f800000u : 0x3f80u : 0u;
                    go = h3 + nS + (unsigned)(2048 + ln * 16); xs = lb + L_XS + (unsigned)(xrow(ln) * 160 + t * 32 + xcol(ln) * 2);
                    PIN3(sel, go, xs);
                    sa = stats_addr(ln, h4 + (unsigned)(H4T + gj * 128), 16);
                }
                if (layer != 0) sched(X3, 0);
                STAMP(18 + 32 * layer);
                if (layer != 0) gather32(go, X3, [&](float s) { xw = pk2(xe + s, 0.f); *(LAS u16*)(size_t)xs = (u16)xw; });
                else { xw = pk2(xe + poll32(go, tp + W3), 0.f); *(LAS u16*)(size_t)xs = (u16)xw; }
                xe = lo16(xw);
                STAMP(9 + 32 * layer);
            }
            BAR();
            if (wave == 3) stats(sa);
            MARK(e_op);
            STAMP(11 + 32 * layer);
            {
                LANE_VARS();
                const unsigned im = lb + L_WD + (unsigned)(32 * t + 8 * p4);
                bf16x8 wd0 = cat8(tr64(im + r0s * 160), tr64(im + r1s * 160)); bf16x4 wd1 = tr64(im + (32 + r2s) * 160);
                const unsigned h5 = tile35(gj, 10 * layer + 5 + t);
                unsigned h16 = lb + L_H + (unsigned)(p4 * 96 + kq * 16), h8 = lb + L_H + (unsigned)(p4 * 96 + 64 + kq * 8);
                unsigned so = h5 + (unsigned)((kq & 1) * 8192 + gi * 128 + (kq >> 1) * 64 + p4 * 16);
                unsigned long long mk = 0x000f000f000f000full;
                unsigned go = h5 + nS + (unsigned)(2048 + lane * 16), xs = (layer ? (unsigned)(xrow(lane) * 1280 + gj * 160) : lb + L_XS + (unsigned)(xrow(lane) * 160)) + (unsigned)(t * 32 + xcol(lane) * 2);
                unsigned sta = lb + L_ST + (unsigned)(xrow(lane) * 16);
                if (layer == 0 && wave < 6) {
                    sa = stats_addr(lane, WS_H1 + qS + (unsigned)(gi * 65536 + gj * 896 + 768), 16384);
                    asm volatile("" : "=v"(qo), "=v"(qw), "=v"(qc), "=v"(cma), "=v"(cxa), "=v"(cra), "=v"(cso), "=v"(omr), "=v"(owa), "=v"(owb));
                    asm volatile("" : "=v"(qa), "=v"(pw), "=v"(ma), "=v"(idx), "=v"(qm), "=v"(pr), "=v"(oa), "=v"(oc));
                } else if (layer == 0) {
                    op_addr(1); qk_addr(false); att_addr();
                    const int l5 = lane & 31, c = l5 < 24 ? 24 * (wave - 6) + l5 : 48;
                    qo = WS_H1 + qS + (unsigned)((gi * 4 + (gj >> 1)) * 16384 + 3584 + (lane >> 5) * 896 + c * 16);
                    qw = lb + L_Q + (unsigned)(l5 < 24 ? (c >> 4) * 128 + (c & 3) * 32 + (c & 12) + (lane >> 5) * 16 : lane < 26 ? 384 + (lane & 1) * 16 : 448);
                    qc = lb + L_CS + (unsigned)(c * 16 + (lane >> 5) * 8);
                    PIN3(qo, qw, qc);
                }
                PIN2(wd0, wd1); PIN3(h16, h8, so); PIN3(go, xs, sta); PINS(mk);
                GUARD(2000);
                MARK(b_dn);
                if (wave == 4) {
                    unsigned os = h4 + (unsigned)(H4T + (q4 + 4 * (kq & 1)) * 128 + p4 * 16);
                    float sw = *reinterpret_cast<const float*>(smem + L_CNT + 12);
                    PIN2(os, sw);
                    if (layer == 0) {
#pragma unroll 1
                        for (unsigned spins = 0; spins < (kSpinCap << 6); ++spins) if (lds_word(lb + L_CNT + 56)) break;
                    }
                    sched(layer ? X4 : X4A, 0);
                    u32x4 as;
#pragma unroll 1
                    for (unsigned spins = 0; spins < kSpinCap; ++spins) { as = ld16_sc1(ws, os); WAIT1(as, 0); if (!any_empty(um4(as))) break; }
                    float ss = __uint_as_float(as[0]), sm = __uint_as_float(as[1]), rd = __uint_as_float(as[2]);
                    ss += dpp<0x128>(ss); sm += dpp<0x128>(sm); rd += dpp<0x128>(rd);
                    ss += dpp<0x124>(ss); sm += dpp<0x124>(sm); rd += dpp<0x124>(rd);
                    ss = x16_add(ss); sm = x16_add(sm); rd = x16_add(rd);
                    if (LANES(0xfull)) *reinterpret_cast<f32x4*>(smem + L_ST + p4 * 16) = (f32x4){ss, sm, rd, sw};
                }
                if (layer == 0 && (wave == 3 || wave == 5)) {
                    unsigned ea = lane < 40 ? tile35(gj, 15 + (lane >> 3)) + (unsigned)(gi * 128 + (lane & 7) * 16) : WS_OWN + rS + (unsigned)(gi * 16384 + 4096 + gj * 512 + (lane - 40) * 16);
                    unsigned eb = ea + (lane < 40 ? 8192u : 256u);
                    PIN2(ea, eb);
#pragma unroll 1
                    for (unsigned spins = 0; spins < (kSpinCap << 6); ++spins) { if (lds_word(lb + L_CNT + 32) >= 6u) break; __builtin_amdgcn_s_sleep(1); }
                    if (wave == 5) empty4(1, fS, lane);
                    else { st16m(ws, ea, (u32x4){~0u, ~0u, ~0u, ~0u}, 0xffffffffffffffull); st16m(ws, eb, (u32x4){~0u, ~0u, ~0u, ~0u}, 0xffffffffffffffull); }
                }
                asm volatile("s_waitcnt lgkmcnt(0)\n\ts_barrier\n\tds_read_b128 v[36:39], %2\n\tds_read_b64 v[40:41], %3\n\t"
                             "s_waitcnt lgkmcnt(1)\n\tv_mfma_f32_16x16x32_bf16 v[32:35], %0, v[36:39], 0\n\ts_waitcnt lgkmcnt(0)\n\tv_mfma_f32_16x16x16_bf16 v[42:45], %1, v[40:41], 0\n\ts_nop 7\n\t"
                             "v_pk_add_f32 v[32:33], v[32:33], v[42:43]\n\tv_pk_add_f32 v[34:35], v[34:35], v[44:45]\n\t"
                             "v_cvt_pk_bf16_f32 v32, v32, v33\n\tv_cvt_pk_bf16_f32 v33, v34, v35\n\t" PUT8("%4", "%5", "%6")
                             :: "v"(wd0), "v"(wd1), "v"(h16), "v"(h8), "v"(so), "s"(ws), "s"(mk)
                             : "memory", "v32", "v33", "v34", "v35", "v36", "v37", "v38", "v39", "v40", "v41", "v42", "v43", "v44", "v45");
                STAMP(13 + 33 * layer);
                if (layer == 0 || __builtin_expect(gi == 24 + (t & 3), 1)) {
                    const f32x4 sr = *(const LAS f32x4*)(size_t)sta;
                    const f32x2 st = ln_stats(sr[0], sr[1]);
                    float rs = bfr(frcp(1.f + ex2(bfr((sr[2] - st[0] * sr[3]) * st[1]) * -LOG2E))), n1 = bfr(bfr(xe - st[0]) * st[1]);
                    PIN2(rs, n1);
                    if (layer == 0) {
                        sched(X5A, 0);
                        STAMP(15);
                        gather32(go, X5A, [&](float y) { xw = pk2(xe + bfr(n1 + bfr(y * rs)), 0.f); *(LAS u16*)(size_t)xs = (u16)xw; });
                        xe = lo16(xw);
                        STAMP(14);
                    } else {
                        st2(out, xs, pk2(xe + bfr(n1 + bfr(poll32(go, when(X5B)) * rs)), 0.f));
                        asm volatile("s_endpgm");
                        MARK(e_dn);
                        STAMP(45);
                    }
                }
            }
            if (layer == 0) {
                BAR();
                if (wave == 3) { stats(sa); STAMP(34); }
                MARK(e_dn);
                if (wave == 3) __builtin_amdgcn_s_setprio(2);
                if (__builtin_expect(wave < 6, 0)) { op_addr(1); qk_addr(false); att_addr(); }
                if (wave >= 6) {
                    const unsigned o = qo;
                    f32x2 cs = -*(const LAS f32x2*)(size_t)qc;
                    float rn = 1.f / DM;
                    unsigned long long m0 = 1ull << 24, m1 = 1ull << 25;
                    PIN2(cs, rn); PINS(m0); PINS(m1);
                    MARK(b_qg);
                    sched(X1, 0);
                    STAMP(51);
                    u32x4 a0, a1, a2, a3; f32x2 u;
                    RETRY({ a0 = ld16_sc1<-3584>(ws, o); a1 = ld16_sc1<-1792>(ws, o); a2 = ld16_sc1<0>(ws, o); a3 = ld16_sc1<1792>(ws, o); },
                          ({ asm volatile("s_waitcnt vmcnt(0)" : "+v"(a0), "+v"(a1), "+v"(a2), "+v"(a3) :: "memory"); max(max(um4(a0), um4(a1)), max(um4(a2), um4(a3))); }),
                          { unsigned sm;
                            asm volatile("s_waitcnt vmcnt(2)\n\tv_pk_add_f32 v[40:41], %2, %4\n\tv_pk_add_f32 v[42:43], %3, %5\n\t"
                                         "s_waitcnt vmcnt(1)\n\tv_pk_add_f32 v[40:41], v[40:41], %6\n\tv_pk_add_f32 v[42:43], v[42:43], %7\n\t"
                                         "s_waitcnt vmcnt(0)\n\tv_pk_add_f32 v[40:41], v[40:41], %8\n\tv_pk_add_f32 v[42:43], v[42:43], %9\n\ts_nop 1\n\t"
                                         "v_permlane32_swap_b32 v40, v42\n\tv_permlane32_swap_b32 v41, v43\n\tv_pk_add_f32 v[40:41], v[40:41], v[42:43]\n\ts_nop 0\n\t"
                                         "v_readlane_b32 %1, v41, 24\n\ts_nop 1\n\tv_mul_f32 v42, %1, %11\n\tv_cvt_pk_bf16_f32 v42, 0, v42\n\tv_pk_fma_f32 %0, %10, v[42:43], v[40:41] op_sel_hi:[1,0,1]"
                                         : "=&v"(u), "=&s"(sm) : "v"(LO2(a0)), "v"(HI2(a0)), "v"(LO2(a1)), "v"(HI2(a1)), "v"(LO2(a2)), "v"(HI2(a2)), "v"(LO2(a3)), "v"(HI2(a3)), "v"(cs), "v"(rn)
                                         : "memory", "v40", "v41", "v42", "v43");
                            unsigned w = pk2v(u);
                            if (LANES(m0)) w = __float_as_uint(u[0]);
                            if (LANES(m1)) w = __float_as_uint(u[1]);
                            *(LAS unsigned*)(size_t)qw = w; __builtin_amdgcn_sched_barrier(0); }, any_nan(u[0], u[1]), X1);
                    MARK(e_qg);
                }
            }
        }
    };
    PINS(ck1); PINS(ck2); PINS(csc);
    chain(IC<0>{});
    STAMP(35);
    att1();
    chain(IC<1>{});
}

__device__ __attribute__((used, noinline)) void mega_code_end() { asm volatile(".Lmega_code_end:"); }

extern "C" void kernel_launch(void* const* d_in, const int* in_sizes, int n_in, void* d_out, int out_size, void* d_ws,
                              size_t ws_size, hipStream_t stream) {
    const u16* x0 = (const u16*)d_in[0];
    const u16* kc = (const u16*)d_in[1];
    const u16* vc = (const u16*)d_in[2];
    const u16* wq = (const u16*)d_in[3];
    const u16* wo = (const u16*)d_in[4];
    const u16* wr = (const u16*)d_in[6];
    const u16* wg = (const u16*)d_in[7];
    const u16* wu = (const u16*)d_in[8];
    const u16* wd = (const u16*)d_in[9];
    (void)hipFuncSetAttribute((const void*)mega_kernel, hipFuncAttributeMaxDynamicSharedMemorySize, L_END);
    mega_kernel<<<256, 512, L_END, stream>>>(x0, kc, vc, wq, wo, wr, wg, wu, wd, (u16*)d_out, (unsigned char*)d_ws
                                             );
}
```

```cpp
#include <hip/hip_runtime.h>
#include <hip/hip_bf16.h>
#include <stdint.h>

typedef unsigned short u16;
typedef unsigned u32x4 __attribute__((ext_vector_type(4)));
typedef unsigned u32x2 __attribute__((ext_vector_type(2)));
typedef unsigned long long u64x8 __attribute__((ext_vector_type(8)));
typedef unsigned long long u64x2 __attribute__((ext_vector_type(2)));
typedef float f32x4 __attribute__((ext_vector_type(4)));
typedef short s16x4 __attribute__((ext_vector_type(4)));
typedef __attribute__((ext_vector_type(4))) __bf16 bf16x4;
typedef __attribute__((ext_vector_type(8))) __bf16 bf16x8;
#define LAS __attribute__((address_space(3)))

constexpr int DM = 640, HQ = 32, SK = 4096, DH = 192, WIN = 1024, FF = 1536;
constexpr int QD = HQ * DH;
constexpr float SCALE = 0.07216878364870322f, LOG2E = 1.44269504088896341f;
constexpr unsigned kSpinCap = 1u << 16;
#define GJ(g) (((g) >> 3) & 7)
#define GI(g) (((g) & 7) * 4 + ((g) >> 6))

#define D4A 105
#define D5A 87
#define D1 84
#define D2 134
#define D3 68
#define D4 78
#define D5B 74
#define PACE 59
#define PINIT 100
#define PMIN 50
#define DM_ 0
constexpr unsigned X4A = D4A + DM_, X5A = X4A + D5A + DM_, X1 = X5A + D1 + DM_, X2 = X1 + D2 + DM_, X3 = X2 + D3 + DM_, X4 = X3 + D4 + DM_, X5B = X4 + D5B + DM_;
#define TLAND 132
#define XCOST 8
#define WOFF 6
#define CL 25
#define BETA 11
constexpr unsigned WOC = 2, WEARLY = 1;
constexpr unsigned NUNIT = 384 + XCOST + 3 * (9 + 2 * WEARLY), PBURST = 30;
constexpr int LEAD = 12, TOFF = 4;
constexpr unsigned DG2 = 12, W3 = 15;
constexpr int GP = 0, GA = 1, GB = 6, GC = 7, HD = 1;
#define VORD 0, 1, 8, 2, 3, 9, 4, 5, 6, 7, 10, 11
constexpr int kVord[12] = {VORD};
constexpr int v_upto(int h) { int n = 0; for (int i = 0; i <= h; ++i) n += kVord[i] < 8 ? 2 : 4; return n; }
constexpr int v_after(int p) { int h = 0; while (kVord[h] != p) ++h; return 32 - v_upto(h); }
constexpr int OML = 2;

constexpr unsigned WS_H1 = 0;
constexpr unsigned WS_H4 = 2u << 20;
constexpr unsigned H4C = 20480, H4T = H4C + 3072;
constexpr unsigned WS_H35 = 4u << 20;
constexpr unsigned WS_OWN = WS_H35 + 80 * 16384;
constexpr unsigned WS_CS = WS_OWN + 32 * 16384;
constexpr unsigned WS_MB = WS_CS + 256 * 768;
constexpr unsigned WS_R = WS_MB + 256 * 128;
constexpr unsigned WS_END = WS_R + 128;
constexpr unsigned WS_Q0 = 6u << 20;
static_assert(WS_END <= (6u << 20), "ws map");
__device__ __forceinline__ unsigned tile35(int j, int q) { return WS_H35 + (unsigned)((j * 10 + (q >> 1)) * 16384 + (q & 1) * 4096); }
__device__ __forceinline__ unsigned h4s(int s) { return (unsigned)((s >> 1) * 16384 + (s & 1) * 4096); }

constexpr int L_WQ = 0;
constexpr int L_WO = 33792;
constexpr int L_GU = L_WO + 30720;
constexpr int L_WD = L_GU + 16384;
constexpr int L_X0 = L_WD + 8192;
constexpr int L_WR = L_X0 + 5120;
constexpr int L_XS = L_WR + 2048;
constexpr int L_Q = L_XS + 1024;
constexpr int L_OP = L_Q + 512;
constexpr int L_M = L_OP + 8192;
constexpr int L_H = L_M + 64;
constexpr int L_ST = L_H + 384;
constexpr int L_CS = L_ST + 64;
constexpr int L_CS2 = L_CS + 1024;
constexpr int L_CNT = L_CS2 + 1536;
constexpr int L_P = L_CNT + 96;
constexpr int L_O2 = L_P + 72 * 192;
constexpr int L_DBG = L_O2 + 3328;
constexpr int L_END = L_DBG + 2048;
static_assert(L_END <= 163840 && L_END > 81920, "LDS");

typedef __attribute__((ext_vector_type(2))) __bf16 bf16x2;
typedef float f32x2 __attribute__((ext_vector_type(2)));
__device__ __forceinline__ unsigned pk2(float a, float b) { return __builtin_bit_cast(unsigned, __builtin_convertvector((f32x2){a, b}, bf16x2)); }
__device__ __forceinline__ unsigned fexp(float x) { const unsigned e = (__float_as_uint(x) >> 23) & 255u; return e == 255u ? 0u : e; }
__device__ __forceinline__ u32x2 enc21(float a, float b, float c) {
    const float lim = 1048575.f;
    const unsigned i = (unsigned)(int)__builtin_rintf(__builtin_amdgcn_fmed3f(a, -lim, lim)) & 0x1fffffu, j = (unsigned)(int)__builtin_rintf(__builtin_amdgcn_fmed3f(b, -lim, lim)) & 0x1fffffu;
    const unsigned k = (unsigned)(int)__builtin_rintf(__builtin_amdgcn_fmed3f(c, -lim, lim)) & 0x1fffffu;
    return (u32x2){i | j << 21, j >> 11 | k << 10 | (__builtin_fabsf(a) + __builtin_fabsf(b) + __builtin_fabsf(c) < 1e30f ? 0u : 0x80000000u)};
}
__device__ __forceinline__ float lo16(unsigned w) { return __uint_as_float(w << 16); }
__device__ __forceinline__ float hi16(unsigned w) { return __uint_as_float(w & 0xffff0000u); }
__device__ __forceinline__ float bfr(float f) { return __uint_as_float(pk2(0.f, f)); }
template <int CTRL> __device__ __forceinline__ float dpp(float x) {
    return __builtin_bit_cast(float, __builtin_amdgcn_mov_dpp(__builtin_bit_cast(int, x), CTRL, 0xf, 0xf, true));
}
template <int CTRL> __device__ __forceinline__ unsigned dppu(unsigned x) { return (unsigned)__builtin_amdgcn_mov_dpp((int)x, CTRL, 0xf, 0xf, true); }
__device__ __forceinline__ float row16_sum(float v) {
    v += dpp<0xB1>(v); v += dpp<0x4E>(v); v += dpp<0x141>(v); v += dpp<0x140>(v); return v;
}
__device__ __forceinline__ float fmax2(float a, float b) { return __builtin_elementwise_maximum(a, b); }
__device__ __forceinline__ float fmax3(float a, float b, float c) { return fmax2(fmax2(a, b), c); }
__device__ __forceinline__ float x16_add(float v) { auto s = __builtin_amdgcn_permlane16_swap(__float_as_uint(v), __float_as_uint(v), false, false); return __uint_as_float(s[0]) + __uint_as_float(s[1]); }
__device__ __forceinline__ float x32_add(float v) { auto s = __builtin_amdgcn_permlane32_swap(__float_as_uint(v), __float_as_uint(v), false, false); return __uint_as_float(s[0]) + __uint_as_float(s[1]); }
__device__ __forceinline__ float wave_sum(float v) { return x32_add(x16_add(row16_sum(v))); }
__device__ __forceinline__ float rs32(float a, float b) { auto s = __builtin_amdgcn_permlane32_swap(__float_as_uint(a), __float_as_uint(b), false, false); return __uint_as_float(s[0]) + __uint_as_float(s[1]); }

#define GUARD(n) asm volatile("s_branch 1f\n\t.p2align 12,," #n "\n1:")
#define MARK(n) asm volatile(".Lcrit_" #n "_%=:" ::)
#define BAR() asm volatile("s_waitcnt lgkmcnt(0)\n\ts_barrier" ::: "memory")
#define VMW(n) asm volatile("s_waitcnt vmcnt(%0)" :: "i"(n) : "memory")

template <int OFF = 0> __device__ __forceinline__ u32x4 ld16_sc1(const void* sbase, unsigned voff) {
    u32x4 r; asm volatile("global_load_dwordx4 %0, %1, %2 offset:%3 sc1" : "=v"(r) : "v"(voff), "s"(sbase), "i"(OFF) : "memory"); return r;
}
__device__ __forceinline__ unsigned ld4_sc1(const void* sbase, unsigned voff) {
    unsigned r; asm volatile("global_load_dword %0, %1, %2 sc1" : "=v"(r) : "v"(voff), "s"(sbase) : "memory"); return r;
}
template <int OFF = 0> __device__ __forceinline__ void st16_sc1(void* sbase, unsigned voff, u32x4 v) {
    asm volatile("global_store_dwordx4 %0, %1, %2 offset:%3 sc1\n\ts_nop 1" :: "v"(voff), "v"(v), "s"(sbase), "i"(OFF) : "memory");
}
__device__ __forceinline__ void st4_sc1(void* sbase, unsigned voff, unsigned v) {
    asm volatile("global_store_dword %0, %1, %2 sc1" :: "v"(voff), "v"(v), "s"(sbase) : "memory");
}
__device__ __forceinline__ void st8(void* sbase, unsigned voff, u32x2 v) {
    asm volatile("global_store_dwordx2 %0, %1, %2" :: "v"(voff), "v"(v), "s"(sbase) : "memory");
}
__device__ __forceinline__ void st2(void* sbase, unsigned voff, unsigned v) {
    asm volatile("global_store_short %0, %1, %2" :: "v"(voff), "v"(v), "s"(sbase) : "memory");
}
constexpr int RK = 64, RV = 160;
template <int R, int IMM> __device__ __forceinline__ void ld16_nt(const void* sbase, unsigned voff) {
    asm volatile("global_load_dwordx4 v[%c3:%c4], %0, %1 offset:%2 nt" :: "v"(voff), "s"(sbase), "i"(IMM), "i"(R), "i"(R + 3) : "memory");
}
template <int R, int IMM> __device__ __forceinline__ void ld8_nt(const void* sbase, unsigned voff) {
    asm volatile("global_load_dwordx2 v[%c3:%c4], %0, %1 offset:%2 nt" :: "v"(voff), "s"(sbase), "i"(IMM), "i"(R), "i"(R + 1) : "memory");
}
template <int R> __device__ __forceinline__ void smf(f32x4& d, u32x4 a, unsigned idx) {
    asm volatile("v_smfmac_f32_16x16x64_bf16 %0, %1, v[%c3:%c4], %2" : "+v"(d) : "v"(a), "v"(idx), "i"(R), "i"(R + 7));
}
template <int A, int B> __device__ __forceinline__ void zip_in() {
    unsigned t;
    asm volatile("v_perm_b32 %0, v%c2, v%c1, %3\n\tv_perm_b32 v%c2, v%c2, v%c1, %4\n\tv_mov_b32 v%c1, %0" : "=&v"(t) : "i"(A), "i"(B), "s"(0x05040100u), "s"(0x07060302u));
}
template <int A, int B> __device__ __forceinline__ void zip_out(unsigned& lo, unsigned& hi) {
    asm volatile("v_perm_b32 %0, v%c3, v%c2, %4\n\tv_perm_b32 %1, v%c3, v%c2, %5" : "=&v"(lo), "=&v"(hi) : "i"(A), "i"(B), "s"(0x05040100u), "s"(0x07060302u));
}
template <int A, int B, int C, int D> __device__ __forceinline__ void zip_x(unsigned lo, unsigned hi) {
    asm volatile("v_perm_b32 v%c4, v%c3, v%c2, %6\n\tv_perm_b32 v%c5, v%c3, v%c2, %7\n\tv_mov_b32 v%c2, %0\n\tv_mov_b32 v%c3, %1"
                 :: "v"(lo), "v"(hi), "i"(A), "i"(B), "i"(C), "i"(D), "s"(0x05040100u), "s"(0x07060302u));
}
__device__ __forceinline__ void dma16(const void* sbase, unsigned voff, unsigned lds_dst) {
    unsigned keep;
    asm volatile("s_mov_b32 %0, m0\n\ts_mov_b32 m0, %3\n\ts_nop 0\n\tglobal_load_lds_dwordx4 %1, %2\n\ts_mov_b32 m0, %0"
                 : "=&s"(keep) : "v"(voff), "s"(sbase), "s"(lds_dst) : "memory");
}
__device__ __forceinline__ unsigned um4(u32x4 a) { return max(max(a[0], a[1]), max(a[2], a[3])); }
__device__ __forceinline__ bool all_set(unsigned t) { return __builtin_amdgcn_ballot_w64(t == 0u) == 0ull; }
__device__ __forceinline__ bool any_empty(unsigned mx) { return __builtin_amdgcn_ballot_w64(mx == ~0u) != 0ull; }
__device__ __forceinline__ unsigned long long any_nan(float a, float b) { return __builtin_amdgcn_ballot_w64(__builtin_isunordered(a, b)); }
__device__ __forceinline__ f32x4 asf(u32x4 a) { return __builtin_bit_cast(f32x4, a); }
__device__ __forceinline__ u32x4 asu(f32x4 a) { return __builtin_bit_cast(u32x4, a); }
#define LANES(m) __builtin_amdgcn_inverse_ballot_w64(m)

__device__ __forceinline__ bf16x4 tr64(unsigned lds_addr) { return __builtin_amdgcn_ds_read_tr16_b64_v4bf16((LAS bf16x4*)(size_t)lds_addr); }
__device__ __forceinline__ bf16x4 tr64o(unsigned lds_addr, int off) { return __builtin_amdgcn_ds_read_tr16_b64_v4bf16((LAS bf16x4*)((LAS char*)(size_t)lds_addr + off)); }
__device__ __forceinline__ bf16x8 cat8(bf16x4 a, bf16x4 b) { return __builtin_shufflevector(a, b, 0, 1, 2, 3, 4, 5, 6, 7); }
__device__ __forceinline__ bf16x8 lds16(unsigned a) { return *(const LAS bf16x8*)(size_t)a; }
__device__ __forceinline__ bf16x4 lds8(unsigned a) { return *(const LAS bf16x4*)(size_t)a; }
__device__ __forceinline__ f32x4 mfma32(bf16x8 a, bf16x8 b, f32x4 c) { return __builtin_amdgcn_mfma_f32_16x16x32_bf16(a, b, c, 0, 0, 0); }
__device__ __forceinline__ f32x4 mfma32u(u32x4 a, u32x4 b, f32x4 c) { return mfma32(__builtin_bit_cast(bf16x8, a), __builtin_bit_cast(bf16x8, b), c); }
__device__ __forceinline__ f32x4 mfma16(bf16x4 a, bf16x4 b, f32x4 c) {
    return __builtin_amdgcn_mfma_f32_16x16x16bf16_1k(__builtin_bit_cast(s16x4, a), __builtin_bit_cast(s16x4, b), c, 0, 0, 0);
}

#define STAMP(i) do {} while (0)

template <int OFF = 0> __device__ __forceinline__ void st16m(void* sbase, unsigned voff, u32x4 v, unsigned long long mask) {
    asm volatile("s_mov_b64 exec, %4\n\tglobal_store_dwordx4 %0, %1, %2 offset:%3 sc1\n\ts_mov_b64 exec, -1\n\ts_nop 0" :: "v"(voff), "v"(v), "s"(sbase), "i"(OFF), "s"(mask) : "memory");
}
template <int OFF> __device__ __forceinline__ void st16m2(void* sbase, unsigned voff, u32x4 v, unsigned voff2, u32x4 v2, unsigned long long mask) {
    asm volatile("s_mov_b64 exec, %6\n\tglobal_store_dwordx4 %0, %1, %2 sc1\n\tglobal_store_dwordx4 %4, %5, %2 offset:%3 sc1\n\ts_mov_b64 exec, -1\n\ts_nop 0"
                 :: "v"(voff), "v"(v), "s"(sbase), "i"(OFF), "v"(voff2), "v"(v2), "s"(mask) : "memory");
}
#define PINS(a) asm volatile("" : "+s"(a))
#define RETRY(LOAD, EMPTY, COMPUTE, NAN_, X) do { LOAD; COMPUTE; if (__builtin_expect((NAN_) != 0ull, 0)) { \
    _Pragma("unroll 1") for (unsigned spins_ = 0; spins_ < kSpinCap; ++spins_) { slip(X); LOAD; if (!any_empty(EMPTY)) break; } COMPUTE; } } while (0)
__device__ __forceinline__ unsigned now() { return (unsigned)__builtin_amdgcn_s_memrealtime(); }
__device__ __forceinline__ void until(unsigned t) {
    asm volatile("1:\n\ts_memrealtime vcc\n\ts_waitcnt lgkmcnt(0)\n\ts_sub_u32 vcc_lo, %0, vcc_lo\n\ts_cmp_lt_u32 vcc_lo, 0x400\n\ts_cbranch_scc1 1b" :: "s"(t - 1u) : "vcc", "scc", "memory");
}
__device__ __forceinline__ unsigned lds_word(unsigned a) {
    unsigned t_; asm volatile("ds_read_b32 %0, %1\n\ts_waitcnt lgkmcnt(0)\n\ts_nop 0" : "=v"(t_) : "v"(a) : "memory");
    return __builtin_amdgcn_readfirstlane(t_);
}
__device__ __forceinline__ void lds_set(unsigned a, unsigned v) { asm volatile("ds_write_b32 %0, %1" :: "v"(a), "v"(v) : "memory"); }
__device__ __forceinline__ int xrow(int lane) { return (lane >> 5) * 2 + ((lane >> 3) & 1); }
__device__ __forceinline__ int xcol(int lane) { return ((lane >> 4) & 1) * 8 + (lane & 7); }
__device__ __forceinline__ int swz(int r) { return r ^ (((r >> 3) & 1) << 2); }
__device__ __forceinline__ f32x2 unpk(unsigned w) { return (f32x2){lo16(w), hi16(w)}; }
__device__ __forceinline__ unsigned pk2v(f32x2 v) { return __builtin_bit_cast(unsigned, __builtin_convertvector(v, bf16x2)); }
__device__ __forceinline__ float frcp(float x) { return __builtin_amdgcn_rcpf(x); }
__device__ __forceinline__ float ex2(float x) { return __builtin_amdgcn_exp2f(x); }
__device__ __forceinline__ int lane_id() { int l; asm volatile("v_mbcnt_lo_u32_b32 %0, -1, 0\n\tv_mbcnt_hi_u32_b32 %0, -1, %0" : "=v"(l)); return l & 63; }
__device__ __forceinline__ f32x2 ln_stats(float ss, float s) {
    const float mean = s * (1.f / DM);
    const float var = fmax2(__builtin_fmaf(-mean, mean, ss * (1.f / DM)), 0.f);
    return (f32x2){bfr(mean), frcp(bfr(__builtin_amdgcn_sqrtf(bfr(bfr(var) + 1e-5f))))};
}
#define GATHER8(a, o, S) do { a[0] = ld16_sc1<-4 * (S)>(ws, o); a[1] = ld16_sc1<-3 * (S)>(ws, o); a[2] = ld16_sc1<-2 * (S)>(ws, o); a[3] = ld16_sc1<-(S)>(ws, o); \
    a[4] = ld16_sc1<0>(ws, o); a[5] = ld16_sc1<(S)>(ws, o); a[6] = ld16_sc1<2 * (S)>(ws, o); a[7] = ld16_sc1<3 * (S)>(ws, o); } while (0)
template <int OFF> __device__ __forceinline__ void again16(u32x4& r, const void* sbase, unsigned voff) {
    asm volatile("v_cmp_eq_u32 vcc, -1, %1\n\ts_mov_b64 exec, vcc\n\tglobal_load_dwordx4 %0, %2, %3 offset:%4 sc1\n\ts_mov_b64 exec, -1" : "+v"(r) : "v"(um4(r)), "v"(voff), "s"(sbase), "i"(OFF) : "memory", "vcc");
}
#define WAIT8(a, N) asm volatile("s_waitcnt vmcnt(%8)" : "+v"((a)[0]), "+v"((a)[1]), "+v"((a)[2]), "+v"((a)[3]), "+v"((a)[4]), "+v"((a)[5]), "+v"((a)[6]), "+v"((a)[7]) : "i"(N) : "memory")
#define SUM8(a) (((((((asf(a[0]) + asf(a[1])) + asf(a[2])) + asf(a[3])) + asf(a[4])) + asf(a[5])) + asf(a[6])) + asf(a[7]))
#define WAIT1(x, N) asm volatile("s_waitcnt vmcnt(%1)" : "+v"(x) : "i"(N) : "memory")
#define LO2(x) __builtin_shufflevector(x, x, 0, 1)
#define HI2(x) __builtin_shufflevector(x, x, 2, 3)
#define PIN1(a) asm volatile("" : "+v"(a))
#define PIN2(a, b) asm volatile("" : "+v"(a), "+v"(b))
#define PIN3(a, b, c) asm volatile("" : "+v"(a), "+v"(b), "+v"(c))
#define LANE_VARS() const int lane = lane_id(); \
    const int kq = lane >> 4, n = lane & 15, q4 = n >> 2, p4 = lane & 3; \
    const int r0s = 8 * kq + q4 + 4 * (kq & 1), r1s = 8 * kq + q4 + 4 * ((kq & 1) ^ 1);     \
    const int r2s = swz(4 * kq + q4);                                                       \
    (void)r0s; (void)r1s; (void)r2s; (void)p4
template <int V> struct IC { static constexpr int value = V; };
struct Frag80 { bf16x8 a0, a1; bf16x4 a2; };

__global__ void __launch_bounds__(512, 2) __attribute__((amdgpu_num_vgpr(32)))
mega_kernel(const u16*, const u16*, const u16*, const u16*, const u16*, const u16*, const u16*, const u16*, const u16*, u16*, unsigned char*
            ) {
    extern __shared__ __attribute__((aligned(16))) unsigned char smem[];
    const unsigned lb = (unsigned)(size_t)smem;
    const int wave = __builtin_amdgcn_readfirstlane(threadIdx.x >> 6);
    u64x8 ar0; u64x2 ar1; unsigned long long ar2, tp, tst64;
    asm volatile("s_memrealtime %4\n\ts_load_dwordx16 %0, %5, 0\n\ts_load_dwordx4 %1, %5, 0x40\n\ts_load_dwordx2 %2, %5, 0x50\n\ts_load_dwordx2 %3, %5, %6"
                 : "=&s"(ar0), "=&s"(ar1), "=&s"(ar2), "=&s"(tp), "=&s"(tst64) : "s"(__builtin_amdgcn_kernarg_segment_ptr()), "s"((0xa9876421u >> (4 * wave) & 15u) * 8u) : "memory");
    const int g = blockIdx.x, gj = GJ(g), gi = GI(g);
    const f32x4 z4 = {0.f, 0.f, 0.f, 0.f};
    asm volatile("" ::: "v255");
    auto when = [&](unsigned X) __attribute__((always_inline)) {
        u32x2 v;
        asm volatile("ds_read2_b32 %0, %1 offset0:14 offset1:5\n\ts_waitcnt lgkmcnt(0)" : "=v"(v) : "v"(lb + L_CNT) : "memory");
        return __builtin_amdgcn_readfirstlane(v[0]) + (X * __builtin_amdgcn_readfirstlane(v[1]) >> 12);
    };
    auto sched = [&](unsigned X, unsigned add) __attribute__((always_inline)) { until(when(X) + add); };
    auto slip = [&](unsigned X) __attribute__((always_inline)) {
        if (X) lds_set(lb + L_CNT + 56, now() - (X * lds_word(lb + L_CNT + 20) >> 12));
    };
    float xe = 0.f;

    auto code_pf = [&](int slice, int lane) __attribute__((always_inline)) {
        unsigned long long pc0; unsigned len, back, t;
        asm volatile("s_getpc_b64 %0\n.Lmega_pf%=:\n\ts_mov_b32 %1, .Lmega_code_end-.Lmega_code_begin\n\ts_mov_b32 %2, .Lmega_pf%=-.Lmega_code_begin" : "=s"(pc0), "=s"(len), "=s"(back));
        unsigned off = (unsigned)(slice * 4096 + lane * 64); if (off > len) off = len;
        asm volatile("global_load_dword %0, %1, %2" : "=v"(t) : "v"(off & ~3u), "s"(pc0 - back) : "memory");
        return t;
    };
    asm volatile(".Lmega_code_begin:");
    unsigned pf = 0u, pt1, dga, dgb;
    const unsigned wofs = (unsigned)((((gj >> 1) * HQ + gi) * SK + (SK - WIN) + (gj & 1) * 512) * (DH * 2));
    {
        const int lane = threadIdx.x & 63, hi = lane >= 26;
        if (threadIdx.x < 24) lds_set(lb + L_CNT + threadIdx.x * 4, 0u);
        unsigned t2 = (unsigned)(gi * (DH * DM * 2)), t7 = (32u << 20) + (unsigned)(g * 128), to = (unsigned)(gj * 128), w2 = wofs;
        PINS(t2); PINS(t7); PINS(to); PINS(w2);
        to = wave == 7 ? t7 : to; to = wave == 2 ? t2 : to; to = wave < 2 ? w2 : to;
        if (wave == 7) pf = code_pf((g >> 3) & 15, lane);
        unsigned vo = (unsigned)((gj * 80 + swz(2 * wave) + hi) * (QD * 2) + gi * (DH * 2) + (lane - 26 * hi) * 16), d = lb + L_WQ + wave * 832, keep;
        unsigned xo = (0x11043210u >> (4 * wave) & 15u) * 1024 + (unsigned)(lane & (wave < 6 ? 63 : 15)) * 16, xd = lb + L_X0 + min(wave, 6) * 1024;
        PIN2(vo, xo); PINS(d); PINS(xd); PINS(to);
        __builtin_amdgcn_sched_barrier(0);
        unsigned long long ta_, tb_;
        asm volatile("s_memrealtime %0\n\ts_waitcnt lgkmcnt(0)\n\ts_memrealtime %1\n\ts_waitcnt lgkmcnt(0)" : "=&s"(ta_), "=&s"(tb_) :: "memory");
        dga = (unsigned)ta_; dgb = (unsigned)tb_;
        asm volatile("s_waitcnt lgkmcnt(0)\n\ts_load_dword %0, %4, %6" : "=&s"(pt1), "+s"(ar0), "+s"(ar1), "+s"(ar2), "+s"(tp), "+s"(tst64) : "s"(to) : "memory");
        dma16((const void*)(wave < 5 ? ar0[0] : ar0[5]), xo, xd);
        asm volatile("s_mov_b32 %0, m0\n\ts_mov_b32 m0, %2\n\ts_mov_b64 exec, %4\n\tglobal_load_lds_dwordx4 %1, %3 nt\n\t"
                     ".rept 4\n\ts_add_u32 m0, m0, 6656\n\tv_add_u32 %1, 0x30000, %1\n\tglobal_load_lds_dwordx4 %1, %3 nt\n\t.endr\n\t"
                     "s_mov_b64 exec, -1\n\ts_mov_b32 m0, %0\n\ts_barrier"
                     : "=&s"(keep), "+v"(vo) : "s"(d), "s"(ar0[3]), "s"(0x0003fffffcffffffull) : "memory", "scc");
        __builtin_amdgcn_sched_barrier(0);
    }
    const u16 *const kc = (const u16*)ar0[1], *const vc = (const u16*)ar0[2], *const wo = (const u16*)ar0[4], *const wg = (const u16*)ar0[6], *const wu = (const u16*)ar0[7], *const wd = (const u16*)ar1[0];
    u16* const out = (u16*)ar1[1];
    unsigned char* const ws = (unsigned char*)((ar2 + (32u << 20) - 1) & ~(unsigned long long)((16u << 20) - 1));
    const unsigned tst = (unsigned)tst64;
    auto fix_k1 = [&](auto IC_, auto NC) __attribute__((always_inline)) {
        constexpr int A = RK + 8 * decltype(IC_)::value, B = A + 4;
        unsigned t0, t1, t2, t3;
        asm volatile("s_waitcnt vmcnt(%12)\n\tv_mov_b32 %0, v%c4\n\tv_mov_b32 %1, v%c5\n\tv_mov_b32 %2, v%c6\n\tv_mov_b32 %3, v%c7\n\t"
                     "v_mov_b32_dpp v%c4, v%c8 row_ror:8 row_mask:0xf bank_mask:0xc\n\tv_mov_b32_dpp v%c5, v%c9 row_ror:8 row_mask:0xf bank_mask:0xc\n\t"
                     "v_mov_b32_dpp v%c6, v%c10 row_ror:8 row_mask:0xf bank_mask:0xc\n\tv_mov_b32_dpp v%c7, v%c11 row_ror:8 row_mask:0xf bank_mask:0xc\n\t"
                     "v_mov_b32_dpp v%c8, %0 row_ror:8 row_mask:0xf bank_mask:0x3\n\tv_mov_b32_dpp v%c9, %1 row_ror:8 row_mask:0xf bank_mask:0x3\n\t"
                     "v_mov_b32_dpp v%c10, %2 row_ror:8 row_mask:0xf bank_mask:0x3\n\tv_mov_b32_dpp v%c11, %3 row_ror:8 row_mask:0xf bank_mask:0x3"
                     : "=&v"(t0), "=&v"(t1), "=&v"(t2), "=&v"(t3)
                     : "i"(A), "i"(A + 1), "i"(A + 2), "i"(A + 3), "i"(B), "i"(B + 1), "i"(B + 2), "i"(B + 3), "i"(decltype(NC)::value) : "memory");
    };
    auto trans_v = [&](auto UC) __attribute__((always_inline)) {
        constexpr int R = RV + 4 * decltype(UC)::value, P = 4 * decltype(UC)::value;
#define CELL(r, c) R + 16 * (r) + (c), R + 16 * (r) + 8 + (c)
        unsigned a0, a1, b0, b1, c0, c1;
        VMW(v_after(P)); zip_in<CELL(0, 0)>(); zip_out<CELL(0, 1)>(a0, a1); zip_out<CELL(0, 2)>(b0, b1); zip_out<CELL(0, 3)>(c0, c1);
        VMW(v_after(P + 1)); zip_x<CELL(1, 0), CELL(0, 1)>(a0, a1); zip_in<CELL(1, 1)>(); zip_out<CELL(1, 2)>(a0, a1);
        VMW(v_after(P + 2)); zip_x<CELL(2, 0), CELL(0, 2)>(b0, b1); zip_x<CELL(2, 1), CELL(1, 2)>(a0, a1); zip_in<CELL(2, 2)>(); zip_out<CELL(1, 3)>(a0, a1); zip_out<CELL(2, 3)>(b0, b1);
        VMW(v_after(P + 3)); zip_x<CELL(3, 0), CELL(0, 3)>(c0, c1); zip_x<CELL(3, 1), CELL(1, 3)>(a0, a1); zip_x<CELL(3, 2), CELL(2, 3)>(b0, b1); zip_in<CELL(3, 3)>();
#undef CELL
    };
    auto trans_w = [&](auto UC) __attribute__((always_inline)) {
        constexpr int R = RV + 64 + 2 * decltype(UC)::value, N = v_after(8 + decltype(UC)::value);
        unsigned a0, a1;
        VMW(N + 2); zip_in<R, R + 8>(); zip_out<R + 1, R + 9>(a0, a1);
        VMW(N); zip_x<R + 16, R + 24, R + 1, R + 9>(a0, a1); zip_in<R + 17, R + 25>();
    };
    auto frag80 = [&](unsigned im, int rs, int r0s, int r1s, int r2s) __attribute__((always_inline)) -> Frag80 {
        return Frag80{cat8(tr64(im + r0s * rs), tr64(im + r1s * rs)), cat8(tr64(im + (r0s + 32) * rs), tr64(im + (r1s + 32) * rs)), tr64(im + (r2s + 64) * rs)};
    };
    auto rows80 = [&](unsigned a16, unsigned a8) __attribute__((always_inline)) -> Frag80 { const Frag80 f{lds16(a16), lds16(a16 + 64), lds8(a8)}; __builtin_amdgcn_sched_barrier(0); return f; };
    auto empty5 = [&](int q0, unsigned S, int lane) __attribute__((always_inline)) {
        st16m(ws, tile35(gj, q0 + (lane >> 3)) + S + (unsigned)(gi * 128 + (lane & 7) * 16), (u32x4){~0u, ~0u, ~0u, ~0u}, 0xffffffffffull);
    };
    auto empty4 = [&](int layer, unsigned S, int lane) __attribute__((always_inline)) {
        const unsigned b = WS_H4 + (unsigned)((layer * 32 + gi) * 32768) + S;
        st16_sc1(ws, b + (unsigned)((lane >> 5) * 4096 + gj * 512 + (lane & 31) * 16), (u32x4){~0u, ~0u, ~0u, ~0u});
        st16m(ws, b + (unsigned)(lane < 32 ? 16384 + gj * 512 + lane * 16 : H4C + ((lane - 32) >> 3) * 1024 + gj * 128 + (lane & 7) * 16), (u32x4){~0u, ~0u, ~0u, ~0u},
              layer ? 0xff000000ffffffffull : ~0ull);
    };
    int lead = (int)((0x00003411u | LEAD << 28) >> (4 * wave) & 15u) * PACE * 4;
    auto pace = [&](unsigned n, bool wait = true) __attribute__((always_inline)) {
        unsigned u, w, t0;
        asm volatile("s_mov_b64 exec, 1\n\tds_add_rtn_u32 %0, %3, %4\n\tds_max_u32 %3, %5 offset:12\n\tds_add_rtn_u32 %1, %3, %6 offset:12\n\tds_read_b32 %2, %3 offset:4\n\ts_mov_b64 exec, -1\n\ts_waitcnt lgkmcnt(0)"
                     : "=&v"(u), "=&v"(w), "=&v"(t0) : "v"(lb + L_CNT + 60), "v"(n), "v"((now() - tst) << 6), "v"(n * PMIN * 4u) : "memory");
        int tn = (int)(__builtin_amdgcn_readfirstlane(t0) - tst + PINIT) * 64 + __builtin_amdgcn_readfirstlane(u) * (PACE * 4);
        const int tw = __builtin_amdgcn_readfirstlane(w) - PBURST * 64;
        if (tw > tn) tn = tw;
        tn -= lead;
        if (wait) {
#pragma unroll 1
            for (int k = 0; k < 2048 && (int)((now() - tst) << 6) < tn; ++k) {}
        }
    };
    auto slot = [&](unsigned u) __attribute__((always_inline)) {
        const unsigned tn = lds_word(lb + L_CNT + 64) + PINIT + u * PACE / 16;
#pragma unroll 1
        for (int k = 0; k < 8192 && (int)(now() - tn) < 0; ++k) __builtin_amdgcn_s_sleep(1);
    };
    auto load_wo = [&](int k, int lane, bool early) __attribute__((always_inline)) {
        const int c = k * 64 + lane, pp = c / 10, cc = c - 10 * pp;
        const unsigned f = (unsigned)(swz(pp) * 1280 + cc * 16);
        const u16* wb = wo + (size_t)gi * DH * DM + gj * 80;
#pragma unroll
        for (int a2 = 0; a2 < 6; ++a2) { if (a2 % 3 == 0) { if (early) pace(3); else slot(NUNIT - WOFF + (2 * k + a2 / 3) * WOC); } dma16(wb + 32 * a2 * DM, f, lb + L_WO + (5 * a2 + k) * 1024); }
    };
    auto load_wd = [&](int lane) __attribute__((always_inline)) {
        const u16* db = wd + gi * 48 * DM + gj * 80;
#pragma unroll 1
        for (int k = 0; k < 5; ++k) {
            const int c = k * 64 + lane, pp = c / 10, cc = c - 10 * pp;
            const unsigned fd = (unsigned)(swz(pp) * 1280 + cc * 16);
            if (!(k & 1)) pace(3);
            dma16(db, fd, lb + L_WD + k * 1024);
            if (k < 3) dma16(db + 32 * DM, (k < 2 || lane < 32) ? fd : 0u, lb + L_WD + (5 + k) * 1024);
        }
    };
    auto load_gu = [&](int m, int lane) __attribute__((always_inline)) {
#pragma unroll 1
        for (int k0 = 0; k0 < 3; ++k0) {
            const int k = (k0 + (g & 7)) % 3, c = k * 64 + lane, row = c / 6, cc = c - 6 * row;
            const unsigned o = (unsigned)((swz(row) * FF + cc * 8) * 2), d = lb + L_GU + m * 8192 + k * 1024;
            const u16* wb = (m == 0 ? wg : wu) + gj * 80 * FF + gi * 48;
            pace(3);
            dma16(wb, o, d); dma16(wb + 32 * FF, o, d + 3072);
            if (k < 2) dma16(wb + 64 * FF, (k == 0 || lane < 32) ? o : 0u, d + 6144);
        }
    };
    auto lds_add = [&](int off, unsigned v) __attribute__((always_inline)) { asm volatile("s_mov_b64 exec, 1\n\tds_add_u32 %0, %1\n\ts_mov_b64 exec, -1" :: "v"(lb + L_CNT + off), "v"(v) : "memory"); };
    auto send_flag = [&](int lane) __attribute__((always_inline)) {
        st4_sc1(ws, WS_MB + (unsigned)((gi * 8 + ((gj == (gi & 7) ? lane : gi) & 7)) * 128 + gj * 4), lds_word(lb + L_CNT + 68));
    };
    auto tiles4 = [&](const Frag80 (&f)[4], const Frag80& b, unsigned long long mk) __attribute__((always_inline)) -> u32x4 {
        f32x4 c[4], d[4];
#pragma unroll
        for (int i = 0; i < 4; ++i) c[i] = mfma32(f[i].a0, b.a0, z4);
#pragma unroll
        for (int i = 0; i < 4; ++i) { c[i] = mfma32(f[i].a1, b.a1, c[i]); d[i] = mfma16(f[i].a2, b.a2, z4); }
        __builtin_amdgcn_sched_barrier(0);
        f32x2 lo, hi;
        asm volatile("s_nop 1\n\ts_mov_b64 exec, %18\n\tv_pk_add_f32 %0, %2, %10\n\tv_pk_add_f32 %1, %3, %11\n\t"
                     "s_lshl_b64 exec, exec, 1\n\tv_pk_add_f32 %0, %4, %12\n\tv_pk_add_f32 %1, %5, %13\n\t"
                     "s_lshl_b64 exec, exec, 1\n\tv_pk_add_f32 %0, %6, %14\n\tv_pk_add_f32 %1, %7, %15\n\t"
                     "s_lshl_b64 exec, exec, 1\n\tv_pk_add_f32 %0, %8, %16\n\tv_pk_add_f32 %1, %9, %17\n\ts_mov_b64 exec, -1"
                     : "=&v"(lo), "=&v"(hi) : "v"(LO2(c[0])), "v"(HI2(c[0])), "v"(LO2(c[1])), "v"(HI2(c[1])), "v"(LO2(c[2])), "v"(HI2(c[2])), "v"(LO2(c[3])), "v"(HI2(c[3])),
                       "v"(LO2(d[0])), "v"(HI2(d[0])), "v"(LO2(d[1])), "v"(HI2(d[1])), "v"(LO2(d[2])), "v"(HI2(d[2])), "v"(LO2(d[3])), "v"(HI2(d[3])), "s"(mk) : "scc");
        return asu(__builtin_shufflevector(lo, hi, 0, 1, 2, 3));
    };
    auto empty1 = [&](int half, unsigned S, int lane) __attribute__((always_inline)) {
        const unsigned o = WS_H1 + S + (unsigned)((gi * 4 + 2 * half) * 16384 + gj * 896 + min(lane, 55) * 16);
        st16_sc1(ws, o, (u32x4){~0u, ~0u, ~0u, ~0u}); st16_sc1(ws, o + 16384, (u32x4){~0u, ~0u, ~0u, ~0u});
    };
    auto report = [&]() __attribute__((always_inline)) { if (gj == (gi & 7)) st4_sc1(ws, WS_R + (unsigned)(gi * 4), lds_word(lb + L_CNT + 88)); };
    auto answer = [&](int lane, unsigned r) __attribute__((always_inline)) {
        int d = min(max((int)(r - (tst << 11)) >> 11, -400), 600);
        unsigned k = (r >> 5) & 63u, v = (r >> 1) & 15u;
        v += dppu<0x124>(v); v += dppu<0x128>(v);
#define VOTES(l) (__builtin_amdgcn_readlane(v, l) + __builtin_amdgcn_readlane(v, 16 + l))
        const unsigned c = (VOTES(0) > 32 ? 2u : 0u) | (VOTES(1) > 32 ? 4u : 0u) | (VOTES(2) + VOTES(3) > 64 ? 8u : 0u);
        d += (int)dppu<0xB1>((unsigned)d); d += (int)dppu<0x4E>((unsigned)d); d += (int)dppu<0x141>((unsigned)d); d += (int)dppu<0x140>((unsigned)d);
        k = max(k, dppu<0xB1>(k)); k = max(k, dppu<0x4E>(k)); k = max(k, dppu<0x141>(k)); k = max(k, dppu<0x140>(k));
        d = __builtin_amdgcn_readlane(d, 0) + __builtin_amdgcn_readlane(d, 16);
        k = max((unsigned)__builtin_amdgcn_readlane(k, 0), (unsigned)__builtin_amdgcn_readlane(k, 16));
        st4_sc1(ws, WS_MB + (unsigned)(((lane & 31) * 8 + (lane & 7)) * 128 + 32), (tst + (d >> 5) + TOFF) << 10 | k << 4 | c | 1u);
        lds_set(lb + L_CNT + 36, 1u);
    };
    auto adopt = [&](unsigned t) __attribute__((always_inline)) {
        const int dt = (int)(t - (tst << 10)) >> 10;
        if (dt > -256 && dt < 512) lds_set(lb + L_CNT + 64, tst + max(dt, (int)(lds_word(lb + L_CNT + 88) - (tst << 11) ) / 2048 - CL));
        lds_set(lb + L_CNT + 20, 4096u + ((t >> 4) & 63u) * BETA);
        lds_set(lb + L_CNT + 80, lds_word(lb + L_CNT + 92) ^ t);
        lds_set(lb + L_CNT + 84, (((t >> 1) ^ lds_word(lb + L_CNT + 68)) & 4u) << 11);
        lds_set(lb + L_CNT + 52, 1u);
    };
    struct StatAddr { unsigned a16, b16, a8, b8, dst; };
    auto stats_addr = [&](int lane, unsigned dbase, int dstride) __attribute__((always_inline)) -> StatAddr {
        const int kq = lane >> 4, q4 = (lane >> 2) & 3, p4 = lane & 3;
        const int x1 = LANES(0x4444444444444444ull) ? L_WR + gj * 160 : L_XS + q4 * 160, xa = LANES(0xaaaaaaaaaaaaaaaaull) ? L_XS + 640 : x1;
        StatAddr r{lb + (unsigned)(xa + kq * 16), lb + (unsigned)(L_XS + p4 * 160 + kq * 16), lb + (unsigned)(xa + 128 + kq * 8), lb + (unsigned)(L_XS + p4 * 160 + 128 + kq * 8), dbase + (unsigned)(kq * dstride)};
        PIN3(r.a16, r.b16, r.dst); PIN2(r.a8, r.b8);
        return r;
    };
    auto stats = [&](const StatAddr& r) __attribute__((always_inline)) {
        const bf16x8 a0 = lds16(r.a16), b0 = lds16(r.b16), a1 = lds16(r.a16 + 64), b1 = lds16(r.b16 + 64);
        const bf16x4 a2 = lds8(r.a8), b2 = lds8(r.b8);
        unsigned long long mk = 0x0008000400020001ull; PINS(mk);
        __builtin_amdgcn_sched_barrier(0);
        st16m(ws, r.dst, asu(mfma32(a1, b1, mfma32(a0, b0, z4)) + mfma16(a2, b2, z4)), mk);
    };
    unsigned qa, pw, ma, idx, qm;
    auto qk_addr = [&](bool L1) __attribute__((always_inline)) {
        LANE_VARS();
        qa = lb + L_Q + (unsigned)(kq * 32 + (lane & 2) * 8);
        pw = lb + L_P + (unsigned)((7 + 8 * wave) * 192 + (kq >> 1) * 64 + (n >> 3) * 32 + (lane & 2) * 8 + (q4 & 1) * 8 + (kq & 1) * 4 + (lane & 1) * 2);
        ma = lb + L_M + (unsigned)(wave * 4);
        idx = (lane & 2) ? 0xeeeeu : 0x4444u;
        qm = lb + L_Q;
        PIN3(qa, pw, ma); PIN2(idx, qm);
        if (L1 && wave < 4) __builtin_amdgcn_s_setprio(2);
    };
    auto att_qk0 = [&]() __attribute__((always_inline)) {
        qk_addr(false);
#pragma unroll 1
        for (unsigned spins = 0; spins < (kSpinCap << 4); ++spins) {
            if (lds_word(lb + L_CNT + 4) != 0u) break;
            __builtin_amdgcn_s_sleep(1);
        }
        STAMP(3);
        const u32x4 q0 = *(const LAS u32x4*)(size_t)qa, q1 = *(const LAS u32x4*)(size_t)(qa + 128), q2 = *(const LAS u32x4*)(size_t)(qa + 256);
        f32x4 c0 = z4, c1 = z4, c2 = z4, c3 = z4;
#define SMF(c, q, r) "v_smfmac_f32_16x16x64_bf16 %" #c ", %" #q ", v[" #r "], %7\n\t"
        asm volatile("s_nop 1\n\t" SMF(0, 4, 64:71) SMF(1, 4, 88:95) SMF(2, 4, 112:119) SMF(3, 4, 136:143) SMF(0, 5, 72:79) SMF(1, 5, 96:103) SMF(2, 5, 120:127) SMF(3, 5, 144:151)
                     SMF(0, 6, 80:87) SMF(1, 6, 104:111) SMF(2, 6, 128:135) SMF(3, 6, 152:159) "s_nop 7"
                     : "+v"(c0), "+v"(c1), "+v"(c2), "+v"(c3) : "v"(q0), "v"(q1), "v"(q2), "v"(idx));
#undef SMF
        const float s0 = c0[0] + c0[2], s1 = c1[0] + c1[2], s2 = c2[0] + c2[2], s3 = c3[0] + c3[2];
        float m = fmax2(fmax3(s0, s1, s2), s3);
        m = fmax2(m, dpp<0xB1>(m)); m = fmax2(m, dpp<0x4E>(m)); m = fmax2(m, dpp<0x124>(m)); m = fmax2(m, dpp<0x128>(m));
        const float s01 = LANES(0xffff0000ffff0000ull) ? s1 : s0, s23 = LANES(0xffff0000ffff0000ull) ? s3 : s2;
        const float sv = LANES(0xffffffff00000000ull) ? s23 : s01;
        m = bfr(bfr(m) * SCALE);
        const float p = ex2((bfr(bfr(sv) * SCALE) - m) * LOG2E);
        *(LAS u16*)(size_t)pw = (u16)pk2(p, p);
        *(LAS float*)(size_t)ma = m;
        __builtin_amdgcn_sched_barrier(0);
    };
    unsigned pr, oa, oc, cma, cxa, cra, cso;
    auto att_addr = [&]() __attribute__((always_inline)) {
        LANE_VARS();
        pr = lb + L_P + (unsigned)((7 + 8 * wave - 2 * q4 - (lane & 1)) * 192 + kq * 32 + (lane & 2) * 8);
        oa = lb + L_OP + (unsigned)(wave * 1024 + (lane & 7) * 64 + kq * 16 + (n >> 3) * 8);
        oc = lb + L_OP + (unsigned)(wave * 1024 + 512 + (kq >> 1) * 256 + (lane & 7) * 32 + (kq & 1) * 16 + (n >> 3) * 8);
        PIN3(pr, oa, oc);
    };
    auto comb_out = [&](float u0, float u1, unsigned so, unsigned long long mk, bool both) __attribute__((always_inline)) {
        const unsigned w = min(pk2(u0, u1), 0xfffffffeu);
        if (!both) { st4_sc1(ws, so, w); return; }
        const u32x4 w4 = {dppu<0x00>(w), dppu<0x55>(w), dppu<0xAA>(w), dppu<0xFF>(w)};
        st16m(ws, so, w4, mk);
    };
    auto max8 = [&](f32x4 m0, f32x4 m1) __attribute__((always_inline)) { return fmax3(fmax3(m0[0], m0[1], m0[2]), fmax3(m0[3], m1[0], m1[1]), fmax2(m1[2], m1[3])) * LOG2E; };
    auto comb_o0 = [&](f32x4 m0, f32x4 m1, f32x4 mm, f32x4 o0, f32x4 o1, f32x4 o2, f32x4 o3, unsigned so, unsigned long long mk, bool both) __attribute__((always_inline)) {
        const float M = max8(m0, m1);
        f32x4 f;
#pragma unroll
        for (int k = 0; k < 4; ++k) f[k] = ex2(__builtin_fmaf(mm[k], LOG2E, -M));
        const f32x4 o = f[3] * o3 + (f[2] * o2 + (f[1] * o1 + f[0] * o0));
        comb_out(rs32(o[0], o[2]), rs32(o[1], o[3]), so, mk, both);
    };
    auto comb_o1 = [&](f32x4 m0, f32x4 m1, f32x2 mm, f32x4 o0, f32x4 o1, unsigned so, unsigned long long mk, bool both) __attribute__((always_inline)) {
        const float M = max8(m0, m1);
        const f32x4 o = ex2(__builtin_fmaf(mm[1], LOG2E, -M)) * o1 + ex2(__builtin_fmaf(mm[0], LOG2E, -M)) * o0;
        comb_out(x16_add(rs32(o[0], o[2])), x16_add(rs32(o[1], o[3])), so, mk, both);
    };
    auto ml_addr = [&](int lane, unsigned& xa, unsigned& ra) __attribute__((always_inline)) {
        xa = lb + L_M + (unsigned)(((lane & 15) >> 1) * 4); ra = lb + L_P + (unsigned)((7 + 8 * ((lane & 15) >> 1)) * 192 + (lane & 1) * 64 + (lane >> 4) * 16);
    };
    auto comb_ml = [&](unsigned ma, unsigned xa, unsigned ra, unsigned so, bool both) __attribute__((always_inline)) {
        const u32x4 x4 = *(const LAS u32x4*)(size_t)ra;
        const f32x4 m0 = *(const LAS f32x4*)(size_t)ma, m1 = *(const LAS f32x4*)(size_t)(ma + 16);
        const float mw = *(const LAS float*)(size_t)xa;
        __builtin_amdgcn_sched_barrier(0);
        const f32x4 sp = mfma32u((u32x4){0x3f803f80u, 0x3f803f80u, 0x3f803f80u, 0x3f803f80u}, x4, z4);
        __builtin_amdgcn_sched_barrier(0);
        const float M = max8(m0, m1);
        float L = row16_sum(sp[0] * ex2(__builtin_fmaf(mw, LOG2E, -M)));
        if (both) L = dpp<0x00>(L);
        st16m(ws, so, asu((f32x4){M, L, M, L}), both ? 3ull : 1ull);
    };
    auto att_pv0 = [&]() __attribute__((always_inline)) {
        u32x4 bm[8];
        trans_v(IC<0>{});
        if (v_after(9) > v_after(7)) { trans_w(IC<0>{}); trans_w(IC<1>{}); }
        trans_v(IC<1>{});
#pragma unroll
        for (int e = 0; e < 8; ++e) bm[e] = *(const LAS u32x4*)(size_t)(pr + e * 192);
        __builtin_amdgcn_sched_barrier(0);
        f32x4 da = z4, dc = z4;
        PIN2(da, dc);
        asm volatile("s_nop 1");
        __builtin_amdgcn_sched_barrier(0);
        f32x2 r;
        smf<RV>(da, bm[0], idx); smf<RV + 8>(da, bm[1], idx); smf<RV + 16>(da, bm[2], idx); smf<RV + 24>(da, bm[3], idx);
        smf<RV + 32>(da, bm[4], idx); smf<RV + 40>(da, bm[5], idx); smf<RV + 48>(da, bm[6], idx); smf<RV + 56>(da, bm[7], idx);
        asm volatile("s_nop 7\n\tv_pk_add_f32 %0, %1, %2\n\tds_write_b64 %3, %0\n\ts_mov_b64 exec, 1\n\tds_add_u32 %4, %5\n\ts_mov_b64 exec, -1"
                     : "=&v"(r) : "v"(LO2(da)), "v"(HI2(da)), "v"(oa), "v"(lb + L_CNT + 24), "v"(1u) : "memory");
        GUARD(900);
        MARK(b_t5);
        if (v_after(9) < v_after(7)) { trans_w(IC<0>{}); trans_w(IC<1>{}); }
        trans_w(IC<2>{}); trans_w(IC<3>{});
        STAMP(4);
        asm volatile("s_nop 1\n\tv_smfmac_f32_16x16x64_bf16 %0, %1, v[224:231], %5\n\tv_smfmac_f32_16x16x64_bf16 %0, %2, v[232:239], %5\n\t"
                     "v_smfmac_f32_16x16x64_bf16 %0, %3, v[240:247], %5\n\tv_smfmac_f32_16x16x64_bf16 %0, %4, v[248:255], %5\n\ts_nop 6"
                     : "+v"(dc) : "v"(bm[0]), "v"(bm[1]), "v"(bm[2]), "v"(bm[3]), "v"(idx));
        asm volatile("s_nop 0\n\tv_pk_add_f32 %0, %1, %2\n\tds_write_b64 %3, %0" : "=&v"(r) : "v"(LO2(dc)), "v"(HI2(dc)), "v"(oc) : "memory");
        STAMP(20);
    };
    unsigned long long ck1 = 0xffff0000ffff0000ull, ck2 = 0xffffffff00000000ull, csc = 0x3d93cd3a3d93cd3aull;
    auto att1 = [&]() __attribute__((always_inline)) {
        constexpr int layer = 1;
        {
#define SMF(c, q, r) "v_smfmac_f32_16x16x64_bf16 v[" #c "], v[" #q "], v[" #r "], %0\n\t"
#define MAXD(x) "v_max_f32_dpp v41, v41, v41 " x " row_mask:0xf bank_mask:0xf\n\t"
            asm volatile("s_waitcnt lgkmcnt(0)\n\ts_barrier\n\t"
                         "ds_read_b128 v[0:3], %1\n\tds_read2_b32 v[12:13], %2 offset0:96 offset1:100\n\tds_read_b128 v[4:7], %1 offset:128\n\tds_read_b128 v[8:11], %1 offset:256\n\t"
                         "v_mov_b64 v[16:17], 0\n\tv_mov_b64 v[18:19], 0\n\tv_mov_b64 v[20:21], 0\n\tv_mov_b64 v[22:23], 0\n\tv_mov_b64 v[24:25], 0\n\tv_mov_b64 v[26:27], 0\n\t"
                         "v_mov_b64 v[28:29], 0\n\tv_mov_b64 v[30:31], 0\n\tv_mov_b64 v[32:33], 0\n\tv_mov_b64 v[34:35], 0\n\tv_mov_b64 v[36:37], 0\n\tv_mov_b64 v[38:39], 0\n\t"
                         "s_waitcnt lgkmcnt(3)\n\t" SMF(16:19, 0:3, 64:71) SMF(20:23, 0:3, 88:95) "s_waitcnt lgkmcnt(1)\n\tv_mul_f32 v13, 0x3acccccd, v13\n\t"
                         SMF(24:27, 0:3, 112:119) "v_mul_f32 v12, 0x3acccccd, v12\n\t" SMF(28:31, 0:3, 136:143) "v_fma_f32 v12, -v13, v13, v12\n\t"
                         SMF(16:19, 4:7, 72:79) "v_maximum3_f32 v12, v12, 0, 0\n\t" SMF(20:23, 4:7, 96:103) "v_cvt_pk_bf16_f32 v12, 0, v12\n\t"
                         SMF(24:27, 4:7, 120:127) "v_add_f32 v12, 0x3727c5ac, v12\n\t" SMF(28:31, 4:7, 144:151) "v_cvt_pk_bf16_f32 v12, 0, v12\n\ts_waitcnt lgkmcnt(0)\n\t"
                         SMF(16:19, 8:11, 80:87) "v_sqrt_f32 v12, v12\n\t" SMF(20:23, 8:11, 104:111) "v_cvt_pk_bf16_f32 v12, 0, v12\n\t"
                         SMF(24:27, 8:11, 128:135) "v_rcp_f32 v12, v12\n\t" SMF(28:31, 8:11, 152:159)
                         "s_nop 2\n\tv_add_f32 v42, v16, v18\n\ts_nop 0\n\tv_add_f32 v43, v20, v22\n\tv_max_f32 v41, v42, v43\n\tv_add_f32 v44, v24, v26\n\tv_add_f32 v45, v28, v30\n\t"
                         "v_max3_f32 v41, v41, v44, v45\n\tv_cndmask_b32 v42, v42, v43, %8\n\tv_cndmask_b32 v44, v44, v45, %8\n\t"
                         MAXD("quad_perm:[1,0,3,2]") "v_cndmask_b32 v40, v42, v44, %9\n\ts_nop 0\n\t" MAXD("quad_perm:[2,3,0,1]") "s_nop 1\n\t" MAXD("row_ror:4") "s_nop 1\n\t" MAXD("row_ror:8")
                         "v_pk_mul_f32 v[40:41], v[12:13], v[40:41] op_sel_hi:[0,1]\n\tv_cvt_pk_bf16_f32 v40, 0, v40\n\tv_cvt_pk_bf16_f32 v41, 0, v41\n\t"
                         "v_pk_mul_f32 v[40:41], %10, v[40:41]\n\tv_cvt_pk_bf16_f32 v40, 0, v40\n\tv_cvt_pk_bf16_f32 v41, 0, v41\n\t"
                         "v_sub_f32 v40, v40, v41\n\tv_mul_f32 v40, 0x3fb8aa3b, v40\n\tv_exp_f32 v40, v40\n\ts_nop 0\n\tv_cvt_pk_bf16_f32 v40, v40, v40\n\tds_write_b16 %3, v40\n\t"
                         "ds_read_b128 v[0:3], %4\n\tds_read_b128 v[4:7], %4 offset:192\n\tds_read_b128 v[8:11], %4 offset:384\n\tds_read_b128 v[12:15], %4 offset:576\n\t"
                         "ds_read_b128 v[16:19], %4 offset:768\n\tds_read_b128 v[20:23], %4 offset:960\n\tds_read_b128 v[24:27], %4 offset:1152\n\tds_read_b128 v[28:31], %4 offset:1344\n\t"
                         "ds_write_b32 %5, v41\n\t"
                         "s_waitcnt lgkmcnt(8)\n\t" SMF(32:35, 0:3, 160:167) SMF(36:39, 0:3, 224:231) "s_waitcnt lgkmcnt(7)\n\t" SMF(32:35, 4:7, 168:175) SMF(36:39, 4:7, 232:239)
                         "s_waitcnt lgkmcnt(6)\n\t" SMF(32:35, 8:11, 176:183) SMF(36:39, 8:11, 240:247) "s_waitcnt lgkmcnt(4)\n\t" SMF(32:35, 12:15, 184:191) SMF(36:39, 12:15, 248:255)
                         SMF(32:35, 16:19, 192:199) "s_waitcnt lgkmcnt(1)\n\t" SMF(32:35, 20:23, 200:207) SMF(32:35, 24:27, 208:215) SMF(32:35, 28:31, 216:223)
                         "s_nop 2\n\tv_pk_add_f32 v[36:37], v[36:37], v[38:39]\n\tds_write_b64 %7, v[36:37]\n\ts_nop 2\n\tv_pk_add_f32 v[32:33], v[32:33], v[34:35]\n\tds_write_b64 %6, v[32:33]"
                         :: "v"(idx), "v"(qa), "v"(qm), "v"(pw), "v"(pr), "v"(ma), "v"(oa), "v"(oc), "s"(ck1), "s"(ck2), "s"(csc)
                         : "memory", "v0", "v1", "v2", "v3", "v4", "v5", "v6", "v7", "v8", "v9", "v10", "v11", "v12", "v13", "v14", "v15", "v16", "v17", "v18", "v19", "v20", "v21", "v22", "v23",
                           "v24", "v25", "v26", "v27", "v28", "v29", "v30", "v31", "v32", "v33", "v34", "v35", "v36", "v37", "v38", "v39", "v40", "v41", "v42", "v43", "v44", "v45");
#undef SMF
#undef MAXD
            STAMP(20 + 32 * layer);
            BAR();
            if (__builtin_expect(wave == 0, 1)) {
                STAMP(5 + 32 * layer);
                comb_o0(*(const LAS f32x4*)(size_t)cma, *(const LAS f32x4*)(size_t)(cma + 16), *(const LAS f32x4*)(size_t)cxa, *(const LAS f32x4*)(size_t)cra,
                        *(const LAS f32x4*)(size_t)(cra + 1024), *(const LAS f32x4*)(size_t)(cra + 2048), *(const LAS f32x4*)(size_t)(cra + 3072), cso, 0x1111111111111111ull, false);
            }
            __builtin_amdgcn_sched_barrier(0);
            if (__builtin_expect(wave == 1, 1))
                comb_o1(*(const LAS f32x4*)(size_t)cma, *(const LAS f32x4*)(size_t)(cma + 16), *(const LAS f32x2*)(size_t)cxa, *(const LAS f32x4*)(size_t)cra, *(const LAS f32x4*)(size_t)(cra + 1024), cso, 0x0000111100001111ull, false);
            __builtin_amdgcn_sched_barrier(0);
            if (__builtin_expect(wave == 2, 1)) comb_ml(cma, cxa, cra, cso, false);
            MARK(e_q);
            if (wave < 3) STAMP(21 + 32 * layer);
            __builtin_amdgcn_s_setprio(0);
        }
    };

    unsigned nj = 0u, qS = 0u, fS = 0u, rS = 0u, nS = 0u; int ord;
    {
        const int tid = threadIdx.x, lane = tid & 63;
        asm volatile("ds_max_u32 %0, %1" :: "v"(lb + L_CNT + 64), "v"(tst) : "memory");
        unsigned mb = 0u;
        const u16* kbase = kc + wofs / 2;
        const u16* vbase = vc + wofs / 2;
        unsigned pt0 = 0u;
        if (wave == 7) {
            __builtin_amdgcn_s_setprio(3);
            if (lane < 10) *reinterpret_cast<u32x4*>(smem + L_XS + 640 + lane * 16) = (u32x4){0x3f803f80u, 0x3f803f80u, 0x3f803f80u, 0x3f803f80u};
            if (lane < 16) { *reinterpret_cast<u32x4*>(smem + L_CS + 768 + lane * 16) = (u32x4){0u, 0u, 0u, 0u}; }
        } else {
            if (wave >= 4) {
                if (wave == 4) st16_sc1(ws, WS_OWN + (unsigned)(gi * 16384 + (lane >> 5) * 8192 + gj * 512 + (lane & 31) * 16), (u32x4){~0u, ~0u, ~0u, ~0u});
                else empty5(0, (unsigned)(wave - 5) * 8192u, lane);
                if (wave == 6) {
                    __builtin_amdgcn_s_setprio(3);
                    if (gi + gj == 0) st4_sc1(ws, WS_R + (lane & 31) * 4, 0u);
                    st4_sc1(ws, WS_MB + (unsigned)((gi * 8 + gj) * 128 + (lane & 31) * 4), 0u);
                }
            } else if (wave >= 2) {
                unsigned long long c0, c1;
                asm volatile("s_waitcnt lgkmcnt(0)\n\ts_memtime %0\n\ts_waitcnt lgkmcnt(0)\n\ts_load_dword %2, %3, %4\n\ts_waitcnt lgkmcnt(0)\n\ts_memtime %1\n\ts_waitcnt lgkmcnt(0)"
                             : "=&s"(c0), "=&s"(c1), "=&s"(pt0) : "s"(ws), "s"((unsigned)(min(gi & 3, 2) << 21) + ((((tst + g * 977u) * 2654435761u) >> 11 & 0x1fdf80u) | ((wave ^ gj) & 1) * 8192)) : "memory");
                pt0 = ((wave ^ gj) & 1) ? (unsigned)c0 - (unsigned)c1 : (unsigned)c1 - (unsigned)c0;
            }
            STAMP(36);
            *reinterpret_cast<u32x4*>(smem + L_P + tid * 16) = (u32x4){0u, 0u, 0u, 0u};
            if (tid < 72 * 12 - 448) *reinterpret_cast<u32x4*>(smem + L_P + 7168 + tid * 16) = (u32x4){0u, 0u, 0u, 0u};
            if (wave >= 4) {
                if (wave == 6) VMW(2); else VMW(1);
                asm volatile("ds_max_u32 %0, %1" :: "v"(lb + L_CNT + 64), "v"(now() - TLAND) : "memory");
                STAMP(40);
                lds_add(0, 1u);
            } else if (wave >= 2) { lds_add(20, pt0); VMW(0); lds_add(0, 1u); }
        }
        asm volatile("s_waitcnt lgkmcnt(0)" :: "s"(pt1) : "memory");
        const int rot = gj + wave;
        const unsigned r0 = now(), c0 = (unsigned)__builtin_amdgcn_s_memtime();
        u32x4 a[4];
        unsigned ko = (unsigned)((wave * 64 + (lane & 7)) * 384 + (((lane >> 3) & 1) * 4 + (lane >> 4)) * 16);
        PIN1(ko);
        auto k_group = [&](auto GC_) __attribute__((always_inline)) {
            constexpr int G = decltype(GC_)::value, R = RK + 24 * (G >> 1) + 4 * (G & 1);
            const unsigned o = ko + (unsigned)((((G >> 1) + rot) & 3) * 6144 + (G & 1) * 3072);
            pace(3);
            ld16_nt<R, 0>(kbase, o); ld16_nt<R + 8, 128>(kbase, o); ld16_nt<R + 16, 256>(kbase, o);
            const int lane = lane_id();
            const unsigned ho = WS_Q0 + (unsigned)((gi * 4 + (gj >> 1)) * 4096 + lane * 16);
            if (wave < 7) {
                asm volatile("; COND_BEGIN");
                if (G == 0 && wave >= 4) { VMW(3); lds_add(32, 1u); }
                if (G == 0) {
                    if (wave < 2) { VMW(3); lds_add(0, 1u); load_gu(wave, lane); }
                    if (wave == 2) load_wd(lane);
                    if (wave == 3) {
#pragma unroll 1
                        for (int k = 5 - WEARLY; k < 5; ++k) load_wo(k, lane, true);
                    }
                }
                asm volatile("; COND_END");
            }
            if (wave == 7) {
                asm volatile("; COND_BEGIN");
                if (G == GP) {
                    VMW(3 * (GP + 1));
#pragma unroll 1
                    for (unsigned spins = 0; spins < (kSpinCap << 4); ++spins) {
                        if (lds_word(lb + L_CNT) >= 7u) break;
                        __builtin_amdgcn_s_sleep(1);
                    }
                    STAMP(26);
                    const unsigned char* p = smem + L_X0 + (lane >> 4) * 1280;
                    const float c0 = lo16(*reinterpret_cast<const u16*>(p));
                    const f32x2 c2 = {c0, c0};
                    f32x2 s2 = {0.f, 0.f}, q2 = s2;
#pragma unroll
                    for (int k = 0; k < 5; ++k) {
                        const u32x4 w = *reinterpret_cast<const u32x4*>(p + k * 256 + (lane & 15) * 16);
#pragma unroll
                        for (int e = 0; e < 4; ++e) { const f32x2 d = unpk(w[e]) - c2; s2 += d; q2 += d * d; }
                    }
                    const float s = row16_sum(s2[0] + s2[1]), qq = row16_sum(q2[0] + q2[1]);
                    const float ms = s * (1.f / DM);
                    const unsigned mv = pk2(c0 + ms, fmax2(qq - s * ms, 0.f) * (1.f / DM));
                    const float mb = lo16(mv), rden = frcp(bfr(__builtin_amdgcn_sqrtf(bfr(hi16(mv) + 1e-5f))));
                    const f32x2 mb2 = {mb, mb}, rd2 = {rden, rden};
                    if ((lane & 15) < 10) {
                        u32x4 w = *reinterpret_cast<const u32x4*>(p + gj * 160 + (lane & 15) * 16);
#pragma unroll
                        for (int e = 0; e < 4; ++e) w[e] = pk2v(unpk(pk2v(unpk(w[e]) - mb2)) * rd2);
                        *reinterpret_cast<u32x4*>(smem + L_XS + (lane >> 4) * 160 + (lane & 15) * 16) = w;
                    }
                    STAMP(30);
                }
                if (G == GP || G == GA) {
#pragma unroll 1
                    for (int blk = 0; blk < 3; ++blk) {
                        LANE_VARS();
                        const unsigned xb = lb + L_XS + (unsigned)((G == GP ? q4 : 4) * 160);
                        Frag80 f[4];
#pragma unroll
                        for (int i = 0; i < 4; ++i) f[i] = frag80(lb + L_WQ + (unsigned)(128 * blk + 32 * i + 8 * p4), 416, r0s, r1s, r2s);
                        const Frag80 b = rows80(xb + kq * 16, xb + 128 + kq * 8);
                        const u32x4 c = tiles4(f, b, 0x1111111111111111ull);
                        if (G == GP) *reinterpret_cast<u32x4*>(smem + L_OP + q4 * 768 + 256 * blk + p4 * 64 + kq * 16) = c;
                        else *reinterpret_cast<u32x4*>(smem + L_CS + 256 * blk + kq * 64 + p4 * 16) = c;
                    }
                    if (G == GP) {
                        asm volatile("" ::: "memory");
                        const int ln = lane_id();
                        f32x2 c[6];
                        unsigned m = 20u;
#pragma unroll
                        for (int k = 0; k < 6; ++k) {
                            c[k] = *reinterpret_cast<const f32x2*>(smem + L_OP + (k / 3) * 1536 + ln * 24 + (k % 3) * 8);
                            m = max(m, max(fexp(c[k][0]), fexp(c[k][1])));
                        }
                        m = max(m, dppu<0xB1>(m)); m = max(m, dppu<0x4E>(m)); m = max(m, dppu<0x141>(m)); m = max(m, dppu<0x140>(m));
                        m = max(max((unsigned)__builtin_amdgcn_readlane(m, 0), (unsigned)__builtin_amdgcn_readlane(m, 16)), max((unsigned)__builtin_amdgcn_readlane(m, 32), (unsigned)__builtin_amdgcn_readlane(m, 48)));
                        lds_set(lb + L_CNT + 76, m);
                        const float f = __uint_as_float((273u - m) << 23);
#pragma unroll
                        for (int k = 0; k < 2; ++k) {
                            const u32x2 w0 = enc21(c[3 * k][0] * f, c[3 * k][1] * f, c[3 * k + 1][0] * f), w1 = enc21(c[3 * k + 1][1] * f, c[3 * k + 2][0] * f, c[3 * k + 2][1] * f);
                            st16_sc1(ws, WS_Q0 + (unsigned)((gi * 4 + 2 * k + (ln >> 5)) * 4096 + gj * 512 + (ln & 31) * 16), (u32x4){w0[0], w0[1], w1[0], w1[1]});
                        }
                        STAMP(2);
                        WAIT1(pf, 3 * (GP + 1) + 2);
                    }
                }
                if (G == GA) {
                    const unsigned half = __builtin_amdgcn_s_getreg(2 << 6 | 20);
                    lds_set(lb + L_CNT + 68, lds_word(lb + L_CNT + 64) << 11 | lds_word(lb + L_CNT + 76) << 3 | 1u | (((int)lds_word(lb + L_CNT + 20) > 0) ^ half) << 1 | half << 2);
                    VMW(3 * (GA - GP));
#pragma unroll 1
                    for (unsigned spins = 0; spins < (kSpinCap << 4); ++spins) if (lds_word(lb + L_CNT + 32) >= 3u) break;
                    send_flag(lane_id());
                    lds_set(lb + L_CNT + 8, 1u);
                    STAMP(17);
                    f32x2 r2 = {0.f, 0.f};
#pragma unroll
                    for (int k = 0; k < 5; ++k) r2 += unpk(*reinterpret_cast<const unsigned*>(smem + L_WR + k * 256 + lane_id() * 4));
                    *reinterpret_cast<float*>(smem + L_CNT + 12) = wave_sum(r2[0] + r2[1]);
                }
                asm volatile("; COND_END");
            }
            if (wave == 6) {
                asm volatile("; COND_BEGIN");
                if (G == GB - 1) mb = ld4_sc1(ws, WS_MB + (unsigned)((gi * 8 + gj) * 128 + (gi & 7) * 4));
                if (G == GB) {
                    WAIT1(mb, 3);
#pragma unroll 1
                    for (unsigned spins = 0; spins < kSpinCap && !all_set(mb); ++spins) {
                        if ((spins & 7u) == 7u && lds_word(lb + L_CNT + 8)) send_flag(lane);
                        mb = ld4_sc1(ws, WS_MB + (unsigned)((gi * 8 + gj) * 128 + (gi & 7) * 4));
                        WAIT1(mb, 0);
                    }
                    mb = ld4_sc1(ws, WS_MB + (unsigned)((gi * 8 + (gi & 7)) * 128 + (lane & 7) * 4));
                }
                if (G == GC) {
                    WAIT1(mb, 3 * (GC - GB));
#pragma unroll 1
                    for (unsigned spins = 0; spins < kSpinCap && !all_set(mb); ++spins) {
                        if ((spins & 7u) == 7u && lds_word(lb + L_CNT + 8)) send_flag(lane);
                        mb = ld4_sc1(ws, WS_MB + (unsigned)((gi * 8 + (gi & 7)) * 128 + (lane & 7) * 4));
                        WAIT1(mb, 0);
                    }
                    lds_set(lb + L_CNT + 92, (__builtin_amdgcn_readlane(mb, gi & 7) & 4u) ? 14u : 0u);
                    int d = min(max((int)(mb - (tst << 11)) >> 11, -400), 600);
                    d += (int)dppu<0xB1>((unsigned)d); d += (int)dppu<0x4E>((unsigned)d); d += (int)dppu<0x141>((unsigned)d);
                    const unsigned tm = tst + (unsigned)(__builtin_amdgcn_readfirstlane(d) >> 3);
                    lds_set(lb + L_CNT + 64, tm);
                    const float slow = (float)(now() - r0) * 24.f * 256.f * frcp((float)((unsigned)__builtin_amdgcn_s_memtime() - c0)) - 256.f;
                    lds_set(lb + L_CNT + 88, tm << 11 | (unsigned)__builtin_amdgcn_fmed3f(slow, 0.f, 63.f) << 5 | __builtin_popcount((unsigned)__builtin_amdgcn_ballot_w64((mb & 2u) != 0u) & 255u) << 1 | 1u);
                    report();
                    a[0] = ld16_sc1<0>(ws, ho); a[1] = ld16_sc1<1024>(ws, ho); a[2] = ld16_sc1<2048>(ws, ho); a[3] = ld16_sc1<3072>(ws, ho);
                    pace(XCOST, false);
                    STAMP(31);
                }
                asm volatile("; COND_END");
            } else if (G == GC) { for (int k = 0; k < 4; ++k) asm volatile("" : "=v"(a[k])); }
        };
        k_group(IC<0>{}); k_group(IC<1>{}); k_group(IC<2>{}); k_group(IC<3>{}); k_group(IC<4>{}); k_group(IC<5>{}); k_group(IC<6>{}); k_group(IC<7>{});
        {
            const int lane = lane_id();
            const unsigned v0 = (unsigned)((wave * 64 + 4 * (lane >> 4)) * 384 + (lane & 15) * 16);
            const unsigned v1 = v0 + 256 - (unsigned)((lane & 15) * 8);
            auto v_group = [&](auto HC) __attribute__((always_inline)) {
                constexpr int h = decltype(HC)::value, p = kVord[h], R = p < 8 ? RV + 16 * (p & 3) + 4 * (p >> 2) : RV + 64 + 2 * (p - 8), NV = v_upto(h);
                const unsigned o = (p < 8 ? v0 : v1) + (unsigned)((((p < 8 ? p >> 1 : p) + rot) & 3) * 6144);
                pace(2);
                if (p < 8) { ld16_nt<R, (p & 1) * 768>(vbase, o); ld16_nt<R + 8, (p & 1) * 768 + 384>(vbase, o); }
                else { ld8_nt<R, 0>(vbase, o); ld8_nt<R + 8, 384>(vbase, o); ld8_nt<R + 16, 768>(vbase, o); ld8_nt<R + 24, 1152>(vbase, o); }
                if (wave == 7 && gi + gj == 0) {
                    asm volatile("; COND_BEGIN");
                    if (h == 3 || h == 6) mb = ld4_sc1(ws, WS_R + (unsigned)((lane & 31) * 4));
                    if (h == 4 || h == 7) {
                        WAIT1(mb, NV - v_upto(h - 1));
                        if (all_set(mb) && !lds_word(lb + L_CNT + 36)) answer(lane, mb);
                    }
                    asm volatile("; COND_END");
                }
                if (wave == 6) {
                    asm volatile("; COND_BEGIN");
                if (h == HD) {
                    asm volatile("s_waitcnt vmcnt(%4)" : "+v"(a[0]), "+v"(a[1]), "+v"(a[2]), "+v"(a[3]) : "i"(3 * (7 - GC) + NV) : "memory");
                    float sq[6] = {0.f, 0.f, 0.f, 0.f, 0.f, 0.f};
#pragma unroll
                    for (int k = 0; k < 4; ++k) {
                        const float f = __uint_as_float(((((unsigned)__builtin_amdgcn_ds_bpermute((2 * k + (lane >> 5)) * 4, (int)mb) >> 3) & 255u) - 19u) << 23);
#pragma unroll
                        for (int e = 0; e < 2; ++e) {
                            const unsigned lo = a[k][2 * e], hi = a[k][2 * e + 1];
                            const float fe = (int)hi < 0 ? __builtin_nanf("") : f;
                            sq[3 * e] += (float)((int)(lo << 11) >> 11) * fe;
                            sq[3 * e + 1] += (float)((int)(__builtin_amdgcn_alignbit(hi, lo, 21) << 11) >> 11) * fe;
                            sq[3 * e + 2] += (float)((int)(hi << 1) >> 11) * fe;
                        }
                    }
#pragma unroll
                    for (int e = 0; e < 3; ++e) {
                        const int pi = 3 * (lane & 31) + e;
                        *reinterpret_cast<unsigned*>(smem + L_Q + (pi >> 3) * 32 + (pi & 6) * 2 + (pi & 1) * 16) = pk2(x32_add(sq[2 * e]), x32_add(sq[2 * e + 1]));
                    }
                    asm volatile("s_waitcnt lgkmcnt(0)\n\tds_write_b32 %0, %1" :: "v"(lb + L_CNT + 4), "v"(1u) : "memory");
                    STAMP(27);
                    lead = LEAD * PACE * 4;
                }
                    if (h == 6) mb = ld4_sc1(ws, WS_MB + (unsigned)((gi * 8 + (gi & 7)) * 128 + 32));
                    if (h == 8) {
                        WAIT1(mb, NV - v_upto(6));
                        const unsigned r = __builtin_amdgcn_readfirstlane(mb);
                        if (r) { adopt(r); STAMP(29); }
                    }
                    asm volatile("; COND_END");
                }
                if constexpr (h < 6) {
                    constexpr int i = 2 * h;
                    fix_k1(IC<i>{}, IC<20 - 6 * (i / 3) - i % 3 + NV>{});
                    fix_k1(IC<i + 1>{}, IC<20 - 6 * ((i + 1) / 3) - (i + 1) % 3 + NV>{});
                }
                if (h == 5) {
                    STAMP(1);
                    __builtin_amdgcn_s_setprio(0);
                    att_qk0();
                }
            };
            v_group(IC<0>{}); v_group(IC<1>{}); v_group(IC<2>{}); v_group(IC<3>{}); v_group(IC<4>{}); v_group(IC<5>{});
            v_group(IC<6>{}); v_group(IC<7>{}); v_group(IC<8>{}); v_group(IC<9>{}); v_group(IC<10>{}); v_group(IC<11>{});
            lds_add(16, 1u);
            if (wave == 3) lds_add(44, WEARLY);
        }
    }
    {
        STAMP(28);
        att_addr();
        const int lane = lane_id();
        cma = lb + L_M; cxa = lb + L_M + (unsigned)((lane >> 4) * 8); cra = lb + L_OP + (unsigned)(512 + (lane >> 4) * 2048 + (lane & 15) * 16);
        cso = WS_OWN + (unsigned)(gi * 16384 + gj * 512 + 256 + (lane >> 5) * 64 + (lane & 12) * 4 + (lane & 1) * 8192);
        PIN2(cma, cxa); PIN2(cra, cso);
        att_pv0();
    }
    {
        f32x4 m0, m1, o0, o1; f32x2 mm; unsigned r, taken;
        asm volatile("s_mov_b64 exec, 1\n\tds_add_rtn_u32 %5, %7, %8 offset:%11\n\ts_mov_b64 exec, -1\n\t"
                     "ds_read_b128 %0, %7\n\tds_read_b128 %1, %7 offset:16\n\tds_read_b64 %2, %9\n\tds_read_b128 %3, %10\n\tds_read_b128 %4, %10 offset:1024\n\t"
                     "ds_read_b32 %6, %7 offset:%12\n\ts_waitcnt lgkmcnt(0)"
                     : "=&v"(m0), "=&v"(m1), "=&v"(mm), "=&v"(o0), "=&v"(o1), "=&v"(r), "=&v"(taken)
                     : "v"(cma), "v"(1u), "v"(cxa), "v"(cra), "i"(L_CNT + 28 - L_M), "i"(L_CNT + 40 - L_M) : "memory");
        ord = __builtin_amdgcn_readfirstlane(r);
        if (ord == 7) { STAMP(5); comb_o1(m0, m1, mm, o0, o1, cso, 0x0000333300003333ull, true); lds_set(lb + L_CNT + 48, now() | 1u); STAMP(21); }
        MARK(e_t5);
        if (ord <= OML + 1 || (unsigned)__builtin_amdgcn_readfirstlane(taken) < 5u - WEARLY) {
        const int lane = lane_id();
        auto wait_cnt = [&](int off, unsigned v) __attribute__((always_inline)) {
#pragma unroll 1
            for (unsigned spins = 0; spins < (kSpinCap << 4); ++spins) { if (lds_word(lb + L_CNT + off) >= v) break; __builtin_amdgcn_s_sleep(1); }
        };
        if (ord == OML) {
            wait_cnt(16, 8u);
            unsigned xa, ra; ml_addr(lane, xa, ra);
            comb_ml(cma, xa, ra, WS_OWN + (unsigned)(gi * 16384 + gj * 512 + 384 + (lane & 1) * 8192), true);
            STAMP(24);
        } else if (ord == 0 && !lds_word(lb + L_CNT + 52)) {
            if (gi + gj == 0 && !lds_word(lb + L_CNT + 36)) {
                unsigned r = 0u;
#pragma unroll 1
                for (unsigned spins = 0; spins < kSpinCap; ++spins) {
                    r = ld4_sc1(ws, WS_R + (unsigned)((lane & 31) * 4));
                    WAIT1(r, 0);
                    if (all_set(r)) break;
                    if ((spins & 7u) == 7u) send_flag(lane);
                }
                answer(lane, r);
            }
#pragma unroll 1
            for (unsigned spins = 0; spins < kSpinCap; ++spins) {
                unsigned r = ld4_sc1(ws, WS_MB + (unsigned)((gi * 8 + (gi & 7)) * 128 + 32));
                WAIT1(r, 0);
                r = __builtin_amdgcn_readfirstlane(r);
                if (r) { adopt(r); break; }
                if ((spins & 7u) == 7u) { send_flag(lane); report(); }
                __builtin_amdgcn_s_sleep(16);
            }
            STAMP(29);
        } else if (ord == OML + 1) {
#pragma unroll 1
            for (unsigned spins = 0; spins < (kSpinCap << 6); ++spins) if (lds_word(lb + L_CNT + 24) >= 8u) break;
            const unsigned ra = lb + L_OP + (unsigned)((lane >> 5) * 4096 + (lane & 31) * 16);
            comb_o0(*(const LAS f32x4*)(size_t)cma, *(const LAS f32x4*)(size_t)(cma + 16), *(const LAS f32x4*)(size_t)(cma + (lane >> 5) * 16), *(const LAS f32x4*)(size_t)ra, *(const LAS f32x4*)(size_t)(ra + 1024),
                    *(const LAS f32x4*)(size_t)(ra + 2048), *(const LAS f32x4*)(size_t)(ra + 3072), WS_OWN + (unsigned)(gi * 16384 + gj * 512 + (lane & 60) * 4 + (lane & 1) * 8192), 0x3333333333333333ull, true);
            STAMP(22);
        }
        if (ord == OML) {
#pragma unroll 1
            for (unsigned spins = 0; spins < (kSpinCap << 8); ++spins) { if (lds_word(lb + L_CNT + 52)) break; __builtin_amdgcn_s_sleep(1); }
            const unsigned S = (lds_word(lb + L_CNT + 80) & 4u) << 11;
            empty4(0, S, lane); empty5(5, 0u, lane); empty5(5, 8192u, lane);
        }
#pragma unroll 1
        for (;;) {
            unsigned j;
            asm volatile("s_mov_b64 exec, 1\n\tds_add_rtn_u32 %0, %1, %2\n\ts_mov_b64 exec, -1\n\ts_waitcnt lgkmcnt(0)" : "=&v"(j) : "v"(lb + L_CNT + 40), "v"(1u) : "memory");
            j = __builtin_amdgcn_readfirstlane(j);
            if (j >= 5u - WEARLY) break;
            load_wo((int)j, lane, false); ++nj;
        }
        }
        STAMP(23);
    }

    unsigned omr, owa, owb;
    auto op_addr = [&](int layer) __attribute__((always_inline)) {
        LANE_VARS();
        const int t = wave - 3;
        cma = WS_OWN + rS + (unsigned)(gi * 16384 + layer * 4096 + (lane >> 3) * 512 + t * 128 + (t == 3 ? 0 : (lane & 7) * 16));
        cxa = lb + L_O2 + (unsigned)((lane >> 3) * 416 + t * 128 + (t == 3 ? 0 : (lane & 7) * 16));
        const int pb = (lane & 4) ? 3 - p4 : p4, jp = 2 * pb + ((lane >> 2) & 1);
        cso = tile35(gj, 10 * layer + t) + (unsigned)((kq & 1) * 8192 + gi * 128 + (kq >> 1) * 64 + p4 * 16);
        cra = lb + L_O2 + (unsigned)(jp * 416 + kq * 16); omr = lb + L_O2 + (unsigned)(jp * 416 + 384);
        owa = lb + L_WO + (unsigned)(r0s * 160 + 32 * t + 8 * p4); owb = lb + L_WO + (unsigned)(r1s * 160 + 32 * t + 8 * p4);
        if (layer) PIN2(cma, cxa);
        PIN2(cso, cra); PIN3(omr, owa, owb);
    };
    auto chain = [&](auto LC) __attribute__((always_inline)) {
        constexpr int layer = decltype(LC)::value;
        unsigned h4 = WS_H4 + (unsigned)((layer * 32 + gi) * 32768) + (layer ? fS : 0u);
        auto sides = [&]() __attribute__((always_inline)) {
            const unsigned m = lds_word(lb + L_CNT + 80);
            qS = (m & 2u) << 12; fS = (m & 4u) << 11; rS = (m & 8u) << 10; nS = lds_word(lb + L_CNT + 84); h4 += fS;
        };
        if (wave < 3) {
            LANE_VARS();
            if (layer == 0) {
                unsigned lo = WS_OWN + lds_word(lb + L_CNT + 84) + (unsigned)(gi * 16384 + (lane >> 3) * 512), lw = lb + L_O2 + (unsigned)((lane >> 3) * 416);
                unsigned lo0 = lo + (unsigned)((lane & 7) * 16), lw0 = lw + (unsigned)((lane & 7) * 16);
                PIN2(lo0, lo); PIN2(lw0, lw);
                GUARD(400);
                MARK(b_g2);
                if (__builtin_expect(wave == 2, 0)) { if (nj | (ord == OML)) { VMW(0); lds_add(44, nj); } }
                else if (__builtin_expect(wave == 1, 0)) {
#pragma unroll 1
                    for (unsigned spins = 0; spins < (kSpinCap << 4); ++spins) if (lds_word(lb + L_CNT + 48)) break;
#pragma unroll 1
                    for (unsigned spins = 0; spins < kSpinCap; ++spins) {
                        u32x4 o0 = ld16_sc1<0>(ws, lo0), o1 = ld16_sc1<128>(ws, lo0), o2 = ld16_sc1<384>(ws, lo);
                        if (nj) { VMW(3); lds_add(44, nj); nj = 0u; }
                        asm volatile("s_waitcnt vmcnt(0)" : "+v"(o0), "+v"(o1), "+v"(o2) :: "memory");
                        *(LAS u32x4*)(size_t)lw0 = o0; *(LAS u32x4*)(size_t)(lw0 + 128) = o1; *(LAS u32x4*)(size_t)(lw + 384) = o2;
                        if (!any_empty(max(max(um4(o0), um4(o1)), um4(o2)))) break;
                    }
                } else {
                    unsigned long long miss; unsigned cnt = kSpinCap << 4, tf, nv = nj;
                    asm volatile("1:\n\tds_read_b32 v4, %4 offset:48\n\ts_sub_u32 %1, %1, 1\n\ts_cbranch_scc1 5f\n\ts_waitcnt lgkmcnt(0)\n\tv_readfirstlane_b32 %2, v4\n\ts_cmp_eq_u32 %2, 0\n\ts_cbranch_scc1 1b\n\t"
                                 "s_add_u32 %2, %2, %8\n"
                                 "2:\n\ts_memrealtime vcc\n\ts_waitcnt lgkmcnt(0)\n\ts_sub_u32 vcc_lo, %2, vcc_lo\n\ts_cmp_lt_u32 vcc_lo, 0x400\n\ts_cbranch_scc1 2b\n"
                                 "5:\n\ts_mov_b32 %1, 15\n"
                                 "3:\n\tglobal_load_dwordx4 v[0:3], %5, %6 offset:256 sc1\n\ts_waitcnt vmcnt(1)\n\ts_mov_b64 exec, 1\n\tds_add_u32 %4, %3 offset:44\n\ts_mov_b64 exec, -1\n\tv_mov_b32 %3, 0\n\t"
                                 "s_sub_u32 %1, %1, 1\n\ts_cselect_b64 vcc, 0, -1\n\ts_waitcnt vmcnt(0)\n\tds_write_b128 %7, v[0:3] offset:256\n\tv_max3_u32 v4, v0, v1, v2\n\tv_max_u32 v4, v4, v3\n\t"
                                 "v_cmp_eq_u32 %0, -1, v4\n\ts_and_b64 vcc, vcc, %0\n\ts_cbranch_scc1 3b"
                                 : "=&s"(miss), "+s"(cnt), "=&s"(tf), "+v"(nv) : "v"(lb + L_CNT), "v"(lo0), "s"(ws), "v"(lw0), "i"(DG2 - 1) : "memory", "vcc", "scc", "v0", "v1", "v2", "v3", "v4");
                    if (__builtin_expect(miss != 0ull, 0)) {
#pragma unroll 1
                        for (unsigned spins = 0; spins < kSpinCap; ++spins) {
                            if ((spins & 15u) == 0u) send_flag(lane);
                            u32x4 o0 = ld16_sc1<256>(ws, lo0);
                            WAIT1(o0, 0);
                            *(LAS u32x4*)(size_t)(lw0 + 256) = o0;
                            if (!any_empty(um4(o0))) break;
                        }
                    }
                }
                MARK(e_g2);
                STAMP(25);
                BAR();
                sides();
                if (wave < 2) empty1(wave, qS, lane);
                else {
                    st16m(ws, WS_CS + (unsigned)((gi * 8 + gj) * 768 + lane * 16), *reinterpret_cast<const u32x4*>(smem + L_CS + lane * 16), 0xffffffffffffull);
                    empty5(10, 0u, lane); empty5(10, 8192u, lane);
                }
            } else BAR();
            {
                const unsigned im = lb + L_GU + (unsigned)(32 * wave + 8 * p4);
                Frag80 fg = frag80(im, 96, r0s, r1s, r2s), fu = frag80(im + 8192, 96, r0s, r1s, r2s);
                const unsigned xb = lb + L_XS + (unsigned)((n < 4 ? n : 4) * 160);
                unsigned b16 = xb + kq * 16, b8 = xb + 128 + kq * 8;
                unsigned sg = h4 + (unsigned)(n < 4 ? h4s(wave) + gj * 512 + kq * 64 + n * 16 : H4C + wave * 1024 + gj * 128 + kq * 16), su = sg + (n < 4 ? 256u : 64u);
                unsigned o = h4 + h4s(wave) + (unsigned)(lane * 16);
                unsigned oc = h4 + (unsigned)(H4C + wave * 1024 + lane * 16), pc = (unsigned)((kq * 16 + q4) * 4);
                unsigned os = h4 + (unsigned)(H4T + (q4 + 4 * (kq & 1)) * 128 + p4 * 16);
                unsigned ha = lb + L_H + (unsigned)(p4 * 96 + wave * 32 + q4 * 8 + kq * 2);
                f32x2 cs = {0.f, 0.f};
                if (layer != 0) cs = *reinterpret_cast<const f32x2*>(smem + L_CS2 + wave * 512 + lane * 8);
                PIN3(fg.a0, fg.a1, fg.a2); PIN3(fu.a0, fu.a1, fu.a2); PIN3(b16, b8, sg); PIN3(su, o, os); PIN2(ha, cs);
                unsigned long long mk = layer ? 0x000f000f000f000full : 0x001f001f001f001full;
                unsigned ta = WS_MB + (unsigned)((lane >> 1) * 1024 + 64 + (lane & 1) * 16);
                if (layer == 0) PIN3(oc, ta, pc);
                unsigned x4a = layer ? 0u : X4A * lds_word(lb + L_CNT + 20) >> 12;
                unsigned long long w0 = __builtin_amdgcn_ballot_w64(wave == 0);
                PINS(x4a); PINS(w0);
                unsigned long long rdm = 0x3acccccd3acccccdull;
                PINS(mk); PINS(rdm);
                if (layer == 0) VMW(0);
                if (layer == 0) GUARD(1200); else GUARD(800);
                MARK(b_gu);
                BAR();
                STAMP(10 + 32 * layer);
                u32x4 tl = {0u, 0u, 0u, 0u};
                if (layer == 0) asm volatile("s_mov_b64 exec, %3\n\tglobal_load_dwordx4 %0, %1, %2 sc1\n\ts_mov_b64 exec, -1" : "+v"(tl) : "v"(ta), "s"(ws), "s"(w0) : "memory");
                const Frag80 b = rows80(b16, b8);
                const f32x4 g0 = mfma32(fg.a0, b.a0, z4), u0 = mfma32(fu.a0, b.a0, z4), g1 = mfma32(fg.a1, b.a1, g0), g2 = mfma16(fg.a2, b.a2, z4), u1 = mfma32(fu.a1, b.a1, u0), u2 = mfma16(fu.a2, b.a2, z4);
                __builtin_amdgcn_sched_barrier(0);
                const f32x4 cg = g1 + g2, cu = u1 + u2;
                st16m2<0>(ws, sg, asu(cg), su, asu(cu), mk);
                STAMP(11 + 32 * layer);
                if (layer == 0 && wave != 0) {
                    unsigned tr = 0u;
#pragma unroll 1
                    for (unsigned spins = 0; spins < (kSpinCap << 6); ++spins) { tr = lds_word(lb + L_CNT + 56); if (tr) break; }
                    until(tr + x4a);
                } else if (layer == 0) {
                    WAIT1(tl, 2);
#pragma unroll 1
                    for (unsigned spins = 0; spins < kSpinCap; ++spins) {
                        const unsigned long long miss = __builtin_amdgcn_ballot_w64(min(min(tl[0], tl[1]), min(tl[2], tl[3])) == 0u);
                        if (!miss) break;
                        asm volatile("s_mov_b64 exec, %3\n\tglobal_load_dwordx4 %0, %1, %2 sc1\n\ts_mov_b64 exec, -1\n\ts_waitcnt vmcnt(0)" : "+v"(tl) : "v"(ta), "s"(ws), "s"(miss) : "memory");
                    }
                    const unsigned tn = now();
                    unsigned d = min(min(min(tn - tl[0], tn - tl[1]), min(tn - tl[2], tn - tl[3])), 800u);
                    d = min(d, dppu<0xB1>(d)); d = min(d, dppu<0x4E>(d)); d = min(d, dppu<0x124>(d)); d = min(d, dppu<0x128>(d));
                    unsigned tr = tn - min(min((unsigned)__builtin_amdgcn_readlane(d, 0), (unsigned)__builtin_amdgcn_readlane(d, 16)), min((unsigned)__builtin_amdgcn_readlane(d, 32), (unsigned)__builtin_amdgcn_readlane(d, 48)));
                    lds_set(lb + L_CNT + 56, tr | 1u);
                    until(__builtin_amdgcn_readfirstlane(tr | 1u) + x4a);
                } else sched(X4, 0);
                STAMP(16 + 32 * layer);
                float hv;
                u32x4 a0, a1, a2, a3, ac, as;
                auto load = [&]() __attribute__((always_inline)) {
                    as = ld16_sc1(ws, os);
                    if (layer == 0) ac = ld16_sc1(ws, oc);
                    a0 = ld16_sc1<0>(ws, o); a1 = ld16_sc1<1024>(ws, o); a2 = ld16_sc1<2048>(ws, o); a3 = ld16_sc1<3072>(ws, o);
                    if (layer == 0) lds_add(32, 1u);
                };
                auto all_in = [&]() __attribute__((always_inline)) {
                    if (layer == 0) asm volatile("s_waitcnt vmcnt(0)" : "+v"(a0), "+v"(a1), "+v"(a2), "+v"(a3), "+v"(ac), "+v"(as) :: "memory");
                    else { asm volatile("s_waitcnt vmcnt(0)" : "+v"(a0), "+v"(a1), "+v"(a2), "+v"(a3), "+v"(as) :: "memory"); ac = as; }
                };
                auto compute = [&]() __attribute__((always_inline)) {
#define ROR(n) " row_ror:" #n " row_mask:0xf bank_mask:0xf\n\t"
                    asm volatile("s_waitcnt vmcnt(%16)\n\tv_add_f32_dpp v40, %2, %2" ROR(8) "v_add_f32_dpp v41, %3, %3" ROR(8) "s_nop 0\n\tv_add_f32_dpp v40, v40, v40" ROR(4) "v_add_f32_dpp v41, v41, v41" ROR(4)
                                 "v_mov_b32 v42, v40\n\tv_mov_b32 v43, v41\n\ts_nop 0\n\tv_permlane16_swap_b32 v40, v42\n\tv_permlane16_swap_b32 v41, v43\n\t"
                                 "v_pk_add_f32 v[40:41], v[40:41], v[42:43]\n\tv_pk_mul_f32 v[40:41], v[40:41], %14\n\t"
                                 ".if %17 == 0\n\ts_waitcnt vmcnt(4)\n\tv_mov_b64 v[44:45], %12\n\tv_mov_b64 v[46:47], %13\n\t.endif\n\t"
                                 "v_fma_f32 v40, -v41, v41, v40\n\tv_cvt_pk_bf16_f32 v42, 0, v41\n\tv_maximum3_f32 v40, v40, 0, 0\n\t"
                                 ".if %17 == 0\n\tv_permlane32_swap_b32 v44, v46\n\tv_permlane32_swap_b32 v45, v47\n\tv_pk_add_f32 v[44:45], v[44:45], v[46:47]\n\t.endif\n\t"
                                 "v_cvt_pk_bf16_f32 v40, 0, v40\n\ts_waitcnt vmcnt(2)\n\tv_pk_add_f32 v[32:33], %4, %6\n\tv_add_f32 v40, 0x3727c5ac, v40\n\tv_pk_add_f32 v[34:35], %5, %7\n\t"
                                 ".if %17 == 0\n\tv_permlane16_swap_b32 v44, v45\n\tv_add_f32 v44, v44, v45\n\t.endif\n\t"
                                 "v_cvt_pk_bf16_f32 v40, 0, v40\n\ts_waitcnt vmcnt(0)\n\tv_pk_add_f32 v[36:37], %8, %10\n\tv_sqrt_f32 v40, v40\n\tv_pk_add_f32 v[38:39], %9, %11\n\t"
                                 ".if %17 == 0\n\tv_add_f32_dpp v44, v44, v44" ROR(8) "ds_bpermute_b32 v46, %19, v44\n\tds_bpermute_b32 v47, %19, v44 offset:16\n\t.endif\n\t"
                                 "v_pk_add_f32 v[32:33], v[32:33], v[36:37]\n\tv_pk_add_f32 v[34:35], v[34:35], v[38:39]\n\tv_cvt_pk_bf16_f32 v40, 0, v40\n\t"
                                 "s_nop 0\n\t"
                                 "v_permlane32_swap_b32 v32, v34\n\tv_permlane32_swap_b32 v33, v35\n\tv_rcp_f32 v40, v40\n\tv_pk_add_f32 v[32:33], v[32:33], v[34:35]\n\ts_nop 1\n\t"
                                 "v_permlane16_swap_b32 v32, v33\n\t.if %17 == 0\n\ts_waitcnt lgkmcnt(0)\n\tv_mov_b64 %1, v[46:47]\n\t.endif\n\t"
                                 "v_pk_fma_f32 v[32:33], %1, v[42:43], v[32:33] op_sel_hi:[1,0,1] neg_lo:[1,0,0] neg_hi:[1,0,0]\n\tv_pk_mul_f32 v[32:33], v[32:33], v[40:41] op_sel_hi:[1,0]\n\t"
                                 "v_cvt_pk_bf16_f32 v32, 0, v32\n\tv_mul_f32 v34, 0xbfb8aa3b, v32\n\tv_exp_f32 v34, v34\n\tv_cvt_pk_bf16_f32 v33, 0, v33\n\tv_add_f32 v34, 1.0, v34\n\t"
                                 "v_rcp_f32 v34, v34\n\ts_nop 0\n\tv_mul_f32 v32, v34, v32\n\tv_cvt_pk_bf16_f32 v32, 0, v32\n\tv_mul_f32 %0, v33, v32\n\tv_cvt_pk_bf16_f32 v32, %0, %0\n\tds_write_b16 %15, v32"
                                 : "=&v"(hv), "+v"(cs)
                                 : "v"(as[0]), "v"(as[1]), "v"(LO2(a0)), "v"(HI2(a0)), "v"(LO2(a1)), "v"(HI2(a1)), "v"(LO2(a2)), "v"(HI2(a2)), "v"(LO2(a3)), "v"(HI2(a3)),
                                   "v"(LO2(layer ? as : ac)), "v"(HI2(layer ? as : ac)), "s"(rdm), "v"(ha), "i"(layer ? 4 : 5), "i"(layer), "v"(as), "v"(layer ? ha : pc)
                                 : "memory", "v32", "v33", "v34", "v35", "v36", "v37", "v38", "v39", "v40", "v41", "v42", "v43", "v44", "v45", "v46", "v47");
#undef ROR
                };
                RETRY(load(), (all_in(), max(max(max(um4(a0), um4(a1)), max(um4(a2), um4(a3))), max(um4(ac), um4(as)))), compute(), any_nan(hv, hv), layer ? X4 : X4A);
                BAR();
                MARK(e_gu);
                STAMP(12 + 32 * layer);
                if (layer == 0) {
                    *reinterpret_cast<f32x2*>(smem + L_CS2 + wave * 512 + lane * 8) = cs;
                }
            }
            if (layer == 0) {
                if (wave == 1) {
                    const unsigned o = WS_CS + (unsigned)(gi * 6144 + 3072 + (lane < 48 ? lane : 0) * 16);
                    u32x4 a[8];
                    GATHER8(a, o, 768);
                    WAIT8(a, 0);
                    if (lane < 48) *reinterpret_cast<f32x4*>(smem + L_CS + lane * 16) = SUM8(a);
                }
                Frag80 f[4];
#pragma unroll
                for (int i = 0; i < 4; ++i) { f[i] = frag80(lb + L_WQ + (unsigned)(128 * wave + 32 * i + 8 * p4), 416, r0s, r1s, r2s); PIN3(f[i].a0, f[i].a1, f[i].a2); }
                unsigned b16 = lb + L_XS + (unsigned)(q4 * 160 + kq * 16), b8 = lb + L_XS + (unsigned)(q4 * 160 + 128 + kq * 8);
                unsigned so = WS_H1 + qS + (unsigned)((gi * 4 + q4) * 16384 + gj * 896 + 256 * wave + kq * 64 + p4 * 16);
                unsigned long long mk = 0x1111111111111111ull;
                PIN3(b16, b8, so); PINS(mk);
                GUARD(3800);
                MARK(b_q);
                BAR();
                STAMP(33);
                const Frag80 b = rows80(b16, b8);
                st16_sc1(ws, so, tiles4(f, b, mk));
                STAMP(34);
                qk_addr(true); att_addr();
                unsigned x0a = lb + L_M + (unsigned)((lane >> 5) * 16), x1a = lb + L_M + (unsigned)((lane >> 4) * 8), x2a, r2a;
                unsigned r0a = lb + L_OP + (unsigned)((lane >> 5) * 4096 + (lane & 31) * 16), r1a = lb + L_OP + (unsigned)(512 + (lane >> 4) * 2048 + (lane & 15) * 16);
                unsigned s0a = (unsigned)(lane * 4), s1a = (unsigned)(256 + (lane >> 5) * 64 + (lane & 15) * 4);
                ml_addr(lane, x2a, r2a);
                PIN3(x0a, x1a, x2a); PIN3(r0a, r1a, r2a); PIN2(s0a, s1a);
                cma = lb + L_M; cxa = wave == 0 ? x0a : wave == 1 ? x1a : x2a; cra = wave == 0 ? r0a : wave == 1 ? r1a : r2a;
                cso = WS_OWN + rS + (unsigned)(gi * 16384 + 4096 + gj * 512) + (wave == 0 ? s0a : wave == 1 ? s1a : 384u);
                PIN2(cma, cxa); PIN2(cra, cso);
                asm volatile("" : "=v"(omr), "=v"(owa), "=v"(owb));
            }
        } else {
            const int t = wave - 3;
            StatAddr sa;
            u32x4 sel;
            unsigned long long hi = 0xff00ff00ff00ff00ull;
            auto gather32 = [&](unsigned o, unsigned X, auto fin) __attribute__((always_inline)) {
                u32x4 a0 = ld16_sc1<-2048>(ws, o), a1 = ld16_sc1<-1024>(ws, o), a2 = ld16_sc1<0>(ws, o), a3 = ld16_sc1<1024>(ws, o);
                float v;
                auto sum = [&]() __attribute__((always_inline)) {
                    asm volatile("s_waitcnt vmcnt(3)\n\tv_mfma_f32_16x16x32_bf16 v[40:43], %1, %5, 0\n\ts_waitcnt vmcnt(2)\n\tv_mfma_f32_16x16x32_bf16 v[40:43], %2, %5, v[40:43]\n\t"
                                 "s_waitcnt vmcnt(1)\n\tv_mfma_f32_16x16x32_bf16 v[40:43], %3, %5, v[40:43]\n\ts_waitcnt vmcnt(0)\n\tv_mfma_f32_16x16x32_bf16 v[40:43], %4, %5, v[40:43]\n\t"
                                 "s_nop 7\n\tv_cndmask_b32 v40, v40, v41, %6\n\tv_cndmask_b32 v42, v42, v43, %6\n\ts_nop 1\n\tv_permlane32_swap_b32 v40, v42\n\tv_add_f32 v40, v40, v42\n\t"
                                 "v_cvt_pk_bf16_f32 %0, 0, v40"
                                 : "=&v"(v), "+v"(a0), "+v"(a1), "+v"(a2), "+v"(a3) : "v"(sel), "s"(hi) : "memory", "v40", "v41", "v42", "v43");
                    fin(v);
                    __builtin_amdgcn_sched_barrier(0);
                };
                sum();
                if (__builtin_expect(any_nan(v, v) != 0ull, 0)) {
#pragma unroll 1
                    for (unsigned spins = 0; spins < kSpinCap; ++spins) {
                        slip(X);
                        again16<-2048>(a0, ws, o); again16<-1024>(a1, ws, o); again16<0>(a2, ws, o); again16<1024>(a3, ws, o);
                        asm volatile("s_waitcnt vmcnt(0)" : "+v"(a0), "+v"(a1), "+v"(a2), "+v"(a3) :: "memory");
                        if (!any_empty(max(max(um4(a0), um4(a1)), max(um4(a2), um4(a3))))) break;
                    }
                    sum();
                }
            };
            auto poll32 = [&](unsigned o, unsigned t0) __attribute__((always_inline)) {
                    unsigned long long m0, m1, m2, m3; unsigned cnt = kSpinCap; float v;
#define CHK(r, m) "v_max3_u32 v44, v" #r ", v[" #r "+1], v[" #r "+2]\n\tv_max_u32 v44, v44, v[" #r "+3]\n\tv_cmp_eq_u32 %" #m ", -1, v44\n\t"
                    asm volatile("s_branch 3f\n"
                                 "1:\n\ts_mov_b64 exec, %1\n\tglobal_load_dwordx4 v[0:3], %6, %7 offset:-2048 sc1\n\ts_mov_b64 exec, %2\n\tglobal_load_dwordx4 v[4:7], %6, %7 offset:-1024 sc1\n\t"
                                 "s_mov_b64 exec, %3\n\tglobal_load_dwordx4 v[8:11], %6, %7 sc1\n\ts_mov_b64 exec, %4\n\tglobal_load_dwordx4 v[12:15], %6, %7 offset:1024 sc1\n\ts_mov_b64 exec, -1\n\t"
                                 "s_sub_u32 %5, %5, 1\n\ts_cselect_b64 vcc, 0, -1\n\ts_waitcnt vmcnt(0)\n\ts_branch 2f\n"
                                 "3:\n\ts_memrealtime vcc\n\ts_waitcnt lgkmcnt(0)\n\ts_sub_u32 vcc_lo, %10, vcc_lo\n\ts_cmp_lt_u32 vcc_lo, 0x400\n\ts_cbranch_scc1 3b\n\t"
                                 "global_load_dwordx4 v[0:3], %6, %7 offset:-2048 sc1\n\tglobal_load_dwordx4 v[4:7], %6, %7 offset:-1024 sc1\n\t"
                                 "global_load_dwordx4 v[8:11], %6, %7 sc1\n\tglobal_load_dwordx4 v[12:15], %6, %7 offset:1024 sc1\n\ts_mov_b64 vcc, -1\n"
                                 "2:\n\ts_waitcnt vmcnt(3)\n\tv_mfma_f32_16x16x32_bf16 v[40:43], v[0:3], %8, 0\n\t" CHK(0, 1) "s_waitcnt vmcnt(2)\n\tv_mfma_f32_16x16x32_bf16 v[40:43], v[4:7], %8, v[40:43]\n\t" CHK(4, 2)
                                 "s_waitcnt vmcnt(1)\n\tv_mfma_f32_16x16x32_bf16 v[40:43], v[8:11], %8, v[40:43]\n\t" CHK(8, 3) "s_waitcnt vmcnt(0)\n\tv_mfma_f32_16x16x32_bf16 v[40:43], v[12:15], %8, v[40:43]\n\t" CHK(12, 4)
                                 "s_or_b64 exec, %1, %2\n\ts_or_b64 exec, exec, %3\n\ts_or_b64 exec, exec, %4\n\ts_and_b64 vcc, exec, vcc\n\ts_cbranch_scc1 1b\n\t"
                                 "s_mov_b64 exec, -1\n\tv_cndmask_b32 v40, v40, v41, %9\n\tv_cndmask_b32 v42, v42, v43, %9\n\ts_nop 1\n\tv_permlane32_swap_b32 v40, v42\n\tv_add_f32 v40, v40, v42\n\t"
                                 "v_cvt_pk_bf16_f32 %0, 0, v40"
                                 : "=&v"(v), "=&s"(m0), "=&s"(m1), "=&s"(m2), "=&s"(m3), "+s"(cnt) : "v"(o), "s"(ws), "v"(sel), "s"(hi), "s"(t0 - 1u)
                                 : "memory", "vcc", "scc", "v0", "v1", "v2", "v3", "v4", "v5", "v6", "v7", "v8", "v9", "v10", "v11", "v12", "v13", "v14", "v15", "v40", "v41", "v42", "v43", "v44");
#undef CHK
                return v;
            };
#define PUT8(A, B, M) "v_min_u32 v34, -2, v32\n\tv_min_u32 v32, -2, v32\n\tv_min_u32 v35, -2, v33\n\tv_min_u32 v33, -2, v33\n\tv_permlane16_swap_b32 v32, v34\n\ts_nop 0\n\t" \
                      "v_permlane16_swap_b32 v33, v35\n\ts_mov_b64 exec, " M "\n\tglobal_store_dwordx4 " A ", v[32:35], " B " sc1\n\ts_mov_b64 exec, -1\n\ts_nop 0"
            unsigned xw, qo, qw, qc;
            __builtin_amdgcn_s_setprio(0);
            {
                STAMP(6 + 32 * layer);
                bf16x8 af[6];
                unsigned long long mk = 0x000f000f000f000full;
                if (layer == 0) {
                    const int lane = lane_id();
                    xe = lo16(*reinterpret_cast<const u16*>(smem + L_X0 + xrow(lane) * 1280 + gj * 160 + t * 32 + xcol(lane) * 2));
                    op_addr(0);
                    GUARD(1600);
                    MARK(b_op);
                    if (nj | (ord == OML)) { VMW(0); lds_add(44, nj); }
#pragma unroll 1
                    for (unsigned spins = 0; spins < (kSpinCap << 4); ++spins) if (lds_word(lb + L_CNT + 44) >= 5u) break;
                    STAMP(8);
                } else MARK(b_op);
                const unsigned lo = cma, lw = cxa, so = cso, br = cra, mr = omr, h3 = tile35(gj, 10 * layer + t);
                unsigned go, xs;
#pragma unroll
                for (int s = 0; s < 6; ++s) af[s] = cat8(tr64o(owa, s * 32 * 160), tr64o(owb, s * 32 * 160));
                PINS(mk); PINS(hi);
                if (layer != 0 && t < 4) {
                    sched(X2, 0);
                    STAMP(49);
                    u32x4 ov = ld16_sc1(ws, lo);
                    WAIT1(ov, 0);
                    *(LAS u32x4*)(size_t)lw = ov;
                    if (__builtin_expect(any_empty(um4(ov)), 0)) {
#pragma unroll 1
                        for (unsigned spins = 0; spins < kSpinCap; ++spins) {
                            slip(X2);
                            ov = ld16_sc1(ws, lo);
                            WAIT1(ov, 0);
                            *(LAS u32x4*)(size_t)lw = ov;
                            if (!any_empty(um4(ov))) break;
                        }
                    }
                }
                {
                    bf16x8 b0, b1, b2, b3, b4, b5;
#define QP " row_half_mirror row_mask:0xf bank_mask:0xf\n\t"
                    asm volatile("s_waitcnt lgkmcnt(0)\n\ts_barrier\n\t"
                                 "ds_read_b128 %0, %12\n\tds_read_b64 v[36:37], %13\n\tds_read_b128 %1, %12 offset:64\n\tds_read_b128 %2, %12 offset:128\n\tds_read_b128 %3, %12 offset:192\n\t"
                                 "ds_read_b128 %4, %12 offset:256\n\tds_read_b128 %5, %12 offset:320\n\t"
                                 "s_waitcnt lgkmcnt(5)\n\tv_mfma_f32_16x16x32_bf16 v[32:35], %6, %0, 0\n\tv_max_f32_dpp v38, v36, v36" QP "v_sub_f32 v38, v36, v38\n\t"
                                 "s_waitcnt lgkmcnt(4)\n\tv_mfma_f32_16x16x32_bf16 v[32:35], %7, %1, v[32:35]\n\tv_exp_f32 v38, v38\n\t"
                                 "s_waitcnt lgkmcnt(3)\n\tv_mfma_f32_16x16x32_bf16 v[32:35], %8, %2, v[32:35]\n\tv_mul_f32 v37, v37, v38\n\t"
                                 "s_waitcnt lgkmcnt(2)\n\tv_mfma_f32_16x16x32_bf16 v[32:35], %9, %3, v[32:35]\n\tv_add_f32_dpp v39, v37, v37" QP
                                 "s_waitcnt lgkmcnt(1)\n\tv_mfma_f32_16x16x32_bf16 v[32:35], %10, %4, v[32:35]\n\tv_rcp_f32 v39, v39\n\t"
                                 "s_waitcnt lgkmcnt(0)\n\tv_mfma_f32_16x16x32_bf16 v[32:35], %11, %5, v[32:35]\n\tv_mul_f32 v38, v38, v39\n\ts_nop 6\n\t"
                                 "v_pk_mul_f32 v[32:33], v[32:33], v[38:39] op_sel_hi:[1,0]\n\tv_pk_mul_f32 v[34:35], v[34:35], v[38:39] op_sel_hi:[1,0]\n\ts_nop 0\n\t"
                                 "v_add_f32_dpp v32, v32, v32" QP "v_add_f32_dpp v33, v33, v33" QP "v_add_f32_dpp v34, v34, v34" QP "v_add_f32_dpp v35, v35, v35" QP
                                 "v_cvt_pk_bf16_f32 v32, v32, v33\n\tv_cvt_pk_bf16_f32 v33, v34, v35\n\t" PUT8("%14", "%15", "%16")
                                 : "=&v"(b0), "=&v"(b1), "=&v"(b2), "=&v"(b3), "=&v"(b4), "=&v"(b5)
                                 : "v"(af[0]), "v"(af[1]), "v"(af[2]), "v"(af[3]), "v"(af[4]), "v"(af[5]), "v"(br), "v"(mr), "v"(so), "s"(ws), "s"(mk)
                                 : "memory", "v32", "v33", "v34", "v35", "v36", "v37", "v38", "v39");
#undef QP
                }
                if (layer == 0 && wave == 7) st4_sc1(ws, WS_MB + (unsigned)(gi * 1024 + 64 + gj * 4), now() | 1u);
                const unsigned tp = layer ? 0u : now();
                if (layer == 0) sides();
                STAMP(7 + 32 * layer);
                {
                    const int ln = lane_id();
#pragma unroll
                    for (int k = 0; k < 4; ++k) sel[k] = ((ln >> 1) & 3) == k ? (ln & 1) ? 0x3f800000u : 0x3f80u : 0u;
                    go = h3 + nS + (unsigned)(2048 + ln * 16); xs = lb + L_XS + (unsigned)(xrow(ln) * 160 + t * 32 + xcol(ln) * 2);
                    PIN3(sel, go, xs);
                    sa = stats_addr(ln, h4 + (unsigned)(H4T + gj * 128), 16);
                }
                if (layer != 0) sched(X3, 0);
                STAMP(18 + 32 * layer);
                if (layer != 0) gather32(go, X3, [&](float s) { xw = pk2(xe + s, 0.f); *(LAS u16*)(size_t)xs = (u16)xw; });
                else { xw = pk2(xe + poll32(go, tp + W3), 0.f); *(LAS u16*)(size_t)xs = (u16)xw; }
                xe = lo16(xw);
                STAMP(9 + 32 * layer);
            }
            BAR();
            if (wave == 3) stats(sa);
            MARK(e_op);
            STAMP(11 + 32 * layer);
            {
                LANE_VARS();
                const unsigned im = lb + L_WD + (unsigned)(32 * t + 8 * p4);
                bf16x8 wd0 = cat8(tr64(im + r0s * 160), tr64(im + r1s * 160)); bf16x4 wd1 = tr64(im + (32 + r2s) * 160);
                const unsigned h5 = tile35(gj, 10 * layer + 5 + t);
                unsigned h16 = lb + L_H + (unsigned)(p4 * 96 + kq * 16), h8 = lb + L_H + (unsigned)(p4 * 96 + 64 + kq * 8);
                unsigned so = h5 + (unsigned)((kq & 1) * 8192 + gi * 128 + (kq >> 1) * 64 + p4 * 16);
                unsigned long long mk = 0x000f000f000f000full;
                unsigned go = h5 + nS + (unsigned)(2048 + lane * 16), xs = (layer ? (unsigned)(xrow(lane) * 1280 + gj * 160) : lb + L_XS + (unsigned)(xrow(lane) * 160)) + (unsigned)(t * 32 + xcol(lane) * 2);
                unsigned sta = lb + L_ST + (unsigned)(xrow(lane) * 16);
                if (layer == 0 && wave < 6) {
                    sa = stats_addr(lane, WS_H1 + qS + (unsigned)(gi * 65536 + gj * 896 + 768), 16384);
                    asm volatile("" : "=v"(qo), "=v"(qw), "=v"(qc), "=v"(cma), "=v"(cxa), "=v"(cra), "=v"(cso), "=v"(omr), "=v"(owa), "=v"(owb));
                    asm volatile("" : "=v"(qa), "=v"(pw), "=v"(ma), "=v"(idx), "=v"(qm), "=v"(pr), "=v"(oa), "=v"(oc));
                } else if (layer == 0) {
                    op_addr(1); qk_addr(false); att_addr();
                    const int l5 = lane & 31, c = l5 < 24 ? 24 * (wave - 6) + l5 : 48;
                    qo = WS_H1 + qS + (unsigned)((gi * 4 + (gj >> 1)) * 16384 + 3584 + (lane >> 5) * 896 + c * 16);
                    qw = lb + L_Q + (unsigned)(l5 < 24 ? (c >> 4) * 128 + (c & 3) * 32 + (c & 12) + (lane >> 5) * 16 : lane < 26 ? 384 + (lane & 1) * 16 : 448);
                    qc = lb + L_CS + (unsigned)(c * 16 + (lane >> 5) * 8);
                    PIN3(qo, qw, qc);
                }
                PIN2(wd0, wd1); PIN3(h16, h8, so); PIN3(go, xs, sta); PINS(mk);
                GUARD(2000);
                MARK(b_dn);
                if (wave == 4) {
                    unsigned os = h4 + (unsigned)(H4T + (q4 + 4 * (kq & 1)) * 128 + p4 * 16);
                    float sw = *reinterpret_cast<const float*>(smem + L_CNT + 12);
                    PIN2(os, sw);
                    if (layer == 0) {
#pragma unroll 1
                        for (unsigned spins = 0; spins < (kSpinCap << 6); ++spins) if (lds_word(lb + L_CNT + 56)) break;
                    }
                    sched(layer ? X4 : X4A, 0);
                    u32x4 as;
#pragma unroll 1
                    for (unsigned spins = 0; spins < kSpinCap; ++spins) { as = ld16_sc1(ws, os); WAIT1(as, 0); if (!any_empty(um4(as))) break; }
                    float ss = __uint_as_float(as[0]), sm = __uint_as_float(as[1]), rd = __uint_as_float(as[2]);
                    ss += dpp<0x128>(ss); sm += dpp<0x128>(sm); rd += dpp<0x128>(rd);
                    ss += dpp<0x124>(ss); sm += dpp<0x124>(sm); rd += dpp<0x124>(rd);
                    ss = x16_add(ss); sm = x16_add(sm); rd = x16_add(rd);
                    if (LANES(0xfull)) *reinterpret_cast<f32x4*>(smem + L_ST + p4 * 16) = (f32x4){ss, sm, rd, sw};
                }
                if (layer == 0 && (wave == 3 || wave == 5)) {
                    unsigned ea = lane < 40 ? tile35(gj, 15 + (lane >> 3)) + (unsigned)(gi * 128 + (lane & 7) * 16) : WS_OWN + rS + (unsigned)(gi * 16384 + 4096 + gj * 512 + (lane - 40) * 16);
                    unsigned eb = ea + (lane < 40 ? 8192u : 256u);
                    PIN2(ea, eb);
#pragma unroll 1
                    for (unsigned spins = 0; spins < (kSpinCap << 6); ++spins) { if (lds_word(lb + L_CNT + 32) >= 6u) break; __builtin_amdgcn_s_sleep(1); }
                    if (wave == 5) empty4(1, fS, lane);
                    else { st16m(ws, ea, (u32x4){~0u, ~0u, ~0u, ~0u}, 0xffffffffffffffull); st16m(ws, eb, (u32x4){~0u, ~0u, ~0u, ~0u}, 0xffffffffffffffull); }
                }
                asm volatile("s_waitcnt lgkmcnt(0)\n\ts_barrier\n\tds_read_b128 v[36:39], %2\n\tds_read_b64 v[40:41], %3\n\t"
                             "s_waitcnt lgkmcnt(1)\n\tv_mfma_f32_16x16x32_bf16 v[32:35], %0, v[36:39], 0\n\ts_waitcnt lgkmcnt(0)\n\tv_mfma_f32_16x16x16_bf16 v[42:45], %1, v[40:41], 0\n\ts_nop 7\n\t"
                             "v_pk_add_f32 v[32:33], v[32:33], v[42:43]\n\tv_pk_add_f32 v[34:35], v[34:35], v[44:45]\n\t"
                             "v_cvt_pk_bf16_f32 v32, v32, v33\n\tv_cvt_pk_bf16_f32 v33, v34, v35\n\t" PUT8("%4", "%5", "%6")
                             :: "v"(wd0), "v"(wd1), "v"(h16), "v"(h8), "v"(so), "s"(ws), "s"(mk)
                             : "memory", "v32", "v33", "v34", "v35", "v36", "v37", "v38", "v39", "v40", "v41", "v42", "v43", "v44", "v45");
                STAMP(13 + 33 * layer);
                if (layer == 0 || __builtin_expect(gi == 20 + 2 * t + (gj & 1), 1)) {
                    const f32x4 sr = *(const LAS f32x4*)(size_t)sta;
                    const f32x2 st = ln_stats(sr[0], sr[1]);
                    float rs = bfr(frcp(1.f + ex2(bfr((sr[2] - st[0] * sr[3]) * st[1]) * -LOG2E))), n1 = bfr(bfr(xe - st[0]) * st[1]);
                    PIN2(rs, n1);
                    if (layer == 0) {
                        sched(X5A, 0);
                        STAMP(15);
                        gather32(go, X5A, [&](float y) { xw = pk2(xe + bfr(n1 + bfr(y * rs)), 0.f); *(LAS u16*)(size_t)xs = (u16)xw; });
                        xe = lo16(xw);
                        STAMP(14);
                    } else {
                        st2(out, xs, pk2(xe + bfr(n1 + bfr(poll32(go, when(X5B)) * rs)), 0.f));
                        asm volatile("s_endpgm");
                        MARK(e_dn);
                        STAMP(45);
                    }
                }
            }
            if (layer == 0) {
                BAR();
                if (wave == 3) { stats(sa); STAMP(34); }
                MARK(e_dn);
                if (wave == 3) __builtin_amdgcn_s_setprio(2);
                if (__builtin_expect(wave < 6, 0)) { op_addr(1); qk_addr(false); att_addr(); }
                if (wave >= 6) {
                    const unsigned o = qo;
                    f32x2 cs = -*(const LAS f32x2*)(size_t)qc;
                    float rn = 1.f / DM;
                    unsigned long long m0 = 1ull << 24, m1 = 1ull << 25;
                    PIN2(cs, rn); PINS(m0); PINS(m1);
                    MARK(b_qg);
                    sched(X1, 0);
                    STAMP(51);
                    u32x4 a0, a1, a2, a3; f32x2 u;
                    RETRY({ a0 = ld16_sc1<-3584>(ws, o); a1 = ld16_sc1<-1792>(ws, o); a2 = ld16_sc1<0>(ws, o); a3 = ld16_sc1<1792>(ws, o); },
                          ({ asm volatile("s_waitcnt vmcnt(0)" : "+v"(a0), "+v"(a1), "+v"(a2), "+v"(a3) :: "memory"); max(max(um4(a0), um4(a1)), max(um4(a2), um4(a3))); }),
                          { unsigned sm;
                            asm volatile("s_waitcnt vmcnt(2)\n\tv_pk_add_f32 v[40:41], %2, %4\n\tv_pk_add_f32 v[42:43], %3, %5\n\t"
                                         "s_waitcnt vmcnt(1)\n\tv_pk_add_f32 v[40:41], v[40:41], %6\n\tv_pk_add_f32 v[42:43], v[42:43], %7\n\t"
                                         "s_waitcnt vmcnt(0)\n\tv_pk_add_f32 v[40:41], v[40:41], %8\n\tv_pk_add_f32 v[42:43], v[42:43], %9\n\ts_nop 1\n\t"
                                         "v_permlane32_swap_b32 v40, v42\n\tv_permlane32_swap_b32 v41, v43\n\tv_pk_add_f32 v[40:41], v[40:41], v[42:43]\n\ts_nop 0\n\t"
                                         "v_readlane_b32 %1, v41, 24\n\ts_nop 1\n\tv_mul_f32 v42, %1, %11\n\tv_cvt_pk_bf16_f32 v42, 0, v42\n\tv_pk_fma_f32 %0, %10, v[42:43], v[40:41] op_sel_hi:[1,0,1]"
                                         : "=&v"(u), "=&s"(sm) : "v"(LO2(a0)), "v"(HI2(a0)), "v"(LO2(a1)), "v"(HI2(a1)), "v"(LO2(a2)), "v"(HI2(a2)), "v"(LO2(a3)), "v"(HI2(a3)), "v"(cs), "v"(rn)
                                         : "memory", "v40", "v41", "v42", "v43");
                            unsigned w = pk2v(u);
                            if (LANES(m0)) w = __float_as_uint(u[0]);
                            if (LANES(m1)) w = __float_as_uint(u[1]);
                            *(LAS unsigned*)(size_t)qw = w; __builtin_amdgcn_sched_barrier(0); }, any_nan(u[0], u[1]), X1);
                    MARK(e_qg);
                }
            }
        }
    };
    PINS(ck1); PINS(ck2); PINS(csc);
    chain(IC<0>{});
    STAMP(35);
    att1();
    chain(IC<1>{});
    if (g == 0 && wave == 0) { const unsigned x = 16u * (dgb - tst); while ((int)(now() - tst - 4000u - x) < 0) __builtin_amdgcn_s_sleep(1); }
}

__device__ __attribute__((used, noinline)) void mega_code_end() { asm volatile(".Lmega_code_end:"); }

extern "C" void kernel_launch(void* const* d_in, const int* in_sizes, int n_in, void* d_out, int out_size, void* d_ws,
                              size_t ws_size, hipStream_t stream) {
    const u16* x0 = (const u16*)d_in[0];
    const u16* kc = (const u16*)d_in[1];
    const u16* vc = (const u16*)d_in[2];
    const u16* wq = (const u16*)d_in[3];
    const u16* wo = (const u16*)d_in[4];
    const u16* wr = (const u16*)d_in[6];
    const u16* wg = (const u16*)d_in[7];
    const u16* wu = (const u16*)d_in[8];
    const u16* wd = (const u16*)d_in[9];
    (void)hipFuncSetAttribute((const void*)mega_kernel, hipFuncAttributeMaxDynamicSharedMemorySize, L_END);
    mega_kernel<<<256, 512, L_END, stream>>>(x0, kc, vc, wq, wo, wr, wg, wu, wd, (u16*)d_out, (unsigned char*)d_ws
                                             );
}
```

```cpp
#include <hip/hip_runtime.h>
#include <hip/hip_bf16.h>
#include <stdint.h>

typedef unsigned short u16;
typedef unsigned u32x4 __attribute__((ext_vector_type(4)));
typedef unsigned u32x2 __attribute__((ext_vector_type(2)));
typedef unsigned long long u64x8 __attribute__((ext_vector_type(8)));
typedef unsigned long long u64x2 __attribute__((ext_vector_type(2)));
typedef float f32x4 __attribute__((ext_vector_type(4)));
typedef short s16x4 __attribute__((ext_vector_type(4)));
typedef __attribute__((ext_vector_type(4))) __bf16 bf16x4;
typedef __attribute__((ext_vector_type(8))) __bf16 bf16x8;
#define LAS __attribute__((address_space(3)))

constexpr int DM = 640, HQ = 32, SK = 4096, DH = 192, WIN = 1024, FF = 1536;
constexpr int QD = HQ * DH;
constexpr float SCALE = 0.07216878364870322f, LOG2E = 1.44269504088896341f;
constexpr unsigned kSpinCap = 1u << 16;
#define GJ(g) (((g) >> 3) & 7)
#define GI(g) (((g) & 7) * 4 + ((g) >> 6))

#define D4A 105
#define D5A 87
#define D1 84
#define D2 134
#define D3 68
#define D4 78
#define D5B 74
#define PACE 59
#define PINIT 100
#define PMIN 50
#define DM_ 0
constexpr unsigned X4A = D4A + DM_, X5A = X4A + D5A + DM_, X1 = X5A + D1 + DM_, X2 = X1 + D2 + DM_, X3 = X2 + D3 + DM_, X4 = X3 + D4 + DM_, X5B = X4 + D5B + DM_;
#define TLAND 132
#define XCOST 8
#define WOFF 6
#define CL 25
#define BETA 11
constexpr unsigned WOC = 2, WEARLY = 1;
constexpr unsigned NUNIT = 384 + XCOST + 3 * (9 + 2 * WEARLY), PBURST = 30;
constexpr int LEAD = 12, TOFF = 4;
constexpr unsigned DG2 = 12, W3 = 15;
constexpr int GP = 0, GA = 1, GB = 6, GC = 7, HD = 1;
#define VORD 0, 1, 8, 2, 3, 9, 4, 5, 6, 7, 10, 11
constexpr int kVord[12] = {VORD};
constexpr int v_upto(int h) { int n = 0; for (int i = 0; i <= h; ++i) n += kVord[i] < 8 ? 2 : 4; return n; }
constexpr int v_after(int p) { int h = 0; while (kVord[h] != p) ++h; return 32 - v_upto(h); }
constexpr int OML = 2;

constexpr unsigned WS_H1 = 0;
constexpr unsigned WS_H4 = 2u << 20;
constexpr unsigned H4C = 20480, H4T = H4C + 3072;
constexpr unsigned WS_H35 = 4u << 20;
constexpr unsigned WS_OWN = WS_H35 + 80 * 16384;
constexpr unsigned WS_CS = WS_OWN + 32 * 16384;
constexpr unsigned WS_MB = WS_CS + 256 * 768;
constexpr unsigned WS_R = WS_MB + 256 * 128;
constexpr unsigned WS_END = WS_R + 128;
constexpr unsigned WS_Q0 = 6u << 20;
static_assert(WS_END <= (6u << 20), "ws map");
__device__ __forceinline__ unsigned tile35(int j, int q) { return WS_H35 + (unsigned)((j * 10 + (q >> 1)) * 16384 + (q & 1) * 4096); }
__device__ __forceinline__ unsigned h4s(int s) { return (unsigned)((s >> 1) * 16384 + (s & 1) * 4096); }

constexpr int L_WQ = 0;
constexpr int L_WO = 33792;
constexpr int L_GU = L_WO + 30720;
constexpr int L_WD = L_GU + 16384;
constexpr int L_X0 = L_WD + 8192;
constexpr int L_WR = L_X0 + 5120;
constexpr int L_XS = L_WR + 2048;
constexpr int L_Q = L_XS + 1024;
constexpr int L_OP = L_Q + 512;
constexpr int L_M = L_OP + 8192;
constexpr int L_H = L_M + 64;
constexpr int L_ST = L_H + 384;
constexpr int L_CS = L_ST + 64;
constexpr int L_CS2 = L_CS + 1024;
constexpr int L_CNT = L_CS2 + 1536;
constexpr int L_P = L_CNT + 96;
constexpr int L_O2 = L_P + 72 * 192;
constexpr int L_DBG = L_O2 + 3328;
constexpr int L_END = L_DBG + 2048;
static_assert(L_END <= 163840 && L_END > 81920, "LDS");

typedef __attribute__((ext_vector_type(2))) __bf16 bf16x2;
typedef float f32x2 __attribute__((ext_vector_type(2)));
__device__ __forceinline__ unsigned pk2(float a, float b) { return __builtin_bit_cast(unsigned, __builtin_convertvector((f32x2){a, b}, bf16x2)); }
__device__ __forceinline__ unsigned fexp(float x) { const unsigned e = (__float_as_uint(x) >> 23) & 255u; return e == 255u ? 0u : e; }
__device__ __forceinline__ u32x2 enc21(float a, float b, float c) {
    const float lim = 1048575.f;
    const unsigned i = (unsigned)(int)__builtin_rintf(__builtin_amdgcn_fmed3f(a, -lim, lim)) & 0x1fffffu, j = (unsigned)(int)__builtin_rintf(__builtin_amdgcn_fmed3f(b, -lim, lim)) & 0x1fffffu;
    const unsigned k = (unsigned)(int)__builtin_rintf(__builtin_amdgcn_fmed3f(c, -lim, lim)) & 0x1fffffu;
    return (u32x2){i | j << 21, j >> 11 | k << 10 | (__builtin_fabsf(a) + __builtin_fabsf(b) + __builtin_fabsf(c) < 1e30f ? 0u : 0x80000000u)};
}
__device__ __forceinline__ float lo16(unsigned w) { return __uint_as_float(w << 16); }
__device__ __forceinline__ float hi16(unsigned w) { return __uint_as_float(w & 0xffff0000u); }
__device__ __forceinline__ float bfr(float f) { return __uint_as_float(pk2(0.f, f)); }
template <int CTRL> __device__ __forceinline__ float dpp(float x) {
    return __builtin_bit_cast(float, __builtin_amdgcn_mov_dpp(__builtin_bit_cast(int, x), CTRL, 0xf, 0xf, true));
}
template <int CTRL> __device__ __forceinline__ unsigned dppu(unsigned x) { return (unsigned)__builtin_amdgcn_mov_dpp((int)x, CTRL, 0xf, 0xf, true); }
__device__ __forceinline__ float row16_sum(float v) {
    v += dpp<0xB1>(v); v += dpp<0x4E>(v); v += dpp<0x141>(v); v += dpp<0x140>(v); return v;
}
__device__ __forceinline__ float fmax2(float a, float b) { return __builtin_elementwise_maximum(a, b); }
__device__ __forceinline__ float fmax3(float a, float b, float c) { return fmax2(fmax2(a, b), c); }
__device__ __forceinline__ float x16_add(float v) { auto s = __builtin_amdgcn_permlane16_swap(__float_as_uint(v), __float_as_uint(v), false, false); return __uint_as_float(s[0]) + __uint_as_float(s[1]); }
__device__ __forceinline__ float x32_add(float v) { auto s = __builtin_amdgcn_permlane32_swap(__float_as_uint(v), __float_as_uint(v), false, false); return __uint_as_float(s[0]) + __uint_as_float(s[1]); }
__device__ __forceinline__ float wave_sum(float v) { return x32_add(x16_add(row16_sum(v))); }
__device__ __forceinline__ float rs32(float a, float b) { auto s = __builtin_amdgcn_permlane32_swap(__float_as_uint(a), __float_as_uint(b), false, false); return __uint_as_float(s[0]) + __uint_as_float(s[1]); }

#define GUARD(n) asm volatile("s_branch 1f\n\t.p2align 12,," #n "\n1:")
#define MARK(n) asm volatile(".Lcrit_" #n "_%=:" ::)
#define BAR() asm volatile("s_waitcnt lgkmcnt(0)\n\ts_barrier" ::: "memory")
#define VMW(n) asm volatile("s_waitcnt vmcnt(%0)" :: "i"(n) : "memory")

template <int OFF = 0> __device__ __forceinline__ u32x4 ld16_sc1(const void* sbase, unsigned voff) {
    u32x4 r; asm volatile("global_load_dwordx4 %0, %1, %2 offset:%3 sc1" : "=v"(r) : "v"(voff), "s"(sbase), "i"(OFF) : "memory"); return r;
}
__device__ __forceinline__ unsigned ld4_sc1(const void* sbase, unsigned voff) {
    unsigned r; asm volatile("global_load_dword %0, %1, %2 sc1" : "=v"(r) : "v"(voff), "s"(sbase) : "memory"); return r;
}
template <int OFF = 0> __device__ __forceinline__ void st16_sc1(void* sbase, unsigned voff, u32x4 v) {
    asm volatile("global_store_dwordx4 %0, %1, %2 offset:%3 sc1\n\ts_nop 1" :: "v"(voff), "v"(v), "s"(sbase), "i"(OFF) : "memory");
}
__device__ __forceinline__ void st4_sc1(void* sbase, unsigned voff, unsigned v) {
    asm volatile("global_store_dword %0, %1, %2 sc1" :: "v"(voff), "v"(v), "s"(sbase) : "memory");
}
__device__ __forceinline__ void st8(void* sbase, unsigned voff, u32x2 v) {
    asm volatile("global_store_dwordx2 %0, %1, %2" :: "v"(voff), "v"(v), "s"(sbase) : "memory");
}
__device__ __forceinline__ void st2(void* sbase, unsigned voff, unsigned v) {
    asm volatile("global_store_short %0, %1, %2" :: "v"(voff), "v"(v), "s"(sbase) : "memory");
}
constexpr int RK = 64, RV = 160;
template <int R, int IMM> __device__ __forceinline__ void ld16_nt(const void* sbase, unsigned voff) {
    asm volatile("global_load_dwordx4 v[%c3:%c4], %0, %1 offset:%2 nt" :: "v"(voff), "s"(sbase), "i"(IMM), "i"(R), "i"(R + 3) : "memory");
}
template <int R, int IMM> __device__ __forceinline__ void ld8_nt(const void* sbase, unsigned voff) {
    asm volatile("global_load_dwordx2 v[%c3:%c4], %0, %1 offset:%2 nt" :: "v"(voff), "s"(sbase), "i"(IMM), "i"(R), "i"(R + 1) : "memory");
}
template <int R> __device__ __forceinline__ void smf(f32x4& d, u32x4 a, unsigned idx) {
    asm volatile("v_smfmac_f32_16x16x64_bf16 %0, %1, v[%c3:%c4], %2" : "+v"(d) : "v"(a), "v"(idx), "i"(R), "i"(R + 7));
}
template <int A, int B> __device__ __forceinline__ void zip_in() {
    unsigned t;
    asm volatile("v_perm_b32 %0, v%c2, v%c1, %3\n\tv_perm_b32 v%c2, v%c2, v%c1, %4\n\tv_mov_b32 v%c1, %0" : "=&v"(t) : "i"(A), "i"(B), "s"(0x05040100u), "s"(0x07060302u));
}
template <int A, int B> __device__ __forceinline__ void zip_out(unsigned& lo, unsigned& hi) {
    asm volatile("v_perm_b32 %0, v%c3, v%c2, %4\n\tv_perm_b32 %1, v%c3, v%c2, %5" : "=&v"(lo), "=&v"(hi) : "i"(A), "i"(B), "s"(0x05040100u), "s"(0x07060302u));
}
template <int A, int B, int C, int D> __device__ __forceinline__ void zip_x(unsigned lo, unsigned hi) {
    asm volatile("v_perm_b32 v%c4, v%c3, v%c2, %6\n\tv_perm_b32 v%c5, v%c3, v%c2, %7\n\tv_mov_b32 v%c2, %0\n\tv_mov_b32 v%c3, %1"
                 :: "v"(lo), "v"(hi), "i"(A), "i"(B), "i"(C), "i"(D), "s"(0x05040100u), "s"(0x07060302u));
}
__device__ __forceinline__ void dma16(const void* sbase, unsigned voff, unsigned lds_dst) {
    unsigned keep;
    asm volatile("s_mov_b32 %0, m0\n\ts_mov_b32 m0, %3\n\ts_nop 0\n\tglobal_load_lds_dwordx4 %1, %2\n\ts_mov_b32 m0, %0"
                 : "=&s"(keep) : "v"(voff), "s"(sbase), "s"(lds_dst) : "memory");
}
__device__ __forceinline__ unsigned um4(u32x4 a) { return max(max(a[0], a[1]), max(a[2], a[3])); }
__device__ __forceinline__ bool all_set(unsigned t) { return __builtin_amdgcn_ballot_w64(t == 0u) == 0ull; }
__device__ __forceinline__ bool any_empty(unsigned mx) { return __builtin_amdgcn_ballot_w64(mx == ~0u) != 0ull; }
__device__ __forceinline__ unsigned long long any_nan(float a, float b) { return __builtin_amdgcn_ballot_w64(__builtin_isunordered(a, b)); }
__device__ __forceinline__ f32x4 asf(u32x4 a) { return __builtin_bit_cast(f32x4, a); }
__device__ __forceinline__ u32x4 asu(f32x4 a) { return __builtin_bit_cast(u32x4, a); }
#define LANES(m) __builtin_amdgcn_inverse_ballot_w64(m)

__device__ __forceinline__ bf16x4 tr64(unsigned lds_addr) { return __builtin_amdgcn_ds_read_tr16_b64_v4bf16((LAS bf16x4*)(size_t)lds_addr); }
__device__ __forceinline__ bf16x4 tr64o(unsigned lds_addr, int off) { return __builtin_amdgcn_ds_read_tr16_b64_v4bf16((LAS bf16x4*)((LAS char*)(size_t)lds_addr + off)); }
__device__ __forceinline__ bf16x8 cat8(bf16x4 a, bf16x4 b) { return __builtin_shufflevector(a, b, 0, 1, 2, 3, 4, 5, 6, 7); }
__device__ __forceinline__ bf16x8 lds16(unsigned a) { return *(const LAS bf16x8*)(size_t)a; }
__device__ __forceinline__ bf16x4 lds8(unsigned a) { return *(const LAS bf16x4*)(size_t)a; }
__device__ __forceinline__ f32x4 mfma32(bf16x8 a, bf16x8 b, f32x4 c) { return __builtin_amdgcn_mfma_f32_16x16x32_bf16(a, b, c, 0, 0, 0); }
__device__ __forceinline__ f32x4 mfma32u(u32x4 a, u32x4 b, f32x4 c) { return mfma32(__builtin_bit_cast(bf16x8, a), __builtin_bit_cast(bf16x8, b), c); }
__device__ __forceinline__ f32x4 mfma16(bf16x4 a, bf16x4 b, f32x4 c) {
    return __builtin_amdgcn_mfma_f32_16x16x16bf16_1k(__builtin_bit_cast(s16x4, a), __builtin_bit_cast(s16x4, b), c, 0, 0, 0);
}

#define STAMP(i) do {} while (0)

template <int OFF = 0> __device__ __forceinline__ void st16m(void* sbase, unsigned voff, u32x4 v, unsigned long long mask) {
    asm volatile("s_mov_b64 exec, %4\n\tglobal_store_dwordx4 %0, %1, %2 offset:%3 sc1\n\ts_mov_b64 exec, -1\n\ts_nop 0" :: "v"(voff), "v"(v), "s"(sbase), "i"(OFF), "s"(mask) : "memory");
}
template <int OFF> __device__ __forceinline__ void st16m2(void* sbase, unsigned voff, u32x4 v, unsigned voff2, u32x4 v2, unsigned long long mask) {
    asm volatile("s_mov_b64 exec, %6\n\tglobal_store_dwordx4 %0, %1, %2 sc1\n\tglobal_store_dwordx4 %4, %5, %2 offset:%3 sc1\n\ts_mov_b64 exec, -1\n\ts_nop 0"
                 :: "v"(voff), "v"(v), "s"(sbase), "i"(OFF), "v"(voff2), "v"(v2), "s"(mask) : "memory");
}
#define PINS(a) asm volatile("" : "+s"(a))
#define RETRY(LOAD, EMPTY, COMPUTE, NAN_, X) do { LOAD; COMPUTE; if (__builtin_expect((NAN_) != 0ull, 0)) { \
    _Pragma("unroll 1") for (unsigned spins_ = 0; spins_ < kSpinCap; ++spins_) { slip(X); LOAD; if (!any_empty(EMPTY)) break; } COMPUTE; } } while (0)
__device__ __forceinline__ unsigned now() { return (unsigned)__builtin_amdgcn_s_memrealtime(); }
__device__ __forceinline__ void until(unsigned t) {
    asm volatile("1:\n\ts_memrealtime vcc\n\ts_waitcnt lgkmcnt(0)\n\ts_sub_u32 vcc_lo, %0, vcc_lo\n\ts_cmp_lt_u32 vcc_lo, 0x400\n\ts_cbranch_scc1 1b" :: "s"(t - 1u) : "vcc", "scc", "memory");
}
__device__ __forceinline__ unsigned lds_word(unsigned a) {
    unsigned t_; asm volatile("ds_read_b32 %0, %1\n\ts_waitcnt lgkmcnt(0)\n\ts_nop 0" : "=v"(t_) : "v"(a) : "memory");
    return __builtin_amdgcn_readfirstlane(t_);
}
__device__ __forceinline__ void lds_set(unsigned a, unsigned v) { asm volatile("ds_write_b32 %0, %1" :: "v"(a), "v"(v) : "memory"); }
__device__ __forceinline__ int xrow(int lane) { return (lane >> 5) * 2 + ((lane >> 3) & 1); }
__device__ __forceinline__ int xcol(int lane) { return ((lane >> 4) & 1) * 8 + (lane & 7); }
__device__ __forceinline__ int swz(int r) { return r ^ (((r >> 3) & 1) << 2); }
__device__ __forceinline__ f32x2 unpk(unsigned w) { return (f32x2){lo16(w), hi16(w)}; }
__device__ __forceinline__ unsigned pk2v(f32x2 v) { return __builtin_bit_cast(unsigned, __builtin_convertvector(v, bf16x2)); }
__device__ __forceinline__ float frcp(float x) { return __builtin_amdgcn_rcpf(x); }
__device__ __forceinline__ float ex2(float x) { return __builtin_amdgcn_exp2f(x); }
__device__ __forceinline__ int lane_id() { int l; asm volatile("v_mbcnt_lo_u32_b32 %0, -1, 0\n\tv_mbcnt_hi_u32_b32 %0, -1, %0" : "=v"(l)); return l & 63; }
__device__ __forceinline__ f32x2 ln_stats(float ss, float s) {
    const float mean = s * (1.f / DM);
    const float var = fmax2(__builtin_fmaf(-mean, mean, ss * (1.f / DM)), 0.f);
    return (f32x2){bfr(mean), frcp(bfr(__builtin_amdgcn_sqrtf(bfr(bfr(var) + 1e-5f))))};
}
#define GATHER8(a, o, S) do { a[0] = ld16_sc1<-4 * (S)>(ws, o); a[1] = ld16_sc1<-3 * (S)>(ws, o); a[2] = ld16_sc1<-2 * (S)>(ws, o); a[3] = ld16_sc1<-(S)>(ws, o); \
    a[4] = ld16_sc1<0>(ws, o); a[5] = ld16_sc1<(S)>(ws, o); a[6] = ld16_sc1<2 * (S)>(ws, o); a[7] = ld16_sc1<3 * (S)>(ws, o); } while (0)
template <int OFF> __device__ __forceinline__ void again16(u32x4& r, const void* sbase, unsigned voff) {
    asm volatile("v_cmp_eq_u32 vcc, -1, %1\n\ts_mov_b64 exec, vcc\n\tglobal_load_dwordx4 %0, %2, %3 offset:%4 sc1\n\ts_mov_b64 exec, -1" : "+v"(r) : "v"(um4(r)), "v"(voff), "s"(sbase), "i"(OFF) : "memory", "vcc");
}
#define WAIT8(a, N) asm volatile("s_waitcnt vmcnt(%8)" : "+v"((a)[0]), "+v"((a)[1]), "+v"((a)[2]), "+v"((a)[3]), "+v"((a)[4]), "+v"((a)[5]), "+v"((a)[6]), "+v"((a)[7]) : "i"(N) : "memory")
#define SUM8(a) (((((((asf(a[0]) + asf(a[1])) + asf(a[2])) + asf(a[3])) + asf(a[4])) + asf(a[5])) + asf(a[6])) + asf(a[7]))
#define WAIT1(x, N) asm volatile("s_waitcnt vmcnt(%1)" : "+v"(x) : "i"(N) : "memory")
#define LO2(x) __builtin_shufflevector(x, x, 0, 1)
#define HI2(x) __builtin_shufflevector(x, x, 2, 3)
#define PIN1(a) asm volatile("" : "+v"(a))
#define PIN2(a, b) asm volatile("" : "+v"(a), "+v"(b))
#define PIN3(a, b, c) asm volatile("" : "+v"(a), "+v"(b), "+v"(c))
#define LANE_VARS() const int lane = lane_id(); \
    const int kq = lane >> 4, n = lane & 15, q4 = n >> 2, p4 = lane & 3; \
    const int r0s = 8 * kq + q4 + 4 * (kq & 1), r1s = 8 * kq + q4 + 4 * ((kq & 1) ^ 1);     \
    const int r2s = swz(4 * kq + q4);                                                       \
    (void)r0s; (void)r1s; (void)r2s; (void)p4
template <int V> struct IC { static constexpr int value = V; };
struct Frag80 { bf16x8 a0, a1; bf16x4 a2; };

__global__ void __launch_bounds__(512, 2) __attribute__((amdgpu_num_vgpr(32)))
mega_kernel(const u16*, const u16*, const u16*, const u16*, const u16*, const u16*, const u16*, const u16*, const u16*, u16*, unsigned char*
            ) {
    extern __shared__ __attribute__((aligned(16))) unsigned char smem[];
    const unsigned lb = (unsigned)(size_t)smem;
    const int wave = __builtin_amdgcn_readfirstlane(threadIdx.x >> 6);
    u64x8 ar0; u64x2 ar1; unsigned long long ar2, tp, tst64;
    asm volatile("s_memrealtime %0" : "=&s"(tst64) :: "memory");
    const int g = blockIdx.x, gj = GJ(g), gi = GI(g);
    const f32x4 z4 = {0.f, 0.f, 0.f, 0.f};
    asm volatile("" ::: "v255");
    auto when = [&](unsigned X) __attribute__((always_inline)) {
        u32x2 v;
        asm volatile("ds_read2_b32 %0, %1 offset0:14 offset1:5\n\ts_waitcnt lgkmcnt(0)" : "=v"(v) : "v"(lb + L_CNT) : "memory");
        return __builtin_amdgcn_readfirstlane(v[0]) + (X * __builtin_amdgcn_readfirstlane(v[1]) >> 12);
    };
    auto sched = [&](unsigned X, unsigned add) __attribute__((always_inline)) { until(when(X) + add); };
    auto slip = [&](unsigned X) __attribute__((always_inline)) {
        if (X) lds_set(lb + L_CNT + 56, now() - (X * lds_word(lb + L_CNT + 20) >> 12));
    };
    float xe = 0.f;

    auto code_pf = [&](int slice, int lane) __attribute__((always_inline)) {
        unsigned long long pc0; unsigned len, back, t;
        asm volatile("s_getpc_b64 %0\n.Lmega_pf%=:\n\ts_mov_b32 %1, .Lmega_code_end-.Lmega_code_begin\n\ts_mov_b32 %2, .Lmega_pf%=-.Lmega_code_begin" : "=s"(pc0), "=s"(len), "=s"(back));
        unsigned off = (unsigned)(slice * 4096 + lane * 64); if (off > len) off = len;
        asm volatile("global_load_dword %0, %1, %2" : "=v"(t) : "v"(off & ~3u), "s"(pc0 - back) : "memory");
        return t;
    };
    asm volatile(".Lmega_code_begin:");
    unsigned pf = 0u, pt1, dga, dgb;
    const unsigned wofs = (unsigned)((((gj >> 1) * HQ + gi) * SK + (SK - WIN) + (gj & 1) * 512) * (DH * 2));
    {
        const int lane = threadIdx.x & 63, hi = lane >= 26;
        if (threadIdx.x < 24) lds_set(lb + L_CNT + threadIdx.x * 4, 0u);
        unsigned t2 = (unsigned)(gi * (DH * DM * 2)), t7 = (32u << 20) + (unsigned)(g * 128), to = (unsigned)(gj * 128), w2 = wofs;
        PINS(t2); PINS(t7); PINS(to); PINS(w2);
        to = wave == 7 ? t7 : to; to = wave == 2 ? t2 : to; to = wave < 2 ? w2 : to;
        if (wave == 7) pf = code_pf((g >> 3) & 15, lane);
        unsigned vo = (unsigned)((gj * 80 + swz(2 * wave) + hi) * (QD * 2) + gi * (DH * 2) + (lane - 26 * hi) * 16), d = lb + L_WQ + wave * 832, keep;
        unsigned xo = (0x11043210u >> (4 * wave) & 15u) * 1024 + (unsigned)(lane & (wave < 6 ? 63 : 15)) * 16, xd = lb + L_X0 + min(wave, 6) * 1024;
        PIN2(vo, xo); PINS(d); PINS(xd); PINS(to);
        __builtin_amdgcn_sched_barrier(0);
        unsigned long long ta_, tb_;
        asm volatile("s_memrealtime %0\n\ts_waitcnt lgkmcnt(0)\n\ts_memrealtime %1\n\ts_waitcnt lgkmcnt(0)" : "=&s"(ta_), "=&s"(tb_) :: "memory");
        dga = (unsigned)ta_; dgb = (unsigned)tb_;
        unsigned long long dum_;
    asm volatile("s_load_dwordx16 %0, %5, 0\n\ts_load_dwordx4 %1, %5, 0x40\n\ts_load_dwordx2 %2, %5, 0x50\n\ts_load_dwordx2 %3, %5, %6"
                 : "=&s"(ar0), "=&s"(ar1), "=&s"(ar2), "=&s"(tp), "=&s"(dum_) : "s"(__builtin_amdgcn_kernarg_segment_ptr()), "s"((0xa9876421u >> (4 * wave) & 15u) * 8u) : "memory");
        asm volatile("s_waitcnt lgkmcnt(0)\n\ts_load_dword %0, %4, %6" : "=&s"(pt1), "+s"(ar0), "+s"(ar1), "+s"(ar2), "+s"(tp), "+s"(tst64) : "s"(to) : "memory");
        dma16((const void*)(wave < 5 ? ar0[0] : ar0[5]), xo, xd);
        asm volatile("s_mov_b32 %0, m0\n\ts_mov_b32 m0, %2\n\ts_mov_b64 exec, %4\n\tglobal_load_lds_dwordx4 %1, %3 nt\n\t"
                     ".rept 4\n\ts_add_u32 m0, m0, 6656\n\tv_add_u32 %1, 0x30000, %1\n\tglobal_load_lds_dwordx4 %1, %3 nt\n\t.endr\n\t"
                     "s_mov_b64 exec, -1\n\ts_mov_b32 m0, %0\n\ts_barrier"
                     : "=&s"(keep), "+v"(vo) : "s"(d), "s"(ar0[3]), "s"(0x0003fffffcffffffull) : "memory", "scc");
        __builtin_amdgcn_sched_barrier(0);
    }
    const u16 *const kc = (const u16*)ar0[1], *const vc = (const u16*)ar0[2], *const wo = (const u16*)ar0[4], *const wg = (const u16*)ar0[6], *const wu = (const u16*)ar0[7], *const wd = (const u16*)ar1[0];
    u16* const out = (u16*)ar1[1];
    unsigned char* const ws = (unsigned char*)((ar2 + (32u << 20) - 1) & ~(unsigned long long)((16u << 20) - 1));
    const unsigned tst = (unsigned)tst64;
    auto fix_k1 = [&](auto IC_, auto NC) __attribute__((always_inline)) {
        constexpr int A = RK + 8 * decltype(IC_)::value, B = A + 4;
        unsigned t0, t1, t2, t3;
        asm volatile("s_waitcnt vmcnt(%12)\n\tv_mov_b32 %0, v%c4\n\tv_mov_b32 %1, v%c5\n\tv_mov_b32 %2, v%c6\n\tv_mov_b32 %3, v%c7\n\t"
                     "v_mov_b32_dpp v%c4, v%c8 row_ror:8 row_mask:0xf bank_mask:0xc\n\tv_mov_b32_dpp v%c5, v%c9 row_ror:8 row_mask:0xf bank_mask:0xc\n\t"
                     "v_mov_b32_dpp v%c6, v%c10 row_ror:8 row_mask:0xf bank_mask:0xc\n\tv_mov_b32_dpp v%c7, v%c11 row_ror:8 row_mask:0xf bank_mask:0xc\n\t"
                     "v_mov_b32_dpp v%c8, %0 row_ror:8 row_mask:0xf bank_mask:0x3\n\tv_mov_b32_dpp v%c9, %1 row_ror:8 row_mask:0xf bank_mask:0x3\n\t"
                     "v_mov_b32_dpp v%c10, %2 row_ror:8 row_mask:0xf bank_mask:0x3\n\tv_mov_b32_dpp v%c11, %3 row_ror:8 row_mask:0xf bank_mask:0x3"
                     : "=&v"(t0), "=&v"(t1), "=&v"(t2), "=&v"(t3)
                     : "i"(A), "i"(A + 1), "i"(A + 2), "i"(A + 3), "i"(B), "i"(B + 1), "i"(B + 2), "i"(B + 3), "i"(decltype(NC)::value) : "memory");
    };
    auto trans_v = [&](auto UC) __attribute__((always_inline)) {
        constexpr int R = RV + 4 * decltype(UC)::value, P = 4 * decltype(UC)::value;
#define CELL(r, c) R + 16 * (r) + (c), R + 16 * (r) + 8 + (c)
        unsigned a0, a1, b0, b1, c0, c1;
        VMW(v_after(P)); zip_in<CELL(0, 0)>(); zip_out<CELL(0, 1)>(a0, a1); zip_out<CELL(0, 2)>(b0, b1); zip_out<CELL(0, 3)>(c0, c1);
        VMW(v_after(P + 1)); zip_x<CELL(1, 0), CELL(0, 1)>(a0, a1); zip_in<CELL(1, 1)>(); zip_out<CELL(1, 2)>(a0, a1);
        VMW(v_after(P + 2)); zip_x<CELL(2, 0), CELL(0, 2)>(b0, b1); zip_x<CELL(2, 1), CELL(1, 2)>(a0, a1); zip_in<CELL(2, 2)>(); zip_out<CELL(1, 3)>(a0, a1); zip_out<CELL(2, 3)>(b0, b1);
        VMW(v_after(P + 3)); zip_x<CELL(3, 0), CELL(0, 3)>(c0, c1); zip_x<CELL(3, 1), CELL(1, 3)>(a0, a1); zip_x<CELL(3, 2), CELL(2, 3)>(b0, b1); zip_in<CELL(3, 3)>();
#undef CELL
    };
    auto trans_w = [&](auto UC) __attribute__((always_inline)) {
        constexpr int R = RV + 64 + 2 * decltype(UC)::value, N = v_after(8 + decltype(UC)::value);
        unsigned a0, a1;
        VMW(N + 2); zip_in<R, R + 8>(); zip_out<R + 1, R + 9>(a0, a1);
        VMW(N); zip_x<R + 16, R + 24, R + 1, R + 9>(a0, a1); zip_in<R + 17, R + 25>();
    };
    auto frag80 = [&](unsigned im, int rs, int r0s, int r1s, int r2s) __attribute__((always_inline)) -> Frag80 {
        return Frag80{cat8(tr64(im + r0s * rs), tr64(im + r1s * rs)), cat8(tr64(im + (r0s + 32) * rs), tr64(im + (r1s + 32) * rs)), tr64(im + (r2s + 64) * rs)};
    };
    auto rows80 = [&](unsigned a16, unsigned a8) __attribute__((always_inline)) -> Frag80 { const Frag80 f{lds16(a16), lds16(a16 + 64), lds8(a8)}; __builtin_amdgcn_sched_barrier(0); return f; };
    auto empty5 = [&](int q0, unsigned S, int lane) __attribute__((always_inline)) {
        st16m(ws, tile35(gj, q0 + (lane >> 3)) + S + (unsigned)(gi * 128 + (lane & 7) * 16), (u32x4){~0u, ~0u, ~0u, ~0u}, 0xffffffffffull);
    };
    auto empty4 = [&](int layer, unsigned S, int lane) __attribute__((always_inline)) {
        const unsigned b = WS_H4 + (unsigned)((layer * 32 + gi) * 32768) + S;
        st16_sc1(ws, b + (unsigned)((lane >> 5) * 4096 + gj * 512 + (lane & 31) * 16), (u32x4){~0u, ~0u, ~0u, ~0u});
        st16m(ws, b + (unsigned)(lane < 32 ? 16384 + gj * 512 + lane * 16 : H4C + ((lane - 32) >> 3) * 1024 + gj * 128 + (lane & 7) * 16), (u32x4){~0u, ~0u, ~0u, ~0u},
              layer ? 0xff000000ffffffffull : ~0ull);
    };
    int lead = (int)((0x00003411u | LEAD << 28) >> (4 * wave) & 15u) * PACE * 4;
    auto pace = [&](unsigned n, bool wait = true) __attribute__((always_inline)) {
        unsigned u, w, t0;
        asm volatile("s_mov_b64 exec, 1\n\tds_add_rtn_u32 %0, %3, %4\n\tds_max_u32 %3, %5 offset:12\n\tds_add_rtn_u32 %1, %3, %6 offset:12\n\tds_read_b32 %2, %3 offset:4\n\ts_mov_b64 exec, -1\n\ts_waitcnt lgkmcnt(0)"
                     : "=&v"(u), "=&v"(w), "=&v"(t0) : "v"(lb + L_CNT + 60), "v"(n), "v"((now() - tst) << 6), "v"(n * PMIN * 4u) : "memory");
        int tn = (int)(__builtin_amdgcn_readfirstlane(t0) - tst + PINIT) * 64 + __builtin_amdgcn_readfirstlane(u) * (PACE * 4);
        const int tw = __builtin_amdgcn_readfirstlane(w) - PBURST * 64;
        if (tw > tn) tn = tw;
        tn -= lead;
        if (wait) {
#pragma unroll 1
            for (int k = 0; k < 2048 && (int)((now() - tst) << 6) < tn; ++k) {}
        }
    };
    auto slot = [&](unsigned u) __attribute__((always_inline)) {
        const unsigned tn = lds_word(lb + L_CNT + 64) + PINIT + u * PACE / 16;
#pragma unroll 1
        for (int k = 0; k < 8192 && (int)(now() - tn) < 0; ++k) __builtin_amdgcn_s_sleep(1);
    };
    auto load_wo = [&](int k, int lane, bool early) __attribute__((always_inline)) {
        const int c = k * 64 + lane, pp = c / 10, cc = c - 10 * pp;
        const unsigned f = (unsigned)(swz(pp) * 1280 + cc * 16);
        const u16* wb = wo + (size_t)gi * DH * DM + gj * 80;
#pragma unroll
        for (int a2 = 0; a2 < 6; ++a2) { if (a2 % 3 == 0) { if (early) pace(3); else slot(NUNIT - WOFF + (2 * k + a2 / 3) * WOC); } dma16(wb + 32 * a2 * DM, f, lb + L_WO + (5 * a2 + k) * 1024); }
    };
    auto load_wd = [&](int lane) __attribute__((always_inline)) {
        const u16* db = wd + gi * 48 * DM + gj * 80;
#pragma unroll 1
        for (int k = 0; k < 5; ++k) {
            const int c = k * 64 + lane, pp = c / 10, cc = c - 10 * pp;
            const unsigned fd = (unsigned)(swz(pp) * 1280 + cc * 16);
            if (!(k & 1)) pace(3);
            dma16(db, fd, lb + L_WD + k * 1024);
            if (k < 3) dma16(db + 32 * DM, (k < 2 || lane < 32) ? fd : 0u, lb + L_WD + (5 + k) * 1024);
        }
    };
    auto load_gu = [&](int m, int lane) __attribute__((always_inline)) {
#pragma unroll 1
        for (int k0 = 0; k0 < 3; ++k0) {
            const int k = (k0 + (g & 7)) % 3, c = k * 64 + lane, row = c / 6, cc = c - 6 * row;
            const unsigned o = (unsigned)((swz(row) * FF + cc * 8) * 2), d = lb + L_GU + m * 8192 + k * 1024;
            const u16* wb = (m == 0 ? wg : wu) + gj * 80 * FF + gi * 48;
            pace(3);
            dma16(wb, o, d); dma16(wb + 32 * FF, o, d + 3072);
            if (k < 2) dma16(wb + 64 * FF, (k == 0 || lane < 32) ? o : 0u, d + 6144);
        }
    };
    auto lds_add = [&](int off, unsigned v) __attribute__((always_inline)) { asm volatile("s_mov_b64 exec, 1\n\tds_add_u32 %0, %1\n\ts_mov_b64 exec, -1" :: "v"(lb + L_CNT + off), "v"(v) : "memory"); };
    auto send_flag = [&](int lane) __attribute__((always_inline)) {
        st4_sc1(ws, WS_MB + (unsigned)((gi * 8 + ((gj == (gi & 7) ? lane : gi) & 7)) * 128 + gj * 4), lds_word(lb + L_CNT + 68));
    };
    auto tiles4 = [&](const Frag80 (&f)[4], const Frag80& b, unsigned long long mk) __attribute__((always_inline)) -> u32x4 {
        f32x4 c[4], d[4];
#pragma unroll
        for (int i = 0; i < 4; ++i) c[i] = mfma32(f[i].a0, b.a0, z4);
#pragma unroll
        for (int i = 0; i < 4; ++i) { c[i] = mfma32(f[i].a1, b.a1, c[i]); d[i] = mfma16(f[i].a2, b.a2, z4); }
        __builtin_amdgcn_sched_barrier(0);
        f32x2 lo, hi;
        asm volatile("s_nop 1\n\ts_mov_b64 exec, %18\n\tv_pk_add_f32 %0, %2, %10\n\tv_pk_add_f32 %1, %3, %11\n\t"
                     "s_lshl_b64 exec, exec, 1\n\tv_pk_add_f32 %0, %4, %12\n\tv_pk_add_f32 %1, %5, %13\n\t"
                     "s_lshl_b64 exec, exec, 1\n\tv_pk_add_f32 %0, %6, %14\n\tv_pk_add_f32 %1, %7, %15\n\t"
                     "s_lshl_b64 exec, exec, 1\n\tv_pk_add_f32 %0, %8, %16\n\tv_pk_add_f32 %1, %9, %17\n\ts_mov_b64 exec, -1"
                     : "=&v"(lo), "=&v"(hi) : "v"(LO2(c[0])), "v"(HI2(c[0])), "v"(LO2(c[1])), "v"(HI2(c[1])), "v"(LO2(c[2])), "v"(HI2(c[2])), "v"(LO2(c[3])), "v"(HI2(c[3])),
                       "v"(LO2(d[0])), "v"(HI2(d[0])), "v"(LO2(d[1])), "v"(HI2(d[1])), "v"(LO2(d[2])), "v"(HI2(d[2])), "v"(LO2(d[3])), "v"(HI2(d[3])), "s"(mk) : "scc");
        return asu(__builtin_shufflevector(lo, hi, 0, 1, 2, 3));
    };
    auto empty1 = [&](int half, unsigned S, int lane) __attribute__((always_inline)) {
        const unsigned o = WS_H1 + S + (unsigned)((gi * 4 + 2 * half) * 16384 + gj * 896 + min(lane, 55) * 16);
        st16_sc1(ws, o, (u32x4){~0u, ~0u, ~0u, ~0u}); st16_sc1(ws, o + 16384, (u32x4){~0u, ~0u, ~0u, ~0u});
    };
    auto report = [&]() __attribute__((always_inline)) { if (gj == (gi & 7)) st4_sc1(ws, WS_R + (unsigned)(gi * 4), lds_word(lb + L_CNT + 88)); };
    auto answer = [&](int lane, unsigned r) __attribute__((always_inline)) {
        int d = min(max((int)(r - (tst << 11)) >> 11, -400), 600);
        unsigned k = (r >> 5) & 63u, v = (r >> 1) & 15u;
        v += dppu<0x124>(v); v += dppu<0x128>(v);
#define VOTES(l) (__builtin_amdgcn_readlane(v, l) + __builtin_amdgcn_readlane(v, 16 + l))
        const unsigned c = (VOTES(0) > 32 ? 2u : 0u) | (VOTES(1) > 32 ? 4u : 0u) | (VOTES(2) + VOTES(3) > 64 ? 8u : 0u);
        d += (int)dppu<0xB1>((unsigned)d); d += (int)dppu<0x4E>((unsigned)d); d += (int)dppu<0x141>((unsigned)d); d += (int)dppu<0x140>((unsigned)d);
        k = max(k, dppu<0xB1>(k)); k = max(k, dppu<0x4E>(k)); k = max(k, dppu<0x141>(k)); k = max(k, dppu<0x140>(k));
        d = __builtin_amdgcn_readlane(d, 0) + __builtin_amdgcn_readlane(d, 16);
        k = max((unsigned)__builtin_amdgcn_readlane(k, 0), (unsigned)__builtin_amdgcn_readlane(k, 16));
        st4_sc1(ws, WS_MB + (unsigned)(((lane & 31) * 8 + (lane & 7)) * 128 + 32), (tst + (d >> 5) + TOFF) << 10 | k << 4 | c | 1u);
        lds_set(lb + L_CNT + 36, 1u);
    };
    auto adopt = [&](unsigned t) __attribute__((always_inline)) {
        const int dt = (int)(t - (tst << 10)) >> 10;
        if (dt > -256 && dt < 512) lds_set(lb + L_CNT + 64, tst + max(dt, (int)(lds_word(lb + L_CNT + 88) - (tst << 11) ) / 2048 - CL));
        lds_set(lb + L_CNT + 20, 4096u + ((t >> 4) & 63u) * BETA);
        lds_set(lb + L_CNT + 80, lds_word(lb + L_CNT + 92) ^ t);
        lds_set(lb + L_CNT + 84, (((t >> 1) ^ lds_word(lb + L_CNT + 68)) & 4u) << 11);
        lds_set(lb + L_CNT + 52, 1u);
    };
    struct StatAddr { unsigned a16, b16, a8, b8, dst; };
    auto stats_addr = [&](int lane, unsigned dbase, int dstride) __attribute__((always_inline)) -> StatAddr {
        const int kq = lane >> 4, q4 = (lane >> 2) & 3, p4 = lane & 3;
        const int x1 = LANES(0x4444444444444444ull) ? L_WR + gj * 160 : L_XS + q4 * 160, xa = LANES(0xaaaaaaaaaaaaaaaaull) ? L_XS + 640 : x1;
        StatAddr r{lb + (unsigned)(xa + kq * 16), lb + (unsigned)(L_XS + p4 * 160 + kq * 16), lb + (unsigned)(xa + 128 + kq * 8), lb + (unsigned)(L_XS + p4 * 160 + 128 + kq * 8), dbase + (unsigned)(kq * dstride)};
        PIN3(r.a16, r.b16, r.dst); PIN2(r.a8, r.b8);
        return r;
    };
    auto stats = [&](const StatAddr& r) __attribute__((always_inline)) {
        const bf16x8 a0 = lds16(r.a16), b0 = lds16(r.b16), a1 = lds16(r.a16 + 64), b1 = lds16(r.b16 + 64);
        const bf16x4 a2 = lds8(r.a8), b2 = lds8(r.b8);
        unsigned long long mk = 0x0008000400020001ull; PINS(mk);
        __builtin_amdgcn_sched_barrier(0);
        st16m(ws, r.dst, asu(mfma32(a1, b1, mfma32(a0, b0, z4)) + mfma16(a2, b2, z4)), mk);
    };
    unsigned qa, pw, ma, idx, qm;
    auto qk_addr = [&](bool L1) __attribute__((always_inline)) {
        LANE_VARS();
        qa = lb + L_Q + (unsigned)(kq * 32 + (lane & 2) * 8);
        pw = lb + L_P + (unsigned)((7 + 8 * wave) * 192 + (kq >> 1) * 64 + (n >> 3) * 32 + (lane & 2) * 8 + (q4 & 1) * 8 + (kq & 1) * 4 + (lane & 1) * 2);
        ma = lb + L_M + (unsigned)(wave * 4);
        idx = (lane & 2) ? 0xeeeeu : 0x4444u;
        qm = lb + L_Q;
        PIN3(qa, pw, ma); PIN2(idx, qm);
        if (L1 && wave < 4) __builtin_amdgcn_s_setprio(2);
    };
    auto att_qk0 = [&]() __attribute__((always_inline)) {
        qk_addr(false);
#pragma unroll 1
        for (unsigned spins = 0; spins < (kSpinCap << 4); ++spins) {
            if (lds_word(lb + L_CNT + 4) != 0u) break;
            __builtin_amdgcn_s_sleep(1);
        }
        STAMP(3);
        const u32x4 q0 = *(const LAS u32x4*)(size_t)qa, q1 = *(const LAS u32x4*)(size_t)(qa + 128), q2 = *(const LAS u32x4*)(size_t)(qa + 256);
        f32x4 c0 = z4, c1 = z4, c2 = z4, c3 = z4;
#define SMF(c, q, r) "v_smfmac_f32_16x16x64_bf16 %" #c ", %" #q ", v[" #r "], %7\n\t"
        asm volatile("s_nop 1\n\t" SMF(0, 4, 64:71) SMF(1, 4, 88:95) SMF(2, 4, 112:119) SMF(3, 4, 136:143) SMF(0, 5, 72:79) SMF(1, 5, 96:103) SMF(2, 5, 120:127) SMF(3, 5, 144:151)
                     SMF(0, 6, 80:87) SMF(1, 6, 104:111) SMF(2, 6, 128:135) SMF(3, 6, 152:159) "s_nop 7"
                     : "+v"(c0), "+v"(c1), "+v"(c2), "+v"(c3) : "v"(q0), "v"(q1), "v"(q2), "v"(idx));
#undef SMF
        const float s0 = c0[0] + c0[2], s1 = c1[0] + c1[2], s2 = c2[0] + c2[2], s3 = c3[0] + c3[2];
        float m = fmax2(fmax3(s0, s1, s2), s3);
        m = fmax2(m, dpp<0xB1>(m)); m = fmax2(m, dpp<0x4E>(m)); m = fmax2(m, dpp<0x124>(m)); m = fmax2(m, dpp<0x128>(m));
        const float s01 = LANES(0xffff0000ffff0000ull) ? s1 : s0, s23 = LANES(0xffff0000ffff0000ull) ? s3 : s2;
        const float sv = LANES(0xffffffff00000000ull) ? s23 : s01;
        m = bfr(bfr(m) * SCALE);
        const float p = ex2((bfr(bfr(sv) * SCALE) - m) * LOG2E);
        *(LAS u16*)(size_t)pw = (u16)pk2(p, p);
        *(LAS float*)(size_t)ma = m;
        __builtin_amdgcn_sched_barrier(0);
    };
    unsigned pr, oa, oc, cma, cxa, cra, cso;
    auto att_addr = [&]() __attribute__((always_inline)) {
        LANE_VARS();
        pr = lb + L_P + (unsigned)((7 + 8 * wave - 2 * q4 - (lane & 1)) * 192 + kq * 32 + (lane & 2) * 8);
        oa = lb + L_OP + (unsigned)(wave * 1024 + (lane & 7) * 64 + kq * 16 + (n >> 3) * 8);
        oc = lb + L_OP + (unsigned)(wave * 1024 + 512 + (kq >> 1) * 256 + (lane & 7) * 32 + (kq & 1) * 16 + (n >> 3) * 8);
        PIN3(pr, oa, oc);
    };
    auto comb_out = [&](float u0, float u1, unsigned so, unsigned long long mk, bool both) __attribute__((always_inline)) {
        const unsigned w = min(pk2(u0, u1), 0xfffffffeu);
        if (!both) { st4_sc1(ws, so, w); return; }
        const u32x4 w4 = {dppu<0x00>(w), dppu<0x55>(w), dppu<0xAA>(w), dppu<0xFF>(w)};
        st16m(ws, so, w4, mk);
    };
    auto max8 = [&](f32x4 m0, f32x4 m1) __attribute__((always_inline)) { return fmax3(fmax3(m0[0], m0[1], m0[2]), fmax3(m0[3], m1[0], m1[1]), fmax2(m1[2], m1[3])) * LOG2E; };
    auto comb_o0 = [&](f32x4 m0, f32x4 m1, f32x4 mm, f32x4 o0, f32x4 o1, f32x4 o2, f32x4 o3, unsigned so, unsigned long long mk, bool both) __attribute__((always_inline)) {
        const float M = max8(m0, m1);
        f32x4 f;
#pragma unroll
        for (int k = 0; k < 4; ++k) f[k] = ex2(__builtin_fmaf(mm[k], LOG2E, -M));
        const f32x4 o = f[3] * o3 + (f[2] * o2 + (f[1] * o1 + f[0] * o0));
        comb_out(rs32(o[0], o[2]), rs32(o[1], o[3]), so, mk, both);
    };
    auto comb_o1 = [&](f32x4 m0, f32x4 m1, f32x2 mm, f32x4 o0, f32x4 o1, unsigned so, unsigned long long mk, bool both) __attribute__((always_inline)) {
        const float M = max8(m0, m1);
        const f32x4 o = ex2(__builtin_fmaf(mm[1], LOG2E, -M)) * o1 + ex2(__builtin_fmaf(mm[0], LOG2E, -M)) * o0;
        comb_out(x16_add(rs32(o[0], o[2])), x16_add(rs32(o[1], o[3])), so, mk, both);
    };
    auto ml_addr = [&](int lane, unsigned& xa, unsigned& ra) __attribute__((always_inline)) {
        xa = lb + L_M + (unsigned)(((lane & 15) >> 1) * 4); ra = lb + L_P + (unsigned)((7 + 8 * ((lane & 15) >> 1)) * 192 + (lane & 1) * 64 + (lane >> 4) * 16);
    };
    auto comb_ml = [&](unsigned ma, unsigned xa, unsigned ra, unsigned so, bool both) __attribute__((always_inline)) {
        const u32x4 x4 = *(const LAS u32x4*)(size_t)ra;
        const f32x4 m0 = *(const LAS f32x4*)(size_t)ma, m1 = *(const LAS f32x4*)(size_t)(ma + 16);
        const float mw = *(const LAS float*)(size_t)xa;
        __builtin_amdgcn_sched_barrier(0);
        const f32x4 sp = mfma32u((u32x4){0x3f803f80u, 0x3f803f80u, 0x3f803f80u, 0x3f803f80u}, x4, z4);
        __builtin_amdgcn_sched_barrier(0);
        const float M = max8(m0, m1);
        float L = row16_sum(sp[0] * ex2(__builtin_fmaf(mw, LOG2E, -M)));
        if (both) L = dpp<0x00>(L);
        st16m(ws, so, asu((f32x4){M, L, M, L}), both ? 3ull : 1ull);
    };
    auto att_pv0 = [&]() __attribute__((always_inline)) {
        u32x4 bm[8];
        trans_v(IC<0>{});
        if (v_after(9) > v_after(7)) { trans_w(IC<0>{}); trans_w(IC<1>{}); }
        trans_v(IC<1>{});
#pragma unroll
        for (int e = 0; e < 8; ++e) bm[e] = *(const LAS u32x4*)(size_t)(pr + e * 192);
        __builtin_amdgcn_sched_barrier(0);
        f32x4 da = z4, dc = z4;
        PIN2(da, dc);
        asm volatile("s_nop 1");
        __builtin_amdgcn_sched_barrier(0);
        f32x2 r;
        smf<RV>(da, bm[0], idx); smf<RV + 8>(da, bm[1], idx); smf<RV + 16>(da, bm[2], idx); smf<RV + 24>(da, bm[3], idx);
        smf<RV + 32>(da, bm[4], idx); smf<RV + 40>(da, bm[5], idx); smf<RV + 48>(da, bm[6], idx); smf<RV + 56>(da, bm[7], idx);
        asm volatile("s_nop 7\n\tv_pk_add_f32 %0, %1, %2\n\tds_write_b64 %3, %0\n\ts_mov_b64 exec, 1\n\tds_add_u32 %4, %5\n\ts_mov_b64 exec, -1"
                     : "=&v"(r) : "v"(LO2(da)), "v"(HI2(da)), "v"(oa), "v"(lb + L_CNT + 24), "v"(1u) : "memory");
        GUARD(900);
        MARK(b_t5);
        if (v_after(9) < v_after(7)) { trans_w(IC<0>{}); trans_w(IC<1>{}); }
        trans_w(IC<2>{}); trans_w(IC<3>{});
        STAMP(4);
        asm volatile("s_nop 1\n\tv_smfmac_f32_16x16x64_bf16 %0, %1, v[224:231], %5\n\tv_smfmac_f32_16x16x64_bf16 %0, %2, v[232:239], %5\n\t"
                     "v_smfmac_f32_16x16x64_bf16 %0, %3, v[240:247], %5\n\tv_smfmac_f32_16x16x64_bf16 %0, %4, v[248:255], %5\n\ts_nop 6"
                     : "+v"(dc) : "v"(bm[0]), "v"(bm[1]), "v"(bm[2]), "v"(bm[3]), "v"(idx));
        asm volatile("s_nop 0\n\tv_pk_add_f32 %0, %1, %2\n\tds_write_b64 %3, %0" : "=&v"(r) : "v"(LO2(dc)), "v"(HI2(dc)), "v"(oc) : "memory");
        STAMP(20);
    };
    unsigned long long ck1 = 0xffff0000ffff0000ull, ck2 = 0xffffffff00000000ull, csc = 0x3d93cd3a3d93cd3aull;
    auto att1 = [&]() __attribute__((always_inline)) {
        constexpr int layer = 1;
        {
#define SMF(c, q, r) "v_smfmac_f32_16x16x64_bf16 v[" #c "], v[" #q "], v[" #r "], %0\n\t"
#define MAXD(x) "v_max_f32_dpp v41, v41, v41 " x " row_mask:0xf bank_mask:0xf\n\t"
            asm volatile("s_waitcnt lgkmcnt(0)\n\ts_barrier\n\t"
                         "ds_read_b128 v[0:3], %1\n\tds_read2_b32 v[12:13], %2 offset0:96 offset1:100\n\tds_read_b128 v[4:7], %1 offset:128\n\tds_read_b128 v[8:11], %1 offset:256\n\t"
                         "v_mov_b64 v[16:17], 0\n\tv_mov_b64 v[18:19], 0\n\tv_mov_b64 v[20:21], 0\n\tv_mov_b64 v[22:23], 0\n\tv_mov_b64 v[24:25], 0\n\tv_mov_b64 v[26:27], 0\n\t"
                         "v_mov_b64 v[28:29], 0\n\tv_mov_b64 v[30:31], 0\n\tv_mov_b64 v[32:33], 0\n\tv_mov_b64 v[34:35], 0\n\tv_mov_b64 v[36:37], 0\n\tv_mov_b64 v[38:39], 0\n\t"
                         "s_waitcnt lgkmcnt(3)\n\t" SMF(16:19, 0:3, 64:71) SMF(20:23, 0:3, 88:95) "s_waitcnt lgkmcnt(1)\n\tv_mul_f32 v13, 0x3acccccd, v13\n\t"
                         SMF(24:27, 0:3, 112:119) "v_mul_f32 v12, 0x3acccccd, v12\n\t" SMF(28:31, 0:3, 136:143) "v_fma_f32 v12, -v13, v13, v12\n\t"
                         SMF(16:19, 4:7, 72:79) "v_maximum3_f32 v12, v12, 0, 0\n\t" SMF(20:23, 4:7, 96:103) "v_cvt_pk_bf16_f32 v12, 0, v12\n\t"
                         SMF(24:27, 4:7, 120:127) "v_add_f32 v12, 0x3727c5ac, v12\n\t" SMF(28:31, 4:7, 144:151) "v_cvt_pk_bf16_f32 v12, 0, v12\n\ts_waitcnt lgkmcnt(0)\n\t"
                         SMF(16:19, 8:11, 80:87) "v_sqrt_f32 v12, v12\n\t" SMF(20:23, 8:11, 104:111) "v_cvt_pk_bf16_f32 v12, 0, v12\n\t"
                         SMF(24:27, 8:11, 128:135) "v_rcp_f32 v12, v12\n\t" SMF(28:31, 8:11, 152:159)
                         "s_nop 2\n\tv_add_f32 v42, v16, v18\n\ts_nop 0\n\tv_add_f32 v43, v20, v22\n\tv_max_f32 v41, v42, v43\n\tv_add_f32 v44, v24, v26\n\tv_add_f32 v45, v28, v30\n\t"
                         "v_max3_f32 v41, v41, v44, v45\n\tv_cndmask_b32 v42, v42, v43, %8\n\tv_cndmask_b32 v44, v44, v45, %8\n\t"
                         MAXD("quad_perm:[1,0,3,2]") "v_cndmask_b32 v40, v42, v44, %9\n\ts_nop 0\n\t" MAXD("quad_perm:[2,3,0,1]") "s_nop 1\n\t" MAXD("row_ror:4") "s_nop 1\n\t" MAXD("row_ror:8")
                         "v_pk_mul_f32 v[40:41], v[12:13], v[40:41] op_sel_hi:[0,1]\n\tv_cvt_pk_bf16_f32 v40, 0, v40\n\tv_cvt_pk_bf16_f32 v41, 0, v41\n\t"
                         "v_pk_mul_f32 v[40:41], %10, v[40:41]\n\tv_cvt_pk_bf16_f32 v40, 0, v40\n\tv_cvt_pk_bf16_f32 v41, 0, v41\n\t"
                         "v_sub_f32 v40, v40, v41\n\tv_mul_f32 v40, 0x3fb8aa3b, v40\n\tv_exp_f32 v40, v40\n\ts_nop 0\n\tv_cvt_pk_bf16_f32 v40, v40, v40\n\tds_write_b16 %3, v40\n\t"
                         "ds_read_b128 v[0:3], %4\n\tds_read_b128 v[4:7], %4 offset:192\n\tds_read_b128 v[8:11], %4 offset:384\n\tds_read_b128 v[12:15], %4 offset:576\n\t"
                         "ds_read_b128 v[16:19], %4 offset:768\n\tds_read_b128 v[20:23], %4 offset:960\n\tds_read_b128 v[24:27], %4 offset:1152\n\tds_read_b128 v[28:31], %4 offset:1344\n\t"
                         "ds_write_b32 %5, v41\n\t"
                         "s_waitcnt lgkmcnt(8)\n\t" SMF(32:35, 0:3, 160:167) SMF(36:39, 0:3, 224:231) "s_waitcnt lgkmcnt(7)\n\t" SMF(32:35, 4:7, 168:175) SMF(36:39, 4:7, 232:239)
                         "s_waitcnt lgkmcnt(6)\n\t" SMF(32:35, 8:11, 176:183) SMF(36:39, 8:11, 240:247) "s_waitcnt lgkmcnt(4)\n\t" SMF(32:35, 12:15, 184:191) SMF(36:39, 12:15, 248:255)
                         SMF(32:35, 16:19, 192:199) "s_waitcnt lgkmcnt(1)\n\t" SMF(32:35, 20:23, 200:207) SMF(32:35, 24:27, 208:215) SMF(32:35, 28:31, 216:223)
                         "s_nop 2\n\tv_pk_add_f32 v[36:37], v[36:37], v[38:39]\n\tds_write_b64 %7, v[36:37]\n\ts_nop 2\n\tv_pk_add_f32 v[32:33], v[32:33], v[34:35]\n\tds_write_b64 %6, v[32:33]"
                         :: "v"(idx), "v"(qa), "v"(qm), "v"(pw), "v"(pr), "v"(ma), "v"(oa), "v"(oc), "s"(ck1), "s"(ck2), "s"(csc)
                         : "memory", "v0", "v1", "v2", "v3", "v4", "v5", "v6", "v7", "v8", "v9", "v10", "v11", "v12", "v13", "v14", "v15", "v16", "v17", "v18", "v19", "v20", "v21", "v22", "v23",
                           "v24", "v25", "v26", "v27", "v28", "v29", "v30", "v31", "v32", "v33", "v34", "v35", "v36", "v37", "v38", "v39", "v40", "v41", "v42", "v43", "v44", "v45");
#undef SMF
#undef MAXD
            STAMP(20 + 32 * layer);
            BAR();
            if (__builtin_expect(wave == 0, 1)) {
                STAMP(5 + 32 * layer);
                comb_o0(*(const LAS f32x4*)(size_t)cma, *(const LAS f32x4*)(size_t)(cma + 16), *(const LAS f32x4*)(size_t)cxa, *(const LAS f32x4*)(size_t)cra,
                        *(const LAS f32x4*)(size_t)(cra + 1024), *(const LAS f32x4*)(size_t)(cra + 2048), *(const LAS f32x4*)(size_t)(cra + 3072), cso, 0x1111111111111111ull, false);
            }
            __builtin_amdgcn_sched_barrier(0);
            if (__builtin_expect(wave == 1, 1))
                comb_o1(*(const LAS f32x4*)(size_t)cma, *(const LAS f32x4*)(size_t)(cma + 16), *(const LAS f32x2*)(size_t)cxa, *(const LAS f32x4*)(size_t)cra, *(const LAS f32x4*)(size_t)(cra + 1024), cso, 0x0000111100001111ull, false);
            __builtin_amdgcn_sched_barrier(0);
            if (__builtin_expect(wave == 2, 1)) comb_ml(cma, cxa, cra, cso, false);
            MARK(e_q);
            if (wave < 3) STAMP(21 + 32 * layer);
            __builtin_amdgcn_s_setprio(0);
        }
    };

    unsigned nj = 0u, qS = 0u, fS = 0u, rS = 0u, nS = 0u; int ord;
    {
        const int tid = threadIdx.x, lane = tid & 63;
        asm volatile("ds_max_u32 %0, %1" :: "v"(lb + L_CNT + 64), "v"(tst) : "memory");
        unsigned mb = 0u;
        const u16* kbase = kc + wofs / 2;
        const u16* vbase = vc + wofs / 2;
        unsigned pt0 = 0u;
        if (wave == 7) {
            __builtin_amdgcn_s_setprio(3);
            if (lane < 10) *reinterpret_cast<u32x4*>(smem + L_XS + 640 + lane * 16) = (u32x4){0x3f803f80u, 0x3f803f80u, 0x3f803f80u, 0x3f803f80u};
            if (lane < 16) { *reinterpret_cast<u32x4*>(smem + L_CS + 768 + lane * 16) = (u32x4){0u, 0u, 0u, 0u}; }
        } else {
            if (wave >= 4) {
                if (wave == 4) st16_sc1(ws, WS_OWN + (unsigned)(gi * 16384 + (lane >> 5) * 8192 + gj * 512 + (lane & 31) * 16), (u32x4){~0u, ~0u, ~0u, ~0u});
                else empty5(0, (unsigned)(wave - 5) * 8192u, lane);
                if (wave == 6) {
                    __builtin_amdgcn_s_setprio(3);
                    if (gi + gj == 0) st4_sc1(ws, WS_R + (lane & 31) * 4, 0u);
                    st4_sc1(ws, WS_MB + (unsigned)((gi * 8 + gj) * 128 + (lane & 31) * 4), 0u);
                }
            } else if (wave >= 2) {
                unsigned long long c0, c1;
                asm volatile("s_waitcnt lgkmcnt(0)\n\ts_memtime %0\n\ts_waitcnt lgkmcnt(0)\n\ts_load_dword %2, %3, %4\n\ts_waitcnt lgkmcnt(0)\n\ts_memtime %1\n\ts_waitcnt lgkmcnt(0)"
                             : "=&s"(c0), "=&s"(c1), "=&s"(pt0) : "s"(ws), "s"((unsigned)(min(gi & 3, 2) << 21) + ((((tst + g * 977u) * 2654435761u) >> 11 & 0x1fdf80u) | ((wave ^ gj) & 1) * 8192)) : "memory");
                pt0 = ((wave ^ gj) & 1) ? (unsigned)c0 - (unsigned)c1 : (unsigned)c1 - (unsigned)c0;
            }
            STAMP(36);
            *reinterpret_cast<u32x4*>(smem + L_P + tid * 16) = (u32x4){0u, 0u, 0u, 0u};
            if (tid < 72 * 12 - 448) *reinterpret_cast<u32x4*>(smem + L_P + 7168 + tid * 16) = (u32x4){0u, 0u, 0u, 0u};
            if (wave >= 4) {
                if (wave == 6) VMW(2); else VMW(1);
                asm volatile("ds_max_u32 %0, %1" :: "v"(lb + L_CNT + 64), "v"(now() - TLAND) : "memory");
                STAMP(40);
                lds_add(0, 1u);
            } else if (wave >= 2) { lds_add(20, pt0); VMW(0); lds_add(0, 1u); }
        }
        asm volatile("s_waitcnt lgkmcnt(0)" :: "s"(pt1) : "memory");
        const int rot = gj + wave;
        const unsigned r0 = now(), c0 = (unsigned)__builtin_amdgcn_s_memtime();
        u32x4 a[4];
        unsigned ko = (unsigned)((wave * 64 + (lane & 7)) * 384 + (((lane >> 3) & 1) * 4 + (lane >> 4)) * 16);
        PIN1(ko);
        auto k_group = [&](auto GC_) __attribute__((always_inline)) {
            constexpr int G = decltype(GC_)::value, R = RK + 24 * (G >> 1) + 4 * (G & 1);
            const unsigned o = ko + (unsigned)((((G >> 1) + rot) & 3) * 6144 + (G & 1) * 3072);
            pace(3);
            ld16_nt<R, 0>(kbase, o); ld16_nt<R + 8, 128>(kbase, o); ld16_nt<R + 16, 256>(kbase, o);
            const int lane = lane_id();
            const unsigned ho = WS_Q0 + (unsigned)((gi * 4 + (gj >> 1)) * 4096 + lane * 16);
            if (wave < 7) {
                asm volatile("; COND_BEGIN");
                if (G == 0 && wave >= 4) { VMW(3); lds_add(32, 1u); }
                if (G == 0) {
                    if (wave < 2) { VMW(3); lds_add(0, 1u); load_gu(wave, lane); }
                    if (wave == 2) load_wd(lane);
                    if (wave == 3) {
#pragma unroll 1
                        for (int k = 5 - WEARLY; k < 5; ++k) load_wo(k, lane, true);
                    }
                }
                asm volatile("; COND_END");
            }
            if (wave == 7) {
                asm volatile("; COND_BEGIN");
                if (G == GP) {
                    VMW(3 * (GP + 1));
#pragma unroll 1
                    for (unsigned spins = 0; spins < (kSpinCap << 4); ++spins) {
                        if (lds_word(lb + L_CNT) >= 7u) break;
                        __builtin_amdgcn_s_sleep(1);
                    }
                    STAMP(26);
                    const unsigned char* p = smem + L_X0 + (lane >> 4) * 1280;
                    const float c0 = lo16(*reinterpret_cast<const u16*>(p));
                    const f32x2 c2 = {c0, c0};
                    f32x2 s2 = {0.f, 0.f}, q2 = s2;
#pragma unroll
                    for (int k = 0; k < 5; ++k) {
                        const u32x4 w = *reinterpret_cast<const u32x4*>(p + k * 256 + (lane & 15) * 16);
#pragma unroll
                        for (int e = 0; e < 4; ++e) { const f32x2 d = unpk(w[e]) - c2; s2 += d; q2 += d * d; }
                    }
                    const float s = row16_sum(s2[0] + s2[1]), qq = row16_sum(q2[0] + q2[1]);
                    const float ms = s * (1.f / DM);
                    const unsigned mv = pk2(c0 + ms, fmax2(qq - s * ms, 0.f) * (1.f / DM));
                    const float mb = lo16(mv), rden = frcp(bfr(__builtin_amdgcn_sqrtf(bfr(hi16(mv) + 1e-5f))));
                    const f32x2 mb2 = {mb, mb}, rd2 = {rden, rden};
                    if ((lane & 15) < 10) {
                        u32x4 w = *reinterpret_cast<const u32x4*>(p + gj * 160 + (lane & 15) * 16);
#pragma unroll
                        for (int e = 0; e < 4; ++e) w[e] = pk2v(unpk(pk2v(unpk(w[e]) - mb2)) * rd2);
                        *reinterpret_cast<u32x4*>(smem + L_XS + (lane >> 4) * 160 + (lane & 15) * 16) = w;
                    }
                    STAMP(30);
                }
                if (G == GP || G == GA) {
#pragma unroll 1
                    for (int blk = 0; blk < 3; ++blk) {
                        LANE_VARS();
                        const unsigned xb = lb + L_XS + (unsigned)((G == GP ? q4 : 4) * 160);
                        Frag80 f[4];
#pragma unroll
                        for (int i = 0; i < 4; ++i) f[i] = frag80(lb + L_WQ + (unsigned)(128 * blk + 32 * i + 8 * p4), 416, r0s, r1s, r2s);
                        const Frag80 b = rows80(xb + kq * 16, xb + 128 + kq * 8);
                        const u32x4 c = tiles4(f, b, 0x1111111111111111ull);
                        if (G == GP) *reinterpret_cast<u32x4*>(smem + L_OP + q4 * 768 + 256 * blk + p4 * 64 + kq * 16) = c;
                        else *reinterpret_cast<u32x4*>(smem + L_CS + 256 * blk + kq * 64 + p4 * 16) = c;
                    }
                    if (G == GP) {
                        asm volatile("" ::: "memory");
                        const int ln = lane_id();
                        f32x2 c[6];
                        unsigned m = 20u;
#pragma unroll
                        for (int k = 0; k < 6; ++k) {
                            c[k] = *reinterpret_cast<const f32x2*>(smem + L_OP + (k / 3) * 1536 + ln * 24 + (k % 3) * 8);
                            m = max(m, max(fexp(c[k][0]), fexp(c[k][1])));
                        }
                        m = max(m, dppu<0xB1>(m)); m = max(m, dppu<0x4E>(m)); m = max(m, dppu<0x141>(m)); m = max(m, dppu<0x140>(m));
                        m = max(max((unsigned)__builtin_amdgcn_readlane(m, 0), (unsigned)__builtin_amdgcn_readlane(m, 16)), max((unsigned)__builtin_amdgcn_readlane(m, 32), (unsigned)__builtin_amdgcn_readlane(m, 48)));
                        lds_set(lb + L_CNT + 76, m);
                        const float f = __uint_as_float((273u - m) << 23);
#pragma unroll
                        for (int k = 0; k < 2; ++k) {
                            const u32x2 w0 = enc21(c[3 * k][0] * f, c[3 * k][1] * f, c[3 * k + 1][0] * f), w1 = enc21(c[3 * k + 1][1] * f, c[3 * k + 2][0] * f, c[3 * k + 2][1] * f);
                            st16_sc1(ws, WS_Q0 + (unsigned)((gi * 4 + 2 * k + (ln >> 5)) * 4096 + gj * 512 + (ln & 31) * 16), (u32x4){w0[0], w0[1], w1[0], w1[1]});
                        }
                        STAMP(2);
                        WAIT1(pf, 3 * (GP + 1) + 2);
                    }
                }
                if (G == GA) {
                    const unsigned half = __builtin_amdgcn_s_getreg(2 << 6 | 20);
                    lds_set(lb + L_CNT + 68, lds_word(lb + L_CNT + 64) << 11 | lds_word(lb + L_CNT + 76) << 3 | 1u | (((int)lds_word(lb + L_CNT + 20) > 0) ^ half) << 1 | half << 2);
                    VMW(3 * (GA - GP));
#pragma unroll 1
                    for (unsigned spins = 0; spins < (kSpinCap << 4); ++spins) if (lds_word(lb + L_CNT + 32) >= 3u) break;
                    send_flag(lane_id());
                    lds_set(lb + L_CNT + 8, 1u);
                    STAMP(17);
                    f32x2 r2 = {0.f, 0.f};
#pragma unroll
                    for (int k = 0; k < 5; ++k) r2 += unpk(*reinterpret_cast<const unsigned*>(smem + L_WR + k * 256 + lane_id() * 4));
                    *reinterpret_cast<float*>(smem + L_CNT + 12) = wave_sum(r2[0] + r2[1]);
                }
                asm volatile("; COND_END");
            }
            if (wave == 6) {
                asm volatile("; COND_BEGIN");
                if (G == GB - 1) mb = ld4_sc1(ws, WS_MB + (unsigned)((gi * 8 + gj) * 128 + (gi & 7) * 4));
                if (G == GB) {
                    WAIT1(mb, 3);
#pragma unroll 1
                    for (unsigned spins = 0; spins < kSpinCap && !all_set(mb); ++spins) {
                        if ((spins & 7u) == 7u && lds_word(lb + L_CNT + 8)) send_flag(lane);
                        mb = ld4_sc1(ws, WS_MB + (unsigned)((gi * 8 + gj) * 128 + (gi & 7) * 4));
                        WAIT1(mb, 0);
                    }
                    mb = ld4_sc1(ws, WS_MB + (unsigned)((gi * 8 + (gi & 7)) * 128 + (lane & 7) * 4));
                }
                if (G == GC) {
                    WAIT1(mb, 3 * (GC - GB));
#pragma unroll 1
                    for (unsigned spins = 0; spins < kSpinCap && !all_set(mb); ++spins) {
                        if ((spins & 7u) == 7u && lds_word(lb + L_CNT + 8)) send_flag(lane);
                        mb = ld4_sc1(ws, WS_MB + (unsigned)((gi * 8 + (gi & 7)) * 128 + (lane & 7) * 4));
                        WAIT1(mb, 0);
                    }
                    lds_set(lb + L_CNT + 92, (__builtin_amdgcn_readlane(mb, gi & 7) & 4u) ? 14u : 0u);
                    int d = min(max((int)(mb - (tst << 11)) >> 11, -400), 600);
                    d += (int)dppu<0xB1>((unsigned)d); d += (int)dppu<0x4E>((unsigned)d); d += (int)dppu<0x141>((unsigned)d);
                    const unsigned tm = tst + (unsigned)(__builtin_amdgcn_readfirstlane(d) >> 3);
                    lds_set(lb + L_CNT + 64, tm);
                    const float slow = (float)(now() - r0) * 24.f * 256.f * frcp((float)((unsigned)__builtin_amdgcn_s_memtime() - c0)) - 256.f;
                    lds_set(lb + L_CNT + 88, tm << 11 | (unsigned)__builtin_amdgcn_fmed3f(slow, 0.f, 63.f) << 5 | __builtin_popcount((unsigned)__builtin_amdgcn_ballot_w64((mb & 2u) != 0u) & 255u) << 1 | 1u);
                    report();
                    a[0] = ld16_sc1<0>(ws, ho); a[1] = ld16_sc1<1024>(ws, ho); a[2] = ld16_sc1<2048>(ws, ho); a[3] = ld16_sc1<3072>(ws, ho);
                    pace(XCOST, false);
                    STAMP(31);
                }
                asm volatile("; COND_END");
            } else if (G == GC) { for (int k = 0; k < 4; ++k) asm volatile("" : "=v"(a[k])); }
        };
        k_group(IC<0>{}); k_group(IC<1>{}); k_group(IC<2>{}); k_group(IC<3>{}); k_group(IC<4>{}); k_group(IC<5>{}); k_group(IC<6>{}); k_group(IC<7>{});
        {
            const int lane = lane_id();
            const unsigned v0 = (unsigned)((wave * 64 + 4 * (lane >> 4)) * 384 + (lane & 15) * 16);
            const unsigned v1 = v0 + 256 - (unsigned)((lane & 15) * 8);
            auto v_group = [&](auto HC) __attribute__((always_inline)) {
                constexpr int h = decltype(HC)::value, p = kVord[h], R = p < 8 ? RV + 16 * (p & 3) + 4 * (p >> 2) : RV + 64 + 2 * (p - 8), NV = v_upto(h);
                const unsigned o = (p < 8 ? v0 : v1) + (unsigned)((((p < 8 ? p >> 1 : p) + rot) & 3) * 6144);
                pace(2);
                if (p < 8) { ld16_nt<R, (p & 1) * 768>(vbase, o); ld16_nt<R + 8, (p & 1) * 768 + 384>(vbase, o); }
                else { ld8_nt<R, 0>(vbase, o); ld8_nt<R + 8, 384>(vbase, o); ld8_nt<R + 16, 768>(vbase, o); ld8_nt<R + 24, 1152>(vbase, o); }
                if (wave == 7 && gi + gj == 0) {
                    asm volatile("; COND_BEGIN");
                    if (h == 3 || h == 6) mb = ld4_sc1(ws, WS_R + (unsigned)((lane & 31) * 4));
                    if (h == 4 || h == 7) {
                        WAIT1(mb, NV - v_upto(h - 1));
                        if (all_set(mb) && !lds_word(lb + L_CNT + 36)) answer(lane, mb);
                    }
                    asm volatile("; COND_END");
                }
                if (wave == 6) {
                    asm volatile("; COND_BEGIN");
                if (h == HD) {
                    asm volatile("s_waitcnt vmcnt(%4)" : "+v"(a[0]), "+v"(a[1]), "+v"(a[2]), "+v"(a[3]) : "i"(3 * (7 - GC) + NV) : "memory");
                    float sq[6] = {0.f, 0.f, 0.f, 0.f, 0.f, 0.f};
#pragma unroll
                    for (int k = 0; k < 4; ++k) {
                        const float f = __uint_as_float(((((unsigned)__builtin_amdgcn_ds_bpermute((2 * k + (lane >> 5)) * 4, (int)mb) >> 3) & 255u) - 19u) << 23);
#pragma unroll
                        for (int e = 0; e < 2; ++e) {
                            const unsigned lo = a[k][2 * e], hi = a[k][2 * e + 1];
                            const float fe = (int)hi < 0 ? __builtin_nanf("") : f;
                            sq[3 * e] += (float)((int)(lo << 11) >> 11) * fe;
                            sq[3 * e + 1] += (float)((int)(__builtin_amdgcn_alignbit(hi, lo, 21) << 11) >> 11) * fe;
                            sq[3 * e + 2] += (float)((int)(hi << 1) >> 11) * fe;
                        }
                    }
#pragma unroll
                    for (int e = 0; e < 3; ++e) {
                        const int pi = 3 * (lane & 31) + e;
                        *reinterpret_cast<unsigned*>(smem + L_Q + (pi >> 3) * 32 + (pi & 6) * 2 + (pi & 1) * 16) = pk2(x32_add(sq[2 * e]), x32_add(sq[2 * e + 1]));
                    }
                    asm volatile("s_waitcnt lgkmcnt(0)\n\tds_write_b32 %0, %1" :: "v"(lb + L_CNT + 4), "v"(1u) : "memory");
                    STAMP(27);
                    lead = LEAD * PACE * 4;
                }
                    if (h == 6) mb = ld4_sc1(ws, WS_MB + (unsigned)((gi * 8 + (gi & 7)) * 128 + 32));
                    if (h == 8) {
                        WAIT1(mb, NV - v_upto(6));
                        const unsigned r = __builtin_amdgcn_readfirstlane(mb);
                        if (r) { adopt(r); STAMP(29); }
                    }
                    asm volatile("; COND_END");
                }
                if constexpr (h < 6) {
                    constexpr int i = 2 * h;
                    fix_k1(IC<i>{}, IC<20 - 6 * (i / 3) - i % 3 + NV>{});
                    fix_k1(IC<i + 1>{}, IC<20 - 6 * ((i + 1) / 3) - (i + 1) % 3 + NV>{});
                }
                if (h == 5) {
                    STAMP(1);
                    __builtin_amdgcn_s_setprio(0);
                    att_qk0();
                }
            };
            v_group(IC<0>{}); v_group(IC<1>{}); v_group(IC<2>{}); v_group(IC<3>{}); v_group(IC<4>{}); v_group(IC<5>{});
            v_group(IC<6>{}); v_group(IC<7>{}); v_group(IC<8>{}); v_group(IC<9>{}); v_group(IC<10>{}); v_group(IC<11>{});
            lds_add(16, 1u);
            if (wave == 3) lds_add(44, WEARLY);
        }
    }
    {
        STAMP(28);
        att_addr();
        const int lane = lane_id();
        cma = lb + L_M; cxa = lb + L_M + (unsigned)((lane >> 4) * 8); cra = lb + L_OP + (unsigned)(512 + (lane >> 4) * 2048 + (lane & 15) * 16);
        cso = WS_OWN + (unsigned)(gi * 16384 + gj * 512 + 256 + (lane >> 5) * 64 + (lane & 12) * 4 + (lane & 1) * 8192);
        PIN2(cma, cxa); PIN2(cra, cso);
        att_pv0();
    }
    {
        f32x4 m0, m1, o0, o1; f32x2 mm; unsigned r, taken;
        asm volatile("s_mov_b64 exec, 1\n\tds_add_rtn_u32 %5, %7, %8 offset:%11\n\ts_mov_b64 exec, -1\n\t"
                     "ds_read_b128 %0, %7\n\tds_read_b128 %1, %7 offset:16\n\tds_read_b64 %2, %9\n\tds_read_b128 %3, %10\n\tds_read_b128 %4, %10 offset:1024\n\t"
                     "ds_read_b32 %6, %7 offset:%12\n\ts_waitcnt lgkmcnt(0)"
                     : "=&v"(m0), "=&v"(m1), "=&v"(mm), "=&v"(o0), "=&v"(o1), "=&v"(r), "=&v"(taken)
                     : "v"(cma), "v"(1u), "v"(cxa), "v"(cra), "i"(L_CNT + 28 - L_M), "i"(L_CNT + 40 - L_M) : "memory");
        ord = __builtin_amdgcn_readfirstlane(r);
        if (ord == 7) { STAMP(5); comb_o1(m0, m1, mm, o0, o1, cso, 0x0000333300003333ull, true); lds_set(lb + L_CNT + 48, now() | 1u); STAMP(21); }
        MARK(e_t5);
        if (ord <= OML + 1 || (unsigned)__builtin_amdgcn_readfirstlane(taken) < 5u - WEARLY) {
        const int lane = lane_id();
        auto wait_cnt = [&](int off, unsigned v) __attribute__((always_inline)) {
#pragma unroll 1
            for (unsigned spins = 0; spins < (kSpinCap << 4); ++spins) { if (lds_word(lb + L_CNT + off) >= v) break; __builtin_amdgcn_s_sleep(1); }
        };
        if (ord == OML) {
            wait_cnt(16, 8u);
            unsigned xa, ra; ml_addr(lane, xa, ra);
            comb_ml(cma, xa, ra, WS_OWN + (unsigned)(gi * 16384 + gj * 512 + 384 + (lane & 1) * 8192), true);
            STAMP(24);
        } else if (ord == 0 && !lds_word(lb + L_CNT + 52)) {
            if (gi + gj == 0 && !lds_word(lb + L_CNT + 36)) {
                unsigned r = 0u;
#pragma unroll 1
                for (unsigned spins = 0; spins < kSpinCap; ++spins) {
                    r = ld4_sc1(ws, WS_R + (unsigned)((lane & 31) * 4));
                    WAIT1(r, 0);
                    if (all_set(r)) break;
                    if ((spins & 7u) == 7u) send_flag(lane);
                }
                answer(lane, r);
            }
#pragma unroll 1
            for (unsigned spins = 0; spins < kSpinCap; ++spins) {
                unsigned r = ld4_sc1(ws, WS_MB + (unsigned)((gi * 8 + (gi & 7)) * 128 + 32));
                WAIT1(r, 0);
                r = __builtin_amdgcn_readfirstlane(r);
                if (r) { adopt(r); break; }
                if ((spins & 7u) == 7u) { send_flag(lane); report(); }
                __builtin_amdgcn_s_sleep(16);
            }
            STAMP(29);
        } else if (ord == OML + 1) {
#pragma unroll 1
            for (unsigned spins = 0; spins < (kSpinCap << 6); ++spins) if (lds_word(lb + L_CNT + 24) >= 8u) break;
            const unsigned ra = lb + L_OP + (unsigned)((lane >> 5) * 4096 + (lane & 31) * 16);
            comb_o0(*(const LAS f32x4*)(size_t)cma, *(const LAS f32x4*)(size_t)(cma + 16), *(const LAS f32x4*)(size_t)(cma + (lane >> 5) * 16), *(const LAS f32x4*)(size_t)ra, *(const LAS f32x4*)(size_t)(ra + 1024),
                    *(const LAS f32x4*)(size_t)(ra + 2048), *(const LAS f32x4*)(size_t)(ra + 3072), WS_OWN + (unsigned)(gi * 16384 + gj * 512 + (lane & 60) * 4 + (lane & 1) * 8192), 0x3333333333333333ull, true);
            STAMP(22);
        }
        if (ord == OML) {
#pragma unroll 1
            for (unsigned spins = 0; spins < (kSpinCap << 8); ++spins) { if (lds_word(lb + L_CNT + 52)) break; __builtin_amdgcn_s_sleep(1); }
            const unsigned S = (lds_word(lb + L_CNT + 80) & 4u) << 11;
            empty4(0, S, lane); empty5(5, 0u, lane); empty5(5, 8192u, lane);
        }
#pragma unroll 1
        for (;;) {
            unsigned j;
            asm volatile("s_mov_b64 exec, 1\n\tds_add_rtn_u32 %0, %1, %2\n\ts_mov_b64 exec, -1\n\ts_waitcnt lgkmcnt(0)" : "=&v"(j) : "v"(lb + L_CNT + 40), "v"(1u) : "memory");
            j = __builtin_amdgcn_readfirstlane(j);
            if (j >= 5u - WEARLY) break;
            load_wo((int)j, lane, false); ++nj;
        }
        }
        STAMP(23);
    }

    unsigned omr, owa, owb;
    auto op_addr = [&](int layer) __attribute__((always_inline)) {
        LANE_VARS();
        const int t = wave - 3;
        cma = WS_OWN + rS + (unsigned)(gi * 16384 + layer * 4096 + (lane >> 3) * 512 + t * 128 + (t == 3 ? 0 : (lane & 7) * 16));
        cxa = lb + L_O2 + (unsigned)((lane >> 3) * 416 + t * 128 + (t == 3 ? 0 : (lane & 7) * 16));
        const int pb = (lane & 4) ? 3 - p4 : p4, jp = 2 * pb + ((lane >> 2) & 1);
        cso = tile35(gj, 10 * layer + t) + (unsigned)((kq & 1) * 8192 + gi * 128 + (kq >> 1) * 64 + p4 * 16);
        cra = lb + L_O2 + (unsigned)(jp * 416 + kq * 16); omr = lb + L_O2 + (unsigned)(jp * 416 + 384);
        owa = lb + L_WO + (unsigned)(r0s * 160 + 32 * t + 8 * p4); owb = lb + L_WO + (unsigned)(r1s * 160 + 32 * t + 8 * p4);
        if (layer) PIN2(cma, cxa);
        PIN2(cso, cra); PIN3(omr, owa, owb);
    };
    auto chain = [&](auto LC) __attribute__((always_inline)) {
        constexpr int layer = decltype(LC)::value;
        unsigned h4 = WS_H4 + (unsigned)((layer * 32 + gi) * 32768) + (layer ? fS : 0u);
        auto sides = [&]() __attribute__((always_inline)) {
            const unsigned m = lds_word(lb + L_CNT + 80);
            qS = (m & 2u) << 12; fS = (m & 4u) << 11; rS = (m & 8u) << 10; nS = lds_word(lb + L_CNT + 84); h4 += fS;
        };
        if (wave < 3) {
            LANE_VARS();
            if (layer == 0) {
                unsigned lo = WS_OWN + lds_word(lb + L_CNT + 84) + (unsigned)(gi * 16384 + (lane >> 3) * 512), lw = lb + L_O2 + (unsigned)((lane >> 3) * 416);
                unsigned lo0 = lo + (unsigned)((lane & 7) * 16), lw0 = lw + (unsigned)((lane & 7) * 16);
                PIN2(lo0, lo); PIN2(lw0, lw);
                GUARD(400);
                MARK(b_g2);
                if (__builtin_expect(wave == 2, 0)) { if (nj | (ord == OML)) { VMW(0); lds_add(44, nj); } }
                else if (__builtin_expect(wave == 1, 0)) {
#pragma unroll 1
                    for (unsigned spins = 0; spins < (kSpinCap << 4); ++spins) if (lds_word(lb + L_CNT + 48)) break;
#pragma unroll 1
                    for (unsigned spins = 0; spins < kSpinCap; ++spins) {
                        u32x4 o0 = ld16_sc1<0>(ws, lo0), o1 = ld16_sc1<128>(ws, lo0), o2 = ld16_sc1<384>(ws, lo);
                        if (nj) { VMW(3); lds_add(44, nj); nj = 0u; }
                        asm volatile("s_waitcnt vmcnt(0)" : "+v"(o0), "+v"(o1), "+v"(o2) :: "memory");
                        *(LAS u32x4*)(size_t)lw0 = o0; *(LAS u32x4*)(size_t)(lw0 + 128) = o1; *(LAS u32x4*)(size_t)(lw + 384) = o2;
                        if (!any_empty(max(max(um4(o0), um4(o1)), um4(o2)))) break;
                    }
                } else {
                    unsigned long long miss; unsigned cnt = kSpinCap << 4, tf, nv = nj;
                    asm volatile("1:\n\tds_read_b32 v4, %4 offset:48\n\ts_sub_u32 %1, %1, 1\n\ts_cbranch_scc1 5f\n\ts_waitcnt lgkmcnt(0)\n\tv_readfirstlane_b32 %2, v4\n\ts_cmp_eq_u32 %2, 0\n\ts_cbranch_scc1 1b\n\t"
                                 "s_add_u32 %2, %2, %8\n"
                                 "2:\n\ts_memrealtime vcc\n\ts_waitcnt lgkmcnt(0)\n\ts_sub_u32 vcc_lo, %2, vcc_lo\n\ts_cmp_lt_u32 vcc_lo, 0x400\n\ts_cbranch_scc1 2b\n"
                                 "5:\n\ts_mov_b32 %1, 15\n"
                                 "3:\n\tglobal_load_dwordx4 v[0:3], %5, %6 offset:256 sc1\n\ts_waitcnt vmcnt(1)\n\ts_mov_b64 exec, 1\n\tds_add_u32 %4, %3 offset:44\n\ts_mov_b64 exec, -1\n\tv_mov_b32 %3, 0\n\t"
                                 "s_sub_u32 %1, %1, 1\n\ts_cselect_b64 vcc, 0, -1\n\ts_waitcnt vmcnt(0)\n\tds_write_b128 %7, v[0:3] offset:256\n\tv_max3_u32 v4, v0, v1, v2\n\tv_max_u32 v4, v4, v3\n\t"
                                 "v_cmp_eq_u32 %0, -1, v4\n\ts_and_b64 vcc, vcc, %0\n\ts_cbranch_scc1 3b"
                                 : "=&s"(miss), "+s"(cnt), "=&s"(tf), "+v"(nv) : "v"(lb + L_CNT), "v"(lo0), "s"(ws), "v"(lw0), "i"(DG2 - 1) : "memory", "vcc", "scc", "v0", "v1", "v2", "v3", "v4");
                    if (__builtin_expect(miss != 0ull, 0)) {
#pragma unroll 1
                        for (unsigned spins = 0; spins < kSpinCap; ++spins) {
                            if ((spins & 15u) == 0u) send_flag(lane);
                            u32x4 o0 = ld16_sc1<256>(ws, lo0);
                            WAIT1(o0, 0);
                            *(LAS u32x4*)(size_t)(lw0 + 256) = o0;
                            if (!any_empty(um4(o0))) break;
                        }
                    }
                }
                MARK(e_g2);
                STAMP(25);
                BAR();
                sides();
                if (wave < 2) empty1(wave, qS, lane);
                else {
                    st16m(ws, WS_CS + (unsigned)((gi * 8 + gj) * 768 + lane * 16), *reinterpret_cast<const u32x4*>(smem + L_CS + lane * 16), 0xffffffffffffull);
                    empty5(10, 0u, lane); empty5(10, 8192u, lane);
                }
            } else BAR();
            {
                const unsigned im = lb + L_GU + (unsigned)(32 * wave + 8 * p4);
                Frag80 fg = frag80(im, 96, r0s, r1s, r2s), fu = frag80(im + 8192, 96, r0s, r1s, r2s);
                const unsigned xb = lb + L_XS + (unsigned)((n < 4 ? n : 4) * 160);
                unsigned b16 = xb + kq * 16, b8 = xb + 128 + kq * 8;
                unsigned sg = h4 + (unsigned)(n < 4 ? h4s(wave) + gj * 512 + kq * 64 + n * 16 : H4C + wave * 1024 + gj * 128 + kq * 16), su = sg + (n < 4 ? 256u : 64u);
                unsigned o = h4 + h4s(wave) + (unsigned)(lane * 16);
                unsigned oc = h4 + (unsigned)(H4C + wave * 1024 + lane * 16), pc = (unsigned)((kq * 16 + q4) * 4);
                unsigned os = h4 + (unsigned)(H4T + (q4 + 4 * (kq & 1)) * 128 + p4 * 16);
                unsigned ha = lb + L_H + (unsigned)(p4 * 96 + wave * 32 + q4 * 8 + kq * 2);
                f32x2 cs = {0.f, 0.f};
                if (layer != 0) cs = *reinterpret_cast<const f32x2*>(smem + L_CS2 + wave * 512 + lane * 8);
                PIN3(fg.a0, fg.a1, fg.a2); PIN3(fu.a0, fu.a1, fu.a2); PIN3(b16, b8, sg); PIN3(su, o, os); PIN2(ha, cs);
                unsigned long long mk = layer ? 0x000f000f000f000full : 0x001f001f001f001full;
                unsigned ta = WS_MB + (unsigned)((lane >> 1) * 1024 + 64 + (lane & 1) * 16);
                if (layer == 0) PIN3(oc, ta, pc);
                unsigned x4a = layer ? 0u : X4A * lds_word(lb + L_CNT + 20) >> 12;
                unsigned long long w0 = __builtin_amdgcn_ballot_w64(wave == 0);
                PINS(x4a); PINS(w0);
                unsigned long long rdm = 0x3acccccd3acccccdull;
                PINS(mk); PINS(rdm);
                if (layer == 0) VMW(0);
                if (layer == 0) GUARD(1200); else GUARD(800);
                MARK(b_gu);
                BAR();
                STAMP(10 + 32 * layer);
                u32x4 tl = {0u, 0u, 0u, 0u};
                if (layer == 0) asm volatile("s_mov_b64 exec, %3\n\tglobal_load_dwordx4 %0, %1, %2 sc1\n\ts_mov_b64 exec, -1" : "+v"(tl) : "v"(ta), "s"(ws), "s"(w0) : "memory");
                const Frag80 b = rows80(b16, b8);
                const f32x4 g0 = mfma32(fg.a0, b.a0, z4), u0 = mfma32(fu.a0, b.a0, z4), g1 = mfma32(fg.a1, b.a1, g0), g2 = mfma16(fg.a2, b.a2, z4), u1 = mfma32(fu.a1, b.a1, u0), u2 = mfma16(fu.a2, b.a2, z4);
                __builtin_amdgcn_sched_barrier(0);
                const f32x4 cg = g1 + g2, cu = u1 + u2;
                st16m2<0>(ws, sg, asu(cg), su, asu(cu), mk);
                STAMP(11 + 32 * layer);
                if (layer == 0 && wave != 0) {
                    unsigned tr = 0u;
#pragma unroll 1
                    for (unsigned spins = 0; spins < (kSpinCap << 6); ++spins) { tr = lds_word(lb + L_CNT + 56); if (tr) break; }
                    until(tr + x4a);
                } else if (layer == 0) {
                    WAIT1(tl, 2);
#pragma unroll 1
                    for (unsigned spins = 0; spins < kSpinCap; ++spins) {
                        const unsigned long long miss = __builtin_amdgcn_ballot_w64(min(min(tl[0], tl[1]), min(tl[2], tl[3])) == 0u);
                        if (!miss) break;
                        asm volatile("s_mov_b64 exec, %3\n\tglobal_load_dwordx4 %0, %1, %2 sc1\n\ts_mov_b64 exec, -1\n\ts_waitcnt vmcnt(0)" : "+v"(tl) : "v"(ta), "s"(ws), "s"(miss) : "memory");
                    }
                    const unsigned tn = now();
                    unsigned d = min(min(min(tn - tl[0], tn - tl[1]), min(tn - tl[2], tn - tl[3])), 800u);
                    d = min(d, dppu<0xB1>(d)); d = min(d, dppu<0x4E>(d)); d = min(d, dppu<0x124>(d)); d = min(d, dppu<0x128>(d));
                    unsigned tr = tn - min(min((unsigned)__builtin_amdgcn_readlane(d, 0), (unsigned)__builtin_amdgcn_readlane(d, 16)), min((unsigned)__builtin_amdgcn_readlane(d, 32), (unsigned)__builtin_amdgcn_readlane(d, 48)));
                    lds_set(lb + L_CNT + 56, tr | 1u);
                    until(__builtin_amdgcn_readfirstlane(tr | 1u) + x4a);
                } else sched(X4, 0);
                STAMP(16 + 32 * layer);
                float hv;
                u32x4 a0, a1, a2, a3, ac, as;
                auto load = [&]() __attribute__((always_inline)) {
                    as = ld16_sc1(ws, os);
                    if (layer == 0) ac = ld16_sc1(ws, oc);
                    a0 = ld16_sc1<0>(ws, o); a1 = ld16_sc1<1024>(ws, o); a2 = ld16_sc1<2048>(ws, o); a3 = ld16_sc1<3072>(ws, o);
                    if (layer == 0) lds_add(32, 1u);
                };
                auto all_in = [&]() __attribute__((always_inline)) {
                    if (layer == 0) asm volatile("s_waitcnt vmcnt(0)" : "+v"(a0), "+v"(a1), "+v"(a2), "+v"(a3), "+v"(ac), "+v"(as) :: "memory");
                    else { asm volatile("s_waitcnt vmcnt(0)" : "+v"(a0), "+v"(a1), "+v"(a2), "+v"(a3), "+v"(as) :: "memory"); ac = as; }
                };
                auto compute = [&]() __attribute__((always_inline)) {
#define ROR(n) " row_ror:" #n " row_mask:0xf bank_mask:0xf\n\t"
                    asm volatile("s_waitcnt vmcnt(%16)\n\tv_add_f32_dpp v40, %2, %2" ROR(8) "v_add_f32_dpp v41, %3, %3" ROR(8) "s_nop 0\n\tv_add_f32_dpp v40, v40, v40" ROR(4) "v_add_f32_dpp v41, v41, v41" ROR(4)
                                 "v_mov_b32 v42, v40\n\tv_mov_b32 v43, v41\n\ts_nop 0\n\tv_permlane16_swap_b32 v40, v42\n\tv_permlane16_swap_b32 v41, v43\n\t"
                                 "v_pk_add_f32 v[40:41], v[40:41], v[42:43]\n\tv_pk_mul_f32 v[40:41], v[40:41], %14\n\t"
                                 ".if %17 == 0\n\ts_waitcnt vmcnt(4)\n\tv_mov_b64 v[44:45], %12\n\tv_mov_b64 v[46:47], %13\n\t.endif\n\t"
                                 "v_fma_f32 v40, -v41, v41, v40\n\tv_cvt_pk_bf16_f32 v42, 0, v41\n\tv_maximum3_f32 v40, v40, 0, 0\n\t"
                                 ".if %17 == 0\n\tv_permlane32_swap_b32 v44, v46\n\tv_permlane32_swap_b32 v45, v47\n\tv_pk_add_f32 v[44:45], v[44:45], v[46:47]\n\t.endif\n\t"
                                 "v_cvt_pk_bf16_f32 v40, 0, v40\n\ts_waitcnt vmcnt(2)\n\tv_pk_add_f32 v[32:33], %4, %6\n\tv_add_f32 v40, 0x3727c5ac, v40\n\tv_pk_add_f32 v[34:35], %5, %7\n\t"
                                 ".if %17 == 0\n\tv_permlane16_swap_b32 v44, v45\n\tv_add_f32 v44, v44, v45\n\t.endif\n\t"
                                 "v_cvt_pk_bf16_f32 v40, 0, v40\n\ts_waitcnt vmcnt(0)\n\tv_pk_add_f32 v[36:37], %8, %10\n\tv_sqrt_f32 v40, v40\n\tv_pk_add_f32 v[38:39], %9, %11\n\t"
                                 ".if %17 == 0\n\tv_add_f32_dpp v44, v44, v44" ROR(8) "ds_bpermute_b32 v46, %19, v44\n\tds_bpermute_b32 v47, %19, v44 offset:16\n\t.endif\n\t"
                                 "v_pk_add_f32 v[32:33], v[32:33], v[36:37]\n\tv_pk_add_f32 v[34:35], v[34:35], v[38:39]\n\tv_cvt_pk_bf16_f32 v40, 0, v40\n\t"
                                 "s_nop 0\n\t"
                                 "v_permlane32_swap_b32 v32, v34\n\tv_permlane32_swap_b32 v33, v35\n\tv_rcp_f32 v40, v40\n\tv_pk_add_f32 v[32:33], v[32:33], v[34:35]\n\ts_nop 1\n\t"
                                 "v_permlane16_swap_b32 v32, v33\n\t.if %17 == 0\n\ts_waitcnt lgkmcnt(0)\n\tv_mov_b64 %1, v[46:47]\n\t.endif\n\t"
                                 "v_pk_fma_f32 v[32:33], %1, v[42:43], v[32:33] op_sel_hi:[1,0,1] neg_lo:[1,0,0] neg_hi:[1,0,0]\n\tv_pk_mul_f32 v[32:33], v[32:33], v[40:41] op_sel_hi:[1,0]\n\t"
                                 "v_cvt_pk_bf16_f32 v32, 0, v32\n\tv_mul_f32 v34, 0xbfb8aa3b, v32\n\tv_exp_f32 v34, v34\n\tv_cvt_pk_bf16_f32 v33, 0, v33\n\tv_add_f32 v34, 1.0, v34\n\t"
                                 "v_rcp_f32 v34, v34\n\ts_nop 0\n\tv_mul_f32 v32, v34, v32\n\tv_cvt_pk_bf16_f32 v32, 0, v32\n\tv_mul_f32 %0, v33, v32\n\tv_cvt_pk_bf16_f32 v32, %0, %0\n\tds_write_b16 %15, v32"
                                 : "=&v"(hv), "+v"(cs)
                                 : "v"(as[0]), "v"(as[1]), "v"(LO2(a0)), "v"(HI2(a0)), "v"(LO2(a1)), "v"(HI2(a1)), "v"(LO2(a2)), "v"(HI2(a2)), "v"(LO2(a3)), "v"(HI2(a3)),
                                   "v"(LO2(layer ? as : ac)), "v"(HI2(layer ? as : ac)), "s"(rdm), "v"(ha), "i"(layer ? 4 : 5), "i"(layer), "v"(as), "v"(layer ? ha : pc)
                                 : "memory", "v32", "v33", "v34", "v35", "v36", "v37", "v38", "v39", "v40", "v41", "v42", "v43", "v44", "v45", "v46", "v47");
#undef ROR
                };
                RETRY(load(), (all_in(), max(max(max(um4(a0), um4(a1)), max(um4(a2), um4(a3))), max(um4(ac), um4(as)))), compute(), any_nan(hv, hv), layer ? X4 : X4A);
                BAR();
                MARK(e_gu);
                STAMP(12 + 32 * layer);
                if (layer == 0) {
                    *reinterpret_cast<f32x2*>(smem + L_CS2 + wave * 512 + lane * 8) = cs;
                }
            }
            if (layer == 0) {
                if (wave == 1) {
                    const unsigned o = WS_CS + (unsigned)(gi * 6144 + 3072 + (lane < 48 ? lane : 0) * 16);
                    u32x4 a[8];
                    GATHER8(a, o, 768);
                    WAIT8(a, 0);
                    if (lane < 48) *reinterpret_cast<f32x4*>(smem + L_CS + lane * 16) = SUM8(a);
                }
                Frag80 f[4];
#pragma unroll
                for (int i = 0; i < 4; ++i) { f[i] = frag80(lb + L_WQ + (unsigned)(128 * wave + 32 * i + 8 * p4), 416, r0s, r1s, r2s); PIN3(f[i].a0, f[i].a1, f[i].a2); }
                unsigned b16 = lb + L_XS + (unsigned)(q4 * 160 + kq * 16), b8 = lb + L_XS + (unsigned)(q4 * 160 + 128 + kq * 8);
                unsigned so = WS_H1 + qS + (unsigned)((gi * 4 + q4) * 16384 + gj * 896 + 256 * wave + kq * 64 + p4 * 16);
                unsigned long long mk = 0x1111111111111111ull;
                PIN3(b16, b8, so); PINS(mk);
                GUARD(3800);
                MARK(b_q);
                BAR();
                STAMP(33);
                const Frag80 b = rows80(b16, b8);
                st16_sc1(ws, so, tiles4(f, b, mk));
                STAMP(34);
                qk_addr(true); att_addr();
                unsigned x0a = lb + L_M + (unsigned)((lane >> 5) * 16), x1a = lb + L_M + (unsigned)((lane >> 4) * 8), x2a, r2a;
                unsigned r0a = lb + L_OP + (unsigned)((lane >> 5) * 4096 + (lane & 31) * 16), r1a = lb + L_OP + (unsigned)(512 + (lane >> 4) * 2048 + (lane & 15) * 16);
                unsigned s0a = (unsigned)(lane * 4), s1a = (unsigned)(256 + (lane >> 5) * 64 + (lane & 15) * 4);
                ml_addr(lane, x2a, r2a);
                PIN3(x0a, x1a, x2a); PIN3(r0a, r1a, r2a); PIN2(s0a, s1a);
                cma = lb + L_M; cxa = wave == 0 ? x0a : wave == 1 ? x1a : x2a; cra = wave == 0 ? r0a : wave == 1 ? r1a : r2a;
                cso = WS_OWN + rS + (unsigned)(gi * 16384 + 4096 + gj * 512) + (wave == 0 ? s0a : wave == 1 ? s1a : 384u);
                PIN2(cma, cxa); PIN2(cra, cso);
                asm volatile("" : "=v"(omr), "=v"(owa), "=v"(owb));
            }
        } else {
            const int t = wave - 3;
            StatAddr sa;
            u32x4 sel;
            unsigned long long hi = 0xff00ff00ff00ff00ull;
            auto gather32 = [&](unsigned o, unsigned X, auto fin) __attribute__((always_inline)) {
                u32x4 a0 = ld16_sc1<-2048>(ws, o), a1 = ld16_sc1<-1024>(ws, o), a2 = ld16_sc1<0>(ws, o), a3 = ld16_sc1<1024>(ws, o);
                float v;
                auto sum = [&]() __attribute__((always_inline)) {
                    asm volatile("s_waitcnt vmcnt(3)\n\tv_mfma_f32_16x16x32_bf16 v[40:43], %1, %5, 0\n\ts_waitcnt vmcnt(2)\n\tv_mfma_f32_16x16x32_bf16 v[40:43], %2, %5, v[40:43]\n\t"
                                 "s_waitcnt vmcnt(1)\n\tv_mfma_f32_16x16x32_bf16 v[40:43], %3, %5, v[40:43]\n\ts_waitcnt vmcnt(0)\n\tv_mfma_f32_16x16x32_bf16 v[40:43], %4, %5, v[40:43]\n\t"
                                 "s_nop 7\n\tv_cndmask_b32 v40, v40, v41, %6\n\tv_cndmask_b32 v42, v42, v43, %6\n\ts_nop 1\n\tv_permlane32_swap_b32 v40, v42\n\tv_add_f32 v40, v40, v42\n\t"
                                 "v_cvt_pk_bf16_f32 %0, 0, v40"
                                 : "=&v"(v), "+v"(a0), "+v"(a1), "+v"(a2), "+v"(a3) : "v"(sel), "s"(hi) : "memory", "v40", "v41", "v42", "v43");
                    fin(v);
                    __builtin_amdgcn_sched_barrier(0);
                };
                sum();
                if (__builtin_expect(any_nan(v, v) != 0ull, 0)) {
#pragma unroll 1
                    for (unsigned spins = 0; spins < kSpinCap; ++spins) {
                        slip(X);
                        again16<-2048>(a0, ws, o); again16<-1024>(a1, ws, o); again16<0>(a2, ws, o); again16<1024>(a3, ws, o);
                        asm volatile("s_waitcnt vmcnt(0)" : "+v"(a0), "+v"(a1), "+v"(a2), "+v"(a3) :: "memory");
                        if (!any_empty(max(max(um4(a0), um4(a1)), max(um4(a2), um4(a3))))) break;
                    }
                    sum();
                }
            };
            auto poll32 = [&](unsigned o, unsigned t0) __attribute__((always_inline)) {
                    unsigned long long m0, m1, m2, m3; unsigned cnt = kSpinCap; float v;
#define CHK(r, m) "v_max3_u32 v44, v" #r ", v[" #r "+1], v[" #r "+2]\n\tv_max_u32 v44, v44, v[" #r "+3]\n\tv_cmp_eq_u32 %" #m ", -1, v44\n\t"
                    asm volatile("s_branch 3f\n"
                                 "1:\n\ts_mov_b64 exec, %1\n\tglobal_load_dwordx4 v[0:3], %6, %7 offset:-2048 sc1\n\ts_mov_b64 exec, %2\n\tglobal_load_dwordx4 v[4:7], %6, %7 offset:-1024 sc1\n\t"
                                 "s_mov_b64 exec, %3\n\tglobal_load_dwordx4 v[8:11], %6, %7 sc1\n\ts_mov_b64 exec, %4\n\tglobal_load_dwordx4 v[12:15], %6, %7 offset:1024 sc1\n\ts_mov_b64 exec, -1\n\t"
                                 "s_sub_u32 %5, %5, 1\n\ts_cselect_b64 vcc, 0, -1\n\ts_waitcnt vmcnt(0)\n\ts_branch 2f\n"
                                 "3:\n\ts_memrealtime vcc\n\ts_waitcnt lgkmcnt(0)\n\ts_sub_u32 vcc_lo, %10, vcc_lo\n\ts_cmp_lt_u32 vcc_lo, 0x400\n\ts_cbranch_scc1 3b\n\t"
                                 "global_load_dwordx4 v[0:3], %6, %7 offset:-2048 sc1\n\tglobal_load_dwordx4 v[4:7], %6, %7 offset:-1024 sc1\n\t"
                                 "global_load_dwordx4 v[8:11], %6, %7 sc1\n\tglobal_load_dwordx4 v[12:15], %6, %7 offset:1024 sc1\n\ts_mov_b64 vcc, -1\n"
                                 "2:\n\ts_waitcnt vmcnt(3)\n\tv_mfma_f32_16x16x32_bf16 v[40:43], v[0:3], %8, 0\n\t" CHK(0, 1) "s_waitcnt vmcnt(2)\n\tv_mfma_f32_16x16x32_bf16 v[40:43], v[4:7], %8, v[40:43]\n\t" CHK(4, 2)
                                 "s_waitcnt vmcnt(1)\n\tv_mfma_f32_16x16x32_bf16 v[40:43], v[8:11], %8, v[40:43]\n\t" CHK(8, 3) "s_waitcnt vmcnt(0)\n\tv_mfma_f32_16x16x32_bf16 v[40:43], v[12:15], %8, v[40:43]\n\t" CHK(12, 4)
                                 "s_or_b64 exec, %1, %2\n\ts_or_b64 exec, exec, %3\n\ts_or_b64 exec, exec, %4\n\ts_and_b64 vcc, exec, vcc\n\ts_cbranch_scc1 1b\n\t"
                                 "s_mov_b64 exec, -1\n\tv_cndmask_b32 v40, v40, v41, %9\n\tv_cndmask_b32 v42, v42, v43, %9\n\ts_nop 1\n\tv_permlane32_swap_b32 v40, v42\n\tv_add_f32 v40, v40, v42\n\t"
                                 "v_cvt_pk_bf16_f32 %0, 0, v40"
                                 : "=&v"(v), "=&s"(m0), "=&s"(m1), "=&s"(m2), "=&s"(m3), "+s"(cnt) : "v"(o), "s"(ws), "v"(sel), "s"(hi), "s"(t0 - 1u)
                                 : "memory", "vcc", "scc", "v0", "v1", "v2", "v3", "v4", "v5", "v6", "v7", "v8", "v9", "v10", "v11", "v12", "v13", "v14", "v15", "v40", "v41", "v42", "v43", "v44");
#undef CHK
                return v;
            };
#define PUT8(A, B, M) "v_min_u32 v34, -2, v32\n\tv_min_u32 v32, -2, v32\n\tv_min_u32 v35, -2, v33\n\tv_min_u32 v33, -2, v33\n\tv_permlane16_swap_b32 v32, v34\n\ts_nop 0\n\t" \
                      "v_permlane16_swap_b32 v33, v35\n\ts_mov_b64 exec, " M "\n\tglobal_store_dwordx4 " A ", v[32:35], " B " sc1\n\ts_mov_b64 exec, -1\n\ts_nop 0"
            unsigned xw, qo, qw, qc;
            __builtin_amdgcn_s_setprio(0);
            {
                STAMP(6 + 32 * layer);
                bf16x8 af[6];
                unsigned long long mk = 0x000f000f000f000full;
                if (layer == 0) {
                    const int lane = lane_id();
                    xe = lo16(*reinterpret_cast<const u16*>(smem + L_X0 + xrow(lane) * 1280 + gj * 160 + t * 32 + xcol(lane) * 2));
                    op_addr(0);
                    GUARD(1600);
                    MARK(b_op);
                    if (nj | (ord == OML)) { VMW(0); lds_add(44, nj); }
#pragma unroll 1
                    for (unsigned spins = 0; spins < (kSpinCap << 4); ++spins) if (lds_word(lb + L_CNT + 44) >= 5u) break;
                    STAMP(8);
                } else MARK(b_op);
                const unsigned lo = cma, lw = cxa, so = cso, br = cra, mr = omr, h3 = tile35(gj, 10 * layer + t);
                unsigned go, xs;
#pragma unroll
                for (int s = 0; s < 6; ++s) af[s] = cat8(tr64o(owa, s * 32 * 160), tr64o(owb, s * 32 * 160));
                PINS(mk); PINS(hi);
                if (layer != 0 && t < 4) {
                    sched(X2, 0);
                    STAMP(49);
                    u32x4 ov = ld16_sc1(ws, lo);
                    WAIT1(ov, 0);
                    *(LAS u32x4*)(size_t)lw = ov;
                    if (__builtin_expect(any_empty(um4(ov)), 0)) {
#pragma unroll 1
                        for (unsigned spins = 0; spins < kSpinCap; ++spins) {
                            slip(X2);
                            ov = ld16_sc1(ws, lo);
                            WAIT1(ov, 0);
                            *(LAS u32x4*)(size_t)lw = ov;
                            if (!any_empty(um4(ov))) break;
                        }
                    }
                }
                {
                    bf16x8 b0, b1, b2, b3, b4, b5;
#define QP " row_half_mirror row_mask:0xf bank_mask:0xf\n\t"
                    asm volatile("s_waitcnt lgkmcnt(0)\n\ts_barrier\n\t"
                                 "ds_read_b128 %0, %12\n\tds_read_b64 v[36:37], %13\n\tds_read_b128 %1, %12 offset:64\n\tds_read_b128 %2, %12 offset:128\n\tds_read_b128 %3, %12 offset:192\n\t"
                                 "ds_read_b128 %4, %12 offset:256\n\tds_read_b128 %5, %12 offset:320\n\t"
                                 "s_waitcnt lgkmcnt(5)\n\tv_mfma_f32_16x16x32_bf16 v[32:35], %6, %0, 0\n\tv_max_f32_dpp v38, v36, v36" QP "v_sub_f32 v38, v36, v38\n\t"
                                 "s_waitcnt lgkmcnt(4)\n\tv_mfma_f32_16x16x32_bf16 v[32:35], %7, %1, v[32:35]\n\tv_exp_f32 v38, v38\n\t"
                                 "s_waitcnt lgkmcnt(3)\n\tv_mfma_f32_16x16x32_bf16 v[32:35], %8, %2, v[32:35]\n\tv_mul_f32 v37, v37, v38\n\t"
                                 "s_waitcnt lgkmcnt(2)\n\tv_mfma_f32_16x16x32_bf16 v[32:35], %9, %3, v[32:35]\n\tv_add_f32_dpp v39, v37, v37" QP
                                 "s_waitcnt lgkmcnt(1)\n\tv_mfma_f32_16x16x32_bf16 v[32:35], %10, %4, v[32:35]\n\tv_rcp_f32 v39, v39\n\t"
                                 "s_waitcnt lgkmcnt(0)\n\tv_mfma_f32_16x16x32_bf16 v[32:35], %11, %5, v[32:35]\n\tv_mul_f32 v38, v38, v39\n\ts_nop 6\n\t"
                                 "v_pk_mul_f32 v[32:33], v[32:33], v[38:39] op_sel_hi:[1,0]\n\tv_pk_mul_f32 v[34:35], v[34:35], v[38:39] op_sel_hi:[1,0]\n\ts_nop 0\n\t"
                                 "v_add_f32_dpp v32, v32, v32" QP "v_add_f32_dpp v33, v33, v33" QP "v_add_f32_dpp v34, v34, v34" QP "v_add_f32_dpp v35, v35, v35" QP
                                 "v_cvt_pk_bf16_f32 v32, v32, v33\n\tv_cvt_pk_bf16_f32 v33, v34, v35\n\t" PUT8("%14", "%15", "%16")
                                 : "=&v"(b0), "=&v"(b1), "=&v"(b2), "=&v"(b3), "=&v"(b4), "=&v"(b5)
                                 : "v"(af[0]), "v"(af[1]), "v"(af[2]), "v"(af[3]), "v"(af[4]), "v"(af[5]), "v"(br), "v"(mr), "v"(so), "s"(ws), "s"(mk)
                                 : "memory", "v32", "v33", "v34", "v35", "v36", "v37", "v38", "v39");
#undef QP
                }
                if (layer == 0 && wave == 7) st4_sc1(ws, WS_MB + (unsigned)(gi * 1024 + 64 + gj * 4), now() | 1u);
                const unsigned tp = layer ? 0u : now();
                if (layer == 0) sides();
                STAMP(7 + 32 * layer);
                {
                    const int ln = lane_id();
#pragma unroll
                    for (int k = 0; k < 4; ++k) sel[k] = ((ln >> 1) & 3) == k ? (ln & 1) ? 0x3f800000u : 0x3f80u : 0u;
                    go = h3 + nS + (unsigned)(2048 + ln * 16); xs = lb + L_XS + (unsigned)(xrow(ln) * 160 + t * 32 + xcol(ln) * 2);
                    PIN3(sel, go, xs);
                    sa = stats_addr(ln, h4 + (unsigned)(H4T + gj * 128), 16);
                }
                if (layer != 0) sched(X3, 0);
                STAMP(18 + 32 * layer);
                if (layer != 0) gather32(go, X3, [&](float s) { xw = pk2(xe + s, 0.f); *(LAS u16*)(size_t)xs = (u16)xw; });
                else { xw = pk2(xe + poll32(go, tp + W3), 0.f); *(LAS u16*)(size_t)xs = (u16)xw; }
                xe = lo16(xw);
                STAMP(9 + 32 * layer);
            }
            BAR();
            if (wave == 3) stats(sa);
            MARK(e_op);
            STAMP(11 + 32 * layer);
            {
                LANE_VARS();
                const unsigned im = lb + L_WD + (unsigned)(32 * t + 8 * p4);
                bf16x8 wd0 = cat8(tr64(im + r0s * 160), tr64(im + r1s * 160)); bf16x4 wd1 = tr64(im + (32 + r2s) * 160);
                const unsigned h5 = tile35(gj, 10 * layer + 5 + t);
                unsigned h16 = lb + L_H + (unsigned)(p4 * 96 + kq * 16), h8 = lb + L_H + (unsigned)(p4 * 96 + 64 + kq * 8);
                unsigned so = h5 + (unsigned)((kq & 1) * 8192 + gi * 128 + (kq >> 1) * 64 + p4 * 16);
                unsigned long long mk = 0x000f000f000f000full;
                unsigned go = h5 + nS + (unsigned)(2048 + lane * 16), xs = (layer ? (unsigned)(xrow(lane) * 1280 + gj * 160) : lb + L_XS + (unsigned)(xrow(lane) * 160)) + (unsigned)(t * 32 + xcol(lane) * 2);
                unsigned sta = lb + L_ST + (unsigned)(xrow(lane) * 16);
                if (layer == 0 && wave < 6) {
                    sa = stats_addr(lane, WS_H1 + qS + (unsigned)(gi * 65536 + gj * 896 + 768), 16384);
                    asm volatile("" : "=v"(qo), "=v"(qw), "=v"(qc), "=v"(cma), "=v"(cxa), "=v"(cra), "=v"(cso), "=v"(omr), "=v"(owa), "=v"(owb));
                    asm volatile("" : "=v"(qa), "=v"(pw), "=v"(ma), "=v"(idx), "=v"(qm), "=v"(pr), "=v"(oa), "=v"(oc));
                } else if (layer == 0) {
                    op_addr(1); qk_addr(false); att_addr();
                    const int l5 = lane & 31, c = l5 < 24 ? 24 * (wave - 6) + l5 : 48;
                    qo = WS_H1 + qS + (unsigned)((gi * 4 + (gj >> 1)) * 16384 + 3584 + (lane >> 5) * 896 + c * 16);
                    qw = lb + L_Q + (unsigned)(l5 < 24 ? (c >> 4) * 128 + (c & 3) * 32 + (c & 12) + (lane >> 5) * 16 : lane < 26 ? 384 + (lane & 1) * 16 : 448);
                    qc = lb + L_CS + (unsigned)(c * 16 + (lane >> 5) * 8);
                    PIN3(qo, qw, qc);
                }
                PIN2(wd0, wd1); PIN3(h16, h8, so); PIN3(go, xs, sta); PINS(mk);
                GUARD(2000);
                MARK(b_dn);
                if (wave == 4) {
                    unsigned os = h4 + (unsigned)(H4T + (q4 + 4 * (kq & 1)) * 128 + p4 * 16);
                    float sw = *reinterpret_cast<const float*>(smem + L_CNT + 12);
                    PIN2(os, sw);
                    if (layer == 0) {
#pragma unroll 1
                        for (unsigned spins = 0; spins < (kSpinCap << 6); ++spins) if (lds_word(lb + L_CNT + 56)) break;
                    }
                    sched(layer ? X4 : X4A, 0);
                    u32x4 as;
#pragma unroll 1
                    for (unsigned spins = 0; spins < kSpinCap; ++spins) { as = ld16_sc1(ws, os); WAIT1(as, 0); if (!any_empty(um4(as))) break; }
                    float ss = __uint_as_float(as[0]), sm = __uint_as_float(as[1]), rd = __uint_as_float(as[2]);
                    ss += dpp<0x128>(ss); sm += dpp<0x128>(sm); rd += dpp<0x128>(rd);
                    ss += dpp<0x124>(ss); sm += dpp<0x124>(sm); rd += dpp<0x124>(rd);
                    ss = x16_add(ss); sm = x16_add(sm); rd = x16_add(rd);
                    if (LANES(0xfull)) *reinterpret_cast<f32x4*>(smem + L_ST + p4 * 16) = (f32x4){ss, sm, rd, sw};
                }
                if (layer == 0 && (wave == 3 || wave == 5)) {
                    unsigned ea = lane < 40 ? tile35(gj, 15 + (lane >> 3)) + (unsigned)(gi * 128 + (lane & 7) * 16) : WS_OWN + rS + (unsigned)(gi * 16384 + 4096 + gj * 512 + (lane - 40) * 16);
                    unsigned eb = ea + (lane < 40 ? 8192u : 256u);
                    PIN2(ea, eb);
#pragma unroll 1
                    for (unsigned spins = 0; spins < (kSpinCap << 6); ++spins) { if (lds_word(lb + L_CNT + 32) >= 6u) break; __builtin_amdgcn_s_sleep(1); }
                    if (wave == 5) empty4(1, fS, lane);
                    else { st16m(ws, ea, (u32x4){~0u, ~0u, ~0u, ~0u}, 0xffffffffffffffull); st16m(ws, eb, (u32x4){~0u, ~0u, ~0u, ~0u}, 0xffffffffffffffull); }
                }
                asm volatile("s_waitcnt lgkmcnt(0)\n\ts_barrier\n\tds_read_b128 v[36:39], %2\n\tds_read_b64 v[40:41], %3\n\t"
                             "s_waitcnt lgkmcnt(1)\n\tv_mfma_f32_16x16x32_bf16 v[32:35], %0, v[36:39], 0\n\ts_waitcnt lgkmcnt(0)\n\tv_mfma_f32_16x16x16_bf16 v[42:45], %1, v[40:41], 0\n\ts_nop 7\n\t"
                             "v_pk_add_f32 v[32:33], v[32:33], v[42:43]\n\tv_pk_add_f32 v[34:35], v[34:35], v[44:45]\n\t"
                             "v_cvt_pk_bf16_f32 v32, v32, v33\n\tv_cvt_pk_bf16_f32 v33, v34, v35\n\t" PUT8("%4", "%5", "%6")
                             :: "v"(wd0), "v"(wd1), "v"(h16), "v"(h8), "v"(so), "s"(ws), "s"(mk)
                             : "memory", "v32", "v33", "v34", "v35", "v36", "v37", "v38", "v39", "v40", "v41", "v42", "v43", "v44", "v45");
                STAMP(13 + 33 * layer);
                if (layer == 0 || __builtin_expect(gi == 20 + 2 * t + (gj & 1), 1)) {
                    const f32x4 sr = *(const LAS f32x4*)(size_t)sta;
                    const f32x2 st = ln_stats(sr[0], sr[1]);
                    float rs = bfr(frcp(1.f + ex2(bfr((sr[2] - st[0] * sr[3]) * st[1]) * -LOG2E))), n1 = bfr(bfr(xe - st[0]) * st[1]);
                    PIN2(rs, n1);
                    if (layer == 0) {
                        sched(X5A, 0);
                        STAMP(15);
                        gather32(go, X5A, [&](float y) { xw = pk2(xe + bfr(n1 + bfr(y * rs)), 0.f); *(LAS u16*)(size_t)xs = (u16)xw; });
                        xe = lo16(xw);
                        STAMP(14);
                    } else {
                        st2(out, xs, pk2(xe + bfr(n1 + bfr(poll32(go, when(X5B)) * rs)), 0.f));
                        asm volatile("s_endpgm");
                        MARK(e_dn);
                        STAMP(45);
                    }
                }
            }
            if (layer == 0) {
                BAR();
                if (wave == 3) { stats(sa); STAMP(34); }
                MARK(e_dn);
                if (wave == 3) __builtin_amdgcn_s_setprio(2);
                if (__builtin_expect(wave < 6, 0)) { op_addr(1); qk_addr(false); att_addr(); }
                if (wave >= 6) {
                    const unsigned o = qo;
                    f32x2 cs = -*(const LAS f32x2*)(size_t)qc;
                    float rn = 1.f / DM;
                    unsigned long long m0 = 1ull << 24, m1 = 1ull << 25;
                    PIN2(cs, rn); PINS(m0); PINS(m1);
                    MARK(b_qg);
                    sched(X1, 0);
                    STAMP(51);
                    u32x4 a0, a1, a2, a3; f32x2 u;
                    RETRY({ a0 = ld16_sc1<-3584>(ws, o); a1 = ld16_sc1<-1792>(ws, o); a2 = ld16_sc1<0>(ws, o); a3 = ld16_sc1<1792>(ws, o); },
                          ({ asm volatile("s_waitcnt vmcnt(0)" : "+v"(a0), "+v"(a1), "+v"(a2), "+v"(a3) :: "memory"); max(max(um4(a0), um4(a1)), max(um4(a2), um4(a3))); }),
                          { unsigned sm;
                            asm volatile("s_waitcnt vmcnt(2)\n\tv_pk_add_f32 v[40:41], %2, %4\n\tv_pk_add_f32 v[42:43], %3, %5\n\t"
                                         "s_waitcnt vmcnt(1)\n\tv_pk_add_f32 v[40:41], v[40:41], %6\n\tv_pk_add_f32 v[42:43], v[42:43], %7\n\t"
                                         "s_waitcnt vmcnt(0)\n\tv_pk_add_f32 v[40:41], v[40:41], %8\n\tv_pk_add_f32 v[42:43], v[42:43], %9\n\ts_nop 1\n\t"
                                         "v_permlane32_swap_b32 v40, v42\n\tv_permlane32_swap_b32 v41, v43\n\tv_pk_add_f32 v[40:41], v[40:41], v[42:43]\n\ts_nop 0\n\t"
                                         "v_readlane_b32 %1, v41, 24\n\ts_nop 1\n\tv_mul_f32 v42, %1, %11\n\tv_cvt_pk_bf16_f32 v42, 0, v42\n\tv_pk_fma_f32 %0, %10, v[42:43], v[40:41] op_sel_hi:[1,0,1]"
                                         : "=&v"(u), "=&s"(sm) : "v"(LO2(a0)), "v"(HI2(a0)), "v"(LO2(a1)), "v"(HI2(a1)), "v"(LO2(a2)), "v"(HI2(a2)), "v"(LO2(a3)), "v"(HI2(a3)), "v"(cs), "v"(rn)
                                         : "memory", "v40", "v41", "v42", "v43");
                            unsigned w = pk2v(u);
                            if (LANES(m0)) w = __float_as_uint(u[0]);
                            if (LANES(m1)) w = __float_as_uint(u[1]);
                            *(LAS unsigned*)(size_t)qw = w; __builtin_amdgcn_sched_barrier(0); }, any_nan(u[0], u[1]), X1);
                    MARK(e_qg);
                }
            }
        }
    };
    PINS(ck1); PINS(ck2); PINS(csc);
    chain(IC<0>{});
    STAMP(35);
    att1();
    chain(IC<1>{});
    if (g == 0 && wave == 0) { const unsigned x = 16u * (dga - tst); while ((int)(now() - tst - 4000u - x) < 0) __builtin_amdgcn_s_sleep(1); }
}

__device__ __attribute__((used, noinline)) void mega_code_end() { asm volatile(".Lmega_code_end:"); }

extern "C" void kernel_launch(void* const* d_in, const int* in_sizes, int n_in, void* d_out, int out_size, void* d_ws,
                              size_t ws_size, hipStream_t stream) {
    const u16* x0 = (const u16*)d_in[0];
    const u16* kc = (const u16*)d_in[1];
    const u16* vc = (const u16*)d_in[2];
    const u16* wq = (const u16*)d_in[3];
    const u16* wo = (const u16*)d_in[4];
    const u16* wr = (const u16*)d_in[6];
    const u16* wg = (const u16*)d_in[7];
    const u16* wu = (const u16*)d_in[8];
    const u16* wd = (const u16*)d_in[9];
    (void)hipFuncSetAttribute((const void*)mega_kernel, hipFuncAttributeMaxDynamicSharedMemorySize, L_END);
    mega_kernel<<<256, 512, L_END, stream>>>(x0, kc, vc, wq, wo, wr, wg, wu, wd, (u16*)d_out, (unsigned char*)d_ws
                                             );
}
```

```cpp
#include <hip/hip_runtime.h>
#include <hip/hip_bf16.h>
#include <stdint.h>

typedef unsigned short u16;
typedef unsigned u32x4 __attribute__((ext_vector_type(4)));
typedef unsigned u32x2 __attribute__((ext_vector_type(2)));
typedef unsigned long long u64x8 __attribute__((ext_vector_type(8)));
typedef unsigned long long u64x2 __attribute__((ext_vector_type(2)));
typedef float f32x4 __attribute__((ext_vector_type(4)));
typedef short s16x4 __attribute__((ext_vector_type(4)));
typedef __attribute__((ext_vector_type(4))) __bf16 bf16x4;
typedef __attribute__((ext_vector_type(8))) __bf16 bf16x8;
#define LAS __attribute__((address_space(3)))

constexpr int DM = 640, HQ = 32, SK = 4096, DH = 192, WIN = 1024, FF = 1536;
constexpr int QD = HQ * DH;
constexpr float SCALE = 0.07216878364870322f, LOG2E = 1.44269504088896341f;
constexpr unsigned kSpinCap = 1u << 16;
#define GJ(g) (((g) >> 3) & 7)
#define GI(g) (((g) & 7) * 4 + ((g) >> 6))

#define D4A 105
#define D5A 87
#define D1 84
#define D2 134
#define D3 68
#define D4 78
#define D5B 74
#define PACE 59
#define PINIT 100
#define PMIN 50
#define DM_ 0
constexpr unsigned X4A = D4A + DM_, X5A = X4A + D5A + DM_, X1 = X5A + D1 + DM_, X2 = X1 + D2 + DM_, X3 = X2 + D3 + DM_, X4 = X3 + D4 + DM_, X5B = X4 + D5B + DM_;
#define TLAND 132
#define XCOST 8
#define WOFF 6
#define CL 25
#define BETA 11
constexpr unsigned WOC = 2, WEARLY = 1;
constexpr unsigned NUNIT = 384 + XCOST + 3 * (9 + 2 * WEARLY), PBURST = 30;
constexpr int LEAD = 12, TOFF = 4;
constexpr unsigned DG2 = 12, W3 = 15;
constexpr int GP = 0, GA = 1, GB = 6, GC = 7, HD = 1;
#define VORD 0, 1, 8, 2, 3, 9, 4, 5, 6, 7, 10, 11
constexpr int kVord[12] = {VORD};
constexpr int v_upto(int h) { int n = 0; for (int i = 0; i <= h; ++i) n += kVord[i] < 8 ? 2 : 4; return n; }
constexpr int v_after(int p) { int h = 0; while (kVord[h] != p) ++h; return 32 - v_upto(h); }
constexpr int OML = 2;

constexpr unsigned WS_H1 = 0;
constexpr unsigned WS_H4 = 2u << 20;
constexpr unsigned H4C = 20480, H4T = H4C + 3072;
constexpr unsigned WS_H35 = 4u << 20;
constexpr unsigned WS_OWN = WS_H35 + 80 * 16384;
constexpr unsigned WS_CS = WS_OWN + 32 * 16384;
constexpr unsigned WS_MB = WS_CS + 256 * 768;
constexpr unsigned WS_R = WS_MB + 256 * 128;
constexpr unsigned WS_END = WS_R + 128;
constexpr unsigned WS_Q0 = 6u << 20;
static_assert(WS_END <= (6u << 20), "ws map");
__device__ __forceinline__ unsigned tile35(int j, int q) { return WS_H35 + (unsigned)((j * 10 + (q >> 1)) * 16384 + (q & 1) * 4096); }
__device__ __forceinline__ unsigned h4s(int s) { return (unsigned)((s >> 1) * 16384 + (s & 1) * 4096); }

constexpr int L_WQ = 0;
constexpr int L_WO = 33792;
constexpr int L_GU = L_WO + 30720;
constexpr int L_WD = L_GU + 16384;
constexpr int L_X0 = L_WD + 8192;
constexpr int L_WR = L_X0 + 5120;
constexpr int L_XS = L_WR + 2048;
constexpr int L_Q = L_XS + 1024;
constexpr int L_OP = L_Q + 512;
constexpr int L_M = L_OP + 8192;
constexpr int L_H = L_M + 64;
constexpr int L_ST = L_H + 384;
constexpr int L_CS = L_ST + 64;
constexpr int L_CS2 = L_CS + 1024;
constexpr int L_CNT = L_CS2 + 1536;
constexpr int L_P = L_CNT + 96;
constexpr int L_O2 = L_P + 72 * 192;
constexpr int L_DBG = L_O2 + 3328;
constexpr int L_END = L_DBG + 2048;
static_assert(L_END <= 163840 && L_END > 81920, "LDS");

typedef __attribute__((ext_vector_type(2))) __bf16 bf16x2;
typedef float f32x2 __attribute__((ext_vector_type(2)));
__device__ __forceinline__ unsigned pk2(float a, float b) { return __builtin_bit_cast(unsigned, __builtin_convertvector((f32x2){a, b}, bf16x2)); }
__device__ __forceinline__ unsigned fexp(float x) { const unsigned e = (__float_as_uint(x) >> 23) & 255u; return e == 255u ? 0u : e; }
__device__ __forceinline__ u32x2 enc21(float a, float b, float c) {
    const float lim = 1048575.f;
    const unsigned i = (unsigned)(int)__builtin_rintf(__builtin_amdgcn_fmed3f(a, -lim, lim)) & 0x1fffffu, j = (unsigned)(int)__builtin_rintf(__builtin_amdgcn_fmed3f(b, -lim, lim)) & 0x1fffffu;
    const unsigned k = (unsigned)(int)__builtin_rintf(__builtin_amdgcn_fmed3f(c, -lim, lim)) & 0x1fffffu;
    return (u32x2){i | j << 21, j >> 11 | k << 10 | (__builtin_fabsf(a) + __builtin_fabsf(b) + __builtin_fabsf(c) < 1e30f ? 0u : 0x80000000u)};
}
__device__ __forceinline__ float lo16(unsigned w) { return __uint_as_float(w << 16); }
__device__ __forceinline__ float hi16(unsigned w) { return __uint_as_float(w & 0xffff0000u); }
__device__ __forceinline__ float bfr(float f) { return __uint_as_float(pk2(0.f, f)); }
template <int CTRL> __device__ __forceinline__ float dpp(float x) {
    return __builtin_bit_cast(float, __builtin_amdgcn_mov_dpp(__builtin_bit_cast(int, x), CTRL, 0xf, 0xf, true));
}
template <int CTRL> __device__ __forceinline__ unsigned dppu(unsigned x) { return (unsigned)__builtin_amdgcn_mov_dpp((int)x, CTRL, 0xf, 0xf, true); }
__device__ __forceinline__ float row16_sum(float v) {
    v += dpp<0xB1>(v); v += dpp<0x4E>(v); v += dpp<0x141>(v); v += dpp<0x140>(v); return v;
}
__device__ __forceinline__ float fmax2(float a, float b) { return __builtin_elementwise_maximum(a, b); }
__device__ __forceinline__ float fmax3(float a, float b, float c) { return fmax2(fmax2(a, b), c); }
__device__ __forceinline__ float x16_add(float v) { auto s = __builtin_amdgcn_permlane16_swap(__float_as_uint(v), __float_as_uint(v), false, false); return __uint_as_float(s[0]) + __uint_as_float(s[1]); }
__device__ __forceinline__ float x32_add(float v) { auto s = __builtin_amdgcn_permlane32_swap(__float_as_uint(v), __float_as_uint(v), false, false); return __uint_as_float(s[0]) + __uint_as_float(s[1]); }
__device__ __forceinline__ float wave_sum(float v) { return x32_add(x16_add(row16_sum(v))); }
__device__ __forceinline__ float rs32(float a, float b) { auto s = __builtin_amdgcn_permlane32_swap(__float_as_uint(a), __float_as_uint(b), false, false); return __uint_as_float(s[0]) + __uint_as_float(s[1]); }

#define GUARD(n) asm volatile("s_branch 1f\n\t.p2align 12,," #n "\n1:")
#define MARK(n) asm volatile(".Lcrit_" #n "_%=:" ::)
#define BAR() asm volatile("s_waitcnt lgkmcnt(0)\n\ts_barrier" ::: "memory")
#define VMW(n) asm volatile("s_waitcnt vmcnt(%0)" :: "i"(n) : "memory")

template <int OFF = 0> __device__ __forceinline__ u32x4 ld16_sc1(const void* sbase, unsigned voff) {
    u32x4 r; asm volatile("global_load_dwordx4 %0, %1, %2 offset:%3 sc1" : "=v"(r) : "v"(voff), "s"(sbase), "i"(OFF) : "memory"); return r;
}
__device__ __forceinline__ unsigned ld4_sc1(const void* sbase, unsigned voff) {
    unsigned r; asm volatile("global_load_dword %0, %1, %2 sc1" : "=v"(r) : "v"(voff), "s"(sbase) : "memory"); return r;
}
template <int OFF = 0> __device__ __forceinline__ void st16_sc1(void* sbase, unsigned voff, u32x4 v) {
    asm volatile("global_store_dwordx4 %0, %1, %2 offset:%3 sc1\n\ts_nop 1" :: "v"(voff), "v"(v), "s"(sbase), "i"(OFF) : "memory");
}
__device__ __forceinline__ void st4_sc1(void* sbase, unsigned voff, unsigned v) {
    asm volatile("global_store_dword %0, %1, %2 sc1" :: "v"(voff), "v"(v), "s"(sbase) : "memory");
}
__device__ __forceinline__ void st8(void* sbase, unsigned voff, u32x2 v) {
    asm volatile("global_store_dwordx2 %0, %1, %2" :: "v"(voff), "v"(v), "s"(sbase) : "memory");
}
__device__ __forceinline__ void st2(void* sbase, unsigned voff, unsigned v) {
    asm volatile("global_store_short %0, %1, %2" :: "v"(voff), "v"(v), "s"(sbase) : "memory");
}
constexpr int RK = 64, RV = 160;
template <int R, int IMM> __device__ __forceinline__ void ld16_nt(const void* sbase, unsigned voff) {
    asm volatile("global_load_dwordx4 v[%c3:%c4], %0, %1 offset:%2 nt" :: "v"(voff), "s"(sbase), "i"(IMM), "i"(R), "i"(R + 3) : "memory");
}
template <int R, int IMM> __device__ __forceinline__ void ld8_nt(const void* sbase, unsigned voff) {
    asm volatile("global_load_dwordx2 v[%c3:%c4], %0, %1 offset:%2 nt" :: "v"(voff), "s"(sbase), "i"(IMM), "i"(R), "i"(R + 1) : "memory");
}
template <int R> __device__ __forceinline__ void smf(f32x4& d, u32x4 a, unsigned idx) {
    asm volatile("v_smfmac_f32_16x16x64_bf16 %0, %1, v[%c3:%c4], %2" : "+v"(d) : "v"(a), "v"(idx), "i"(R), "i"(R + 7));
}
template <int A, int B> __device__ __forceinline__ void zip_in() {
    unsigned t;
    asm volatile("v_perm_b32 %0, v%c2, v%c1, %3\n\tv_perm_b32 v%c2, v%c2, v%c1, %4\n\tv_mov_b32 v%c1, %0" : "=&v"(t) : "i"(A), "i"(B), "s"(0x05040100u), "s"(0x07060302u));
}
template <int A, int B> __device__ __forceinline__ void zip_out(unsigned& lo, unsigned& hi) {
    asm volatile("v_perm_b32 %0, v%c3, v%c2, %4\n\tv_perm_b32 %1, v%c3, v%c2, %5" : "=&v"(lo), "=&v"(hi) : "i"(A), "i"(B), "s"(0x05040100u), "s"(0x07060302u));
}
template <int A, int B, int C, int D> __device__ __forceinline__ void zip_x(unsigned lo, unsigned hi) {
    asm volatile("v_perm_b32 v%c4, v%c3, v%c2, %6\n\tv_perm_b32 v%c5, v%c3, v%c2, %7\n\tv_mov_b32 v%c2, %0\n\tv_mov_b32 v%c3, %1"
                 :: "v"(lo), "v"(hi), "i"(A), "i"(B), "i"(C), "i"(D), "s"(0x05040100u), "s"(0x07060302u));
}
__device__ __forceinline__ void dma16(const void* sbase, unsigned voff, unsigned lds_dst) {
    unsigned keep;
    asm volatile("s_mov_b32 %0, m0\n\ts_mov_b32 m0, %3\n\ts_nop 0\n\tglobal_load_lds_dwordx4 %1, %2\n\ts_mov_b32 m0, %0"
                 : "=&s"(keep) : "v"(voff), "s"(sbase), "s"(lds_dst) : "memory");
}
__device__ __forceinline__ unsigned um4(u32x4 a) { return max(max(a[0], a[1]), max(a[2], a[3])); }
__device__ __forceinline__ bool all_set(unsigned t) { return __builtin_amdgcn_ballot_w64(t == 0u) == 0ull; }
__device__ __forceinline__ bool any_empty(unsigned mx) { return __builtin_amdgcn_ballot_w64(mx == ~0u) != 0ull; }
__device__ __forceinline__ unsigned long long any_nan(float a, float b) { return __builtin_amdgcn_ballot_w64(__builtin_isunordered(a, b)); }
__device__ __forceinline__ f32x4 asf(u32x4 a) { return __builtin_bit_cast(f32x4, a); }
__device__ __forceinline__ u32x4 asu(f32x4 a) { return __builtin_bit_cast(u32x4, a); }
#define LANES(m) __builtin_amdgcn_inverse_ballot_w64(m)

__device__ __forceinline__ bf16x4 tr64(unsigned lds_addr) { return __builtin_amdgcn_ds_read_tr16_b64_v4bf16((LAS bf16x4*)(size_t)lds_addr); }
__device__ __forceinline__ bf16x4 tr64o(unsigned lds_addr, int off) { return __builtin_amdgcn_ds_read_tr16_b64_v4bf16((LAS bf16x4*)((LAS char*)(size_t)lds_addr + off)); }
__device__ __forceinline__ bf16x8 cat8(bf16x4 a, bf16x4 b) { return __builtin_shufflevector(a, b, 0, 1, 2, 3, 4, 5, 6, 7); }
__device__ __forceinline__ bf16x8 lds16(unsigned a) { return *(const LAS bf16x8*)(size_t)a; }
__device__ __forceinline__ bf16x4 lds8(unsigned a) { return *(const LAS bf16x4*)(size_t)a; }
__device__ __forceinline__ f32x4 mfma32(bf16x8 a, bf16x8 b, f32x4 c) { return __builtin_amdgcn_mfma_f32_16x16x32_bf16(a, b, c, 0, 0, 0); }
__device__ __forceinline__ f32x4 mfma32u(u32x4 a, u32x4 b, f32x4 c) { return mfma32(__builtin_bit_cast(bf16x8, a), __builtin_bit_cast(bf16x8, b), c); }
__device__ __forceinline__ f32x4 mfma16(bf16x4 a, bf16x4 b, f32x4 c) {
    return __builtin_amdgcn_mfma_f32_16x16x16bf16_1k(__builtin_bit_cast(s16x4, a), __builtin_bit_cast(s16x4, b), c, 0, 0, 0);
}

#define STAMP(i) do {} while (0)

template <int OFF = 0> __device__ __forceinline__ void st16m(void* sbase, unsigned voff, u32x4 v, unsigned long long mask) {
    asm volatile("s_mov_b64 exec, %4\n\tglobal_store_dwordx4 %0, %1, %2 offset:%3 sc1\n\ts_mov_b64 exec, -1\n\ts_nop 0" :: "v"(voff), "v"(v), "s"(sbase), "i"(OFF), "s"(mask) : "memory");
}
template <int OFF> __device__ __forceinline__ void st16m2(void* sbase, unsigned voff, u32x4 v, unsigned voff2, u32x4 v2, unsigned long long mask) {
    asm volatile("s_mov_b64 exec, %6\n\tglobal_store_dwordx4 %0, %1, %2 sc1\n\tglobal_store_dwordx4 %4, %5, %2 offset:%3 sc1\n\ts_mov_b64 exec, -1\n\ts_nop 0"
                 :: "v"(voff), "v"(v), "s"(sbase), "i"(OFF), "v"(voff2), "v"(v2), "s"(mask) : "memory");
}
#define PINS(a) asm volatile("" : "+s"(a))
#define RETRY(LOAD, EMPTY, COMPUTE, NAN_, X) do { LOAD; COMPUTE; if (__builtin_expect((NAN_) != 0ull, 0)) { \
    _Pragma("unroll 1") for (unsigned spins_ = 0; spins_ < kSpinCap; ++spins_) { slip(X); LOAD; if (!any_empty(EMPTY)) break; } COMPUTE; } } while (0)
__device__ __forceinline__ unsigned now() { return (unsigned)__builtin_amdgcn_s_memrealtime(); }
__device__ __forceinline__ void until(unsigned t) {
    asm volatile("1:\n\ts_memrealtime vcc\n\ts_waitcnt lgkmcnt(0)\n\ts_sub_u32 vcc_lo, %0, vcc_lo\n\ts_cmp_lt_u32 vcc_lo, 0x400\n\ts_cbranch_scc1 1b" :: "s"(t - 1u) : "vcc", "scc", "memory");
}
__device__ __forceinline__ unsigned lds_word(unsigned a) {
    unsigned t_; asm volatile("ds_read_b32 %0, %1\n\ts_waitcnt lgkmcnt(0)\n\ts_nop 0" : "=v"(t_) : "v"(a) : "memory");
    return __builtin_amdgcn_readfirstlane(t_);
}
__device__ __forceinline__ void lds_set(unsigned a, unsigned v) { asm volatile("ds_write_b32 %0, %1" :: "v"(a), "v"(v) : "memory"); }
__device__ __forceinline__ int xrow(int lane) { return (lane >> 5) * 2 + ((lane >> 3) & 1); }
__device__ __forceinline__ int xcol(int lane) { return ((lane >> 4) & 1) * 8 + (lane & 7); }
__device__ __forceinline__ int swz(int r) { return r ^ (((r >> 3) & 1) << 2); }
__device__ __forceinline__ f32x2 unpk(unsigned w) { return (f32x2){lo16(w), hi16(w)}; }
__device__ __forceinline__ unsigned pk2v(f32x2 v) { return __builtin_bit_cast(unsigned, __builtin_convertvector(v, bf16x2)); }
__device__ __forceinline__ float frcp(float x) { return __builtin_amdgcn_rcpf(x); }
__device__ __forceinline__ float ex2(float x) { return __builtin_amdgcn_exp2f(x); }
__device__ __forceinline__ int lane_id() { int l; asm volatile("v_mbcnt_lo_u32_b32 %0, -1, 0\n\tv_mbcnt_hi_u32_b32 %0, -1, %0" : "=v"(l)); return l & 63; }
__device__ __forceinline__ f32x2 ln_stats(float ss, float s) {
    const float mean = s * (1.f / DM);
    const float var = fmax2(__builtin_fmaf(-mean, mean, ss * (1.f / DM)), 0.f);
    return (f32x2){bfr(mean), frcp(bfr(__builtin_amdgcn_sqrtf(bfr(bfr(var) + 1e-5f))))};
}
#define GATHER8(a, o, S) do { a[0] = ld16_sc1<-4 * (S)>(ws, o); a[1] = ld16_sc1<-3 * (S)>(ws, o); a[2] = ld16_sc1<-2 * (S)>(ws, o); a[3] = ld16_sc1<-(S)>(ws, o); \
    a[4] = ld16_sc1<0>(ws, o); a[5] = ld16_sc1<(S)>(ws, o); a[6] = ld16_sc1<2 * (S)>(ws, o); a[7] = ld16_sc1<3 * (S)>(ws, o); } while (0)
template <int OFF> __device__ __forceinline__ void again16(u32x4& r, const void* sbase, unsigned voff) {
    asm volatile("v_cmp_eq_u32 vcc, -1, %1\n\ts_mov_b64 exec, vcc\n\tglobal_load_dwordx4 %0, %2, %3 offset:%4 sc1\n\ts_mov_b64 exec, -1" : "+v"(r) : "v"(um4(r)), "v"(voff), "s"(sbase), "i"(OFF) : "memory", "vcc");
}
#define WAIT8(a, N) asm volatile("s_waitcnt vmcnt(%8)" : "+v"((a)[0]), "+v"((a)[1]), "+v"((a)[2]), "+v"((a)[3]), "+v"((a)[4]), "+v"((a)[5]), "+v"((a)[6]), "+v"((a)[7]) : "i"(N) : "memory")
#define SUM8(a) (((((((asf(a[0]) + asf(a[1])) + asf(a[2])) + asf(a[3])) + asf(a[4])) + asf(a[5])) + asf(a[6])) + asf(a[7]))
#define WAIT1(x, N) asm volatile("s_waitcnt vmcnt(%1)" : "+v"(x) : "i"(N) : "memory")
#define LO2(x) __builtin_shufflevector(x, x, 0, 1)
#define HI2(x) __builtin_shufflevector(x, x, 2, 3)
#define PIN1(a) asm volatile("" : "+v"(a))
#define PIN2(a, b) asm volatile("" : "+v"(a), "+v"(b))
#define PIN3(a, b, c) asm volatile("" : "+v"(a), "+v"(b), "+v"(c))
#define LANE_VARS() const int lane = lane_id(); \
    const int kq = lane >> 4, n = lane & 15, q4 = n >> 2, p4 = lane & 3; \
    const int r0s = 8 * kq + q4 + 4 * (kq & 1), r1s = 8 * kq + q4 + 4 * ((kq & 1) ^ 1);     \
    const int r2s = swz(4 * kq + q4);                                                       \
    (void)r0s; (void)r1s; (void)r2s; (void)p4
template <int V> struct IC { static constexpr int value = V; };
struct Frag80 { bf16x8 a0, a1; bf16x4 a2; };

__global__ void __launch_bounds__(512, 2) __attribute__((amdgpu_num_vgpr(32)))
mega_kernel(const u16*, const u16*, const u16*, const u16*, const u16*, const u16*, const u16*, const u16*, const u16*, u16*, unsigned char*
            ) {
    extern __shared__ __attribute__((aligned(16))) unsigned char smem[];
    const unsigned lb = (unsigned)(size_t)smem;
    const int wave = __builtin_amdgcn_readfirstlane(threadIdx.x >> 6);
    u64x8 ar0; u64x2 ar1; unsigned long long ar2, tp, tst64;
    asm volatile("s_load_dwordx16 %0, %5, 0\n\ts_load_dwordx4 %1, %5, 0x40\n\ts_load_dwordx2 %2, %5, 0x50\n\ts_load_dwordx2 %3, %5, %6\n\ts_memrealtime %4"
                 : "=&s"(ar0), "=&s"(ar1), "=&s"(ar2), "=&s"(tp), "=&s"(tst64) : "s"(__builtin_amdgcn_kernarg_segment_ptr()), "s"((0xa9876421u >> (4 * wave) & 15u) * 8u) : "memory");
    const int g = blockIdx.x, gj = GJ(g), gi = GI(g);
    const f32x4 z4 = {0.f, 0.f, 0.f, 0.f};
    asm volatile("" ::: "v255");
    auto when = [&](unsigned X) __attribute__((always_inline)) {
        u32x2 v;
        asm volatile("ds_read2_b32 %0, %1 offset0:14 offset1:5\n\ts_waitcnt lgkmcnt(0)" : "=v"(v) : "v"(lb + L_CNT) : "memory");
        return __builtin_amdgcn_readfirstlane(v[0]) + (X * __builtin_amdgcn_readfirstlane(v[1]) >> 12);
    };
    auto sched = [&](unsigned X, unsigned add) __attribute__((always_inline)) { until(when(X) + add); };
    auto slip = [&](unsigned X) __attribute__((always_inline)) {
        if (X) lds_set(lb + L_CNT + 56, now() - (X * lds_word(lb + L_CNT + 20) >> 12));
    };
    float xe = 0.f;

    auto code_pf = [&](int slice, int lane) __attribute__((always_inline)) {
        unsigned long long pc0; unsigned len, back, t;
        asm volatile("s_getpc_b64 %0\n.Lmega_pf%=:\n\ts_mov_b32 %1, .Lmega_code_end-.Lmega_code_begin\n\ts_mov_b32 %2, .Lmega_pf%=-.Lmega_code_begin" : "=s"(pc0), "=s"(len), "=s"(back));
        unsigned off = (unsigned)(slice * 4096 + lane * 64); if (off > len) off = len;
        asm volatile("global_load_dword %0, %1, %2" : "=v"(t) : "v"(off & ~3u), "s"(pc0 - back) : "memory");
        return t;
    };
    asm volatile(".Lmega_code_begin:");
    unsigned pf = 0u, pt1;
    const unsigned wofs = (unsigned)((((gj >> 1) * HQ + gi) * SK + (SK - WIN) + (gj & 1) * 512) * (DH * 2));
    {
        const int lane = threadIdx.x & 63, hi = lane >= 26;
        if (threadIdx.x < 24) lds_set(lb + L_CNT + threadIdx.x * 4, 0u);
        unsigned t2 = (unsigned)(gi * (DH * DM * 2)), t7 = (32u << 20) + (unsigned)(g * 128), to = (unsigned)(gj * 128), w2 = wofs;
        PINS(t2); PINS(t7); PINS(to); PINS(w2);
        to = wave == 7 ? t7 : to; to = wave == 2 ? t2 : to; to = wave < 2 ? w2 : to;
        if (wave == 7) pf = code_pf((g >> 3) & 15, lane);
        unsigned vo = (unsigned)((gj * 80 + swz(2 * wave) + hi) * (QD * 2) + gi * (DH * 2) + (lane - 26 * hi) * 16), d = lb + L_WQ + wave * 832, keep;
        unsigned xo = (0x11043210u >> (4 * wave) & 15u) * 1024 + (unsigned)(lane & (wave < 6 ? 63 : 15)) * 16, xd = lb + L_X0 + min(wave, 6) * 1024;
        PIN2(vo, xo); PINS(d); PINS(xd); PINS(to);
        __builtin_amdgcn_sched_barrier(0);
        asm volatile("s_waitcnt lgkmcnt(0)\n\ts_load_dword %0, %4, %6" : "=&s"(pt1), "+s"(ar0), "+s"(ar1), "+s"(ar2), "+s"(tp), "+s"(tst64) : "s"(to) : "memory");
        dma16((const void*)(wave < 5 ? ar0[0] : ar0[5]), xo, xd);
        asm volatile("s_mov_b32 %0, m0\n\ts_mov_b32 m0, %2\n\ts_mov_b64 exec, %4\n\tglobal_load_lds_dwordx4 %1, %3 nt\n\t"
                     ".rept 4\n\ts_add_u32 m0, m0, 6656\n\tv_add_u32 %1, 0x30000, %1\n\tglobal_load_lds_dwordx4 %1, %3 nt\n\t.endr\n\t"
                     "s_mov_b64 exec, -1\n\ts_mov_b32 m0, %0\n\ts_barrier"
                     : "=&s"(keep), "+v"(vo) : "s"(d), "s"(ar0[3]), "s"(0x0003fffffcffffffull) : "memory", "scc");
        __builtin_amdgcn_sched_barrier(0);
    }
    const u16 *const kc = (const u16*)ar0[1], *const vc = (const u16*)ar0[2], *const wo = (const u16*)ar0[4], *const wg = (const u16*)ar0[6], *const wu = (const u16*)ar0[7], *const wd = (const u16*)ar1[0];
    u16* const out = (u16*)ar1[1];
    unsigned char* const ws = (unsigned char*)((ar2 + (32u << 20) - 1) & ~(unsigned long long)((16u << 20) - 1));
    const unsigned tst = (unsigned)tst64;
    auto fix_k1 = [&](auto IC_, auto NC) __attribute__((always_inline)) {
        constexpr int A = RK + 8 * decltype(IC_)::value, B = A + 4;
        unsigned t0, t1, t2, t3;
        asm volatile("s_waitcnt vmcnt(%12)\n\tv_mov_b32 %0, v%c4\n\tv_mov_b32 %1, v%c5\n\tv_mov_b32 %2, v%c6\n\tv_mov_b32 %3, v%c7\n\t"
                     "v_mov_b32_dpp v%c4, v%c8 row_ror:8 row_mask:0xf bank_mask:0xc\n\tv_mov_b32_dpp v%c5, v%c9 row_ror:8 row_mask:0xf bank_mask:0xc\n\t"
                     "v_mov_b32_dpp v%c6, v%c10 row_ror:8 row_mask:0xf bank_mask:0xc\n\tv_mov_b32_dpp v%c7, v%c11 row_ror:8 row_mask:0xf bank_mask:0xc\n\t"
                     "v_mov_b32_dpp v%c8, %0 row_ror:8 row_mask:0xf bank_mask:0x3\n\tv_mov_b32_dpp v%c9, %1 row_ror:8 row_mask:0xf bank_mask:0x3\n\t"
                     "v_mov_b32_dpp v%c10, %2 row_ror:8 row_mask:0xf bank_mask:0x3\n\tv_mov_b32_dpp v%c11, %3 row_ror:8 row_mask:0xf bank_mask:0x3"
                     : "=&v"(t0), "=&v"(t1), "=&v"(t2), "=&v"(t3)
                     : "i"(A), "i"(A + 1), "i"(A + 2), "i"(A + 3), "i"(B), "i"(B + 1), "i"(B + 2), "i"(B + 3), "i"(decltype(NC)::value) : "memory");
    };
    auto trans_v = [&](auto UC) __attribute__((always_inline)) {
        constexpr int R = RV + 4 * decltype(UC)::value, P = 4 * decltype(UC)::value;
#define CELL(r, c) R + 16 * (r) + (c), R + 16 * (r) + 8 + (c)
        unsigned a0, a1, b0, b1, c0, c1;
        VMW(v_after(P)); zip_in<CELL(0, 0)>(); zip_out<CELL(0, 1)>(a0, a1); zip_out<CELL(0, 2)>(b0, b1); zip_out<CELL(0, 3)>(c0, c1);
        VMW(v_after(P + 1)); zip_x<CELL(1, 0), CELL(0, 1)>(a0, a1); zip_in<CELL(1, 1)>(); zip_out<CELL(1, 2)>(a0, a1);
        VMW(v_after(P + 2)); zip_x<CELL(2, 0), CELL(0, 2)>(b0, b1); zip_x<CELL(2, 1), CELL(1, 2)>(a0, a1); zip_in<CELL(2, 2)>(); zip_out<CELL(1, 3)>(a0, a1); zip_out<CELL(2, 3)>(b0, b1);
        VMW(v_after(P + 3)); zip_x<CELL(3, 0), CELL(0, 3)>(c0, c1); zip_x<CELL(3, 1), CELL(1, 3)>(a0, a1); zip_x<CELL(3, 2), CELL(2, 3)>(b0, b1); zip_in<CELL(3, 3)>();
#undef CELL
    };
    auto trans_w = [&](auto UC) __attribute__((always_inline)) {
        constexpr int R = RV + 64 + 2 * decltype(UC)::value, N = v_after(8 + decltype(UC)::value);
        unsigned a0, a1;
        VMW(N + 2); zip_in<R, R + 8>(); zip_out<R + 1, R + 9>(a0, a1);
        VMW(N); zip_x<R + 16, R + 24, R + 1, R + 9>(a0, a1); zip_in<R + 17, R + 25>();
    };
    auto frag80 = [&](unsigned im, int rs, int r0s, int r1s, int r2s) __attribute__((always_inline)) -> Frag80 {
        return Frag80{cat8(tr64(im + r0s * rs), tr64(im + r1s * rs)), cat8(tr64(im + (r0s + 32) * rs), tr64(im + (r1s + 32) * rs)), tr64(im + (r2s + 64) * rs)};
    };
    auto rows80 = [&](unsigned a16, unsigned a8) __attribute__((always_inline)) -> Frag80 { const Frag80 f{lds16(a16), lds16(a16 + 64), lds8(a8)}; __builtin_amdgcn_sched_barrier(0); return f; };
    auto empty5 = [&](int q0, unsigned S, int lane) __attribute__((always_inline)) {
        st16m(ws, tile35(gj, q0 + (lane >> 3)) + S + (unsigned)(gi * 128 + (lane & 7) * 16), (u32x4){~0u, ~0u, ~0u, ~0u}, 0xffffffffffull);
    };
    auto empty4 = [&](int layer, unsigned S, int lane) __attribute__((always_inline)) {
        const unsigned b = WS_H4 + (unsigned)((layer * 32 + gi) * 32768) + S;
        st16_sc1(ws, b + (unsigned)((lane >> 5) * 4096 + gj * 512 + (lane & 31) * 16), (u32x4){~0u, ~0u, ~0u, ~0u});
        st16m(ws, b + (unsigned)(lane < 32 ? 16384 + gj * 512 + lane * 16 : H4C + ((lane - 32) >> 3) * 1024 + gj * 128 + (lane & 7) * 16), (u32x4){~0u, ~0u, ~0u, ~0u},
              layer ? 0xff000000ffffffffull : ~0ull);
    };
    int lead = (int)((0x00003411u | LEAD << 28) >> (4 * wave) & 15u) * PACE * 4;
    auto pace = [&](unsigned n, bool wait = true) __attribute__((always_inline)) {
        unsigned u, w, t0;
        asm volatile("s_mov_b64 exec, 1\n\tds_add_rtn_u32 %0, %3, %4\n\tds_max_u32 %3, %5 offset:12\n\tds_add_rtn_u32 %1, %3, %6 offset:12\n\tds_read_b32 %2, %3 offset:4\n\ts_mov_b64 exec, -1\n\ts_waitcnt lgkmcnt(0)"
                     : "=&v"(u), "=&v"(w), "=&v"(t0) : "v"(lb + L_CNT + 60), "v"(n), "v"((now() - tst) << 6), "v"(n * PMIN * 4u) : "memory");
        int tn = (int)(__builtin_amdgcn_readfirstlane(t0) - tst + PINIT) * 64 + __builtin_amdgcn_readfirstlane(u) * (PACE * 4);
        const int tw = __builtin_amdgcn_readfirstlane(w) - PBURST * 64;
        if (tw > tn) tn = tw;
        tn -= lead;
        if (wait) {
#pragma unroll 1
            for (int k = 0; k < 2048 && (int)((now() - tst) << 6) < tn; ++k) {}
        }
    };
    auto slot = [&](unsigned u) __attribute__((always_inline)) {
        const unsigned tn = lds_word(lb + L_CNT + 64) + PINIT + u * PACE / 16;
#pragma unroll 1
        for (int k = 0; k < 8192 && (int)(now() - tn) < 0; ++k) __builtin_amdgcn_s_sleep(1);
    };
    auto load_wo = [&](int k, int lane, bool early) __attribute__((always_inline)) {
        const int c = k * 64 + lane, pp = c / 10, cc = c - 10 * pp;
        const unsigned f = (unsigned)(swz(pp) * 1280 + cc * 16);
        const u16* wb = wo + (size_t)gi * DH * DM + gj * 80;
#pragma unroll
        for (int a2 = 0; a2 < 6; ++a2) { if (a2 % 3 == 0) { if (early) pace(3); else slot(NUNIT - WOFF + (2 * k + a2 / 3) * WOC); } dma16(wb + 32 * a2 * DM, f, lb + L_WO + (5 * a2 + k) * 1024); }
    };
    auto load_wd = [&](int lane) __attribute__((always_inline)) {
        const u16* db = wd + gi * 48 * DM + gj * 80;
#pragma unroll 1
        for (int k = 0; k < 5; ++k) {
            const int c = k * 64 + lane, pp = c / 10, cc = c - 10 * pp;
            const unsigned fd = (unsigned)(swz(pp) * 1280 + cc * 16);
            if (!(k & 1)) pace(3);
            dma16(db, fd, lb + L_WD + k * 1024);
            if (k < 3) dma16(db + 32 * DM, (k < 2 || lane < 32) ? fd : 0u, lb + L_WD + (5 + k) * 1024);
        }
    };
    auto load_gu = [&](int m, int lane) __attribute__((always_inline)) {
#pragma unroll 1
        for (int k0 = 0; k0 < 3; ++k0) {
            const int k = (k0 + (g & 7)) % 3, c = k * 64 + lane, row = c / 6, cc = c - 6 * row;
            const unsigned o = (unsigned)((swz(row) * FF + cc * 8) * 2), d = lb + L_GU + m * 8192 + k * 1024;
            const u16* wb = (m == 0 ? wg : wu) + gj * 80 * FF + gi * 48;
            pace(3);
            dma16(wb, o, d); dma16(wb + 32 * FF, o, d + 3072);
            if (k < 2) dma16(wb + 64 * FF, (k == 0 || lane < 32) ? o : 0u, d + 6144);
        }
    };
    auto lds_add = [&](int off, unsigned v) __attribute__((always_inline)) { asm volatile("s_mov_b64 exec, 1\n\tds_add_u32 %0, %1\n\ts_mov_b64 exec, -1" :: "v"(lb + L_CNT + off), "v"(v) : "memory"); };
    auto send_flag = [&](int lane) __attribute__((always_inline)) {
        st4_sc1(ws, WS_MB + (unsigned)((gi * 8 + ((gj == (gi & 7) ? lane : gi) & 7)) * 128 + gj * 4), lds_word(lb + L_CNT + 68));
    };
    auto tiles4 = [&](const Frag80 (&f)[4], const Frag80& b, unsigned long long mk) __attribute__((always_inline)) -> u32x4 {
        f32x4 c[4], d[4];
#pragma unroll
        for (int i = 0; i < 4; ++i) c[i] = mfma32(f[i].a0, b.a0, z4);
#pragma unroll
        for (int i = 0; i < 4; ++i) { c[i] = mfma32(f[i].a1, b.a1, c[i]); d[i] = mfma16(f[i].a2, b.a2, z4); }
        __builtin_amdgcn_sched_barrier(0);
        f32x2 lo, hi;
        asm volatile("s_nop 1\n\ts_mov_b64 exec, %18\n\tv_pk_add_f32 %0, %2, %10\n\tv_pk_add_f32 %1, %3, %11\n\t"
                     "s_lshl_b64 exec, exec, 1\n\tv_pk_add_f32 %0, %4, %12\n\tv_pk_add_f32 %1, %5, %13\n\t"
                     "s_lshl_b64 exec, exec, 1\n\tv_pk_add_f32 %0, %6, %14\n\tv_pk_add_f32 %1, %7, %15\n\t"
                     "s_lshl_b64 exec, exec, 1\n\tv_pk_add_f32 %0, %8, %16\n\tv_pk_add_f32 %1, %9, %17\n\ts_mov_b64 exec, -1"
                     : "=&v"(lo), "=&v"(hi) : "v"(LO2(c[0])), "v"(HI2(c[0])), "v"(LO2(c[1])), "v"(HI2(c[1])), "v"(LO2(c[2])), "v"(HI2(c[2])), "v"(LO2(c[3])), "v"(HI2(c[3])),
                       "v"(LO2(d[0])), "v"(HI2(d[0])), "v"(LO2(d[1])), "v"(HI2(d[1])), "v"(LO2(d[2])), "v"(HI2(d[2])), "v"(LO2(d[3])), "v"(HI2(d[3])), "s"(mk) : "scc");
        return asu(__builtin_shufflevector(lo, hi, 0, 1, 2, 3));
    };
    auto empty1 = [&](int half, unsigned S, int lane) __attribute__((always_inline)) {
        const unsigned o = WS_H1 + S + (unsigned)((gi * 4 + 2 * half) * 16384 + gj * 896 + min(lane, 55) * 16);
        st16_sc1(ws, o, (u32x4){~0u, ~0u, ~0u, ~0u}); st16_sc1(ws, o + 16384, (u32x4){~0u, ~0u, ~0u, ~0u});
    };
    auto report = [&]() __attribute__((always_inline)) { if (gj == (gi & 7)) st4_sc1(ws, WS_R + (unsigned)(gi * 4), lds_word(lb + L_CNT + 88)); };
    auto answer = [&](int lane, unsigned r) __attribute__((always_inline)) {
        int d = min(max((int)(r - (tst << 11)) >> 11, -400), 600);
        unsigned k = (r >> 5) & 63u, v = (r >> 1) & 15u;
        v += dppu<0x124>(v); v += dppu<0x128>(v);
#define VOTES(l) (__builtin_amdgcn_readlane(v, l) + __builtin_amdgcn_readlane(v, 16 + l))
        const unsigned c = (VOTES(0) > 32 ? 2u : 0u) | (VOTES(1) > 32 ? 4u : 0u) | (VOTES(2) + VOTES(3) > 64 ? 8u : 0u);
        d += (int)dppu<0xB1>((unsigned)d); d += (int)dppu<0x4E>((unsigned)d); d += (int)dppu<0x141>((unsigned)d); d += (int)dppu<0x140>((unsigned)d);
        k = max(k, dppu<0xB1>(k)); k = max(k, dppu<0x4E>(k)); k = max(k, dppu<0x141>(k)); k = max(k, dppu<0x140>(k));
        d = __builtin_amdgcn_readlane(d, 0) + __builtin_amdgcn_readlane(d, 16);
        k = max((unsigned)__builtin_amdgcn_readlane(k, 0), (unsigned)__builtin_amdgcn_readlane(k, 16));
        st4_sc1(ws, WS_MB + (unsigned)(((lane & 31) * 8 + (lane & 7)) * 128 + 32), (tst + (d >> 5) + TOFF) << 10 | k << 4 | c | 1u);
        lds_set(lb + L_CNT + 36, 1u);
    };
    auto adopt = [&](unsigned t) __attribute__((always_inline)) {
        const int dt = (int)(t - (tst << 10)) >> 10;
        if (dt > -256 && dt < 512) lds_set(lb + L_CNT + 64, tst + max(dt, (int)(lds_word(lb + L_CNT + 88) - (tst << 11) ) / 2048 - CL));
        lds_set(lb + L_CNT + 20, 4096u + ((t >> 4) & 63u) * BETA);
        lds_set(lb + L_CNT + 80, lds_word(lb + L_CNT + 92) ^ t);
        lds_set(lb + L_CNT + 84, (((t >> 1) ^ lds_word(lb + L_CNT + 68)) & 4u) << 11);
        lds_set(lb + L_CNT + 52, 1u);
    };
    struct StatAddr { unsigned a16, b16, a8, b8, dst; };
    auto stats_addr = [&](int lane, unsigned dbase, int dstride) __attribute__((always_inline)) -> StatAddr {
        const int kq = lane >> 4, q4 = (lane >> 2) & 3, p4 = lane & 3;
        const int x1 = LANES(0x4444444444444444ull) ? L_WR + gj * 160 : L_XS + q4 * 160, xa = LANES(0xaaaaaaaaaaaaaaaaull) ? L_XS + 640 : x1;
        StatAddr r{lb + (unsigned)(xa + kq * 16), lb + (unsigned)(L_XS + p4 * 160 + kq * 16), lb + (unsigned)(xa + 128 + kq * 8), lb + (unsigned)(L_XS + p4 * 160 + 128 + kq * 8), dbase + (unsigned)(kq * dstride)};
        PIN3(r.a16, r.b16, r.dst); PIN2(r.a8, r.b8);
        return r;
    };
    auto stats = [&](const StatAddr& r) __attribute__((always_inline)) {
        const bf16x8 a0 = lds16(r.a16), b0 = lds16(r.b16), a1 = lds16(r.a16 + 64), b1 = lds16(r.b16 + 64);
        const bf16x4 a2 = lds8(r.a8), b2 = lds8(r.b8);
        unsigned long long mk = 0x0008000400020001ull; PINS(mk);
        __builtin_amdgcn_sched_barrier(0);
        st16m(ws, r.dst, asu(mfma32(a1, b1, mfma32(a0, b0, z4)) + mfma16(a2, b2, z4)), mk);
    };
    unsigned qa, pw, ma, idx, qm;
    auto qk_addr = [&](bool L1) __attribute__((always_inline)) {
        LANE_VARS();
        qa = lb + L_Q + (unsigned)(kq * 32 + (lane & 2) * 8);
        pw = lb + L_P + (unsigned)((7 + 8 * wave) * 192 + (kq >> 1) * 64 + (n >> 3) * 32 + (lane & 2) * 8 + (q4 & 1) * 8 + (kq & 1) * 4 + (lane & 1) * 2);
        ma = lb + L_M + (unsigned)(wave * 4);
        idx = (lane & 2) ? 0xeeeeu : 0x4444u;
        qm = lb + L_Q;
        PIN3(qa, pw, ma); PIN2(idx, qm);
        if (L1 && wave < 4) __builtin_amdgcn_s_setprio(2);
    };
    auto att_qk0 = [&]() __attribute__((always_inline)) {
        qk_addr(false);
#pragma unroll 1
        for (unsigned spins = 0; spins < (kSpinCap << 4); ++spins) {
            if (lds_word(lb + L_CNT + 4) != 0u) break;
            __builtin_amdgcn_s_sleep(1);
        }
        STAMP(3);
        const u32x4 q0 = *(const LAS u32x4*)(size_t)qa, q1 = *(const LAS u32x4*)(size_t)(qa + 128), q2 = *(const LAS u32x4*)(size_t)(qa + 256);
        f32x4 c0 = z4, c1 = z4, c2 = z4, c3 = z4;
#define SMF(c, q, r) "v_smfmac_f32_16x16x64_bf16 %" #c ", %" #q ", v[" #r "], %7\n\t"
        asm volatile("s_nop 1\n\t" SMF(0, 4, 64:71) SMF(1, 4, 88:95) SMF(2, 4, 112:119) SMF(3, 4, 136:143) SMF(0, 5, 72:79) SMF(1, 5, 96:103) SMF(2, 5, 120:127) SMF(3, 5, 144:151)
                     SMF(0, 6, 80:87) SMF(1, 6, 104:111) SMF(2, 6, 128:135) SMF(3, 6, 152:159) "s_nop 7"
                     : "+v"(c0), "+v"(c1), "+v"(c2), "+v"(c3) : "v"(q0), "v"(q1), "v"(q2), "v"(idx));
#undef SMF
        const float s0 = c0[0] + c0[2], s1 = c1[0] + c1[2], s2 = c2[0] + c2[2], s3 = c3[0] + c3[2];
        float m = fmax2(fmax3(s0, s1, s2), s3);
        m = fmax2(m, dpp<0xB1>(m)); m = fmax2(m, dpp<0x4E>(m)); m = fmax2(m, dpp<0x124>(m)); m = fmax2(m, dpp<0x128>(m));
        const float s01 = LANES(0xffff0000ffff0000ull) ? s1 : s0, s23 = LANES(0xffff0000ffff0000ull) ? s3 : s2;
        const float sv = LANES(0xffffffff00000000ull) ? s23 : s01;
        m = bfr(bfr(m) * SCALE);
        const float p = ex2((bfr(bfr(sv) * SCALE) - m) * LOG2E);
        *(LAS u16*)(size_t)pw = (u16)pk2(p, p);
        *(LAS float*)(size_t)ma = m;
        __builtin_amdgcn_sched_barrier(0);
    };
    unsigned pr, oa, oc, cma, cxa, cra, cso;
    auto att_addr = [&]() __attribute__((always_inline)) {
        LANE_VARS();
        pr = lb + L_P + (unsigned)((7 + 8 * wave - 2 * q4 - (lane & 1)) * 192 + kq * 32 + (lane & 2) * 8);
        oa = lb + L_OP + (unsigned)(wave * 1024 + (lane & 7) * 64 + kq * 16 + (n >> 3) * 8);
        oc = lb + L_OP + (unsigned)(wave * 1024 + 512 + (kq >> 1) * 256 + (lane & 7) * 32 + (kq & 1) * 16 + (n >> 3) * 8);
        PIN3(pr, oa, oc);
    };
    auto comb_out = [&](float u0, float u1, unsigned so, unsigned long long mk, bool both) __attribute__((always_inline)) {
        const unsigned w = min(pk2(u0, u1), 0xfffffffeu);
        if (!both) { st4_sc1(ws, so, w); return; }
        const u32x4 w4 = {dppu<0x00>(w), dppu<0x55>(w), dppu<0xAA>(w), dppu<0xFF>(w)};
        st16m(ws, so, w4, mk);
    };
    auto max8 = [&](f32x4 m0, f32x4 m1) __attribute__((always_inline)) { return fmax3(fmax3(m0[0], m0[1], m0[2]), fmax3(m0[3], m1[0], m1[1]), fmax2(m1[2], m1[3])) * LOG2E; };
    auto comb_o0 = [&](f32x4 m0, f32x4 m1, f32x4 mm, f32x4 o0, f32x4 o1, f32x4 o2, f32x4 o3, unsigned so, unsigned long long mk, bool both) __attribute__((always_inline)) {
        const float M = max8(m0, m1);
        f32x4 f;
#pragma unroll
        for (int k = 0; k < 4; ++k) f[k] = ex2(__builtin_fmaf(mm[k], LOG2E, -M));
        const f32x4 o = f[3] * o3 + (f[2] * o2 + (f[1] * o1 + f[0] * o0));
        comb_out(rs32(o[0], o[2]), rs32(o[1], o[3]), so, mk, both);
    };
    auto comb_o1 = [&](f32x4 m0, f32x4 m1, f32x2 mm, f32x4 o0, f32x4 o1, unsigned so, unsigned long long mk, bool both) __attribute__((always_inline)) {
        const float M = max8(m0, m1);
        const f32x4 o = ex2(__builtin_fmaf(mm[1], LOG2E, -M)) * o1 + ex2(__builtin_fmaf(mm[0], LOG2E, -M)) * o0;
        comb_out(x16_add(rs32(o[0], o[2])), x16_add(rs32(o[1], o[3])), so, mk, both);
    };
    auto ml_addr = [&](int lane, unsigned& xa, unsigned& ra) __attribute__((always_inline)) {
        xa = lb + L_M + (unsigned)(((lane & 15) >> 1) * 4); ra = lb + L_P + (unsigned)((7 + 8 * ((lane & 15) >> 1)) * 192 + (lane & 1) * 64 + (lane >> 4) * 16);
    };
    auto comb_ml = [&](unsigned ma, unsigned xa, unsigned ra, unsigned so, bool both) __attribute__((always_inline)) {
        const u32x4 x4 = *(const LAS u32x4*)(size_t)ra;
        const f32x4 m0 = *(const LAS f32x4*)(size_t)ma, m1 = *(const LAS f32x4*)(size_t)(ma + 16);
        const float mw = *(const LAS float*)(size_t)xa;
        __builtin_amdgcn_sched_barrier(0);
        const f32x4 sp = mfma32u((u32x4){0x3f803f80u, 0x3f803f80u, 0x3f803f80u, 0x3f803f80u}, x4, z4);
        __builtin_amdgcn_sched_barrier(0);
        const float M = max8(m0, m1);
        float L = row16_sum(sp[0] * ex2(__builtin_fmaf(mw, LOG2E, -M)));
        if (both) L = dpp<0x00>(L);
        st16m(ws, so, asu((f32x4){M, L, M, L}), both ? 3ull : 1ull);
    };
    auto att_pv0 = [&]() __attribute__((always_inline)) {
        u32x4 bm[8];
        trans_v(IC<0>{});
        if (v_after(9) > v_after(7)) { trans_w(IC<0>{}); trans_w(IC<1>{}); }
        trans_v(IC<1>{});
#pragma unroll
        for (int e = 0; e < 8; ++e) bm[e] = *(const LAS u32x4*)(size_t)(pr + e * 192);
        __builtin_amdgcn_sched_barrier(0);
        f32x4 da = z4, dc = z4;
        PIN2(da, dc);
        asm volatile("s_nop 1");
        __builtin_amdgcn_sched_barrier(0);
        f32x2 r;
        smf<RV>(da, bm[0], idx); smf<RV + 8>(da, bm[1], idx); smf<RV + 16>(da, bm[2], idx); smf<RV + 24>(da, bm[3], idx);
        smf<RV + 32>(da, bm[4], idx); smf<RV + 40>(da, bm[5], idx); smf<RV + 48>(da, bm[6], idx); smf<RV + 56>(da, bm[7], idx);
        asm volatile("s_nop 7\n\tv_pk_add_f32 %0, %1, %2\n\tds_write_b64 %3, %0\n\ts_mov_b64 exec, 1\n\tds_add_u32 %4, %5\n\ts_mov_b64 exec, -1"
                     : "=&v"(r) : "v"(LO2(da)), "v"(HI2(da)), "v"(oa), "v"(lb + L_CNT + 24), "v"(1u) : "memory");
        GUARD(900);
        MARK(b_t5);
        if (v_after(9) < v_after(7)) { trans_w(IC<0>{}); trans_w(IC<1>{}); }
        trans_w(IC<2>{}); trans_w(IC<3>{});
        STAMP(4);
        asm volatile("s_nop 1\n\tv_smfmac_f32_16x16x64_bf16 %0, %1, v[224:231], %5\n\tv_smfmac_f32_16x16x64_bf16 %0, %2, v[232:239], %5\n\t"
                     "v_smfmac_f32_16x16x64_bf16 %0, %3, v[240:247], %5\n\tv_smfmac_f32_16x16x64_bf16 %0, %4, v[248:255], %5\n\ts_nop 6"
                     : "+v"(dc) : "v"(bm[0]), "v"(bm[1]), "v"(bm[2]), "v"(bm[3]), "v"(idx));
        asm volatile("s_nop 0\n\tv_pk_add_f32 %0, %1, %2\n\tds_write_b64 %3, %0" : "=&v"(r) : "v"(LO2(dc)), "v"(HI2(dc)), "v"(oc) : "memory");
        STAMP(20);
    };
    unsigned long long ck1 = 0xffff0000ffff0000ull, ck2 = 0xffffffff00000000ull, csc = 0x3d93cd3a3d93cd3aull;
    auto att1 = [&]() __attribute__((always_inline)) {
        constexpr int layer = 1;
        {
#define SMF(c, q, r) "v_smfmac_f32_16x16x64_bf16 v[" #c "], v[" #q "], v[" #r "], %0\n\t"
#define MAXD(x) "v_max_f32_dpp v41, v41, v41 " x " row_mask:0xf bank_mask:0xf\n\t"
            asm volatile("s_waitcnt lgkmcnt(0)\n\ts_barrier\n\t"
                         "ds_read_b128 v[0:3], %1\n\tds_read2_b32 v[12:13], %2 offset0:96 offset1:100\n\tds_read_b128 v[4:7], %1 offset:128\n\tds_read_b128 v[8:11], %1 offset:256\n\t"
                         "v_mov_b64 v[16:17], 0\n\tv_mov_b64 v[18:19], 0\n\tv_mov_b64 v[20:21], 0\n\tv_mov_b64 v[22:23], 0\n\tv_mov_b64 v[24:25], 0\n\tv_mov_b64 v[26:27], 0\n\t"
                         "v_mov_b64 v[28:29], 0\n\tv_mov_b64 v[30:31], 0\n\tv_mov_b64 v[32:33], 0\n\tv_mov_b64 v[34:35], 0\n\tv_mov_b64 v[36:37], 0\n\tv_mov_b64 v[38:39], 0\n\t"
                         "s_waitcnt lgkmcnt(3)\n\t" SMF(16:19, 0:3, 64:71) SMF(20:23, 0:3, 88:95) "s_waitcnt lgkmcnt(1)\n\tv_mul_f32 v13, 0x3acccccd, v13\n\t"
                         SMF(24:27, 0:3, 112:119) "v_mul_f32 v12, 0x3acccccd, v12\n\t" SMF(28:31, 0:3, 136:143) "v_fma_f32 v12, -v13, v13, v12\n\t"
                         SMF(16:19, 4:7, 72:79) "v_maximum3_f32 v12, v12, 0, 0\n\t" SMF(20:23, 4:7, 96:103) "v_cvt_pk_bf16_f32 v12, 0, v12\n\t"
                         SMF(24:27, 4:7, 120:127) "v_add_f32 v12, 0x3727c5ac, v12\n\t" SMF(28:31, 4:7, 144:151) "v_cvt_pk_bf16_f32 v12, 0, v12\n\ts_waitcnt lgkmcnt(0)\n\t"
                         SMF(16:19, 8:11, 80:87) "v_sqrt_f32 v12, v12\n\t" SMF(20:23, 8:11, 104:111) "v_cvt_pk_bf16_f32 v12, 0, v12\n\t"
                         SMF(24:27, 8:11, 128:135) "v_rcp_f32 v12, v12\n\t" SMF(28:31, 8:11, 152:159)
                         "s_nop 2\n\tv_add_f32 v42, v16, v18\n\ts_nop 0\n\tv_add_f32 v43, v20, v22\n\tv_max_f32 v41, v42, v43\n\tv_add_f32 v44, v24, v26\n\tv_add_f32 v45, v28, v30\n\t"
                         "v_max3_f32 v41, v41, v44, v45\n\tv_cndmask_b32 v42, v42, v43, %8\n\tv_cndmask_b32 v44, v44, v45, %8\n\t"
                         MAXD("quad_perm:[1,0,3,2]") "v_cndmask_b32 v40, v42, v44, %9\n\ts_nop 0\n\t" MAXD("quad_perm:[2,3,0,1]") "s_nop 1\n\t" MAXD("row_ror:4") "s_nop 1\n\t" MAXD("row_ror:8")
                         "v_pk_mul_f32 v[40:41], v[12:13], v[40:41] op_sel_hi:[0,1]\n\tv_cvt_pk_bf16_f32 v40, 0, v40\n\tv_cvt_pk_bf16_f32 v41, 0, v41\n\t"
                         "v_pk_mul_f32 v[40:41], %10, v[40:41]\n\tv_cvt_pk_bf16_f32 v40, 0, v40\n\tv_cvt_pk_bf16_f32 v41, 0, v41\n\t"
                         "v_sub_f32 v40, v40, v41\n\tv_mul_f32 v40, 0x3fb8aa3b, v40\n\tv_exp_f32 v40, v40\n\ts_nop 0\n\tv_cvt_pk_bf16_f32 v40, v40, v40\n\tds_write_b16 %3, v40\n\t"
                         "ds_read_b128 v[0:3], %4\n\tds_read_b128 v[4:7], %4 offset:192\n\tds_read_b128 v[8:11], %4 offset:384\n\tds_read_b128 v[12:15], %4 offset:576\n\t"
                         "ds_read_b128 v[16:19], %4 offset:768\n\tds_read_b128 v[20:23], %4 offset:960\n\tds_read_b128 v[24:27], %4 offset:1152\n\tds_read_b128 v[28:31], %4 offset:1344\n\t"
                         "ds_write_b32 %5, v41\n\t"
                         "s_waitcnt lgkmcnt(8)\n\t" SMF(32:35, 0:3, 160:167) SMF(36:39, 0:3, 224:231) "s_waitcnt lgkmcnt(7)\n\t" SMF(32:35, 4:7, 168:175) SMF(36:39, 4:7, 232:239)
                         "s_waitcnt lgkmcnt(6)\n\t" SMF(32:35, 8:11, 176:183) SMF(36:39, 8:11, 240:247) "s_waitcnt lgkmcnt(4)\n\t" SMF(32:35, 12:15, 184:191) SMF(36:39, 12:15, 248:255)
                         SMF(32:35, 16:19, 192:199) "s_waitcnt lgkmcnt(1)\n\t" SMF(32:35, 20:23, 200:207) SMF(32:35, 24:27, 208:215) SMF(32:35, 28:31, 216:223)
                         "s_nop 2\n\tv_pk_add_f32 v[36:37], v[36:37], v[38:39]\n\tds_write_b64 %7, v[36:37]\n\ts_nop 2\n\tv_pk_add_f32 v[32:33], v[32:33], v[34:35]\n\tds_write_b64 %6, v[32:33]"
                         :: "v"(idx), "v"(qa), "v"(qm), "v"(pw), "v"(pr), "v"(ma), "v"(oa), "v"(oc), "s"(ck1), "s"(ck2), "s"(csc)
                         : "memory", "v0", "v1", "v2", "v3", "v4", "v5", "v6", "v7", "v8", "v9", "v10", "v11", "v12", "v13", "v14", "v15", "v16", "v17", "v18", "v19", "v20", "v21", "v22", "v23",
                           "v24", "v25", "v26", "v27", "v28", "v29", "v30", "v31", "v32", "v33", "v34", "v35", "v36", "v37", "v38", "v39", "v40", "v41", "v42", "v43", "v44", "v45");
#undef SMF
#undef MAXD
            STAMP(20 + 32 * layer);
            BAR();
            if (__builtin_expect(wave == 0, 1)) {
                STAMP(5 + 32 * layer);
                comb_o0(*(const LAS f32x4*)(size_t)cma, *(const LAS f32x4*)(size_t)(cma + 16), *(const LAS f32x4*)(size_t)cxa, *(const LAS f32x4*)(size_t)cra,
                        *(const LAS f32x4*)(size_t)(cra + 1024), *(const LAS f32x4*)(size_t)(cra + 2048), *(const LAS f32x4*)(size_t)(cra + 3072), cso, 0x1111111111111111ull, false);
            }
            __builtin_amdgcn_sched_barrier(0);
            if (__builtin_expect(wave == 1, 1))
                comb_o1(*(const LAS f32x4*)(size_t)cma, *(const LAS f32x4*)(size_t)(cma + 16), *(const LAS f32x2*)(size_t)cxa, *(const LAS f32x4*)(size_t)cra, *(const LAS f32x4*)(size_t)(cra + 1024), cso, 0x0000111100001111ull, false);
            __builtin_amdgcn_sched_barrier(0);
            if (__builtin_expect(wave == 2, 1)) comb_ml(cma, cxa, cra, cso, false);
            MARK(e_q);
            if (wave < 3) STAMP(21 + 32 * layer);
            __builtin_amdgcn_s_setprio(0);
        }
    };

    unsigned nj = 0u, qS = 0u, fS = 0u, rS = 0u, nS = 0u; int ord;
    {
        const int tid = threadIdx.x, lane = tid & 63;
        asm volatile("ds_max_u32 %0, %1" :: "v"(lb + L_CNT + 64), "v"(tst) : "memory");
        unsigned mb = 0u;
        const u16* kbase = kc + wofs / 2;
        const u16* vbase = vc + wofs / 2;
        unsigned pt0 = 0u;
        if (wave == 7) {
            __builtin_amdgcn_s_setprio(3);
            if (lane < 10) *reinterpret_cast<u32x4*>(smem + L_XS + 640 + lane * 16) = (u32x4){0x3f803f80u, 0x3f803f80u, 0x3f803f80u, 0x3f803f80u};
            if (lane < 16) { *reinterpret_cast<u32x4*>(smem + L_CS + 768 + lane * 16) = (u32x4){0u, 0u, 0u, 0u}; }
        } else {
            if (wave >= 4) {
                if (wave == 4) st16_sc1(ws, WS_OWN + (unsigned)(gi * 16384 + (lane >> 5) * 8192 + gj * 512 + (lane & 31) * 16), (u32x4){~0u, ~0u, ~0u, ~0u});
                else empty5(0, (unsigned)(wave - 5) * 8192u, lane);
                if (wave == 6) {
                    __builtin_amdgcn_s_setprio(3);
                    if (gi + gj == 0) st4_sc1(ws, WS_R + (lane & 31) * 4, 0u);
                    st4_sc1(ws, WS_MB + (unsigned)((gi * 8 + gj) * 128 + (lane & 31) * 4), 0u);
                }
            } else if (wave >= 2) {
                unsigned long long c0, c1;
                asm volatile("s_waitcnt lgkmcnt(0)\n\ts_memtime %0\n\ts_waitcnt lgkmcnt(0)\n\ts_load_dword %2, %3, %4\n\ts_waitcnt lgkmcnt(0)\n\ts_memtime %1\n\ts_waitcnt lgkmcnt(0)"
                             : "=&s"(c0), "=&s"(c1), "=&s"(pt0) : "s"(ws), "s"((unsigned)(min(gi & 3, 2) << 21) + ((((tst + g * 977u) * 2654435761u) >> 11 & 0x1fdf80u) | ((wave ^ gj) & 1) * 8192)) : "memory");
                pt0 = ((wave ^ gj) & 1) ? (unsigned)c0 - (unsigned)c1 : (unsigned)c1 - (unsigned)c0;
            }
            STAMP(36);
            *reinterpret_cast<u32x4*>(smem + L_P + tid * 16) = (u32x4){0u, 0u, 0u, 0u};
            if (tid < 72 * 12 - 448) *reinterpret_cast<u32x4*>(smem + L_P + 7168 + tid * 16) = (u32x4){0u, 0u, 0u, 0u};
            if (wave >= 4) {
                if (wave == 6) VMW(2); else VMW(1);
                asm volatile("ds_max_u32 %0, %1" :: "v"(lb + L_CNT + 64), "v"(now() - TLAND) : "memory");
                STAMP(40);
                lds_add(0, 1u);
            } else if (wave >= 2) { lds_add(20, pt0); VMW(0); lds_add(0, 1u); }
        }
        asm volatile("s_waitcnt lgkmcnt(0)" :: "s"(pt1) : "memory");
        const int rot = gj + wave;
        const unsigned r0 = now(), c0 = (unsigned)__builtin_amdgcn_s_memtime();
        u32x4 a[4];
        unsigned ko = (unsigned)((wave * 64 + (lane & 7)) * 384 + (((lane >> 3) & 1) * 4 + (lane >> 4)) * 16);
        PIN1(ko);
        auto k_group = [&](auto GC_) __attribute__((always_inline)) {
            constexpr int G = decltype(GC_)::value, R = RK + 24 * (G >> 1) + 4 * (G & 1);
            const unsigned o = ko + (unsigned)((((G >> 1) + rot) & 3) * 6144 + (G & 1) * 3072);
            pace(3);
            ld16_nt<R, 0>(kbase, o); ld16_nt<R + 8, 128>(kbase, o); ld16_nt<R + 16, 256>(kbase, o);
            const int lane = lane_id();
            const unsigned ho = WS_Q0 + (unsigned)((gi * 4 + (gj >> 1)) * 4096 + lane * 16);
            if (wave < 7) {
                asm volatile("; COND_BEGIN");
                if (G == 0 && wave >= 4) { VMW(3); lds_add(32, 1u); }
                if (G == 0) {
                    if (wave < 2) { VMW(3); lds_add(0, 1u); load_gu(wave, lane); }
                    if (wave == 2) load_wd(lane);
                    if (wave == 3) {
#pragma unroll 1
                        for (int k = 5 - WEARLY; k < 5; ++k) load_wo(k, lane, true);
                    }
                }
                asm volatile("; COND_END");
            }
            if (wave == 7) {
                asm volatile("; COND_BEGIN");
                if (G == GP) {
                    VMW(3 * (GP + 1));
#pragma unroll 1
                    for (unsigned spins = 0; spins < (kSpinCap << 4); ++spins) {
                        if (lds_word(lb + L_CNT) >= 7u) break;
                        __builtin_amdgcn_s_sleep(1);
                    }
                    STAMP(26);
                    const unsigned char* p = smem + L_X0 + (lane >> 4) * 1280;
                    const float c0 = lo16(*reinterpret_cast<const u16*>(p));
                    const f32x2 c2 = {c0, c0};
                    f32x2 s2 = {0.f, 0.f}, q2 = s2;
#pragma unroll
                    for (int k = 0; k < 5; ++k) {
                        const u32x4 w = *reinterpret_cast<const u32x4*>(p + k * 256 + (lane & 15) * 16);
#pragma unroll
                        for (int e = 0; e < 4; ++e) { const f32x2 d = unpk(w[e]) - c2; s2 += d; q2 += d * d; }
                    }
                    const float s = row16_sum(s2[0] + s2[1]), qq = row16_sum(q2[0] + q2[1]);
                    const float ms = s * (1.f / DM);
                    const unsigned mv = pk2(c0 + ms, fmax2(qq - s * ms, 0.f) * (1.f / DM));
                    const float mb = lo16(mv), rden = frcp(bfr(__builtin_amdgcn_sqrtf(bfr(hi16(mv) + 1e-5f))));
                    const f32x2 mb2 = {mb, mb}, rd2 = {rden, rden};
                    if ((lane & 15) < 10) {
                        u32x4 w = *reinterpret_cast<const u32x4*>(p + gj * 160 + (lane & 15) * 16);
#pragma unroll
                        for (int e = 0; e < 4; ++e) w[e] = pk2v(unpk(pk2v(unpk(w[e]) - mb2)) * rd2);
                        *reinterpret_cast<u32x4*>(smem + L_XS + (lane >> 4) * 160 + (lane & 15) * 16) = w;
                    }
                    STAMP(30);
                }
                if (G == GP || G == GA) {
#pragma unroll 1
                    for (int blk = 0; blk < 3; ++blk) {
                        LANE_VARS();
                        const unsigned xb = lb + L_XS + (unsigned)((G == GP ? q4 : 4) * 160);
                        Frag80 f[4];
#pragma unroll
                        for (int i = 0; i < 4; ++i) f[i] = frag80(lb + L_WQ + (unsigned)(128 * blk + 32 * i + 8 * p4), 416, r0s, r1s, r2s);
                        const Frag80 b = rows80(xb + kq * 16, xb + 128 + kq * 8);
                        const u32x4 c = tiles4(f, b, 0x1111111111111111ull);
                        if (G == GP) *reinterpret_cast<u32x4*>(smem + L_OP + q4 * 768 + 256 * blk + p4 * 64 + kq * 16) = c;
                        else *reinterpret_cast<u32x4*>(smem + L_CS + 256 * blk + kq * 64 + p4 * 16) = c;
                    }
                    if (G == GP) {
                        asm volatile("" ::: "memory");
                        const int ln = lane_id();
                        f32x2 c[6];
                        unsigned m = 20u;
#pragma unroll
                        for (int k = 0; k < 6; ++k) {
                            c[k] = *reinterpret_cast<const f32x2*>(smem + L_OP + (k / 3) * 1536 + ln * 24 + (k % 3) * 8);
                            m = max(m, max(fexp(c[k][0]), fexp(c[k][1])));
                        }
                        m = max(m, dppu<0xB1>(m)); m = max(m, dppu<0x4E>(m)); m = max(m, dppu<0x141>(m)); m = max(m, dppu<0x140>(m));
                        m = max(max((unsigned)__builtin_amdgcn_readlane(m, 0), (unsigned)__builtin_amdgcn_readlane(m, 16)), max((unsigned)__builtin_amdgcn_readlane(m, 32), (unsigned)__builtin_amdgcn_readlane(m, 48)));
                        lds_set(lb + L_CNT + 76, m);
                        const float f = __uint_as_float((273u - m) << 23);
#pragma unroll
                        for (int k = 0; k < 2; ++k) {
                            const u32x2 w0 = enc21(c[3 * k][0] * f, c[3 * k][1] * f, c[3 * k + 1][0] * f), w1 = enc21(c[3 * k + 1][1] * f, c[3 * k + 2][0] * f, c[3 * k + 2][1] * f);
                            st16_sc1(ws, WS_Q0 + (unsigned)((gi * 4 + 2 * k + (ln >> 5)) * 4096 + gj * 512 + (ln & 31) * 16), (u32x4){w0[0], w0[1], w1[0], w1[1]});
                        }
                        STAMP(2);
                        WAIT1(pf, 3 * (GP + 1) + 2);
                    }
                }
                if (G == GA) {
                    const unsigned half = __builtin_amdgcn_s_getreg(2 << 6 | 20);
                    lds_set(lb + L_CNT + 68, lds_word(lb + L_CNT + 64) << 11 | lds_word(lb + L_CNT + 76) << 3 | 1u | (((int)lds_word(lb + L_CNT + 20) > 0) ^ half) << 1 | half << 2);
                    VMW(3 * (GA - GP));
#pragma unroll 1
                    for (unsigned spins = 0; spins < (kSpinCap << 4); ++spins) if (lds_word(lb + L_CNT + 32) >= 3u) break;
                    send_flag(lane_id());
                    lds_set(lb + L_CNT + 8, 1u);
                    STAMP(17);
                    f32x2 r2 = {0.f, 0.f};
#pragma unroll
                    for (int k = 0; k < 5; ++k) r2 += unpk(*reinterpret_cast<const unsigned*>(smem + L_WR + k * 256 + lane_id() * 4));
                    *reinterpret_cast<float*>(smem + L_CNT + 12) = wave_sum(r2[0] + r2[1]);
                }
                asm volatile("; COND_END");
            }
            if (wave == 6) {
                asm volatile("; COND_BEGIN");
                if (G == GB - 1) mb = ld4_sc1(ws, WS_MB + (unsigned)((gi * 8 + gj) * 128 + (gi & 7) * 4));
                if (G == GB) {
                    WAIT1(mb, 3);
#pragma unroll 1
                    for (unsigned spins = 0; spins < kSpinCap && !all_set(mb); ++spins) {
                        if ((spins & 7u) == 7u && lds_word(lb + L_CNT + 8)) send_flag(lane);
                        mb = ld4_sc1(ws, WS_MB + (unsigned)((gi * 8 + gj) * 128 + (gi & 7) * 4));
                        WAIT1(mb, 0);
                    }
                    mb = ld4_sc1(ws, WS_MB + (unsigned)((gi * 8 + (gi & 7)) * 128 + (lane & 7) * 4));
                }
                if (G == GC) {
                    WAIT1(mb, 3 * (GC - GB));
#pragma unroll 1
                    for (unsigned spins = 0; spins < kSpinCap && !all_set(mb); ++spins) {
                        if ((spins & 7u) == 7u && lds_word(lb + L_CNT + 8)) send_flag(lane);
                        mb = ld4_sc1(ws, WS_MB + (unsigned)((gi * 8 + (gi & 7)) * 128 + (lane & 7) * 4));
                        WAIT1(mb, 0);
                    }
                    lds_set(lb + L_CNT + 92, (__builtin_amdgcn_readlane(mb, gi & 7) & 4u) ? 14u : 0u);
                    int d = min(max((int)(mb - (tst << 11)) >> 11, -400), 600);
                    d += (int)dppu<0xB1>((unsigned)d); d += (int)dppu<0x4E>((unsigned)d); d += (int)dppu<0x141>((unsigned)d);
                    const unsigned tm = tst + (unsigned)(__builtin_amdgcn_readfirstlane(d) >> 3);
                    lds_set(lb + L_CNT + 64, tm);
                    const float slow = (float)(now() - r0) * 24.f * 256.f * frcp((float)((unsigned)__builtin_amdgcn_s_memtime() - c0)) - 256.f;
                    lds_set(lb + L_CNT + 88, tm << 11 | (unsigned)__builtin_amdgcn_fmed3f(slow, 0.f, 63.f) << 5 | __builtin_popcount((unsigned)__builtin_amdgcn_ballot_w64((mb & 2u) != 0u) & 255u) << 1 | 1u);
                    report();
                    a[0] = ld16_sc1<0>(ws, ho); a[1] = ld16_sc1<1024>(ws, ho); a[2] = ld16_sc1<2048>(ws, ho); a[3] = ld16_sc1<3072>(ws, ho);
                    pace(XCOST, false);
                    STAMP(31);
                }
                asm volatile("; COND_END");
            } else if (G == GC) { for (int k = 0; k < 4; ++k) asm volatile("" : "=v"(a[k])); }
        };
        k_group(IC<0>{}); k_group(IC<1>{}); k_group(IC<2>{}); k_group(IC<3>{}); k_group(IC<4>{}); k_group(IC<5>{}); k_group(IC<6>{}); k_group(IC<7>{});
        {
            const int lane = lane_id();
            const unsigned v0 = (unsigned)((wave * 64 + 4 * (lane >> 4)) * 384 + (lane & 15) * 16);
            const unsigned v1 = v0 + 256 - (unsigned)((lane & 15) * 8);
            auto v_group = [&](auto HC) __attribute__((always_inline)) {
                constexpr int h = decltype(HC)::value, p = kVord[h], R = p < 8 ? RV + 16 * (p & 3) + 4 * (p >> 2) : RV + 64 + 2 * (p - 8), NV = v_upto(h);
                const unsigned o = (p < 8 ? v0 : v1) + (unsigned)((((p < 8 ? p >> 1 : p) + rot) & 3) * 6144);
                pace(2);
                if (p < 8) { ld16_nt<R, (p & 1) * 768>(vbase, o); ld16_nt<R + 8, (p & 1) * 768 + 384>(vbase, o); }
                else { ld8_nt<R, 0>(vbase, o); ld8_nt<R + 8, 384>(vbase, o); ld8_nt<R + 16, 768>(vbase, o); ld8_nt<R + 24, 1152>(vbase, o); }
                if (wave == 7 && gi + gj == 0) {
                    asm volatile("; COND_BEGIN");
                    if (h == 3 || h == 6) mb = ld4_sc1(ws, WS_R + (unsigned)((lane & 31) * 4));
                    if (h == 4 || h == 7) {
                        WAIT1(mb, NV - v_upto(h - 1));
                        if (all_set(mb) && !lds_word(lb + L_CNT + 36)) answer(lane, mb);
                    }
                    asm volatile("; COND_END");
                }
                if (wave == 6) {
                    asm volatile("; COND_BEGIN");
                if (h == HD) {
                    asm volatile("s_waitcnt vmcnt(%4)" : "+v"(a[0]), "+v"(a[1]), "+v"(a[2]), "+v"(a[3]) : "i"(3 * (7 - GC) + NV) : "memory");
                    float sq[6] = {0.f, 0.f, 0.f, 0.f, 0.f, 0.f};
#pragma unroll
                    for (int k = 0; k < 4; ++k) {
                        const float f = __uint_as_float(((((unsigned)__builtin_amdgcn_ds_bpermute((2 * k + (lane >> 5)) * 4, (int)mb) >> 3) & 255u) - 19u) << 23);
#pragma unroll
                        for (int e = 0; e < 2; ++e) {
                            const unsigned lo = a[k][2 * e], hi = a[k][2 * e + 1];
                            const float fe = (int)hi < 0 ? __builtin_nanf("") : f;
                            sq[3 * e] += (float)((int)(lo << 11) >> 11) * fe;
                            sq[3 * e + 1] += (float)((int)(__builtin_amdgcn_alignbit(hi, lo, 21) << 11) >> 11) * fe;
                            sq[3 * e + 2] += (float)((int)(hi << 1) >> 11) * fe;
                        }
                    }
#pragma unroll
                    for (int e = 0; e < 3; ++e) {
                        const int pi = 3 * (lane & 31) + e;
                        *reinterpret_cast<unsigned*>(smem + L_Q + (pi >> 3) * 32 + (pi & 6) * 2 + (pi & 1) * 16) = pk2(x32_add(sq[2 * e]), x32_add(sq[2 * e + 1]));
                    }
                    asm volatile("s_waitcnt lgkmcnt(0)\n\tds_write_b32 %0, %1" :: "v"(lb + L_CNT + 4), "v"(1u) : "memory");
                    STAMP(27);
                    lead = LEAD * PACE * 4;
                }
                    if (h == 6) mb = ld4_sc1(ws, WS_MB + (unsigned)((gi * 8 + (gi & 7)) * 128 + 32));
                    if (h == 8) {
                        WAIT1(mb, NV - v_upto(6));
                        const unsigned r = __builtin_amdgcn_readfirstlane(mb);
                        if (r) { adopt(r); STAMP(29); }
                    }
                    asm volatile("; COND_END");
                }
                if constexpr (h < 6) {
                    constexpr int i = 2 * h;
                    fix_k1(IC<i>{}, IC<20 - 6 * (i / 3) - i % 3 + NV>{});
                    fix_k1(IC<i + 1>{}, IC<20 - 6 * ((i + 1) / 3) - (i + 1) % 3 + NV>{});
                }
                if (h == 5) {
                    STAMP(1);
                    __builtin_amdgcn_s_setprio(0);
                    att_qk0();
                }
            };
            v_group(IC<0>{}); v_group(IC<1>{}); v_group(IC<2>{}); v_group(IC<3>{}); v_group(IC<4>{}); v_group(IC<5>{});
            v_group(IC<6>{}); v_group(IC<7>{}); v_group(IC<8>{}); v_group(IC<9>{}); v_group(IC<10>{}); v_group(IC<11>{});
            lds_add(16, 1u);
            if (wave == 3) lds_add(44, WEARLY);
        }
    }
    {
        STAMP(28);
        att_addr();
        const int lane = lane_id();
        cma = lb + L_M; cxa = lb + L_M + (unsigned)((lane >> 4) * 8); cra = lb + L_OP + (unsigned)(512 + (lane >> 4) * 2048 + (lane & 15) * 16);
        cso = WS_OWN + (unsigned)(gi * 16384 + gj * 512 + 256 + (lane >> 5) * 64 + (lane & 12) * 4 + (lane & 1) * 8192);
        PIN2(cma, cxa); PIN2(cra, cso);
        att_pv0();
    }
    {
        f32x4 m0, m1, o0, o1; f32x2 mm; unsigned r, taken;
        asm volatile("s_mov_b64 exec, 1\n\tds_add_rtn_u32 %5, %7, %8 offset:%11\n\ts_mov_b64 exec, -1\n\t"
                     "ds_read_b128 %0, %7\n\tds_read_b128 %1, %7 offset:16\n\tds_read_b64 %2, %9\n\tds_read_b128 %3, %10\n\tds_read_b128 %4, %10 offset:1024\n\t"
                     "ds_read_b32 %6, %7 offset:%12\n\ts_waitcnt lgkmcnt(0)"
                     : "=&v"(m0), "=&v"(m1), "=&v"(mm), "=&v"(o0), "=&v"(o1), "=&v"(r), "=&v"(taken)
                     : "v"(cma), "v"(1u), "v"(cxa), "v"(cra), "i"(L_CNT + 28 - L_M), "i"(L_CNT + 40 - L_M) : "memory");
        ord = __builtin_amdgcn_readfirstlane(r);
        if (ord == 7) { STAMP(5); comb_o1(m0, m1, mm, o0, o1, cso, 0x0000333300003333ull, true); lds_set(lb + L_CNT + 48, now() | 1u); STAMP(21); }
        MARK(e_t5);
        if (ord <= OML + 1 || (unsigned)__builtin_amdgcn_readfirstlane(taken) < 5u - WEARLY) {
        const int lane = lane_id();
        auto wait_cnt = [&](int off, unsigned v) __attribute__((always_inline)) {
#pragma unroll 1
            for (unsigned spins = 0; spins < (kSpinCap << 4); ++spins) { if (lds_word(lb + L_CNT + off) >= v) break; __builtin_amdgcn_s_sleep(1); }
        };
        if (ord == OML) {
            wait_cnt(16, 8u);
            unsigned xa, ra; ml_addr(lane, xa, ra);
            comb_ml(cma, xa, ra, WS_OWN + (unsigned)(gi * 16384 + gj * 512 + 384 + (lane & 1) * 8192), true);
            STAMP(24);
        } else if (ord == 0 && !lds_word(lb + L_CNT + 52)) {
            if (gi + gj == 0 && !lds_word(lb + L_CNT + 36)) {
                unsigned r = 0u;
#pragma unroll 1
                for (unsigned spins = 0; spins < kSpinCap; ++spins) {
                    r = ld4_sc1(ws, WS_R + (unsigned)((lane & 31) * 4));
                    WAIT1(r, 0);
                    if (all_set(r)) break;
                    if ((spins & 7u) == 7u) send_flag(lane);
                }
                answer(lane, r);
            }
#pragma unroll 1
            for (unsigned spins = 0; spins < kSpinCap; ++spins) {
                unsigned r = ld4_sc1(ws, WS_MB + (unsigned)((gi * 8 + (gi & 7)) * 128 + 32));
                WAIT1(r, 0);
                r = __builtin_amdgcn_readfirstlane(r);
                if (r) { adopt(r); break; }
                if ((spins & 7u) == 7u) { send_flag(lane); report(); }
                __builtin_amdgcn_s_sleep(16);
            }
            STAMP(29);
        } else if (ord == OML + 1) {
#pragma unroll 1
            for (unsigned spins = 0; spins < (kSpinCap << 6); ++spins) if (lds_word(lb + L_CNT + 24) >= 8u) break;
            const unsigned ra = lb + L_OP + (unsigned)((lane >> 5) * 4096 + (lane & 31) * 16);
            comb_o0(*(const LAS f32x4*)(size_t)cma, *(const LAS f32x4*)(size_t)(cma + 16), *(const LAS f32x4*)(size_t)(cma + (lane >> 5) * 16), *(const LAS f32x4*)(size_t)ra, *(const LAS f32x4*)(size_t)(ra + 1024),
                    *(const LAS f32x4*)(size_t)(ra + 2048), *(const LAS f32x4*)(size_t)(ra + 3072), WS_OWN + (unsigned)(gi * 16384 + gj * 512 + (lane & 60) * 4 + (lane & 1) * 8192), 0x3333333333333333ull, true);
            STAMP(22);
        }
        if (ord == OML) {
#pragma unroll 1
            for (unsigned spins = 0; spins < (kSpinCap << 8); ++spins) { if (lds_word(lb + L_CNT + 52)) break; __builtin_amdgcn_s_sleep(1); }
            const unsigned S = (lds_word(lb + L_CNT + 80) & 4u) << 11;
            empty4(0, S, lane); empty5(5, 0u, lane); empty5(5, 8192u, lane);
        }
#pragma unroll 1
        for (;;) {
            unsigned j;
            asm volatile("s_mov_b64 exec, 1\n\tds_add_rtn_u32 %0, %1, %2\n\ts_mov_b64 exec, -1\n\ts_waitcnt lgkmcnt(0)" : "=&v"(j) : "v"(lb + L_CNT + 40), "v"(1u) : "memory");
            j = __builtin_amdgcn_readfirstlane(j);
            if (j >= 5u - WEARLY) break;
            load_wo((int)j, lane, false); ++nj;
        }
        }
        STAMP(23);
    }

    unsigned omr, owa, owb;
    auto op_addr = [&](int layer) __attribute__((always_inline)) {
        LANE_VARS();
        const int t = wave - 3;
        cma = WS_OWN + rS + (unsigned)(gi * 16384 + layer * 4096 + (lane >> 3) * 512 + t * 128 + (t == 3 ? 0 : (lane & 7) * 16));
        cxa = lb + L_O2 + (unsigned)((lane >> 3) * 416 + t * 128 + (t == 3 ? 0 : (lane & 7) * 16));
        const int pb = (lane & 4) ? 3 - p4 : p4, jp = 2 * pb + ((lane >> 2) & 1);
        cso = tile35(gj, 10 * layer + t) + (unsigned)((kq & 1) * 8192 + gi * 128 + (kq >> 1) * 64 + p4 * 16);
        cra = lb + L_O2 + (unsigned)(jp * 416 + kq * 16); omr = lb + L_O2 + (unsigned)(jp * 416 + 384);
        owa = lb + L_WO + (unsigned)(r0s * 160 + 32 * t + 8 * p4); owb = lb + L_WO + (unsigned)(r1s * 160 + 32 * t + 8 * p4);
        if (layer) PIN2(cma, cxa);
        PIN2(cso, cra); PIN3(omr, owa, owb);
    };
    auto chain = [&](auto LC) __attribute__((always_inline)) {
        constexpr int layer = decltype(LC)::value;
        unsigned h4 = WS_H4 + (unsigned)((layer * 32 + gi) * 32768) + (layer ? fS : 0u);
        auto sides = [&]() __attribute__((always_inline)) {
            const unsigned m = lds_word(lb + L_CNT + 80);
            qS = (m & 2u) << 12; fS = (m & 4u) << 11; rS = (m & 8u) << 10; nS = lds_word(lb + L_CNT + 84); h4 += fS;
        };
        if (wave < 3) {
            LANE_VARS();
            if (layer == 0) {
                unsigned lo = WS_OWN + lds_word(lb + L_CNT + 84) + (unsigned)(gi * 16384 + (lane >> 3) * 512), lw = lb + L_O2 + (unsigned)((lane >> 3) * 416);
                unsigned lo0 = lo + (unsigned)((lane & 7) * 16), lw0 = lw + (unsigned)((lane & 7) * 16);
                PIN2(lo0, lo); PIN2(lw0, lw);
                GUARD(400);
                MARK(b_g2);
                if (__builtin_expect(wave == 2, 0)) { if (nj | (ord == OML)) { VMW(0); lds_add(44, nj); } }
                else if (__builtin_expect(wave == 1, 0)) {
#pragma unroll 1
                    for (unsigned spins = 0; spins < (kSpinCap << 4); ++spins) if (lds_word(lb + L_CNT + 48)) break;
#pragma unroll 1
                    for (unsigned spins = 0; spins < kSpinCap; ++spins) {
                        u32x4 o0 = ld16_sc1<0>(ws, lo0), o1 = ld16_sc1<128>(ws, lo0), o2 = ld16_sc1<384>(ws, lo);
                        if (nj) { VMW(3); lds_add(44, nj); nj = 0u; }
                        asm volatile("s_waitcnt vmcnt(0)" : "+v"(o0), "+v"(o1), "+v"(o2) :: "memory");
                        *(LAS u32x4*)(size_t)lw0 = o0; *(LAS u32x4*)(size_t)(lw0 + 128) = o1; *(LAS u32x4*)(size_t)(lw + 384) = o2;
                        if (!any_empty(max(max(um4(o0), um4(o1)), um4(o2)))) break;
                    }
                } else {
                    unsigned long long miss; unsigned cnt = kSpinCap << 4, tf, nv = nj;
                    asm volatile("1:\n\tds_read_b32 v4, %4 offset:48\n\ts_sub_u32 %1, %1, 1\n\ts_cbranch_scc1 5f\n\ts_waitcnt lgkmcnt(0)\n\tv_readfirstlane_b32 %2, v4\n\ts_cmp_eq_u32 %2, 0\n\ts_cbranch_scc1 1b\n\t"
                                 "s_add_u32 %2, %2, %8\n"
                                 "2:\n\ts_memrealtime vcc\n\ts_waitcnt lgkmcnt(0)\n\ts_sub_u32 vcc_lo, %2, vcc_lo\n\ts_cmp_lt_u32 vcc_lo, 0x400\n\ts_cbranch_scc1 2b\n"
                                 "5:\n\ts_mov_b32 %1, 15\n"
                                 "3:\n\tglobal_load_dwordx4 v[0:3], %5, %6 offset:256 sc1\n\ts_waitcnt vmcnt(1)\n\ts_mov_b64 exec, 1\n\tds_add_u32 %4, %3 offset:44\n\ts_mov_b64 exec, -1\n\tv_mov_b32 %3, 0\n\t"
                                 "s_sub_u32 %1, %1, 1\n\ts_cselect_b64 vcc, 0, -1\n\ts_waitcnt vmcnt(0)\n\tds_write_b128 %7, v[0:3] offset:256\n\tv_max3_u32 v4, v0, v1, v2\n\tv_max_u32 v4, v4, v3\n\t"
                                 "v_cmp_eq_u32 %0, -1, v4\n\ts_and_b64 vcc, vcc, %0\n\ts_cbranch_scc1 3b"
                                 : "=&s"(miss), "+s"(cnt), "=&s"(tf), "+v"(nv) : "v"(lb + L_CNT), "v"(lo0), "s"(ws), "v"(lw0), "i"(DG2 - 1) : "memory", "vcc", "scc", "v0", "v1", "v2", "v3", "v4");
                    if (__builtin_expect(miss != 0ull, 0)) {
#pragma unroll 1
                        for (unsigned spins = 0; spins < kSpinCap; ++spins) {
                            if ((spins & 15u) == 0u) send_flag(lane);
                            u32x4 o0 = ld16_sc1<256>(ws, lo0);
                            WAIT1(o0, 0);
                            *(LAS u32x4*)(size_t)(lw0 + 256) = o0;
                            if (!any_empty(um4(o0))) break;
                        }
                    }
                }
                MARK(e_g2);
                STAMP(25);
                BAR();
                sides();
                if (wave < 2) empty1(wave, qS, lane);
                else {
                    st16m(ws, WS_CS + (unsigned)((gi * 8 + gj) * 768 + lane * 16), *reinterpret_cast<const u32x4*>(smem + L_CS + lane * 16), 0xffffffffffffull);
                    empty5(10, 0u, lane); empty5(10, 8192u, lane);
                }
            } else BAR();
            {
                const unsigned im = lb + L_GU + (unsigned)(32 * wave + 8 * p4);
                Frag80 fg = frag80(im, 96, r0s, r1s, r2s), fu = frag80(im + 8192, 96, r0s, r1s, r2s);
                const unsigned xb = lb + L_XS + (unsigned)((n < 4 ? n : 4) * 160);
                unsigned b16 = xb + kq * 16, b8 = xb + 128 + kq * 8;
                unsigned sg = h4 + (unsigned)(n < 4 ? h4s(wave) + gj * 512 + kq * 64 + n * 16 : H4C + wave * 1024 + gj * 128 + kq * 16), su = sg + (n < 4 ? 256u : 64u);
                unsigned o = h4 + h4s(wave) + (unsigned)(lane * 16);
                unsigned oc = h4 + (unsigned)(H4C + wave * 1024 + lane * 16), pc = (unsigned)((kq * 16 + q4) * 4);
                unsigned os = h4 + (unsigned)(H4T + (q4 + 4 * (kq & 1)) * 128 + p4 * 16);
                unsigned ha = lb + L_H + (unsigned)(p4 * 96 + wave * 32 + q4 * 8 + kq * 2);
                f32x2 cs = {0.f, 0.f};
                if (layer != 0) cs = *reinterpret_cast<const f32x2*>(smem + L_CS2 + wave * 512 + lane * 8);
                PIN3(fg.a0, fg.a1, fg.a2); PIN3(fu.a0, fu.a1, fu.a2); PIN3(b16, b8, sg); PIN3(su, o, os); PIN2(ha, cs);
                unsigned long long mk = layer ? 0x000f000f000f000full : 0x001f001f001f001full;
                unsigned ta = WS_MB + (unsigned)((lane >> 1) * 1024 + 64 + (lane & 1) * 16);
                if (layer == 0) PIN3(oc, ta, pc);
                unsigned x4a = layer ? 0u : X4A * lds_word(lb + L_CNT + 20) >> 12;
                unsigned long long w0 = __builtin_amdgcn_ballot_w64(wave == 0);
                PINS(x4a); PINS(w0);
                unsigned long long rdm = 0x3acccccd3acccccdull;
                PINS(mk); PINS(rdm);
                if (layer == 0) VMW(0);
                if (layer == 0) GUARD(1200); else GUARD(800);
                MARK(b_gu);
                BAR();
                STAMP(10 + 32 * layer);
                u32x4 tl = {0u, 0u, 0u, 0u};
                if (layer == 0) asm volatile("s_mov_b64 exec, %3\n\tglobal_load_dwordx4 %0, %1, %2 sc1\n\ts_mov_b64 exec, -1" : "+v"(tl) : "v"(ta), "s"(ws), "s"(w0) : "memory");
                const Frag80 b = rows80(b16, b8);
                const f32x4 g0 = mfma32(fg.a0, b.a0, z4), u0 = mfma32(fu.a0, b.a0, z4), g1 = mfma32(fg.a1, b.a1, g0), g2 = mfma16(fg.a2, b.a2, z4), u1 = mfma32(fu.a1, b.a1, u0), u2 = mfma16(fu.a2, b.a2, z4);
                __builtin_amdgcn_sched_barrier(0);
                const f32x4 cg = g1 + g2, cu = u1 + u2;
                st16m2<0>(ws, sg, asu(cg), su, asu(cu), mk);
                STAMP(11 + 32 * layer);
                if (layer == 0 && wave != 0) {
                    unsigned tr = 0u;
#pragma unroll 1
                    for (unsigned spins = 0; spins < (kSpinCap << 6); ++spins) { tr = lds_word(lb + L_CNT + 56); if (tr) break; }
                    until(tr + x4a);
                } else if (layer == 0) {
                    WAIT1(tl, 2);
#pragma unroll 1
                    for (unsigned spins = 0; spins < kSpinCap; ++spins) {
                        const unsigned long long miss = __builtin_amdgcn_ballot_w64(min(min(tl[0], tl[1]), min(tl[2], tl[3])) == 0u);
                        if (!miss) break;
                        asm volatile("s_mov_b64 exec, %3\n\tglobal_load_dwordx4 %0, %1, %2 sc1\n\ts_mov_b64 exec, -1\n\ts_waitcnt vmcnt(0)" : "+v"(tl) : "v"(ta), "s"(ws), "s"(miss) : "memory");
                    }
                    const unsigned tn = now();
                    unsigned d = min(min(min(tn - tl[0], tn - tl[1]), min(tn - tl[2], tn - tl[3])), 800u);
                    d = min(d, dppu<0xB1>(d)); d = min(d, dppu<0x4E>(d)); d = min(d, dppu<0x124>(d)); d = min(d, dppu<0x128>(d));
                    unsigned tr = tn - min(min((unsigned)__builtin_amdgcn_readlane(d, 0), (unsigned)__builtin_amdgcn_readlane(d, 16)), min((unsigned)__builtin_amdgcn_readlane(d, 32), (unsigned)__builtin_amdgcn_readlane(d, 48)));
                    lds_set(lb + L_CNT + 56, tr | 1u);
                    until(__builtin_amdgcn_readfirstlane(tr | 1u) + x4a);
                } else sched(X4, 0);
                STAMP(16 + 32 * layer);
                float hv;
                u32x4 a0, a1, a2, a3, ac, as;
                auto load = [&]() __attribute__((always_inline)) {
                    as = ld16_sc1(ws, os);
                    if (layer == 0) ac = ld16_sc1(ws, oc);
                    a0 = ld16_sc1<0>(ws, o); a1 = ld16_sc1<1024>(ws, o); a2 = ld16_sc1<2048>(ws, o); a3 = ld16_sc1<3072>(ws, o);
                    if (layer == 0) lds_add(32, 1u);
                };
                auto all_in = [&]() __attribute__((always_inline)) {
                    if (layer == 0) asm volatile("s_waitcnt vmcnt(0)" : "+v"(a0), "+v"(a1), "+v"(a2), "+v"(a3), "+v"(ac), "+v"(as) :: "memory");
                    else { asm volatile("s_waitcnt vmcnt(0)" : "+v"(a0), "+v"(a1), "+v"(a2), "+v"(a3), "+v"(as) :: "memory"); ac = as; }
                };
                auto compute = [&]() __attribute__((always_inline)) {
#define ROR(n) " row_ror:" #n " row_mask:0xf bank_mask:0xf\n\t"
                    asm volatile("s_waitcnt vmcnt(%16)\n\tv_add_f32_dpp v40, %2, %2" ROR(8) "v_add_f32_dpp v41, %3, %3" ROR(8) "s_nop 0\n\tv_add_f32_dpp v40, v40, v40" ROR(4) "v_add_f32_dpp v41, v41, v41" ROR(4)
                                 "v_mov_b32 v42, v40\n\tv_mov_b32 v43, v41\n\ts_nop 0\n\tv_permlane16_swap_b32 v40, v42\n\tv_permlane16_swap_b32 v41, v43\n\t"
                                 "v_pk_add_f32 v[40:41], v[40:41], v[42:43]\n\tv_pk_mul_f32 v[40:41], v[40:41], %14\n\t"
                                 ".if %17 == 0\n\ts_waitcnt vmcnt(4)\n\tv_mov_b64 v[44:45], %12\n\tv_mov_b64 v[46:47], %13\n\t.endif\n\t"
                                 "v_fma_f32 v40, -v41, v41, v40\n\tv_cvt_pk_bf16_f32 v42, 0, v41\n\tv_maximum3_f32 v40, v40, 0, 0\n\t"
                                 ".if %17 == 0\n\tv_permlane32_swap_b32 v44, v46\n\tv_permlane32_swap_b32 v45, v47\n\tv_pk_add_f32 v[44:45], v[44:45], v[46:47]\n\t.endif\n\t"
                                 "v_cvt_pk_bf16_f32 v40, 0, v40\n\ts_waitcnt vmcnt(2)\n\tv_pk_add_f32 v[32:33], %4, %6\n\tv_add_f32 v40, 0x3727c5ac, v40\n\tv_pk_add_f32 v[34:35], %5, %7\n\t"
                                 ".if %17 == 0\n\tv_permlane16_swap_b32 v44, v45\n\tv_add_f32 v44, v44, v45\n\t.endif\n\t"
                                 "v_cvt_pk_bf16_f32 v40, 0, v40\n\ts_waitcnt vmcnt(0)\n\tv_pk_add_f32 v[36:37], %8, %10\n\tv_sqrt_f32 v40, v40\n\tv_pk_add_f32 v[38:39], %9, %11\n\t"
                                 ".if %17 == 0\n\tv_add_f32_dpp v44, v44, v44" ROR(8) "ds_bpermute_b32 v46, %19, v44\n\tds_bpermute_b32 v47, %19, v44 offset:16\n\t.endif\n\t"
                                 "v_pk_add_f32 v[32:33], v[32:33], v[36:37]\n\tv_pk_add_f32 v[34:35], v[34:35], v[38:39]\n\tv_cvt_pk_bf16_f32 v40, 0, v40\n\t"
                                 "s_nop 0\n\t"
                                 "v_permlane32_swap_b32 v32, v34\n\tv_permlane32_swap_b32 v33, v35\n\tv_rcp_f32 v40, v40\n\tv_pk_add_f32 v[32:33], v[32:33], v[34:35]\n\ts_nop 1\n\t"
                                 "v_permlane16_swap_b32 v32, v33\n\t.if %17 == 0\n\ts_waitcnt lgkmcnt(0)\n\tv_mov_b64 %1, v[46:47]\n\t.endif\n\t"
                                 "v_pk_fma_f32 v[32:33], %1, v[42:43], v[32:33] op_sel_hi:[1,0,1] neg_lo:[1,0,0] neg_hi:[1,0,0]\n\tv_pk_mul_f32 v[32:33], v[32:33], v[40:41] op_sel_hi:[1,0]\n\t"
                                 "v_cvt_pk_bf16_f32 v32, 0, v32\n\tv_mul_f32 v34, 0xbfb8aa3b, v32\n\tv_exp_f32 v34, v34\n\tv_cvt_pk_bf16_f32 v33, 0, v33\n\tv_add_f32 v34, 1.0, v34\n\t"
                                 "v_rcp_f32 v34, v34\n\ts_nop 0\n\tv_mul_f32 v32, v34, v32\n\tv_cvt_pk_bf16_f32 v32, 0, v32\n\tv_mul_f32 %0, v33, v32\n\tv_cvt_pk_bf16_f32 v32, %0, %0\n\tds_write_b16 %15, v32"
                                 : "=&v"(hv), "+v"(cs)
                                 : "v"(as[0]), "v"(as[1]), "v"(LO2(a0)), "v"(HI2(a0)), "v"(LO2(a1)), "v"(HI2(a1)), "v"(LO2(a2)), "v"(HI2(a2)), "v"(LO2(a3)), "v"(HI2(a3)),
                                   "v"(LO2(layer ? as : ac)), "v"(HI2(layer ? as : ac)), "s"(rdm), "v"(ha), "i"(layer ? 4 : 5), "i"(layer), "v"(as), "v"(layer ? ha : pc)
                                 : "memory", "v32", "v33", "v34", "v35", "v36", "v37", "v38", "v39", "v40", "v41", "v42", "v43", "v44", "v45", "v46", "v47");
#undef ROR
                };
                RETRY(load(), (all_in(), max(max(max(um4(a0), um4(a1)), max(um4(a2), um4(a3))), max(um4(ac), um4(as)))), compute(), any_nan(hv, hv), layer ? X4 : X4A);
                BAR();
                MARK(e_gu);
                STAMP(12 + 32 * layer);
                if (layer == 0) {
                    *reinterpret_cast<f32x2*>(smem + L_CS2 + wave * 512 + lane * 8) = cs;
                }
            }
            if (layer == 0) {
                if (wave == 1) {
                    const unsigned o = WS_CS + (unsigned)(gi * 6144 + 3072 + (lane < 48 ? lane : 0) * 16);
                    u32x4 a[8];
                    GATHER8(a, o, 768);
                    WAIT8(a, 0);
                    if (lane < 48) *reinterpret_cast<f32x4*>(smem + L_CS + lane * 16) = SUM8(a);
                }
                Frag80 f[4];
#pragma unroll
                for (int i = 0; i < 4; ++i) { f[i] = frag80(lb + L_WQ + (unsigned)(128 * wave + 32 * i + 8 * p4), 416, r0s, r1s, r2s); PIN3(f[i].a0, f[i].a1, f[i].a2); }
                unsigned b16 = lb + L_XS + (unsigned)(q4 * 160 + kq * 16), b8 = lb + L_XS + (unsigned)(q4 * 160 + 128 + kq * 8);
                unsigned so = WS_H1 + qS + (unsigned)((gi * 4 + q4) * 16384 + gj * 896 + 256 * wave + kq * 64 + p4 * 16);
                unsigned long long mk = 0x1111111111111111ull;
                PIN3(b16, b8, so); PINS(mk);
                GUARD(3800);
                MARK(b_q);
                BAR();
                STAMP(33);
                const Frag80 b = rows80(b16, b8);
                st16_sc1(ws, so, tiles4(f, b, mk));
                STAMP(34);
                qk_addr(true); att_addr();
                unsigned x0a = lb + L_M + (unsigned)((lane >> 5) * 16), x1a = lb + L_M + (unsigned)((lane >> 4) * 8), x2a, r2a;
                unsigned r0a = lb + L_OP + (unsigned)((lane >> 5) * 4096 + (lane & 31) * 16), r1a = lb + L_OP + (unsigned)(512 + (lane >> 4) * 2048 + (lane & 15) * 16);
                unsigned s0a = (unsigned)(lane * 4), s1a = (unsigned)(256 + (lane >> 5) * 64 + (lane & 15) * 4);
                ml_addr(lane, x2a, r2a);
                PIN3(x0a, x1a, x2a); PIN3(r0a, r1a, r2a); PIN2(s0a, s1a);
                cma = lb + L_M; cxa = wave == 0 ? x0a : wave == 1 ? x1a : x2a; cra = wave == 0 ? r0a : wave == 1 ? r1a : r2a;
                cso = WS_OWN + rS + (unsigned)(gi * 16384 + 4096 + gj * 512) + (wave == 0 ? s0a : wave == 1 ? s1a : 384u);
                PIN2(cma, cxa); PIN2(cra, cso);
                asm volatile("" : "=v"(omr), "=v"(owa), "=v"(owb));
            }
        } else {
            const int t = wave - 3;
            StatAddr sa;
            u32x4 sel;
            unsigned long long hi = 0xff00ff00ff00ff00ull;
            auto gather32 = [&](unsigned o, unsigned X, auto fin) __attribute__((always_inline)) {
                u32x4 a0 = ld16_sc1<-2048>(ws, o), a1 = ld16_sc1<-1024>(ws, o), a2 = ld16_sc1<0>(ws, o), a3 = ld16_sc1<1024>(ws, o);
                float v;
                auto sum = [&]() __attribute__((always_inline)) {
                    asm volatile("s_waitcnt vmcnt(3)\n\tv_mfma_f32_16x16x32_bf16 v[40:43], %1, %5, 0\n\ts_waitcnt vmcnt(2)\n\tv_mfma_f32_16x16x32_bf16 v[40:43], %2, %5, v[40:43]\n\t"
                                 "s_waitcnt vmcnt(1)\n\tv_mfma_f32_16x16x32_bf16 v[40:43], %3, %5, v[40:43]\n\ts_waitcnt vmcnt(0)\n\tv_mfma_f32_16x16x32_bf16 v[40:43], %4, %5, v[40:43]\n\t"
                                 "s_nop 7\n\tv_cndmask_b32 v40, v40, v41, %6\n\tv_cndmask_b32 v42, v42, v43, %6\n\ts_nop 1\n\tv_permlane32_swap_b32 v40, v42\n\tv_add_f32 v40, v40, v42\n\t"
                                 "v_cvt_pk_bf16_f32 %0, 0, v40"
                                 : "=&v"(v), "+v"(a0), "+v"(a1), "+v"(a2), "+v"(a3) : "v"(sel), "s"(hi) : "memory", "v40", "v41", "v42", "v43");
                    fin(v);
                    __builtin_amdgcn_sched_barrier(0);
                };
                sum();
                if (__builtin_expect(any_nan(v, v) != 0ull, 0)) {
#pragma unroll 1
                    for (unsigned spins = 0; spins < kSpinCap; ++spins) {
                        slip(X);
                        again16<-2048>(a0, ws, o); again16<-1024>(a1, ws, o); again16<0>(a2, ws, o); again16<1024>(a3, ws, o);
                        asm volatile("s_waitcnt vmcnt(0)" : "+v"(a0), "+v"(a1), "+v"(a2), "+v"(a3) :: "memory");
                        if (!any_empty(max(max(um4(a0), um4(a1)), max(um4(a2), um4(a3))))) break;
                    }
                    sum();
                }
            };
            auto poll32 = [&](unsigned o, unsigned t0) __attribute__((always_inline)) {
                    unsigned long long m0, m1, m2, m3; unsigned cnt = kSpinCap; float v;
#define CHK(r, m) "v_max3_u32 v44, v" #r ", v[" #r "+1], v[" #r "+2]\n\tv_max_u32 v44, v44, v[" #r "+3]\n\tv_cmp_eq_u32 %" #m ", -1, v44\n\t"
                    asm volatile("s_branch 3f\n"
                                 "1:\n\ts_mov_b64 exec, %1\n\tglobal_load_dwordx4 v[0:3], %6, %7 offset:-2048 sc1\n\ts_mov_b64 exec, %2\n\tglobal_load_dwordx4 v[4:7], %6, %7 offset:-1024 sc1\n\t"
                                 "s_mov_b64 exec, %3\n\tglobal_load_dwordx4 v[8:11], %6, %7 sc1\n\ts_mov_b64 exec, %4\n\tglobal_load_dwordx4 v[12:15], %6, %7 offset:1024 sc1\n\ts_mov_b64 exec, -1\n\t"
                                 "s_sub_u32 %5, %5, 1\n\ts_cselect_b64 vcc, 0, -1\n\ts_waitcnt vmcnt(0)\n\ts_branch 2f\n"
                                 "3:\n\ts_memrealtime vcc\n\ts_waitcnt lgkmcnt(0)\n\ts_sub_u32 vcc_lo, %10, vcc_lo\n\ts_cmp_lt_u32 vcc_lo, 0x400\n\ts_cbranch_scc1 3b\n\t"
                                 "global_load_dwordx4 v[0:3], %6, %7 offset:-2048 sc1\n\tglobal_load_dwordx4 v[4:7], %6, %7 offset:-1024 sc1\n\t"
                                 "global_load_dwordx4 v[8:11], %6, %7 sc1\n\tglobal_load_dwordx4 v[12:15], %6, %7 offset:1024 sc1\n\ts_mov_b64 vcc, -1\n"
                                 "2:\n\ts_waitcnt vmcnt(3)\n\tv_mfma_f32_16x16x32_bf16 v[40:43], v[0:3], %8, 0\n\t" CHK(0, 1) "s_waitcnt vmcnt(2)\n\tv_mfma_f32_16x16x32_bf16 v[40:43], v[4:7], %8, v[40:43]\n\t" CHK(4, 2)
                                 "s_waitcnt vmcnt(1)\n\tv_mfma_f32_16x16x32_bf16 v[40:43], v[8:11], %8, v[40:43]\n\t" CHK(8, 3) "s_waitcnt vmcnt(0)\n\tv_mfma_f32_16x16x32_bf16 v[40:43], v[12:15], %8, v[40:43]\n\t" CHK(12, 4)
                                 "s_or_b64 exec, %1, %2\n\ts_or_b64 exec, exec, %3\n\ts_or_b64 exec, exec, %4\n\ts_and_b64 vcc, exec, vcc\n\ts_cbranch_scc1 1b\n\t"
                                 "s_mov_b64 exec, -1\n\tv_cndmask_b32 v40, v40, v41, %9\n\tv_cndmask_b32 v42, v42, v43, %9\n\ts_nop 1\n\tv_permlane32_swap_b32 v40, v42\n\tv_add_f32 v40, v40, v42\n\t"
                                 "v_cvt_pk_bf16_f32 %0, 0, v40"
                                 : "=&v"(v), "=&s"(m0), "=&s"(m1), "=&s"(m2), "=&s"(m3), "+s"(cnt) : "v"(o), "s"(ws), "v"(sel), "s"(hi), "s"(t0 - 1u)
                                 : "memory", "vcc", "scc", "v0", "v1", "v2", "v3", "v4", "v5", "v6", "v7", "v8", "v9", "v10", "v11", "v12", "v13", "v14", "v15", "v40", "v41", "v42", "v43", "v44");
#undef CHK
                return v;
            };
#define PUT8(A, B, M) "v_min_u32 v34, -2, v32\n\tv_min_u32 v32, -2, v32\n\tv_min_u32 v35, -2, v33\n\tv_min_u32 v33, -2, v33\n\tv_permlane16_swap_b32 v32, v34\n\ts_nop 0\n\t" \
                      "v_permlane16_swap_b32 v33, v35\n\ts_mov_b64 exec, " M "\n\tglobal_store_dwordx4 " A ", v[32:35], " B " sc1\n\ts_mov_b64 exec, -1\n\ts_nop 0"
            unsigned xw, qo, qw, qc;
            __builtin_amdgcn_s_setprio(0);
            {
                STAMP(6 + 32 * layer);
                bf16x8 af[6];
                unsigned long long mk = 0x000f000f000f000full;
                if (layer == 0) {
                    const int lane = lane_id();
                    xe = lo16(*reinterpret_cast<const u16*>(smem + L_X0 + xrow(lane) * 1280 + gj * 160 + t * 32 + xcol(lane) * 2));
                    op_addr(0);
                    GUARD(1600);
                    MARK(b_op);
                    if (nj | (ord == OML)) { VMW(0); lds_add(44, nj); }
#pragma unroll 1
                    for (unsigned spins = 0; spins < (kSpinCap << 4); ++spins) if (lds_word(lb + L_CNT + 44) >= 5u) break;
                    STAMP(8);
                } else MARK(b_op);
                const unsigned lo = cma, lw = cxa, so = cso, br = cra, mr = omr, h3 = tile35(gj, 10 * layer + t);
                unsigned go, xs;
#pragma unroll
                for (int s = 0; s < 6; ++s) af[s] = cat8(tr64o(owa, s * 32 * 160), tr64o(owb, s * 32 * 160));
                PINS(mk); PINS(hi);
                if (layer != 0 && t < 4) {
                    sched(X2, 0);
                    STAMP(49);
                    u32x4 ov = ld16_sc1(ws, lo);
                    WAIT1(ov, 0);
                    *(LAS u32x4*)(size_t)lw = ov;
                    if (__builtin_expect(any_empty(um4(ov)), 0)) {
#pragma unroll 1
                        for (unsigned spins = 0; spins < kSpinCap; ++spins) {
                            slip(X2);
                            ov = ld16_sc1(ws, lo);
                            WAIT1(ov, 0);
                            *(LAS u32x4*)(size_t)lw = ov;
                            if (!any_empty(um4(ov))) break;
                        }
                    }
                }
                {
                    bf16x8 b0, b1, b2, b3, b4, b5;
#define QP " row_half_mirror row_mask:0xf bank_mask:0xf\n\t"
                    asm volatile("s_waitcnt lgkmcnt(0)\n\ts_barrier\n\t"
                                 "ds_read_b128 %0, %12\n\tds_read_b64 v[36:37], %13\n\tds_read_b128 %1, %12 offset:64\n\tds_read_b128 %2, %12 offset:128\n\tds_read_b128 %3, %12 offset:192\n\t"
                                 "ds_read_b128 %4, %12 offset:256\n\tds_read_b128 %5, %12 offset:320\n\t"
                                 "s_waitcnt lgkmcnt(5)\n\tv_mfma_f32_16x16x32_bf16 v[32:35], %6, %0, 0\n\tv_max_f32_dpp v38, v36, v36" QP "v_sub_f32 v38, v36, v38\n\t"
                                 "s_waitcnt lgkmcnt(4)\n\tv_mfma_f32_16x16x32_bf16 v[32:35], %7, %1, v[32:35]\n\tv_exp_f32 v38, v38\n\t"
                                 "s_waitcnt lgkmcnt(3)\n\tv_mfma_f32_16x16x32_bf16 v[32:35], %8, %2, v[32:35]\n\tv_mul_f32 v37, v37, v38\n\t"
                                 "s_waitcnt lgkmcnt(2)\n\tv_mfma_f32_16x16x32_bf16 v[32:35], %9, %3, v[32:35]\n\tv_add_f32_dpp v39, v37, v37" QP
                                 "s_waitcnt lgkmcnt(1)\n\tv_mfma_f32_16x16x32_bf16 v[32:35], %10, %4, v[32:35]\n\tv_rcp_f32 v39, v39\n\t"
                                 "s_waitcnt lgkmcnt(0)\n\tv_mfma_f32_16x16x32_bf16 v[32:35], %11, %5, v[32:35]\n\tv_mul_f32 v38, v38, v39\n\ts_nop 6\n\t"
                                 "v_pk_mul_f32 v[32:33], v[32:33], v[38:39] op_sel_hi:[1,0]\n\tv_pk_mul_f32 v[34:35], v[34:35], v[38:39] op_sel_hi:[1,0]\n\ts_nop 0\n\t"
                                 "v_add_f32_dpp v32, v32, v32" QP "v_add_f32_dpp v33, v33, v33" QP "v_add_f32_dpp v34, v34, v34" QP "v_add_f32_dpp v35, v35, v35" QP
                                 "v_cvt_pk_bf16_f32 v32, v32, v33\n\tv_cvt_pk_bf16_f32 v33, v34, v35\n\t" PUT8("%14", "%15", "%16")
                                 : "=&v"(b0), "=&v"(b1), "=&v"(b2), "=&v"(b3), "=&v"(b4), "=&v"(b5)
                                 : "v"(af[0]), "v"(af[1]), "v"(af[2]), "v"(af[3]), "v"(af[4]), "v"(af[5]), "v"(br), "v"(mr), "v"(so), "s"(ws), "s"(mk)
                                 : "memory", "v32", "v33", "v34", "v35", "v36", "v37", "v38", "v39");
#undef QP
                }
                if (layer == 0 && wave == 7) st4_sc1(ws, WS_MB + (unsigned)(gi * 1024 + 64 + gj * 4), now() | 1u);
                const unsigned tp = layer ? 0u : now();
                if (layer == 0) sides();
                STAMP(7 + 32 * layer);
                {
                    const int ln = lane_id();
#pragma unroll
                    for (int k = 0; k < 4; ++k) sel[k] = ((ln >> 1) & 3) == k ? (ln & 1) ? 0x3f800000u : 0x3f80u : 0u;
                    go = h3 + nS + (unsigned)(2048 + ln * 16); xs = lb + L_XS + (unsigned)(xrow(ln) * 160 + t * 32 + xcol(ln) * 2);
                    PIN3(sel, go, xs);
                    sa = stats_addr(ln, h4 + (unsigned)(H4T + gj * 128), 16);
                }
                if (layer != 0) sched(X3, 0);
                STAMP(18 + 32 * layer);
                if (layer != 0) gather32(go, X3, [&](float s) { xw = pk2(xe + s, 0.f); *(LAS u16*)(size_t)xs = (u16)xw; });
                else { xw = pk2(xe + poll32(go, tp + W3), 0.f); *(LAS u16*)(size_t)xs = (u16)xw; }
                xe = lo16(xw);
                STAMP(9 + 32 * layer);
            }
            BAR();
            if (wave == 3) stats(sa);
            MARK(e_op);
            STAMP(11 + 32 * layer);
            {
                LANE_VARS();
                const unsigned im = lb + L_WD + (unsigned)(32 * t + 8 * p4);
                bf16x8 wd0 = cat8(tr64(im + r0s * 160), tr64(im + r1s * 160)); bf16x4 wd1 = tr64(im + (32 + r2s) * 160);
                const unsigned h5 = tile35(gj, 10 * layer + 5 + t);
                unsigned h16 = lb + L_H + (unsigned)(p4 * 96 + kq * 16), h8 = lb + L_H + (unsigned)(p4 * 96 + 64 + kq * 8);
                unsigned so = h5 + (unsigned)((kq & 1) * 8192 + gi * 128 + (kq >> 1) * 64 + p4 * 16);
                unsigned long long mk = 0x000f000f000f000full;
                unsigned go = h5 + nS + (unsigned)(2048 + lane * 16), xs = (layer ? (unsigned)(xrow(lane) * 1280 + gj * 160) : lb + L_XS + (unsigned)(xrow(lane) * 160)) + (unsigned)(t * 32 + xcol(lane) * 2);
                unsigned sta = lb + L_ST + (unsigned)(xrow(lane) * 16);
                if (layer == 0 && wave < 6) {
                    sa = stats_addr(lane, WS_H1 + qS + (unsigned)(gi * 65536 + gj * 896 + 768), 16384);
                    asm volatile("" : "=v"(qo), "=v"(qw), "=v"(qc), "=v"(cma), "=v"(cxa), "=v"(cra), "=v"(cso), "=v"(omr), "=v"(owa), "=v"(owb));
                    asm volatile("" : "=v"(qa), "=v"(pw), "=v"(ma), "=v"(idx), "=v"(qm), "=v"(pr), "=v"(oa), "=v"(oc));
                } else if (layer == 0) {
                    op_addr(1); qk_addr(false); att_addr();
                    const int l5 = lane & 31, c = l5 < 24 ? 24 * (wave - 6) + l5 : 48;
                    qo = WS_H1 + qS + (unsigned)((gi * 4 + (gj >> 1)) * 16384 + 3584 + (lane >> 5) * 896 + c * 16);
                    qw = lb + L_Q + (unsigned)(l5 < 24 ? (c >> 4) * 128 + (c & 3) * 32 + (c & 12) + (lane >> 5) * 16 : lane < 26 ? 384 + (lane & 1) * 16 : 448);
                    qc = lb + L_CS + (unsigned)(c * 16 + (lane >> 5) * 8);
                    PIN3(qo, qw, qc);
                }
                PIN2(wd0, wd1); PIN3(h16, h8, so); PIN3(go, xs, sta); PINS(mk);
                GUARD(2000);
                MARK(b_dn);
                if (wave == 4) {
                    unsigned os = h4 + (unsigned)(H4T + (q4 + 4 * (kq & 1)) * 128 + p4 * 16);
                    float sw = *reinterpret_cast<const float*>(smem + L_CNT + 12);
                    PIN2(os, sw);
                    if (layer == 0) {
#pragma unroll 1
                        for (unsigned spins = 0; spins < (kSpinCap << 6); ++spins) if (lds_word(lb + L_CNT + 56)) break;
                    }
                    sched(layer ? X4 : X4A, 0);
                    u32x4 as;
#pragma unroll 1
                    for (unsigned spins = 0; spins < kSpinCap; ++spins) { as = ld16_sc1(ws, os); WAIT1(as, 0); if (!any_empty(um4(as))) break; }
                    float ss = __uint_as_float(as[0]), sm = __uint_as_float(as[1]), rd = __uint_as_float(as[2]);
                    ss += dpp<0x128>(ss); sm += dpp<0x128>(sm); rd += dpp<0x128>(rd);
                    ss += dpp<0x124>(ss); sm += dpp<0x124>(sm); rd += dpp<0x124>(rd);
                    ss = x16_add(ss); sm = x16_add(sm); rd = x16_add(rd);
                    if (LANES(0xfull)) *reinterpret_cast<f32x4*>(smem + L_ST + p4 * 16) = (f32x4){ss, sm, rd, sw};
                }
                if (layer == 0 && (wave == 3 || wave == 5)) {
                    unsigned ea = lane < 40 ? tile35(gj, 15 + (lane >> 3)) + (unsigned)(gi * 128 + (lane & 7) * 16) : WS_OWN + rS + (unsigned)(gi * 16384 + 4096 + gj * 512 + (lane - 40) * 16);
                    unsigned eb = ea + (lane < 40 ? 8192u : 256u);
                    PIN2(ea, eb);
#pragma unroll 1
                    for (unsigned spins = 0; spins < (kSpinCap << 6); ++spins) { if (lds_word(lb + L_CNT + 32) >= 6u) break; __builtin_amdgcn_s_sleep(1); }
                    if (wave == 5) empty4(1, fS, lane);
                    else { st16m(ws, ea, (u32x4){~0u, ~0u, ~0u, ~0u}, 0xffffffffffffffull); st16m(ws, eb, (u32x4){~0u, ~0u, ~0u, ~0u}, 0xffffffffffffffull); }
                }
                asm volatile("s_waitcnt lgkmcnt(0)\n\ts_barrier\n\tds_read_b128 v[36:39], %2\n\tds_read_b64 v[40:41], %3\n\t"
                             "s_waitcnt lgkmcnt(1)\n\tv_mfma_f32_16x16x32_bf16 v[32:35], %0, v[36:39], 0\n\ts_waitcnt lgkmcnt(0)\n\tv_mfma_f32_16x16x16_bf16 v[42:45], %1, v[40:41], 0\n\ts_nop 7\n\t"
                             "v_pk_add_f32 v[32:33], v[32:33], v[42:43]\n\tv_pk_add_f32 v[34:35], v[34:35], v[44:45]\n\t"
                             "v_cvt_pk_bf16_f32 v32, v32, v33\n\tv_cvt_pk_bf16_f32 v33, v34, v35\n\t" PUT8("%4", "%5", "%6")
                             :: "v"(wd0), "v"(wd1), "v"(h16), "v"(h8), "v"(so), "s"(ws), "s"(mk)
                             : "memory", "v32", "v33", "v34", "v35", "v36", "v37", "v38", "v39", "v40", "v41", "v42", "v43", "v44", "v45");
                STAMP(13 + 33 * layer);
                if (layer == 0 || __builtin_expect(gi == 20 + 2 * t + (gj & 1), 1)) {
                    const f32x4 sr = *(const LAS f32x4*)(size_t)sta;
                    const f32x2 st = ln_stats(sr[0], sr[1]);
                    float rs = bfr(frcp(1.f + ex2(bfr((sr[2] - st[0] * sr[3]) * st[1]) * -LOG2E))), n1 = bfr(bfr(xe - st[0]) * st[1]);
                    PIN2(rs, n1);
                    if (layer == 0) {
                        sched(X5A, 0);
                        STAMP(15);
                        gather32(go, X5A, [&](float y) { xw = pk2(xe + bfr(n1 + bfr(y * rs)), 0.f); *(LAS u16*)(size_t)xs = (u16)xw; });
                        xe = lo16(xw);
                        STAMP(14);
                    } else {
                        st2(out, xs, pk2(xe + bfr(n1 + bfr(poll32(go, when(X5B)) * rs)), 0.f));
                        asm volatile("s_endpgm");
                        MARK(e_dn);
                        STAMP(45);
                    }
                }
            }
            if (layer == 0) {
                BAR();
                if (wave == 3) { stats(sa); STAMP(34); }
                MARK(e_dn);
                if (wave == 3) __builtin_amdgcn_s_setprio(2);
                if (__builtin_expect(wave < 6, 0)) { op_addr(1); qk_addr(false); att_addr(); }
                if (wave >= 6) {
                    const unsigned o = qo;
                    f32x2 cs = -*(const LAS f32x2*)(size_t)qc;
                    float rn = 1.f / DM;
                    unsigned long long m0 = 1ull << 24, m1 = 1ull << 25;
                    PIN2(cs, rn); PINS(m0); PINS(m1);
                    MARK(b_qg);
                    sched(X1, 0);
                    STAMP(51);
                    u32x4 a0, a1, a2, a3; f32x2 u;
                    RETRY({ a0 = ld16_sc1<-3584>(ws, o); a1 = ld16_sc1<-1792>(ws, o); a2 = ld16_sc1<0>(ws, o); a3 = ld16_sc1<1792>(ws, o); },
                          ({ asm volatile("s_waitcnt vmcnt(0)" : "+v"(a0), "+v"(a1), "+v"(a2), "+v"(a3) :: "memory"); max(max(um4(a0), um4(a1)), max(um4(a2), um4(a3))); }),
                          { unsigned sm;
                            asm volatile("s_waitcnt vmcnt(2)\n\tv_pk_add_f32 v[40:41], %2, %4\n\tv_pk_add_f32 v[42:43], %3, %5\n\t"
                                         "s_waitcnt vmcnt(1)\n\tv_pk_add_f32 v[40:41], v[40:41], %6\n\tv_pk_add_f32 v[42:43], v[42:43], %7\n\t"
                                         "s_waitcnt vmcnt(0)\n\tv_pk_add_f32 v[40:41], v[40:41], %8\n\tv_pk_add_f32 v[42:43], v[42:43], %9\n\ts_nop 1\n\t"
                                         "v_permlane32_swap_b32 v40, v42\n\tv_permlane32_swap_b32 v41, v43\n\tv_pk_add_f32 v[40:41], v[40:41], v[42:43]\n\ts_nop 0\n\t"
                                         "v_readlane_b32 %1, v41, 24\n\ts_nop 1\n\tv_mul_f32 v42, %1, %11\n\tv_cvt_pk_bf16_f32 v42, 0, v42\n\tv_pk_fma_f32 %0, %10, v[42:43], v[40:41] op_sel_hi:[1,0,1]"
                                         : "=&v"(u), "=&s"(sm) : "v"(LO2(a0)), "v"(HI2(a0)), "v"(LO2(a1)), "v"(HI2(a1)), "v"(LO2(a2)), "v"(HI2(a2)), "v"(LO2(a3)), "v"(HI2(a3)), "v"(cs), "v"(rn)
                                         : "memory", "v40", "v41", "v42", "v43");
                            unsigned w = pk2v(u);
                            if (LANES(m0)) w = __float_as_uint(u[0]);
                            if (LANES(m1)) w = __float_as_uint(u[1]);
                            *(LAS unsigned*)(size_t)qw = w; __builtin_amdgcn_sched_barrier(0); }, any_nan(u[0], u[1]), X1);
                    MARK(e_qg);
                }
            }
        }
    };
    PINS(ck1); PINS(ck2); PINS(csc);
    chain(IC<0>{});
    STAMP(35);
    att1();
    chain(IC<1>{});
}

__device__ __attribute__((used, noinline)) void mega_code_end() { asm volatile(".Lmega_code_end:"); }

extern "C" void kernel_launch(void* const* d_in, const int* in_sizes, int n_in, void* d_out, int out_size, void* d_ws,
                              size_t ws_size, hipStream_t stream) {
    const u16* x0 = (const u16*)d_in[0];
    const u16* kc = (const u16*)d_in[1];
    const u16* vc = (const u16*)d_in[2];
    const u16* wq = (const u16*)d_in[3];
    const u16* wo = (const u16*)d_in[4];
    const u16* wr = (const u16*)d_in[6];
    const u16* wg = (const u16*)d_in[7];
    const u16* wu = (const u16*)d_in[8];
    const u16* wd = (const u16*)d_in[9];
    (void)hipFuncSetAttribute((const void*)mega_kernel, hipFuncAttributeMaxDynamicSharedMemorySize, L_END);
    mega_kernel<<<256, 512, L_END, stream>>>(x0, kc, vc, wq, wo, wr, wg, wu, wd, (u16*)d_out, (unsigned char*)d_ws
                                             );
}
```
